```python
import torch

def _softmax(x, axis=-1):
    return torch.softmax(x, dim=axis)

def _gelu(x):
    return torch.nn.functional.gelu(x, approximate='tanh')

def _silu(x):
    return torch.nn.functional.silu(x)

def _sigmoid(x):
    return torch.sigmoid(x)

def _layernorm(x, axis=-1, eps=1e-05):
    m = x.mean(dim=axis, keepdim=True)
    v = x.var(dim=axis, keepdim=True, unbiased=False)
    return (x - m) / torch.sqrt(v + eps)

def _scatter_add(tgt, idx, src, axis=0):
    return tgt.clone().index_add_(axis, idx, src)

def _segment_sum(src, seg, n):
    out = torch.zeros((n,) + src.shape[1:], dtype=src.dtype)
    return out.index_add_(0, seg, src)

def _pad(x, pw):
    flat = tuple((v for pair in reversed(pw) for v in pair))
    return torch.nn.functional.pad(x, flat)
kernel_name = 'sequential_preLN__memory_aug_recurrent_mem_tr__moe_arch_mod__prompt'

def setup_inputs():

    _w = lambda a: (lambda t: t.to(torch.bfloat16) if t.is_floating_point() else t)(torch.as_tensor(a))

    def _A_setup():
        B, S, Dm = (1, 2048, 512)
        H, Dh = (32, 128)
        M, seg = (8, 32)
        seg = min(seg, S)
        S = S // seg * seg
        return {'x': _w((lambda *s: torch.randn(*s, dtype=torch.bfloat16) * 0.05)(B, S, Dm)), 'mem0': _w((lambda *s: torch.randn(*s, dtype=torch.bfloat16) * 0.2)(B, M, Dm)), 'Wq': _w((lambda *s: torch.randn(*s, dtype=torch.bfloat16) * 1.6)(Dm, H, Dh) * Dm ** (-0.5)), 'Wk': _w((lambda *s: torch.randn(*s, dtype=torch.bfloat16) * 1.6)(Dm, H, Dh) * Dm ** (-0.5)), 'Wv': _w((lambda *s: torch.randn(*s, dtype=torch.bfloat16) * 0.2)(Dm, H, Dh) * Dm ** (-0.5)), 'Wo': _w((lambda *s: torch.randn(*s, dtype=torch.bfloat16) * 0.2)(H, Dh, Dm) * (H * Dh) ** (-0.5)), 'seg_len': seg}
    _A_in = _A_setup()

    def _B_setup():
        B, S, D, F, C = (1, 2048, 512, 1792, 2)
        return {'x': _w((lambda *s: torch.randn(*s, dtype=torch.bfloat16) * 0.05)(B, S, D)), 'w_r': _w((lambda *s: torch.randn(*s, dtype=torch.bfloat16) * 0.05)(D)), 'w_gate': _w((lambda *s: torch.randn(*s, dtype=torch.bfloat16) * 0.05)(D, F)), 'w_up': _w((lambda *s: torch.randn(*s, dtype=torch.bfloat16) * 0.05)(D, F)), 'w_down': _w((lambda *s: torch.randn(*s, dtype=torch.bfloat16) * 0.05)(F, D))}
    _B_in = _B_setup()
    return {'A_x': _A_in['x'], 'A_mem0': _A_in['mem0'], 'A_Wq': _A_in['Wq'], 'A_Wk': _A_in['Wk'], 'A_Wv': _A_in['Wv'], 'A_Wo': _A_in['Wo'], 'A_seg_len': _A_in['seg_len'], 'B_x': _B_in['x'], 'B_w_r': _B_in['w_r'], 'B_w_gate': _B_in['w_gate'], 'B_w_up': _B_in['w_up'], 'B_w_down': _B_in['w_down']}

def reference(A_x, A_mem0, A_Wq, A_Wk, A_Wv, A_Wo, A_seg_len, B_x, B_w_r, B_w_gate, B_w_up, B_w_down):

    def _A_ref(x, mem0, Wq, Wk, Wv, Wo, seg_len):
        B, S, Dm = x.shape
        H, Dh = (Wq.shape[1], Wq.shape[2])
        M = mem0.shape[1]
        scale = Dh ** (-0.5)
        n_seg = S // seg_len
        L = M + seg_len
        idx = torch.arange(L)
        mask = (idx[:, None] < M) | (idx[:, None] >= idx[None, :])
        mem = mem0
        outs = []
        for s in range(n_seg):
            xs = x[:, s * seg_len:(s + 1) * seg_len, :].clone()
            h = torch.concatenate([mem, xs], axis=1)
            q = torch.einsum('btd,dhe->bhte', h, Wq)
            k = torch.einsum('btd,dhe->bhte', h, Wk)
            v = torch.einsum('btd,dhe->bhte', h, Wv)
            scores = torch.einsum('bhqe,bhke->bhqk', q, k) * scale
            scores = torch.where(mask[None, None], scores, -1000000000.0)
            p = _softmax(scores, axis=-1)
            o = torch.einsum('bhqk,bhke->bhqe', p, v)
            o = torch.einsum('bhte,hed->btd', o, Wo)
            mem = o[:, :M, :].clone()
            outs.append(o[:, M:, :])
        return torch.concatenate(outs, axis=1)

    def _B_ref(x, w_r, w_gate, w_up, w_down):
        B, S, D = x.shape
        C = 2
        r = x @ w_r
        idx = torch.argsort(-r, axis=-1)[:, :C]
        idx_e = (lambda x, n, axis: torch.repeat_interleave(x, n, dim=axis))(idx.reshape(B, C, 1), D, axis=2)
        x_sel = (lambda x, i, axis: torch.take_along_dim(x, i, dim=axis))(x, idx_e, axis=1)
        g = _silu(x_sel @ w_gate)
        u = x_sel @ w_up
        y = g * u @ w_down
        r_sel = _sigmoid((lambda x, i, axis: torch.take_along_dim(x, i, dim=axis))(r, idx, axis=1))
        y = y * r_sel.reshape(B, C, 1)
        b_off = (torch.arange(B) * S).reshape(B, 1)
        flat_idx = (idx + b_off).reshape(B * C)
        zero = (x * 0.0).reshape(B * S, D)
        upd = _scatter_add(zero, flat_idx, y.reshape(B * C, D), axis=0)
        return x + upd.reshape(B, S, D)
    x = A_x
    h = x + _A_ref(_layernorm(x, axis=-1), A_mem0, A_Wq, A_Wk, A_Wv, A_Wo, A_seg_len)
    h2 = h + _B_ref(_layernorm(h, axis=-1), B_w_r, B_w_gate, B_w_up, B_w_down)
    return h2
```

```cpp
#include <hip/hip_runtime.h>
#include <hip/hip_bf16.h>
#define SWAIT 0
#define SW2 0
#define ASLEEP 32
#define ASALL 0
#define PSLEEP 8
#define FSLEEP 6

typedef unsigned short u16;
typedef unsigned long long u64;
typedef __attribute__((ext_vector_type(8))) short bf16x8;
typedef __attribute__((ext_vector_type(4))) float f32x4;
typedef __attribute__((ext_vector_type(4))) unsigned u32x4;
#define GAS __attribute__((address_space(1)))
typedef GAS u64 gu64;
typedef GAS unsigned gu32;
#define RLX_AGENT __ATOMIC_RELAXED, __HIP_MEMORY_SCOPE_AGENT

constexpr int S_ = 2048, DM = 512, NH = 32, DH = 128, MM = 8, SEG = 32, NSEG = 64;
constexpr int HD = NH * DH;
constexpr int QKVN = 3 * HD;
constexpr int FF = 1792;

__device__ __forceinline__ float bf2f(u16 v) { return __builtin_bit_cast(float, (unsigned)v << 16); }
__device__ __forceinline__ u16 f2bf(float f) { unsigned u = __builtin_bit_cast(unsigned, f); return (u16)((u + 0x7fffu + ((u >> 16) & 1u)) >> 16); }
__device__ __forceinline__ float rbf(float f) { return bf2f(f2bf(f)); }

typedef __attribute__((ext_vector_type(2))) float f32x2;
typedef __attribute__((ext_vector_type(2))) __bf16 bf16x2_t;
typedef __attribute__((ext_vector_type(2))) unsigned u32x2;
__device__ __forceinline__ unsigned pk2(float a, float b) { return __builtin_bit_cast(unsigned, __builtin_convertvector((f32x2){a, b}, bf16x2_t)); }
__device__ __forceinline__ unsigned f2u(float f) { return __builtin_bit_cast(unsigned, f); }
__device__ __forceinline__ float u2f(unsigned u) { return __builtin_bit_cast(float, u); }
__device__ __forceinline__ float lo2f(unsigned u) { return __builtin_bit_cast(float, u << 16); }
__device__ __forceinline__ float hi2f(unsigned u) { return __builtin_bit_cast(float, u & 0xffff0000u); }
__device__ __forceinline__ void unpack8(const u32x4 v, float (&f)[8]) {
#pragma unroll
  for (int i = 0; i < 4; ++i) { f[2 * i] = lo2f(v[i]); f[2 * i + 1] = hi2f(v[i]); }
}

constexpr unsigned O_EPOCH = 0;
constexpr unsigned O_TFLAG = 256;
constexpr unsigned O_MFLAG = O_TFLAG + 16 * 32 * 64;
constexpr unsigned O_LFLAG = O_MFLAG + 32 * 128;
constexpr unsigned O_WM = O_LFLAG + 128 * 128;
constexpr unsigned O_KVSTEP = O_WM + 128 * 128;
constexpr unsigned O_CNT = O_KVSTEP + 2048;
constexpr unsigned O_PFLAG = O_CNT + 2048;
constexpr unsigned O_SEL2 = O_PFLAG + 4 * 128 * 128;
constexpr unsigned O_ACT = O_SEL2 + 32 * 128;
constexpr unsigned O_RBUF = O_ACT + 3 * 112 * 128;
constexpr unsigned O_INIT_END = O_RBUF + 2 * S_ * 4 + 256 * 128;
constexpr unsigned O_STAMPS = 196608 + 65 * 32768;
constexpr unsigned O_XN = O_STAMPS + (4 << 20);
constexpr unsigned O_QKV = O_XN + S_ * DM * 2;
constexpr unsigned O_KVM = O_QKV + S_ * QKVN * 2;
constexpr unsigned O_VTM = O_KVM + NSEG * 8 * HD * 2;
constexpr unsigned O_PART = O_VTM + NSEG * 8 * HD * 2;
constexpr unsigned O_HBUF = O_PART + S_ * 8 * DM * 4;
constexpr unsigned O_LNH = O_HBUF + S_ * DM * 2;
constexpr unsigned O_MFRAG = O_LNH + S_ * DM * 2;
constexpr unsigned O_SLOTS = (O_MFRAG + 2 * DM * FF * 2 + 0x1fffff) & ~0x1fffffu;
constexpr unsigned WS_BYTES = O_SLOTS + 65 * 32768;
#define QO(i) (((i) & 1) * 16384 + ((i) >> 1) * 8192)
#define CAL(b) (O_SLOTS + 128 + ((b) >> 3) * 16384 + ((b) & 7) * 1024)
static_assert(O_INIT_END <= O_STAMPS && (O_INIT_END - O_KVSTEP) / 16 <= 128 * 128, "layout");
static_assert(O_TFLAG == 256 && O_MFLAG == 33024 && O_LFLAG == 37120 && O_STAMPS == 2326528 && O_XN == 6520832 && O_QKV == 8617984 && O_KVM == 58949632 && O_VTM == 63143936 && O_PART == 67338240 && O_MFRAG == 105086976 && O_SLOTS == 109051904 && O_CNT + 1024 == 72960 && O_INIT_END <= 262144 && O_WM == 53504, "layout");
constexpr int L_BS = 520, L_QN = 49152, L_TB = 98304, L_VT = 32768;
struct MegaParams {
  const u16* x; const u16* mem0; const u16* Wq; const u16* Wk; const u16* Wv; const u16* Wo;
  const u16* w_r; const u16* wg; const u16* wu; const u16* wd;
  u16* out;
  char* ws;
};
__device__ __forceinline__ unsigned mktag(unsigned e) { return e * 0x9e3779b1u + 0x7f4a7c15u; }

typedef __attribute__((ext_vector_type(4))) short s16x4;
#define LAS __attribute__((address_space(3)))
constexpr int LDS_BYTES = 163840;
#define SB() __builtin_amdgcn_sched_barrier(0)
#define LDS_BARRIER() asm volatile("s_waitcnt lgkmcnt(0)\n\ts_barrier" ::: "memory")
#define VM_DRAIN() asm volatile("s_waitcnt vmcnt(0)" ::: "memory")
__device__ __forceinline__ float xmax16_32(float m) {
  u32x2 p = __builtin_amdgcn_permlane16_swap(f2u(m), f2u(m), false, false);
  m = fmaxf(u2f(p[0]), u2f(p[1]));
  p = __builtin_amdgcn_permlane32_swap(f2u(m), f2u(m), false, false);
  return fmaxf(u2f(p[0]), u2f(p[1]));
}
__device__ __forceinline__ float xsum16_32(float m) {
  u32x2 p = __builtin_amdgcn_permlane16_swap(f2u(m), f2u(m), false, false);
  m = u2f(p[0]) + u2f(p[1]);
  p = __builtin_amdgcn_permlane32_swap(f2u(m), f2u(m), false, false);
  return u2f(p[0]) + u2f(p[1]);
}
__device__ __forceinline__ unsigned wave_max_u32(unsigned v) {
  v = max(v, (unsigned)__builtin_amdgcn_mov_dpp(v, 0xb1, 0xf, 0xf, true));
  v = max(v, (unsigned)__builtin_amdgcn_mov_dpp(v, 0x4e, 0xf, 0xf, true));
  v = max(v, (unsigned)__builtin_amdgcn_mov_dpp(v, 0x124, 0xf, 0xf, true));
  v = max(v, (unsigned)__builtin_amdgcn_mov_dpp(v, 0x128, 0xf, 0xf, true));
  u32x2 p = __builtin_amdgcn_permlane16_swap(v, v, false, false);
  v = max(p[0], p[1]);
  p = __builtin_amdgcn_permlane32_swap(v, v, false, false);
  return max(p[0], p[1]);
}
__device__ __forceinline__ u32x4 ld_sc1(__amdgpu_buffer_rsrc_t rs, unsigned off) { return __builtin_amdgcn_raw_buffer_load_b128(rs, off, 0, 16); }
__device__ __forceinline__ u32x2 ld8_sc1(__amdgpu_buffer_rsrc_t rs, unsigned off) { return __builtin_amdgcn_raw_buffer_load_b64(rs, off, 0, 16); }

#define XS 0
#define VBX __builtin_amdgcn_readfirstlane((int)(((blockIdx.x & 4) << 5) + (blockIdx.x >> 3) * 4 + (blockIdx.x & 3)))
constexpr int NREC = 128, NWORK = 128;
typedef __attribute__((ext_vector_type(8))) unsigned u32x8;
typedef __attribute__((ext_vector_type(16))) unsigned u32x16;
typedef __attribute__((ext_vector_type(32))) unsigned u32x32;
#define RSRC(p, n) __builtin_amdgcn_make_buffer_rsrc((void*)(p), 0, (n), 0x00020000)

__device__ __forceinline__ int fresh_lane() {
  int l;
  asm volatile("v_mbcnt_lo_u32_b32 %0, -1, 0\n\tv_mbcnt_hi_u32_b32 %0, -1, %0" : "=v"(l));
  return l;
}
typedef const __attribute__((address_space(4))) MegaParams* KArgs;
#define ARGS() KArgs A = (KArgs)__builtin_amdgcn_kernarg_segment_ptr(); asm volatile("" : "+s"(A))
#define MS(k)
__device__ __forceinline__ void rec_role(char* lds, KArgs& A, int& b, int& wv, const u32x16 ka, const u32x8 kb) {
#define HMAP 0
  const int h = HMAP == 1 ? (b & 3) * 8 + ((b >> 2) & 7) : (HMAP == 2 ? (b & 1) * 16 + (b & 2) / 2 + ((b >> 2) & 7) * 2 : b & 31), part = b >> 5;
  const int lane = fresh_lane(), l15 = lane & 15, l4 = lane >> 4;
  const int b1 = (l15 >> 1) & 1, mrow = 2 * (l15 >> 2) + (l15 & 1);
  const int r4 = l15 >> 2, c4 = l15 & 3;
#define KA(v, i) ((u64)v[i] | (u64)v[(i) + 1] << 32)
  const u64 wsa = KA(kb, 6), woa = KA(ka, 10), wva = KA(ka, 8), wqa = KA(ka, 4), wka = KA(ka, 6), m0a = KA(ka, 2);
  const unsigned awv = (unsigned)((8 * wv + l4) * 8192 + h * 256 + ((l15 ^ (l4 | (wv << 2))) << 4)), rot = 1 ? part : 0;
  const int sig = 1 ? (h >> 2) & 1 : 0;
  unsigned tch;
  asm volatile(
      "s_lshl_b32 s91, %5, 4\n\t"
      "s_add_u32 m0, %5, 0\n\t"
      "s_xor_b32 s90, s91, 0\n\t"
      "buffer_load_dwordx4 %1, %3, s90 offen lds\n\t"
      "s_add_u32 m0, %5, 1024\n\t"
      "s_xor_b32 s90, s91, 16384\n\t"
      "buffer_load_dwordx4 %1, %3, s90 offen lds\n\t"
      "s_add_u32 m0, %5, 8192\n\t"
      "s_xor_b32 s90, s91, 32768\n\t"
      "buffer_load_dwordx4 %1, %3, s90 offen lds\n\t"
      "s_add_u32 m0, %5, 9216\n\t"
      "s_xor_b32 s90, s91, 49152\n\t"
      "buffer_load_dwordx4 %1, %3, s90 offen lds\n\t"
      "s_add_u32 m0, %5, 16384\n\t"
      "s_xor_b32 s90, s91, 65536\n\t"
      "buffer_load_dwordx4 %1, %3, s90 offen lds\n\t"
      "s_add_u32 m0, %5, 17408\n\t"
      "s_xor_b32 s90, s91, 81920\n\t"
      "buffer_load_dwordx4 %1, %3, s90 offen lds\n\t"
      "s_add_u32 m0, %5, 24576\n\t"
      "s_xor_b32 s90, s91, 98304\n\t"
      "buffer_load_dwordx4 %1, %3, s90 offen lds\n\t"
      "s_add_u32 m0, %5, 25600\n\t"
      "s_xor_b32 s90, s91, 114688\n\t"
      "buffer_load_dwordx4 %1, %3, s90 offen lds\n\t"
      : "=&{s87}"(tch)
      : "v"((unsigned)((h * DH + (lane >> 2)) * DM + 128 * part + 32 * wv + 8 * (2 * (lane & 1) + (((lane >> 1) & 1) ^ sig))) * 2), "v"(awv), "s"(RSRC(woa, DM * HD * 2)), "s"(RSRC(wva, DM * HD * 2)),
        "s"(wv * 2048), "s"(rot << 19), "s"(wva), "s"(wqa), "s"(wka), "s"(wsa), "s"(m0a)
      : "memory", "scc", "s90", "s91", "s92");
  SB();
  const unsigned i0 = 1024 + b * 512 + wv * 64 + lane, o1 = O_KVSTEP + ((wv >> 1) * 8192 + b * 64 + lane) * 16;
  const unsigned oz = wv == 2 ? CAL(b ^ 1) + (lane >> 5) * 8192 + ((lane >> 3) & 3) * 256 + (lane & 7) * 16 : ((wv & 1) && o1 < O_INIT_END ? o1 : 0xffffff00u);
  const int sl = lane ^ ((l4 & 1) << 1), su = ((l15 >> 3) * 16 + c4 * 4 + l4) ^ ((l15 >> 3) << 1);
  const int jt = wv ^ rot;
  u32x8 cc = {oz, (unsigned)lane, (unsigned)lane,
              wv == 2 || o1 < O_ACT ? 0u : ~0u,
              (unsigned)(QO(jt >> 1) + (((jt & 1) * 2 + (l4 >> 1)) * 8 + (r4 & 1) * 2 + (l15 >> 3)) * 256 + (l15 & 1) * 64 + (2 * b1 + (l4 & 1)) * 16),
              (unsigned)(h * 2048 + (2 * (part & 1) + (l4 >> 1)) * 512 + (2 * b1 + (l4 & 1)) * 128 + mrow * 16),
              (unsigned)(h * 2048 + part * 512 + (l4 >> 1) * 256 + (8 * wv + 4 * (l15 >> 3) + c4) * 16 + (r4 & 1) * 8 + (l4 & 1) * 4),
              (unsigned)((2 * (wv >> 1) + (l4 >> 1)) * 2048 + ((2 * (wv & 1) + (l4 & 1)) * 16 + l15) * 16)};
  u32x8 c3 = {(unsigned)lane, O_SLOTS + (i0 >> 3) * 256 + (i0 & 7) * 16,
              (unsigned)(wv * 2048 + (8 * l4 + r4) * 64 + c4 * 16),
              (unsigned)(wv * 2048 + (r4 >> 1) * 1024 + (l4 * 16 + 8 * (c4 >> 1) + 4 * (r4 & 1) + (c4 & 1)) * 16),
              (unsigned)(L_VT + (8 * r4 + c4) * 256 + (((4 * wv + l4) ^ l15) << 4)),
              awv,
              (unsigned)(L_VT + ((wv & 1) * 32 + 8 * l4 + r4) * 256 + (((4 * part + 2 * (wv >> 1) + (c4 >> 1)) ^ (r4 | (l4 << 2))) << 4) + (c4 & 1) * 8),
              (unsigned)(QO(part) + h * 256 + (lane & 7) * 16)};
  const int L = (h & 7) * 8 + (lane & 7);
  const u64 mp = (u64)((const u16*)m0a + (2 * ((L >> 2) & 3) + ((L >> 4) & 1)) * DM + 128 * part + 32 * (h >> 3) + 8 * (L >> 5) + 16 * (L & 1) + (L & 2));
  u32x8 c0 = {(unsigned)((8 * wv + l4) * 8192 + h * 256 + ((l15 ^ (2 * l4 + 8 * (wv & 1))) << 4)),
              (unsigned)(L_TB + (8 * l4 + r4) * 256 + (((4 * wv + (c4 >> 1)) ^ (2 * r4 + 8 * (l4 & 1))) << 4) + (c4 & 1) * 8),
              (unsigned)mp, (unsigned)(mp >> 32), (unsigned)(CAL(b) + wv * 256 + ((wv ^ (b >> 2)) & 1) * 8192) & (unsigned)((b - 32) >> 31) & (unsigned)((1 - wv) >> 31), (unsigned)(QO(part) + sig * 1024),
              b1 ? 0xEEEEu : 0x4444u, (unsigned)((wv * 8 + (lane >> 5)) * 256 + (lane & 31) * 4)};
  u32x4 c1 = {(unsigned)(sl * 8),
                    (unsigned)(L_QN + ((wv >> 1) * 2 + (r4 & 1)) * L_BS + ((wv & 1) * 32 + su) * 8),
                    (unsigned)lane, (unsigned)lane * 16};
  const unsigned k0 = O_QKV + h * 24576, v0 = O_QKV + 8192 + SEG * QKVN * 2 + h * 24576 + wv * 2048;
  const unsigned role = wv < 2 ? wv : (wv == 3 ? 4 : (part < 2 ? 2 + part : 5));
  u32x16 sc = {(unsigned)lane, (unsigned)wv, role, O_SLOTS, k0, O_MFRAG + (min(h, 27) * 4 + part) * 32768 + wv * 4096, v0, rot << 19, O_KVM, O_VTM,
                     O_TFLAG + h * 64, O_KVSTEP + h * 64 + part * 4, O_STAMPS + (b * 4 + wv) * 65 * 64,
                     O_EPOCH, O_MFLAG + h * 128, (unsigned)wv * 2048};
  u32x16 s2 = {(unsigned)woa, (unsigned)(woa >> 32) & 0xffffu, DM * HD * 2, 0x00020000u, (unsigned)wva, (unsigned)(wva >> 32) & 0xffffu, O_WM, O_WM + b * 128,
               (unsigned)wsa, (unsigned)(wsa >> 32) & 0xffffu, WS_BYTES, 0x00020000u,
               (unsigned)wqa, (unsigned)(wqa >> 32) & 0xffffu, (unsigned)wka, (unsigned)(wka >> 32) & 0xffffu};
  asm volatile(
      "s_nop 1\n\t"
      "v_readfirstlane_b32 s40, v0\n\t"
      "v_readfirstlane_b32 s41, v1\n\t"
      "v_readfirstlane_b32 s42, v2\n\t"
      "v_readfirstlane_b32 s43, v3\n\t"
      "v_readfirstlane_b32 s44, v4\n\t"
      "v_readfirstlane_b32 s45, v5\n\t"
      "v_readfirstlane_b32 s46, v6\n\t"
      "v_readfirstlane_b32 s47, v7\n\t"
      "v_readfirstlane_b32 s48, v8\n\t"
      "v_readfirstlane_b32 s49, v9\n\t"
      "v_readfirstlane_b32 s50, v10\n\t"
      "v_readfirstlane_b32 s51, v11\n\t"
      "v_readfirstlane_b32 s52, v12\n\t"
      "v_readfirstlane_b32 s53, v13\n\t"
      "v_readfirstlane_b32 s54, v14\n\t"
      "v_readfirstlane_b32 s55, v15\n\t"
      "v_readfirstlane_b32 s88, v16\n\t"
      "v_readfirstlane_b32 s89, v17\n\t"
      "v_readfirstlane_b32 s90, v18\n\t"
      "v_readfirstlane_b32 s91, v19\n\t"
      "v_readfirstlane_b32 s92, v20\n\t"
      "v_readfirstlane_b32 s93, v21\n\t"
      "v_readfirstlane_b32 s94, v22\n\t"
      "v_readfirstlane_b32 s95, v23\n\t"
      "v_readfirstlane_b32 s36, v24\n\t"
      "v_readfirstlane_b32 s37, v25\n\t"
      "v_readfirstlane_b32 s38, v26\n\t"
      "v_readfirstlane_b32 s39, v27\n\t"
      "v_readfirstlane_b32 s96, v28\n\t"
      "v_readfirstlane_b32 s97, v29\n\t"
      "v_readfirstlane_b32 s98, v30\n\t"
      "v_readfirstlane_b32 s99, v31\n\t"
      "s_nop 4\n\t"
      "v_readfirstlane_b32 s86, v120\n\t"
      "v_add_u32 v100, s55, v127\n\t"
      "s_mov_b64 s[88:89], s[92:93]\n\t"
      "s_add_u32 m0, s55, 32768\n\t"
      "s_xor_b32 s61, s47, 0\n\t"
      "buffer_load_dwordx4 v105, s[88:91], s61 offen lds\n\t"
      "s_add_u32 m0, s55, 33792\n\t"
      "s_xor_b32 s61, s47, 32768\n\t"
      "buffer_load_dwordx4 v105, s[88:91], s61 offen lds\n\t"
      "s_add_u32 m0, s55, 40960\n\t"
      "s_xor_b32 s61, s47, 262144\n\t"
      "buffer_load_dwordx4 v105, s[88:91], s61 offen lds\n\t"
      "s_add_u32 m0, s55, 41984\n\t"
      "s_xor_b32 s61, s47, 294912\n\t"
      "buffer_load_dwordx4 v105, s[88:91], s61 offen lds\n\t"
      "s_add_u32 m0, s55, 49152\n\t"
      "s_xor_b32 s61, s47, 524288\n\t"
      "buffer_load_dwordx4 v105, s[88:91], s61 offen lds\n\t"
      "s_add_u32 m0, s55, 50176\n\t"
      "s_xor_b32 s61, s47, 557056\n\t"
      "buffer_load_dwordx4 v105, s[88:91], s61 offen lds\n\t"
      "s_add_u32 m0, s55, 57344\n\t"
      "s_xor_b32 s61, s47, 786432\n\t"
      "buffer_load_dwordx4 v105, s[88:91], s61 offen lds\n\t"
      "s_add_u32 m0, s55, 58368\n\t"
      "s_xor_b32 s61, s47, 819200\n\t"
      "buffer_load_dwordx4 v105, s[88:91], s61 offen lds\n\t"
      "buffer_load_dword v126, off, s[36:39], s53 sc1\n\t"
      "global_load_ushort v96, v[118:119], off offset:0\n\t"
      "global_load_ushort v97, v[118:119], off offset:2\n\t"
      "global_load_ushort v98, v[118:119], off offset:8\n\t"
      "global_load_ushort v99, v[118:119], off offset:10\n\t"
      "s_xor_b32 s61, s86, 0x2000\n\t"
      "s_buffer_load_dword s87, s[36:39], s61\n\t"
      "s_waitcnt vmcnt(13)\n\t"
      "s_waitcnt lgkmcnt(0)\n\t"
      "ds_read_b64_tr_b16 v[64:65], v102\n\t"
      "ds_read_b64_tr_b16 v[66:67], v102 offset:256\n\t"
      "ds_read_b64_tr_b16 v[68:69], v102 offset:8\n\t"
      "ds_read_b64_tr_b16 v[70:71], v102 offset:264\n\t"
      "ds_read_b64_tr_b16 v[72:73], v102 offset:8192\n\t"
      "ds_read_b64_tr_b16 v[74:75], v102 offset:8448\n\t"
      "ds_read_b64_tr_b16 v[76:77], v102 offset:8200\n\t"
      "ds_read_b64_tr_b16 v[78:79], v102 offset:8456\n\t"
      "ds_read_b64_tr_b16 v[80:81], v102 offset:16384\n\t"
      "ds_read_b64_tr_b16 v[82:83], v102 offset:16640\n\t"
      "ds_read_b64_tr_b16 v[84:85], v102 offset:16392\n\t"
      "ds_read_b64_tr_b16 v[86:87], v102 offset:16648\n\t"
      "s_waitcnt lgkmcnt(4)\n\t"
      "ds_read_b64_tr_b16 v[88:89], v102 offset:24576\n\t"
      "ds_read_b64_tr_b16 v[90:91], v102 offset:24832\n\t"
      "ds_read_b64_tr_b16 v[92:93], v102 offset:24584\n\t"
      "ds_read_b64_tr_b16 v[94:95], v102 offset:24840\n\t"
      "s_mov_b64 s[88:89], s[96:97]\n\t"
      "s_add_u32 m0, s55, 98304\n\t"
      "s_xor_b32 s61, s47, 0\n\t"
      "buffer_load_dwordx4 v116, s[88:91], s61 offen lds\n\t"
      "s_add_u32 m0, s55, 99328\n\t"
      "s_xor_b32 s61, s47, 32768\n\t"
      "buffer_load_dwordx4 v116, s[88:91], s61 offen lds\n\t"
      "s_add_u32 m0, s55, 106496\n\t"
      "s_xor_b32 s61, s47, 262144\n\t"
      "buffer_load_dwordx4 v116, s[88:91], s61 offen lds\n\t"
      "s_add_u32 m0, s55, 107520\n\t"
      "s_xor_b32 s61, s47, 294912\n\t"
      "buffer_load_dwordx4 v116, s[88:91], s61 offen lds\n\t"
      "s_waitcnt lgkmcnt(0)\n\t"
      "ds_write_b128 v103, v[64:67]\n\t"
      "ds_write_b128 v103, v[68:71] offset:32\n\t"
      "ds_write_b128 v103, v[72:75] offset:8192\n\t"
      "ds_write_b128 v103, v[76:79] offset:8224\n\t"
      "ds_write_b128 v103, v[80:83] offset:16384\n\t"
      "ds_write_b128 v103, v[84:87] offset:16416\n\t"
      "ds_write_b128 v103, v[88:91] offset:24576\n\t"
      "ds_write_b128 v103, v[92:95] offset:24608\n\t"
      "s_waitcnt lgkmcnt(4)\n\t"
      "ds_read_b128 v[32:35], v100\n\t"
      "ds_read_b128 v[36:39], v100 offset:1024\n\t"
      "ds_read_b128 v[40:43], v100 offset:8192\n\t"
      "ds_read_b128 v[44:47], v100 offset:9216\n\t"
      "ds_read_b128 v[48:51], v100 offset:16384\n\t"
      "ds_read_b128 v[52:55], v100 offset:17408\n\t"
      "ds_read_b128 v[56:59], v100 offset:24576\n\t"
      "ds_read_b128 v[60:63], v100 offset:25600\n\t"
      "v_xor_b32 v118, 32, v117\n\t"
      "v_xor_b32 v119, 64, v104\n\t"
      "v_xor_b32 v102, 128, v104\n\t"
      "v_xor_b32 v103, 192, v104\n\t"
      "s_mov_b64 s[88:89], s[92:93]\n\t"
      "s_add_u32 m0, s55, 65536\n\t"
      "s_xor_b32 s61, s47, 1048576\n\t"
      "buffer_load_dwordx4 v105, s[88:91], s61 offen lds\n\t"
      "s_add_u32 m0, s55, 66560\n\t"
      "s_xor_b32 s61, s47, 1081344\n\t"
      "buffer_load_dwordx4 v105, s[88:91], s61 offen lds\n\t"
      "s_mov_b64 s[88:89], s[96:97]\n\t"
      "s_add_u32 m0, s55, 114688\n\t"
      "s_xor_b32 s61, s47, 524288\n\t"
      "buffer_load_dwordx4 v116, s[88:91], s61 offen lds\n\t"
      "s_add_u32 m0, s55, 115712\n\t"
      "s_xor_b32 s61, s47, 557056\n\t"
      "buffer_load_dwordx4 v116, s[88:91], s61 offen lds\n\t"
      "s_add_u32 m0, s55, 122880\n\t"
      "s_xor_b32 s61, s47, 786432\n\t"
      "buffer_load_dwordx4 v116, s[88:91], s61 offen lds\n\t"
      "s_add_u32 m0, s55, 123904\n\t"
      "s_xor_b32 s61, s47, 819200\n\t"
      "buffer_load_dwordx4 v116, s[88:91], s61 offen lds\n\t"
      "s_waitcnt lgkmcnt(0)\n\t"
      "s_waitcnt vmcnt(21)\n\t"
      "s_barrier\n\t"
      "ds_read_b128 v[0:3], v104\n\t"
      "ds_read_b128 v[4:7], v119\n\t"
      "ds_read_b128 v[8:11], v102\n\t"
      "ds_read_b128 v[12:15], v103\n\t"
      "ds_read_b128 v[64:67], v104 offset:1024\n\t"
      "ds_read_b128 v[68:71], v119 offset:1024\n\t"
      "ds_read_b128 v[72:75], v102 offset:1024\n\t"
      "ds_read_b128 v[76:79], v103 offset:1024\n\t"
      "s_mov_b64 s[88:89], s[92:93]\n\t"
      "s_add_u32 m0, s55, 73728\n\t"
      "s_xor_b32 s61, s47, 1310720\n\t"
      "buffer_load_dwordx4 v105, s[88:91], s61 offen lds\n\t"
      "s_add_u32 m0, s55, 74752\n\t"
      "s_xor_b32 s61, s47, 1343488\n\t"
      "buffer_load_dwordx4 v105, s[88:91], s61 offen lds\n\t"
      "s_mov_b64 s[88:89], s[96:97]\n\t"
      "s_add_u32 m0, s55, 131072\n\t"
      "s_xor_b32 s61, s47, 1048576\n\t"
      "buffer_load_dwordx4 v116, s[88:91], s61 offen lds\n\t"
      "s_add_u32 m0, s55, 132096\n\t"
      "s_xor_b32 s61, s47, 1081344\n\t"
      "buffer_load_dwordx4 v116, s[88:91], s61 offen lds\n\t"
      "s_add_u32 m0, s55, 139264\n\t"
      "s_xor_b32 s61, s47, 1310720\n\t"
      "buffer_load_dwordx4 v116, s[88:91], s61 offen lds\n\t"
      "s_add_u32 m0, s55, 140288\n\t"
      "s_xor_b32 s61, s47, 1343488\n\t"
      "buffer_load_dwordx4 v116, s[88:91], s61 offen lds\n\t"
      "s_mov_b64 s[88:89], s[92:93]\n\t"
      "s_add_u32 m0, s55, 81920\n\t"
      "s_xor_b32 s61, s47, 1572864\n\t"
      "buffer_load_dwordx4 v105, s[88:91], s61 offen lds\n\t"
      "s_add_u32 m0, s55, 82944\n\t"
      "s_xor_b32 s61, s47, 1605632\n\t"
      "buffer_load_dwordx4 v105, s[88:91], s61 offen lds\n\t"
      "s_waitcnt vmcnt(27)\n\t"
      "s_barrier\n\t"
      "s_waitcnt lgkmcnt(7)\n\t"
      "v_mfma_f32_16x16x32_bf16 v[16:19], v[0:3], v[32:35], 0\n\t"
      "ds_read_b128 v[80:83], v104 offset:8192\n\t"
      "v_mfma_f32_16x16x32_bf16 v[20:23], v[0:3], v[36:39], 0\n\t"
      "s_mov_b64 s[88:89], s[96:97]\n\t"
      "s_add_u32 m0, s55, 147456\n\t"
      "s_xor_b32 s61, s47, 1572864\n\t"
      "buffer_load_dwordx4 v116, s[88:91], s61 offen lds\n\t"
      "ds_read_b64_tr_b16 a[176:177], v106\n\t"
      "s_waitcnt lgkmcnt(8)\n\t"
      "v_mfma_f32_16x16x32_bf16 v[16:19], v[4:7], v[40:43], v[16:19]\n\t"
      "ds_read_b128 v[84:87], v119 offset:8192\n\t"
      "v_mfma_f32_16x16x32_bf16 v[20:23], v[4:7], v[44:47], v[20:23]\n\t"
      "s_add_u32 m0, s55, 148480\n\t"
      "s_xor_b32 s61, s47, 1605632\n\t"
      "buffer_load_dwordx4 v116, s[88:91], s61 offen lds\n\t"
      "ds_read_b64_tr_b16 a[178:179], v106 offset:1024\n\t"
      "s_waitcnt lgkmcnt(9)\n\t"
      "v_mfma_f32_16x16x32_bf16 v[16:19], v[8:11], v[48:51], v[16:19]\n\t"
      "ds_read_b128 v[88:91], v102 offset:8192\n\t"
      "v_mfma_f32_16x16x32_bf16 v[20:23], v[8:11], v[52:55], v[20:23]\n\t"
      "s_add_u32 m0, s55, 155648\n\t"
      "s_xor_b32 s61, s47, 1835008\n\t"
      "buffer_load_dwordx4 v116, s[88:91], s61 offen lds\n\t"
      "s_add_u32 m0, s55, 156672\n\t"
      "s_xor_b32 s61, s47, 1867776\n\t"
      "buffer_load_dwordx4 v116, s[88:91], s61 offen lds\n\t"
      "s_waitcnt lgkmcnt(9)\n\t"
      "v_mfma_f32_16x16x32_bf16 v[16:19], v[12:15], v[56:59], v[16:19]\n\t"
      "ds_read_b128 v[92:95], v103 offset:8192\n\t"
      "v_mfma_f32_16x16x32_bf16 v[20:23], v[12:15], v[60:63], v[20:23]\n\t"
      "s_mov_b64 s[88:89], s[92:93]\n\t"
      "s_add_u32 m0, s55, 90112\n\t"
      "s_xor_b32 s61, s47, 1835008\n\t"
      "buffer_load_dwordx4 v105, s[88:91], s61 offen lds\n\t"
      "s_add_u32 m0, s55, 91136\n\t"
      "s_xor_b32 s61, s47, 1867776\n\t"
      "buffer_load_dwordx4 v105, s[88:91], s61 offen lds\n\t"
      "s_waitcnt lgkmcnt(9)\n\t"
      "v_mfma_f32_16x16x32_bf16 v[24:27], v[64:67], v[32:35], 0\n\t"
      "ds_read_b128 v[0:3], v104 offset:9216\n\t"
      "v_mfma_f32_16x16x32_bf16 v[28:31], v[64:67], v[36:39], 0\n\t"
      "s_waitcnt lgkmcnt(9)\n\t"
      "v_mfma_f32_16x16x32_bf16 v[24:27], v[68:71], v[40:43], v[24:27]\n\t"
      "ds_read_b128 v[4:7], v119 offset:9216\n\t"
      "v_cvt_pk_bf16_f32 v128, v16, v17\n\t"
      "v_mfma_f32_16x16x32_bf16 v[28:31], v[68:71], v[44:47], v[28:31]\n\t"
      "v_cvt_pk_bf16_f32 v129, v18, v19\n\t"
      "s_waitcnt lgkmcnt(9)\n\t"
      "v_mfma_f32_16x16x32_bf16 v[24:27], v[72:75], v[48:51], v[24:27]\n\t"
      "ds_read_b128 v[8:11], v102 offset:9216\n\t"
      "v_cvt_pk_bf16_f32 v192, v20, v21\n\t"
      "v_mfma_f32_16x16x32_bf16 v[28:31], v[72:75], v[52:55], v[28:31]\n\t"
      "v_cvt_pk_bf16_f32 v193, v22, v23\n\t"
      "s_waitcnt lgkmcnt(9)\n\t"
      "v_mfma_f32_16x16x32_bf16 v[24:27], v[76:79], v[56:59], v[24:27]\n\t"
      "ds_read_b128 v[12:15], v103 offset:9216\n\t"
      "v_mfma_f32_16x16x32_bf16 v[28:31], v[76:79], v[60:63], v[28:31]\n\t"
      "s_waitcnt vmcnt(31)\n\t"
      "s_waitcnt lgkmcnt(6)\n\t"
      "s_barrier\n\t"
      "v_mfma_f32_16x16x32_bf16 v[16:19], v[80:83], v[32:35], 0\n\t"
      "ds_read_b128 v[64:67], v104 offset:16384\n\t"
      "v_mfma_f32_16x16x32_bf16 v[20:23], v[80:83], v[36:39], 0\n\t"
      "s_add_u32 m0, s55, 32768\n\t"
      "s_xor_b32 s61, s47, 2097152\n\t"
      "buffer_load_dwordx4 v105, s[88:91], s61 offen lds\n\t"
      "s_add_u32 m0, s55, 33792\n\t"
      "s_xor_b32 s61, s47, 2129920\n\t"
      "buffer_load_dwordx4 v105, s[88:91], s61 offen lds\n\t"
      "v_mfma_f32_16x16x32_bf16 v[16:19], v[84:87], v[40:43], v[16:19]\n\t"
      "ds_read_b128 v[68:71], v119 offset:16384\n\t"
      "v_cvt_pk_bf16_f32 v130, v24, v25\n\t"
      "v_mfma_f32_16x16x32_bf16 v[20:23], v[84:87], v[44:47], v[20:23]\n\t"
      "v_cvt_pk_bf16_f32 v131, v26, v27\n\t"
      "s_waitcnt lgkmcnt(7)\n\t"
      "v_mfma_f32_16x16x32_bf16 v[16:19], v[88:91], v[48:51], v[16:19]\n\t"
      "ds_read_b128 v[72:75], v102 offset:16384\n\t"
      "v_cvt_pk_bf16_f32 v194, v28, v29\n\t"
      "v_mfma_f32_16x16x32_bf16 v[20:23], v[88:91], v[52:55], v[20:23]\n\t"
      "v_cvt_pk_bf16_f32 v195, v30, v31\n\t"
      "s_waitcnt lgkmcnt(7)\n\t"
      "v_mfma_f32_16x16x32_bf16 v[16:19], v[92:95], v[56:59], v[16:19]\n\t"
      "ds_read_b128 v[76:79], v103 offset:16384\n\t"
      "v_mfma_f32_16x16x32_bf16 v[20:23], v[92:95], v[60:63], v[20:23]\n\t"
      "v_mfma_f32_16x16x32_bf16 a[168:171], v[128:131], v[128:131], 0\n\t"
      "s_waitcnt lgkmcnt(7)\n\t"
      "v_mfma_f32_16x16x32_bf16 v[24:27], v[0:3], v[32:35], 0\n\t"
      "ds_read_b128 v[80:83], v104 offset:17408\n\t"
      "v_mfma_f32_16x16x32_bf16 v[28:31], v[0:3], v[36:39], 0\n\t"
      "s_waitcnt lgkmcnt(7)\n\t"
      "v_mfma_f32_16x16x32_bf16 v[24:27], v[4:7], v[40:43], v[24:27]\n\t"
      "ds_read_b128 v[84:87], v119 offset:17408\n\t"
      "v_cvt_pk_bf16_f32 v132, v16, v17\n\t"
      "v_mfma_f32_16x16x32_bf16 v[28:31], v[4:7], v[44:47], v[28:31]\n\t"
      "v_cvt_pk_bf16_f32 v133, v18, v19\n\t"
      "s_waitcnt lgkmcnt(7)\n\t"
      "v_mfma_f32_16x16x32_bf16 v[24:27], v[8:11], v[48:51], v[24:27]\n\t"
      "ds_read_b128 v[88:91], v102 offset:17408\n\t"
      "v_cvt_pk_bf16_f32 v196, v20, v21\n\t"
      "v_mfma_f32_16x16x32_bf16 v[28:31], v[8:11], v[52:55], v[28:31]\n\t"
      "v_cvt_pk_bf16_f32 v197, v22, v23\n\t"
      "s_waitcnt lgkmcnt(7)\n\t"
      "v_mfma_f32_16x16x32_bf16 v[24:27], v[12:15], v[56:59], v[24:27]\n\t"
      "ds_read_b128 v[92:95], v103 offset:17408\n\t"
      "v_mfma_f32_16x16x32_bf16 v[28:31], v[12:15], v[60:63], v[28:31]\n\t"
      "v_mfma_f32_16x16x32_bf16 a[232:235], v[192:195], v[192:195], 0\n\t"
      "s_waitcnt vmcnt(31)\n\t"
      "s_waitcnt vmcnt(22)\n\t"
      "s_barrier\n\t"
      "s_waitcnt lgkmcnt(7)\n\t"
      "v_mfma_f32_16x16x32_bf16 v[16:19], v[64:67], v[32:35], 0\n\t"
      "ds_read_b128 v[0:3], v104 offset:24576\n\t"
      "v_mfma_f32_16x16x32_bf16 v[20:23], v[64:67], v[36:39], 0\n\t"
      "s_add_u32 m0, s55, 40960\n\t"
      "s_xor_b32 s61, s47, 2359296\n\t"
      "buffer_load_dwordx4 v105, s[88:91], s61 offen lds\n\t"
      "ds_read_b64_tr_b16 a[180:181], v106 offset:16384\n\t"
      "s_waitcnt lgkmcnt(8)\n\t"
      "v_mfma_f32_16x16x32_bf16 v[16:19], v[68:71], v[40:43], v[16:19]\n\t"
      "ds_read_b128 v[4:7], v119 offset:24576\n\t"
      "v_cvt_pk_bf16_f32 v134, v24, v25\n\t"
      "v_mfma_f32_16x16x32_bf16 v[20:23], v[68:71], v[44:47], v[20:23]\n\t"
      "s_add_u32 m0, s55, 41984\n\t"
      "s_xor_b32 s61, s47, 2392064\n\t"
      "buffer_load_dwordx4 v105, s[88:91], s61 offen lds\n\t"
      "ds_read_b64_tr_b16 a[182:183], v106 offset:17408\n\t"
      "v_cvt_pk_bf16_f32 v135, v26, v27\n\t"
      "s_waitcnt lgkmcnt(9)\n\t"
      "v_mfma_f32_16x16x32_bf16 v[16:19], v[72:75], v[48:51], v[16:19]\n\t"
      "ds_read_b128 v[8:11], v102 offset:24576\n\t"
      "v_cvt_pk_bf16_f32 v198, v28, v29\n\t"
      "v_mfma_f32_16x16x32_bf16 v[20:23], v[72:75], v[52:55], v[20:23]\n\t"
      "ds_read_b64_tr_b16 a[0:1], v117\n\t"
      "ds_read_b64_tr_b16 a[2:3], v117 offset:1024\n\t"
      "v_cvt_pk_bf16_f32 v199, v30, v31\n\t"
      "s_waitcnt lgkmcnt(11)\n\t"
      "v_mfma_f32_16x16x32_bf16 v[16:19], v[76:79], v[56:59], v[16:19]\n\t"
      "ds_read_b128 v[12:15], v103 offset:24576\n\t"
      "v_mfma_f32_16x16x32_bf16 v[20:23], v[76:79], v[60:63], v[20:23]\n\t"
      "ds_read_b64_tr_b16 a[64:65], v118\n\t"
      "ds_read_b64_tr_b16 a[66:67], v118 offset:1024\n\t"
      "v_mfma_f32_16x16x32_bf16 a[168:171], v[132:135], v[132:135], a[168:171]\n\t"
      "s_waitcnt lgkmcnt(13)\n\t"
      "v_mfma_f32_16x16x32_bf16 v[24:27], v[80:83], v[32:35], 0\n\t"
      "ds_read_b128 v[64:67], v104 offset:25600\n\t"
      "v_mfma_f32_16x16x32_bf16 v[28:31], v[80:83], v[36:39], 0\n\t"
      "ds_read_b64_tr_b16 a[4:5], v117 offset:8192\n\t"
      "s_waitcnt lgkmcnt(12)\n\t"
      "ds_read_b64_tr_b16 a[6:7], v117 offset:9216\n\t"
      "v_mfma_f32_16x16x32_bf16 v[24:27], v[84:87], v[40:43], v[24:27]\n\t"
      "ds_read_b128 v[68:71], v119 offset:25600\n\t"
      "v_cvt_pk_bf16_f32 v136, v16, v17\n\t"
      "v_mfma_f32_16x16x32_bf16 v[28:31], v[84:87], v[44:47], v[28:31]\n\t"
      "ds_read_b64_tr_b16 a[68:69], v118 offset:8192\n\t"
      "s_waitcnt lgkmcnt(12)\n\t"
      "ds_read_b64_tr_b16 a[70:71], v118 offset:9216\n\t"
      "v_cvt_pk_bf16_f32 v137, v18, v19\n\t"
      "v_mfma_f32_16x16x32_bf16 v[24:27], v[88:91], v[48:51], v[24:27]\n\t"
      "ds_read_b128 v[72:75], v102 offset:25600\n\t"
      "v_cvt_pk_bf16_f32 v200, v20, v21\n\t"
      "v_mfma_f32_16x16x32_bf16 v[28:31], v[88:91], v[52:55], v[28:31]\n\t"
      "v_cvt_pk_bf16_f32 v201, v22, v23\n\t"
      "v_mfma_f32_16x16x32_bf16 v[24:27], v[92:95], v[56:59], v[24:27]\n\t"
      "ds_read_b128 v[76:79], v103 offset:25600\n\t"
      "v_mfma_f32_16x16x32_bf16 v[28:31], v[92:95], v[60:63], v[28:31]\n\t"
      "v_mfma_f32_16x16x32_bf16 a[232:235], v[196:199], v[196:199], a[232:235]\n\t"
      "s_waitcnt vmcnt(22)\n\t"
      "s_waitcnt vmcnt(18)\n\t"
      "s_waitcnt lgkmcnt(2)\n\t"
      "s_barrier\n\t"
      "v_mfma_f32_16x16x32_bf16 v[16:19], v[0:3], v[32:35], 0\n\t"
      "ds_read_b128 v[80:83], v104 offset:32768\n\t"
      "v_mfma_f32_16x16x32_bf16 v[20:23], v[0:3], v[36:39], 0\n\t"
      "s_add_u32 m0, s55, 49152\n\t"
      "s_xor_b32 s61, s47, 2621440\n\t"
      "buffer_load_dwordx4 v105, s[88:91], s61 offen lds\n\t"
      "ds_read_b64_tr_b16 a[8:9], v117 offset:16384\n\t"
      "v_mfma_f32_16x16x32_bf16 v[16:19], v[4:7], v[40:43], v[16:19]\n\t"
      "ds_read_b128 v[84:87], v119 offset:32768\n\t"
      "v_cvt_pk_bf16_f32 v138, v24, v25\n\t"
      "v_mfma_f32_16x16x32_bf16 v[20:23], v[4:7], v[44:47], v[20:23]\n\t"
      "s_add_u32 m0, s55, 50176\n\t"
      "s_xor_b32 s61, s47, 2654208\n\t"
      "buffer_load_dwordx4 v105, s[88:91], s61 offen lds\n\t"
      "ds_read_b64_tr_b16 a[10:11], v117 offset:17408\n\t"
      "v_cvt_pk_bf16_f32 v139, v26, v27\n\t"
      "v_mfma_f32_16x16x32_bf16 v[16:19], v[8:11], v[48:51], v[16:19]\n\t"
      "ds_read_b128 v[88:91], v102 offset:32768\n\t"
      "v_cvt_pk_bf16_f32 v202, v28, v29\n\t"
      "v_mfma_f32_16x16x32_bf16 v[20:23], v[8:11], v[52:55], v[20:23]\n\t"
      "s_mov_b64 s[88:89], s[96:97]\n\t"
      "s_add_u32 m0, s55, 98304\n\t"
      "s_xor_b32 s61, s47, 2097152\n\t"
      "buffer_load_dwordx4 v116, s[88:91], s61 offen lds\n\t"
      "ds_read_b64_tr_b16 a[72:73], v118 offset:16384\n\t"
      "v_cvt_pk_bf16_f32 v203, v30, v31\n\t"
      "v_mfma_f32_16x16x32_bf16 v[16:19], v[12:15], v[56:59], v[16:19]\n\t"
      "ds_read_b128 v[92:95], v103 offset:32768\n\t"
      "v_mfma_f32_16x16x32_bf16 v[20:23], v[12:15], v[60:63], v[20:23]\n\t"
      "s_add_u32 m0, s55, 99328\n\t"
      "s_xor_b32 s61, s47, 2129920\n\t"
      "buffer_load_dwordx4 v116, s[88:91], s61 offen lds\n\t"
      "ds_read_b64_tr_b16 a[74:75], v118 offset:17408\n\t"
      "v_mfma_f32_16x16x32_bf16 a[168:171], v[136:139], v[136:139], a[168:171]\n\t"
      "v_mfma_f32_16x16x32_bf16 v[24:27], v[64:67], v[32:35], 0\n\t"
      "ds_read_b128 v[0:3], v104 offset:33792\n\t"
      "v_mfma_f32_16x16x32_bf16 v[28:31], v[64:67], v[36:39], 0\n\t"
      "s_add_u32 m0, s55, 106496\n\t"
      "s_xor_b32 s61, s47, 2359296\n\t"
      "buffer_load_dwordx4 v116, s[88:91], s61 offen lds\n\t"
      "ds_read_b64_tr_b16 a[12:13], v117 offset:24576\n\t"
      "v_mfma_f32_16x16x32_bf16 v[24:27], v[68:71], v[40:43], v[24:27]\n\t"
      "ds_read_b128 v[4:7], v119 offset:33792\n\t"
      "v_cvt_pk_bf16_f32 v140, v16, v17\n\t"
      "v_mfma_f32_16x16x32_bf16 v[28:31], v[68:71], v[44:47], v[28:31]\n\t"
      "s_add_u32 m0, s55, 107520\n\t"
      "s_xor_b32 s61, s47, 2392064\n\t"
      "buffer_load_dwordx4 v116, s[88:91], s61 offen lds\n\t"
      "ds_read_b64_tr_b16 a[14:15], v117 offset:25600\n\t"
      "v_cvt_pk_bf16_f32 v141, v18, v19\n\t"
      "s_waitcnt lgkmcnt(13)\n\t"
      "v_mfma_f32_16x16x32_bf16 v[24:27], v[72:75], v[48:51], v[24:27]\n\t"
      "ds_read_b128 v[8:11], v102 offset:33792\n\t"
      "v_cvt_pk_bf16_f32 v204, v20, v21\n\t"
      "v_mfma_f32_16x16x32_bf16 v[28:31], v[72:75], v[52:55], v[28:31]\n\t"
      "ds_read_b64_tr_b16 a[76:77], v118 offset:24576\n\t"
      "s_waitcnt lgkmcnt(12)\n\t"
      "ds_read_b64_tr_b16 a[78:79], v118 offset:25600\n\t"
      "v_cvt_pk_bf16_f32 v205, v22, v23\n\t"
      "v_mfma_f32_16x16x32_bf16 v[24:27], v[76:79], v[56:59], v[24:27]\n\t"
      "ds_read_b128 v[12:15], v103 offset:33792\n\t"
      "v_mfma_f32_16x16x32_bf16 v[28:31], v[76:79], v[60:63], v[28:31]\n\t"
      "v_mfma_f32_16x16x32_bf16 a[232:235], v[200:203], v[200:203], a[232:235]\n\t"
      "s_waitcnt vmcnt(22)\n\t"
      "s_waitcnt vmcnt(18)\n\t"
      "s_waitcnt lgkmcnt(1)\n\t"
      "s_barrier\n\t"
      "v_mfma_f32_16x16x32_bf16 v[16:19], v[80:83], v[32:35], 0\n\t"
      "ds_read_b128 v[64:67], v104 offset:40960\n\t"
      "v_mfma_f32_16x16x32_bf16 v[20:23], v[80:83], v[36:39], 0\n\t"
      "s_mov_b64 s[88:89], s[92:93]\n\t"
      "s_add_u32 m0, s55, 57344\n\t"
      "s_xor_b32 s61, s47, 2883584\n\t"
      "buffer_load_dwordx4 v105, s[88:91], s61 offen lds\n\t"
      "ds_read_b64_tr_b16 a[184:185], v106 offset:32768\n\t"
      "v_mfma_f32_16x16x32_bf16 v[16:19], v[84:87], v[40:43], v[16:19]\n\t"
      "ds_read_b128 v[68:71], v119 offset:40960\n\t"
      "v_cvt_pk_bf16_f32 v142, v24, v25\n\t"
      "v_mfma_f32_16x16x32_bf16 v[20:23], v[84:87], v[44:47], v[20:23]\n\t"
      "s_add_u32 m0, s55, 58368\n\t"
      "s_xor_b32 s61, s47, 2916352\n\t"
      "buffer_load_dwordx4 v105, s[88:91], s61 offen lds\n\t"
      "ds_read_b64_tr_b16 a[186:187], v106 offset:33792\n\t"
      "v_cvt_pk_bf16_f32 v143, v26, v27\n\t"
      "v_mfma_f32_16x16x32_bf16 v[16:19], v[88:91], v[48:51], v[16:19]\n\t"
      "ds_read_b128 v[72:75], v102 offset:40960\n\t"
      "v_cvt_pk_bf16_f32 v206, v28, v29\n\t"
      "v_mfma_f32_16x16x32_bf16 v[20:23], v[88:91], v[52:55], v[20:23]\n\t"
      "s_mov_b64 s[88:89], s[96:97]\n\t"
      "s_add_u32 m0, s55, 114688\n\t"
      "s_xor_b32 s61, s47, 2621440\n\t"
      "buffer_load_dwordx4 v116, s[88:91], s61 offen lds\n\t"
      "ds_read_b64_tr_b16 a[16:17], v117 offset:32768\n\t"
      "v_cvt_pk_bf16_f32 v207, v30, v31\n\t"
      "v_mfma_f32_16x16x32_bf16 v[16:19], v[92:95], v[56:59], v[16:19]\n\t"
      "ds_read_b128 v[76:79], v103 offset:40960\n\t"
      "v_mfma_f32_16x16x32_bf16 v[20:23], v[92:95], v[60:63], v[20:23]\n\t"
      "s_add_u32 m0, s55, 115712\n\t"
      "s_xor_b32 s61, s47, 2654208\n\t"
      "buffer_load_dwordx4 v116, s[88:91], s61 offen lds\n\t"
      "ds_read_b64_tr_b16 a[18:19], v117 offset:33792\n\t"
      "v_mfma_f32_16x16x32_bf16 a[168:171], v[140:143], v[140:143], a[168:171]\n\t"
      "v_mfma_f32_16x16x32_bf16 v[24:27], v[0:3], v[32:35], 0\n\t"
      "ds_read_b128 v[80:83], v104 offset:41984\n\t"
      "v_mfma_f32_16x16x32_bf16 v[28:31], v[0:3], v[36:39], 0\n\t"
      "s_add_u32 m0, s55, 122880\n\t"
      "s_xor_b32 s61, s47, 2883584\n\t"
      "buffer_load_dwordx4 v116, s[88:91], s61 offen lds\n\t"
      "ds_read_b64_tr_b16 a[80:81], v118 offset:32768\n\t"
      "v_mfma_f32_16x16x32_bf16 v[24:27], v[4:7], v[40:43], v[24:27]\n\t"
      "ds_read_b128 v[84:87], v119 offset:41984\n\t"
      "v_cvt_pk_bf16_f32 v144, v16, v17\n\t"
      "v_mfma_f32_16x16x32_bf16 v[28:31], v[4:7], v[44:47], v[28:31]\n\t"
      "s_add_u32 m0, s55, 123904\n\t"
      "s_xor_b32 s61, s47, 2916352\n\t"
      "buffer_load_dwordx4 v116, s[88:91], s61 offen lds\n\t"
      "ds_read_b64_tr_b16 a[82:83], v118 offset:33792\n\t"
      "v_cvt_pk_bf16_f32 v145, v18, v19\n\t"
      "v_mfma_f32_16x16x32_bf16 v[24:27], v[8:11], v[48:51], v[24:27]\n\t"
      "ds_read_b128 v[88:91], v102 offset:41984\n\t"
      "v_cvt_pk_bf16_f32 v208, v20, v21\n\t"
      "v_mfma_f32_16x16x32_bf16 v[28:31], v[8:11], v[52:55], v[28:31]\n\t"
      "ds_read_b64_tr_b16 a[20:21], v117 offset:40960\n\t"
      "s_waitcnt lgkmcnt(12)\n\t"
      "ds_read_b64_tr_b16 a[22:23], v117 offset:41984\n\t"
      "v_cvt_pk_bf16_f32 v209, v22, v23\n\t"
      "v_mfma_f32_16x16x32_bf16 v[24:27], v[12:15], v[56:59], v[24:27]\n\t"
      "ds_read_b128 v[92:95], v103 offset:41984\n\t"
      "v_mfma_f32_16x16x32_bf16 v[28:31], v[12:15], v[60:63], v[28:31]\n\t"
      "ds_read_b64_tr_b16 a[84:85], v118 offset:40960\n\t"
      "s_waitcnt lgkmcnt(12)\n\t"
      "ds_read_b64_tr_b16 a[86:87], v118 offset:41984\n\t"
      "v_mfma_f32_16x16x32_bf16 a[232:235], v[204:207], v[204:207], a[232:235]\n\t"
      "s_waitcnt vmcnt(22)\n\t"
      "s_waitcnt vmcnt(18)\n\t"
      "s_waitcnt lgkmcnt(0)\n\t"
      "s_barrier\n\t"
      "v_mfma_f32_16x16x32_bf16 v[16:19], v[64:67], v[32:35], 0\n\t"
      "ds_read_b128 v[0:3], v104 offset:49152\n\t"
      "v_mfma_f32_16x16x32_bf16 v[20:23], v[64:67], v[36:39], 0\n\t"
      "s_mov_b64 s[88:89], s[92:93]\n\t"
      "s_add_u32 m0, s55, 65536\n\t"
      "s_xor_b32 s61, s47, 3145728\n\t"
      "buffer_load_dwordx4 v105, s[88:91], s61 offen lds\n\t"
      "ds_read_b64_tr_b16 a[24:25], v117 offset:49152\n\t"
      "v_mfma_f32_16x16x32_bf16 v[16:19], v[68:71], v[40:43], v[16:19]\n\t"
      "ds_read_b128 v[4:7], v119 offset:49152\n\t"
      "v_cvt_pk_bf16_f32 v146, v24, v25\n\t"
      "v_mfma_f32_16x16x32_bf16 v[20:23], v[68:71], v[44:47], v[20:23]\n\t"
      "s_add_u32 m0, s55, 66560\n\t"
      "s_xor_b32 s61, s47, 3178496\n\t"
      "buffer_load_dwordx4 v105, s[88:91], s61 offen lds\n\t"
      "ds_read_b64_tr_b16 a[26:27], v117 offset:50176\n\t"
      "v_cvt_pk_bf16_f32 v147, v26, v27\n\t"
      "v_mfma_f32_16x16x32_bf16 v[16:19], v[72:75], v[48:51], v[16:19]\n\t"
      "ds_read_b128 v[8:11], v102 offset:49152\n\t"
      "v_cvt_pk_bf16_f32 v210, v28, v29\n\t"
      "v_mfma_f32_16x16x32_bf16 v[20:23], v[72:75], v[52:55], v[20:23]\n\t"
      "s_mov_b64 s[88:89], s[96:97]\n\t"
      "s_add_u32 m0, s55, 131072\n\t"
      "s_xor_b32 s61, s47, 3145728\n\t"
      "buffer_load_dwordx4 v116, s[88:91], s61 offen lds\n\t"
      "ds_read_b64_tr_b16 a[88:89], v118 offset:49152\n\t"
      "v_cvt_pk_bf16_f32 v211, v30, v31\n\t"
      "v_mfma_f32_16x16x32_bf16 v[16:19], v[76:79], v[56:59], v[16:19]\n\t"
      "ds_read_b128 v[12:15], v103 offset:49152\n\t"
      "v_mfma_f32_16x16x32_bf16 v[20:23], v[76:79], v[60:63], v[20:23]\n\t"
      "s_add_u32 m0, s55, 132096\n\t"
      "s_xor_b32 s61, s47, 3178496\n\t"
      "buffer_load_dwordx4 v116, s[88:91], s61 offen lds\n\t"
      "ds_read_b64_tr_b16 a[90:91], v118 offset:50176\n\t"
      "v_mfma_f32_16x16x32_bf16 a[168:171], v[144:147], v[144:147], a[168:171]\n\t"
      "v_mfma_f32_16x16x32_bf16 v[24:27], v[80:83], v[32:35], 0\n\t"
      "ds_read_b128 v[64:67], v104 offset:50176\n\t"
      "v_mfma_f32_16x16x32_bf16 v[28:31], v[80:83], v[36:39], 0\n\t"
      "s_add_u32 m0, s55, 139264\n\t"
      "s_xor_b32 s61, s47, 3407872\n\t"
      "buffer_load_dwordx4 v116, s[88:91], s61 offen lds\n\t"
      "ds_read_b64_tr_b16 a[28:29], v117 offset:57344\n\t"
      "v_mfma_f32_16x16x32_bf16 v[24:27], v[84:87], v[40:43], v[24:27]\n\t"
      "ds_read_b128 v[68:71], v119 offset:50176\n\t"
      "v_cvt_pk_bf16_f32 v148, v16, v17\n\t"
      "v_mfma_f32_16x16x32_bf16 v[28:31], v[84:87], v[44:47], v[28:31]\n\t"
      "s_add_u32 m0, s55, 140288\n\t"
      "s_xor_b32 s61, s47, 3440640\n\t"
      "buffer_load_dwordx4 v116, s[88:91], s61 offen lds\n\t"
      "ds_read_b64_tr_b16 a[30:31], v117 offset:58368\n\t"
      "v_cvt_pk_bf16_f32 v149, v18, v19\n\t"
      "v_mfma_f32_16x16x32_bf16 v[24:27], v[88:91], v[48:51], v[24:27]\n\t"
      "ds_read_b128 v[72:75], v102 offset:50176\n\t"
      "v_cvt_pk_bf16_f32 v212, v20, v21\n\t"
      "v_mfma_f32_16x16x32_bf16 v[28:31], v[88:91], v[52:55], v[28:31]\n\t"
      "ds_read_b64_tr_b16 a[92:93], v118 offset:57344\n\t"
      "ds_read_b64_tr_b16 a[94:95], v118 offset:58368\n\t"
      "v_cvt_pk_bf16_f32 v213, v22, v23\n\t"
      "v_mfma_f32_16x16x32_bf16 v[24:27], v[92:95], v[56:59], v[24:27]\n\t"
      "s_waitcnt lgkmcnt(12)\n\t"
      "ds_read_b128 v[76:79], v103 offset:50176\n\t"
      "v_mfma_f32_16x16x32_bf16 v[28:31], v[92:95], v[60:63], v[28:31]\n\t"
      "v_mfma_f32_16x16x32_bf16 a[232:235], v[208:211], v[208:211], a[232:235]\n\t"
      "s_waitcnt vmcnt(22)\n\t"
      "s_waitcnt vmcnt(12)\n\t"
      "s_waitcnt lgkmcnt(1)\n\t"
      "s_barrier\n\t"
      "v_mfma_f32_16x16x32_bf16 v[16:19], v[0:3], v[32:35], 0\n\t"
      "ds_read_b128 v[80:83], v104 offset:57344\n\t"
      "v_mfma_f32_16x16x32_bf16 v[20:23], v[0:3], v[36:39], 0\n\t"
      "s_mov_b64 s[88:89], s[92:93]\n\t"
      "s_add_u32 m0, s55, 73728\n\t"
      "s_xor_b32 s61, s47, 3407872\n\t"
      "buffer_load_dwordx4 v105, s[88:91], s61 offen lds\n\t"
      "ds_read_b64_tr_b16 a[188:189], v106 offset:49152\n\t"
      "v_mfma_f32_16x16x32_bf16 v[16:19], v[4:7], v[40:43], v[16:19]\n\t"
      "ds_read_b128 v[84:87], v119 offset:57344\n\t"
      "v_cvt_pk_bf16_f32 v150, v24, v25\n\t"
      "v_mfma_f32_16x16x32_bf16 v[20:23], v[4:7], v[44:47], v[20:23]\n\t"
      "s_add_u32 m0, s55, 74752\n\t"
      "s_xor_b32 s61, s47, 3440640\n\t"
      "buffer_load_dwordx4 v105, s[88:91], s61 offen lds\n\t"
      "ds_read_b64_tr_b16 a[190:191], v106 offset:50176\n\t"
      "v_cvt_pk_bf16_f32 v151, v26, v27\n\t"
      "v_mfma_f32_16x16x32_bf16 v[16:19], v[8:11], v[48:51], v[16:19]\n\t"
      "ds_read_b128 v[88:91], v102 offset:57344\n\t"
      "v_cvt_pk_bf16_f32 v214, v28, v29\n\t"
      "v_mfma_f32_16x16x32_bf16 v[20:23], v[8:11], v[52:55], v[20:23]\n\t"
      "s_mov_b64 s[88:89], s[96:97]\n\t"
      "s_add_u32 m0, s55, 147456\n\t"
      "s_xor_b32 s61, s47, 3670016\n\t"
      "buffer_load_dwordx4 v116, s[88:91], s61 offen lds\n\t"
      "ds_read_b64_tr_b16 a[32:33], v117\n\t"
      "v_cvt_pk_bf16_f32 v215, v30, v31\n\t"
      "v_mfma_f32_16x16x32_bf16 v[16:19], v[12:15], v[56:59], v[16:19]\n\t"
      "ds_read_b128 v[92:95], v103 offset:57344\n\t"
      "v_mfma_f32_16x16x32_bf16 v[20:23], v[12:15], v[60:63], v[20:23]\n\t"
      "s_add_u32 m0, s55, 148480\n\t"
      "s_xor_b32 s61, s47, 3702784\n\t"
      "buffer_load_dwordx4 v116, s[88:91], s61 offen lds\n\t"
      "ds_read_b64_tr_b16 a[34:35], v117 offset:1024\n\t"
      "v_mfma_f32_16x16x32_bf16 a[168:171], v[148:151], v[148:151], a[168:171]\n\t"
      "v_readfirstlane_b32 s53, v126\n\t"
      "v_lshl_or_b32 v96, v96, 16, 1\n\t"
      "v_lshl_or_b32 v97, v97, 16, 1\n\t"
      "v_lshl_or_b32 v98, v98, 16, 1\n\t"
      "v_lshl_or_b32 v99, v99, 16, 1\n\t"
      "s_mov_b64 exec, 0xff\n\t"
      "buffer_store_dwordx4 v[96:99], v107, s[36:39], s43 offen sc1\n\t"
      "s_mov_b64 exec, -1\n\t"
      "s_mul_i32 s53, s53, 0x9e3779b1\n\t"
      "s_add_u32 s53, s53, 0x7f4a7c15\n\t"
      "v_mov_b32 v96, 0x20000000\n\t"
      "v_mov_b32 v97, 0x20000000\n\t"
      "v_mov_b32 v98, 0x20000000\n\t"
      "v_mov_b32 v99, 0x20000000\n\t"
      "s_mov_b32 s61, 8192\n\t"
      "buffer_store_dwordx4 v[96:99], v101, s[36:39], 0 offen sc1\n\t"
      "buffer_store_dwordx4 v[96:99], v101, s[36:39], s61 offen sc1\n\t"
      "s_nop 1\n\t"
      "v_mov_b32 v96, v111\n\t"
      "v_mov_b32 v97, v111\n\t"
      "v_mov_b32 v98, v111\n\t"
      "v_mov_b32 v99, v111\n\t"
      "buffer_store_dwordx4 v[96:99], v108, s[36:39], 0 offen sc1\n\t"
      "v_mfma_f32_16x16x32_bf16 v[24:27], v[64:67], v[32:35], 0\n\t"
      "ds_read_b128 v[0:3], v104 offset:58368\n\t"
      "v_mfma_f32_16x16x32_bf16 v[28:31], v[64:67], v[36:39], 0\n\t"
      "s_add_u32 m0, s55, 155648\n\t"
      "s_xor_b32 s61, s47, 3932160\n\t"
      "buffer_load_dwordx4 v116, s[88:91], s61 offen lds\n\t"
      "ds_read_b64_tr_b16 a[96:97], v118\n\t"
      "v_mfma_f32_16x16x32_bf16 v[24:27], v[68:71], v[40:43], v[24:27]\n\t"
      "ds_read_b128 v[4:7], v119 offset:58368\n\t"
      "v_cvt_pk_bf16_f32 v152, v16, v17\n\t"
      "v_mfma_f32_16x16x32_bf16 v[28:31], v[68:71], v[44:47], v[28:31]\n\t"
      "s_add_u32 m0, s55, 156672\n\t"
      "s_xor_b32 s61, s47, 3964928\n\t"
      "buffer_load_dwordx4 v116, s[88:91], s61 offen lds\n\t"
      "ds_read_b64_tr_b16 a[98:99], v118 offset:1024\n\t"
      "v_cvt_pk_bf16_f32 v153, v18, v19\n\t"
      "v_mfma_f32_16x16x32_bf16 v[24:27], v[72:75], v[48:51], v[24:27]\n\t"
      "ds_read_b128 v[8:11], v102 offset:58368\n\t"
      "v_cvt_pk_bf16_f32 v216, v20, v21\n\t"
      "v_mfma_f32_16x16x32_bf16 v[28:31], v[72:75], v[52:55], v[28:31]\n\t"
      "ds_read_b64_tr_b16 a[36:37], v117 offset:8192\n\t"
      "s_waitcnt lgkmcnt(12)\n\t"
      "ds_read_b64_tr_b16 a[38:39], v117 offset:9216\n\t"
      "v_cvt_pk_bf16_f32 v217, v22, v23\n\t"
      "v_mfma_f32_16x16x32_bf16 v[24:27], v[76:79], v[56:59], v[24:27]\n\t"
      "ds_read_b128 v[12:15], v103 offset:58368\n\t"
      "v_mfma_f32_16x16x32_bf16 v[28:31], v[76:79], v[60:63], v[28:31]\n\t"
      "ds_read_b64_tr_b16 a[100:101], v118 offset:8192\n\t"
      "s_waitcnt lgkmcnt(12)\n\t"
      "ds_read_b64_tr_b16 a[102:103], v118 offset:9216\n\t"
      "v_mfma_f32_16x16x32_bf16 a[232:235], v[212:215], v[212:215], a[232:235]\n\t"
      "s_waitcnt vmcnt(16)\n\t"
      "s_waitcnt lgkmcnt(0)\n\t"
      "s_barrier\n\t"
      "v_mfma_f32_16x16x32_bf16 v[16:19], v[80:83], v[32:35], 0\n\t"
      "ds_read_b128 v[64:67], v104\n\t"
      "v_mfma_f32_16x16x32_bf16 v[20:23], v[80:83], v[36:39], 0\n\t"
      "s_mov_b64 s[88:89], s[92:93]\n\t"
      "s_add_u32 m0, s55, 81920\n\t"
      "s_xor_b32 s61, s47, 3670016\n\t"
      "buffer_load_dwordx4 v105, s[88:91], s61 offen lds\n\t"
      "ds_read_b64_tr_b16 a[40:41], v117 offset:16384\n\t"
      "v_mfma_f32_16x16x32_bf16 v[16:19], v[84:87], v[40:43], v[16:19]\n\t"
      "ds_read_b128 v[68:71], v119\n\t"
      "v_cvt_pk_bf16_f32 v154, v24, v25\n\t"
      "v_mfma_f32_16x16x32_bf16 v[20:23], v[84:87], v[44:47], v[20:23]\n\t"
      "s_add_u32 m0, s55, 82944\n\t"
      "s_xor_b32 s61, s47, 3702784\n\t"
      "buffer_load_dwordx4 v105, s[88:91], s61 offen lds\n\t"
      "ds_read_b64_tr_b16 a[42:43], v117 offset:17408\n\t"
      "v_cvt_pk_bf16_f32 v155, v26, v27\n\t"
      "v_mfma_f32_16x16x32_bf16 v[16:19], v[88:91], v[48:51], v[16:19]\n\t"
      "ds_read_b128 v[72:75], v102\n\t"
      "v_cvt_pk_bf16_f32 v218, v28, v29\n\t"
      "v_mfma_f32_16x16x32_bf16 v[20:23], v[88:91], v[52:55], v[20:23]\n\t"
      "s_mov_b64 s[88:89], s[98:99]\n\t"
      "s_add_u32 m0, s55, 98304\n\t"
      "s_xor_b32 s61, s47, 0\n\t"
      "buffer_load_dwordx4 v116, s[88:91], s61 offen lds\n\t"
      "ds_read_b64_tr_b16 a[104:105], v118 offset:16384\n\t"
      "v_cvt_pk_bf16_f32 v219, v30, v31\n\t"
      "v_mfma_f32_16x16x32_bf16 v[16:19], v[92:95], v[56:59], v[16:19]\n\t"
      "ds_read_b128 v[76:79], v103\n\t"
      "v_mfma_f32_16x16x32_bf16 v[20:23], v[92:95], v[60:63], v[20:23]\n\t"
      "s_add_u32 m0, s55, 99328\n\t"
      "s_xor_b32 s61, s47, 32768\n\t"
      "buffer_load_dwordx4 v116, s[88:91], s61 offen lds\n\t"
      "ds_read_b64_tr_b16 a[106:107], v118 offset:17408\n\t"
      "v_mfma_f32_16x16x32_bf16 a[168:171], v[152:155], v[152:155], a[168:171]\n\t"
      "v_mfma_f32_16x16x32_bf16 v[24:27], v[0:3], v[32:35], 0\n\t"
      "ds_read_b128 v[80:83], v104 offset:1024\n\t"
      "v_mfma_f32_16x16x32_bf16 v[28:31], v[0:3], v[36:39], 0\n\t"
      "s_add_u32 m0, s55, 106496\n\t"
      "s_xor_b32 s61, s47, 262144\n\t"
      "buffer_load_dwordx4 v116, s[88:91], s61 offen lds\n\t"
      "ds_read_b64_tr_b16 a[44:45], v117 offset:24576\n\t"
      "v_mfma_f32_16x16x32_bf16 v[24:27], v[4:7], v[40:43], v[24:27]\n\t"
      "ds_read_b128 v[84:87], v119 offset:1024\n\t"
      "v_cvt_pk_bf16_f32 v156, v16, v17\n\t"
      "v_mfma_f32_16x16x32_bf16 v[28:31], v[4:7], v[44:47], v[28:31]\n\t"
      "s_add_u32 m0, s55, 107520\n\t"
      "s_xor_b32 s61, s47, 294912\n\t"
      "buffer_load_dwordx4 v116, s[88:91], s61 offen lds\n\t"
      "ds_read_b64_tr_b16 a[46:47], v117 offset:25600\n\t"
      "v_cvt_pk_bf16_f32 v157, v18, v19\n\t"
      "v_mfma_f32_16x16x32_bf16 v[24:27], v[8:11], v[48:51], v[24:27]\n\t"
      "ds_read_b128 v[88:91], v102 offset:1024\n\t"
      "v_cvt_pk_bf16_f32 v220, v20, v21\n\t"
      "v_mfma_f32_16x16x32_bf16 v[28:31], v[8:11], v[52:55], v[28:31]\n\t"
      "ds_read_b64_tr_b16 a[108:109], v118 offset:24576\n\t"
      "ds_read_b64_tr_b16 a[110:111], v118 offset:25600\n\t"
      "v_cvt_pk_bf16_f32 v221, v22, v23\n\t"
      "v_mfma_f32_16x16x32_bf16 v[24:27], v[12:15], v[56:59], v[24:27]\n\t"
      "s_waitcnt lgkmcnt(12)\n\t"
      "ds_read_b128 v[92:95], v103 offset:1024\n\t"
      "v_mfma_f32_16x16x32_bf16 v[28:31], v[12:15], v[60:63], v[28:31]\n\t"
      "v_mfma_f32_16x16x32_bf16 a[232:235], v[216:219], v[216:219], a[232:235]\n\t"
      "s_waitcnt vmcnt(16)\n\t"
      "s_waitcnt lgkmcnt(1)\n\t"
      "s_barrier\n\t"
      "v_mfma_f32_16x16x32_bf16 v[16:19], v[64:67], v[32:35], 0\n\t"
      "ds_read_b128 v[0:3], v104 offset:8192\n\t"
      "v_mfma_f32_16x16x32_bf16 v[20:23], v[64:67], v[36:39], 0\n\t"
      "s_mov_b64 s[88:89], s[92:93]\n\t"
      "s_add_u32 m0, s55, 90112\n\t"
      "s_xor_b32 s61, s47, 3932160\n\t"
      "buffer_load_dwordx4 v105, s[88:91], s61 offen lds\n\t"
      "ds_read_b64_tr_b16 a[240:241], v106\n\t"
      "v_mfma_f32_16x16x32_bf16 v[16:19], v[68:71], v[40:43], v[16:19]\n\t"
      "ds_read_b128 v[4:7], v119 offset:8192\n\t"
      "v_cvt_pk_bf16_f32 v158, v24, v25\n\t"
      "v_mfma_f32_16x16x32_bf16 v[20:23], v[68:71], v[44:47], v[20:23]\n\t"
      "s_add_u32 m0, s55, 91136\n\t"
      "s_xor_b32 s61, s47, 3964928\n\t"
      "buffer_load_dwordx4 v105, s[88:91], s61 offen lds\n\t"
      "ds_read_b64_tr_b16 a[242:243], v106 offset:1024\n\t"
      "v_cvt_pk_bf16_f32 v159, v26, v27\n\t"
      "v_mfma_f32_16x16x32_bf16 v[16:19], v[72:75], v[48:51], v[16:19]\n\t"
      "ds_read_b128 v[8:11], v102 offset:8192\n\t"
      "v_cvt_pk_bf16_f32 v222, v28, v29\n\t"
      "v_mfma_f32_16x16x32_bf16 v[20:23], v[72:75], v[52:55], v[20:23]\n\t"
      "s_mov_b64 s[88:89], s[98:99]\n\t"
      "s_add_u32 m0, s55, 114688\n\t"
      "s_xor_b32 s61, s47, 524288\n\t"
      "buffer_load_dwordx4 v116, s[88:91], s61 offen lds\n\t"
      "ds_read_b64_tr_b16 a[48:49], v117 offset:32768\n\t"
      "v_cvt_pk_bf16_f32 v223, v30, v31\n\t"
      "v_mfma_f32_16x16x32_bf16 v[16:19], v[76:79], v[56:59], v[16:19]\n\t"
      "ds_read_b128 v[12:15], v103 offset:8192\n\t"
      "v_mfma_f32_16x16x32_bf16 v[20:23], v[76:79], v[60:63], v[20:23]\n\t"
      "s_add_u32 m0, s55, 115712\n\t"
      "s_xor_b32 s61, s47, 557056\n\t"
      "buffer_load_dwordx4 v116, s[88:91], s61 offen lds\n\t"
      "ds_read_b64_tr_b16 a[50:51], v117 offset:33792\n\t"
      "v_mfma_f32_16x16x32_bf16 a[168:171], v[156:159], v[156:159], a[168:171]\n\t"
      "v_mfma_f32_16x16x32_bf16 v[24:27], v[80:83], v[32:35], 0\n\t"
      "ds_read_b128 v[64:67], v104 offset:9216\n\t"
      "v_mfma_f32_16x16x32_bf16 v[28:31], v[80:83], v[36:39], 0\n\t"
      "s_add_u32 m0, s55, 122880\n\t"
      "s_xor_b32 s61, s47, 786432\n\t"
      "buffer_load_dwordx4 v116, s[88:91], s61 offen lds\n\t"
      "ds_read_b64_tr_b16 a[112:113], v118 offset:32768\n\t"
      "v_mfma_f32_16x16x32_bf16 v[24:27], v[84:87], v[40:43], v[24:27]\n\t"
      "ds_read_b128 v[68:71], v119 offset:9216\n\t"
      "v_cvt_pk_bf16_f32 v160, v16, v17\n\t"
      "v_mfma_f32_16x16x32_bf16 v[28:31], v[84:87], v[44:47], v[28:31]\n\t"
      "s_add_u32 m0, s55, 123904\n\t"
      "s_xor_b32 s61, s47, 819200\n\t"
      "buffer_load_dwordx4 v116, s[88:91], s61 offen lds\n\t"
      "ds_read_b64_tr_b16 a[114:115], v118 offset:33792\n\t"
      "v_cvt_pk_bf16_f32 v161, v18, v19\n\t"
      "v_mfma_f32_16x16x32_bf16 v[24:27], v[88:91], v[48:51], v[24:27]\n\t"
      "ds_read_b128 v[72:75], v102 offset:9216\n\t"
      "v_cvt_pk_bf16_f32 v224, v20, v21\n\t"
      "v_mfma_f32_16x16x32_bf16 v[28:31], v[88:91], v[52:55], v[28:31]\n\t"
      "ds_read_b64_tr_b16 a[52:53], v117 offset:40960\n\t"
      "s_waitcnt lgkmcnt(12)\n\t"
      "ds_read_b64_tr_b16 a[54:55], v117 offset:41984\n\t"
      "v_cvt_pk_bf16_f32 v225, v22, v23\n\t"
      "v_mfma_f32_16x16x32_bf16 v[24:27], v[92:95], v[56:59], v[24:27]\n\t"
      "ds_read_b128 v[76:79], v103 offset:9216\n\t"
      "v_mfma_f32_16x16x32_bf16 v[28:31], v[92:95], v[60:63], v[28:31]\n\t"
      "ds_read_b64_tr_b16 a[116:117], v118 offset:40960\n\t"
      "s_waitcnt lgkmcnt(12)\n\t"
      "ds_read_b64_tr_b16 a[118:119], v118 offset:41984\n\t"
      "v_mfma_f32_16x16x32_bf16 a[232:235], v[220:223], v[220:223], a[232:235]\n\t"
      "s_waitcnt vmcnt(12)\n\t"
      "s_waitcnt lgkmcnt(0)\n\t"
      "s_barrier\n\t"
      "v_mfma_f32_16x16x32_bf16 v[16:19], v[0:3], v[32:35], 0\n\t"
      "ds_read_b128 v[80:83], v104 offset:16384\n\t"
      "v_mfma_f32_16x16x32_bf16 v[20:23], v[0:3], v[36:39], 0\n\t"
      "s_add_u32 m0, s55, 131072\n\t"
      "s_xor_b32 s61, s47, 1048576\n\t"
      "buffer_load_dwordx4 v116, s[88:91], s61 offen lds\n\t"
      "ds_read_b64_tr_b16 a[56:57], v117 offset:49152\n\t"
      "v_mfma_f32_16x16x32_bf16 v[16:19], v[4:7], v[40:43], v[16:19]\n\t"
      "ds_read_b128 v[84:87], v119 offset:16384\n\t"
      "v_cvt_pk_bf16_f32 v162, v24, v25\n\t"
      "v_mfma_f32_16x16x32_bf16 v[20:23], v[4:7], v[44:47], v[20:23]\n\t"
      "s_add_u32 m0, s55, 132096\n\t"
      "s_xor_b32 s61, s47, 1081344\n\t"
      "buffer_load_dwordx4 v116, s[88:91], s61 offen lds\n\t"
      "ds_read_b64_tr_b16 a[58:59], v117 offset:50176\n\t"
      "v_cvt_pk_bf16_f32 v163, v26, v27\n\t"
      "v_mfma_f32_16x16x32_bf16 v[16:19], v[8:11], v[48:51], v[16:19]\n\t"
      "ds_read_b128 v[88:91], v102 offset:16384\n\t"
      "v_cvt_pk_bf16_f32 v226, v28, v29\n\t"
      "v_mfma_f32_16x16x32_bf16 v[20:23], v[8:11], v[52:55], v[20:23]\n\t"
      "s_add_u32 m0, s55, 139264\n\t"
      "s_xor_b32 s61, s47, 1310720\n\t"
      "buffer_load_dwordx4 v116, s[88:91], s61 offen lds\n\t"
      "ds_read_b64_tr_b16 a[120:121], v118 offset:49152\n\t"
      "v_cvt_pk_bf16_f32 v227, v30, v31\n\t"
      "v_mfma_f32_16x16x32_bf16 v[16:19], v[12:15], v[56:59], v[16:19]\n\t"
      "ds_read_b128 v[92:95], v103 offset:16384\n\t"
      "v_mfma_f32_16x16x32_bf16 v[20:23], v[12:15], v[60:63], v[20:23]\n\t"
      "s_add_u32 m0, s55, 140288\n\t"
      "s_xor_b32 s61, s47, 1343488\n\t"
      "buffer_load_dwordx4 v116, s[88:91], s61 offen lds\n\t"
      "ds_read_b64_tr_b16 a[122:123], v118 offset:50176\n\t"
      "v_mfma_f32_16x16x32_bf16 a[168:171], v[160:163], v[160:163], a[168:171]\n\t"
      "v_mfma_f32_16x16x32_bf16 v[24:27], v[64:67], v[32:35], 0\n\t"
      "ds_read_b128 v[0:3], v104 offset:17408\n\t"
      "v_mfma_f32_16x16x32_bf16 v[28:31], v[64:67], v[36:39], 0\n\t"
      "ds_read_b64_tr_b16 a[60:61], v117 offset:57344\n\t"
      "ds_read_b64_tr_b16 a[62:63], v117 offset:58368\n\t"
      "v_mfma_f32_16x16x32_bf16 v[24:27], v[68:71], v[40:43], v[24:27]\n\t"
      "ds_read_b128 v[4:7], v119 offset:17408\n\t"
      "v_cvt_pk_bf16_f32 v164, v16, v17\n\t"
      "v_mfma_f32_16x16x32_bf16 v[28:31], v[68:71], v[44:47], v[28:31]\n\t"
      "ds_read_b64_tr_b16 a[124:125], v118 offset:57344\n\t"
      "ds_read_b64_tr_b16 a[126:127], v118 offset:58368\n\t"
      "v_cvt_pk_bf16_f32 v165, v18, v19\n\t"
      "v_mfma_f32_16x16x32_bf16 v[24:27], v[72:75], v[48:51], v[24:27]\n\t"
      "ds_read_b128 v[8:11], v102 offset:17408\n\t"
      "v_cvt_pk_bf16_f32 v228, v20, v21\n\t"
      "v_mfma_f32_16x16x32_bf16 v[28:31], v[72:75], v[52:55], v[28:31]\n\t"
      "v_cvt_pk_bf16_f32 v229, v22, v23\n\t"
      "v_mfma_f32_16x16x32_bf16 v[24:27], v[76:79], v[56:59], v[24:27]\n\t"
      "s_waitcnt lgkmcnt(12)\n\t"
      "ds_read_b128 v[12:15], v103 offset:17408\n\t"
      "v_mfma_f32_16x16x32_bf16 v[28:31], v[76:79], v[60:63], v[28:31]\n\t"
      "v_mfma_f32_16x16x32_bf16 a[232:235], v[224:227], v[224:227], a[232:235]\n\t"
      "s_waitcnt vmcnt(10)\n\t"
      "s_waitcnt lgkmcnt(2)\n\t"
      "s_barrier\n\t"
      "v_mfma_f32_16x16x32_bf16 v[16:19], v[80:83], v[32:35], 0\n\t"
      "ds_read_b128 v[64:67], v104 offset:24576\n\t"
      "v_mfma_f32_16x16x32_bf16 v[20:23], v[80:83], v[36:39], 0\n\t"
      "s_add_u32 m0, s55, 147456\n\t"
      "s_xor_b32 s61, s47, 1572864\n\t"
      "buffer_load_dwordx4 v116, s[88:91], s61 offen lds\n\t"
      "ds_read_b64_tr_b16 a[244:245], v106 offset:16384\n\t"
      "v_mfma_f32_16x16x32_bf16 v[16:19], v[84:87], v[40:43], v[16:19]\n\t"
      "ds_read_b128 v[68:71], v119 offset:24576\n\t"
      "v_cvt_pk_bf16_f32 v166, v24, v25\n\t"
      "v_mfma_f32_16x16x32_bf16 v[20:23], v[84:87], v[44:47], v[20:23]\n\t"
      "s_add_u32 m0, s55, 148480\n\t"
      "s_xor_b32 s61, s47, 1605632\n\t"
      "buffer_load_dwordx4 v116, s[88:91], s61 offen lds\n\t"
      "ds_read_b64_tr_b16 a[246:247], v106 offset:17408\n\t"
      "v_cvt_pk_bf16_f32 v167, v26, v27\n\t"
      "v_mfma_f32_16x16x32_bf16 v[16:19], v[88:91], v[48:51], v[16:19]\n\t"
      "ds_read_b128 v[72:75], v102 offset:24576\n\t"
      "v_cvt_pk_bf16_f32 v230, v28, v29\n\t"
      "v_mfma_f32_16x16x32_bf16 v[20:23], v[88:91], v[52:55], v[20:23]\n\t"
      "s_add_u32 m0, s55, 155648\n\t"
      "s_xor_b32 s61, s47, 1835008\n\t"
      "buffer_load_dwordx4 v116, s[88:91], s61 offen lds\n\t"
      "ds_read_b64_tr_b16 a[128:129], v117\n\t"
      "v_cvt_pk_bf16_f32 v231, v30, v31\n\t"
      "v_mfma_f32_16x16x32_bf16 v[16:19], v[92:95], v[56:59], v[16:19]\n\t"
      "ds_read_b128 v[76:79], v103 offset:24576\n\t"
      "v_mfma_f32_16x16x32_bf16 v[20:23], v[92:95], v[60:63], v[20:23]\n\t"
      "s_add_u32 m0, s55, 156672\n\t"
      "s_xor_b32 s61, s47, 1867776\n\t"
      "buffer_load_dwordx4 v116, s[88:91], s61 offen lds\n\t"
      "ds_read_b64_tr_b16 a[130:131], v117 offset:1024\n\t"
      "v_mfma_f32_16x16x32_bf16 a[168:171], v[164:167], v[164:167], a[168:171]\n\t"
      "v_mfma_f32_16x16x32_bf16 v[24:27], v[0:3], v[32:35], 0\n\t"
      "ds_read_b128 v[80:83], v104 offset:25600\n\t"
      "v_mfma_f32_16x16x32_bf16 v[28:31], v[0:3], v[36:39], 0\n\t"
      "ds_read_b64_tr_b16 a[192:193], v118\n\t"
      "ds_read_b64_tr_b16 a[194:195], v118 offset:1024\n\t"
      "v_mfma_f32_16x16x32_bf16 v[24:27], v[4:7], v[40:43], v[24:27]\n\t"
      "ds_read_b128 v[84:87], v119 offset:25600\n\t"
      "v_cvt_pk_bf16_f32 v168, v16, v17\n\t"
      "v_mfma_f32_16x16x32_bf16 v[28:31], v[4:7], v[44:47], v[28:31]\n\t"
      "ds_read_b64_tr_b16 a[132:133], v117 offset:8192\n\t"
      "s_waitcnt lgkmcnt(12)\n\t"
      "ds_read_b64_tr_b16 a[134:135], v117 offset:9216\n\t"
      "v_cvt_pk_bf16_f32 v169, v18, v19\n\t"
      "v_mfma_f32_16x16x32_bf16 v[24:27], v[8:11], v[48:51], v[24:27]\n\t"
      "ds_read_b128 v[88:91], v102 offset:25600\n\t"
      "v_cvt_pk_bf16_f32 v232, v20, v21\n\t"
      "v_mfma_f32_16x16x32_bf16 v[28:31], v[8:11], v[52:55], v[28:31]\n\t"
      "ds_read_b64_tr_b16 a[196:197], v118 offset:8192\n\t"
      "s_waitcnt lgkmcnt(12)\n\t"
      "ds_read_b64_tr_b16 a[198:199], v118 offset:9216\n\t"
      "v_cvt_pk_bf16_f32 v233, v22, v23\n\t"
      "v_mfma_f32_16x16x32_bf16 v[24:27], v[12:15], v[56:59], v[24:27]\n\t"
      "ds_read_b128 v[92:95], v103 offset:25600\n\t"
      "v_mfma_f32_16x16x32_bf16 v[28:31], v[12:15], v[60:63], v[28:31]\n\t"
      "v_mfma_f32_16x16x32_bf16 a[232:235], v[228:231], v[228:231], a[232:235]\n\t"
      "s_waitcnt vmcnt(8)\n\t"
      "s_waitcnt lgkmcnt(1)\n\t"
      "s_barrier\n\t"
      "v_mfma_f32_16x16x32_bf16 v[16:19], v[64:67], v[32:35], 0\n\t"
      "ds_read_b128 v[0:3], v104 offset:32768\n\t"
      "v_mfma_f32_16x16x32_bf16 v[20:23], v[64:67], v[36:39], 0\n\t"
      "s_add_u32 m0, s55, 98304\n\t"
      "s_xor_b32 s61, s47, 2097152\n\t"
      "buffer_load_dwordx4 v116, s[88:91], s61 offen lds\n\t"
      "ds_read_b64_tr_b16 a[136:137], v117 offset:16384\n\t"
      "v_mfma_f32_16x16x32_bf16 v[16:19], v[68:71], v[40:43], v[16:19]\n\t"
      "ds_read_b128 v[4:7], v119 offset:32768\n\t"
      "v_cvt_pk_bf16_f32 v170, v24, v25\n\t"
      "v_mfma_f32_16x16x32_bf16 v[20:23], v[68:71], v[44:47], v[20:23]\n\t"
      "s_add_u32 m0, s55, 99328\n\t"
      "s_xor_b32 s61, s47, 2129920\n\t"
      "buffer_load_dwordx4 v116, s[88:91], s61 offen lds\n\t"
      "ds_read_b64_tr_b16 a[138:139], v117 offset:17408\n\t"
      "v_cvt_pk_bf16_f32 v171, v26, v27\n\t"
      "v_mfma_f32_16x16x32_bf16 v[16:19], v[72:75], v[48:51], v[16:19]\n\t"
      "ds_read_b128 v[8:11], v102 offset:32768\n\t"
      "v_cvt_pk_bf16_f32 v234, v28, v29\n\t"
      "v_mfma_f32_16x16x32_bf16 v[20:23], v[72:75], v[52:55], v[20:23]\n\t"
      "s_add_u32 m0, s55, 106496\n\t"
      "s_xor_b32 s61, s47, 2359296\n\t"
      "buffer_load_dwordx4 v116, s[88:91], s61 offen lds\n\t"
      "ds_read_b64_tr_b16 a[200:201], v118 offset:16384\n\t"
      "v_cvt_pk_bf16_f32 v235, v30, v31\n\t"
      "v_mfma_f32_16x16x32_bf16 v[16:19], v[76:79], v[56:59], v[16:19]\n\t"
      "ds_read_b128 v[12:15], v103 offset:32768\n\t"
      "v_mfma_f32_16x16x32_bf16 v[20:23], v[76:79], v[60:63], v[20:23]\n\t"
      "s_add_u32 m0, s55, 107520\n\t"
      "s_xor_b32 s61, s47, 2392064\n\t"
      "buffer_load_dwordx4 v116, s[88:91], s61 offen lds\n\t"
      "ds_read_b64_tr_b16 a[202:203], v118 offset:17408\n\t"
      "v_mfma_f32_16x16x32_bf16 a[168:171], v[168:171], v[168:171], a[168:171]\n\t"
      "v_mfma_f32_16x16x32_bf16 v[24:27], v[80:83], v[32:35], 0\n\t"
      "ds_read_b128 v[64:67], v104 offset:33792\n\t"
      "v_mfma_f32_16x16x32_bf16 v[28:31], v[80:83], v[36:39], 0\n\t"
      "ds_read_b64_tr_b16 a[140:141], v117 offset:24576\n\t"
      "ds_read_b64_tr_b16 a[142:143], v117 offset:25600\n\t"
      "v_mfma_f32_16x16x32_bf16 v[24:27], v[84:87], v[40:43], v[24:27]\n\t"
      "ds_read_b128 v[68:71], v119 offset:33792\n\t"
      "v_cvt_pk_bf16_f32 v172, v16, v17\n\t"
      "v_mfma_f32_16x16x32_bf16 v[28:31], v[84:87], v[44:47], v[28:31]\n\t"
      "ds_read_b64_tr_b16 a[204:205], v118 offset:24576\n\t"
      "ds_read_b64_tr_b16 a[206:207], v118 offset:25600\n\t"
      "v_cvt_pk_bf16_f32 v173, v18, v19\n\t"
      "v_mfma_f32_16x16x32_bf16 v[24:27], v[88:91], v[48:51], v[24:27]\n\t"
      "s_waitcnt lgkmcnt(12)\n\t"
      "ds_read_b128 v[72:75], v102 offset:33792\n\t"
      "v_cvt_pk_bf16_f32 v236, v20, v21\n\t"
      "v_mfma_f32_16x16x32_bf16 v[28:31], v[88:91], v[52:55], v[28:31]\n\t"
      "v_cvt_pk_bf16_f32 v237, v22, v23\n\t"
      "v_mfma_f32_16x16x32_bf16 v[24:27], v[92:95], v[56:59], v[24:27]\n\t"
      "ds_read_b128 v[76:79], v103 offset:33792\n\t"
      "v_mfma_f32_16x16x32_bf16 v[28:31], v[92:95], v[60:63], v[28:31]\n\t"
      "v_mfma_f32_16x16x32_bf16 a[232:235], v[232:235], v[232:235], a[232:235]\n\t"
      "s_waitcnt vmcnt(8)\n\t"
      "s_waitcnt lgkmcnt(2)\n\t"
      "s_barrier\n\t"
      "v_mfma_f32_16x16x32_bf16 v[16:19], v[0:3], v[32:35], 0\n\t"
      "ds_read_b128 v[80:83], v104 offset:40960\n\t"
      "v_mfma_f32_16x16x32_bf16 v[20:23], v[0:3], v[36:39], 0\n\t"
      "s_add_u32 m0, s55, 114688\n\t"
      "s_xor_b32 s61, s47, 2621440\n\t"
      "buffer_load_dwordx4 v116, s[88:91], s61 offen lds\n\t"
      "ds_read_b64_tr_b16 a[248:249], v106 offset:32768\n\t"
      "v_mfma_f32_16x16x32_bf16 v[16:19], v[4:7], v[40:43], v[16:19]\n\t"
      "ds_read_b128 v[84:87], v119 offset:40960\n\t"
      "v_cvt_pk_bf16_f32 v174, v24, v25\n\t"
      "v_mfma_f32_16x16x32_bf16 v[20:23], v[4:7], v[44:47], v[20:23]\n\t"
      "s_add_u32 m0, s55, 115712\n\t"
      "s_xor_b32 s61, s47, 2654208\n\t"
      "buffer_load_dwordx4 v116, s[88:91], s61 offen lds\n\t"
      "ds_read_b64_tr_b16 a[250:251], v106 offset:33792\n\t"
      "v_cvt_pk_bf16_f32 v175, v26, v27\n\t"
      "v_mfma_f32_16x16x32_bf16 v[16:19], v[8:11], v[48:51], v[16:19]\n\t"
      "ds_read_b128 v[88:91], v102 offset:40960\n\t"
      "v_cvt_pk_bf16_f32 v238, v28, v29\n\t"
      "v_mfma_f32_16x16x32_bf16 v[20:23], v[8:11], v[52:55], v[20:23]\n\t"
      "s_add_u32 m0, s55, 122880\n\t"
      "s_xor_b32 s61, s47, 2883584\n\t"
      "buffer_load_dwordx4 v116, s[88:91], s61 offen lds\n\t"
      "ds_read_b64_tr_b16 a[144:145], v117 offset:32768\n\t"
      "v_cvt_pk_bf16_f32 v239, v30, v31\n\t"
      "v_mfma_f32_16x16x32_bf16 v[16:19], v[12:15], v[56:59], v[16:19]\n\t"
      "ds_read_b128 v[92:95], v103 offset:40960\n\t"
      "v_mfma_f32_16x16x32_bf16 v[20:23], v[12:15], v[60:63], v[20:23]\n\t"
      "s_add_u32 m0, s55, 123904\n\t"
      "s_xor_b32 s61, s47, 2916352\n\t"
      "buffer_load_dwordx4 v116, s[88:91], s61 offen lds\n\t"
      "ds_read_b64_tr_b16 a[146:147], v117 offset:33792\n\t"
      "v_mfma_f32_16x16x32_bf16 a[168:171], v[172:175], v[172:175], a[168:171]\n\t"
      "v_mfma_f32_16x16x32_bf16 v[24:27], v[64:67], v[32:35], 0\n\t"
      "ds_read_b128 v[0:3], v104 offset:41984\n\t"
      "v_mfma_f32_16x16x32_bf16 v[28:31], v[64:67], v[36:39], 0\n\t"
      "ds_read_b64_tr_b16 a[208:209], v118 offset:32768\n\t"
      "ds_read_b64_tr_b16 a[210:211], v118 offset:33792\n\t"
      "v_mfma_f32_16x16x32_bf16 v[24:27], v[68:71], v[40:43], v[24:27]\n\t"
      "ds_read_b128 v[4:7], v119 offset:41984\n\t"
      "v_cvt_pk_bf16_f32 v176, v16, v17\n\t"
      "v_mfma_f32_16x16x32_bf16 v[28:31], v[68:71], v[44:47], v[28:31]\n\t"
      "ds_read_b64_tr_b16 a[148:149], v117 offset:40960\n\t"
      "s_waitcnt lgkmcnt(12)\n\t"
      "ds_read_b64_tr_b16 a[150:151], v117 offset:41984\n\t"
      "v_cvt_pk_bf16_f32 v177, v18, v19\n\t"
      "v_mfma_f32_16x16x32_bf16 v[24:27], v[72:75], v[48:51], v[24:27]\n\t"
      "ds_read_b128 v[8:11], v102 offset:41984\n\t"
      "v_cvt_pk_bf16_f32 v240, v20, v21\n\t"
      "v_mfma_f32_16x16x32_bf16 v[28:31], v[72:75], v[52:55], v[28:31]\n\t"
      "ds_read_b64_tr_b16 a[212:213], v118 offset:40960\n\t"
      "s_waitcnt lgkmcnt(12)\n\t"
      "ds_read_b64_tr_b16 a[214:215], v118 offset:41984\n\t"
      "v_cvt_pk_bf16_f32 v241, v22, v23\n\t"
      "v_mfma_f32_16x16x32_bf16 v[24:27], v[76:79], v[56:59], v[24:27]\n\t"
      "ds_read_b128 v[12:15], v103 offset:41984\n\t"
      "v_mfma_f32_16x16x32_bf16 v[28:31], v[76:79], v[60:63], v[28:31]\n\t"
      "v_mfma_f32_16x16x32_bf16 a[232:235], v[236:239], v[236:239], a[232:235]\n\t"
      "s_waitcnt vmcnt(8)\n\t"
      "s_waitcnt lgkmcnt(1)\n\t"
      "s_barrier\n\t"
      "v_mfma_f32_16x16x32_bf16 v[16:19], v[80:83], v[32:35], 0\n\t"
      "ds_read_b128 v[64:67], v104 offset:49152\n\t"
      "v_mfma_f32_16x16x32_bf16 v[20:23], v[80:83], v[36:39], 0\n\t"
      "s_add_u32 m0, s55, 131072\n\t"
      "s_xor_b32 s61, s47, 3145728\n\t"
      "buffer_load_dwordx4 v116, s[88:91], s61 offen lds\n\t"
      "ds_read_b64_tr_b16 a[152:153], v117 offset:49152\n\t"
      "v_mfma_f32_16x16x32_bf16 v[16:19], v[84:87], v[40:43], v[16:19]\n\t"
      "ds_read_b128 v[68:71], v119 offset:49152\n\t"
      "v_cvt_pk_bf16_f32 v178, v24, v25\n\t"
      "v_mfma_f32_16x16x32_bf16 v[20:23], v[84:87], v[44:47], v[20:23]\n\t"
      "s_add_u32 m0, s55, 132096\n\t"
      "s_xor_b32 s61, s47, 3178496\n\t"
      "buffer_load_dwordx4 v116, s[88:91], s61 offen lds\n\t"
      "ds_read_b64_tr_b16 a[154:155], v117 offset:50176\n\t"
      "v_cvt_pk_bf16_f32 v179, v26, v27\n\t"
      "v_mfma_f32_16x16x32_bf16 v[16:19], v[88:91], v[48:51], v[16:19]\n\t"
      "ds_read_b128 v[72:75], v102 offset:49152\n\t"
      "v_cvt_pk_bf16_f32 v242, v28, v29\n\t"
      "v_mfma_f32_16x16x32_bf16 v[20:23], v[88:91], v[52:55], v[20:23]\n\t"
      "s_add_u32 m0, s55, 139264\n\t"
      "s_xor_b32 s61, s47, 3407872\n\t"
      "buffer_load_dwordx4 v116, s[88:91], s61 offen lds\n\t"
      "ds_read_b64_tr_b16 a[216:217], v118 offset:49152\n\t"
      "v_cvt_pk_bf16_f32 v243, v30, v31\n\t"
      "v_mfma_f32_16x16x32_bf16 v[16:19], v[92:95], v[56:59], v[16:19]\n\t"
      "ds_read_b128 v[76:79], v103 offset:49152\n\t"
      "v_mfma_f32_16x16x32_bf16 v[20:23], v[92:95], v[60:63], v[20:23]\n\t"
      "s_add_u32 m0, s55, 140288\n\t"
      "s_xor_b32 s61, s47, 3440640\n\t"
      "buffer_load_dwordx4 v116, s[88:91], s61 offen lds\n\t"
      "ds_read_b64_tr_b16 a[218:219], v118 offset:50176\n\t"
      "v_mfma_f32_16x16x32_bf16 a[168:171], v[176:179], v[176:179], a[168:171]\n\t"
      "v_mfma_f32_16x16x32_bf16 v[24:27], v[0:3], v[32:35], 0\n\t"
      "ds_read_b128 v[80:83], v104 offset:50176\n\t"
      "v_mfma_f32_16x16x32_bf16 v[28:31], v[0:3], v[36:39], 0\n\t"
      "ds_read_b64_tr_b16 a[156:157], v117 offset:57344\n\t"
      "ds_read_b64_tr_b16 a[158:159], v117 offset:58368\n\t"
      "v_mfma_f32_16x16x32_bf16 v[24:27], v[4:7], v[40:43], v[24:27]\n\t"
      "ds_read_b128 v[84:87], v119 offset:50176\n\t"
      "v_cvt_pk_bf16_f32 v180, v16, v17\n\t"
      "v_mfma_f32_16x16x32_bf16 v[28:31], v[4:7], v[44:47], v[28:31]\n\t"
      "ds_read_b64_tr_b16 a[220:221], v118 offset:57344\n\t"
      "ds_read_b64_tr_b16 a[222:223], v118 offset:58368\n\t"
      "v_cvt_pk_bf16_f32 v181, v18, v19\n\t"
      "v_mfma_f32_16x16x32_bf16 v[24:27], v[8:11], v[48:51], v[24:27]\n\t"
      "s_waitcnt lgkmcnt(12)\n\t"
      "ds_read_b128 v[88:91], v102 offset:50176\n\t"
      "v_cvt_pk_bf16_f32 v244, v20, v21\n\t"
      "v_mfma_f32_16x16x32_bf16 v[28:31], v[8:11], v[52:55], v[28:31]\n\t"
      "v_cvt_pk_bf16_f32 v245, v22, v23\n\t"
      "v_mfma_f32_16x16x32_bf16 v[24:27], v[12:15], v[56:59], v[24:27]\n\t"
      "ds_read_b128 v[92:95], v103 offset:50176\n\t"
      "v_mfma_f32_16x16x32_bf16 v[28:31], v[12:15], v[60:63], v[28:31]\n\t"
      "v_mfma_f32_16x16x32_bf16 a[232:235], v[240:243], v[240:243], a[232:235]\n\t"
      "s_waitcnt vmcnt(8)\n\t"
      "s_waitcnt lgkmcnt(2)\n\t"
      "s_barrier\n\t"
      "v_mfma_f32_16x16x32_bf16 v[16:19], v[64:67], v[32:35], 0\n\t"
      "ds_read_b128 v[0:3], v104 offset:57344\n\t"
      "v_mfma_f32_16x16x32_bf16 v[20:23], v[64:67], v[36:39], 0\n\t"
      "s_add_u32 m0, s55, 147456\n\t"
      "s_xor_b32 s61, s47, 3670016\n\t"
      "buffer_load_dwordx4 v116, s[88:91], s61 offen lds\n\t"
      "ds_read_b64_tr_b16 a[252:253], v106 offset:49152\n\t"
      "v_mfma_f32_16x16x32_bf16 v[16:19], v[68:71], v[40:43], v[16:19]\n\t"
      "ds_read_b128 v[4:7], v119 offset:57344\n\t"
      "v_cvt_pk_bf16_f32 v182, v24, v25\n\t"
      "v_mfma_f32_16x16x32_bf16 v[20:23], v[68:71], v[44:47], v[20:23]\n\t"
      "s_add_u32 m0, s55, 148480\n\t"
      "s_xor_b32 s61, s47, 3702784\n\t"
      "buffer_load_dwordx4 v116, s[88:91], s61 offen lds\n\t"
      "ds_read_b64_tr_b16 a[254:255], v106 offset:50176\n\t"
      "v_cvt_pk_bf16_f32 v183, v26, v27\n\t"
      "v_mfma_f32_16x16x32_bf16 v[16:19], v[72:75], v[48:51], v[16:19]\n\t"
      "ds_read_b128 v[8:11], v102 offset:57344\n\t"
      "v_cvt_pk_bf16_f32 v246, v28, v29\n\t"
      "v_mfma_f32_16x16x32_bf16 v[20:23], v[72:75], v[52:55], v[20:23]\n\t"
      "s_add_u32 m0, s55, 155648\n\t"
      "s_xor_b32 s61, s47, 3932160\n\t"
      "buffer_load_dwordx4 v116, s[88:91], s61 offen lds\n\t"
      "ds_read_b64_tr_b16 a[160:161], v117\n\t"
      "v_cvt_pk_bf16_f32 v247, v30, v31\n\t"
      "v_mfma_f32_16x16x32_bf16 v[16:19], v[76:79], v[56:59], v[16:19]\n\t"
      "ds_read_b128 v[12:15], v103 offset:57344\n\t"
      "v_mfma_f32_16x16x32_bf16 v[20:23], v[76:79], v[60:63], v[20:23]\n\t"
      "s_add_u32 m0, s55, 156672\n\t"
      "s_xor_b32 s61, s47, 3964928\n\t"
      "buffer_load_dwordx4 v116, s[88:91], s61 offen lds\n\t"
      "ds_read_b64_tr_b16 a[162:163], v117 offset:1024\n\t"
      "v_mfma_f32_16x16x32_bf16 a[168:171], v[180:183], v[180:183], a[168:171]\n\t"
      "v_mfma_f32_16x16x32_bf16 v[24:27], v[80:83], v[32:35], 0\n\t"
      "ds_read_b128 v[64:67], v104 offset:58368\n\t"
      "v_mfma_f32_16x16x32_bf16 v[28:31], v[80:83], v[36:39], 0\n\t"
      "ds_read_b64_tr_b16 a[224:225], v118\n\t"
      "ds_read_b64_tr_b16 a[226:227], v118 offset:1024\n\t"
      "v_mfma_f32_16x16x32_bf16 v[24:27], v[84:87], v[40:43], v[24:27]\n\t"
      "ds_read_b128 v[68:71], v119 offset:58368\n\t"
      "v_cvt_pk_bf16_f32 v184, v16, v17\n\t"
      "v_mfma_f32_16x16x32_bf16 v[28:31], v[84:87], v[44:47], v[28:31]\n\t"
      "ds_read_b64_tr_b16 a[164:165], v117 offset:8192\n\t"
      "s_waitcnt lgkmcnt(12)\n\t"
      "ds_read_b64_tr_b16 a[166:167], v117 offset:9216\n\t"
      "v_cvt_pk_bf16_f32 v185, v18, v19\n\t"
      "v_mfma_f32_16x16x32_bf16 v[24:27], v[88:91], v[48:51], v[24:27]\n\t"
      "ds_read_b128 v[72:75], v102 offset:58368\n\t"
      "v_cvt_pk_bf16_f32 v248, v20, v21\n\t"
      "v_mfma_f32_16x16x32_bf16 v[28:31], v[88:91], v[52:55], v[28:31]\n\t"
      "ds_read_b64_tr_b16 a[228:229], v118 offset:8192\n\t"
      "s_waitcnt lgkmcnt(12)\n\t"
      "ds_read_b64_tr_b16 a[230:231], v118 offset:9216\n\t"
      "v_cvt_pk_bf16_f32 v249, v22, v23\n\t"
      "v_mfma_f32_16x16x32_bf16 v[24:27], v[92:95], v[56:59], v[24:27]\n\t"
      "ds_read_b128 v[76:79], v103 offset:58368\n\t"
      "v_mfma_f32_16x16x32_bf16 v[28:31], v[92:95], v[60:63], v[28:31]\n\t"
      "v_mfma_f32_16x16x32_bf16 a[232:235], v[244:247], v[244:247], a[232:235]\n\t"
      "v_mfma_f32_16x16x32_bf16 v[16:19], v[0:3], v[32:35], 0\n\t"
      "v_mfma_f32_16x16x32_bf16 v[20:23], v[0:3], v[36:39], 0\n\t"
      "v_mfma_f32_16x16x32_bf16 v[16:19], v[4:7], v[40:43], v[16:19]\n\t"
      "v_cvt_pk_bf16_f32 v186, v24, v25\n\t"
      "v_mfma_f32_16x16x32_bf16 v[20:23], v[4:7], v[44:47], v[20:23]\n\t"
      "v_cvt_pk_bf16_f32 v187, v26, v27\n\t"
      "s_waitcnt lgkmcnt(13)\n\t"
      "v_mfma_f32_16x16x32_bf16 v[16:19], v[8:11], v[48:51], v[16:19]\n\t"
      "v_cvt_pk_bf16_f32 v250, v28, v29\n\t"
      "v_mfma_f32_16x16x32_bf16 v[20:23], v[8:11], v[52:55], v[20:23]\n\t"
      "v_cvt_pk_bf16_f32 v251, v30, v31\n\t"
      "s_waitcnt lgkmcnt(11)\n\t"
      "v_mfma_f32_16x16x32_bf16 v[16:19], v[12:15], v[56:59], v[16:19]\n\t"
      "v_mfma_f32_16x16x32_bf16 v[20:23], v[12:15], v[60:63], v[20:23]\n\t"
      "v_mfma_f32_16x16x32_bf16 a[168:171], v[184:187], v[184:187], a[168:171]\n\t"
      "buffer_load_dwordx4 v[96:99], off, s[36:39], s54 sc1\n\t"
      "s_waitcnt lgkmcnt(9)\n\t"
      "v_mfma_f32_16x16x32_bf16 v[24:27], v[64:67], v[32:35], 0\n\t"
      "v_mfma_f32_16x16x32_bf16 v[28:31], v[64:67], v[36:39], 0\n\t"
      "s_waitcnt lgkmcnt(6)\n\t"
      "v_mfma_f32_16x16x32_bf16 v[24:27], v[68:71], v[40:43], v[24:27]\n\t"
      "v_cvt_pk_bf16_f32 v188, v16, v17\n\t"
      "v_mfma_f32_16x16x32_bf16 v[28:31], v[68:71], v[44:47], v[28:31]\n\t"
      "v_cvt_pk_bf16_f32 v189, v18, v19\n\t"
      "s_waitcnt lgkmcnt(3)\n\t"
      "v_mfma_f32_16x16x32_bf16 v[24:27], v[72:75], v[48:51], v[24:27]\n\t"
      "v_cvt_pk_bf16_f32 v252, v20, v21\n\t"
      "v_mfma_f32_16x16x32_bf16 v[28:31], v[72:75], v[52:55], v[28:31]\n\t"
      "v_cvt_pk_bf16_f32 v253, v22, v23\n\t"
      "s_waitcnt lgkmcnt(0)\n\t"
      "v_mfma_f32_16x16x32_bf16 v[24:27], v[76:79], v[56:59], v[24:27]\n\t"
      "v_mfma_f32_16x16x32_bf16 v[28:31], v[76:79], v[60:63], v[28:31]\n\t"
      "v_mfma_f32_16x16x32_bf16 a[232:235], v[248:251], v[248:251], a[232:235]\n\t"
      "v_lshl_add_u32 v8, s41, 10, v127\n\t"
      "v_add_u32 v8, 65536, v8\n\t"
      "v_mov_b32 v12, 0\n\t"
      "v_mov_b32 v9, s41\n\t"
      "v_lshlrev_b32 v9, 2, v9\n\t"
      "v_cvt_pk_bf16_f32 v190, v24, v25\n\t"
      "v_cvt_pk_bf16_f32 v191, v26, v27\n\t"
      "v_cvt_pk_bf16_f32 v254, v28, v29\n\t"
      "v_cvt_pk_bf16_f32 v255, v30, v31\n\t"
      "s_nop 1\n\t"
      "v_mfma_f32_16x16x32_bf16 a[168:171], v[188:191], v[188:191], a[168:171]\n\t"
      "v_mfma_f32_16x16x32_bf16 a[232:235], v[252:255], v[252:255], a[232:235]\n\t"
      "s_sub_u32 s64, s46, 786432\n\t"
      "s_mov_b32 s71, 98304\n\t"
      "s_mov_b32 s63, 0x10000\n\t"
      "s_nop 4\n\t"
      "v_accvgpr_read_b32 v0, a168\n\t"
      "v_accvgpr_read_b32 v1, a169\n\t"
      "v_accvgpr_read_b32 v2, a170\n\t"
      "v_accvgpr_read_b32 v3, a171\n\t"
      "v_accvgpr_read_b32 v4, a232\n\t"
      "v_accvgpr_read_b32 v5, a233\n\t"
      "v_accvgpr_read_b32 v6, a234\n\t"
      "v_accvgpr_read_b32 v7, a235\n\t"
      "v_and_b32 v0, 0x7fffffff, v0\n\t"
      "v_and_b32 v1, 0x7fffffff, v1\n\t"
      "v_and_b32 v2, 0x7fffffff, v2\n\t"
      "v_and_b32 v3, 0x7fffffff, v3\n\t"
      "v_and_b32 v4, 0x7fffffff, v4\n\t"
      "v_and_b32 v5, 0x7fffffff, v5\n\t"
      "v_and_b32 v6, 0x7fffffff, v6\n\t"
      "v_and_b32 v7, 0x7fffffff, v7\n\t"
      "v_max3_u32 v0, v0, v1, v2\n\t"
      "v_max3_u32 v3, v3, v4, v5\n\t"
      "v_max3_u32 v0, v0, v6, v7\n\t"
      "v_max_u32 v0, v0, v3\n\t"
      "s_nop 1\n\t"
      "v_max_u32_dpp v0, v0, v0 quad_perm:[1,0,3,2] row_mask:0xf bank_mask:0xf\n\t"
      "s_nop 1\n\t"
      "v_max_u32_dpp v0, v0, v0 quad_perm:[2,3,0,1] row_mask:0xf bank_mask:0xf\n\t"
      "s_nop 1\n\t"
      "v_max_u32_dpp v0, v0, v0 row_ror:4 row_mask:0xf bank_mask:0xf\n\t"
      "s_nop 1\n\t"
      "v_max_u32_dpp v0, v0, v0 row_ror:8 row_mask:0xf bank_mask:0xf\n\t"
      "v_mov_b32 v1, v0\n\t"
      "s_nop 1\n\t"
      "v_permlane16_swap_b32 v0, v1\n\t"
      "v_max_u32 v0, v0, v1\n\t"
      "v_mov_b32 v1, v0\n\t"
      "s_nop 1\n\t"
      "v_permlane32_swap_b32 v0, v1\n\t"
      "v_max_u32 v0, v0, v1\n\t"
      "ds_write_b32 v9, v0 offset:32768\n\t"
      "s_waitcnt lgkmcnt(0)\n\t"
      "s_barrier\n\t"
      "ds_read_b128 v[4:7], v12 offset:32768\n\t"
      "s_waitcnt lgkmcnt(0)\n\t"
      "v_max_u32 v4, v4, v5\n\t"
      "v_max3_u32 v4, v4, v6, v7\n\t"
      "v_mov_b32 v5, s53\n\t"
      "s_memrealtime s[92:93]\n\t"
      "v_add_u32 v4, 0x10000, v4\n\t"
      "s_mov_b64 exec, 1\n\t"
      "s_mov_b32 s61, 0xffff\n\t"
      "s_waitcnt lgkmcnt(0)\n\t"
      "v_mov_b32 v6, s92\n\t"
      "v_bfi_b32 v4, s61, v6, v4\n\t"
      "buffer_store_dwordx2 v[4:5], off, s[36:39], s95 sc1\n\t"
      "s_mov_b64 exec, -1\n\t"
      "s_waitcnt vmcnt(1)\n\t"
      "v_bitop3_b32 v96, v96, v97, s53 bitop3:0x7e\n\t"
      "v_bitop3_b32 v98, v98, v99, s53 bitop3:0x7e\n\t"
      "v_or_b32 v96, v96, v98\n\t"
      "s_add_u32 m0, s55, 98304\n\t"
      "v_readfirstlane_b32 s56, v96\n\t"
      "s_nop 1\n\t"
      "s_cmp_eq_u32 s56, 0\n\t"
      "s_cbranch_scc0 LF2%=\n\t"
      "buffer_load_dwordx4 v127, s[36:39], s64 offen sc1 lds\n\t"
      "buffer_load_dwordx4 v127, s[36:39], s64 offen offset:1024 sc1 lds\n\t"
      "LF2%=:\n\t"
      "buffer_load_dwordx4 v[96:99], off, s[36:39], s54 sc1\n\t"
      "ds_write_b128 v8, a[176:179]\n\t"
      "ds_write_b128 v8, a[180:183] offset:4096\n\t"
      "ds_write_b128 v8, a[184:187] offset:8192\n\t"
      "ds_write_b128 v8, a[188:191] offset:12288\n\t"
      "ds_write_b128 v8, a[240:243] offset:16384\n\t"
      "ds_write_b128 v8, a[244:247] offset:20480\n\t"
      "ds_write_b128 v8, a[248:251] offset:24576\n\t"
      "ds_write_b128 v8, a[252:255] offset:28672\n\t"
      "ds_write_b128 v100, v[112:115] offset:57344\n\t"
      "s_cbranch_scc1 LF3%=\n\t"
      "LF0%=:\n\t"
      "s_waitcnt vmcnt(0)\n\t"
      "v_bitop3_b32 v96, v96, v97, s53 bitop3:0x7e\n\t"
      "v_bitop3_b32 v98, v98, v99, s53 bitop3:0x7e\n\t"
      "v_or_b32 v96, v96, v98\n\t"
      "s_add_u32 m0, s55, 98304\n\t"
      "v_readfirstlane_b32 s56, v96\n\t"
      "s_nop 1\n\t"
      "s_cmp_eq_u32 s56, 0\n\t"
      "s_cbranch_scc1 LF1%=\n\t"
      "buffer_load_dwordx4 v[96:99], off, s[36:39], s54 sc1\n\t"
      "s_sub_u32 s63, s63, 1\n\t"
      "s_cmp_lg_u32 s63, 0\n\t"
      "s_cbranch_scc1 LF0%=\n\t"
      "LF1%=:\n\t"
      "buffer_load_dwordx4 v127, s[36:39], s64 offen sc1 lds\n\t"
      "buffer_load_dwordx4 v127, s[36:39], s64 offen offset:1024 sc1 lds\n\t"
      "LF3%=:\n\t"
      "v_lshlrev_b32 v91, 4, v127\n\t"
      "s_cmp_eq_u32 s86, 0\n\t"
      "s_cbranch_scc1 LC9%=\n\t"
      "s_waitcnt lgkmcnt(0)\n\t"
      "s_memtime s[96:97]\n\t"
      "s_waitcnt lgkmcnt(0)\n\t"
      "s_buffer_load_dword s87, s[36:39], s86\n\t"
      "s_waitcnt lgkmcnt(0)\n\t"
      "s_memtime s[98:99]\n\t"
      "s_waitcnt lgkmcnt(0)\n\t"
      "LC9%=:\n\t"
      "s_sub_u32 s61, s98, s96\n\t"
      "s_lshr_b32 s61, s61, 2\n\t"
      "s_min_u32 s61, s61, 511\n\t"
      "s_sub_u32 s62, 0, s61\n\t"
      "s_bitcmp1_b32 s86, 13\n\t"
      "s_cselect_b32 s61, s62, s61\n\t"
      "s_add_u32 s65, s61, 1049088\n\t"
      "v_readfirstlane_b32 s97, v121\n\t"
      "v_mov_b32 v65, v115\n\t"
      "v_mov_b32 v64, 0\n\t"
      "v_mov_b32 v66, 0\n\t"
      "v_mov_b32 v67, 0\n\t"
      "v_mov_b32 v109, 0\n\t"
      "v_mov_b32 v110, 0\n\t"
      "v_mov_b32 v111, 0\n\t"
      "s_mov_b32 s40, -1\n\t"
      "s_add_u32 s45, s55, 32768\n\t"
      "v_mov_b32 v126, 0\n\t"
      "s_mov_b32 s56, 0\n\t"
      "s_mov_b32 s58, 1\n\t"
      "s_mov_b32 s59, 0x3e0293ee\n\t"
      "s_mov_b32 s96, 0x40820000\n\t"
      "s_mov_b32 s57, 1.0\n\t"
      "s_add_u32 s68, s43, 8192\n\t"
      "s_add_u32 s97, s97, s43\n\t"
      "s_add_u32 s97, s97, 0x8000\n\t"
      "s_mov_b32 s99, 0\n\t"
      "s_mov_b32 s26, 2460\n\t"
      "s_mov_b32 s25, 0\n\t"
      "s_memtime s[78:79]\n\t"
      "s_waitcnt lgkmcnt(0)\n\t"
      "s_sub_u32 s78, s78, 4000\n\t"
      "s_mov_b32 s84, s78\n\t"
      "s_mov_b32 s88, s78\n\t"
      "s_add_u32 s50, s50, 32\n\t"
      "s_mov_b32 s72, 0xcccccccc\n\t"
      "s_mov_b32 s73, 0xcccccccc\n\t"
      "s_waitcnt vmcnt(0) lgkmcnt(0)\n\t"
      "s_barrier\n\t"
      "s_mov_b32 m0, s45\n\t"
      "s_sub_u32 s61, 204800, s71\n\t"
      "buffer_load_dwordx4 v65, s[36:39], s44 offen sc1 lds\n\t"
      "buffer_load_dwordx4 v65, s[36:39], s44 offen offset:1024 sc1 lds\n\t"
      "s_add_u32 m0, s55, s61\n\t"
      "s_nop 0\n\t"
      "buffer_load_dwordx4 v127, s[36:39], s46 offen sc1 lds\n\t"
      "buffer_load_dwordx4 v127, s[36:39], s46 offen offset:1024 sc1 lds\n\t"
      "buffer_load_dwordx2 v[92:93], v91, s[36:39], s94 offen sc1\n\t"
      "buffer_load_dwordx2 v[94:95], v91, s[36:39], s94 offen offset:128 sc1\n\t"
      "ds_read_b64_tr_b16 a[168:169], v117 offset:16384\n\t"
      "ds_read_b64_tr_b16 a[170:171], v117 offset:17408\n\t"
      "ds_read_b64_tr_b16 a[232:233], v118 offset:16384\n\t"
      "ds_read_b64_tr_b16 a[234:235], v118 offset:17408\n\t"
      "ds_read_b64_tr_b16 a[172:173], v117 offset:24576\n\t"
      "ds_read_b64_tr_b16 a[174:175], v117 offset:25600\n\t"
      "ds_read_b64_tr_b16 a[236:237], v118 offset:24576\n\t"
      "ds_read_b64_tr_b16 a[238:239], v118 offset:25600\n\t"
      "ds_read_b64_tr_b16 a[176:177], v117 offset:32768\n\t"
      "ds_read_b64_tr_b16 a[178:179], v117 offset:33792\n\t"
      "ds_read_b64_tr_b16 a[240:241], v118 offset:32768\n\t"
      "ds_read_b64_tr_b16 a[242:243], v118 offset:33792\n\t"
      "ds_read_b64_tr_b16 a[180:181], v117 offset:40960\n\t"
      "ds_read_b64_tr_b16 a[182:183], v117 offset:41984\n\t"
      "ds_read_b64_tr_b16 a[244:245], v118 offset:40960\n\t"
      "s_waitcnt lgkmcnt(12)\n\t"
      "ds_read_b64_tr_b16 a[246:247], v118 offset:41984\n\t"
      "ds_read_b64_tr_b16 a[184:185], v117 offset:49152\n\t"
      "ds_read_b64_tr_b16 a[186:187], v117 offset:50176\n\t"
      "s_waitcnt lgkmcnt(12)\n\t"
      "ds_read_b64_tr_b16 a[248:249], v118 offset:49152\n\t"
      "ds_read_b64_tr_b16 a[250:251], v118 offset:50176\n\t"
      "ds_read_b64_tr_b16 a[188:189], v117 offset:57344\n\t"
      "s_waitcnt lgkmcnt(12)\n\t"
      "ds_read_b64_tr_b16 a[190:191], v117 offset:58368\n\t"
      "ds_read_b64_tr_b16 a[252:253], v118 offset:57344\n\t"
      "ds_read_b64_tr_b16 a[254:255], v118 offset:58368\n\t"
      "s_waitcnt lgkmcnt(6)\n\t"
      "s_cmp_eq_u32 s41, 1\n\t"
      "s_cbranch_scc1 LWIN_1%=\n\t"
      "s_cmp_eq_u32 s41, 2\n\t"
      "s_cbranch_scc1 LWIN_2%=\n\t"
      "s_cmp_eq_u32 s41, 3\n\t"
      "s_cbranch_scc1 LWIN_3%=\n\t"
      "s_branch LWIN_0%=\n\t"
      ".p2align 6\n\t"
      "LPOLL_0%=:\n\t"
      "buffer_load_dwordx4 v[84:87], v60, s[36:39], s43 offen sc1\n\t"
      "buffer_load_dwordx4 v[88:91], v60, s[36:39], s43 offen offset:1024 sc1\n\t"
      "s_waitcnt lgkmcnt(4)\n\t"
      "v_mfma_f32_16x16x32_bf16 v[32:35], v[16:19], v[24:27], v[32:35]\n\t"
      "v_mfma_f32_16x16x32_bf16 v[36:39], v[20:23], v[24:27], v[36:39]\n\t"
      "v_mfma_f32_16x16x32_bf16 v[40:43], v[16:19], v[28:31], v[40:43]\n\t"
      "v_mfma_f32_16x16x32_bf16 v[44:47], v[20:23], v[28:31], v[44:47]\n\t"
      "buffer_load_dwordx4 v[92:95], v60, s[36:39], s68 offen sc1\n\t"
      "buffer_load_dwordx4 v[96:99], v60, s[36:39], s68 offen offset:1024 sc1\n\t"
      "v_mov_b64 v[48:49], 0\n\t"
      "v_mov_b64 v[50:51], 0\n\t"
      "v_mov_b64 v[52:53], 0\n\t"
      "v_mov_b64 v[54:55], 0\n\t"
      "s_xor_b32 s69, s97, 1024\n\t"
      "s_waitcnt lgkmcnt(0)\n\t"
      "v_mfma_f32_16x16x32_bf16 v[32:35], v[76:79], v[112:115], v[32:35]\n\t"
      "v_mfma_f32_16x16x32_bf16 v[36:39], v[80:83], v[112:115], v[36:39]\n\t"
      "v_mfma_f32_16x16x32_bf16 v[40:43], v[76:79], v[116:119], v[40:43]\n\t"
      "v_mfma_f32_16x16x32_bf16 v[44:47], v[80:83], v[116:119], v[44:47]\n\t"
      "s_mov_b64 exec, 1\n\t"
      "ds_write_b64 v109, v[110:111] offset:55392\n\t"
      "s_mov_b64 exec, -1\n\t"
      "s_nop 4\n\t"
      "v_cvt_pk_bf16_f32 v68, v32, v33\n\t"
      "v_cvt_pk_bf16_f32 v69, v34, v35\n\t"
      "v_cvt_pk_bf16_f32 v70, v36, v37\n\t"
      "v_cvt_pk_bf16_f32 v71, v38, v39\n\t"
      "v_cvt_pk_bf16_f32 v74, v40, v41\n\t"
      "v_cvt_pk_bf16_f32 v75, v42, v43\n\t"
      "v_cvt_pk_bf16_f32 v72, v44, v45\n\t"
      "v_cvt_pk_bf16_f32 v73, v46, v47\n\t"
      "v_mov_b64 v[32:33], 0\n\t"
      "v_mov_b64 v[34:35], 0\n\t"
      "v_mov_b64 v[36:37], 0\n\t"
      "v_mov_b64 v[38:39], 0\n\t"
      "v_mov_b64 v[40:41], 0\n\t"
      "v_mov_b64 v[42:43], 0\n\t"
      "v_mov_b64 v[44:45], 0\n\t"
      "v_mov_b64 v[46:47], 0\n\t"
      "s_nop 0\n\t"
      "s_nop 0\n\t"
      "s_waitcnt vmcnt(3)\n\t"
      "v_pk_fma_f32 v[4:5], v[84:85], s[56:57], v[126:127] op_sel:[0,1,0] op_sel_hi:[1,1,0]\n\t"
      "v_pk_fma_f32 v[6:7], v[86:87], s[56:57], v[126:127] op_sel:[0,1,0] op_sel_hi:[1,1,0]\n\t"
      "v_cvt_pk_bf16_f32 v0, v4, v5\n\t"
      "v_cvt_pk_bf16_f32 v1, v6, v7\n\t"
      "s_nop 0\n\t"
      "ds_write_b64 v127, v[0:1] offset:40960\n\t"
      "s_waitcnt vmcnt(2)\n\t"
      "v_pk_fma_f32 v[10:11], v[90:91], s[56:57], v[126:127] op_sel:[0,1,0] op_sel_hi:[1,1,0]\n\t"
      "v_pk_fma_f32 v[8:9], v[88:89], s[56:57], v[126:127] op_sel:[0,1,0] op_sel_hi:[1,1,0]\n\t"
      "v_cvt_pk_bf16_f32 v3, v10, v11\n\t"
      "v_cvt_pk_bf16_f32 v2, v8, v9\n\t"
      "ds_write_b64 v127, v[2:3] offset:40968\n\t"
      "v_bitop3_b32 v64, v84, v85, v86 bitop3:0x80\n\t"
      "v_bitop3_b32 v64, v64, v87, v88 bitop3:0x80\n\t"
      "v_bitop3_b32 v64, v64, v89, v90 bitop3:0x80\n\t"
      "v_bitop3_b32 v120, v120, v121, s53 bitop3:0x7e\n\t"
      "v_bitop3_b32 v64, v64, v91, s58 bitop3:0x80\n\t"
      "v_cmp_eq_u32 vcc, 0, v64\n\t"
      "s_cbranch_vccnz LR0_0%=\n\t"
      "LC0_0%=:\n\t"
      "v_smfmac_f32_16x16x64_bf16 v[44:47], v[0:3], a[192:199], v122\n\t"
      "s_waitcnt lgkmcnt(0)\n\t"
      "v_smfmac_f32_16x16x64_bf16 v[32:35], v[0:3], a[0:7], v122\n\t"
      "s_barrier\n\t"
      "v_mfma_f32_16x16x32_bf16 v[56:59], v[0:3], v[0:3], 0\n\t"
      "ds_read_b128 v[4:7], v127 offset:41984\n\t"
      "v_smfmac_f32_16x16x64_bf16 v[36:39], v[0:3], a[64:71], v122\n\t"
      "ds_read_b128 v[76:79], v127 offset:32768\n\t"
      "v_smfmac_f32_16x16x64_bf16 v[52:55], v[0:3], v[192:199], v122\n\t"
      "ds_read_b128 v[12:15], v127 offset:44032\n\t"
      "v_smfmac_f32_16x16x64_bf16 v[40:43], v[0:3], a[128:135], v122\n\t"
      "ds_read_b128 v[8:11], v127 offset:43008\n\t"
      "v_smfmac_f32_16x16x64_bf16 v[48:51], v[0:3], v[128:135], v122\n\t"
      "s_waitcnt lgkmcnt(3)\n\t"
      "v_smfmac_f32_16x16x64_bf16 v[32:35], v[4:7], a[8:15], v122\n\t"
      "s_waitcnt vmcnt(1)\n\t"
      "v_fma_f32 v22, v94, s57, v126\n\t"
      "v_bitop3_b32 v64, v92, v93, v94 bitop3:0x80\n\t"
      "v_smfmac_f32_16x16x64_bf16 v[44:47], v[4:7], a[200:207], v122\n\t"
      "ds_read_b128 v[80:83], v127 offset:35840\n\t"
      "v_fma_f32 v20, v92, s57, v126\n\t"
      "v_fma_f32 v21, v93, s57, v126\n\t"
      "v_smfmac_f32_16x16x64_bf16 v[36:39], v[4:7], a[72:79], v122\n\t"
      "v_cvt_pk_bf16_f32 v16, v20, v21\n\t"
      "v_fma_f32 v23, v95, s57, v126\n\t"
      "v_smfmac_f32_16x16x64_bf16 v[40:43], v[4:7], a[136:143], v122\n\t"
      "s_waitcnt lgkmcnt(1)\n\t"
      "s_waitcnt vmcnt(0)\n\t"
      "v_fma_f32 v26, v98, s57, v126\n\t"
      "v_cvt_pk_bf16_f32 v17, v22, v23\n\t"
      "v_fma_f32 v24, v96, s57, v126\n\t"
      "v_smfmac_f32_16x16x64_bf16 v[36:39], v[8:11], a[80:87], v122\n\t"
      "ds_write_b64 v127, v[16:17] offset:45056\n\t"
      "v_fma_f32 v27, v99, s57, v126\n\t"
      "v_bitop3_b32 v64, v64, v95, v96 bitop3:0x80\n\t"
      "v_fma_f32 v25, v97, s57, v126\n\t"
      "v_cvt_pk_bf16_f32 v18, v24, v25\n\t"
      "v_cvt_pk_bf16_f32 v19, v26, v27\n\t"
      "v_smfmac_f32_16x16x64_bf16 v[36:39], v[12:15], a[88:95], v122\n\t"
      "ds_write_b64 v127, v[18:19] offset:45064\n\t"
      "v_bitop3_b32 v64, v64, v97, v98 bitop3:0x80\n\t"
      "v_bitop3_b32 v64, v64, v99, s60 bitop3:0x80\n\t"
      "v_smfmac_f32_16x16x64_bf16 v[44:47], v[8:11], a[208:215], v122\n\t"
      "ds_read_b128 v[84:87], v127 offset:36864\n\t"
      "v_cmp_eq_u32_e64 vcc, 0, v64\n\t"
      "v_smfmac_f32_16x16x64_bf16 v[40:43], v[8:11], a[144:151], v122\n\t"
      "v_smfmac_f32_16x16x64_bf16 v[32:35], v[8:11], a[16:23], v122\n\t"
      "s_cbranch_vccnz LR1_0%=\n\t"
      "LC1_0%=:\n\t"
      "v_mfma_f32_16x16x32_bf16 v[56:59], v[16:19], v[16:19], v[56:59]\n\t"
      "s_waitcnt lgkmcnt(1)\n\t"
      "v_readfirstlane_b32 s56, v120\n\t"
      "v_smfmac_f32_16x16x64_bf16 v[32:35], v[12:15], a[24:31], v122\n\t"
      "s_barrier\n\t"
      "v_smfmac_f32_16x16x64_bf16 v[40:43], v[12:15], a[152:159], v122\n\t"
      "ds_read_b128 v[20:23], v127 offset:46080\n\t"
      "v_smfmac_f32_16x16x64_bf16 v[44:47], v[12:15], a[216:223], v122\n\t"
      "ds_read_b128 v[28:31], v127 offset:48128\n\t"
      "v_smfmac_f32_16x16x64_bf16 v[32:35], v[16:19], a[32:39], v122\n\t"
      "ds_read_b128 v[24:27], v127 offset:47104\n\t"
      "s_mov_b32 s58, 0x10000000\n\t"
      "v_smfmac_f32_16x16x64_bf16 v[36:39], v[16:19], a[96:103], v122\n\t"
      "v_max3_f32 v56, v56, v57, v58\n\t"
      "v_smfmac_f32_16x16x64_bf16 v[44:47], v[16:19], a[224:231], v122\n\t"
      "s_waitcnt lgkmcnt(2)\n\t"
      "v_max_f32 v56, v56, v59\n\t"
      "v_smfmac_f32_16x16x64_bf16 v[32:35], v[20:23], a[40:47], v122\n\t"
      "ds_read_b128 v[88:91], v127 offset:39936\n\t"
      "v_max_f32_dpp v56, v56, v56 quad_perm:[1,0,3,2] row_mask:0xf bank_mask:0xf\n\t"
      "v_smfmac_f32_16x16x64_bf16 v[36:39], v[20:23], a[104:111], v122\n\t"
      "s_waitcnt lgkmcnt(1)\n\t"
      "s_nop 0\n\t"
      "v_smfmac_f32_16x16x64_bf16 v[32:35], v[24:27], a[48:55], v122\n\t"
      "v_max_f32_dpp v56, v56, v56 quad_perm:[2,3,0,1] row_mask:0xf bank_mask:0xf\n\t"
      "v_smfmac_f32_16x16x64_bf16 v[32:35], v[28:31], a[56:63], v122\n\t"
      "s_mov_b32 exec_lo, 0x100001\n\t"
      "v_smfmac_f32_16x16x64_bf16 v[36:39], v[24:27], a[112:119], v122\n\t"
      "s_mov_b32 exec_hi, 0x10000100\n\t"
      "ds_max_i32 v109, v56 offset:55392\n\t"
      "v_smfmac_f32_16x16x64_bf16 v[36:39], v[28:31], a[120:127], v122\n\t"
      "s_mov_b64 exec, -1\n\t"
      "v_add_f32 v32, v32, v34\n\t"
      "v_smfmac_f32_16x16x64_bf16 v[40:43], v[16:19], a[160:167], v122\n\t"
      "v_add_f32 v33, v33, v35\n\t"
      "v_mul_f32 v33, s59, v33\n\t"
      "v_smfmac_f32_16x16x64_bf16 v[40:43], v[20:23], a[168:175], v122\n\t"
      "v_add_f32 v36, v36, v38\n\t"
      "v_mul_f32 v36, s59, v36\n\t"
      "v_smfmac_f32_16x16x64_bf16 v[40:43], v[24:27], a[176:183], v122\n\t"
      "ds_read_b128 v[96:99], v127 offset:33792\n\t"
      "v_mul_f32_e64 v32, s59, v32\n\t"
      "v_smfmac_f32_16x16x64_bf16 v[44:47], v[20:23], a[232:239], v122\n\t"
      "v_add_f32_e64 v37, v37, v39\n\t"
      "v_smfmac_f32_16x16x64_bf16 v[44:47], v[24:27], a[240:247], v122\n\t"
      "ds_read_b128 v[60:63], v127 offset:37888\n\t"
      "v_mul_f32_e64 v37, s59, v37\n\t"
      "v_cvt_pk_bf16_f32 v112, v32, v33\n\t"
      "v_smfmac_f32_16x16x64_bf16 v[40:43], v[28:31], a[184:191], v122\n\t"
      "s_add_u32 s43, s43, 0x8000\n\t"
      "ds_read_b128 v[92:95], v127 offset:34816\n\t"
      "v_cvt_pk_bf16_f32 v113, v36, v37\n\t"
      "v_smfmac_f32_16x16x64_bf16 v[44:47], v[28:31], a[248:255], v122\n\t"
      "ds_write_b64 v125, v[112:113]\n\t"
      "v_smfmac_f32_16x16x64_bf16 v[48:51], v[4:7], v[136:143], v122\n\t"
      "ds_read_b128 v[56:59], v127 offset:38912\n\t"
      "v_add_f32 v40, v40, v42\n\t"
      "v_add_f32 v41, v41, v43\n\t"
      "v_cvt_pk_bf16_f32 v120, v40, v41\n\t"
      "v_add_f32 v45, v45, v47\n\t"
      "v_add_f32 v44, v44, v46\n\t"
      "v_cvt_pk_bf16_f32 v121, v44, v45\n\t"
      "v_smfmac_f32_16x16x64_bf16 v[48:51], v[8:11], v[144:151], v122\n\t"
      "ds_write_b64 v125, v[120:121] offset:2080\n\t"
      "v_smfmac_f32_16x16x64_bf16 v[48:51], v[12:15], v[152:159], v122\n\t"
      "s_waitcnt lgkmcnt(2)\n\t"
      "s_barrier\n\t"
      "ds_read_b32 v108, v109 offset:55392\n\t"
      "v_smfmac_f32_16x16x64_bf16 v[48:51], v[16:19], v[160:167], v122\n\t"
      "ds_read_b64_tr_b16 v[32:33], v124 offset:49152\n\t"
      "ds_read_b64_tr_b16 v[34:35], v124 offset:49672\n\t"
      "v_smfmac_f32_16x16x64_bf16 v[48:51], v[20:23], v[168:175], v122\n\t"
      "ds_read_b64_tr_b16 v[38:39], v124 offset:50712\n\t"
      "ds_read_b64_tr_b16 v[36:37], v124 offset:50192\n\t"
      "s_waitcnt lgkmcnt(5)\n\t"
      "v_smfmac_f32_16x16x64_bf16 v[48:51], v[24:27], v[176:183], v122\n\t"
      "s_barrier\n\t"
      "ds_read_b64_tr_b16 v[46:47], v124 offset:52792\n\t"
      "v_smfmac_f32_16x16x64_bf16 v[48:51], v[28:31], v[184:191], v122\n\t"
      "ds_read_b64_tr_b16 v[42:43], v124 offset:51752\n\t"
      "ds_read_b64_tr_b16 v[44:45], v124 offset:52272\n\t"
      "ds_read_b64_tr_b16 v[40:41], v124 offset:51232\n\t"
      "s_waitcnt lgkmcnt(6)\n\t"
      "v_readfirstlane_b32 s61, v108\n\t"
      "v_mov_b32_dpp v102, v34 quad_perm:[2,3,0,1] row_mask:0xf bank_mask:0xf\n\t"
      "v_mfma_f32_16x16x32_bf16 v[116:119], v[80:83], v[32:35], 0\n\t"
      "v_mov_b32_dpp v103, v35 quad_perm:[2,3,0,1] row_mask:0xf bank_mask:0xf\n\t"
      "v_mov_b32_dpp v100, v32 quad_perm:[2,3,0,1] row_mask:0xf bank_mask:0xf\n\t"
      "v_mfma_f32_16x16x32_bf16 v[112:115], v[76:79], v[32:35], 0\n\t"
      "s_waitcnt lgkmcnt(4)\n\t"
      "s_lshr_b32 s61, s61, 23\n\t"
      "v_mov_b32_dpp v101, v33 quad_perm:[2,3,0,1] row_mask:0xf bank_mask:0xf\n\t"
      "v_mfma_f32_16x16x32_bf16 v[112:115], v[84:87], v[36:39], v[112:115]\n\t"
      "s_add_u32 s61, s61, s67\n\t"
      "s_max_u32 s61, s61, s66\n\t"
      "v_mov_b32_dpp v107, v39 quad_perm:[2,3,0,1] row_mask:0xf bank_mask:0xf\n\t"
      "s_waitcnt lgkmcnt(0)\n\t"
      "v_mfma_f32_16x16x32_bf16 v[76:79], v[40:43], v[32:35], 0\n\t"
      "s_lshr_b32 s61, s61, 1\n\t"
      "buffer_load_dwordx4 v127, s[36:39], s46 offen sc1 lds\n\t"
      "v_mfma_f32_16x16x32_bf16 v[76:79], v[44:47], v[36:39], v[76:79]\n\t"
      "s_min_u32 s61, s61, 200\n\t"
      "v_mov_b32_dpp v106, v38 quad_perm:[2,3,0,1] row_mask:0xf bank_mask:0xf\n\t"
      "v_mov_b32_dpp v105, v37 quad_perm:[2,3,0,1] row_mask:0xf bank_mask:0xf\n\t"
      "v_mfma_f32_16x16x32_bf16 v[112:115], v[92:95], v[100:103], v[112:115]\n\t"
      "s_lshl_b32 s61, s61, 23\n\t"
      "v_add_f32 v49, v49, v51\n\t"
      "v_mov_b32_dpp v104, v36 quad_perm:[2,3,0,1] row_mask:0xf bank_mask:0xf\n\t"
      "v_mfma_f32_16x16x32_bf16 v[116:119], v[88:91], v[36:39], v[116:119]\n\t"
      "s_add_u32 s65, s61, 33554432\n\t"
      "v_cndmask_b32_e64 v121, v77, v79, s[72:73]\n\t"
      "v_cndmask_b32_e64 v120, v76, v78, s[72:73]\n\t"
      "v_mfma_f32_16x16x32_bf16 v[112:115], v[56:59], v[104:107], v[112:115]\n\t"
      "buffer_load_dwordx4 v127, s[36:39], s46 offen offset:1024 sc1 lds\n\t"
      "v_mfma_f32_16x16x32_bf16 v[116:119], v[96:99], v[100:103], v[116:119]\n\t"
      "s_pack_hh_b32_b16 s75, s65, s65\n\t"
      "v_add_f32 v48, v48, v50\n\t"
      "v_add_f32_dpp v120, v120, v120 quad_perm:[2,3,0,1] row_mask:0xf bank_mask:0xf\n\t"
      "v_add_f32_dpp v121, v121, v121 quad_perm:[2,3,0,1] row_mask:0xf bank_mask:0xf\n\t"
      "v_mfma_f32_16x16x32_bf16 v[116:119], v[60:63], v[104:107], v[116:119]\n\t"
      "s_mov_b32 m0, s45\n\t"
      "v_exp_f32 v81, v121\n\t"
      "v_exp_f32_e64 v80, v120\n\t"
      "v_cvt_pk_bf16_f32 v64, v48, v49\n\t"
      "v_cvt_pk_bf16_f32 v108, v80, v81\n\t"
      "v_smfmac_f32_16x16x64_bf16 v[52:55], v[4:7], v[200:207], v122\n\t"
      "buffer_load_dwordx4 v65, s[36:39], s44 offen sc1 lds\n\t"
      "v_mfma_f32_16x16x32_bf16 v[84:87], v[108:111], v[64:67], 0\n\t"
      "s_pack_hh_b32_b16 s74, s65, s65\n\t"
      "v_mov_b64 v[88:89], s[74:75]\n\t"
      "v_cndmask_b32_e64 v112, v112, v116, s[72:73]\n\t"
      "v_cndmask_b32_e64 v115, v115, v119, s[72:73]\n\t"
      "v_cndmask_b32_e64 v113, v113, v117, s[72:73]\n\t"
      "v_exp_f32 v59, v115\n\t"
      "v_exp_f32 v56, v112\n\t"
      "v_exp_f32_e64 v57, v113\n\t"
      "v_cndmask_b32_e64 v114, v114, v118, s[72:73]\n\t"
      "v_cvt_pk_bf16_f32 v96, v56, v57\n\t"
      "v_exp_f32 v58, v114\n\t"
      "v_mov_b64 v[90:91], s[74:75]\n\t"
      "v_cvt_pk_bf16_f32 v97, v58, v59\n\t"
      "v_mov_b32_dpp v98, v96 quad_perm:[2,3,0,1] row_mask:0xf bank_mask:0xf\n\t"
      "v_mfma_f32_16x16x32_bf16 v[48:51], v[108:111], v[88:91], 0\n\t"
      "v_mov_b32_dpp v99, v97 quad_perm:[2,3,0,1] row_mask:0xf bank_mask:0xf\n\t"
      "v_max3_f32 v82, |v112|, |v113|, |v114|\n\t"
      "v_max3_f32 v83, |v115|, |v120|, |v121|\n\t"
      "v_mfma_f32_16x16x32_bf16 v[48:51], v[96:99], v[88:91], v[48:51]\n\t"
      "buffer_load_dwordx4 v65, s[36:39], s44 offen offset:1024 sc1 lds\n\t"
      "v_mfma_f32_16x16x32_bf16 v[84:87], v[96:99], v[68:71], v[84:87]\n\t"
      "s_add_u32 s57, s61, 310378496\n\t"
      "v_max_f32 v82, v82, v83\n\t"
      "s_nop 1\n\t"
      "v_rcp_f32_e64 v48, v48\n\t"
      "v_cmp_nge_f32 vcc, 0x42000000, v82\n\t"
      "v_rcp_f32 v49, v49\n\t"
      "s_nop 0\n\t"
      "v_fma_f32 v85, v85, v49, s96\n\t"
      "s_cbranch_vccnz LMX_0%=\n\t"
      "buffer_atomic_add v85, v123, s[36:39], s97 offen offset:512\n\t"
      "v_fma_f32 v84, v84, v48, s96\n\t"
      "v_smfmac_f32_16x16x64_bf16 v[52:55], v[8:11], v[208:215], v122\n\t"
      "buffer_atomic_add v84, v123, s[36:39], s97 offen\n\t"
      "v_smfmac_f32_16x16x64_bf16 v[52:55], v[12:15], v[216:223], v122\n\t"
      "v_smfmac_f32_16x16x64_bf16 v[52:55], v[16:19], v[224:231], v122\n\t"
      "v_smfmac_f32_16x16x64_bf16 v[52:55], v[20:23], v[232:239], v122\n\t"
      "v_smfmac_f32_16x16x64_bf16 v[52:55], v[24:27], v[240:247], v122\n\t"
      "v_smfmac_f32_16x16x64_bf16 v[52:55], v[28:31], v[248:255], v122\n\t"
      "v_mfma_f32_16x16x32_bf16 v[92:95], v[96:99], v[72:75], 0\n\t"
      "s_add_u32 s62, s61, 0x83400000\n\t"
      "s_nop 0\n\t"
      "v_mov_b32 v126, s62\n\t"
      "s_nop 0\n\t"
      "v_add_f32 v53, v53, v55\n\t"
      "v_add_f32 v52, v52, v54\n\t"
      "v_cvt_pk_bf16_f32 v66, v52, v53\n\t"
      "s_add_u32 s68, s68, 0x8000\n\t"
      "s_add_u32 s97, s97, 0x8000\n\t"
      "v_mfma_f32_16x16x32_bf16 v[92:95], v[108:111], v[66:69], v[92:95]\n\t"
      "s_nop 0\n\t"
      "s_nop 6\n\t"
      "v_fma_f32 v95, v95, v49, s96\n\t"
      "buffer_atomic_add v95, v123, s[36:39], s69 offen offset:512\n\t"
      "v_fma_f32 v94, v94, v48, s96\n\t"
      "buffer_atomic_add v94, v123, s[36:39], s69 offen\n\t"
      "LWIN_0%=:\n\t"
      "v_add_u32 v100, s55, v127\n\t"
      "ds_read_b128 v[60:63], v100 offset:57344\n\t"
      "s_cmp_eq_u32 s56, 0\n\t"
      "s_cbranch_scc0 LSLOW_0%=\n\t"
      "LWIN1_0%=:\n\t"
      "buffer_load_dwordx2 v[120:121], off, s[36:39], s50 sc1\n\t"
      "s_mov_b32 s64, s50\n\t"
      "s_cmp_lt_i32 s40, 2\n\t"
      "s_cbranch_scc1 LEARLY_0%=\n\t"
      "LROLE_0%=:\n\t"
      "v_add_u32 v52, 65536, v100\n\t"
      "ds_read_b128 v[88:91], v52\n\t"
      "ds_read_b128 v[92:95], v52 offset:1024\n\t"
      "ds_read_b128 v[32:35], v52 offset:4096\n\t"
      "ds_read_b128 v[36:39], v52 offset:5120\n\t"
      "ds_read_b128 v[40:43], v52 offset:8192\n\t"
      "ds_read_b128 v[44:47], v52 offset:9216\n\t"
      "v_mov_b64 v[48:49], 0\n\t"
      "v_mov_b64 v[50:51], 0\n\t"
      "v_add_u32 v104, s71, v100\n\t"
      "ds_read_b128 v[76:79], v104\n\t"
      "ds_read_b128 v[80:83], v104 offset:1024\n\t"
      "ds_read_b128 v[112:115], v100\n\t"
      "ds_read_b128 v[116:119], v100 offset:1024\n\t"
      "v_xor_b32 v101, 2048, v104\n\t"
      "v_xor_b32 v102, 4096, v104\n\t"
      "v_xor_b32 v103, 6144, v104\n\t"
      "s_waitcnt lgkmcnt(8)\n\t"
      "v_smfmac_f32_16x16x64_bf16 v[48:51], v[0:3], v[88:95], v122\n\t"
      "ds_read_b128 v[88:91], v52 offset:12288\n\t"
      "ds_read_b128 v[92:95], v52 offset:13312\n\t"
      "s_add_u32 m0, s55, s71\n\t"
      "s_sub_u32 s71, 204800, s71\n\t"
      "s_waitcnt lgkmcnt(8)\n\t"
      "v_smfmac_f32_16x16x64_bf16 v[48:51], v[4:7], v[32:39], v122\n\t"
      "ds_read_b128 v[32:35], v52 offset:16384\n\t"
      "ds_read_b128 v[36:39], v52 offset:17408\n\t"
      "s_add_u32 s40, s40, 1\n\t"
      "s_cmp_lt_u32 s40, 63\n\t"
      "s_waitcnt lgkmcnt(8)\n\t"
      "v_smfmac_f32_16x16x64_bf16 v[48:51], v[8:11], v[40:47], v122\n\t"
      "ds_read_b128 v[40:43], v52 offset:20480\n\t"
      "ds_read_b128 v[44:47], v52 offset:21504\n\t"
      "s_cselect_b32 s61, 786432, 0\n\t"
      "s_cselect_b32 s60, s58, 0\n\t"
      "s_waitcnt lgkmcnt(4)\n\t"
      "v_smfmac_f32_16x16x64_bf16 v[48:51], v[12:15], v[88:95], v122\n\t"
      "ds_read_b128 v[88:91], v52 offset:24576\n\t"
      "ds_read_b128 v[92:95], v52 offset:25600\n\t"
      "s_add_u32 s44, s44, s61\n\t"
      "s_cmp_lt_u32 s40, 62\n\t"
      "s_waitcnt lgkmcnt(4)\n\t"
      "v_smfmac_f32_16x16x64_bf16 v[48:51], v[16:19], v[32:39], v122\n\t"
      "ds_read_b128 v[32:35], v52 offset:28672\n\t"
      "ds_read_b128 v[36:39], v52 offset:29696\n\t"
      "s_cselect_b32 s61, 786432, 0\n\t"
      "s_add_u32 s46, s46, s61\n\t"
      "s_waitcnt lgkmcnt(4)\n\t"
      "v_smfmac_f32_16x16x64_bf16 v[48:51], v[20:23], v[40:47], v122\n\t"
      "ds_read_b128 v[0:3], v101\n\t"
      "ds_read_b128 v[4:7], v101 offset:1024\n\t"
      "s_add_u32 s61, s40, 3\n\t"
      "s_and_b32 s62, s61, 3\n\t"
      "s_waitcnt lgkmcnt(4)\n\t"
      "v_smfmac_f32_16x16x64_bf16 v[48:51], v[24:27], v[88:95], v122\n\t"
      "ds_read_b128 v[8:11], v100 offset:8192\n\t"
      "ds_read_b128 v[12:15], v100 offset:9216\n\t"
      "s_cselect_b32 s62, 16, 2000\n\t"
      "s_cmp_lt_u32 s61, 64\n\t"
      "s_waitcnt lgkmcnt(4)\n\t"
      "v_smfmac_f32_16x16x64_bf16 v[48:51], v[28:31], v[32:39], v122\n\t"
      "s_cselect_b32 s62, s62, 0\n\t"
      "s_add_u32 s50, s50, s62\n\t"
      "ds_read_b128 v[16:19], v102\n\t"
      "ds_read_b128 v[20:23], v102 offset:1024\n\t"
      "ds_read_b128 v[24:27], v100 offset:16384\n\t"
      "ds_read_b128 v[28:31], v100 offset:17408\n\t"
      "s_mov_b32 s63, 0x2000\n\t"
      "s_cmp_eq_u32 s40, 64\n\t"
      "v_add_f32 v48, v48, v50\n\t"
      "v_add_f32 v49, v49, v51\n\t"
      "v_cvt_pk_bf16_f32 v48, v48, v49\n\t"
      "buffer_store_dword v48, v62, s[36:39], s49 offen sc1\n\t"
      "s_cbranch_scc1 LEND%=\n\t"
      "s_add_u32 s49, s49, 65536\n\t"
      "v_mfma_f32_16x16x32_bf16 v[32:35], v[76:79], v[112:115], 0\n\t"
      "v_mfma_f32_16x16x32_bf16 v[36:39], v[80:83], v[112:115], 0\n\t"
      "v_mfma_f32_16x16x32_bf16 v[40:43], v[76:79], v[116:119], 0\n\t"
      "v_mfma_f32_16x16x32_bf16 v[44:47], v[80:83], v[116:119], 0\n\t"
      "ds_read_b128 v[76:79], v103\n\t"
      "ds_read_b128 v[80:83], v103 offset:1024\n\t"
      "ds_read_b128 v[112:115], v100 offset:24576\n\t"
      "ds_read_b128 v[116:119], v100 offset:25600\n\t"
      "s_waitcnt lgkmcnt(8)\n\t"
      "v_mfma_f32_16x16x32_bf16 v[32:35], v[0:3], v[8:11], v[32:35]\n\t"
      "v_mfma_f32_16x16x32_bf16 v[36:39], v[4:7], v[8:11], v[36:39]\n\t"
      "v_mfma_f32_16x16x32_bf16 v[40:43], v[0:3], v[12:15], v[40:43]\n\t"
      "v_mfma_f32_16x16x32_bf16 v[44:47], v[4:7], v[12:15], v[44:47]\n\t"
      "LBAR_0%=:\n\t"
      "s_cmp_gt_u32 s40, 1\n\t"
      "s_barrier\n\t"
      "s_cbranch_scc1 LPOLL_0%=\n\t"
      "s_branch LFIRST_0%=\n\t"
      ".p2align 6\n\t"
      "LPOLL_1%=:\n\t"
      "buffer_load_dwordx4 v[84:87], v60, s[36:39], s43 offen sc1\n\t"
      "buffer_load_dwordx4 v[88:91], v60, s[36:39], s43 offen offset:1024 sc1\n\t"
      "s_waitcnt lgkmcnt(4)\n\t"
      "v_mfma_f32_16x16x32_bf16 v[32:35], v[16:19], v[24:27], v[32:35]\n\t"
      "v_mfma_f32_16x16x32_bf16 v[36:39], v[20:23], v[24:27], v[36:39]\n\t"
      "v_mfma_f32_16x16x32_bf16 v[40:43], v[16:19], v[28:31], v[40:43]\n\t"
      "v_mfma_f32_16x16x32_bf16 v[44:47], v[20:23], v[28:31], v[44:47]\n\t"
      "buffer_load_dwordx4 v[92:95], v60, s[36:39], s68 offen sc1\n\t"
      "buffer_load_dwordx4 v[96:99], v60, s[36:39], s68 offen offset:1024 sc1\n\t"
      "v_mov_b64 v[48:49], 0\n\t"
      "v_mov_b64 v[50:51], 0\n\t"
      "v_mov_b64 v[52:53], 0\n\t"
      "v_mov_b64 v[54:55], 0\n\t"
      "s_xor_b32 s69, s97, 1024\n\t"
      "s_waitcnt lgkmcnt(0)\n\t"
      "v_mfma_f32_16x16x32_bf16 v[32:35], v[76:79], v[112:115], v[32:35]\n\t"
      "v_mfma_f32_16x16x32_bf16 v[36:39], v[80:83], v[112:115], v[36:39]\n\t"
      "v_mfma_f32_16x16x32_bf16 v[40:43], v[76:79], v[116:119], v[40:43]\n\t"
      "v_mfma_f32_16x16x32_bf16 v[44:47], v[80:83], v[116:119], v[44:47]\n\t"
      "s_mov_b64 exec, 1\n\t"
      "ds_write_b64 v109, v[110:111] offset:55392\n\t"
      "s_mov_b64 exec, -1\n\t"
      "s_nop 4\n\t"
      "v_cvt_pk_bf16_f32 v68, v32, v33\n\t"
      "v_cvt_pk_bf16_f32 v69, v34, v35\n\t"
      "v_cvt_pk_bf16_f32 v70, v36, v37\n\t"
      "v_cvt_pk_bf16_f32 v71, v38, v39\n\t"
      "v_cvt_pk_bf16_f32 v74, v40, v41\n\t"
      "v_cvt_pk_bf16_f32 v75, v42, v43\n\t"
      "v_cvt_pk_bf16_f32 v72, v44, v45\n\t"
      "v_cvt_pk_bf16_f32 v73, v46, v47\n\t"
      "v_mov_b64 v[32:33], 0\n\t"
      "v_mov_b64 v[34:35], 0\n\t"
      "v_mov_b64 v[36:37], 0\n\t"
      "v_mov_b64 v[38:39], 0\n\t"
      "v_mov_b64 v[40:41], 0\n\t"
      "v_mov_b64 v[42:43], 0\n\t"
      "v_mov_b64 v[44:45], 0\n\t"
      "v_mov_b64 v[46:47], 0\n\t"
      "s_nop 0\n\t"
      "s_nop 0\n\t"
      "s_waitcnt vmcnt(3)\n\t"
      "v_pk_fma_f32 v[0:1], v[84:85], s[56:57], v[126:127] op_sel:[0,1,0] op_sel_hi:[1,1,0]\n\t"
      "v_pk_fma_f32 v[2:3], v[86:87], s[56:57], v[126:127] op_sel:[0,1,0] op_sel_hi:[1,1,0]\n\t"
      "v_cvt_pk_bf16_f32 v4, v0, v1\n\t"
      "v_cvt_pk_bf16_f32 v5, v2, v3\n\t"
      "s_nop 0\n\t"
      "ds_write_b64 v127, v[4:5] offset:41984\n\t"
      "s_waitcnt vmcnt(2)\n\t"
      "v_pk_fma_f32 v[14:15], v[90:91], s[56:57], v[126:127] op_sel:[0,1,0] op_sel_hi:[1,1,0]\n\t"
      "v_pk_fma_f32 v[12:13], v[88:89], s[56:57], v[126:127] op_sel:[0,1,0] op_sel_hi:[1,1,0]\n\t"
      "v_cvt_pk_bf16_f32 v7, v14, v15\n\t"
      "v_cvt_pk_bf16_f32 v6, v12, v13\n\t"
      "ds_write_b64 v127, v[6:7] offset:41992\n\t"
      "v_bitop3_b32 v64, v84, v85, v86 bitop3:0x80\n\t"
      "v_bitop3_b32 v64, v64, v87, v88 bitop3:0x80\n\t"
      "v_bitop3_b32 v64, v64, v89, v90 bitop3:0x80\n\t"
      "v_bitop3_b32 v120, v120, v121, s53 bitop3:0x7e\n\t"
      "v_bitop3_b32 v64, v64, v91, s58 bitop3:0x80\n\t"
      "v_cmp_eq_u32 vcc, 0, v64\n\t"
      "s_cbranch_vccnz LR0_1%=\n\t"
      "LC0_1%=:\n\t"
      "v_smfmac_f32_16x16x64_bf16 v[44:47], v[4:7], a[200:207], v122\n\t"
      "s_waitcnt lgkmcnt(0)\n\t"
      "v_smfmac_f32_16x16x64_bf16 v[32:35], v[4:7], a[8:15], v122\n\t"
      "s_barrier\n\t"
      "v_mfma_f32_16x16x32_bf16 v[56:59], v[4:7], v[4:7], 0\n\t"
      "ds_read_b128 v[0:3], v127 offset:40960\n\t"
      "v_smfmac_f32_16x16x64_bf16 v[36:39], v[4:7], a[72:79], v122\n\t"
      "ds_read_b128 v[76:79], v127 offset:32768\n\t"
      "v_smfmac_f32_16x16x64_bf16 v[52:55], v[4:7], v[200:207], v122\n\t"
      "ds_read_b128 v[8:11], v127 offset:43008\n\t"
      "v_smfmac_f32_16x16x64_bf16 v[40:43], v[4:7], a[136:143], v122\n\t"
      "ds_read_b128 v[12:15], v127 offset:44032\n\t"
      "v_smfmac_f32_16x16x64_bf16 v[48:51], v[4:7], v[136:143], v122\n\t"
      "s_waitcnt lgkmcnt(3)\n\t"
      "v_smfmac_f32_16x16x64_bf16 v[32:35], v[0:3], a[0:7], v122\n\t"
      "s_waitcnt vmcnt(1)\n\t"
      "v_fma_f32 v18, v94, s57, v126\n\t"
      "v_bitop3_b32 v64, v92, v93, v94 bitop3:0x80\n\t"
      "v_smfmac_f32_16x16x64_bf16 v[44:47], v[0:3], a[192:199], v122\n\t"
      "ds_read_b128 v[80:83], v127 offset:35840\n\t"
      "v_fma_f32 v16, v92, s57, v126\n\t"
      "v_fma_f32 v17, v93, s57, v126\n\t"
      "v_smfmac_f32_16x16x64_bf16 v[36:39], v[0:3], a[64:71], v122\n\t"
      "v_cvt_pk_bf16_f32 v20, v16, v17\n\t"
      "v_fma_f32 v19, v95, s57, v126\n\t"
      "v_smfmac_f32_16x16x64_bf16 v[40:43], v[0:3], a[128:135], v122\n\t"
      "s_waitcnt lgkmcnt(1)\n\t"
      "s_waitcnt vmcnt(0)\n\t"
      "v_fma_f32 v30, v98, s57, v126\n\t"
      "v_cvt_pk_bf16_f32 v21, v18, v19\n\t"
      "v_fma_f32 v28, v96, s57, v126\n\t"
      "v_smfmac_f32_16x16x64_bf16 v[36:39], v[12:15], a[88:95], v122\n\t"
      "ds_write_b64 v127, v[20:21] offset:46080\n\t"
      "v_fma_f32 v31, v99, s57, v126\n\t"
      "v_bitop3_b32 v64, v64, v95, v96 bitop3:0x80\n\t"
      "v_fma_f32 v29, v97, s57, v126\n\t"
      "v_cvt_pk_bf16_f32 v22, v28, v29\n\t"
      "v_cvt_pk_bf16_f32 v23, v30, v31\n\t"
      "v_smfmac_f32_16x16x64_bf16 v[36:39], v[8:11], a[80:87], v122\n\t"
      "ds_write_b64 v127, v[22:23] offset:46088\n\t"
      "v_bitop3_b32 v64, v64, v97, v98 bitop3:0x80\n\t"
      "v_bitop3_b32 v64, v64, v99, s60 bitop3:0x80\n\t"
      "v_smfmac_f32_16x16x64_bf16 v[44:47], v[12:15], a[216:223], v122\n\t"
      "ds_read_b128 v[84:87], v127 offset:36864\n\t"
      "v_cmp_eq_u32_e64 vcc, 0, v64\n\t"
      "v_smfmac_f32_16x16x64_bf16 v[40:43], v[12:15], a[152:159], v122\n\t"
      "v_smfmac_f32_16x16x64_bf16 v[32:35], v[12:15], a[24:31], v122\n\t"
      "s_cbranch_vccnz LR1_1%=\n\t"
      "LC1_1%=:\n\t"
      "v_mfma_f32_16x16x32_bf16 v[56:59], v[20:23], v[20:23], v[56:59]\n\t"
      "s_waitcnt lgkmcnt(1)\n\t"
      "v_readfirstlane_b32 s56, v120\n\t"
      "v_smfmac_f32_16x16x64_bf16 v[32:35], v[8:11], a[16:23], v122\n\t"
      "s_barrier\n\t"
      "v_smfmac_f32_16x16x64_bf16 v[40:43], v[8:11], a[144:151], v122\n\t"
      "ds_read_b128 v[16:19], v127 offset:45056\n\t"
      "v_smfmac_f32_16x16x64_bf16 v[44:47], v[8:11], a[208:215], v122\n\t"
      "ds_read_b128 v[24:27], v127 offset:47104\n\t"
      "v_smfmac_f32_16x16x64_bf16 v[32:35], v[20:23], a[40:47], v122\n\t"
      "ds_read_b128 v[28:31], v127 offset:48128\n\t"
      "s_mov_b32 s58, 0x10000000\n\t"
      "v_smfmac_f32_16x16x64_bf16 v[36:39], v[20:23], a[104:111], v122\n\t"
      "v_max3_f32 v56, v56, v57, v58\n\t"
      "v_smfmac_f32_16x16x64_bf16 v[44:47], v[20:23], a[232:239], v122\n\t"
      "s_waitcnt lgkmcnt(2)\n\t"
      "v_max_f32 v56, v56, v59\n\t"
      "v_smfmac_f32_16x16x64_bf16 v[32:35], v[16:19], a[32:39], v122\n\t"
      "ds_read_b128 v[88:91], v127 offset:39936\n\t"
      "v_max_f32_dpp v56, v56, v56 quad_perm:[1,0,3,2] row_mask:0xf bank_mask:0xf\n\t"
      "v_smfmac_f32_16x16x64_bf16 v[36:39], v[16:19], a[96:103], v122\n\t"
      "s_waitcnt lgkmcnt(1)\n\t"
      "s_nop 0\n\t"
      "v_smfmac_f32_16x16x64_bf16 v[32:35], v[28:31], a[56:63], v122\n\t"
      "v_max_f32_dpp v56, v56, v56 quad_perm:[2,3,0,1] row_mask:0xf bank_mask:0xf\n\t"
      "v_smfmac_f32_16x16x64_bf16 v[32:35], v[24:27], a[48:55], v122\n\t"
      "s_mov_b32 exec_lo, 0x100001\n\t"
      "v_smfmac_f32_16x16x64_bf16 v[36:39], v[28:31], a[120:127], v122\n\t"
      "s_mov_b32 exec_hi, 0x10000100\n\t"
      "ds_max_i32 v109, v56 offset:55392\n\t"
      "v_smfmac_f32_16x16x64_bf16 v[36:39], v[24:27], a[112:119], v122\n\t"
      "s_mov_b64 exec, -1\n\t"
      "v_add_f32 v32, v32, v34\n\t"
      "v_smfmac_f32_16x16x64_bf16 v[40:43], v[20:23], a[168:175], v122\n\t"
      "v_add_f32 v33, v33, v35\n\t"
      "v_mul_f32 v33, s59, v33\n\t"
      "v_smfmac_f32_16x16x64_bf16 v[40:43], v[16:19], a[160:167], v122\n\t"
      "v_add_f32 v36, v36, v38\n\t"
      "v_mul_f32 v36, s59, v36\n\t"
      "v_smfmac_f32_16x16x64_bf16 v[40:43], v[28:31], a[184:191], v122\n\t"
      "ds_read_b128 v[96:99], v127 offset:33792\n\t"
      "v_mul_f32_e64 v32, s59, v32\n\t"
      "v_smfmac_f32_16x16x64_bf16 v[44:47], v[16:19], a[224:231], v122\n\t"
      "v_add_f32_e64 v37, v37, v39\n\t"
      "v_smfmac_f32_16x16x64_bf16 v[44:47], v[28:31], a[248:255], v122\n\t"
      "ds_read_b128 v[60:63], v127 offset:37888\n\t"
      "v_mul_f32_e64 v37, s59, v37\n\t"
      "v_cvt_pk_bf16_f32 v112, v32, v33\n\t"
      "v_smfmac_f32_16x16x64_bf16 v[40:43], v[24:27], a[176:183], v122\n\t"
      "s_add_u32 s43, s43, 0x8000\n\t"
      "ds_read_b128 v[92:95], v127 offset:34816\n\t"
      "v_cvt_pk_bf16_f32 v113, v36, v37\n\t"
      "v_smfmac_f32_16x16x64_bf16 v[44:47], v[24:27], a[240:247], v122\n\t"
      "ds_write_b64 v125, v[112:113]\n\t"
      "v_smfmac_f32_16x16x64_bf16 v[48:51], v[0:3], v[128:135], v122\n\t"
      "ds_read_b128 v[56:59], v127 offset:38912\n\t"
      "v_add_f32 v40, v40, v42\n\t"
      "v_add_f32 v41, v41, v43\n\t"
      "v_cvt_pk_bf16_f32 v120, v40, v41\n\t"
      "v_add_f32 v45, v45, v47\n\t"
      "v_add_f32 v44, v44, v46\n\t"
      "v_cvt_pk_bf16_f32 v121, v44, v45\n\t"
      "v_smfmac_f32_16x16x64_bf16 v[48:51], v[12:15], v[152:159], v122\n\t"
      "ds_write_b64 v125, v[120:121] offset:2080\n\t"
      "v_smfmac_f32_16x16x64_bf16 v[48:51], v[8:11], v[144:151], v122\n\t"
      "s_waitcnt lgkmcnt(2)\n\t"
      "s_barrier\n\t"
      "ds_read_b32 v108, v109 offset:55392\n\t"
      "v_smfmac_f32_16x16x64_bf16 v[48:51], v[20:23], v[168:175], v122\n\t"
      "ds_read_b64_tr_b16 v[32:33], v124 offset:49152\n\t"
      "ds_read_b64_tr_b16 v[34:35], v124 offset:49672\n\t"
      "v_smfmac_f32_16x16x64_bf16 v[48:51], v[16:19], v[160:167], v122\n\t"
      "ds_read_b64_tr_b16 v[38:39], v124 offset:50712\n\t"
      "ds_read_b64_tr_b16 v[36:37], v124 offset:50192\n\t"
      "s_waitcnt lgkmcnt(5)\n\t"
      "v_smfmac_f32_16x16x64_bf16 v[48:51], v[28:31], v[184:191], v122\n\t"
      "s_barrier\n\t"
      "ds_read_b64_tr_b16 v[46:47], v124 offset:52792\n\t"
      "v_smfmac_f32_16x16x64_bf16 v[48:51], v[24:27], v[176:183], v122\n\t"
      "ds_read_b64_tr_b16 v[42:43], v124 offset:51752\n\t"
      "ds_read_b64_tr_b16 v[44:45], v124 offset:52272\n\t"
      "ds_read_b64_tr_b16 v[40:41], v124 offset:51232\n\t"
      "s_waitcnt lgkmcnt(6)\n\t"
      "v_readfirstlane_b32 s61, v108\n\t"
      "v_mov_b32_dpp v102, v34 quad_perm:[2,3,0,1] row_mask:0xf bank_mask:0xf\n\t"
      "v_mfma_f32_16x16x32_bf16 v[116:119], v[80:83], v[32:35], 0\n\t"
      "v_mov_b32_dpp v103, v35 quad_perm:[2,3,0,1] row_mask:0xf bank_mask:0xf\n\t"
      "v_mov_b32_dpp v100, v32 quad_perm:[2,3,0,1] row_mask:0xf bank_mask:0xf\n\t"
      "v_mfma_f32_16x16x32_bf16 v[112:115], v[76:79], v[32:35], 0\n\t"
      "s_waitcnt lgkmcnt(4)\n\t"
      "s_lshr_b32 s61, s61, 23\n\t"
      "v_mov_b32_dpp v101, v33 quad_perm:[2,3,0,1] row_mask:0xf bank_mask:0xf\n\t"
      "v_mfma_f32_16x16x32_bf16 v[112:115], v[84:87], v[36:39], v[112:115]\n\t"
      "s_add_u32 s61, s61, s67\n\t"
      "s_max_u32 s61, s61, s66\n\t"
      "v_mov_b32_dpp v107, v39 quad_perm:[2,3,0,1] row_mask:0xf bank_mask:0xf\n\t"
      "s_waitcnt lgkmcnt(0)\n\t"
      "v_mfma_f32_16x16x32_bf16 v[76:79], v[40:43], v[32:35], 0\n\t"
      "s_lshr_b32 s61, s61, 1\n\t"
      "buffer_load_dwordx4 v127, s[36:39], s46 offen sc1 lds\n\t"
      "v_mfma_f32_16x16x32_bf16 v[76:79], v[44:47], v[36:39], v[76:79]\n\t"
      "s_min_u32 s61, s61, 200\n\t"
      "v_mov_b32_dpp v106, v38 quad_perm:[2,3,0,1] row_mask:0xf bank_mask:0xf\n\t"
      "v_mov_b32_dpp v105, v37 quad_perm:[2,3,0,1] row_mask:0xf bank_mask:0xf\n\t"
      "v_mfma_f32_16x16x32_bf16 v[112:115], v[92:95], v[100:103], v[112:115]\n\t"
      "s_lshl_b32 s61, s61, 23\n\t"
      "v_add_f32 v49, v49, v51\n\t"
      "v_mov_b32_dpp v104, v36 quad_perm:[2,3,0,1] row_mask:0xf bank_mask:0xf\n\t"
      "v_mfma_f32_16x16x32_bf16 v[116:119], v[88:91], v[36:39], v[116:119]\n\t"
      "s_add_u32 s65, s61, 33554432\n\t"
      "v_cndmask_b32_e64 v121, v77, v79, s[72:73]\n\t"
      "v_cndmask_b32_e64 v120, v76, v78, s[72:73]\n\t"
      "v_mfma_f32_16x16x32_bf16 v[112:115], v[56:59], v[104:107], v[112:115]\n\t"
      "buffer_load_dwordx4 v127, s[36:39], s46 offen offset:1024 sc1 lds\n\t"
      "v_mfma_f32_16x16x32_bf16 v[116:119], v[96:99], v[100:103], v[116:119]\n\t"
      "s_pack_hh_b32_b16 s75, s65, s65\n\t"
      "v_add_f32 v48, v48, v50\n\t"
      "v_add_f32_dpp v120, v120, v120 quad_perm:[2,3,0,1] row_mask:0xf bank_mask:0xf\n\t"
      "v_add_f32_dpp v121, v121, v121 quad_perm:[2,3,0,1] row_mask:0xf bank_mask:0xf\n\t"
      "v_mfma_f32_16x16x32_bf16 v[116:119], v[60:63], v[104:107], v[116:119]\n\t"
      "s_mov_b32 m0, s45\n\t"
      "v_exp_f32 v81, v121\n\t"
      "v_exp_f32_e64 v80, v120\n\t"
      "v_cvt_pk_bf16_f32 v64, v48, v49\n\t"
      "v_cvt_pk_bf16_f32 v108, v80, v81\n\t"
      "v_smfmac_f32_16x16x64_bf16 v[52:55], v[0:3], v[192:199], v122\n\t"
      "buffer_load_dwordx4 v65, s[36:39], s44 offen sc1 lds\n\t"
      "v_mfma_f32_16x16x32_bf16 v[84:87], v[108:111], v[64:67], 0\n\t"
      "s_pack_hh_b32_b16 s74, s65, s65\n\t"
      "v_mov_b64 v[88:89], s[74:75]\n\t"
      "v_cndmask_b32_e64 v112, v112, v116, s[72:73]\n\t"
      "v_cndmask_b32_e64 v115, v115, v119, s[72:73]\n\t"
      "v_cndmask_b32_e64 v113, v113, v117, s[72:73]\n\t"
      "v_exp_f32 v59, v115\n\t"
      "v_exp_f32 v56, v112\n\t"
      "v_exp_f32_e64 v57, v113\n\t"
      "v_cndmask_b32_e64 v114, v114, v118, s[72:73]\n\t"
      "v_cvt_pk_bf16_f32 v96, v56, v57\n\t"
      "v_exp_f32 v58, v114\n\t"
      "v_mov_b64 v[90:91], s[74:75]\n\t"
      "v_cvt_pk_bf16_f32 v97, v58, v59\n\t"
      "v_mov_b32_dpp v98, v96 quad_perm:[2,3,0,1] row_mask:0xf bank_mask:0xf\n\t"
      "v_mfma_f32_16x16x32_bf16 v[48:51], v[108:111], v[88:91], 0\n\t"
      "v_mov_b32_dpp v99, v97 quad_perm:[2,3,0,1] row_mask:0xf bank_mask:0xf\n\t"
      "v_max3_f32 v82, |v112|, |v113|, |v114|\n\t"
      "v_max3_f32 v83, |v115|, |v120|, |v121|\n\t"
      "v_mfma_f32_16x16x32_bf16 v[48:51], v[96:99], v[88:91], v[48:51]\n\t"
      "buffer_load_dwordx4 v65, s[36:39], s44 offen offset:1024 sc1 lds\n\t"
      "v_mfma_f32_16x16x32_bf16 v[84:87], v[96:99], v[68:71], v[84:87]\n\t"
      "s_add_u32 s57, s61, 310378496\n\t"
      "v_max_f32 v82, v82, v83\n\t"
      "s_nop 1\n\t"
      "v_rcp_f32_e64 v48, v48\n\t"
      "v_cmp_nge_f32 vcc, 0x42000000, v82\n\t"
      "v_rcp_f32 v49, v49\n\t"
      "s_nop 0\n\t"
      "v_fma_f32 v85, v85, v49, s96\n\t"
      "s_cbranch_vccnz LMX_1%=\n\t"
      "buffer_atomic_add v85, v123, s[36:39], s97 offen offset:512\n\t"
      "v_fma_f32 v84, v84, v48, s96\n\t"
      "v_smfmac_f32_16x16x64_bf16 v[52:55], v[12:15], v[216:223], v122\n\t"
      "buffer_atomic_add v84, v123, s[36:39], s97 offen\n\t"
      "v_smfmac_f32_16x16x64_bf16 v[52:55], v[8:11], v[208:215], v122\n\t"
      "v_smfmac_f32_16x16x64_bf16 v[52:55], v[20:23], v[232:239], v122\n\t"
      "v_smfmac_f32_16x16x64_bf16 v[52:55], v[16:19], v[224:231], v122\n\t"
      "v_smfmac_f32_16x16x64_bf16 v[52:55], v[28:31], v[248:255], v122\n\t"
      "v_smfmac_f32_16x16x64_bf16 v[52:55], v[24:27], v[240:247], v122\n\t"
      "v_mfma_f32_16x16x32_bf16 v[92:95], v[96:99], v[72:75], 0\n\t"
      "s_add_u32 s62, s61, 0x83400000\n\t"
      "s_nop 0\n\t"
      "v_mov_b32 v126, s62\n\t"
      "s_nop 0\n\t"
      "v_add_f32 v53, v53, v55\n\t"
      "v_add_f32 v52, v52, v54\n\t"
      "v_cvt_pk_bf16_f32 v66, v52, v53\n\t"
      "s_add_u32 s68, s68, 0x8000\n\t"
      "s_add_u32 s97, s97, 0x8000\n\t"
      "v_mfma_f32_16x16x32_bf16 v[92:95], v[108:111], v[66:69], v[92:95]\n\t"
      "s_nop 0\n\t"
      "s_nop 6\n\t"
      "v_fma_f32 v95, v95, v49, s96\n\t"
      "buffer_atomic_add v95, v123, s[36:39], s69 offen offset:512\n\t"
      "v_fma_f32 v94, v94, v48, s96\n\t"
      "buffer_atomic_add v94, v123, s[36:39], s69 offen\n\t"
      "LWIN_1%=:\n\t"
      "v_add_u32 v100, s55, v127\n\t"
      "ds_read_b128 v[60:63], v100 offset:57344\n\t"
      "s_cmp_eq_u32 s56, 0\n\t"
      "s_cbranch_scc0 LSLOW_1%=\n\t"
      "LWIN1_1%=:\n\t"
      "buffer_load_dwordx2 v[120:121], off, s[36:39], s50 sc1\n\t"
      "s_mov_b32 s64, s50\n\t"
      "s_cmp_lt_i32 s40, 2\n\t"
      "s_cbranch_scc1 LEARLY_1%=\n\t"
      "LROLE_1%=:\n\t"
      "v_add_u32 v52, 65536, v100\n\t"
      "ds_read_b128 v[88:91], v52\n\t"
      "ds_read_b128 v[92:95], v52 offset:1024\n\t"
      "ds_read_b128 v[32:35], v52 offset:4096\n\t"
      "ds_read_b128 v[36:39], v52 offset:5120\n\t"
      "ds_read_b128 v[40:43], v52 offset:8192\n\t"
      "ds_read_b128 v[44:47], v52 offset:9216\n\t"
      "v_mov_b64 v[48:49], 0\n\t"
      "v_mov_b64 v[50:51], 0\n\t"
      "v_add_u32 v104, s71, v100\n\t"
      "ds_read_b128 v[76:79], v104\n\t"
      "ds_read_b128 v[80:83], v104 offset:1024\n\t"
      "ds_read_b128 v[112:115], v100\n\t"
      "ds_read_b128 v[116:119], v100 offset:1024\n\t"
      "v_xor_b32 v101, 2048, v104\n\t"
      "v_xor_b32 v102, 4096, v104\n\t"
      "v_xor_b32 v103, 6144, v104\n\t"
      "s_waitcnt lgkmcnt(8)\n\t"
      "v_smfmac_f32_16x16x64_bf16 v[48:51], v[0:3], v[88:95], v122\n\t"
      "ds_read_b128 v[88:91], v52 offset:12288\n\t"
      "ds_read_b128 v[92:95], v52 offset:13312\n\t"
      "s_add_u32 m0, s55, s71\n\t"
      "s_sub_u32 s71, 204800, s71\n\t"
      "s_waitcnt lgkmcnt(8)\n\t"
      "v_smfmac_f32_16x16x64_bf16 v[48:51], v[4:7], v[32:39], v122\n\t"
      "ds_read_b128 v[32:35], v52 offset:16384\n\t"
      "ds_read_b128 v[36:39], v52 offset:17408\n\t"
      "s_add_u32 s40, s40, 1\n\t"
      "s_cmp_lt_u32 s40, 63\n\t"
      "s_waitcnt lgkmcnt(8)\n\t"
      "v_smfmac_f32_16x16x64_bf16 v[48:51], v[8:11], v[40:47], v122\n\t"
      "ds_read_b128 v[40:43], v52 offset:20480\n\t"
      "ds_read_b128 v[44:47], v52 offset:21504\n\t"
      "s_cselect_b32 s61, 786432, 0\n\t"
      "s_cselect_b32 s60, s58, 0\n\t"
      "s_waitcnt lgkmcnt(4)\n\t"
      "v_smfmac_f32_16x16x64_bf16 v[48:51], v[12:15], v[88:95], v122\n\t"
      "ds_read_b128 v[88:91], v52 offset:24576\n\t"
      "ds_read_b128 v[92:95], v52 offset:25600\n\t"
      "s_add_u32 s44, s44, s61\n\t"
      "s_cmp_lt_u32 s40, 62\n\t"
      "s_waitcnt lgkmcnt(4)\n\t"
      "v_smfmac_f32_16x16x64_bf16 v[48:51], v[16:19], v[32:39], v122\n\t"
      "ds_read_b128 v[32:35], v52 offset:28672\n\t"
      "ds_read_b128 v[36:39], v52 offset:29696\n\t"
      "s_cselect_b32 s61, 786432, 0\n\t"
      "s_add_u32 s46, s46, s61\n\t"
      "s_waitcnt lgkmcnt(4)\n\t"
      "v_smfmac_f32_16x16x64_bf16 v[48:51], v[20:23], v[40:47], v122\n\t"
      "ds_read_b128 v[0:3], v101\n\t"
      "ds_read_b128 v[4:7], v101 offset:1024\n\t"
      "s_add_u32 s61, s40, 3\n\t"
      "s_and_b32 s62, s61, 3\n\t"
      "s_waitcnt lgkmcnt(4)\n\t"
      "v_smfmac_f32_16x16x64_bf16 v[48:51], v[24:27], v[88:95], v122\n\t"
      "ds_read_b128 v[8:11], v100 offset:8192\n\t"
      "ds_read_b128 v[12:15], v100 offset:9216\n\t"
      "s_cselect_b32 s62, 16, 2000\n\t"
      "s_cmp_lt_u32 s61, 64\n\t"
      "s_waitcnt lgkmcnt(4)\n\t"
      "v_smfmac_f32_16x16x64_bf16 v[48:51], v[28:31], v[32:39], v122\n\t"
      "s_cselect_b32 s62, s62, 0\n\t"
      "s_add_u32 s50, s50, s62\n\t"
      "ds_read_b128 v[16:19], v102\n\t"
      "ds_read_b128 v[20:23], v102 offset:1024\n\t"
      "ds_read_b128 v[24:27], v100 offset:16384\n\t"
      "ds_read_b128 v[28:31], v100 offset:17408\n\t"
      "s_mov_b32 s63, 0x2000\n\t"
      "s_cmp_eq_u32 s40, 64\n\t"
      "v_add_f32 v48, v48, v50\n\t"
      "v_add_f32 v49, v49, v51\n\t"
      "v_cvt_pk_bf16_f32 v48, v48, v49\n\t"
      "buffer_store_dword v48, v62, s[36:39], s49 offen sc1\n\t"
      "s_cbranch_scc1 LEND%=\n\t"
      "s_add_u32 s49, s49, 65536\n\t"
      "v_mfma_f32_16x16x32_bf16 v[32:35], v[76:79], v[112:115], 0\n\t"
      "v_mfma_f32_16x16x32_bf16 v[36:39], v[80:83], v[112:115], 0\n\t"
      "v_mfma_f32_16x16x32_bf16 v[40:43], v[76:79], v[116:119], 0\n\t"
      "v_mfma_f32_16x16x32_bf16 v[44:47], v[80:83], v[116:119], 0\n\t"
      "ds_read_b128 v[76:79], v103\n\t"
      "ds_read_b128 v[80:83], v103 offset:1024\n\t"
      "ds_read_b128 v[112:115], v100 offset:24576\n\t"
      "ds_read_b128 v[116:119], v100 offset:25600\n\t"
      "s_waitcnt lgkmcnt(8)\n\t"
      "v_mfma_f32_16x16x32_bf16 v[32:35], v[0:3], v[8:11], v[32:35]\n\t"
      "v_mfma_f32_16x16x32_bf16 v[36:39], v[4:7], v[8:11], v[36:39]\n\t"
      "v_mfma_f32_16x16x32_bf16 v[40:43], v[0:3], v[12:15], v[40:43]\n\t"
      "v_mfma_f32_16x16x32_bf16 v[44:47], v[4:7], v[12:15], v[44:47]\n\t"
      "LBAR_1%=:\n\t"
      "s_cmp_gt_u32 s40, 1\n\t"
      "s_barrier\n\t"
      "s_cbranch_scc1 LPOLL_1%=\n\t"
      "s_branch LFIRST_1%=\n\t"
      ".p2align 6\n\t"
      "LPOLL_2%=:\n\t"
      "buffer_load_dwordx4 v[84:87], v60, s[36:39], s43 offen sc1\n\t"
      "buffer_load_dwordx4 v[88:91], v60, s[36:39], s43 offen offset:1024 sc1\n\t"
      "s_waitcnt lgkmcnt(4)\n\t"
      "v_mfma_f32_16x16x32_bf16 v[32:35], v[16:19], v[24:27], v[32:35]\n\t"
      "v_mfma_f32_16x16x32_bf16 v[36:39], v[20:23], v[24:27], v[36:39]\n\t"
      "v_mfma_f32_16x16x32_bf16 v[40:43], v[16:19], v[28:31], v[40:43]\n\t"
      "v_mfma_f32_16x16x32_bf16 v[44:47], v[20:23], v[28:31], v[44:47]\n\t"
      "buffer_load_dwordx4 v[92:95], v60, s[36:39], s68 offen sc1\n\t"
      "buffer_load_dwordx4 v[96:99], v60, s[36:39], s68 offen offset:1024 sc1\n\t"
      "v_mov_b64 v[48:49], 0\n\t"
      "v_mov_b64 v[50:51], 0\n\t"
      "v_mov_b64 v[52:53], 0\n\t"
      "v_mov_b64 v[54:55], 0\n\t"
      "s_xor_b32 s69, s97, 1024\n\t"
      "s_waitcnt lgkmcnt(0)\n\t"
      "v_mfma_f32_16x16x32_bf16 v[32:35], v[76:79], v[112:115], v[32:35]\n\t"
      "v_mfma_f32_16x16x32_bf16 v[36:39], v[80:83], v[112:115], v[36:39]\n\t"
      "v_mfma_f32_16x16x32_bf16 v[40:43], v[76:79], v[116:119], v[40:43]\n\t"
      "v_mfma_f32_16x16x32_bf16 v[44:47], v[80:83], v[116:119], v[44:47]\n\t"
      "s_mov_b64 exec, 1\n\t"
      "ds_write_b64 v109, v[110:111] offset:55392\n\t"
      "s_mov_b64 exec, -1\n\t"
      "s_nop 4\n\t"
      "v_cvt_pk_bf16_f32 v68, v32, v33\n\t"
      "v_cvt_pk_bf16_f32 v69, v34, v35\n\t"
      "v_cvt_pk_bf16_f32 v70, v36, v37\n\t"
      "v_cvt_pk_bf16_f32 v71, v38, v39\n\t"
      "v_cvt_pk_bf16_f32 v74, v40, v41\n\t"
      "v_cvt_pk_bf16_f32 v75, v42, v43\n\t"
      "v_cvt_pk_bf16_f32 v72, v44, v45\n\t"
      "v_cvt_pk_bf16_f32 v73, v46, v47\n\t"
      "v_mov_b64 v[32:33], 0\n\t"
      "v_mov_b64 v[34:35], 0\n\t"
      "v_mov_b64 v[36:37], 0\n\t"
      "v_mov_b64 v[38:39], 0\n\t"
      "v_mov_b64 v[40:41], 0\n\t"
      "v_mov_b64 v[42:43], 0\n\t"
      "v_mov_b64 v[44:45], 0\n\t"
      "v_mov_b64 v[46:47], 0\n\t"
      "s_nop 0\n\t"
      "s_nop 0\n\t"
      "s_waitcnt vmcnt(3)\n\t"
      "v_pk_fma_f32 v[12:13], v[84:85], s[56:57], v[126:127] op_sel:[0,1,0] op_sel_hi:[1,1,0]\n\t"
      "v_pk_fma_f32 v[14:15], v[86:87], s[56:57], v[126:127] op_sel:[0,1,0] op_sel_hi:[1,1,0]\n\t"
      "v_cvt_pk_bf16_f32 v8, v12, v13\n\t"
      "v_cvt_pk_bf16_f32 v9, v14, v15\n\t"
      "s_nop 0\n\t"
      "ds_write_b64 v127, v[8:9] offset:43008\n\t"
      "s_waitcnt vmcnt(2)\n\t"
      "v_pk_fma_f32 v[2:3], v[90:91], s[56:57], v[126:127] op_sel:[0,1,0] op_sel_hi:[1,1,0]\n\t"
      "v_pk_fma_f32 v[0:1], v[88:89], s[56:57], v[126:127] op_sel:[0,1,0] op_sel_hi:[1,1,0]\n\t"
      "v_cvt_pk_bf16_f32 v11, v2, v3\n\t"
      "v_cvt_pk_bf16_f32 v10, v0, v1\n\t"
      "ds_write_b64 v127, v[10:11] offset:43016\n\t"
      "v_bitop3_b32 v64, v84, v85, v86 bitop3:0x80\n\t"
      "v_bitop3_b32 v64, v64, v87, v88 bitop3:0x80\n\t"
      "v_bitop3_b32 v64, v64, v89, v90 bitop3:0x80\n\t"
      "v_bitop3_b32 v120, v120, v121, s53 bitop3:0x7e\n\t"
      "v_bitop3_b32 v64, v64, v91, s58 bitop3:0x80\n\t"
      "v_cmp_eq_u32 vcc, 0, v64\n\t"
      "s_cbranch_vccnz LR0_2%=\n\t"
      "LC0_2%=:\n\t"
      "v_smfmac_f32_16x16x64_bf16 v[44:47], v[8:11], a[208:215], v122\n\t"
      "s_waitcnt lgkmcnt(0)\n\t"
      "v_smfmac_f32_16x16x64_bf16 v[32:35], v[8:11], a[16:23], v122\n\t"
      "s_barrier\n\t"
      "v_mfma_f32_16x16x32_bf16 v[56:59], v[8:11], v[8:11], 0\n\t"
      "ds_read_b128 v[12:15], v127 offset:44032\n\t"
      "v_smfmac_f32_16x16x64_bf16 v[36:39], v[8:11], a[80:87], v122\n\t"
      "ds_read_b128 v[76:79], v127 offset:32768\n\t"
      "v_smfmac_f32_16x16x64_bf16 v[52:55], v[8:11], v[208:215], v122\n\t"
      "ds_read_b128 v[4:7], v127 offset:41984\n\t"
      "v_smfmac_f32_16x16x64_bf16 v[40:43], v[8:11], a[144:151], v122\n\t"
      "ds_read_b128 v[0:3], v127 offset:40960\n\t"
      "v_smfmac_f32_16x16x64_bf16 v[48:51], v[8:11], v[144:151], v122\n\t"
      "s_waitcnt lgkmcnt(3)\n\t"
      "v_smfmac_f32_16x16x64_bf16 v[32:35], v[12:15], a[24:31], v122\n\t"
      "s_waitcnt vmcnt(1)\n\t"
      "v_fma_f32 v30, v94, s57, v126\n\t"
      "v_bitop3_b32 v64, v92, v93, v94 bitop3:0x80\n\t"
      "v_smfmac_f32_16x16x64_bf16 v[44:47], v[12:15], a[216:223], v122\n\t"
      "ds_read_b128 v[80:83], v127 offset:35840\n\t"
      "v_fma_f32 v28, v92, s57, v126\n\t"
      "v_fma_f32 v29, v93, s57, v126\n\t"
      "v_smfmac_f32_16x16x64_bf16 v[36:39], v[12:15], a[88:95], v122\n\t"
      "v_cvt_pk_bf16_f32 v24, v28, v29\n\t"
      "v_fma_f32 v31, v95, s57, v126\n\t"
      "v_smfmac_f32_16x16x64_bf16 v[40:43], v[12:15], a[152:159], v122\n\t"
      "s_waitcnt lgkmcnt(1)\n\t"
      "s_waitcnt vmcnt(0)\n\t"
      "v_fma_f32 v18, v98, s57, v126\n\t"
      "v_cvt_pk_bf16_f32 v25, v30, v31\n\t"
      "v_fma_f32 v16, v96, s57, v126\n\t"
      "v_smfmac_f32_16x16x64_bf16 v[36:39], v[0:3], a[64:71], v122\n\t"
      "ds_write_b64 v127, v[24:25] offset:47104\n\t"
      "v_fma_f32 v19, v99, s57, v126\n\t"
      "v_bitop3_b32 v64, v64, v95, v96 bitop3:0x80\n\t"
      "v_fma_f32 v17, v97, s57, v126\n\t"
      "v_cvt_pk_bf16_f32 v26, v16, v17\n\t"
      "v_cvt_pk_bf16_f32 v27, v18, v19\n\t"
      "v_smfmac_f32_16x16x64_bf16 v[36:39], v[4:7], a[72:79], v122\n\t"
      "ds_write_b64 v127, v[26:27] offset:47112\n\t"
      "v_bitop3_b32 v64, v64, v97, v98 bitop3:0x80\n\t"
      "v_bitop3_b32 v64, v64, v99, s60 bitop3:0x80\n\t"
      "v_smfmac_f32_16x16x64_bf16 v[44:47], v[0:3], a[192:199], v122\n\t"
      "ds_read_b128 v[84:87], v127 offset:36864\n\t"
      "v_cmp_eq_u32_e64 vcc, 0, v64\n\t"
      "v_smfmac_f32_16x16x64_bf16 v[40:43], v[0:3], a[128:135], v122\n\t"
      "v_smfmac_f32_16x16x64_bf16 v[32:35], v[0:3], a[0:7], v122\n\t"
      "s_cbranch_vccnz LR1_2%=\n\t"
      "LC1_2%=:\n\t"
      "v_mfma_f32_16x16x32_bf16 v[56:59], v[24:27], v[24:27], v[56:59]\n\t"
      "s_waitcnt lgkmcnt(1)\n\t"
      "v_readfirstlane_b32 s56, v120\n\t"
      "v_smfmac_f32_16x16x64_bf16 v[32:35], v[4:7], a[8:15], v122\n\t"
      "s_barrier\n\t"
      "v_smfmac_f32_16x16x64_bf16 v[40:43], v[4:7], a[136:143], v122\n\t"
      "ds_read_b128 v[28:31], v127 offset:48128\n\t"
      "v_smfmac_f32_16x16x64_bf16 v[44:47], v[4:7], a[200:207], v122\n\t"
      "ds_read_b128 v[20:23], v127 offset:46080\n\t"
      "v_smfmac_f32_16x16x64_bf16 v[32:35], v[24:27], a[48:55], v122\n\t"
      "ds_read_b128 v[16:19], v127 offset:45056\n\t"
      "s_mov_b32 s58, 0x10000000\n\t"
      "v_smfmac_f32_16x16x64_bf16 v[36:39], v[24:27], a[112:119], v122\n\t"
      "v_max3_f32 v56, v56, v57, v58\n\t"
      "v_smfmac_f32_16x16x64_bf16 v[44:47], v[24:27], a[240:247], v122\n\t"
      "s_waitcnt lgkmcnt(2)\n\t"
      "v_max_f32 v56, v56, v59\n\t"
      "v_smfmac_f32_16x16x64_bf16 v[32:35], v[28:31], a[56:63], v122\n\t"
      "ds_read_b128 v[88:91], v127 offset:39936\n\t"
      "v_max_f32_dpp v56, v56, v56 quad_perm:[1,0,3,2] row_mask:0xf bank_mask:0xf\n\t"
      "v_smfmac_f32_16x16x64_bf16 v[36:39], v[28:31], a[120:127], v122\n\t"
      "s_waitcnt lgkmcnt(1)\n\t"
      "s_nop 0\n\t"
      "v_smfmac_f32_16x16x64_bf16 v[32:35], v[16:19], a[32:39], v122\n\t"
      "v_max_f32_dpp v56, v56, v56 quad_perm:[2,3,0,1] row_mask:0xf bank_mask:0xf\n\t"
      "v_smfmac_f32_16x16x64_bf16 v[32:35], v[20:23], a[40:47], v122\n\t"
      "s_mov_b32 exec_lo, 0x100001\n\t"
      "v_smfmac_f32_16x16x64_bf16 v[36:39], v[16:19], a[96:103], v122\n\t"
      "s_mov_b32 exec_hi, 0x10000100\n\t"
      "ds_max_i32 v109, v56 offset:55392\n\t"
      "v_smfmac_f32_16x16x64_bf16 v[36:39], v[20:23], a[104:111], v122\n\t"
      "s_mov_b64 exec, -1\n\t"
      "v_add_f32 v32, v32, v34\n\t"
      "v_smfmac_f32_16x16x64_bf16 v[40:43], v[24:27], a[176:183], v122\n\t"
      "v_add_f32 v33, v33, v35\n\t"
      "v_mul_f32 v33, s59, v33\n\t"
      "v_smfmac_f32_16x16x64_bf16 v[40:43], v[28:31], a[184:191], v122\n\t"
      "v_add_f32 v36, v36, v38\n\t"
      "v_mul_f32 v36, s59, v36\n\t"
      "v_smfmac_f32_16x16x64_bf16 v[40:43], v[16:19], a[160:167], v122\n\t"
      "ds_read_b128 v[96:99], v127 offset:33792\n\t"
      "v_mul_f32_e64 v32, s59, v32\n\t"
      "v_smfmac_f32_16x16x64_bf16 v[44:47], v[28:31], a[248:255], v122\n\t"
      "v_add_f32_e64 v37, v37, v39\n\t"
      "v_smfmac_f32_16x16x64_bf16 v[44:47], v[16:19], a[224:231], v122\n\t"
      "ds_read_b128 v[60:63], v127 offset:37888\n\t"
      "v_mul_f32_e64 v37, s59, v37\n\t"
      "v_cvt_pk_bf16_f32 v112, v32, v33\n\t"
      "v_smfmac_f32_16x16x64_bf16 v[40:43], v[20:23], a[168:175], v122\n\t"
      "s_add_u32 s43, s43, 0x8000\n\t"
      "ds_read_b128 v[92:95], v127 offset:34816\n\t"
      "v_cvt_pk_bf16_f32 v113, v36, v37\n\t"
      "v_smfmac_f32_16x16x64_bf16 v[44:47], v[20:23], a[232:239], v122\n\t"
      "ds_write_b64 v125, v[112:113]\n\t"
      "v_smfmac_f32_16x16x64_bf16 v[48:51], v[12:15], v[152:159], v122\n\t"
      "ds_read_b128 v[56:59], v127 offset:38912\n\t"
      "v_add_f32 v40, v40, v42\n\t"
      "v_add_f32 v41, v41, v43\n\t"
      "v_cvt_pk_bf16_f32 v120, v40, v41\n\t"
      "v_add_f32 v45, v45, v47\n\t"
      "v_add_f32 v44, v44, v46\n\t"
      "v_cvt_pk_bf16_f32 v121, v44, v45\n\t"
      "v_smfmac_f32_16x16x64_bf16 v[48:51], v[0:3], v[128:135], v122\n\t"
      "ds_write_b64 v125, v[120:121] offset:2080\n\t"
      "v_smfmac_f32_16x16x64_bf16 v[48:51], v[4:7], v[136:143], v122\n\t"
      "s_waitcnt lgkmcnt(2)\n\t"
      "s_barrier\n\t"
      "ds_read_b32 v108, v109 offset:55392\n\t"
      "v_smfmac_f32_16x16x64_bf16 v[48:51], v[24:27], v[176:183], v122\n\t"
      "ds_read_b64_tr_b16 v[32:33], v124 offset:49152\n\t"
      "ds_read_b64_tr_b16 v[34:35], v124 offset:49672\n\t"
      "v_smfmac_f32_16x16x64_bf16 v[48:51], v[28:31], v[184:191], v122\n\t"
      "ds_read_b64_tr_b16 v[38:39], v124 offset:50712\n\t"
      "ds_read_b64_tr_b16 v[36:37], v124 offset:50192\n\t"
      "s_waitcnt lgkmcnt(5)\n\t"
      "v_smfmac_f32_16x16x64_bf16 v[48:51], v[16:19], v[160:167], v122\n\t"
      "s_barrier\n\t"
      "ds_read_b64_tr_b16 v[46:47], v124 offset:52792\n\t"
      "v_smfmac_f32_16x16x64_bf16 v[48:51], v[20:23], v[168:175], v122\n\t"
      "ds_read_b64_tr_b16 v[42:43], v124 offset:51752\n\t"
      "ds_read_b64_tr_b16 v[44:45], v124 offset:52272\n\t"
      "ds_read_b64_tr_b16 v[40:41], v124 offset:51232\n\t"
      "s_waitcnt lgkmcnt(6)\n\t"
      "v_readfirstlane_b32 s61, v108\n\t"
      "v_mov_b32_dpp v102, v34 quad_perm:[2,3,0,1] row_mask:0xf bank_mask:0xf\n\t"
      "v_mfma_f32_16x16x32_bf16 v[116:119], v[80:83], v[32:35], 0\n\t"
      "v_mov_b32_dpp v103, v35 quad_perm:[2,3,0,1] row_mask:0xf bank_mask:0xf\n\t"
      "v_mov_b32_dpp v100, v32 quad_perm:[2,3,0,1] row_mask:0xf bank_mask:0xf\n\t"
      "v_mfma_f32_16x16x32_bf16 v[112:115], v[76:79], v[32:35], 0\n\t"
      "s_waitcnt lgkmcnt(4)\n\t"
      "s_lshr_b32 s61, s61, 23\n\t"
      "v_mov_b32_dpp v101, v33 quad_perm:[2,3,0,1] row_mask:0xf bank_mask:0xf\n\t"
      "v_mfma_f32_16x16x32_bf16 v[112:115], v[84:87], v[36:39], v[112:115]\n\t"
      "s_add_u32 s61, s61, s67\n\t"
      "s_max_u32 s61, s61, s66\n\t"
      "v_mov_b32_dpp v107, v39 quad_perm:[2,3,0,1] row_mask:0xf bank_mask:0xf\n\t"
      "s_waitcnt lgkmcnt(0)\n\t"
      "v_mfma_f32_16x16x32_bf16 v[76:79], v[40:43], v[32:35], 0\n\t"
      "s_lshr_b32 s61, s61, 1\n\t"
      "buffer_load_dwordx4 v127, s[36:39], s46 offen sc1 lds\n\t"
      "v_mfma_f32_16x16x32_bf16 v[76:79], v[44:47], v[36:39], v[76:79]\n\t"
      "s_min_u32 s61, s61, 200\n\t"
      "v_mov_b32_dpp v106, v38 quad_perm:[2,3,0,1] row_mask:0xf bank_mask:0xf\n\t"
      "v_mov_b32_dpp v105, v37 quad_perm:[2,3,0,1] row_mask:0xf bank_mask:0xf\n\t"
      "v_mfma_f32_16x16x32_bf16 v[112:115], v[92:95], v[100:103], v[112:115]\n\t"
      "s_lshl_b32 s61, s61, 23\n\t"
      "v_add_f32 v49, v49, v51\n\t"
      "v_mov_b32_dpp v104, v36 quad_perm:[2,3,0,1] row_mask:0xf bank_mask:0xf\n\t"
      "v_mfma_f32_16x16x32_bf16 v[116:119], v[88:91], v[36:39], v[116:119]\n\t"
      "s_add_u32 s65, s61, 33554432\n\t"
      "v_cndmask_b32_e64 v121, v77, v79, s[72:73]\n\t"
      "v_cndmask_b32_e64 v120, v76, v78, s[72:73]\n\t"
      "v_mfma_f32_16x16x32_bf16 v[112:115], v[56:59], v[104:107], v[112:115]\n\t"
      "buffer_load_dwordx4 v127, s[36:39], s46 offen offset:1024 sc1 lds\n\t"
      "v_mfma_f32_16x16x32_bf16 v[116:119], v[96:99], v[100:103], v[116:119]\n\t"
      "s_pack_hh_b32_b16 s75, s65, s65\n\t"
      "v_add_f32 v48, v48, v50\n\t"
      "v_add_f32_dpp v120, v120, v120 quad_perm:[2,3,0,1] row_mask:0xf bank_mask:0xf\n\t"
      "v_add_f32_dpp v121, v121, v121 quad_perm:[2,3,0,1] row_mask:0xf bank_mask:0xf\n\t"
      "v_mfma_f32_16x16x32_bf16 v[116:119], v[60:63], v[104:107], v[116:119]\n\t"
      "s_mov_b32 m0, s45\n\t"
      "v_exp_f32 v81, v121\n\t"
      "v_exp_f32_e64 v80, v120\n\t"
      "v_cvt_pk_bf16_f32 v64, v48, v49\n\t"
      "v_cvt_pk_bf16_f32 v108, v80, v81\n\t"
      "v_smfmac_f32_16x16x64_bf16 v[52:55], v[12:15], v[216:223], v122\n\t"
      "buffer_load_dwordx4 v65, s[36:39], s44 offen sc1 lds\n\t"
      "v_mfma_f32_16x16x32_bf16 v[84:87], v[108:111], v[64:67], 0\n\t"
      "s_pack_hh_b32_b16 s74, s65, s65\n\t"
      "v_mov_b64 v[88:89], s[74:75]\n\t"
      "v_cndmask_b32_e64 v112, v112, v116, s[72:73]\n\t"
      "v_cndmask_b32_e64 v115, v115, v119, s[72:73]\n\t"
      "v_cndmask_b32_e64 v113, v113, v117, s[72:73]\n\t"
      "v_exp_f32 v59, v115\n\t"
      "v_exp_f32 v56, v112\n\t"
      "v_exp_f32_e64 v57, v113\n\t"
      "v_cndmask_b32_e64 v114, v114, v118, s[72:73]\n\t"
      "v_cvt_pk_bf16_f32 v96, v56, v57\n\t"
      "v_exp_f32 v58, v114\n\t"
      "v_mov_b64 v[90:91], s[74:75]\n\t"
      "v_cvt_pk_bf16_f32 v97, v58, v59\n\t"
      "v_mov_b32_dpp v98, v96 quad_perm:[2,3,0,1] row_mask:0xf bank_mask:0xf\n\t"
      "v_mfma_f32_16x16x32_bf16 v[48:51], v[108:111], v[88:91], 0\n\t"
      "v_mov_b32_dpp v99, v97 quad_perm:[2,3,0,1] row_mask:0xf bank_mask:0xf\n\t"
      "v_max3_f32 v82, |v112|, |v113|, |v114|\n\t"
      "v_max3_f32 v83, |v115|, |v120|, |v121|\n\t"
      "v_mfma_f32_16x16x32_bf16 v[48:51], v[96:99], v[88:91], v[48:51]\n\t"
      "buffer_load_dwordx4 v65, s[36:39], s44 offen offset:1024 sc1 lds\n\t"
      "v_mfma_f32_16x16x32_bf16 v[84:87], v[96:99], v[68:71], v[84:87]\n\t"
      "s_add_u32 s57, s61, 310378496\n\t"
      "v_max_f32 v82, v82, v83\n\t"
      "s_nop 1\n\t"
      "v_rcp_f32_e64 v48, v48\n\t"
      "v_cmp_nge_f32 vcc, 0x42000000, v82\n\t"
      "v_rcp_f32 v49, v49\n\t"
      "s_nop 0\n\t"
      "v_fma_f32 v85, v85, v49, s96\n\t"
      "s_cbranch_vccnz LMX_2%=\n\t"
      "buffer_atomic_add v85, v123, s[36:39], s97 offen offset:512\n\t"
      "v_fma_f32 v84, v84, v48, s96\n\t"
      "v_smfmac_f32_16x16x64_bf16 v[52:55], v[0:3], v[192:199], v122\n\t"
      "buffer_atomic_add v84, v123, s[36:39], s97 offen\n\t"
      "v_smfmac_f32_16x16x64_bf16 v[52:55], v[4:7], v[200:207], v122\n\t"
      "v_smfmac_f32_16x16x64_bf16 v[52:55], v[24:27], v[240:247], v122\n\t"
      "v_smfmac_f32_16x16x64_bf16 v[52:55], v[28:31], v[248:255], v122\n\t"
      "v_smfmac_f32_16x16x64_bf16 v[52:55], v[16:19], v[224:231], v122\n\t"
      "v_smfmac_f32_16x16x64_bf16 v[52:55], v[20:23], v[232:239], v122\n\t"
      "v_mfma_f32_16x16x32_bf16 v[92:95], v[96:99], v[72:75], 0\n\t"
      "s_add_u32 s62, s61, 0x83400000\n\t"
      "s_nop 0\n\t"
      "v_mov_b32 v126, s62\n\t"
      "s_nop 0\n\t"
      "v_add_f32 v53, v53, v55\n\t"
      "v_add_f32 v52, v52, v54\n\t"
      "v_cvt_pk_bf16_f32 v66, v52, v53\n\t"
      "s_add_u32 s68, s68, 0x8000\n\t"
      "s_add_u32 s97, s97, 0x8000\n\t"
      "v_mfma_f32_16x16x32_bf16 v[92:95], v[108:111], v[66:69], v[92:95]\n\t"
      "s_nop 0\n\t"
      "s_nop 6\n\t"
      "v_fma_f32 v95, v95, v49, s96\n\t"
      "buffer_atomic_add v95, v123, s[36:39], s69 offen offset:512\n\t"
      "v_fma_f32 v94, v94, v48, s96\n\t"
      "buffer_atomic_add v94, v123, s[36:39], s69 offen\n\t"
      "LWIN_2%=:\n\t"
      "v_add_u32 v100, s55, v127\n\t"
      "ds_read_b128 v[60:63], v100 offset:57344\n\t"
      "s_cmp_eq_u32 s56, 0\n\t"
      "s_cbranch_scc0 LSLOW_2%=\n\t"
      "LWIN1_2%=:\n\t"
      "buffer_load_dwordx2 v[120:121], off, s[36:39], s50 sc1\n\t"
      "s_mov_b32 s64, s50\n\t"
      "s_cmp_lt_i32 s40, 2\n\t"
      "s_cbranch_scc1 LEARLY_2%=\n\t"
      "LROLE_2%=:\n\t"
      "s_cmp_eq_u32 s42, 3\n\t"
      "s_waitcnt lgkmcnt(0)\n\t"
      "s_cbranch_scc1 LWK1_2%=\n\t"
      "s_cmp_eq_u32 s42, 2\n\t"
      "s_cbranch_scc0 LW4_2%=\n\t"
      "buffer_store_dwordx4 v[40:43], v61, s[36:39], s48 offen sc1\n\t"
      "LW4_2%=:\n\t"
      "s_add_u32 s48, s48, 65536\n\t"
      "s_cmp_eq_u32 s40, 63\n\t"
      "s_cbranch_scc1 LEND%=\n\t"
      "LUTOK_2%=:\n\t"
      "v_add_u32 v104, s71, v100\n\t"
      "ds_read_b128 v[76:79], v104\n\t"
      "ds_read_b128 v[80:83], v104 offset:1024\n\t"
      "ds_read_b128 v[112:115], v100\n\t"
      "ds_read_b128 v[116:119], v100 offset:1024\n\t"
      "v_xor_b32 v101, 2048, v104\n\t"
      "v_xor_b32 v102, 4096, v104\n\t"
      "v_xor_b32 v103, 6144, v104\n\t"
      "ds_read_b128 v[0:3], v101\n\t"
      "ds_read_b128 v[4:7], v101 offset:1024\n\t"
      "ds_read_b128 v[8:11], v100 offset:8192\n\t"
      "ds_read_b128 v[12:15], v100 offset:9216\n\t"
      "ds_read_b128 v[16:19], v102\n\t"
      "ds_read_b128 v[20:23], v102 offset:1024\n\t"
      "ds_read_b128 v[24:27], v100 offset:16384\n\t"
      "ds_read_b128 v[28:31], v100 offset:17408\n\t"
      "s_add_u32 m0, s55, s71\n\t"
      "s_sub_u32 s71, 204800, s71\n\t"
      "s_add_u32 s40, s40, 1\n\t"
      "s_cmp_lt_u32 s40, 63\n\t"
      "s_cselect_b32 s61, 786432, 0\n\t"
      "s_cselect_b32 s60, s58, 0\n\t"
      "s_add_u32 s44, s44, s61\n\t"
      "s_cmp_lt_u32 s40, 62\n\t"
      "s_cselect_b32 s61, 786432, 0\n\t"
      "s_add_u32 s46, s46, s61\n\t"
      "s_add_u32 s61, s40, 3\n\t"
      "s_and_b32 s62, s61, 3\n\t"
      "s_cselect_b32 s62, 16, 2000\n\t"
      "s_cmp_lt_u32 s61, 64\n\t"
      "s_cselect_b32 s62, s62, 0\n\t"
      "s_add_u32 s50, s50, s62\n\t"
      "s_mov_b32 s63, 0x2000\n\t"
      "s_waitcnt lgkmcnt(8)\n\t"
      "v_mfma_f32_16x16x32_bf16 v[32:35], v[76:79], v[112:115], 0\n\t"
      "v_mfma_f32_16x16x32_bf16 v[36:39], v[80:83], v[112:115], 0\n\t"
      "v_mfma_f32_16x16x32_bf16 v[40:43], v[76:79], v[116:119], 0\n\t"
      "v_mfma_f32_16x16x32_bf16 v[44:47], v[80:83], v[116:119], 0\n\t"
      "ds_read_b128 v[76:79], v103\n\t"
      "ds_read_b128 v[80:83], v103 offset:1024\n\t"
      "ds_read_b128 v[112:115], v100 offset:24576\n\t"
      "ds_read_b128 v[116:119], v100 offset:25600\n\t"
      "s_waitcnt lgkmcnt(8)\n\t"
      "v_mfma_f32_16x16x32_bf16 v[32:35], v[0:3], v[8:11], v[32:35]\n\t"
      "v_mfma_f32_16x16x32_bf16 v[36:39], v[4:7], v[8:11], v[36:39]\n\t"
      "v_mfma_f32_16x16x32_bf16 v[40:43], v[0:3], v[12:15], v[40:43]\n\t"
      "v_mfma_f32_16x16x32_bf16 v[44:47], v[4:7], v[12:15], v[44:47]\n\t"
      "LBAR_2%=:\n\t"
      "s_cmp_gt_u32 s40, 1\n\t"
      "s_barrier\n\t"
      "s_cbranch_scc1 LPOLL_2%=\n\t"
      "s_branch LFIRST_2%=\n\t"
      ".p2align 6\n\t"
      "LPOLL_3%=:\n\t"
      "s_memtime s[78:79]\n\t"
      "buffer_load_dwordx4 v[84:87], v60, s[36:39], s43 offen sc1\n\t"
      "buffer_load_dwordx4 v[88:91], v60, s[36:39], s43 offen offset:1024 sc1\n\t"
      "s_waitcnt lgkmcnt(4)\n\t"
      "v_mfma_f32_16x16x32_bf16 v[32:35], v[16:19], v[24:27], v[32:35]\n\t"
      "v_mfma_f32_16x16x32_bf16 v[36:39], v[20:23], v[24:27], v[36:39]\n\t"
      "v_mfma_f32_16x16x32_bf16 v[40:43], v[16:19], v[28:31], v[40:43]\n\t"
      "v_mfma_f32_16x16x32_bf16 v[44:47], v[20:23], v[28:31], v[44:47]\n\t"
      "buffer_load_dwordx4 v[92:95], v60, s[36:39], s68 offen sc1\n\t"
      "buffer_load_dwordx4 v[96:99], v60, s[36:39], s68 offen offset:1024 sc1\n\t"
      "v_mov_b64 v[48:49], 0\n\t"
      "v_mov_b64 v[50:51], 0\n\t"
      "v_mov_b64 v[52:53], 0\n\t"
      "v_mov_b64 v[54:55], 0\n\t"
      "s_xor_b32 s69, s97, 1024\n\t"
      "s_waitcnt lgkmcnt(0)\n\t"
      "v_mfma_f32_16x16x32_bf16 v[32:35], v[76:79], v[112:115], v[32:35]\n\t"
      "v_mfma_f32_16x16x32_bf16 v[36:39], v[80:83], v[112:115], v[36:39]\n\t"
      "v_mfma_f32_16x16x32_bf16 v[40:43], v[76:79], v[116:119], v[40:43]\n\t"
      "v_mfma_f32_16x16x32_bf16 v[44:47], v[80:83], v[116:119], v[44:47]\n\t"
      "s_mov_b64 exec, 1\n\t"
      "ds_write_b64 v109, v[110:111] offset:55392\n\t"
      "s_mov_b64 exec, -1\n\t"
      "s_nop 4\n\t"
      "v_cvt_pk_bf16_f32 v68, v32, v33\n\t"
      "v_cvt_pk_bf16_f32 v69, v34, v35\n\t"
      "v_cvt_pk_bf16_f32 v70, v36, v37\n\t"
      "v_cvt_pk_bf16_f32 v71, v38, v39\n\t"
      "v_cvt_pk_bf16_f32 v74, v40, v41\n\t"
      "v_cvt_pk_bf16_f32 v75, v42, v43\n\t"
      "v_cvt_pk_bf16_f32 v72, v44, v45\n\t"
      "v_cvt_pk_bf16_f32 v73, v46, v47\n\t"
      "v_mov_b64 v[32:33], 0\n\t"
      "v_mov_b64 v[34:35], 0\n\t"
      "v_mov_b64 v[36:37], 0\n\t"
      "v_mov_b64 v[38:39], 0\n\t"
      "v_mov_b64 v[40:41], 0\n\t"
      "v_mov_b64 v[42:43], 0\n\t"
      "v_mov_b64 v[44:45], 0\n\t"
      "v_mov_b64 v[46:47], 0\n\t"
      "s_nop 0\n\t"
      "s_nop 0\n\t"
      "s_nop 0\n\t"
      "s_nop 0\n\t"
      "s_nop 0\n\t"
      "s_nop 0\n\t"
      "s_nop 0\n\t"
      "s_nop 0\n\t"
      "s_nop 0\n\t"
      "s_nop 0\n\t"
      "s_nop 0\n\t"
      "s_nop 0\n\t"
      "s_nop 0\n\t"
      "s_nop 0\n\t"
      "s_waitcnt vmcnt(3)\n\t"
      "s_memtime s[88:89]\n\t"
      "v_pk_fma_f32 v[8:9], v[84:85], s[56:57], v[126:127] op_sel:[0,1,0] op_sel_hi:[1,1,0]\n\t"
      "v_pk_fma_f32 v[10:11], v[86:87], s[56:57], v[126:127] op_sel:[0,1,0] op_sel_hi:[1,1,0]\n\t"
      "v_cvt_pk_bf16_f32 v12, v8, v9\n\t"
      "v_cvt_pk_bf16_f32 v13, v10, v11\n\t"
      "s_nop 0\n\t"
      "ds_write_b64 v127, v[12:13] offset:44032\n\t"
      "s_waitcnt vmcnt(2)\n\t"
      "v_pk_fma_f32 v[6:7], v[90:91], s[56:57], v[126:127] op_sel:[0,1,0] op_sel_hi:[1,1,0]\n\t"
      "v_pk_fma_f32 v[4:5], v[88:89], s[56:57], v[126:127] op_sel:[0,1,0] op_sel_hi:[1,1,0]\n\t"
      "v_cvt_pk_bf16_f32 v15, v6, v7\n\t"
      "v_cvt_pk_bf16_f32 v14, v4, v5\n\t"
      "ds_write_b64 v127, v[14:15] offset:44040\n\t"
      "v_bitop3_b32 v64, v84, v85, v86 bitop3:0x80\n\t"
      "v_bitop3_b32 v64, v64, v87, v88 bitop3:0x80\n\t"
      "v_bitop3_b32 v64, v64, v89, v90 bitop3:0x80\n\t"
      "v_bitop3_b32 v120, v120, v121, s53 bitop3:0x7e\n\t"
      "v_bitop3_b32 v64, v64, v91, s58 bitop3:0x80\n\t"
      "v_cmp_eq_u32 vcc, 0, v64\n\t"
      "s_cbranch_vccnz LR0_3%=\n\t"
      "LC0_3%=:\n\t"
      "v_smfmac_f32_16x16x64_bf16 v[44:47], v[12:15], a[216:223], v122\n\t"
      "s_waitcnt lgkmcnt(0)\n\t"
      "v_smfmac_f32_16x16x64_bf16 v[32:35], v[12:15], a[24:31], v122\n\t"
      "s_barrier\n\t"
      "v_mfma_f32_16x16x32_bf16 v[56:59], v[12:15], v[12:15], 0\n\t"
      "ds_read_b128 v[8:11], v127 offset:43008\n\t"
      "v_smfmac_f32_16x16x64_bf16 v[36:39], v[12:15], a[88:95], v122\n\t"
      "ds_read_b128 v[76:79], v127 offset:32768\n\t"
      "v_smfmac_f32_16x16x64_bf16 v[52:55], v[12:15], v[216:223], v122\n\t"
      "ds_read_b128 v[0:3], v127 offset:40960\n\t"
      "v_smfmac_f32_16x16x64_bf16 v[40:43], v[12:15], a[152:159], v122\n\t"
      "ds_read_b128 v[4:7], v127 offset:41984\n\t"
      "v_smfmac_f32_16x16x64_bf16 v[48:51], v[12:15], v[152:159], v122\n\t"
      "s_waitcnt lgkmcnt(3)\n\t"
      "v_smfmac_f32_16x16x64_bf16 v[32:35], v[8:11], a[16:23], v122\n\t"
      "s_waitcnt vmcnt(1)\n\t"
      "v_fma_f32 v26, v94, s57, v126\n\t"
      "v_bitop3_b32 v64, v92, v93, v94 bitop3:0x80\n\t"
      "v_smfmac_f32_16x16x64_bf16 v[44:47], v[8:11], a[208:215], v122\n\t"
      "ds_read_b128 v[80:83], v127 offset:35840\n\t"
      "v_fma_f32 v24, v92, s57, v126\n\t"
      "v_fma_f32 v25, v93, s57, v126\n\t"
      "v_smfmac_f32_16x16x64_bf16 v[36:39], v[8:11], a[80:87], v122\n\t"
      "v_cvt_pk_bf16_f32 v28, v24, v25\n\t"
      "v_fma_f32 v27, v95, s57, v126\n\t"
      "v_smfmac_f32_16x16x64_bf16 v[40:43], v[8:11], a[144:151], v122\n\t"
      "s_waitcnt lgkmcnt(1)\n\t"
      "s_waitcnt vmcnt(0)\n\t"
      "v_fma_f32 v22, v98, s57, v126\n\t"
      "v_cvt_pk_bf16_f32 v29, v26, v27\n\t"
      "v_fma_f32 v20, v96, s57, v126\n\t"
      "v_smfmac_f32_16x16x64_bf16 v[36:39], v[4:7], a[72:79], v122\n\t"
      "ds_write_b64 v127, v[28:29] offset:48128\n\t"
      "v_fma_f32 v23, v99, s57, v126\n\t"
      "v_bitop3_b32 v64, v64, v95, v96 bitop3:0x80\n\t"
      "v_fma_f32 v21, v97, s57, v126\n\t"
      "v_cvt_pk_bf16_f32 v30, v20, v21\n\t"
      "v_cvt_pk_bf16_f32 v31, v22, v23\n\t"
      "v_smfmac_f32_16x16x64_bf16 v[36:39], v[0:3], a[64:71], v122\n\t"
      "ds_write_b64 v127, v[30:31] offset:48136\n\t"
      "v_bitop3_b32 v64, v64, v97, v98 bitop3:0x80\n\t"
      "v_bitop3_b32 v64, v64, v99, s60 bitop3:0x80\n\t"
      "v_smfmac_f32_16x16x64_bf16 v[44:47], v[4:7], a[200:207], v122\n\t"
      "ds_read_b128 v[84:87], v127 offset:36864\n\t"
      "v_cmp_eq_u32_e64 vcc, 0, v64\n\t"
      "v_smfmac_f32_16x16x64_bf16 v[40:43], v[4:7], a[136:143], v122\n\t"
      "v_smfmac_f32_16x16x64_bf16 v[32:35], v[4:7], a[8:15], v122\n\t"
      "s_cbranch_vccnz LR1_3%=\n\t"
      "LC1_3%=:\n\t"
      "v_mfma_f32_16x16x32_bf16 v[56:59], v[28:31], v[28:31], v[56:59]\n\t"
      "s_waitcnt lgkmcnt(1)\n\t"
      "v_readfirstlane_b32 s56, v120\n\t"
      "v_smfmac_f32_16x16x64_bf16 v[32:35], v[0:3], a[0:7], v122\n\t"
      "s_barrier\n\t"
      "v_smfmac_f32_16x16x64_bf16 v[40:43], v[0:3], a[128:135], v122\n\t"
      "ds_read_b128 v[24:27], v127 offset:47104\n\t"
      "v_smfmac_f32_16x16x64_bf16 v[44:47], v[0:3], a[192:199], v122\n\t"
      "ds_read_b128 v[16:19], v127 offset:45056\n\t"
      "v_smfmac_f32_16x16x64_bf16 v[32:35], v[28:31], a[56:63], v122\n\t"
      "ds_read_b128 v[20:23], v127 offset:46080\n\t"
      "s_mov_b32 s58, 0x10000000\n\t"
      "v_smfmac_f32_16x16x64_bf16 v[36:39], v[28:31], a[120:127], v122\n\t"
      "v_max3_f32 v56, v56, v57, v58\n\t"
      "v_smfmac_f32_16x16x64_bf16 v[44:47], v[28:31], a[248:255], v122\n\t"
      "s_waitcnt lgkmcnt(2)\n\t"
      "v_max_f32 v56, v56, v59\n\t"
      "v_smfmac_f32_16x16x64_bf16 v[32:35], v[24:27], a[48:55], v122\n\t"
      "ds_read_b128 v[88:91], v127 offset:39936\n\t"
      "v_max_f32_dpp v56, v56, v56 quad_perm:[1,0,3,2] row_mask:0xf bank_mask:0xf\n\t"
      "v_smfmac_f32_16x16x64_bf16 v[36:39], v[24:27], a[112:119], v122\n\t"
      "s_waitcnt lgkmcnt(1)\n\t"
      "s_nop 0\n\t"
      "v_smfmac_f32_16x16x64_bf16 v[32:35], v[20:23], a[40:47], v122\n\t"
      "v_max_f32_dpp v56, v56, v56 quad_perm:[2,3,0,1] row_mask:0xf bank_mask:0xf\n\t"
      "v_smfmac_f32_16x16x64_bf16 v[32:35], v[16:19], a[32:39], v122\n\t"
      "s_mov_b32 exec_lo, 0x100001\n\t"
      "v_smfmac_f32_16x16x64_bf16 v[36:39], v[20:23], a[104:111], v122\n\t"
      "s_mov_b32 exec_hi, 0x10000100\n\t"
      "ds_max_i32 v109, v56 offset:55392\n\t"
      "v_smfmac_f32_16x16x64_bf16 v[36:39], v[16:19], a[96:103], v122\n\t"
      "s_mov_b64 exec, -1\n\t"
      "v_add_f32 v32, v32, v34\n\t"
      "v_smfmac_f32_16x16x64_bf16 v[40:43], v[28:31], a[184:191], v122\n\t"
      "v_add_f32 v33, v33, v35\n\t"
      "v_mul_f32 v33, s59, v33\n\t"
      "v_smfmac_f32_16x16x64_bf16 v[40:43], v[24:27], a[176:183], v122\n\t"
      "v_add_f32 v36, v36, v38\n\t"
      "v_mul_f32 v36, s59, v36\n\t"
      "v_smfmac_f32_16x16x64_bf16 v[40:43], v[20:23], a[168:175], v122\n\t"
      "ds_read_b128 v[96:99], v127 offset:33792\n\t"
      "v_mul_f32_e64 v32, s59, v32\n\t"
      "v_smfmac_f32_16x16x64_bf16 v[44:47], v[24:27], a[240:247], v122\n\t"
      "v_add_f32_e64 v37, v37, v39\n\t"
      "v_smfmac_f32_16x16x64_bf16 v[44:47], v[20:23], a[232:239], v122\n\t"
      "ds_read_b128 v[60:63], v127 offset:37888\n\t"
      "v_mul_f32_e64 v37, s59, v37\n\t"
      "v_cvt_pk_bf16_f32 v112, v32, v33\n\t"
      "v_smfmac_f32_16x16x64_bf16 v[40:43], v[16:19], a[160:167], v122\n\t"
      "s_add_u32 s43, s43, 0x8000\n\t"
      "ds_read_b128 v[92:95], v127 offset:34816\n\t"
      "v_cvt_pk_bf16_f32 v113, v36, v37\n\t"
      "v_smfmac_f32_16x16x64_bf16 v[44:47], v[16:19], a[224:231], v122\n\t"
      "ds_write_b64 v125, v[112:113]\n\t"
      "v_smfmac_f32_16x16x64_bf16 v[48:51], v[8:11], v[144:151], v122\n\t"
      "ds_read_b128 v[56:59], v127 offset:38912\n\t"
      "v_add_f32 v40, v40, v42\n\t"
      "v_add_f32 v41, v41, v43\n\t"
      "v_cvt_pk_bf16_f32 v120, v40, v41\n\t"
      "v_add_f32 v45, v45, v47\n\t"
      "v_add_f32 v44, v44, v46\n\t"
      "v_cvt_pk_bf16_f32 v121, v44, v45\n\t"
      "v_smfmac_f32_16x16x64_bf16 v[48:51], v[4:7], v[136:143], v122\n\t"
      "ds_write_b64 v125, v[120:121] offset:2080\n\t"
      "v_smfmac_f32_16x16x64_bf16 v[48:51], v[0:3], v[128:135], v122\n\t"
      "s_waitcnt lgkmcnt(2)\n\t"
      "s_barrier\n\t"
      "ds_read_b32 v108, v109 offset:55392\n\t"
      "v_smfmac_f32_16x16x64_bf16 v[48:51], v[28:31], v[184:191], v122\n\t"
      "ds_read_b64_tr_b16 v[32:33], v124 offset:49152\n\t"
      "ds_read_b64_tr_b16 v[34:35], v124 offset:49672\n\t"
      "v_smfmac_f32_16x16x64_bf16 v[48:51], v[24:27], v[176:183], v122\n\t"
      "ds_read_b64_tr_b16 v[38:39], v124 offset:50712\n\t"
      "ds_read_b64_tr_b16 v[36:37], v124 offset:50192\n\t"
      "s_waitcnt lgkmcnt(5)\n\t"
      "v_smfmac_f32_16x16x64_bf16 v[48:51], v[20:23], v[168:175], v122\n\t"
      "s_barrier\n\t"
      "ds_read_b64_tr_b16 v[46:47], v124 offset:52792\n\t"
      "v_smfmac_f32_16x16x64_bf16 v[48:51], v[16:19], v[160:167], v122\n\t"
      "ds_read_b64_tr_b16 v[42:43], v124 offset:51752\n\t"
      "ds_read_b64_tr_b16 v[44:45], v124 offset:52272\n\t"
      "ds_read_b64_tr_b16 v[40:41], v124 offset:51232\n\t"
      "s_waitcnt lgkmcnt(6)\n\t"
      "v_readfirstlane_b32 s61, v108\n\t"
      "v_mov_b32_dpp v102, v34 quad_perm:[2,3,0,1] row_mask:0xf bank_mask:0xf\n\t"
      "v_mfma_f32_16x16x32_bf16 v[116:119], v[80:83], v[32:35], 0\n\t"
      "v_mov_b32_dpp v103, v35 quad_perm:[2,3,0,1] row_mask:0xf bank_mask:0xf\n\t"
      "v_mov_b32_dpp v100, v32 quad_perm:[2,3,0,1] row_mask:0xf bank_mask:0xf\n\t"
      "v_mfma_f32_16x16x32_bf16 v[112:115], v[76:79], v[32:35], 0\n\t"
      "s_waitcnt lgkmcnt(4)\n\t"
      "s_lshr_b32 s61, s61, 23\n\t"
      "v_mov_b32_dpp v101, v33 quad_perm:[2,3,0,1] row_mask:0xf bank_mask:0xf\n\t"
      "v_mfma_f32_16x16x32_bf16 v[112:115], v[84:87], v[36:39], v[112:115]\n\t"
      "s_add_u32 s61, s61, s67\n\t"
      "s_max_u32 s61, s61, s66\n\t"
      "v_mov_b32_dpp v107, v39 quad_perm:[2,3,0,1] row_mask:0xf bank_mask:0xf\n\t"
      "s_waitcnt lgkmcnt(0)\n\t"
      "v_mfma_f32_16x16x32_bf16 v[76:79], v[40:43], v[32:35], 0\n\t"
      "s_lshr_b32 s61, s61, 1\n\t"
      "buffer_load_dwordx4 v127, s[36:39], s46 offen sc1 lds\n\t"
      "v_mfma_f32_16x16x32_bf16 v[76:79], v[44:47], v[36:39], v[76:79]\n\t"
      "s_min_u32 s61, s61, 200\n\t"
      "v_mov_b32_dpp v106, v38 quad_perm:[2,3,0,1] row_mask:0xf bank_mask:0xf\n\t"
      "v_mov_b32_dpp v105, v37 quad_perm:[2,3,0,1] row_mask:0xf bank_mask:0xf\n\t"
      "v_mfma_f32_16x16x32_bf16 v[112:115], v[92:95], v[100:103], v[112:115]\n\t"
      "s_lshl_b32 s61, s61, 23\n\t"
      "v_add_f32 v49, v49, v51\n\t"
      "v_mov_b32_dpp v104, v36 quad_perm:[2,3,0,1] row_mask:0xf bank_mask:0xf\n\t"
      "v_mfma_f32_16x16x32_bf16 v[116:119], v[88:91], v[36:39], v[116:119]\n\t"
      "s_add_u32 s65, s61, 33554432\n\t"
      "v_cndmask_b32_e64 v121, v77, v79, s[72:73]\n\t"
      "v_cndmask_b32_e64 v120, v76, v78, s[72:73]\n\t"
      "v_mfma_f32_16x16x32_bf16 v[112:115], v[56:59], v[104:107], v[112:115]\n\t"
      "buffer_load_dwordx4 v127, s[36:39], s46 offen offset:1024 sc1 lds\n\t"
      "v_mfma_f32_16x16x32_bf16 v[116:119], v[96:99], v[100:103], v[116:119]\n\t"
      "s_pack_hh_b32_b16 s75, s65, s65\n\t"
      "v_add_f32 v48, v48, v50\n\t"
      "v_add_f32_dpp v120, v120, v120 quad_perm:[2,3,0,1] row_mask:0xf bank_mask:0xf\n\t"
      "v_add_f32_dpp v121, v121, v121 quad_perm:[2,3,0,1] row_mask:0xf bank_mask:0xf\n\t"
      "v_mfma_f32_16x16x32_bf16 v[116:119], v[60:63], v[104:107], v[116:119]\n\t"
      "s_mov_b32 m0, s45\n\t"
      "v_exp_f32 v81, v121\n\t"
      "v_exp_f32_e64 v80, v120\n\t"
      "v_cvt_pk_bf16_f32 v64, v48, v49\n\t"
      "v_cvt_pk_bf16_f32 v108, v80, v81\n\t"
      "v_smfmac_f32_16x16x64_bf16 v[52:55], v[8:11], v[208:215], v122\n\t"
      "buffer_load_dwordx4 v65, s[36:39], s44 offen sc1 lds\n\t"
      "v_mfma_f32_16x16x32_bf16 v[84:87], v[108:111], v[64:67], 0\n\t"
      "s_pack_hh_b32_b16 s74, s65, s65\n\t"
      "v_mov_b64 v[88:89], s[74:75]\n\t"
      "v_cndmask_b32_e64 v112, v112, v116, s[72:73]\n\t"
      "v_cndmask_b32_e64 v115, v115, v119, s[72:73]\n\t"
      "v_cndmask_b32_e64 v113, v113, v117, s[72:73]\n\t"
      "v_exp_f32 v59, v115\n\t"
      "v_exp_f32 v56, v112\n\t"
      "v_exp_f32_e64 v57, v113\n\t"
      "v_cndmask_b32_e64 v114, v114, v118, s[72:73]\n\t"
      "v_cvt_pk_bf16_f32 v96, v56, v57\n\t"
      "v_exp_f32 v58, v114\n\t"
      "v_mov_b64 v[90:91], s[74:75]\n\t"
      "v_cvt_pk_bf16_f32 v97, v58, v59\n\t"
      "v_mov_b32_dpp v98, v96 quad_perm:[2,3,0,1] row_mask:0xf bank_mask:0xf\n\t"
      "v_mfma_f32_16x16x32_bf16 v[48:51], v[108:111], v[88:91], 0\n\t"
      "v_mov_b32_dpp v99, v97 quad_perm:[2,3,0,1] row_mask:0xf bank_mask:0xf\n\t"
      "v_max3_f32 v82, |v112|, |v113|, |v114|\n\t"
      "v_max3_f32 v83, |v115|, |v120|, |v121|\n\t"
      "v_mfma_f32_16x16x32_bf16 v[48:51], v[96:99], v[88:91], v[48:51]\n\t"
      "buffer_load_dwordx4 v65, s[36:39], s44 offen offset:1024 sc1 lds\n\t"
      "v_mfma_f32_16x16x32_bf16 v[84:87], v[96:99], v[68:71], v[84:87]\n\t"
      "s_add_u32 s57, s61, 310378496\n\t"
      "v_max_f32 v82, v82, v83\n\t"
      "s_nop 1\n\t"
      "v_rcp_f32_e64 v48, v48\n\t"
      "v_cmp_nge_f32 vcc, 0x42000000, v82\n\t"
      "v_rcp_f32 v49, v49\n\t"
      "s_nop 0\n\t"
      "v_fma_f32 v85, v85, v49, s96\n\t"
      "s_cbranch_vccnz LMX_3%=\n\t"
      "buffer_atomic_add v85, v123, s[36:39], s97 offen offset:512\n\t"
      "v_fma_f32 v84, v84, v48, s96\n\t"
      "v_smfmac_f32_16x16x64_bf16 v[52:55], v[4:7], v[200:207], v122\n\t"
      "buffer_atomic_add v84, v123, s[36:39], s97 offen\n\t"
      "v_smfmac_f32_16x16x64_bf16 v[52:55], v[0:3], v[192:199], v122\n\t"
      "v_smfmac_f32_16x16x64_bf16 v[52:55], v[28:31], v[248:255], v122\n\t"
      "v_smfmac_f32_16x16x64_bf16 v[52:55], v[24:27], v[240:247], v122\n\t"
      "v_smfmac_f32_16x16x64_bf16 v[52:55], v[20:23], v[232:239], v122\n\t"
      "v_smfmac_f32_16x16x64_bf16 v[52:55], v[16:19], v[224:231], v122\n\t"
      "v_mfma_f32_16x16x32_bf16 v[92:95], v[96:99], v[72:75], 0\n\t"
      "s_add_u32 s62, s61, 0x83400000\n\t"
      "s_nop 0\n\t"
      "v_mov_b32 v126, s62\n\t"
      "s_nop 0\n\t"
      "v_add_f32 v53, v53, v55\n\t"
      "v_add_f32 v52, v52, v54\n\t"
      "v_cvt_pk_bf16_f32 v66, v52, v53\n\t"
      "s_add_u32 s68, s68, 0x8000\n\t"
      "s_add_u32 s97, s97, 0x8000\n\t"
      "v_mfma_f32_16x16x32_bf16 v[92:95], v[108:111], v[66:69], v[92:95]\n\t"
      "s_nop 0\n\t"
      "s_nop 6\n\t"
      "v_fma_f32 v95, v95, v49, s96\n\t"
      "buffer_atomic_add v95, v123, s[36:39], s69 offen offset:512\n\t"
      "v_fma_f32 v94, v94, v48, s96\n\t"
      "buffer_atomic_add v94, v123, s[36:39], s69 offen\n\t"
      "LWIN_3%=:\n\t"
      "v_add_u32 v100, s55, v127\n\t"
      "ds_read_b128 v[60:63], v100 offset:57344\n\t"
      "s_cmp_eq_u32 s56, 0\n\t"
      "s_cbranch_scc0 LSLOW_3%=\n\t"
      "LWIN1_3%=:\n\t"
      "buffer_load_dwordx2 v[120:121], off, s[36:39], s50 sc1\n\t"
      "s_mov_b32 s64, s50\n\t"
      "s_cmp_lt_i32 s40, 2\n\t"
      "s_cbranch_scc1 LEARLY_3%=\n\t"
      "LROLE_3%=:\n\t"
      "s_memrealtime s[92:93]\n\t"
      "s_waitcnt lgkmcnt(0)\n\t"
      "s_sub_u32 s61, s92, s95\n\t"
      "s_mov_b32 s95, s92\n\t"
      "s_min_u32 s61, s61, 190\n\t"
      "s_add_u32 s61, s61, s92\n\t"
      "s_pack_ll_b32_b16 s61, s61, s40\n\t"
      "v_mov_b32 v56, s61\n\t"
      "s_mov_b64 exec, 1\n\t"
      "buffer_store_dword v56, off, s[36:39], s51 sc1\n\t"
      "s_mov_b64 exec, -1\n\t"
      "s_cmp_eq_u32 s40, 63\n\t"
      "s_cbranch_scc1 LEND%=\n\t"
      "LUTOK_3%=:\n\t"
      "v_add_u32 v104, s71, v100\n\t"
      "ds_read_b128 v[76:79], v104\n\t"
      "ds_read_b128 v[80:83], v104 offset:1024\n\t"
      "ds_read_b128 v[112:115], v100\n\t"
      "ds_read_b128 v[116:119], v100 offset:1024\n\t"
      "v_xor_b32 v101, 2048, v104\n\t"
      "v_xor_b32 v102, 4096, v104\n\t"
      "v_xor_b32 v103, 6144, v104\n\t"
      "ds_read_b128 v[0:3], v101\n\t"
      "ds_read_b128 v[4:7], v101 offset:1024\n\t"
      "ds_read_b128 v[8:11], v100 offset:8192\n\t"
      "ds_read_b128 v[12:15], v100 offset:9216\n\t"
      "ds_read_b128 v[16:19], v102\n\t"
      "ds_read_b128 v[20:23], v102 offset:1024\n\t"
      "ds_read_b128 v[24:27], v100 offset:16384\n\t"
      "ds_read_b128 v[28:31], v100 offset:17408\n\t"
      "s_sub_u32 s61, s88, s78\n\t"
      "s_sub_i32 s61, s61, 564\n\t"
      "s_min_i32 s25, s25, 0\n\t"
      "s_sub_i32 s25, s25, 0\n\t"
      "s_max_i32 s25, s25, -240\n\t"
      "s_cmp_lt_i32 s61, -25\n\t"
      "s_cselect_b32 s25, s25, 0\n\t"
      "s_cmp_gt_i32 s61, 150\n\t"
      "s_cselect_b32 s25, 0, s25\n\t"
      "s_min_i32 s61, s61, 64\n\t"
      "s_max_i32 s61, s61, -96\n\t"
      "s_ashr_i32 s61, s61, 2\n\t"
      "s_cmp_eq_u32 s63, 0x2000\n\t"
      "s_cselect_b32 s61, s61, 24\n\t"
      "s_cselect_b32 s25, s25, 0\n\t"
      "s_add_u32 s26, s26, s61\n\t"
      "s_max_u32 s26, s26, 2100\n\t"
      "s_min_u32 s26, s26, 2900\n\t"
      "s_add_u32 s24, s26, s25\n\t"
      "s_add_u32 m0, s55, s71\n\t"
      "s_sub_u32 s71, 204800, s71\n\t"
      "s_add_u32 s40, s40, 1\n\t"
      "s_cmp_lt_u32 s40, 63\n\t"
      "s_cselect_b32 s61, 786432, 0\n\t"
      "s_cselect_b32 s60, s58, 0\n\t"
      "s_add_u32 s44, s44, s61\n\t"
      "s_cmp_lt_u32 s40, 62\n\t"
      "s_cselect_b32 s61, 786432, 0\n\t"
      "s_add_u32 s46, s46, s61\n\t"
      "s_add_u32 s61, s40, 3\n\t"
      "s_and_b32 s62, s61, 3\n\t"
      "s_cselect_b32 s62, 16, 2000\n\t"
      "s_cmp_lt_u32 s61, 64\n\t"
      "s_cselect_b32 s62, s62, 0\n\t"
      "s_add_u32 s50, s50, s62\n\t"
      "s_mov_b32 s63, 0x2000\n\t"
      "s_waitcnt lgkmcnt(8)\n\t"
      "v_mfma_f32_16x16x32_bf16 v[32:35], v[76:79], v[112:115], 0\n\t"
      "v_mfma_f32_16x16x32_bf16 v[36:39], v[80:83], v[112:115], 0\n\t"
      "v_mfma_f32_16x16x32_bf16 v[40:43], v[76:79], v[116:119], 0\n\t"
      "v_mfma_f32_16x16x32_bf16 v[44:47], v[80:83], v[116:119], 0\n\t"
      "ds_read_b128 v[76:79], v103\n\t"
      "ds_read_b128 v[80:83], v103 offset:1024\n\t"
      "ds_read_b128 v[112:115], v100 offset:24576\n\t"
      "ds_read_b128 v[116:119], v100 offset:25600\n\t"
      "s_waitcnt lgkmcnt(8)\n\t"
      "v_mfma_f32_16x16x32_bf16 v[32:35], v[0:3], v[8:11], v[32:35]\n\t"
      "v_mfma_f32_16x16x32_bf16 v[36:39], v[4:7], v[8:11], v[36:39]\n\t"
      "v_mfma_f32_16x16x32_bf16 v[40:43], v[0:3], v[12:15], v[40:43]\n\t"
      "v_mfma_f32_16x16x32_bf16 v[44:47], v[4:7], v[12:15], v[44:47]\n\t"
      "LDLY_3%=:\n\t"
      "s_memtime s[92:93]\n\t"
      "s_waitcnt lgkmcnt(0)\n\t"
      "s_sub_u32 s61, s92, s88\n\t"
      "s_cmp_lt_u32 s61, s24\n\t"
      "s_cbranch_scc1 LDLY_3%=\n\t"
      "LBAR_3%=:\n\t"
      "s_cmp_gt_u32 s40, 1\n\t"
      "s_barrier\n\t"
      "s_cbranch_scc1 LPOLL_3%=\n\t"
      "s_branch LFIRST_3%=\n\t"
      "LUTOK_0%=:\n\t"
      "v_add_u32 v104, s71, v100\n\t"
      "ds_read_b128 v[76:79], v104\n\t"
      "ds_read_b128 v[80:83], v104 offset:1024\n\t"
      "ds_read_b128 v[112:115], v100\n\t"
      "ds_read_b128 v[116:119], v100 offset:1024\n\t"
      "v_xor_b32 v101, 2048, v104\n\t"
      "v_xor_b32 v102, 4096, v104\n\t"
      "v_xor_b32 v103, 6144, v104\n\t"
      "ds_read_b128 v[0:3], v101\n\t"
      "ds_read_b128 v[4:7], v101 offset:1024\n\t"
      "ds_read_b128 v[8:11], v100 offset:8192\n\t"
      "ds_read_b128 v[12:15], v100 offset:9216\n\t"
      "ds_read_b128 v[16:19], v102\n\t"
      "ds_read_b128 v[20:23], v102 offset:1024\n\t"
      "ds_read_b128 v[24:27], v100 offset:16384\n\t"
      "ds_read_b128 v[28:31], v100 offset:17408\n\t"
      "s_add_u32 m0, s55, s71\n\t"
      "s_sub_u32 s71, 204800, s71\n\t"
      "s_add_u32 s40, s40, 1\n\t"
      "s_cmp_lt_u32 s40, 63\n\t"
      "s_cselect_b32 s61, 786432, 0\n\t"
      "s_cselect_b32 s60, s58, 0\n\t"
      "s_add_u32 s44, s44, s61\n\t"
      "s_cmp_lt_u32 s40, 62\n\t"
      "s_cselect_b32 s61, 786432, 0\n\t"
      "s_add_u32 s46, s46, s61\n\t"
      "s_add_u32 s61, s40, 3\n\t"
      "s_and_b32 s62, s61, 3\n\t"
      "s_cselect_b32 s62, 16, 2000\n\t"
      "s_cmp_lt_u32 s61, 64\n\t"
      "s_cselect_b32 s62, s62, 0\n\t"
      "s_add_u32 s50, s50, s62\n\t"
      "s_mov_b32 s63, 0x2000\n\t"
      "s_waitcnt lgkmcnt(8)\n\t"
      "v_mfma_f32_16x16x32_bf16 v[32:35], v[76:79], v[112:115], 0\n\t"
      "v_mfma_f32_16x16x32_bf16 v[36:39], v[80:83], v[112:115], 0\n\t"
      "v_mfma_f32_16x16x32_bf16 v[40:43], v[76:79], v[116:119], 0\n\t"
      "v_mfma_f32_16x16x32_bf16 v[44:47], v[80:83], v[116:119], 0\n\t"
      "ds_read_b128 v[76:79], v103\n\t"
      "ds_read_b128 v[80:83], v103 offset:1024\n\t"
      "ds_read_b128 v[112:115], v100 offset:24576\n\t"
      "ds_read_b128 v[116:119], v100 offset:25600\n\t"
      "s_waitcnt lgkmcnt(8)\n\t"
      "v_mfma_f32_16x16x32_bf16 v[32:35], v[0:3], v[8:11], v[32:35]\n\t"
      "v_mfma_f32_16x16x32_bf16 v[36:39], v[4:7], v[8:11], v[36:39]\n\t"
      "v_mfma_f32_16x16x32_bf16 v[40:43], v[0:3], v[12:15], v[40:43]\n\t"
      "v_mfma_f32_16x16x32_bf16 v[44:47], v[4:7], v[12:15], v[44:47]\n\t"
      "s_branch LBAR_0%=\n\t"
      "LFIRST_0%=:\n\t"
      "s_cmp_eq_u32 s40, 0\n\t"
      "s_cbranch_scc1 LWM0_0%=\n\t"
      "LDEC_0%=:\n\t"
      "s_waitcnt vmcnt(0)\n\t"
      "v_readfirstlane_b32 s61, v106\n\t"
      "s_mov_b32 s62, 72960\n\t"
      "s_sub_u32 s63, s63, 1\n\t"
      "buffer_load_dword v106, off, s[36:39], s62 sc1\n\t"
      "s_cmp_eq_u32 s63, 0\n\t"
      "s_cbranch_scc1 LDEC1_0%=\n\t"
      "s_cmp_lt_u32 s61, 67108864\n\t"
      "s_cbranch_scc1 LDEC_0%=\n\t"
      "LDEC1_0%=:\n\t"
      "s_cmp_gt_u32 s61, 67141632\n\t"
      "s_cselect_b32 s99, 0x2000, 0\n\t"
      "s_xor_b32 s97, s97, s99\n\t"
      "s_barrier\n\t"
      "s_mov_b32 s63, 0x2000\n\t"
      "s_branch LPOLL_0%=\n\t"
      "LWM0_0%=:\n\t"
      "s_mov_b32 s63, 0x10000\n\t"
      "LWM_0%=:\n\t"
      "s_waitcnt vmcnt(0) lgkmcnt(0)\n\t"
      "v_bitop3_b32 v93, v93, v95, s53 bitop3:0x7e\n\t"
      "v_max_u32 v92, v92, v94\n\t"
      "v_cmp_ne_u32 vcc, 0, v93\n\t"
      "s_nop 1\n\t"
      "v_max_u32_dpp v92, v92, v92 quad_perm:[1,0,3,2] row_mask:0xf bank_mask:0xf\n\t"
      "s_nop 1\n\t"
      "v_max_u32_dpp v92, v92, v92 quad_perm:[2,3,0,1] row_mask:0xf bank_mask:0xf\n\t"
      "s_nop 1\n\t"
      "v_max_u32_dpp v92, v92, v92 row_ror:4 row_mask:0xf bank_mask:0xf\n\t"
      "s_nop 1\n\t"
      "v_max_u32_dpp v92, v92, v92 row_ror:8 row_mask:0xf bank_mask:0xf\n\t"
      "v_mov_b32 v93, v92\n\t"
      "s_nop 1\n\t"
      "v_permlane16_swap_b32 v92, v93\n\t"
      "v_max_u32 v92, v92, v93\n\t"
      "v_mov_b32 v93, v92\n\t"
      "s_nop 1\n\t"
      "v_permlane32_swap_b32 v92, v93\n\t"
      "v_max_u32 v92, v92, v93\n\t"
      "v_mul_f32 v93, 0x40a00000, v92\n\t"
      "s_nop 0\n\t"
      "v_readfirstlane_b32 s67, v93\n\t"
      "v_mul_f32 v93, 0x43030000, v93\n\t"
      "s_nop 0\n\t"
      "v_readfirstlane_b32 s66, v93\n\t"
      "s_cbranch_vccz LWM1_0%=\n\t"
      "s_sleep 4\n\t"
      "buffer_load_dwordx2 v[92:93], v91, s[36:39], s94 offen sc1\n\t"
      "buffer_load_dwordx2 v[94:95], v91, s[36:39], s94 offen offset:128 sc1\n\t"
      "s_sub_u32 s63, s63, 1\n\t"
      "s_cmp_lg_u32 s63, 0\n\t"
      "s_cbranch_scc1 LWM_0%=\n\t"
      "s_waitcnt vmcnt(0)\n\t"
      "LWM1_0%=:\n\t"
      "s_lshr_b32 s67, s67, 23\n\t"
      "s_add_u32 s67, s67, 5\n\t"
      "s_lshr_b32 s66, s66, 23\n\t"
      "s_add_u32 s66, s66, 129\n\t"
      "s_max_u32 s66, s66, 80\n\t"
      "buffer_load_dwordx2 v[120:121], off, s[36:39], s64 sc1\n\t"
      "s_mov_b32 s63, 0x2000\n\t"
      "s_branch LPOLL_0%=\n\t"
      "LR0_0%=:\n\t"
      "buffer_load_dwordx4 v[84:87], v60, s[36:39], s43 offen sc1\n\t"
      "buffer_load_dwordx4 v[88:91], v60, s[36:39], s43 offen offset:1024 sc1\n\t"
      "s_waitcnt vmcnt(2)\n\t"
      "buffer_load_dwordx4 v[92:95], v60, s[36:39], s68 offen sc1\n\t"
      "buffer_load_dwordx4 v[96:99], v60, s[36:39], s68 offen offset:1024 sc1\n\t"
      "s_sub_u32 s63, s63, 1\n\t"
      "s_waitcnt vmcnt(2) lgkmcnt(0)\n\t"
      "v_bitop3_b32 v64, v84, v85, v86 bitop3:0x80\n\t"
      "v_bitop3_b32 v64, v64, v87, v88 bitop3:0x80\n\t"
      "v_bitop3_b32 v64, v64, v89, v90 bitop3:0x80\n\t"
      "v_bitop3_b32 v64, v64, v91, s58 bitop3:0x80\n\t"
      "v_cmp_eq_u32 vcc, 0, v64\n\t"
      "v_pk_fma_f32 v[4:5], v[84:85], s[56:57], v[126:127] op_sel:[0,1,0] op_sel_hi:[1,1,0]\n\t"
      "v_pk_fma_f32 v[6:7], v[86:87], s[56:57], v[126:127] op_sel:[0,1,0] op_sel_hi:[1,1,0]\n\t"
      "v_cvt_pk_bf16_f32 v0, v4, v5\n\t"
      "v_cvt_pk_bf16_f32 v1, v6, v7\n\t"
      "ds_write_b64 v127, v[0:1] offset:40960\n\t"
      "v_pk_fma_f32 v[8:9], v[88:89], s[56:57], v[126:127] op_sel:[0,1,0] op_sel_hi:[1,1,0]\n\t"
      "v_pk_fma_f32 v[10:11], v[90:91], s[56:57], v[126:127] op_sel:[0,1,0] op_sel_hi:[1,1,0]\n\t"
      "v_cvt_pk_bf16_f32 v2, v8, v9\n\t"
      "v_cvt_pk_bf16_f32 v3, v10, v11\n\t"
      "ds_write_b64 v127, v[2:3] offset:40968\n\t"
      "s_cmp_le_i32 s63, 0\n\t"
      "s_waitcnt lgkmcnt(0)\n\t"
      "s_cbranch_scc1 LRZ_0%=\n\t"
      "s_cbranch_vccnz LR0_0%=\n\t"
      "LRZ_0%=:\n\t"
      "s_branch LC0_0%=\n\t"
      "LR1_0%=:\n\t"
      "v_bitop3_b32 v64, v92, v93, v94 bitop3:0x80\n\t"
      "v_bitop3_b32 v64, v64, v95, v96 bitop3:0x80\n\t"
      "v_bitop3_b32 v64, v64, v97, v98 bitop3:0x80\n\t"
      "v_bitop3_b32 v64, v64, v99, s58 bitop3:0x80\n\t"
      "v_cmp_eq_u32 vcc, 0, v64\n\t"
      "s_nop 0\n\t"
      "s_cbranch_vccz LRX_0%=\n\t"
      "LRY_0%=:\n\t"
      "buffer_load_dwordx4 v[92:95], v60, s[36:39], s68 offen sc1\n\t"
      "buffer_load_dwordx4 v[96:99], v60, s[36:39], s68 offen offset:1024 sc1\n\t"
      "s_sub_u32 s63, s63, 1\n\t"
      "s_waitcnt vmcnt(0) lgkmcnt(0)\n\t"
      "v_bitop3_b32 v64, v92, v93, v94 bitop3:0x80\n\t"
      "v_bitop3_b32 v64, v64, v95, v96 bitop3:0x80\n\t"
      "v_bitop3_b32 v64, v64, v97, v98 bitop3:0x80\n\t"
      "v_bitop3_b32 v64, v64, v99, s58 bitop3:0x80\n\t"
      "v_cmp_eq_u32 vcc, 0, v64\n\t"
      "v_pk_fma_f32 v[20:21], v[92:93], s[56:57], v[126:127] op_sel:[0,1,0] op_sel_hi:[1,1,0]\n\t"
      "v_pk_fma_f32 v[22:23], v[94:95], s[56:57], v[126:127] op_sel:[0,1,0] op_sel_hi:[1,1,0]\n\t"
      "v_cvt_pk_bf16_f32 v16, v20, v21\n\t"
      "v_cvt_pk_bf16_f32 v17, v22, v23\n\t"
      "ds_write_b64 v127, v[16:17] offset:45056\n\t"
      "v_pk_fma_f32 v[24:25], v[96:97], s[56:57], v[126:127] op_sel:[0,1,0] op_sel_hi:[1,1,0]\n\t"
      "v_pk_fma_f32 v[26:27], v[98:99], s[56:57], v[126:127] op_sel:[0,1,0] op_sel_hi:[1,1,0]\n\t"
      "v_cvt_pk_bf16_f32 v18, v24, v25\n\t"
      "v_cvt_pk_bf16_f32 v19, v26, v27\n\t"
      "ds_write_b64 v127, v[18:19] offset:45064\n\t"
      "s_cmp_le_i32 s63, 0\n\t"
      "s_waitcnt lgkmcnt(0)\n\t"
      "s_cbranch_scc1 LRX_0%=\n\t"
      "s_cbranch_vccnz LRY_0%=\n\t"
      "LRX_0%=:\n\t"
      "s_cmp_eq_u32 s40, 63\n\t"
      "s_cbranch_scc1 LLAST_0%=\n\t"
      "s_branch LC1_0%=\n\t"
      "LMX_0%=:\n\t"
      "s_waitcnt lgkmcnt(0)\n\t"
      "s_nop 7\n\t"
      "v_smfmac_f32_16x16x64_bf16 v[52:55], v[8:11], v[208:215], v122\n\t"
      "v_smfmac_f32_16x16x64_bf16 v[52:55], v[12:15], v[216:223], v122\n\t"
      "v_smfmac_f32_16x16x64_bf16 v[52:55], v[16:19], v[224:231], v122\n\t"
      "v_smfmac_f32_16x16x64_bf16 v[52:55], v[20:23], v[232:239], v122\n\t"
      "v_smfmac_f32_16x16x64_bf16 v[52:55], v[24:27], v[240:247], v122\n\t"
      "v_smfmac_f32_16x16x64_bf16 v[52:55], v[28:31], v[248:255], v122\n\t"
      "s_add_u32 s62, s61, 0x83400000\n\t"
      "v_mov_b32 v126, s62\n\t"
      "s_nop 5\n\t"
      "v_add_f32 v53, v53, v55\n\t"
      "v_add_f32 v52, v52, v54\n\t"
      "v_cvt_pk_bf16_f32 v66, v52, v53\n\t"
      "s_add_u32 s68, s68, 0x8000\n\t"
      "v_max3_f32 v82, v112, v113, v114\n\t"
      "v_max_f32 v82, v82, v115\n\t"
      "s_nop 1\n\t"
      "v_max_f32_dpp v82, v82, v82 quad_perm:[2,3,0,1] row_mask:0xf bank_mask:0xf\n\t"
      "v_max3_f32 v83, v82, v120, v121\n\t"
      "v_max3_f32 v60, v82, v120, v121\n\t"
      "s_nop 1\n\t"
      "v_permlane16_swap_b32 v83, v60\n\t"
      "v_max_f32 v61, v83, v60\n\t"
      "v_max_f32 v60, v83, v60\n\t"
      "s_nop 1\n\t"
      "v_permlane32_swap_b32 v61, v60\n\t"
      "v_max_f32 v83, v61, v60\n\t"
      "v_sub_f32 v112, v112, v83\n\t"
      "v_sub_f32 v113, v113, v83\n\t"
      "v_sub_f32 v114, v114, v83\n\t"
      "v_sub_f32 v115, v115, v83\n\t"
      "v_sub_f32 v120, v120, v83\n\t"
      "v_sub_f32 v121, v121, v83\n\t"
      "v_exp_f32 v56, v112\n\t"
      "v_exp_f32_e64 v57, v113\n\t"
      "v_exp_f32 v58, v114\n\t"
      "v_exp_f32 v59, v115\n\t"
      "v_exp_f32_e64 v80, v120\n\t"
      "v_exp_f32 v81, v121\n\t"
      "v_cvt_pk_bf16_f32 v96, v56, v57\n\t"
      "v_cvt_pk_bf16_f32 v97, v58, v59\n\t"
      "v_cvt_pk_bf16_f32 v108, v80, v81\n\t"
      "v_mov_b32_dpp v98, v96 quad_perm:[2,3,0,1] row_mask:0xf bank_mask:0xf\n\t"
      "v_mov_b32_dpp v99, v97 quad_perm:[2,3,0,1] row_mask:0xf bank_mask:0xf\n\t"
      "v_mfma_f32_16x16x32_bf16 v[48:51], v[108:111], v[88:91], 0\n\t"
      "s_nop 0\n\t"
      "v_mfma_f32_16x16x32_bf16 v[48:51], v[96:99], v[88:91], v[48:51]\n\t"
      "v_mfma_f32_16x16x32_bf16 v[84:87], v[108:111], v[64:67], 0\n\t"
      "v_mfma_f32_16x16x32_bf16 v[84:87], v[96:99], v[68:71], v[84:87]\n\t"
      "v_mfma_f32_16x16x32_bf16 v[92:95], v[96:99], v[72:75], 0\n\t"
      "v_mfma_f32_16x16x32_bf16 v[92:95], v[108:111], v[66:69], v[92:95]\n\t"
      "s_nop 3\n\t"
      "v_rcp_f32_e64 v48, v48\n\t"
      "v_rcp_f32 v49, v49\n\t"
      "v_fma_f32 v84, v84, v48, s96\n\t"
      "buffer_atomic_add v84, v123, s[36:39], s97 offen\n\t"
      "v_fma_f32 v85, v85, v49, s96\n\t"
      "buffer_atomic_add v85, v123, s[36:39], s97 offen offset:512\n\t"
      "v_fma_f32 v94, v94, v48, s96\n\t"
      "buffer_atomic_add v94, v123, s[36:39], s69 offen\n\t"
      "v_fma_f32 v95, v95, v49, s96\n\t"
      "buffer_atomic_add v95, v123, s[36:39], s69 offen offset:512\n\t"
      "s_add_u32 s97, s97, 0x8000\n\t"
      "s_branch LWIN_0%=\n\t"
      "LEARLY_0%=:\n\t"
      "s_cmp_lt_i32 s40, 0\n\t"
      "s_waitcnt lgkmcnt(0)\n\t"
      "s_cbranch_scc1 LUTOK_0%=\n\t"
      "s_xor_b32 s43, s43, s99\n\t"
      "s_xor_b32 s68, s68, s99\n\t"
      "s_mov_b32 s61, 72960\n\t"
      "buffer_load_dword v106, off, s[36:39], s61 sc1\n\t"
      "s_branch LROLE_0%=\n\t"
      "LSLOW_0%=:\n\t"
      "s_mov_b32 s63, 0x10000\n\t"
      "LSLOW1_0%=:\n\t"
      "buffer_load_dwordx2 v[92:93], off, s[36:39], s64 sc1\n\t"
      "s_waitcnt vmcnt(0)\n\t"
      "v_bitop3_b32 v92, v92, v93, s53 bitop3:0x7e\n\t"
      "s_nop 0\n\t"
      "v_readfirstlane_b32 s56, v92\n\t"
      "s_nop 1\n\t"
      "s_cmp_eq_u32 s56, 0\n\t"
      "s_cbranch_scc1 LSLOW2_0%=\n\t"
      "s_sleep 8\n\t"
      "s_sub_u32 s63, s63, 1\n\t"
      "s_cmp_lg_u32 s63, 0\n\t"
      "s_cbranch_scc1 LSLOW1_0%=\n\t"
      "LSLOW2_0%=:\n\t"
      "s_mov_b32 m0, s45\n\t"
      "s_sub_u32 s61, 204800, s71\n\t"
      "buffer_load_dwordx4 v65, s[36:39], s44 offen sc1 lds\n\t"
      "buffer_load_dwordx4 v65, s[36:39], s44 offen offset:1024 sc1 lds\n\t"
      "s_add_u32 m0, s55, s61\n\t"
      "s_nop 0\n\t"
      "buffer_load_dwordx4 v127, s[36:39], s46 offen sc1 lds\n\t"
      "buffer_load_dwordx4 v127, s[36:39], s46 offen offset:1024 sc1 lds\n\t"
      "s_branch LWIN1_0%=\n\t"
      "LLAST_0%=:\n\t"
      "s_waitcnt lgkmcnt(0)\n\t"
      "s_barrier\n\t"
      "ds_read_b128 v[20:23], v127 offset:46080\n\t"
      "ds_read_b128 v[24:27], v127 offset:47104\n\t"
      "ds_read_b128 v[28:31], v127 offset:48128\n\t"
      "v_smfmac_f32_16x16x64_bf16 v[40:43], v[12:15], a[152:159], v122\n\t"
      "v_smfmac_f32_16x16x64_bf16 v[44:47], v[12:15], a[216:223], v122\n\t"
      "v_smfmac_f32_16x16x64_bf16 v[44:47], v[16:19], a[224:231], v122\n\t"
      "v_smfmac_f32_16x16x64_bf16 v[40:43], v[16:19], a[160:167], v122\n\t"
      "s_waitcnt lgkmcnt(2)\n\t"
      "v_smfmac_f32_16x16x64_bf16 v[40:43], v[20:23], a[168:175], v122\n\t"
      "v_smfmac_f32_16x16x64_bf16 v[44:47], v[20:23], a[232:239], v122\n\t"
      "s_waitcnt lgkmcnt(1)\n\t"
      "v_smfmac_f32_16x16x64_bf16 v[40:43], v[24:27], a[176:183], v122\n\t"
      "v_smfmac_f32_16x16x64_bf16 v[44:47], v[24:27], a[240:247], v122\n\t"
      "s_waitcnt lgkmcnt(0)\n\t"
      "v_smfmac_f32_16x16x64_bf16 v[40:43], v[28:31], a[184:191], v122\n\t"
      "v_smfmac_f32_16x16x64_bf16 v[44:47], v[28:31], a[248:255], v122\n\t"
      "s_nop 3\n\t"
      "v_add_f32 v40, v40, v42\n\t"
      "v_add_f32 v41, v41, v43\n\t"
      "v_cvt_pk_bf16_f32 v120, v40, v41\n\t"
      "s_nop 0\n\t"
      "v_add_f32 v44, v44, v46\n\t"
      "v_add_f32 v45, v45, v47\n\t"
      "v_cvt_pk_bf16_f32 v121, v44, v45\n\t"
      "ds_write_b64 v125, v[120:121] offset:2080\n\t"
      "s_waitcnt lgkmcnt(0)\n\t"
      "s_barrier\n\t"
      "ds_read_b64_tr_b16 v[40:41], v124 offset:51232\n\t"
      "ds_read_b64_tr_b16 v[42:43], v124 offset:51752\n\t"
      "ds_read_b64_tr_b16 v[44:45], v124 offset:52272\n\t"
      "ds_read_b64_tr_b16 v[46:47], v124 offset:52792\n\t"
      "v_add_u32 v100, s55, v127\n\t"
      "ds_read_b128 v[60:63], v100 offset:57344\n\t"
      "s_waitcnt lgkmcnt(0)\n\t"
      "s_branch LROLE_0%=\n\t"
      "LUTOK_1%=:\n\t"
      "v_add_u32 v104, s71, v100\n\t"
      "ds_read_b128 v[76:79], v104\n\t"
      "ds_read_b128 v[80:83], v104 offset:1024\n\t"
      "ds_read_b128 v[112:115], v100\n\t"
      "ds_read_b128 v[116:119], v100 offset:1024\n\t"
      "v_xor_b32 v101, 2048, v104\n\t"
      "v_xor_b32 v102, 4096, v104\n\t"
      "v_xor_b32 v103, 6144, v104\n\t"
      "ds_read_b128 v[0:3], v101\n\t"
      "ds_read_b128 v[4:7], v101 offset:1024\n\t"
      "ds_read_b128 v[8:11], v100 offset:8192\n\t"
      "ds_read_b128 v[12:15], v100 offset:9216\n\t"
      "ds_read_b128 v[16:19], v102\n\t"
      "ds_read_b128 v[20:23], v102 offset:1024\n\t"
      "ds_read_b128 v[24:27], v100 offset:16384\n\t"
      "ds_read_b128 v[28:31], v100 offset:17408\n\t"
      "s_add_u32 m0, s55, s71\n\t"
      "s_sub_u32 s71, 204800, s71\n\t"
      "s_add_u32 s40, s40, 1\n\t"
      "s_cmp_lt_u32 s40, 63\n\t"
      "s_cselect_b32 s61, 786432, 0\n\t"
      "s_cselect_b32 s60, s58, 0\n\t"
      "s_add_u32 s44, s44, s61\n\t"
      "s_cmp_lt_u32 s40, 62\n\t"
      "s_cselect_b32 s61, 786432, 0\n\t"
      "s_add_u32 s46, s46, s61\n\t"
      "s_add_u32 s61, s40, 3\n\t"
      "s_and_b32 s62, s61, 3\n\t"
      "s_cselect_b32 s62, 16, 2000\n\t"
      "s_cmp_lt_u32 s61, 64\n\t"
      "s_cselect_b32 s62, s62, 0\n\t"
      "s_add_u32 s50, s50, s62\n\t"
      "s_mov_b32 s63, 0x2000\n\t"
      "s_waitcnt lgkmcnt(8)\n\t"
      "v_mfma_f32_16x16x32_bf16 v[32:35], v[76:79], v[112:115], 0\n\t"
      "v_mfma_f32_16x16x32_bf16 v[36:39], v[80:83], v[112:115], 0\n\t"
      "v_mfma_f32_16x16x32_bf16 v[40:43], v[76:79], v[116:119], 0\n\t"
      "v_mfma_f32_16x16x32_bf16 v[44:47], v[80:83], v[116:119], 0\n\t"
      "ds_read_b128 v[76:79], v103\n\t"
      "ds_read_b128 v[80:83], v103 offset:1024\n\t"
      "ds_read_b128 v[112:115], v100 offset:24576\n\t"
      "ds_read_b128 v[116:119], v100 offset:25600\n\t"
      "s_waitcnt lgkmcnt(8)\n\t"
      "v_mfma_f32_16x16x32_bf16 v[32:35], v[0:3], v[8:11], v[32:35]\n\t"
      "v_mfma_f32_16x16x32_bf16 v[36:39], v[4:7], v[8:11], v[36:39]\n\t"
      "v_mfma_f32_16x16x32_bf16 v[40:43], v[0:3], v[12:15], v[40:43]\n\t"
      "v_mfma_f32_16x16x32_bf16 v[44:47], v[4:7], v[12:15], v[44:47]\n\t"
      "s_branch LBAR_1%=\n\t"
      "LFIRST_1%=:\n\t"
      "s_cmp_eq_u32 s40, 0\n\t"
      "s_cbranch_scc1 LWM0_1%=\n\t"
      "LDEC_1%=:\n\t"
      "s_waitcnt vmcnt(0)\n\t"
      "v_readfirstlane_b32 s61, v106\n\t"
      "s_mov_b32 s62, 72960\n\t"
      "s_sub_u32 s63, s63, 1\n\t"
      "buffer_load_dword v106, off, s[36:39], s62 sc1\n\t"
      "s_cmp_eq_u32 s63, 0\n\t"
      "s_cbranch_scc1 LDEC1_1%=\n\t"
      "s_cmp_lt_u32 s61, 67108864\n\t"
      "s_cbranch_scc1 LDEC_1%=\n\t"
      "LDEC1_1%=:\n\t"
      "s_cmp_gt_u32 s61, 67141632\n\t"
      "s_cselect_b32 s99, 0x2000, 0\n\t"
      "s_xor_b32 s97, s97, s99\n\t"
      "s_barrier\n\t"
      "s_mov_b32 s63, 0x2000\n\t"
      "s_branch LPOLL_1%=\n\t"
      "LWM0_1%=:\n\t"
      "s_mov_b32 s63, 0x10000\n\t"
      "LWM_1%=:\n\t"
      "s_waitcnt vmcnt(0) lgkmcnt(0)\n\t"
      "v_bitop3_b32 v93, v93, v95, s53 bitop3:0x7e\n\t"
      "v_max_u32 v92, v92, v94\n\t"
      "v_cmp_ne_u32 vcc, 0, v93\n\t"
      "s_nop 1\n\t"
      "v_max_u32_dpp v92, v92, v92 quad_perm:[1,0,3,2] row_mask:0xf bank_mask:0xf\n\t"
      "s_nop 1\n\t"
      "v_max_u32_dpp v92, v92, v92 quad_perm:[2,3,0,1] row_mask:0xf bank_mask:0xf\n\t"
      "s_nop 1\n\t"
      "v_max_u32_dpp v92, v92, v92 row_ror:4 row_mask:0xf bank_mask:0xf\n\t"
      "s_nop 1\n\t"
      "v_max_u32_dpp v92, v92, v92 row_ror:8 row_mask:0xf bank_mask:0xf\n\t"
      "v_mov_b32 v93, v92\n\t"
      "s_nop 1\n\t"
      "v_permlane16_swap_b32 v92, v93\n\t"
      "v_max_u32 v92, v92, v93\n\t"
      "v_mov_b32 v93, v92\n\t"
      "s_nop 1\n\t"
      "v_permlane32_swap_b32 v92, v93\n\t"
      "v_max_u32 v92, v92, v93\n\t"
      "v_mul_f32 v93, 0x40a00000, v92\n\t"
      "s_nop 0\n\t"
      "v_readfirstlane_b32 s67, v93\n\t"
      "v_mul_f32 v93, 0x43030000, v93\n\t"
      "s_nop 0\n\t"
      "v_readfirstlane_b32 s66, v93\n\t"
      "s_cbranch_vccz LWM1_1%=\n\t"
      "s_sleep 4\n\t"
      "buffer_load_dwordx2 v[92:93], v91, s[36:39], s94 offen sc1\n\t"
      "buffer_load_dwordx2 v[94:95], v91, s[36:39], s94 offen offset:128 sc1\n\t"
      "s_sub_u32 s63, s63, 1\n\t"
      "s_cmp_lg_u32 s63, 0\n\t"
      "s_cbranch_scc1 LWM_1%=\n\t"
      "s_waitcnt vmcnt(0)\n\t"
      "LWM1_1%=:\n\t"
      "s_lshr_b32 s67, s67, 23\n\t"
      "s_add_u32 s67, s67, 5\n\t"
      "s_lshr_b32 s66, s66, 23\n\t"
      "s_add_u32 s66, s66, 129\n\t"
      "s_max_u32 s66, s66, 80\n\t"
      "buffer_load_dwordx2 v[120:121], off, s[36:39], s64 sc1\n\t"
      "s_mov_b32 s63, 0x2000\n\t"
      "s_branch LPOLL_1%=\n\t"
      "LR0_1%=:\n\t"
      "buffer_load_dwordx4 v[84:87], v60, s[36:39], s43 offen sc1\n\t"
      "buffer_load_dwordx4 v[88:91], v60, s[36:39], s43 offen offset:1024 sc1\n\t"
      "s_waitcnt vmcnt(2)\n\t"
      "buffer_load_dwordx4 v[92:95], v60, s[36:39], s68 offen sc1\n\t"
      "buffer_load_dwordx4 v[96:99], v60, s[36:39], s68 offen offset:1024 sc1\n\t"
      "s_sub_u32 s63, s63, 1\n\t"
      "s_waitcnt vmcnt(2) lgkmcnt(0)\n\t"
      "v_bitop3_b32 v64, v84, v85, v86 bitop3:0x80\n\t"
      "v_bitop3_b32 v64, v64, v87, v88 bitop3:0x80\n\t"
      "v_bitop3_b32 v64, v64, v89, v90 bitop3:0x80\n\t"
      "v_bitop3_b32 v64, v64, v91, s58 bitop3:0x80\n\t"
      "v_cmp_eq_u32 vcc, 0, v64\n\t"
      "v_pk_fma_f32 v[0:1], v[84:85], s[56:57], v[126:127] op_sel:[0,1,0] op_sel_hi:[1,1,0]\n\t"
      "v_pk_fma_f32 v[2:3], v[86:87], s[56:57], v[126:127] op_sel:[0,1,0] op_sel_hi:[1,1,0]\n\t"
      "v_cvt_pk_bf16_f32 v4, v0, v1\n\t"
      "v_cvt_pk_bf16_f32 v5, v2, v3\n\t"
      "ds_write_b64 v127, v[4:5] offset:41984\n\t"
      "v_pk_fma_f32 v[12:13], v[88:89], s[56:57], v[126:127] op_sel:[0,1,0] op_sel_hi:[1,1,0]\n\t"
      "v_pk_fma_f32 v[14:15], v[90:91], s[56:57], v[126:127] op_sel:[0,1,0] op_sel_hi:[1,1,0]\n\t"
      "v_cvt_pk_bf16_f32 v6, v12, v13\n\t"
      "v_cvt_pk_bf16_f32 v7, v14, v15\n\t"
      "ds_write_b64 v127, v[6:7] offset:41992\n\t"
      "s_cmp_le_i32 s63, 0\n\t"
      "s_waitcnt lgkmcnt(0)\n\t"
      "s_cbranch_scc1 LRZ_1%=\n\t"
      "s_cbranch_vccnz LR0_1%=\n\t"
      "LRZ_1%=:\n\t"
      "s_branch LC0_1%=\n\t"
      "LR1_1%=:\n\t"
      "v_bitop3_b32 v64, v92, v93, v94 bitop3:0x80\n\t"
      "v_bitop3_b32 v64, v64, v95, v96 bitop3:0x80\n\t"
      "v_bitop3_b32 v64, v64, v97, v98 bitop3:0x80\n\t"
      "v_bitop3_b32 v64, v64, v99, s58 bitop3:0x80\n\t"
      "v_cmp_eq_u32 vcc, 0, v64\n\t"
      "s_nop 0\n\t"
      "s_cbranch_vccz LRX_1%=\n\t"
      "LRY_1%=:\n\t"
      "buffer_load_dwordx4 v[92:95], v60, s[36:39], s68 offen sc1\n\t"
      "buffer_load_dwordx4 v[96:99], v60, s[36:39], s68 offen offset:1024 sc1\n\t"
      "s_sub_u32 s63, s63, 1\n\t"
      "s_waitcnt vmcnt(0) lgkmcnt(0)\n\t"
      "v_bitop3_b32 v64, v92, v93, v94 bitop3:0x80\n\t"
      "v_bitop3_b32 v64, v64, v95, v96 bitop3:0x80\n\t"
      "v_bitop3_b32 v64, v64, v97, v98 bitop3:0x80\n\t"
      "v_bitop3_b32 v64, v64, v99, s58 bitop3:0x80\n\t"
      "v_cmp_eq_u32 vcc, 0, v64\n\t"
      "v_pk_fma_f32 v[16:17], v[92:93], s[56:57], v[126:127] op_sel:[0,1,0] op_sel_hi:[1,1,0]\n\t"
      "v_pk_fma_f32 v[18:19], v[94:95], s[56:57], v[126:127] op_sel:[0,1,0] op_sel_hi:[1,1,0]\n\t"
      "v_cvt_pk_bf16_f32 v20, v16, v17\n\t"
      "v_cvt_pk_bf16_f32 v21, v18, v19\n\t"
      "ds_write_b64 v127, v[20:21] offset:46080\n\t"
      "v_pk_fma_f32 v[28:29], v[96:97], s[56:57], v[126:127] op_sel:[0,1,0] op_sel_hi:[1,1,0]\n\t"
      "v_pk_fma_f32 v[30:31], v[98:99], s[56:57], v[126:127] op_sel:[0,1,0] op_sel_hi:[1,1,0]\n\t"
      "v_cvt_pk_bf16_f32 v22, v28, v29\n\t"
      "v_cvt_pk_bf16_f32 v23, v30, v31\n\t"
      "ds_write_b64 v127, v[22:23] offset:46088\n\t"
      "s_cmp_le_i32 s63, 0\n\t"
      "s_waitcnt lgkmcnt(0)\n\t"
      "s_cbranch_scc1 LRX_1%=\n\t"
      "s_cbranch_vccnz LRY_1%=\n\t"
      "LRX_1%=:\n\t"
      "s_cmp_eq_u32 s40, 63\n\t"
      "s_cbranch_scc1 LLAST_1%=\n\t"
      "s_branch LC1_1%=\n\t"
      "LMX_1%=:\n\t"
      "s_waitcnt lgkmcnt(0)\n\t"
      "s_nop 7\n\t"
      "v_smfmac_f32_16x16x64_bf16 v[52:55], v[12:15], v[216:223], v122\n\t"
      "v_smfmac_f32_16x16x64_bf16 v[52:55], v[8:11], v[208:215], v122\n\t"
      "v_smfmac_f32_16x16x64_bf16 v[52:55], v[20:23], v[232:239], v122\n\t"
      "v_smfmac_f32_16x16x64_bf16 v[52:55], v[16:19], v[224:231], v122\n\t"
      "v_smfmac_f32_16x16x64_bf16 v[52:55], v[28:31], v[248:255], v122\n\t"
      "v_smfmac_f32_16x16x64_bf16 v[52:55], v[24:27], v[240:247], v122\n\t"
      "s_add_u32 s62, s61, 0x83400000\n\t"
      "v_mov_b32 v126, s62\n\t"
      "s_nop 5\n\t"
      "v_add_f32 v53, v53, v55\n\t"
      "v_add_f32 v52, v52, v54\n\t"
      "v_cvt_pk_bf16_f32 v66, v52, v53\n\t"
      "s_add_u32 s68, s68, 0x8000\n\t"
      "v_max3_f32 v82, v112, v113, v114\n\t"
      "v_max_f32 v82, v82, v115\n\t"
      "s_nop 1\n\t"
      "v_max_f32_dpp v82, v82, v82 quad_perm:[2,3,0,1] row_mask:0xf bank_mask:0xf\n\t"
      "v_max3_f32 v83, v82, v120, v121\n\t"
      "v_max3_f32 v60, v82, v120, v121\n\t"
      "s_nop 1\n\t"
      "v_permlane16_swap_b32 v83, v60\n\t"
      "v_max_f32 v61, v83, v60\n\t"
      "v_max_f32 v60, v83, v60\n\t"
      "s_nop 1\n\t"
      "v_permlane32_swap_b32 v61, v60\n\t"
      "v_max_f32 v83, v61, v60\n\t"
      "v_sub_f32 v112, v112, v83\n\t"
      "v_sub_f32 v113, v113, v83\n\t"
      "v_sub_f32 v114, v114, v83\n\t"
      "v_sub_f32 v115, v115, v83\n\t"
      "v_sub_f32 v120, v120, v83\n\t"
      "v_sub_f32 v121, v121, v83\n\t"
      "v_exp_f32 v56, v112\n\t"
      "v_exp_f32_e64 v57, v113\n\t"
      "v_exp_f32 v58, v114\n\t"
      "v_exp_f32 v59, v115\n\t"
      "v_exp_f32_e64 v80, v120\n\t"
      "v_exp_f32 v81, v121\n\t"
      "v_cvt_pk_bf16_f32 v96, v56, v57\n\t"
      "v_cvt_pk_bf16_f32 v97, v58, v59\n\t"
      "v_cvt_pk_bf16_f32 v108, v80, v81\n\t"
      "v_mov_b32_dpp v98, v96 quad_perm:[2,3,0,1] row_mask:0xf bank_mask:0xf\n\t"
      "v_mov_b32_dpp v99, v97 quad_perm:[2,3,0,1] row_mask:0xf bank_mask:0xf\n\t"
      "v_mfma_f32_16x16x32_bf16 v[48:51], v[108:111], v[88:91], 0\n\t"
      "s_nop 0\n\t"
      "v_mfma_f32_16x16x32_bf16 v[48:51], v[96:99], v[88:91], v[48:51]\n\t"
      "v_mfma_f32_16x16x32_bf16 v[84:87], v[108:111], v[64:67], 0\n\t"
      "v_mfma_f32_16x16x32_bf16 v[84:87], v[96:99], v[68:71], v[84:87]\n\t"
      "v_mfma_f32_16x16x32_bf16 v[92:95], v[96:99], v[72:75], 0\n\t"
      "v_mfma_f32_16x16x32_bf16 v[92:95], v[108:111], v[66:69], v[92:95]\n\t"
      "s_nop 3\n\t"
      "v_rcp_f32_e64 v48, v48\n\t"
      "v_rcp_f32 v49, v49\n\t"
      "v_fma_f32 v84, v84, v48, s96\n\t"
      "buffer_atomic_add v84, v123, s[36:39], s97 offen\n\t"
      "v_fma_f32 v85, v85, v49, s96\n\t"
      "buffer_atomic_add v85, v123, s[36:39], s97 offen offset:512\n\t"
      "v_fma_f32 v94, v94, v48, s96\n\t"
      "buffer_atomic_add v94, v123, s[36:39], s69 offen\n\t"
      "v_fma_f32 v95, v95, v49, s96\n\t"
      "buffer_atomic_add v95, v123, s[36:39], s69 offen offset:512\n\t"
      "s_add_u32 s97, s97, 0x8000\n\t"
      "s_branch LWIN_1%=\n\t"
      "LEARLY_1%=:\n\t"
      "s_cmp_lt_i32 s40, 0\n\t"
      "s_waitcnt lgkmcnt(0)\n\t"
      "s_cbranch_scc1 LUTOK_1%=\n\t"
      "s_xor_b32 s43, s43, s99\n\t"
      "s_xor_b32 s68, s68, s99\n\t"
      "s_mov_b32 s61, 72960\n\t"
      "buffer_load_dword v106, off, s[36:39], s61 sc1\n\t"
      "s_branch LROLE_1%=\n\t"
      "LSLOW_1%=:\n\t"
      "s_mov_b32 s63, 0x10000\n\t"
      "LSLOW1_1%=:\n\t"
      "buffer_load_dwordx2 v[92:93], off, s[36:39], s64 sc1\n\t"
      "s_waitcnt vmcnt(0)\n\t"
      "v_bitop3_b32 v92, v92, v93, s53 bitop3:0x7e\n\t"
      "s_nop 0\n\t"
      "v_readfirstlane_b32 s56, v92\n\t"
      "s_nop 1\n\t"
      "s_cmp_eq_u32 s56, 0\n\t"
      "s_cbranch_scc1 LSLOW2_1%=\n\t"
      "s_sleep 8\n\t"
      "s_sub_u32 s63, s63, 1\n\t"
      "s_cmp_lg_u32 s63, 0\n\t"
      "s_cbranch_scc1 LSLOW1_1%=\n\t"
      "LSLOW2_1%=:\n\t"
      "s_mov_b32 m0, s45\n\t"
      "s_sub_u32 s61, 204800, s71\n\t"
      "buffer_load_dwordx4 v65, s[36:39], s44 offen sc1 lds\n\t"
      "buffer_load_dwordx4 v65, s[36:39], s44 offen offset:1024 sc1 lds\n\t"
      "s_add_u32 m0, s55, s61\n\t"
      "s_nop 0\n\t"
      "buffer_load_dwordx4 v127, s[36:39], s46 offen sc1 lds\n\t"
      "buffer_load_dwordx4 v127, s[36:39], s46 offen offset:1024 sc1 lds\n\t"
      "s_branch LWIN1_1%=\n\t"
      "LLAST_1%=:\n\t"
      "s_waitcnt lgkmcnt(0)\n\t"
      "s_barrier\n\t"
      "ds_read_b128 v[16:19], v127 offset:45056\n\t"
      "ds_read_b128 v[28:31], v127 offset:48128\n\t"
      "ds_read_b128 v[24:27], v127 offset:47104\n\t"
      "v_smfmac_f32_16x16x64_bf16 v[40:43], v[8:11], a[144:151], v122\n\t"
      "v_smfmac_f32_16x16x64_bf16 v[44:47], v[8:11], a[208:215], v122\n\t"
      "v_smfmac_f32_16x16x64_bf16 v[44:47], v[20:23], a[232:239], v122\n\t"
      "v_smfmac_f32_16x16x64_bf16 v[40:43], v[20:23], a[168:175], v122\n\t"
      "s_waitcnt lgkmcnt(2)\n\t"
      "v_smfmac_f32_16x16x64_bf16 v[40:43], v[16:19], a[160:167], v122\n\t"
      "v_smfmac_f32_16x16x64_bf16 v[44:47], v[16:19], a[224:231], v122\n\t"
      "s_waitcnt lgkmcnt(1)\n\t"
      "v_smfmac_f32_16x16x64_bf16 v[40:43], v[28:31], a[184:191], v122\n\t"
      "v_smfmac_f32_16x16x64_bf16 v[44:47], v[28:31], a[248:255], v122\n\t"
      "s_waitcnt lgkmcnt(0)\n\t"
      "v_smfmac_f32_16x16x64_bf16 v[40:43], v[24:27], a[176:183], v122\n\t"
      "v_smfmac_f32_16x16x64_bf16 v[44:47], v[24:27], a[240:247], v122\n\t"
      "s_nop 3\n\t"
      "v_add_f32 v40, v40, v42\n\t"
      "v_add_f32 v41, v41, v43\n\t"
      "v_cvt_pk_bf16_f32 v120, v40, v41\n\t"
      "s_nop 0\n\t"
      "v_add_f32 v44, v44, v46\n\t"
      "v_add_f32 v45, v45, v47\n\t"
      "v_cvt_pk_bf16_f32 v121, v44, v45\n\t"
      "ds_write_b64 v125, v[120:121] offset:2080\n\t"
      "s_waitcnt lgkmcnt(0)\n\t"
      "s_barrier\n\t"
      "ds_read_b64_tr_b16 v[40:41], v124 offset:51232\n\t"
      "ds_read_b64_tr_b16 v[42:43], v124 offset:51752\n\t"
      "ds_read_b64_tr_b16 v[44:45], v124 offset:52272\n\t"
      "ds_read_b64_tr_b16 v[46:47], v124 offset:52792\n\t"
      "v_add_u32 v100, s55, v127\n\t"
      "ds_read_b128 v[60:63], v100 offset:57344\n\t"
      "s_waitcnt lgkmcnt(0)\n\t"
      "s_branch LROLE_1%=\n\t"
      "LWK1_2%=:\n\t"
      "buffer_store_dwordx4 v[44:47], v61, s[36:39], s48 offen sc1\n\t"
      "s_branch LW4_2%=\n\t"
      "LFIRST_2%=:\n\t"
      "s_cmp_eq_u32 s40, 0\n\t"
      "s_cbranch_scc1 LWM0_2%=\n\t"
      "LDEC_2%=:\n\t"
      "s_waitcnt vmcnt(0)\n\t"
      "v_readfirstlane_b32 s61, v106\n\t"
      "s_mov_b32 s62, 72960\n\t"
      "s_sub_u32 s63, s63, 1\n\t"
      "buffer_load_dword v106, off, s[36:39], s62 sc1\n\t"
      "s_cmp_eq_u32 s63, 0\n\t"
      "s_cbranch_scc1 LDEC1_2%=\n\t"
      "s_cmp_lt_u32 s61, 67108864\n\t"
      "s_cbranch_scc1 LDEC_2%=\n\t"
      "LDEC1_2%=:\n\t"
      "s_cmp_gt_u32 s61, 67141632\n\t"
      "s_cselect_b32 s99, 0x2000, 0\n\t"
      "s_xor_b32 s97, s97, s99\n\t"
      "s_barrier\n\t"
      "s_mov_b32 s63, 0x2000\n\t"
      "s_branch LPOLL_2%=\n\t"
      "LWM0_2%=:\n\t"
      "s_mov_b32 s63, 0x10000\n\t"
      "LWM_2%=:\n\t"
      "s_waitcnt vmcnt(0) lgkmcnt(0)\n\t"
      "v_bitop3_b32 v93, v93, v95, s53 bitop3:0x7e\n\t"
      "v_max_u32 v92, v92, v94\n\t"
      "v_cmp_ne_u32 vcc, 0, v93\n\t"
      "s_nop 1\n\t"
      "v_max_u32_dpp v92, v92, v92 quad_perm:[1,0,3,2] row_mask:0xf bank_mask:0xf\n\t"
      "s_nop 1\n\t"
      "v_max_u32_dpp v92, v92, v92 quad_perm:[2,3,0,1] row_mask:0xf bank_mask:0xf\n\t"
      "s_nop 1\n\t"
      "v_max_u32_dpp v92, v92, v92 row_ror:4 row_mask:0xf bank_mask:0xf\n\t"
      "s_nop 1\n\t"
      "v_max_u32_dpp v92, v92, v92 row_ror:8 row_mask:0xf bank_mask:0xf\n\t"
      "v_mov_b32 v93, v92\n\t"
      "s_nop 1\n\t"
      "v_permlane16_swap_b32 v92, v93\n\t"
      "v_max_u32 v92, v92, v93\n\t"
      "v_mov_b32 v93, v92\n\t"
      "s_nop 1\n\t"
      "v_permlane32_swap_b32 v92, v93\n\t"
      "v_max_u32 v92, v92, v93\n\t"
      "v_mul_f32 v93, 0x40a00000, v92\n\t"
      "s_nop 0\n\t"
      "v_readfirstlane_b32 s67, v93\n\t"
      "v_mul_f32 v93, 0x43030000, v93\n\t"
      "s_nop 0\n\t"
      "v_readfirstlane_b32 s66, v93\n\t"
      "s_cbranch_vccz LWM1_2%=\n\t"
      "s_sleep 4\n\t"
      "buffer_load_dwordx2 v[92:93], v91, s[36:39], s94 offen sc1\n\t"
      "buffer_load_dwordx2 v[94:95], v91, s[36:39], s94 offen offset:128 sc1\n\t"
      "s_sub_u32 s63, s63, 1\n\t"
      "s_cmp_lg_u32 s63, 0\n\t"
      "s_cbranch_scc1 LWM_2%=\n\t"
      "s_waitcnt vmcnt(0)\n\t"
      "LWM1_2%=:\n\t"
      "s_cmp_lg_u32 s86, 0\n\t"
      "v_mov_b32 v92, s65\n\t"
      "s_mov_b32 s62, 72960\n\t"
      "s_cselect_b64 exec, 1, 0\n\t"
      "buffer_atomic_add v92, off, s[36:39], s62\n\t"
      "s_mov_b64 exec, -1\n\t"
      "s_lshr_b32 s67, s67, 23\n\t"
      "s_add_u32 s67, s67, 5\n\t"
      "s_lshr_b32 s66, s66, 23\n\t"
      "s_add_u32 s66, s66, 129\n\t"
      "s_max_u32 s66, s66, 80\n\t"
      "buffer_load_dwordx2 v[120:121], off, s[36:39], s64 sc1\n\t"
      "s_mov_b32 s63, 0x2000\n\t"
      "s_branch LPOLL_2%=\n\t"
      "LR0_2%=:\n\t"
      "buffer_load_dwordx4 v[84:87], v60, s[36:39], s43 offen sc1\n\t"
      "buffer_load_dwordx4 v[88:91], v60, s[36:39], s43 offen offset:1024 sc1\n\t"
      "s_waitcnt vmcnt(2)\n\t"
      "buffer_load_dwordx4 v[92:95], v60, s[36:39], s68 offen sc1\n\t"
      "buffer_load_dwordx4 v[96:99], v60, s[36:39], s68 offen offset:1024 sc1\n\t"
      "s_sub_u32 s63, s63, 1\n\t"
      "s_waitcnt vmcnt(2) lgkmcnt(0)\n\t"
      "v_bitop3_b32 v64, v84, v85, v86 bitop3:0x80\n\t"
      "v_bitop3_b32 v64, v64, v87, v88 bitop3:0x80\n\t"
      "v_bitop3_b32 v64, v64, v89, v90 bitop3:0x80\n\t"
      "v_bitop3_b32 v64, v64, v91, s58 bitop3:0x80\n\t"
      "v_cmp_eq_u32 vcc, 0, v64\n\t"
      "v_pk_fma_f32 v[12:13], v[84:85], s[56:57], v[126:127] op_sel:[0,1,0] op_sel_hi:[1,1,0]\n\t"
      "v_pk_fma_f32 v[14:15], v[86:87], s[56:57], v[126:127] op_sel:[0,1,0] op_sel_hi:[1,1,0]\n\t"
      "v_cvt_pk_bf16_f32 v8, v12, v13\n\t"
      "v_cvt_pk_bf16_f32 v9, v14, v15\n\t"
      "ds_write_b64 v127, v[8:9] offset:43008\n\t"
      "v_pk_fma_f32 v[0:1], v[88:89], s[56:57], v[126:127] op_sel:[0,1,0] op_sel_hi:[1,1,0]\n\t"
      "v_pk_fma_f32 v[2:3], v[90:91], s[56:57], v[126:127] op_sel:[0,1,0] op_sel_hi:[1,1,0]\n\t"
      "v_cvt_pk_bf16_f32 v10, v0, v1\n\t"
      "v_cvt_pk_bf16_f32 v11, v2, v3\n\t"
      "ds_write_b64 v127, v[10:11] offset:43016\n\t"
      "s_cmp_le_i32 s63, 0\n\t"
      "s_waitcnt lgkmcnt(0)\n\t"
      "s_cbranch_scc1 LRZ_2%=\n\t"
      "s_cbranch_vccnz LR0_2%=\n\t"
      "LRZ_2%=:\n\t"
      "s_branch LC0_2%=\n\t"
      "LR1_2%=:\n\t"
      "v_bitop3_b32 v64, v92, v93, v94 bitop3:0x80\n\t"
      "v_bitop3_b32 v64, v64, v95, v96 bitop3:0x80\n\t"
      "v_bitop3_b32 v64, v64, v97, v98 bitop3:0x80\n\t"
      "v_bitop3_b32 v64, v64, v99, s58 bitop3:0x80\n\t"
      "v_cmp_eq_u32 vcc, 0, v64\n\t"
      "s_nop 0\n\t"
      "s_cbranch_vccz LRX_2%=\n\t"
      "LRY_2%=:\n\t"
      "buffer_load_dwordx4 v[92:95], v60, s[36:39], s68 offen sc1\n\t"
      "buffer_load_dwordx4 v[96:99], v60, s[36:39], s68 offen offset:1024 sc1\n\t"
      "s_sub_u32 s63, s63, 1\n\t"
      "s_waitcnt vmcnt(0) lgkmcnt(0)\n\t"
      "v_bitop3_b32 v64, v92, v93, v94 bitop3:0x80\n\t"
      "v_bitop3_b32 v64, v64, v95, v96 bitop3:0x80\n\t"
      "v_bitop3_b32 v64, v64, v97, v98 bitop3:0x80\n\t"
      "v_bitop3_b32 v64, v64, v99, s58 bitop3:0x80\n\t"
      "v_cmp_eq_u32 vcc, 0, v64\n\t"
      "v_pk_fma_f32 v[28:29], v[92:93], s[56:57], v[126:127] op_sel:[0,1,0] op_sel_hi:[1,1,0]\n\t"
      "v_pk_fma_f32 v[30:31], v[94:95], s[56:57], v[126:127] op_sel:[0,1,0] op_sel_hi:[1,1,0]\n\t"
      "v_cvt_pk_bf16_f32 v24, v28, v29\n\t"
      "v_cvt_pk_bf16_f32 v25, v30, v31\n\t"
      "ds_write_b64 v127, v[24:25] offset:47104\n\t"
      "v_pk_fma_f32 v[16:17], v[96:97], s[56:57], v[126:127] op_sel:[0,1,0] op_sel_hi:[1,1,0]\n\t"
      "v_pk_fma_f32 v[18:19], v[98:99], s[56:57], v[126:127] op_sel:[0,1,0] op_sel_hi:[1,1,0]\n\t"
      "v_cvt_pk_bf16_f32 v26, v16, v17\n\t"
      "v_cvt_pk_bf16_f32 v27, v18, v19\n\t"
      "ds_write_b64 v127, v[26:27] offset:47112\n\t"
      "s_cmp_le_i32 s63, 0\n\t"
      "s_waitcnt lgkmcnt(0)\n\t"
      "s_cbranch_scc1 LRX_2%=\n\t"
      "s_cbranch_vccnz LRY_2%=\n\t"
      "LRX_2%=:\n\t"
      "s_cmp_eq_u32 s40, 63\n\t"
      "s_cbranch_scc1 LLAST_2%=\n\t"
      "s_branch LC1_2%=\n\t"
      "LMX_2%=:\n\t"
      "s_waitcnt lgkmcnt(0)\n\t"
      "s_nop 7\n\t"
      "v_smfmac_f32_16x16x64_bf16 v[52:55], v[0:3], v[192:199], v122\n\t"
      "v_smfmac_f32_16x16x64_bf16 v[52:55], v[4:7], v[200:207], v122\n\t"
      "v_smfmac_f32_16x16x64_bf16 v[52:55], v[24:27], v[240:247], v122\n\t"
      "v_smfmac_f32_16x16x64_bf16 v[52:55], v[28:31], v[248:255], v122\n\t"
      "v_smfmac_f32_16x16x64_bf16 v[52:55], v[16:19], v[224:231], v122\n\t"
      "v_smfmac_f32_16x16x64_bf16 v[52:55], v[20:23], v[232:239], v122\n\t"
      "s_add_u32 s62, s61, 0x83400000\n\t"
      "v_mov_b32 v126, s62\n\t"
      "s_nop 5\n\t"
      "v_add_f32 v53, v53, v55\n\t"
      "v_add_f32 v52, v52, v54\n\t"
      "v_cvt_pk_bf16_f32 v66, v52, v53\n\t"
      "s_add_u32 s68, s68, 0x8000\n\t"
      "v_max3_f32 v82, v112, v113, v114\n\t"
      "v_max_f32 v82, v82, v115\n\t"
      "s_nop 1\n\t"
      "v_max_f32_dpp v82, v82, v82 quad_perm:[2,3,0,1] row_mask:0xf bank_mask:0xf\n\t"
      "v_max3_f32 v83, v82, v120, v121\n\t"
      "v_max3_f32 v60, v82, v120, v121\n\t"
      "s_nop 1\n\t"
      "v_permlane16_swap_b32 v83, v60\n\t"
      "v_max_f32 v61, v83, v60\n\t"
      "v_max_f32 v60, v83, v60\n\t"
      "s_nop 1\n\t"
      "v_permlane32_swap_b32 v61, v60\n\t"
      "v_max_f32 v83, v61, v60\n\t"
      "v_sub_f32 v112, v112, v83\n\t"
      "v_sub_f32 v113, v113, v83\n\t"
      "v_sub_f32 v114, v114, v83\n\t"
      "v_sub_f32 v115, v115, v83\n\t"
      "v_sub_f32 v120, v120, v83\n\t"
      "v_sub_f32 v121, v121, v83\n\t"
      "v_exp_f32 v56, v112\n\t"
      "v_exp_f32_e64 v57, v113\n\t"
      "v_exp_f32 v58, v114\n\t"
      "v_exp_f32 v59, v115\n\t"
      "v_exp_f32_e64 v80, v120\n\t"
      "v_exp_f32 v81, v121\n\t"
      "v_cvt_pk_bf16_f32 v96, v56, v57\n\t"
      "v_cvt_pk_bf16_f32 v97, v58, v59\n\t"
      "v_cvt_pk_bf16_f32 v108, v80, v81\n\t"
      "v_mov_b32_dpp v98, v96 quad_perm:[2,3,0,1] row_mask:0xf bank_mask:0xf\n\t"
      "v_mov_b32_dpp v99, v97 quad_perm:[2,3,0,1] row_mask:0xf bank_mask:0xf\n\t"
      "v_mfma_f32_16x16x32_bf16 v[48:51], v[108:111], v[88:91], 0\n\t"
      "s_nop 0\n\t"
      "v_mfma_f32_16x16x32_bf16 v[48:51], v[96:99], v[88:91], v[48:51]\n\t"
      "v_mfma_f32_16x16x32_bf16 v[84:87], v[108:111], v[64:67], 0\n\t"
      "v_mfma_f32_16x16x32_bf16 v[84:87], v[96:99], v[68:71], v[84:87]\n\t"
      "v_mfma_f32_16x16x32_bf16 v[92:95], v[96:99], v[72:75], 0\n\t"
      "v_mfma_f32_16x16x32_bf16 v[92:95], v[108:111], v[66:69], v[92:95]\n\t"
      "s_nop 3\n\t"
      "v_rcp_f32_e64 v48, v48\n\t"
      "v_rcp_f32 v49, v49\n\t"
      "v_fma_f32 v84, v84, v48, s96\n\t"
      "buffer_atomic_add v84, v123, s[36:39], s97 offen\n\t"
      "v_fma_f32 v85, v85, v49, s96\n\t"
      "buffer_atomic_add v85, v123, s[36:39], s97 offen offset:512\n\t"
      "v_fma_f32 v94, v94, v48, s96\n\t"
      "buffer_atomic_add v94, v123, s[36:39], s69 offen\n\t"
      "v_fma_f32 v95, v95, v49, s96\n\t"
      "buffer_atomic_add v95, v123, s[36:39], s69 offen offset:512\n\t"
      "s_add_u32 s97, s97, 0x8000\n\t"
      "s_branch LWIN_2%=\n\t"
      "LEARLY_2%=:\n\t"
      "s_cmp_lt_i32 s40, 0\n\t"
      "s_waitcnt lgkmcnt(0)\n\t"
      "s_cbranch_scc1 LUTOK_2%=\n\t"
      "s_xor_b32 s43, s43, s99\n\t"
      "s_xor_b32 s68, s68, s99\n\t"
      "s_mov_b32 s61, 72960\n\t"
      "buffer_load_dword v106, off, s[36:39], s61 sc1\n\t"
      "s_branch LROLE_2%=\n\t"
      "LSLOW_2%=:\n\t"
      "s_mov_b32 s63, 0x10000\n\t"
      "LSLOW1_2%=:\n\t"
      "buffer_load_dwordx2 v[92:93], off, s[36:39], s64 sc1\n\t"
      "s_waitcnt vmcnt(0)\n\t"
      "v_bitop3_b32 v92, v92, v93, s53 bitop3:0x7e\n\t"
      "s_nop 0\n\t"
      "v_readfirstlane_b32 s56, v92\n\t"
      "s_nop 1\n\t"
      "s_cmp_eq_u32 s56, 0\n\t"
      "s_cbranch_scc1 LSLOW2_2%=\n\t"
      "s_sleep 8\n\t"
      "s_sub_u32 s63, s63, 1\n\t"
      "s_cmp_lg_u32 s63, 0\n\t"
      "s_cbranch_scc1 LSLOW1_2%=\n\t"
      "LSLOW2_2%=:\n\t"
      "s_mov_b32 m0, s45\n\t"
      "s_sub_u32 s61, 204800, s71\n\t"
      "buffer_load_dwordx4 v65, s[36:39], s44 offen sc1 lds\n\t"
      "buffer_load_dwordx4 v65, s[36:39], s44 offen offset:1024 sc1 lds\n\t"
      "s_add_u32 m0, s55, s61\n\t"
      "s_nop 0\n\t"
      "buffer_load_dwordx4 v127, s[36:39], s46 offen sc1 lds\n\t"
      "buffer_load_dwordx4 v127, s[36:39], s46 offen offset:1024 sc1 lds\n\t"
      "s_branch LWIN1_2%=\n\t"
      "LLAST_2%=:\n\t"
      "s_waitcnt lgkmcnt(0)\n\t"
      "s_barrier\n\t"
      "ds_read_b128 v[28:31], v127 offset:48128\n\t"
      "ds_read_b128 v[16:19], v127 offset:45056\n\t"
      "ds_read_b128 v[20:23], v127 offset:46080\n\t"
      "v_smfmac_f32_16x16x64_bf16 v[40:43], v[4:7], a[136:143], v122\n\t"
      "v_smfmac_f32_16x16x64_bf16 v[44:47], v[4:7], a[200:207], v122\n\t"
      "v_smfmac_f32_16x16x64_bf16 v[44:47], v[24:27], a[240:247], v122\n\t"
      "v_smfmac_f32_16x16x64_bf16 v[40:43], v[24:27], a[176:183], v122\n\t"
      "s_waitcnt lgkmcnt(2)\n\t"
      "v_smfmac_f32_16x16x64_bf16 v[40:43], v[28:31], a[184:191], v122\n\t"
      "v_smfmac_f32_16x16x64_bf16 v[44:47], v[28:31], a[248:255], v122\n\t"
      "s_waitcnt lgkmcnt(1)\n\t"
      "v_smfmac_f32_16x16x64_bf16 v[40:43], v[16:19], a[160:167], v122\n\t"
      "v_smfmac_f32_16x16x64_bf16 v[44:47], v[16:19], a[224:231], v122\n\t"
      "s_waitcnt lgkmcnt(0)\n\t"
      "v_smfmac_f32_16x16x64_bf16 v[40:43], v[20:23], a[168:175], v122\n\t"
      "v_smfmac_f32_16x16x64_bf16 v[44:47], v[20:23], a[232:239], v122\n\t"
      "s_nop 3\n\t"
      "v_add_f32 v40, v40, v42\n\t"
      "v_add_f32 v41, v41, v43\n\t"
      "v_cvt_pk_bf16_f32 v120, v40, v41\n\t"
      "s_nop 0\n\t"
      "v_add_f32 v44, v44, v46\n\t"
      "v_add_f32 v45, v45, v47\n\t"
      "v_cvt_pk_bf16_f32 v121, v44, v45\n\t"
      "ds_write_b64 v125, v[120:121] offset:2080\n\t"
      "s_waitcnt lgkmcnt(0)\n\t"
      "s_barrier\n\t"
      "ds_read_b64_tr_b16 v[40:41], v124 offset:51232\n\t"
      "ds_read_b64_tr_b16 v[42:43], v124 offset:51752\n\t"
      "ds_read_b64_tr_b16 v[44:45], v124 offset:52272\n\t"
      "ds_read_b64_tr_b16 v[46:47], v124 offset:52792\n\t"
      "v_add_u32 v100, s55, v127\n\t"
      "ds_read_b128 v[60:63], v100 offset:57344\n\t"
      "s_waitcnt lgkmcnt(0)\n\t"
      "s_branch LROLE_2%=\n\t"
      "LFIRST_3%=:\n\t"
      "s_cmp_eq_u32 s40, 0\n\t"
      "s_cbranch_scc1 LWM0_3%=\n\t"
      "LDEC_3%=:\n\t"
      "s_waitcnt vmcnt(0)\n\t"
      "v_readfirstlane_b32 s61, v106\n\t"
      "s_mov_b32 s62, 72960\n\t"
      "s_sub_u32 s63, s63, 1\n\t"
      "buffer_load_dword v106, off, s[36:39], s62 sc1\n\t"
      "s_cmp_eq_u32 s63, 0\n\t"
      "s_cbranch_scc1 LDEC1_3%=\n\t"
      "s_cmp_lt_u32 s61, 67108864\n\t"
      "s_cbranch_scc1 LDEC_3%=\n\t"
      "LDEC1_3%=:\n\t"
      "s_cmp_gt_u32 s61, 67141632\n\t"
      "s_cselect_b32 s99, 0x2000, 0\n\t"
      "s_xor_b32 s97, s97, s99\n\t"
      "s_mov_b32 s62, 64\n\t"
      "LST0_3%=:\n\t"
      "v_bitop3_b32 v93, v93, v95, s53 bitop3:0x7e\n\t"
      "v_cmp_ne_u32 vcc, 0, v93\n\t"
      "v_subrev_u32 v97, s87, v92\n\t"
      "v_subrev_u32 v98, s87, v94\n\t"
      "v_max_i32 v97, v97, v98\n\t"
      "s_cbranch_vccz LST3_3%=\n\t"
      "buffer_load_dwordx2 v[92:93], v96, s[36:39], s86 offen sc1\n\t"
      "buffer_load_dwordx2 v[94:95], v96, s[36:39], s86 offen offset:128 sc1\n\t"
      "s_sub_u32 s62, s62, 1\n\t"
      "s_cmp_eq_u32 s62, 0\n\t"
      "s_waitcnt vmcnt(0)\n\t"
      "s_cbranch_scc0 LST0_3%=\n\t"
      "s_branch LST2_3%=\n\t"
      "LST3_3%=:\n\t"
      "s_nop 1\n\t"
      "v_max_i32_dpp v97, v97, v97 quad_perm:[1,0,3,2] row_mask:0xf bank_mask:0xf\n\t"
      "s_nop 1\n\t"
      "v_max_i32_dpp v97, v97, v97 quad_perm:[2,3,0,1] row_mask:0xf bank_mask:0xf\n\t"
      "s_nop 1\n\t"
      "v_max_i32_dpp v97, v97, v97 row_ror:4 row_mask:0xf bank_mask:0xf\n\t"
      "s_nop 1\n\t"
      "v_max_i32_dpp v97, v97, v97 row_ror:8 row_mask:0xf bank_mask:0xf\n\t"
      "v_mov_b32 v98, v97\n\t"
      "s_nop 1\n\t"
      "v_permlane16_swap_b32 v97, v98\n\t"
      "v_max_i32 v97, v97, v98\n\t"
      "v_mov_b32 v98, v97\n\t"
      "s_nop 1\n\t"
      "v_permlane32_swap_b32 v97, v98\n\t"
      "v_max_i32 v97, v97, v98\n\t"
      "s_nop 0\n\t"
      "v_readfirstlane_b32 s61, v97\n\t"
      "s_nop 1\n\t"
      "s_min_u32 s61, s61, 500\n\t"
      "s_add_u32 s61, s61, 170\n\t"
      "LST1_3%=:\n\t"
      "s_memrealtime s[92:93]\n\t"
      "s_waitcnt lgkmcnt(0)\n\t"
      "s_sub_u32 s62, s92, s87\n\t"
      "s_cmp_lt_u32 s62, s61\n\t"
      "s_cbranch_scc1 LST1_3%=\n\t"
      "LST2_3%=:\n\t"
      "s_barrier\n\t"
      "s_mov_b32 s63, 0x2000\n\t"
      "s_branch LPOLL_3%=\n\t"
      "LWM0_3%=:\n\t"
      "s_mov_b32 s63, 0x10000\n\t"
      "LWM_3%=:\n\t"
      "s_waitcnt vmcnt(0) lgkmcnt(0)\n\t"
      "v_bitop3_b32 v93, v93, v95, s53 bitop3:0x7e\n\t"
      "v_max_u32 v92, v92, v94\n\t"
      "v_cmp_ne_u32 vcc, 0, v93\n\t"
      "s_nop 1\n\t"
      "v_max_u32_dpp v92, v92, v92 quad_perm:[1,0,3,2] row_mask:0xf bank_mask:0xf\n\t"
      "s_nop 1\n\t"
      "v_max_u32_dpp v92, v92, v92 quad_perm:[2,3,0,1] row_mask:0xf bank_mask:0xf\n\t"
      "s_nop 1\n\t"
      "v_max_u32_dpp v92, v92, v92 row_ror:4 row_mask:0xf bank_mask:0xf\n\t"
      "s_nop 1\n\t"
      "v_max_u32_dpp v92, v92, v92 row_ror:8 row_mask:0xf bank_mask:0xf\n\t"
      "v_mov_b32 v93, v92\n\t"
      "s_nop 1\n\t"
      "v_permlane16_swap_b32 v92, v93\n\t"
      "v_max_u32 v92, v92, v93\n\t"
      "v_mov_b32 v93, v92\n\t"
      "s_nop 1\n\t"
      "v_permlane32_swap_b32 v92, v93\n\t"
      "v_max_u32 v92, v92, v93\n\t"
      "v_mul_f32 v93, 0x40a00000, v92\n\t"
      "s_nop 0\n\t"
      "v_readfirstlane_b32 s67, v93\n\t"
      "v_mul_f32 v93, 0x43030000, v93\n\t"
      "s_nop 0\n\t"
      "v_readfirstlane_b32 s66, v93\n\t"
      "s_cbranch_vccz LWM1_3%=\n\t"
      "s_sleep 4\n\t"
      "buffer_load_dwordx2 v[92:93], v91, s[36:39], s94 offen sc1\n\t"
      "buffer_load_dwordx2 v[94:95], v91, s[36:39], s94 offen offset:128 sc1\n\t"
      "s_sub_u32 s63, s63, 1\n\t"
      "s_cmp_lg_u32 s63, 0\n\t"
      "s_cbranch_scc1 LWM_3%=\n\t"
      "s_waitcnt vmcnt(0)\n\t"
      "LWM1_3%=:\n\t"
      "s_cmp_lg_u32 s86, 0\n\t"
      "v_mov_b32 v92, s65\n\t"
      "s_mov_b32 s62, 72960\n\t"
      "s_cselect_b64 exec, 1, 0\n\t"
      "buffer_atomic_add v92, off, s[36:39], s62\n\t"
      "s_mov_b64 exec, -1\n\t"
      "s_lshr_b32 s67, s67, 23\n\t"
      "s_add_u32 s67, s67, 5\n\t"
      "s_lshr_b32 s66, s66, 23\n\t"
      "s_add_u32 s66, s66, 129\n\t"
      "s_max_u32 s66, s66, 80\n\t"
      "s_memrealtime s[92:93]\n\t"
      "s_mov_b64 exec, 1\n\t"
      "v_mov_b32 v89, s53\n\t"
      "s_add_u32 s86, s94, 208640\n\t"
      "s_add_u32 s61, s95, 208640\n\t"
      "s_waitcnt lgkmcnt(0)\n\t"
      "v_mov_b32 v88, s92\n\t"
      "s_mov_b32 s87, s92\n\t"
      "buffer_store_dwordx2 v[88:89], off, s[36:39], s61 sc1\n\t"
      "s_mov_b64 exec, -1\n\t"
      "buffer_load_dwordx2 v[120:121], off, s[36:39], s64 sc1\n\t"
      "s_mov_b32 s63, 0x2000\n\t"
      "s_branch LPOLL_3%=\n\t"
      "LR0_3%=:\n\t"
      "buffer_load_dwordx4 v[84:87], v60, s[36:39], s43 offen sc1\n\t"
      "buffer_load_dwordx4 v[88:91], v60, s[36:39], s43 offen offset:1024 sc1\n\t"
      "s_waitcnt vmcnt(2)\n\t"
      "buffer_load_dwordx4 v[92:95], v60, s[36:39], s68 offen sc1\n\t"
      "buffer_load_dwordx4 v[96:99], v60, s[36:39], s68 offen offset:1024 sc1\n\t"
      "s_sub_u32 s63, s63, 1\n\t"
      "s_waitcnt vmcnt(2) lgkmcnt(0)\n\t"
      "v_bitop3_b32 v64, v84, v85, v86 bitop3:0x80\n\t"
      "v_bitop3_b32 v64, v64, v87, v88 bitop3:0x80\n\t"
      "v_bitop3_b32 v64, v64, v89, v90 bitop3:0x80\n\t"
      "v_bitop3_b32 v64, v64, v91, s58 bitop3:0x80\n\t"
      "v_cmp_eq_u32 vcc, 0, v64\n\t"
      "v_pk_fma_f32 v[8:9], v[84:85], s[56:57], v[126:127] op_sel:[0,1,0] op_sel_hi:[1,1,0]\n\t"
      "v_pk_fma_f32 v[10:11], v[86:87], s[56:57], v[126:127] op_sel:[0,1,0] op_sel_hi:[1,1,0]\n\t"
      "v_cvt_pk_bf16_f32 v12, v8, v9\n\t"
      "v_cvt_pk_bf16_f32 v13, v10, v11\n\t"
      "ds_write_b64 v127, v[12:13] offset:44032\n\t"
      "v_pk_fma_f32 v[4:5], v[88:89], s[56:57], v[126:127] op_sel:[0,1,0] op_sel_hi:[1,1,0]\n\t"
      "v_pk_fma_f32 v[6:7], v[90:91], s[56:57], v[126:127] op_sel:[0,1,0] op_sel_hi:[1,1,0]\n\t"
      "v_cvt_pk_bf16_f32 v14, v4, v5\n\t"
      "v_cvt_pk_bf16_f32 v15, v6, v7\n\t"
      "ds_write_b64 v127, v[14:15] offset:44040\n\t"
      "s_memtime s[88:89]\n\t"
      "s_cmp_le_i32 s63, 0\n\t"
      "s_waitcnt lgkmcnt(0)\n\t"
      "s_cbranch_scc1 LRZ_3%=\n\t"
      "s_cbranch_vccnz LR0_3%=\n\t"
      "LRZ_3%=:\n\t"
      "s_sub_u32 s88, s88, 0\n\t"
      "s_branch LC0_3%=\n\t"
      "LR1_3%=:\n\t"
      "v_bitop3_b32 v64, v92, v93, v94 bitop3:0x80\n\t"
      "v_bitop3_b32 v64, v64, v95, v96 bitop3:0x80\n\t"
      "v_bitop3_b32 v64, v64, v97, v98 bitop3:0x80\n\t"
      "v_bitop3_b32 v64, v64, v99, s58 bitop3:0x80\n\t"
      "v_cmp_eq_u32 vcc, 0, v64\n\t"
      "s_nop 0\n\t"
      "s_cbranch_vccz LRX_3%=\n\t"
      "LRY_3%=:\n\t"
      "buffer_load_dwordx4 v[92:95], v60, s[36:39], s68 offen sc1\n\t"
      "buffer_load_dwordx4 v[96:99], v60, s[36:39], s68 offen offset:1024 sc1\n\t"
      "s_sub_u32 s63, s63, 1\n\t"
      "s_waitcnt vmcnt(0) lgkmcnt(0)\n\t"
      "v_bitop3_b32 v64, v92, v93, v94 bitop3:0x80\n\t"
      "v_bitop3_b32 v64, v64, v95, v96 bitop3:0x80\n\t"
      "v_bitop3_b32 v64, v64, v97, v98 bitop3:0x80\n\t"
      "v_bitop3_b32 v64, v64, v99, s58 bitop3:0x80\n\t"
      "v_cmp_eq_u32 vcc, 0, v64\n\t"
      "v_pk_fma_f32 v[24:25], v[92:93], s[56:57], v[126:127] op_sel:[0,1,0] op_sel_hi:[1,1,0]\n\t"
      "v_pk_fma_f32 v[26:27], v[94:95], s[56:57], v[126:127] op_sel:[0,1,0] op_sel_hi:[1,1,0]\n\t"
      "v_cvt_pk_bf16_f32 v28, v24, v25\n\t"
      "v_cvt_pk_bf16_f32 v29, v26, v27\n\t"
      "ds_write_b64 v127, v[28:29] offset:48128\n\t"
      "v_pk_fma_f32 v[20:21], v[96:97], s[56:57], v[126:127] op_sel:[0,1,0] op_sel_hi:[1,1,0]\n\t"
      "v_pk_fma_f32 v[22:23], v[98:99], s[56:57], v[126:127] op_sel:[0,1,0] op_sel_hi:[1,1,0]\n\t"
      "v_cvt_pk_bf16_f32 v30, v20, v21\n\t"
      "v_cvt_pk_bf16_f32 v31, v22, v23\n\t"
      "ds_write_b64 v127, v[30:31] offset:48136\n\t"
      "s_memtime s[88:89]\n\t"
      "s_cmp_le_i32 s63, 0\n\t"
      "s_waitcnt lgkmcnt(0)\n\t"
      "s_cbranch_scc1 LRX_3%=\n\t"
      "s_cbranch_vccnz LRY_3%=\n\t"
      "s_sub_u32 s88, s88, 336\n\t"
      "LRX_3%=:\n\t"
      "s_cmp_eq_u32 s40, 63\n\t"
      "s_cbranch_scc1 LLAST_3%=\n\t"
      "s_branch LC1_3%=\n\t"
      "LMX_3%=:\n\t"
      "s_waitcnt lgkmcnt(0)\n\t"
      "s_nop 7\n\t"
      "v_smfmac_f32_16x16x64_bf16 v[52:55], v[4:7], v[200:207], v122\n\t"
      "v_smfmac_f32_16x16x64_bf16 v[52:55], v[0:3], v[192:199], v122\n\t"
      "v_smfmac_f32_16x16x64_bf16 v[52:55], v[28:31], v[248:255], v122\n\t"
      "v_smfmac_f32_16x16x64_bf16 v[52:55], v[24:27], v[240:247], v122\n\t"
      "v_smfmac_f32_16x16x64_bf16 v[52:55], v[20:23], v[232:239], v122\n\t"
      "v_smfmac_f32_16x16x64_bf16 v[52:55], v[16:19], v[224:231], v122\n\t"
      "s_add_u32 s62, s61, 0x83400000\n\t"
      "v_mov_b32 v126, s62\n\t"
      "s_nop 5\n\t"
      "v_add_f32 v53, v53, v55\n\t"
      "v_add_f32 v52, v52, v54\n\t"
      "v_cvt_pk_bf16_f32 v66, v52, v53\n\t"
      "s_add_u32 s68, s68, 0x8000\n\t"
      "v_max3_f32 v82, v112, v113, v114\n\t"
      "v_max_f32 v82, v82, v115\n\t"
      "s_nop 1\n\t"
      "v_max_f32_dpp v82, v82, v82 quad_perm:[2,3,0,1] row_mask:0xf bank_mask:0xf\n\t"
      "v_max3_f32 v83, v82, v120, v121\n\t"
      "v_max3_f32 v60, v82, v120, v121\n\t"
      "s_nop 1\n\t"
      "v_permlane16_swap_b32 v83, v60\n\t"
      "v_max_f32 v61, v83, v60\n\t"
      "v_max_f32 v60, v83, v60\n\t"
      "s_nop 1\n\t"
      "v_permlane32_swap_b32 v61, v60\n\t"
      "v_max_f32 v83, v61, v60\n\t"
      "v_sub_f32 v112, v112, v83\n\t"
      "v_sub_f32 v113, v113, v83\n\t"
      "v_sub_f32 v114, v114, v83\n\t"
      "v_sub_f32 v115, v115, v83\n\t"
      "v_sub_f32 v120, v120, v83\n\t"
      "v_sub_f32 v121, v121, v83\n\t"
      "v_exp_f32 v56, v112\n\t"
      "v_exp_f32_e64 v57, v113\n\t"
      "v_exp_f32 v58, v114\n\t"
      "v_exp_f32 v59, v115\n\t"
      "v_exp_f32_e64 v80, v120\n\t"
      "v_exp_f32 v81, v121\n\t"
      "v_cvt_pk_bf16_f32 v96, v56, v57\n\t"
      "v_cvt_pk_bf16_f32 v97, v58, v59\n\t"
      "v_cvt_pk_bf16_f32 v108, v80, v81\n\t"
      "v_mov_b32_dpp v98, v96 quad_perm:[2,3,0,1] row_mask:0xf bank_mask:0xf\n\t"
      "v_mov_b32_dpp v99, v97 quad_perm:[2,3,0,1] row_mask:0xf bank_mask:0xf\n\t"
      "v_mfma_f32_16x16x32_bf16 v[48:51], v[108:111], v[88:91], 0\n\t"
      "s_nop 0\n\t"
      "v_mfma_f32_16x16x32_bf16 v[48:51], v[96:99], v[88:91], v[48:51]\n\t"
      "v_mfma_f32_16x16x32_bf16 v[84:87], v[108:111], v[64:67], 0\n\t"
      "v_mfma_f32_16x16x32_bf16 v[84:87], v[96:99], v[68:71], v[84:87]\n\t"
      "v_mfma_f32_16x16x32_bf16 v[92:95], v[96:99], v[72:75], 0\n\t"
      "v_mfma_f32_16x16x32_bf16 v[92:95], v[108:111], v[66:69], v[92:95]\n\t"
      "s_nop 3\n\t"
      "v_rcp_f32_e64 v48, v48\n\t"
      "v_rcp_f32 v49, v49\n\t"
      "v_fma_f32 v84, v84, v48, s96\n\t"
      "buffer_atomic_add v84, v123, s[36:39], s97 offen\n\t"
      "v_fma_f32 v85, v85, v49, s96\n\t"
      "buffer_atomic_add v85, v123, s[36:39], s97 offen offset:512\n\t"
      "v_fma_f32 v94, v94, v48, s96\n\t"
      "buffer_atomic_add v94, v123, s[36:39], s69 offen\n\t"
      "v_fma_f32 v95, v95, v49, s96\n\t"
      "buffer_atomic_add v95, v123, s[36:39], s69 offen offset:512\n\t"
      "s_add_u32 s97, s97, 0x8000\n\t"
      "s_branch LWIN_3%=\n\t"
      "LEARLY_3%=:\n\t"
      "s_cmp_lt_i32 s40, 0\n\t"
      "s_waitcnt lgkmcnt(0)\n\t"
      "s_cbranch_scc1 LUTOK_3%=\n\t"
      "s_xor_b32 s43, s43, s99\n\t"
      "s_xor_b32 s68, s68, s99\n\t"
      "s_mov_b32 s61, 72960\n\t"
      "buffer_load_dword v106, off, s[36:39], s61 sc1\n\t"
      "s_cmp_lg_u32 s40, 0\n\t"
      "s_cbranch_scc1 LROLE_3%=\n\t"
      "v_lshlrev_b32 v96, 4, v127\n\t"
      "buffer_load_dwordx2 v[92:93], v96, s[36:39], s86 offen sc1\n\t"
      "buffer_load_dwordx2 v[94:95], v96, s[36:39], s86 offen offset:128 sc1\n\t"
      "s_branch LROLE_3%=\n\t"
      "LSLOW_3%=:\n\t"
      "s_mov_b32 s63, 0x10000\n\t"
      "LSLOW1_3%=:\n\t"
      "buffer_load_dwordx2 v[92:93], off, s[36:39], s64 sc1\n\t"
      "s_waitcnt vmcnt(0)\n\t"
      "v_bitop3_b32 v92, v92, v93, s53 bitop3:0x7e\n\t"
      "s_nop 0\n\t"
      "v_readfirstlane_b32 s56, v92\n\t"
      "s_nop 1\n\t"
      "s_cmp_eq_u32 s56, 0\n\t"
      "s_cbranch_scc1 LSLOW2_3%=\n\t"
      "s_sleep 8\n\t"
      "s_sub_u32 s63, s63, 1\n\t"
      "s_cmp_lg_u32 s63, 0\n\t"
      "s_cbranch_scc1 LSLOW1_3%=\n\t"
      "LSLOW2_3%=:\n\t"
      "s_mov_b32 m0, s45\n\t"
      "s_sub_u32 s61, 204800, s71\n\t"
      "buffer_load_dwordx4 v65, s[36:39], s44 offen sc1 lds\n\t"
      "buffer_load_dwordx4 v65, s[36:39], s44 offen offset:1024 sc1 lds\n\t"
      "s_add_u32 m0, s55, s61\n\t"
      "s_nop 0\n\t"
      "buffer_load_dwordx4 v127, s[36:39], s46 offen sc1 lds\n\t"
      "buffer_load_dwordx4 v127, s[36:39], s46 offen offset:1024 sc1 lds\n\t"
      "s_branch LWIN1_3%=\n\t"
      "LLAST_3%=:\n\t"
      "s_waitcnt lgkmcnt(0)\n\t"
      "s_barrier\n\t"
      "ds_read_b128 v[24:27], v127 offset:47104\n\t"
      "ds_read_b128 v[20:23], v127 offset:46080\n\t"
      "ds_read_b128 v[16:19], v127 offset:45056\n\t"
      "v_smfmac_f32_16x16x64_bf16 v[40:43], v[0:3], a[128:135], v122\n\t"
      "v_smfmac_f32_16x16x64_bf16 v[44:47], v[0:3], a[192:199], v122\n\t"
      "v_smfmac_f32_16x16x64_bf16 v[44:47], v[28:31], a[248:255], v122\n\t"
      "v_smfmac_f32_16x16x64_bf16 v[40:43], v[28:31], a[184:191], v122\n\t"
      "s_waitcnt lgkmcnt(2)\n\t"
      "v_smfmac_f32_16x16x64_bf16 v[40:43], v[24:27], a[176:183], v122\n\t"
      "v_smfmac_f32_16x16x64_bf16 v[44:47], v[24:27], a[240:247], v122\n\t"
      "s_waitcnt lgkmcnt(1)\n\t"
      "v_smfmac_f32_16x16x64_bf16 v[40:43], v[20:23], a[168:175], v122\n\t"
      "v_smfmac_f32_16x16x64_bf16 v[44:47], v[20:23], a[232:239], v122\n\t"
      "s_waitcnt lgkmcnt(0)\n\t"
      "v_smfmac_f32_16x16x64_bf16 v[40:43], v[16:19], a[160:167], v122\n\t"
      "v_smfmac_f32_16x16x64_bf16 v[44:47], v[16:19], a[224:231], v122\n\t"
      "s_nop 3\n\t"
      "v_add_f32 v40, v40, v42\n\t"
      "v_add_f32 v41, v41, v43\n\t"
      "v_cvt_pk_bf16_f32 v120, v40, v41\n\t"
      "s_nop 0\n\t"
      "v_add_f32 v44, v44, v46\n\t"
      "v_add_f32 v45, v45, v47\n\t"
      "v_cvt_pk_bf16_f32 v121, v44, v45\n\t"
      "ds_write_b64 v125, v[120:121] offset:2080\n\t"
      "s_waitcnt lgkmcnt(0)\n\t"
      "s_barrier\n\t"
      "ds_read_b64_tr_b16 v[40:41], v124 offset:51232\n\t"
      "ds_read_b64_tr_b16 v[42:43], v124 offset:51752\n\t"
      "ds_read_b64_tr_b16 v[44:45], v124 offset:52272\n\t"
      "ds_read_b64_tr_b16 v[46:47], v124 offset:52792\n\t"
      "v_add_u32 v100, s55, v127\n\t"
      "ds_read_b128 v[60:63], v100 offset:57344\n\t"
      "s_waitcnt lgkmcnt(0)\n\t"
      "s_branch LROLE_3%=\n\t"
      "LEND%=:\n\t"
      "s_waitcnt vmcnt(1) lgkmcnt(0)\n\t"
      "s_cmp_lg_u32 s42, 5\n\t"
      "s_cbranch_scc1 LOUT%=\n\t"
      "s_waitcnt vmcnt(0)\n\t"
      "LOUT%=:\n\t"
      : "+{v[116:123]}"(c0), "+{v[124:127]}"(c1), "+{v[0:15]}"(sc), "+{v[16:31]}"(s2), "+{v[100:107]}"(c3), "+{v[108:115]}"(cc), "+s"(A), "+s"(b), "+s"(wv), "+{s87}"(tch)
      :
      : "memory", "vcc", "scc", "v32", "v33", "v34", "v35", "v36", "v37", "v38", "v39", "v40", "v41", "v42", "v43", "v44", "v45", "v46", "v47", "v48", "v49", "v50", "v51", "v52", "v53", "v54", "v55", "v56", "v57", "v58", "v59", "v60", "v61", "v62", "v63", "v64", "v65", "v66", "v67", "v68", "v69", "v70", "v71", "v72", "v73", "v74", "v75", "v76", "v77", "v78", "v79", "v80", "v81", "v82", "v83", "v84", "v85", "v86", "v87", "v88", "v89", "v90", "v91", "v92", "v93", "v94", "v95", "v96", "v97", "v98", "v99", "v128", "v129", "v130", "v131", "v132", "v133", "v134", "v135", "v136", "v137", "v138", "v139", "v140", "v141", "v142", "v143", "v144", "v145", "v146", "v147", "v148", "v149", "v150", "v151", "v152", "v153", "v154", "v155", "v156", "v157", "v158", "v159", "v160", "v161", "v162", "v163", "v164", "v165", "v166", "v167", "v168", "v169", "v170", "v171", "v172", "v173", "v174", "v175", "v176", "v177", "v178", "v179", "v180", "v181", "v182", "v183", "v184", "v185", "v186", "v187", "v188", "v189", "v190", "v191", "v192", "v193", "v194", "v195", "v196", "v197", "v198", "v199", "v200", "v201", "v202", "v203", "v204", "v205", "v206", "v207", "v208", "v209", "v210", "v211", "v212", "v213", "v214", "v215", "v216", "v217", "v218", "v219", "v220", "v221", "v222", "v223", "v224", "v225", "v226", "v227", "v228", "v229", "v230", "v231", "v232", "v233", "v234", "v235", "v236", "v237", "v238", "v239", "v240", "v241", "v242", "v243", "v244", "v245", "v246", "v247", "v248", "v249", "v250", "v251", "v252", "v253", "v254", "v255", "a0", "a1", "a2", "a3", "a4", "a5", "a6", "a7", "a8", "a9", "a10", "a11", "a12", "a13", "a14", "a15", "a16", "a17", "a18", "a19", "a20", "a21", "a22", "a23", "a24", "a25", "a26", "a27", "a28", "a29", "a30", "a31", "a32", "a33", "a34", "a35", "a36", "a37", "a38", "a39", "a40", "a41", "a42", "a43", "a44", "a45", "a46", "a47", "a48", "a49", "a50", "a51", "a52", "a53", "a54", "a55", "a56", "a57", "a58", "a59", "a60", "a61", "a62", "a63", "a64", "a65", "a66", "a67", "a68", "a69", "a70", "a71", "a72", "a73", "a74", "a75", "a76", "a77", "a78", "a79", "a80", "a81", "a82", "a83", "a84", "a85", "a86", "a87", "a88", "a89", "a90", "a91", "a92", "a93", "a94", "a95", "a96", "a97", "a98", "a99", "a100", "a101", "a102", "a103", "a104", "a105", "a106", "a107", "a108", "a109", "a110", "a111", "a112", "a113", "a114", "a115", "a116", "a117", "a118", "a119", "a120", "a121", "a122", "a123", "a124", "a125", "a126", "a127", "a128", "a129", "a130", "a131", "a132", "a133", "a134", "a135", "a136", "a137", "a138", "a139", "a140", "a141", "a142", "a143", "a144", "a145", "a146", "a147", "a148", "a149", "a150", "a151", "a152", "a153", "a154", "a155", "a156", "a157", "a158", "a159", "a160", "a161", "a162", "a163", "a164", "a165", "a166", "a167", "a168", "a169", "a170", "a171", "a172", "a173", "a174", "a175", "a176", "a177", "a178", "a179", "a180", "a181", "a182", "a183", "a184", "a185", "a186", "a187", "a188", "a189", "a190", "a191", "a192", "a193", "a194", "a195", "a196", "a197", "a198", "a199", "a200", "a201", "a202", "a203", "a204", "a205", "a206", "a207", "a208", "a209", "a210", "a211", "a212", "a213", "a214", "a215", "a216", "a217", "a218", "a219", "a220", "a221", "a222", "a223", "a224", "a225", "a226", "a227", "a228", "a229", "a230", "a231", "a232", "a233", "a234", "a235", "a236", "a237", "a238", "a239", "a240", "a241", "a242", "a243", "a244", "a245", "a246", "a247", "a248", "a249", "a250", "a251", "a252", "a253", "a254", "a255", "s24", "s25", "s26", "s27", "s28", "s29", "s30", "s31", "s36", "s37", "s38", "s39", "s40", "s41", "s42", "s43", "s44", "s45", "s46", "s47", "s48", "s49", "s50", "s51", "s52", "s53", "s54", "s55", "s56", "s57", "s58", "s59", "s60", "s61", "s62", "s63", "s64", "s65", "s66", "s67", "s68", "s69", "s70", "s71", "s72", "s73", "s74", "s75", "s76", "s77", "s78", "s79", "s80", "s81", "s82", "s83", "s84", "s85", "s86", "s88", "s89", "s90", "s91", "s92", "s93", "s94", "s95", "s96", "s97", "s98", "s99");
}

#define SSLEEP 1
#define SNEED 0
#define TSLEEP 4
__device__ __forceinline__ unsigned mkkey(unsigned w, int idx) {
  unsigned u = w >> 16;
  u = (u & 0x7fffu) == 0 ? 0u : u;
  unsigned o = (u & 0x8000u) ? (~u & 0xffffu) : (u | 0x8000u);
  o = (u & 0x7fffu) > 0x7f80u ? 0u : o;
  return (o << 16) | (0xffffu - (unsigned)idx);
}
template <bool spread>
__device__ __forceinline__ u32x2 top2(const float* rbuf, int tid, int need, unsigned* sk) {
  unsigned key[9];
  const __amdgpu_buffer_rsrc_t rrs = __builtin_amdgcn_make_buffer_rsrc((void*)rbuf, 0, spread ? 2 * S_ * 4 + 256 * 128 : S_ * 4, 0x00020000);
  const int t8 = spread && tid >= 224 ? 0 : tid * 8;
#pragma unroll 1
  for (unsigned spins = 0; spins < (1u << 20); ++spins) {
    asm volatile("" ::: "memory");
    const u32x4 a = ld_sc1(rrs, t8 * 4), b = ld_sc1(rrs, t8 * 4 + 16);
    key[8] = spread ? __builtin_amdgcn_raw_buffer_load_b32(rrs, 2 * S_ * 4 + tid * 128, 0, 16) : a[0];
    unsigned mx = key[8];
#pragma unroll
    for (int k = 0; k < 4; ++k) { key[k] = a[k]; key[4 + k] = b[k]; mx = max(mx, max(a[k], b[k])); }
    if (mx != ~0u || tid * 8 >= need) break;
    __builtin_amdgcn_s_sleep(TSLEEP);
  }
  unsigned m = 0;
#pragma unroll
  for (int k = 0; k < 9; ++k) {
    key[k] = mkkey(key[k], k < 8 ? t8 + k : (spread ? S_ - 256 + tid : t8));
    m = max(m, key[k]);
  }
  m = wave_max_u32(m);
  __syncthreads();
  if ((tid & 63) == 0) sk[tid >> 6] = m;
  __syncthreads();
  const unsigned k0 = max(max(sk[0], sk[1]), max(sk[2], sk[3]));
  m = 0;
#pragma unroll
  for (int k = 0; k < 9; ++k) m = max(m, key[k] == k0 ? 0u : key[k]);
  m = wave_max_u32(m);
  if ((tid & 63) == 0) sk[4 + (tid >> 6)] = m;
  __syncthreads();
  const unsigned k1 = max(max(sk[4], sk[5]), max(sk[6], sk[7]));
  return (u32x2){(unsigned)__builtin_amdgcn_readfirstlane(k0), (unsigned)__builtin_amdgcn_readfirstlane(k1)};
}
__device__ __forceinline__ u32x2 top2_wave(const float* rbuf, int lane, bool dry, bool strict) {
  const __amdgpu_buffer_rsrc_t rrs = __builtin_amdgcn_make_buffer_rsrc((void*)rbuf, 0, S_ * 4, 0x00020000);
  asm volatile("" ::: "memory");
  u32x4 a[8];
#pragma unroll
  for (int i = 0; i < 8; ++i) a[i] = (u32x4){0u, 0u, 0u, 0u};
  if (!dry) {
#pragma unroll
    for (int i = 0; i < 8; ++i) a[i] = ld_sc1(rrs, (i * 64 + lane) * 16);
  }
  if (SNEED) {
    unsigned mx = 0;
#pragma unroll
    for (int i = 0; i < 8; ++i) {
      const unsigned x = max(max(a[i][0], a[i][1]), max(a[i][2], a[i][3]));
      mx = max(mx, i * 8 + (lane >> 3) < SNEED ? x : 0u);
    }
    if (strict && __builtin_amdgcn_ballot_w64(mx == ~0u)) return (u32x2){0u, 0u};
  }
  unsigned m1 = 0, m2 = 0;
#pragma unroll
  for (int i = 0; i < 32; ++i) {
    const unsigned w = a[i >> 2][i & 3], o = w ^ ((unsigned)((int)w >> 31) | 0x80000000u);
    const unsigned k = (o & 0xffff0000u) | ((0xffffu - (unsigned)(((i >> 2) * 64 + lane) * 4 + (i & 3))) & ~w);
    m2 = max(min(m1, k), m2);
    m1 = max(m1, k);
  }
  unsigned k[2];
  k[0] = wave_max_u32(m1);
  k[1] = wave_max_u32(m1 == k[0] ? m2 : m1);
  if (!k[1]) return (u32x2){0u, 0u};
#pragma unroll
  for (int i = 0; i < 2; ++i) k[i] = mkkey((k[i] >> 31) ? k[i] & 0x7fffffffu : ~k[i], 0xffff - (k[i] & 0xffff));
  return (u32x2){max(k[0], k[1]), min(k[0], k[1])};
}
__device__ __forceinline__ bool other_top2a(const float* rbuf, int tid, unsigned k0, unsigned k1) {
  const __amdgpu_buffer_rsrc_t rrs = __builtin_amdgcn_make_buffer_rsrc((void*)rbuf, 0, S_ * 4, 0x00020000);
  const int t8 = tid >= 224 ? 0 : tid * 8;
  bool bad = false;
#pragma unroll 1
  for (unsigned spins = 0; spins < (1u << 20); ++spins) {
    asm volatile("" ::: "memory");
    const u32x4 a = ld_sc1(rrs, t8 * 4), b = ld_sc1(rrs, t8 * 4 + 16);
    unsigned mx = 0;
    bad = false;
#pragma unroll
    for (int k = 0; k < 4; ++k) {
      const unsigned ka = mkkey(a[k], t8 + k), kb = mkkey(b[k], t8 + 4 + k);
      bad |= (ka > k1 && ka != k0) || (kb > k1 && kb != k0);
      mx = max(mx, max(a[k], b[k]));
    }
    if (__builtin_amdgcn_ballot_w64(mx == ~0u) == 0) break;
    __builtin_amdgcn_s_sleep(TSLEEP);
  }
  return __builtin_amdgcn_ballot_w64(bad) != 0;
}
__device__ __forceinline__ bool other_top2b(const float* rbuf, int tid, unsigned k0, unsigned k1, bool bad, unsigned* sk, unsigned& tmp) {
  const __amdgpu_buffer_rsrc_t rrs = __builtin_amdgcn_make_buffer_rsrc((void*)rbuf, 0, 2 * S_ * 4 + 256 * 128, 0x00020000);
  unsigned w, n;
  asm volatile("buffer_load_dword %1, %3, %4, 0 offen sc1\n\ts_sleep %5\n\t"
               "buffer_load_dword %1, %3, %4, 0 offen sc1\n\ts_sleep %5\n\t"
               "s_mov_b32 %2, 0x100000\n"
               "TP%=:\n\t"
               "buffer_load_dword %1, %3, %4, 0 offen sc1\n\t"
               "s_sub_u32 %2, %2, 1\n\t"
               "s_waitcnt vmcnt(2)\n\t"
               "v_mov_b32 %0, %1\n\t"
               "v_cmp_eq_u32 vcc, -1, %0\n\t"
               "s_cbranch_scc1 TQ%=\n\t"
               "s_cbranch_vccnz TP%=\n"
               "TQ%=:"
               : "=&v"(w), "=&v"(tmp), "=&s"(n) : "v"(2 * S_ * 4 + tid * 128), "s"(rrs), "n"(PSLEEP) : "memory", "vcc", "scc");
  const unsigned k = mkkey(w, S_ - 256 + tid);
  bad |= k > k1 && k != k0;
  const unsigned any = __builtin_amdgcn_ballot_w64(bad) != 0;
  if ((tid & 63) == 0) sk[tid >> 6] = any;
  LDS_BARRIER();
  const u32x4 v = *(const u32x4*)sk;
  return __builtin_amdgcn_readfirstlane(v[0] | v[1] | v[2] | v[3]) != 0;
}

constexpr int M1_WGS = FF / 16;
constexpr int L_WD = 131072;
__device__ __forceinline__ void wait_set(gu32* f) {
#pragma unroll 1
  for (unsigned spins = 0; spins < (1u << 18); ++spins) {
    if (__hip_atomic_load(f, RLX_AGENT) != ~0u) return;
    __builtin_amdgcn_s_sleep(4);
  }
}
__device__ __forceinline__ u32x2 wait_sel(gu32* f, u64 v = 0) {
#pragma unroll 1
  for (unsigned spins = 0; spins < (1u << 18) && !v; ++spins) {
    v = __hip_atomic_load((gu64*)f, RLX_AGENT);
    if (v) break;
    __builtin_amdgcn_s_sleep(FSLEEP);
  }
  return (u32x2){(unsigned)__builtin_amdgcn_readfirstlane((unsigned)v), (unsigned)__builtin_amdgcn_readfirstlane((unsigned)(v >> 32))};
}
__device__ __forceinline__ bf16x8 as_frag(u32x4 v) { return __builtin_bit_cast(bf16x8, v); }
__device__ __forceinline__ u32x4 tr_frag(char* p0, char* p1) {
  const s16x4 a = __builtin_amdgcn_ds_read_tr16_b64_v4i16((LAS s16x4*)p0), b = __builtin_amdgcn_ds_read_tr16_b64_v4i16((LAS s16x4*)p1);
  const u32x2 x = __builtin_bit_cast(u32x2, a), y = __builtin_bit_cast(u32x2, b);
  return (u32x4){x[0], x[1], y[0], y[1]};
}
__device__ __forceinline__ void mlp1_role(KArgs P, char* lds, int b, int tid) {
  float* part = (float*)lds;
  unsigned* si = (unsigned*)(lds + 4096);
  const int lane = tid & 63, wv = tid >> 6, l15 = lane & 15, l4 = lane >> 4;
  const __amdgpu_buffer_rsrc_t lrs = __builtin_amdgcn_make_buffer_rsrc((void*)(P->ws + O_LNH), 0, S_ * DM * 2, 0x00020000);
  const __amdgpu_buffer_rsrc_t mrs = __builtin_amdgcn_make_buffer_rsrc((void*)(P->ws + O_MFRAG), 0, 2 * DM * FF * 2, 0x00020000);
  u32x4 aw[2][4];
  MS(0);
  const u64 s0 = __hip_atomic_load((gu64*)(P->ws + O_CNT + (b & 7) * 64), RLX_AGENT);
#pragma unroll
  for (int i = 0; i < 8; ++i) aw[i >> 2][i & 3] = ld_sc1(mrs, (unsigned)(b * 32768 + (i >> 2) * 16384 + (wv * 4 + (i & 3)) * 1024 + lane * 16));
  u32x2 sel = wait_sel((gu32*)(P->ws + O_CNT) + (b & 7) * 16, s0);
  MS(1);
  int round = 0;
#pragma unroll 1
  for (;;) {
    const int idx = 0xffff - (((l15 & 1) ? sel[1] : sel[0]) & 0xffff);
    if (round == 2) wait_set((gu32*)(P->ws + O_RBUF) + S_ + idx);
    u32x4 bx[4];
#pragma unroll
    for (int i = 0; i < 4; ++i) bx[i] = ld_sc1(lrs, (unsigned)(idx * 1024 + (wv * 4 + i) * 64 + l4 * 16));
    f32x4 ag = {0.f, 0.f, 0.f, 0.f}, au = {0.f, 0.f, 0.f, 0.f};
#pragma unroll
    for (int i = 0; i < 4; ++i) {
      ag = __builtin_amdgcn_mfma_f32_16x16x32_bf16(as_frag(aw[0][i]), as_frag(bx[i]), ag, 0, 0, 0);
      au = __builtin_amdgcn_mfma_f32_16x16x32_bf16(as_frag(aw[1][i]), as_frag(bx[i]), au, 0, 0, 0);
    }
    if (l15 < 2) {
      *(f32x4*)(part + wv * 64 + l15 * 16 + l4 * 4) = ag;
      *(f32x4*)(part + wv * 64 + 32 + l15 * 16 + l4 * 4) = au;
    }
    LDS_BARRIER();
    MS(2);
    if (tid < 16) {
      float a[2];
#pragma unroll
      for (int e = 0; e < 2; ++e) {
        const float* q = part + tid * 2 + e;
        const float g = rbf((q[0] + q[64]) + (q[128] + q[192])), u = rbf((q[32] + q[96]) + (q[160] + q[224]));
        a[e] = rbf(g / (1.0f + __expf(-g))) * u;
      }
      unsigned av = pk2(a[0], a[1]);
      av = av == ~0u ? 0x7fc07fc0u : av;
      __hip_atomic_store((gu32*)(P->ws + O_ACT) + (round * M1_WGS + b) * 32 + tid, av, RLX_AGENT);
    }
    MS(4);
    if (round == 2) break;
    if (SW2 && !round) {
      const u32x2 s2 = wait_sel((gu32*)(P->ws + O_SEL2) + (b & 31) * 32);
      if (s2[0] != sel[0] || s2[1] != sel[1]) { sel = s2; round = 1; __syncthreads(); continue; }
    }
    unsigned tmp;
    const bool again = other_top2b((const float*)(P->ws + O_RBUF), tid, sel[0], sel[1], other_top2a((const float*)(P->ws + O_RBUF), tid, sel[0], sel[1]), si, tmp);
    asm volatile("s_waitcnt vmcnt(0)" :: "v"(tmp) : "memory");
    if (!again) break;
    sel = top2<true>((const float*)(P->ws + O_RBUF), tid, S_, si + 4);
    round = 2;
  }
}
__device__ __forceinline__ void mlp2_role(KArgs P, char* lds, int b, int tid) {
  float* part = (float*)lds;
  unsigned* si = (unsigned*)(lds + 4096);
  const int lane = tid & 63, wv = tid >> 6, l15 = lane & 15, l4 = lane >> 4;
  const __amdgpu_buffer_rsrc_t hrs = __builtin_amdgcn_make_buffer_rsrc((void*)(P->ws + O_HBUF), 0, S_ * DM * 2, 0x00020000);
  const __amdgpu_buffer_rsrc_t lrs = __builtin_amdgcn_make_buffer_rsrc((void*)(P->ws + O_LNH), 0, S_ * DM * 2, 0x00020000);
  const __amdgpu_buffer_rsrc_t ors = __builtin_amdgcn_make_buffer_rsrc((void*)P->out, 0, S_ * DM * 2, 0x00020000);
  const __amdgpu_buffer_rsrc_t ars = __builtin_amdgcn_make_buffer_rsrc((void*)(P->ws + O_ACT), 0, 3 * M1_WGS * 128, 0x00020000);
  u32x4 aw[7];
  const int c0 = b * 8;
  MS(0);
  {
    char* p = lds + L_WD + (((l15 >> 1) & 1) * 896 + wv * 224 + l4 * 8 + (l15 >> 2)) * 16 + (l15 & 1) * 8;
#pragma unroll
    for (int i = 0; i < 7; ++i) aw[i] = tr_frag(p + i * 512, p + i * 512 + 64);
  }
  u32x2 sel = wait_sel((gu32*)(P->ws + O_CNT) + (b & 7) * 16);
  MS(1);
  int round = 0;
#pragma unroll 1
  for (;;) {
    const int idx = 0xffff - (((tid & 8) ? sel[1] : sel[0]) & 0xffff), col = c0 + (tid & 7);
    if (round == 2) wait_set((gu32*)(P->ws + O_RBUF) + S_ + idx);
    const float rr = __hip_atomic_load((const GAS float*)(P->ws + O_RBUF) + S_ + idx, RLX_AGENT);
    const float lv = bf2f(__builtin_amdgcn_raw_buffer_load_b16(lrs, (unsigned)(idx * DM + col) * 2, 0, 16));
    const float hv = bf2f(__builtin_amdgcn_raw_buffer_load_b16(hrs, (unsigned)(idx * DM + col) * 2, 0, 16));
    const float sg = rbf(1.0f / (1.0f + __expf(-rr)));
    const bool bad = other_top2a((const float*)(P->ws + O_RBUF), tid, sel[0], sel[1]);
    if (!round && (ASALL || c0 < 256)) __builtin_amdgcn_s_sleep(ASLEEP);
    u32x4 av[7];
    u64 v2 = 0;
#pragma unroll 1
    for (unsigned spins = 0; spins < (1u << 20); ++spins) {
      asm volatile("" ::: "memory");
      unsigned mx = 0;
      if (SW2) v2 = __hip_atomic_load((gu64*)(P->ws + O_SEL2 + (b & 31) * 128), RLX_AGENT);
#pragma unroll
      for (int i = 0; i < 7; ++i) {
        av[i] = ld_sc1(ars, (unsigned)((round * M1_WGS + ((l15 >> 1) & 1) * 56 + (wv * 7 + i) * 2 + (l4 >> 1)) * 128 + (l15 & 1) * 32 + (l4 & 1) * 16));
        mx = max(max(mx, max(av[i][0], av[i][1])), max(av[i][2], av[i][3]));
      }
      if (__builtin_amdgcn_ballot_w64(mx == ~0u) == 0) break;
      __builtin_amdgcn_s_sleep(4);
    }
    MS(2);
    if (SW2 && !round) {
      const u32x2 s2 = wait_sel((gu32*)(P->ws + O_SEL2) + (b & 31) * 32, v2);
      if (s2[0] != sel[0] || s2[1] != sel[1]) { sel = s2; round = 1; continue; }
    }
    f32x4 a0 = {0.f, 0.f, 0.f, 0.f}, a1 = {0.f, 0.f, 0.f, 0.f};
#pragma unroll
    for (int i = 0; i < 7; ++i) {
      if (i & 1) a1 = __builtin_amdgcn_mfma_f32_16x16x32_bf16(as_frag(aw[i]), as_frag(av[i]), a1, 0, 0, 0);
      else a0 = __builtin_amdgcn_mfma_f32_16x16x32_bf16(as_frag(aw[i]), as_frag(av[i]), a0, 0, 0, 0);
    }
    if ((l15 >> 1) == (l4 >> 1)) *(f32x4*)(part + wv * 32 + l15 * 8 + (l4 & 1) * 4) = a0 + a1;
    LDS_BARRIER();
    const float* q = part + (tid & 15);
    const float y = rbf(((q[0] + q[16]) + (q[32] + q[48])) + ((q[64] + q[80]) + (q[96] + q[112])));
    const float upd = rbf(y * sg);
    const float bb = rbf(lv + upd);
    const u16 res = f2bf(hv + bb);
    MS(4);
    if (tid < 16) __builtin_amdgcn_raw_buffer_store_b16(res, ors, (unsigned)(idx * DM + col) * 2, 0, 16);
    if (round == 2) break;
    unsigned tmp;
    const bool again = other_top2b((const float*)(P->ws + O_RBUF), tid, sel[0], sel[1], bad, si, tmp);
    asm volatile("s_waitcnt vmcnt(0)" :: "v"(tmp) : "memory");
    if (!again) break;
    if (tid < 16) __builtin_amdgcn_raw_buffer_store_b16(f2bf(hv + lv), ors, (unsigned)(idx * DM + col) * 2, 0, 16);
    VM_DRAIN();
    sel = top2<true>((const float*)(P->ws + O_RBUF), tid, S_, si + 4);
    round = 2;
  }
}

struct WoRegs { u32x32 a, b, c, d; };
__device__ __forceinline__ unsigned worker_role(int& w, int& wv, KArgs& A, WoRegs& wo) {
  unsigned tag;
  asm volatile(
      "GSTART:\n\t"
      "v_mbcnt_lo_u32_b32 v96, -1, 0\n\t"
      "v_mbcnt_hi_u32_b32 v96, -1, v96\n\t"
      "s_and_b32 s82, %0, 31\n\t"
      "s_mov_b32 s66, 4194304\n\t"
      "s_mov_b32 s67, 0x20000\n\t"
      "s_mov_b32 s62, 111181824\n\t"
      "s_mov_b32 s63, 0x20000\n\t"
      "v_and_b32 v104, 3, v96\n\t"
      "v_lshrrev_b32 v105, 2, v96\n\t"
      "s_lshl_b32 s78, %0, 14\n\t"
      "s_lshl_b32 s79, %1, 12\n\t"
      "s_add_u32 s79, s79, s78\n\t"
      "v_lshlrev_b32 v106, 10, v104\n\t"
      "v_lshl_add_u32 v106, v105, 4, v106\n\t"
      "v_lshlrev_b32 v99, 13, v105\n\t"
      "v_lshl_add_u32 v99, v104, 4, v99\n\t"
      "s_lshl_b32 s80, s82, 8\n\t"
      "s_lshl_b32 s72, %1, 6\n\t"
      "s_add_u32 s80, s80, s72\n\t"
      "v_add_u32 v99, s80, v99\n\t"
      "s_bitcmp1_b32 %0, 5\n\t"
      "s_waitcnt lgkmcnt(0)\n\t"
      "s_mov_b64 s[64:65], s[36:37]\n\t"
      "s_mov_b64 s[60:61], s[58:59]\n\t"
      "buffer_load_dwordx4 v[64:67], v106, s[64:67], s79 offen \n\t"
      "buffer_load_dwordx4 v[68:71], v106, s[64:67], s79 offen offset:256 \n\t"
      "buffer_load_dwordx4 v[72:75], v106, s[64:67], s79 offen offset:512 \n\t"
      "buffer_load_dwordx4 v[76:79], v106, s[64:67], s79 offen offset:768 \n\t"
      "s_cselect_b64 s[38:39], s[44:45], s[42:43]\n\t"
      "s_mov_b32 s78, 0\n\t"
      "s_load_dword s87, s[38:39], s78\n\t"
      "s_mov_b32 s78, 4194300\n\t"
      "s_load_dword s87, s[38:39], s78\n\t"
      "s_mov_b32 s78, 128\n\t"
      "s_load_dword s87, s[60:61], s78\n\t"
      "s_mov_b32 s78, 6520704\n\t"
      "s_load_dword s87, s[60:61], s78\n\t"
      "v_mov_b32 v118, v106\n\t"
      "s_mov_b64 s[64:65], s[36:37]\n\t"
      "s_lshr_b32 s78, %0, 6\n\t"
      "s_lshl_b32 s78, s78, 15\n\t"
      "s_lshl_b32 s79, %1, 12\n\t"
      "s_add_u32 s79, s79, s78\n\t"
      "buffer_load_dwordx4 v[128:131], v118, s[64:67], s79 offen \n\t"
      "buffer_load_dwordx4 v[132:135], v118, s[64:67], s79 offen offset:256 \n\t"
      "buffer_load_dwordx4 v[136:139], v118, s[64:67], s79 offen offset:512 \n\t"
      "buffer_load_dwordx4 v[140:143], v118, s[64:67], s79 offen offset:768 \n\t"
      "s_add_u32 s79, s79, 16384\n\t"
      "buffer_load_dwordx4 v[144:147], v118, s[64:67], s79 offen \n\t"
      "buffer_load_dwordx4 v[148:151], v118, s[64:67], s79 offen offset:256 \n\t"
      "buffer_load_dwordx4 v[152:155], v118, s[64:67], s79 offen offset:512 \n\t"
      "buffer_load_dwordx4 v[156:159], v118, s[64:67], s79 offen offset:768 \n\t"
      "s_mov_b64 s[64:65], s[38:39]\n\t"
      "s_mov_b64 s[64:65], s[38:39]\n\t"
      "buffer_load_dword v107, off, s[60:63], 0 sc1\n\t"
      "v_lshrrev_b32 v101, 4, v96\n\t"
      "v_and_b32 v102, 15, v96\n\t"
      "v_lshlrev_b32 v103, 2, v101\n\t"
      "v_xor_b32 v102, v102, v103\n\t"
      "v_lshlrev_b32 v108, 13, v101\n\t"
      "v_lshl_add_u32 v108, v102, 4, v108\n\t"
      "s_lshl_b32 s78, s82, 8\n\t"
      "s_lshl_b32 s79, %1, 16\n\t"
      "s_add_u32 s78, s78, s79\n\t"
      "v_add_u32 v108, s78, v108\n\t"
      "s_lshl_b32 s76, %1, 11\n\t"
      "s_mov_b32 s77, 0\n\t"
      "s_add_u32 m0, s76, 0\n\t"
      "s_add_u32 s68, s77, 0\n\t"
      "buffer_load_dwordx4 v108, s[64:67], s68 offen lds\n\t"
      "s_add_u32 m0, s76, 1024\n\t"
      "s_add_u32 s68, s77, 32768\n\t"
      "s_add_u32 s76, s76, 8192\n\t"
      "s_add_u32 s77, s77, 0x40000\n\t"
      "buffer_load_dwordx4 v108, s[64:67], s68 offen lds\n\t"
      "s_mov_b32 s84, 0xffff0000\n\t"
      "s_mov_b32 s86, 0\n\t"
      "s_lshl_b32 s85, %1, 13\n\t"
      "v_lshlrev_b32 v97, 4, v96\n\t"
      "v_lshrrev_b32 v101, 4, v96\n\t"
      "v_and_b32 v102, 3, v105\n\t"
      "v_lshlrev_b32 v100, 9, v101\n\t"
      "v_lshl_add_u32 v100, v102, 6, v100\n\t"
      "v_lshl_add_u32 v100, v104, 4, v100\n\t"
      "v_lshlrev_b32 v106, 10, v101\n\t"
      "v_lshl_add_u32 v106, v102, 8, v106\n\t"
      "v_lshl_add_u32 v106, v104, 4, v106\n\t"
      "s_lshl_b32 s74, %0, 14\n\t"
      "s_add_u32 s74, s74, s72\n\t"
      "s_add_u32 s74, s74, 6520832\n\t"
      "s_lshr_b32 s78, %0, 6\n\t"
      "v_min_u32 v104, 31, v96\n\t"
      "v_lshl_add_u32 v104, v104, 1, s78\n\t"
      "s_lshr_b32 s79, %0, 5\n\t"
      "s_and_b32 s83, s79, 1\n\t"
      "s_add_u32 s80, s85, 6520832\n\t"
      "v_lshl_add_u32 v101, v104, 15, s80\n\t"
      "s_mul_i32 s80, s82, 24576\n\t"
      "s_lshl_b32 s72, %1, 11\n\t"
      "s_add_u32 s80, s80, s72\n\t"
      "s_add_u32 s80, s80, 8617984\n\t"
      "s_lshl_b32 s72, s83, 13\n\t"
      "s_add_u32 s80, s80, s72\n\t"
      "v_mov_b32 v102, s80\n\t"
      "s_mov_b32 s72, 786432\n\t"
      "v_mad_u32_u24 v102, v104, s72, v102\n\t"
      "s_lshl_b32 s80, s82, 6\n\t"
      "s_lshl_b32 s72, s83, 2\n\t"
      "s_add_u32 s80, s80, s72\n\t"
      "s_add_u32 s80, s80, 256\n\t"
      "v_lshrrev_b32 v105, 2, v104\n\t"
      "v_lshl_add_u32 v103, v105, 11, s80\n\t"
      "v_and_b32 v104, 3, v104\n\t"
      "v_lshl_add_u32 v103, v104, 4, v103\n\t"
      "s_lshl_b32 s80, s82, 7\n\t"
      "s_lshl_b32 s79, s79, 2\n\t"
      "s_add_u32 s80, s80, s79\n\t"
      "s_add_u32 s80, s80, 33024\n\t"
      "v_writelane_b32 v103, s80, 0\n\t"
      "v_writelane_b32 v103, 0, 63\n\t"
      "s_cmp_eq_u32 %1, 0\n\t"
      "s_cselect_b64 s[90:91], 1, 0\n\t"
      "v_lshrrev_b32 v104, 4, v96\n\t"
      "v_bfe_u32 v105, v96, 2, 2\n\t"
      "v_and_b32 v110, 3, v96\n\t"
      "s_lshl_b32 s78, %1, 2\n\t"
      "v_lshl_add_u32 v109, v104, 3, v105\n\t"
      "v_lshlrev_b32 v109, 8, v109\n\t"
      "v_or_b32 v110, s78, v110\n\t"
      "v_lshlrev_b32 v105, 2, v105\n\t"
      "v_xor_b32 v110, v110, v105\n\t"
      "v_lshl_add_u32 v109, v110, 4, v109\n\t"
      "s_lshl_b32 s78, %1, 6\n\t"
      "s_add_u32 s78, s78, 131072\n\t"
      "v_add_u32 v110, s78, v106\n\t"
      "s_bfe_u32 s80, %0, 0x10005\n\t"
      "s_and_b32 s79, %1, 1\n\t"
      "v_bfe_u32 v105, v96, 4, 1\n\t"
      "v_lshlrev_b32 v105, 1, v105\n\t"
      "v_and_b32 v116, 7, v96\n\t"
      "v_xor_b32 v116, v116, v105\n\t"
      "s_lshl_b32 s78, s79, 2\n\t"
      "v_xor_b32 v116, s78, v116\n\t"
      "v_lshlrev_b32 v116, 4, v116\n\t"
      "v_lshrrev_b32 v105, 3, v96\n\t"
      "v_lshl_add_u32 v116, v105, 13, v116\n\t"
      "s_lshl_b32 s78, s82, 8\n\t"
      "s_lshl_b32 s72, s80, 7\n\t"
      "s_add_u32 s78, s78, s72\n\t"
      "s_lshl_b32 s72, %1, 16\n\t"
      "s_add_u32 s78, s78, s72\n\t"
      "v_add_u32 v116, s78, v116\n\t"
      "v_bfe_u32 v105, v96, 2, 2\n\t"
      "v_lshl_add_u32 v117, v104, 3, v105\n\t"
      "v_lshlrev_b32 v117, 7, v117\n\t"
      "v_bfe_u32 v105, v96, 3, 2\n\t"
      "v_xor_b32 v105, %1, v105\n\t"
      "v_lshl_add_u32 v117, v105, 5, v117\n\t"
      "v_and_b32 v105, 3, v96\n\t"
      "v_lshl_add_u32 v117, v105, 3, v117\n\t"
      "v_and_b32 v111, 15, v96\n\t"
      "v_lshlrev_b32 v111, 4, v111\n\t"
      "v_and_b32 v105, 1, v104\n\t"
      "v_lshl_add_u32 v111, v105, 10, v111\n\t"
      "v_lshrrev_b32 v105, 1, v104\n\t"
      "v_lshl_add_u32 v111, v105, 8, v111\n\t"
      "s_lshl_b32 s78, s79, 9\n\t"
      "s_add_u32 s78, s78, 16384\n\t"
      "s_lshl_b32 s80, s80, 12\n\t"
      "s_sub_u32 s78, s78, s80\n\t"
      "s_add_u32 s80, %1, 1\n\t"
      "s_lshr_b32 s80, s80, 1\n\t"
      "s_lshl_b32 s80, s80, 11\n\t"
      "s_sub_u32 s78, s78, s80\n\t"
      "v_add_u32 v111, s78, v111\n\t"
      "s_mov_b32 s69, 0\n\t"
      "s_add_u32 m0, s76, 0\n\t"
      "s_add_u32 s68, s77, 0\n\t"
      "buffer_load_dwordx4 v108, s[64:67], s68 offen lds\n\t"
      "s_add_u32 m0, s76, 1024\n\t"
      "s_add_u32 s68, s77, 32768\n\t"
      "s_add_u32 s76, s76, 8192\n\t"
      "s_add_u32 s77, s77, 0x40000\n\t"
      "buffer_load_dwordx4 v108, s[64:67], s68 offen lds\n\t"
      "s_add_u32 m0, s76, 0\n\t"
      "s_add_u32 s68, s77, 0\n\t"
      "buffer_load_dwordx4 v108, s[64:67], s68 offen lds\n\t"
      "s_add_u32 m0, s76, 1024\n\t"
      "s_add_u32 s68, s77, 32768\n\t"
      "s_add_u32 s76, s76, 8192\n\t"
      "s_add_u32 s77, s77, 0x40000\n\t"
      "buffer_load_dwordx4 v108, s[64:67], s68 offen lds\n\t"
      "s_add_u32 m0, s76, 0\n\t"
      "s_add_u32 s68, s77, 0\n\t"
      "buffer_load_dwordx4 v108, s[64:67], s68 offen lds\n\t"
      "s_add_u32 m0, s76, 1024\n\t"
      "s_add_u32 s68, s77, 32768\n\t"
      "s_add_u32 s76, s76, 8192\n\t"
      "s_add_u32 s77, s77, 0x40000\n\t"
      "buffer_load_dwordx4 v108, s[64:67], s68 offen lds\n\t"
      "s_mov_b64 s[88:89], 0\n\t"
      "s_waitcnt vmcnt(8)\n\t"
      "GLN%=:\n\t"
      "v_and_b32 v1, s84, v64\n\t"
      "v_lshlrev_b32 v0, 16, v64\n\t"
      "v_and_b32 v3, s84, v65\n\t"
      "v_lshlrev_b32 v2, 16, v65\n\t"
      "v_and_b32 v5, s84, v66\n\t"
      "v_lshlrev_b32 v4, 16, v66\n\t"
      "v_and_b32 v7, s84, v67\n\t"
      "v_lshlrev_b32 v6, 16, v67\n\t"
      "v_and_b32 v9, s84, v68\n\t"
      "v_lshlrev_b32 v8, 16, v68\n\t"
      "v_and_b32 v11, s84, v69\n\t"
      "v_lshlrev_b32 v10, 16, v69\n\t"
      "v_and_b32 v13, s84, v70\n\t"
      "v_lshlrev_b32 v12, 16, v70\n\t"
      "v_and_b32 v15, s84, v71\n\t"
      "v_lshlrev_b32 v14, 16, v71\n\t"
      "s_mov_b64 exec, s[88:89]\n\t"
      "s_add_u32 m0, s76, 0\n\t"
      "s_add_u32 s68, s77, 0\n\t"
      "buffer_load_dwordx4 v108, s[64:67], s68 offen lds\n\t"
      "s_mov_b64 exec, -1\n\t"
      "v_and_b32 v17, s84, v72\n\t"
      "v_lshlrev_b32 v16, 16, v72\n\t"
      "v_and_b32 v19, s84, v73\n\t"
      "v_lshlrev_b32 v18, 16, v73\n\t"
      "v_and_b32 v21, s84, v74\n\t"
      "v_lshlrev_b32 v20, 16, v74\n\t"
      "v_and_b32 v23, s84, v75\n\t"
      "v_lshlrev_b32 v22, 16, v75\n\t"
      "v_and_b32 v25, s84, v76\n\t"
      "v_lshlrev_b32 v24, 16, v76\n\t"
      "v_and_b32 v27, s84, v77\n\t"
      "v_lshlrev_b32 v26, 16, v77\n\t"
      "v_and_b32 v29, s84, v78\n\t"
      "v_lshlrev_b32 v28, 16, v78\n\t"
      "v_and_b32 v31, s84, v79\n\t"
      "v_lshlrev_b32 v30, 16, v79\n\t"
      "s_mov_b64 exec, s[88:89]\n\t"
      "s_add_u32 m0, s76, 1024\n\t"
      "s_add_u32 s68, s77, 32768\n\t"
      "s_add_u32 s76, s76, 8192\n\t"
      "s_add_u32 s77, s77, 0x40000\n\t"
      "buffer_load_dwordx4 v108, s[64:67], s68 offen lds\n\t"
      "s_mov_b64 exec, -1\n\t"
      "v_pk_add_f32 v[32:33], v[0:1], v[2:3]\n\t"
      "v_pk_add_f32 v[34:35], v[4:5], v[6:7]\n\t"
      "v_pk_add_f32 v[36:37], v[8:9], v[10:11]\n\t"
      "v_pk_add_f32 v[38:39], v[12:13], v[14:15]\n\t"
      "v_pk_add_f32 v[40:41], v[16:17], v[18:19]\n\t"
      "v_pk_add_f32 v[42:43], v[20:21], v[22:23]\n\t"
      "v_pk_add_f32 v[44:45], v[24:25], v[26:27]\n\t"
      "v_pk_add_f32 v[46:47], v[28:29], v[30:31]\n\t"
      "v_pk_add_f32 v[32:33], v[32:33], v[40:41]\n\t"
      "v_pk_add_f32 v[34:35], v[34:35], v[42:43]\n\t"
      "v_pk_add_f32 v[36:37], v[36:37], v[44:45]\n\t"
      "v_pk_add_f32 v[38:39], v[38:39], v[46:47]\n\t"
      "v_pk_add_f32 v[32:33], v[32:33], v[36:37]\n\t"
      "v_pk_add_f32 v[34:35], v[34:35], v[38:39]\n\t"
      "v_pk_add_f32 v[32:33], v[32:33], v[34:35]\n\t"
      "v_add_f32 v32, v32, v33\n\t"
      "s_mov_b64 exec, s[88:89]\n\t"
      "s_add_u32 m0, s76, 0\n\t"
      "s_add_u32 s68, s77, 0\n\t"
      "buffer_load_dwordx4 v108, s[64:67], s68 offen lds\n\t"
      "s_mov_b64 exec, -1\n\t"
      "v_add_f32_dpp v32, v32, v32 row_ror:4 row_mask:0xf bank_mask:0xf\n\t"
      "s_nop 1\n\t"
      "v_add_f32_dpp v32, v32, v32 row_ror:8 row_mask:0xf bank_mask:0xf\n\t"
      "v_mov_b32 v33, v32\n\t"
      "s_nop 1\n\t"
      "v_permlane16_swap_b32 v32, v33\n\t"
      "v_add_f32 v32, v32, v33\n\t"
      "v_mov_b32 v33, v32\n\t"
      "s_nop 1\n\t"
      "v_permlane32_swap_b32 v32, v33\n\t"
      "v_add_f32 v32, v32, v33\n\t"
      "v_mul_f32 v80, 0x3b000000, v32\n\t"
      "v_pk_add_f32 v[32:33], v[0:1], v[80:81] op_sel_hi:[1,0] neg_lo:[0,1] neg_hi:[0,1]\n\t"
      "v_pk_add_f32 v[34:35], v[2:3], v[80:81] op_sel_hi:[1,0] neg_lo:[0,1] neg_hi:[0,1]\n\t"
      "v_pk_add_f32 v[36:37], v[4:5], v[80:81] op_sel_hi:[1,0] neg_lo:[0,1] neg_hi:[0,1]\n\t"
      "v_pk_add_f32 v[38:39], v[6:7], v[80:81] op_sel_hi:[1,0] neg_lo:[0,1] neg_hi:[0,1]\n\t"
      "v_pk_add_f32 v[40:41], v[8:9], v[80:81] op_sel_hi:[1,0] neg_lo:[0,1] neg_hi:[0,1]\n\t"
      "v_pk_add_f32 v[42:43], v[10:11], v[80:81] op_sel_hi:[1,0] neg_lo:[0,1] neg_hi:[0,1]\n\t"
      "v_pk_add_f32 v[44:45], v[12:13], v[80:81] op_sel_hi:[1,0] neg_lo:[0,1] neg_hi:[0,1]\n\t"
      "s_mov_b64 exec, s[88:89]\n\t"
      "s_add_u32 m0, s76, 1024\n\t"
      "s_add_u32 s68, s77, 32768\n\t"
      "s_add_u32 s76, s76, 8192\n\t"
      "s_add_u32 s77, s77, 0x40000\n\t"
      "buffer_load_dwordx4 v108, s[64:67], s68 offen lds\n\t"
      "s_mov_b64 exec, -1\n\t"
      "v_pk_add_f32 v[46:47], v[14:15], v[80:81] op_sel_hi:[1,0] neg_lo:[0,1] neg_hi:[0,1]\n\t"
      "v_pk_add_f32 v[48:49], v[16:17], v[80:81] op_sel_hi:[1,0] neg_lo:[0,1] neg_hi:[0,1]\n\t"
      "v_pk_add_f32 v[50:51], v[18:19], v[80:81] op_sel_hi:[1,0] neg_lo:[0,1] neg_hi:[0,1]\n\t"
      "v_pk_add_f32 v[52:53], v[20:21], v[80:81] op_sel_hi:[1,0] neg_lo:[0,1] neg_hi:[0,1]\n\t"
      "v_pk_add_f32 v[54:55], v[22:23], v[80:81] op_sel_hi:[1,0] neg_lo:[0,1] neg_hi:[0,1]\n\t"
      "v_pk_add_f32 v[56:57], v[24:25], v[80:81] op_sel_hi:[1,0] neg_lo:[0,1] neg_hi:[0,1]\n\t"
      "v_pk_add_f32 v[58:59], v[26:27], v[80:81] op_sel_hi:[1,0] neg_lo:[0,1] neg_hi:[0,1]\n\t"
      "v_pk_add_f32 v[60:61], v[28:29], v[80:81] op_sel_hi:[1,0] neg_lo:[0,1] neg_hi:[0,1]\n\t"
      "v_pk_add_f32 v[62:63], v[30:31], v[80:81] op_sel_hi:[1,0] neg_lo:[0,1] neg_hi:[0,1]\n\t"
      "v_pk_mul_f32 v[64:65], v[32:33], v[32:33]\n\t"
      "v_pk_mul_f32 v[66:67], v[34:35], v[34:35]\n\t"
      "v_pk_mul_f32 v[68:69], v[36:37], v[36:37]\n\t"
      "v_pk_mul_f32 v[70:71], v[38:39], v[38:39]\n\t"
      "v_pk_fma_f32 v[64:65], v[40:41], v[40:41], v[64:65]\n\t"
      "v_pk_fma_f32 v[66:67], v[42:43], v[42:43], v[66:67]\n\t"
      "v_pk_fma_f32 v[68:69], v[44:45], v[44:45], v[68:69]\n\t"
      "s_mov_b64 exec, s[88:89]\n\t"
      "s_add_u32 m0, s76, 0\n\t"
      "s_add_u32 s68, s77, 0\n\t"
      "buffer_load_dwordx4 v108, s[64:67], s68 offen lds\n\t"
      "s_mov_b64 exec, -1\n\t"
      "v_pk_fma_f32 v[70:71], v[46:47], v[46:47], v[70:71]\n\t"
      "v_pk_fma_f32 v[64:65], v[48:49], v[48:49], v[64:65]\n\t"
      "v_pk_fma_f32 v[66:67], v[50:51], v[50:51], v[66:67]\n\t"
      "v_pk_fma_f32 v[68:69], v[52:53], v[52:53], v[68:69]\n\t"
      "v_pk_fma_f32 v[70:71], v[54:55], v[54:55], v[70:71]\n\t"
      "v_pk_fma_f32 v[64:65], v[56:57], v[56:57], v[64:65]\n\t"
      "v_pk_fma_f32 v[66:67], v[58:59], v[58:59], v[66:67]\n\t"
      "v_pk_fma_f32 v[68:69], v[60:61], v[60:61], v[68:69]\n\t"
      "v_pk_fma_f32 v[70:71], v[62:63], v[62:63], v[70:71]\n\t"
      "v_pk_add_f32 v[64:65], v[64:65], v[66:67]\n\t"
      "v_pk_add_f32 v[68:69], v[68:69], v[70:71]\n\t"
      "v_pk_add_f32 v[64:65], v[64:65], v[68:69]\n\t"
      "v_add_f32 v64, v64, v65\n\t"
      "s_nop 1\n\t"
      "v_add_f32_dpp v64, v64, v64 row_ror:4 row_mask:0xf bank_mask:0xf\n\t"
      "s_nop 1\n\t"
      "v_add_f32_dpp v64, v64, v64 row_ror:8 row_mask:0xf bank_mask:0xf\n\t"
      "v_mov_b32 v65, v64\n\t"
      "s_mov_b64 exec, s[88:89]\n\t"
      "s_add_u32 m0, s76, 1024\n\t"
      "s_add_u32 s68, s77, 32768\n\t"
      "s_add_u32 s76, s76, 8192\n\t"
      "s_add_u32 s77, s77, 0x40000\n\t"
      "buffer_load_dwordx4 v108, s[64:67], s68 offen lds\n\t"
      "s_mov_b64 exec, -1\n\t"
      "v_permlane16_swap_b32 v64, v65\n\t"
      "v_add_f32 v64, v64, v65\n\t"
      "v_mov_b32 v65, v64\n\t"
      "s_nop 1\n\t"
      "v_permlane32_swap_b32 v64, v65\n\t"
      "v_add_f32 v64, v64, v65\n\t"
      "v_mul_f32 v81, 0x3b000000, v64\n\t"
      "v_cvt_pk_bf16_f32 v80, v80, v81\n\t"
      "v_and_b32 v81, s84, v80\n\t"
      "v_lshlrev_b32 v80, 16, v80\n\t"
      "v_add_f32 v81, 0x3727c5ac, v81\n\t"
      "v_pk_add_f32 v[32:33], v[0:1], v[80:81] op_sel_hi:[1,0] neg_lo:[0,1] neg_hi:[0,1]\n\t"
      "v_pk_add_f32 v[34:35], v[2:3], v[80:81] op_sel_hi:[1,0] neg_lo:[0,1] neg_hi:[0,1]\n\t"
      "v_pk_add_f32 v[36:37], v[4:5], v[80:81] op_sel_hi:[1,0] neg_lo:[0,1] neg_hi:[0,1]\n\t"
      "v_pk_add_f32 v[38:39], v[6:7], v[80:81] op_sel_hi:[1,0] neg_lo:[0,1] neg_hi:[0,1]\n\t"
      "v_pk_add_f32 v[40:41], v[8:9], v[80:81] op_sel_hi:[1,0] neg_lo:[0,1] neg_hi:[0,1]\n\t"
      "v_pk_add_f32 v[42:43], v[10:11], v[80:81] op_sel_hi:[1,0] neg_lo:[0,1] neg_hi:[0,1]\n\t"
      "s_mov_b64 exec, s[88:89]\n\t"
      "s_add_u32 m0, s76, 0\n\t"
      "s_add_u32 s68, s77, 0\n\t"
      "buffer_load_dwordx4 v108, s[64:67], s68 offen lds\n\t"
      "s_mov_b64 exec, -1\n\t"
      "v_pk_add_f32 v[44:45], v[12:13], v[80:81] op_sel_hi:[1,0] neg_lo:[0,1] neg_hi:[0,1]\n\t"
      "v_pk_add_f32 v[46:47], v[14:15], v[80:81] op_sel_hi:[1,0] neg_lo:[0,1] neg_hi:[0,1]\n\t"
      "v_pk_add_f32 v[48:49], v[16:17], v[80:81] op_sel_hi:[1,0] neg_lo:[0,1] neg_hi:[0,1]\n\t"
      "v_pk_add_f32 v[50:51], v[18:19], v[80:81] op_sel_hi:[1,0] neg_lo:[0,1] neg_hi:[0,1]\n\t"
      "v_pk_add_f32 v[52:53], v[20:21], v[80:81] op_sel_hi:[1,0] neg_lo:[0,1] neg_hi:[0,1]\n\t"
      "v_pk_add_f32 v[54:55], v[22:23], v[80:81] op_sel_hi:[1,0] neg_lo:[0,1] neg_hi:[0,1]\n\t"
      "v_pk_add_f32 v[56:57], v[24:25], v[80:81] op_sel_hi:[1,0] neg_lo:[0,1] neg_hi:[0,1]\n\t"
      "v_pk_add_f32 v[58:59], v[26:27], v[80:81] op_sel_hi:[1,0] neg_lo:[0,1] neg_hi:[0,1]\n\t"
      "v_pk_add_f32 v[60:61], v[28:29], v[80:81] op_sel_hi:[1,0] neg_lo:[0,1] neg_hi:[0,1]\n\t"
      "v_pk_add_f32 v[62:63], v[30:31], v[80:81] op_sel_hi:[1,0] neg_lo:[0,1] neg_hi:[0,1]\n\t"
      "v_cvt_pk_bf16_f32 v81, v81, v81\n\t"
      "v_and_b32 v81, s84, v81\n\t"
      "v_sqrt_f32 v81, v81\n\t"
      "v_cvt_pk_bf16_f32 v32, v32, v33\n\t"
      "v_cvt_pk_bf16_f32 v34, v34, v35\n\t"
      "v_cvt_pk_bf16_f32 v36, v36, v37\n\t"
      "s_mov_b64 exec, s[88:89]\n\t"
      "s_add_u32 m0, s76, 1024\n\t"
      "s_add_u32 s68, s77, 32768\n\t"
      "s_add_u32 s76, s76, 8192\n\t"
      "s_add_u32 s77, s77, 0x40000\n\t"
      "buffer_load_dwordx4 v108, s[64:67], s68 offen lds\n\t"
      "s_mov_b64 exec, -1\n\t"
      "v_cvt_pk_bf16_f32 v38, v38, v39\n\t"
      "v_cvt_pk_bf16_f32 v40, v40, v41\n\t"
      "v_cvt_pk_bf16_f32 v42, v42, v43\n\t"
      "v_cvt_pk_bf16_f32 v44, v44, v45\n\t"
      "v_cvt_pk_bf16_f32 v46, v46, v47\n\t"
      "v_cvt_pk_bf16_f32 v48, v48, v49\n\t"
      "v_cvt_pk_bf16_f32 v50, v50, v51\n\t"
      "v_cvt_pk_bf16_f32 v52, v52, v53\n\t"
      "v_cvt_pk_bf16_f32 v54, v54, v55\n\t"
      "v_cvt_pk_bf16_f32 v56, v56, v57\n\t"
      "v_cvt_pk_bf16_f32 v58, v58, v59\n\t"
      "v_cvt_pk_bf16_f32 v60, v60, v61\n\t"
      "v_cvt_pk_bf16_f32 v62, v62, v63\n\t"
      "v_cvt_pk_bf16_f32 v81, v81, v81\n\t"
      "v_and_b32 v81, s84, v81\n\t"
      "v_rcp_f32 v80, v81\n\t"
      "s_mov_b64 exec, s[88:89]\n\t"
      "s_add_u32 m0, s76, 0\n\t"
      "s_add_u32 s68, s77, 0\n\t"
      "buffer_load_dwordx4 v108, s[64:67], s68 offen lds\n\t"
      "s_mov_b64 exec, -1\n\t"
      "v_and_b32 v33, s84, v32\n\t"
      "v_lshlrev_b32 v32, 16, v32\n\t"
      "v_and_b32 v35, s84, v34\n\t"
      "v_lshlrev_b32 v34, 16, v34\n\t"
      "v_and_b32 v37, s84, v36\n\t"
      "v_lshlrev_b32 v36, 16, v36\n\t"
      "v_and_b32 v39, s84, v38\n\t"
      "v_lshlrev_b32 v38, 16, v38\n\t"
      "v_pk_mul_f32 v[32:33], v[32:33], v[80:81] op_sel_hi:[1,0]\n\t"
      "v_pk_mul_f32 v[34:35], v[34:35], v[80:81] op_sel_hi:[1,0]\n\t"
      "v_pk_mul_f32 v[36:37], v[36:37], v[80:81] op_sel_hi:[1,0]\n\t"
      "v_pk_mul_f32 v[38:39], v[38:39], v[80:81] op_sel_hi:[1,0]\n\t"
      "v_cvt_pk_bf16_f32 v64, v32, v33\n\t"
      "v_cvt_pk_bf16_f32 v65, v34, v35\n\t"
      "v_cvt_pk_bf16_f32 v66, v36, v37\n\t"
      "v_cvt_pk_bf16_f32 v67, v38, v39\n\t"
      "s_mov_b64 exec, s[88:89]\n\t"
      "s_add_u32 m0, s76, 1024\n\t"
      "s_add_u32 s68, s77, 32768\n\t"
      "s_add_u32 s76, s76, 8192\n\t"
      "s_add_u32 s77, s77, 0x40000\n\t"
      "buffer_load_dwordx4 v108, s[64:67], s68 offen lds\n\t"
      "s_mov_b64 exec, -1\n\t"
      "v_and_b32 v41, s84, v40\n\t"
      "v_lshlrev_b32 v40, 16, v40\n\t"
      "v_and_b32 v43, s84, v42\n\t"
      "v_lshlrev_b32 v42, 16, v42\n\t"
      "v_and_b32 v45, s84, v44\n\t"
      "v_lshlrev_b32 v44, 16, v44\n\t"
      "v_and_b32 v47, s84, v46\n\t"
      "v_lshlrev_b32 v46, 16, v46\n\t"
      "v_pk_mul_f32 v[40:41], v[40:41], v[80:81] op_sel_hi:[1,0]\n\t"
      "v_pk_mul_f32 v[42:43], v[42:43], v[80:81] op_sel_hi:[1,0]\n\t"
      "v_pk_mul_f32 v[44:45], v[44:45], v[80:81] op_sel_hi:[1,0]\n\t"
      "v_pk_mul_f32 v[46:47], v[46:47], v[80:81] op_sel_hi:[1,0]\n\t"
      "v_cvt_pk_bf16_f32 v68, v40, v41\n\t"
      "v_cvt_pk_bf16_f32 v69, v42, v43\n\t"
      "v_cvt_pk_bf16_f32 v70, v44, v45\n\t"
      "v_cvt_pk_bf16_f32 v71, v46, v47\n\t"
      "s_mov_b64 exec, s[88:89]\n\t"
      "s_add_u32 m0, s76, 0\n\t"
      "s_add_u32 s68, s77, 0\n\t"
      "buffer_load_dwordx4 v108, s[64:67], s68 offen lds\n\t"
      "s_mov_b64 exec, -1\n\t"
      "v_and_b32 v49, s84, v48\n\t"
      "v_lshlrev_b32 v48, 16, v48\n\t"
      "v_and_b32 v51, s84, v50\n\t"
      "v_lshlrev_b32 v50, 16, v50\n\t"
      "v_and_b32 v53, s84, v52\n\t"
      "v_lshlrev_b32 v52, 16, v52\n\t"
      "v_and_b32 v55, s84, v54\n\t"
      "v_lshlrev_b32 v54, 16, v54\n\t"
      "v_pk_mul_f32 v[48:49], v[48:49], v[80:81] op_sel_hi:[1,0]\n\t"
      "v_pk_mul_f32 v[50:51], v[50:51], v[80:81] op_sel_hi:[1,0]\n\t"
      "v_pk_mul_f32 v[52:53], v[52:53], v[80:81] op_sel_hi:[1,0]\n\t"
      "v_pk_mul_f32 v[54:55], v[54:55], v[80:81] op_sel_hi:[1,0]\n\t"
      "v_cvt_pk_bf16_f32 v72, v48, v49\n\t"
      "v_cvt_pk_bf16_f32 v73, v50, v51\n\t"
      "v_cvt_pk_bf16_f32 v74, v52, v53\n\t"
      "v_cvt_pk_bf16_f32 v75, v54, v55\n\t"
      "s_mov_b64 exec, s[88:89]\n\t"
      "s_add_u32 m0, s76, 1024\n\t"
      "s_add_u32 s68, s77, 32768\n\t"
      "s_add_u32 s76, s76, 8192\n\t"
      "s_add_u32 s77, s77, 0x40000\n\t"
      "buffer_load_dwordx4 v108, s[64:67], s68 offen lds\n\t"
      "s_mov_b64 exec, -1\n\t"
      "v_and_b32 v57, s84, v56\n\t"
      "v_lshlrev_b32 v56, 16, v56\n\t"
      "v_and_b32 v59, s84, v58\n\t"
      "v_lshlrev_b32 v58, 16, v58\n\t"
      "v_and_b32 v61, s84, v60\n\t"
      "v_lshlrev_b32 v60, 16, v60\n\t"
      "v_and_b32 v63, s84, v62\n\t"
      "v_lshlrev_b32 v62, 16, v62\n\t"
      "v_pk_mul_f32 v[56:57], v[56:57], v[80:81] op_sel_hi:[1,0]\n\t"
      "v_pk_mul_f32 v[58:59], v[58:59], v[80:81] op_sel_hi:[1,0]\n\t"
      "v_pk_mul_f32 v[60:61], v[60:61], v[80:81] op_sel_hi:[1,0]\n\t"
      "v_pk_mul_f32 v[62:63], v[62:63], v[80:81] op_sel_hi:[1,0]\n\t"
      "v_cvt_pk_bf16_f32 v76, v56, v57\n\t"
      "v_cvt_pk_bf16_f32 v77, v58, v59\n\t"
      "v_cvt_pk_bf16_f32 v78, v60, v61\n\t"
      "v_cvt_pk_bf16_f32 v79, v62, v63\n\t"
      "s_cmp_lg_u32 s69, 0\n\t"
      "s_cbranch_scc1 GLN1%=\n\t"
      "buffer_store_dwordx4 v[64:67], v106, s[60:63], s74 offen sc1\n\t"
      "s_add_u32 s74, s74, 4096\n\t"
      "buffer_store_dwordx4 v[68:71], v106, s[60:63], s74 offen sc1\n\t"
      "s_add_u32 s74, s74, 4096\n\t"
      "buffer_store_dwordx4 v[72:75], v106, s[60:63], s74 offen sc1\n\t"
      "s_add_u32 s74, s74, 4096\n\t"
      "buffer_store_dwordx4 v[76:79], v106, s[60:63], s74 offen sc1\n\t"
      "s_add_u32 s74, s74, 4096\n\t"
      "s_sub_u32 s76, s76, 49152\n\t"
      "s_sub_u32 s77, s77, 1572864\n\t"
      "s_mov_b64 s[88:89], -1\n\t"
      "v_readfirstlane_b32 %3, v107\n\t"
      "v_mov_b64 v[64:65], v[128:129]\n\t"
      "v_mov_b64 v[66:67], v[130:131]\n\t"
      "v_mov_b64 v[68:69], v[132:133]\n\t"
      "v_mov_b64 v[70:71], v[134:135]\n\t"
      "v_mov_b64 v[72:73], v[136:137]\n\t"
      "v_mov_b64 v[74:75], v[138:139]\n\t"
      "v_mov_b64 v[76:77], v[140:141]\n\t"
      "v_mov_b64 v[78:79], v[142:143]\n\t"
      "s_mul_i32 %3, %3, 0x9e3779b1\n\t"
      "s_add_u32 %3, %3, 0x7f4a7c15\n\t"
      "s_mov_b32 s69, 1\n\t"
      "s_branch GLN%=\n\t"
      "GLN1%=:\n\t"
      "ds_write_b128 v110, v[64:67]\n\t"
      "ds_write_b128 v110, v[68:71] offset:4096\n\t"
      "ds_write_b128 v110, v[72:75] offset:8192\n\t"
      "ds_write_b128 v110, v[76:79] offset:12288\n\t"
      "s_cmp_eq_u32 s69, 2\n\t"
      "s_cbranch_scc1 GLN2%=\n\t"
      "v_mov_b32 v107, %3\n\t"
      "v_add_u32 v110, 0x4000, v110\n\t"
      "s_lshl_b32 s79, %0, 7\n\t"
      "s_add_u32 s79, s79, 37120\n\t"
      "s_waitcnt vmcnt(12) lgkmcnt(0)\n\t"
      "s_barrier\n\t"
      "s_mov_b64 exec, s[90:91]\n\t"
      "buffer_store_dword v107, off, s[60:63], s79 sc1\n\t"
      "s_mov_b64 exec, -1\n\t"
      "v_mov_b64 v[64:65], v[144:145]\n\t"
      "v_mov_b64 v[66:67], v[146:147]\n\t"
      "v_mov_b64 v[68:69], v[148:149]\n\t"
      "v_mov_b64 v[70:71], v[150:151]\n\t"
      "v_mov_b64 v[72:73], v[152:153]\n\t"
      "v_mov_b64 v[74:75], v[154:155]\n\t"
      "v_mov_b64 v[76:77], v[156:157]\n\t"
      "v_mov_b64 v[78:79], v[158:159]\n\t"
      "s_mov_b32 s69, 2\n\t"
      "s_branch GLN%=\n\t"
      "GLN2%=:\n\t"
      "s_waitcnt lgkmcnt(10)\n\t"
      "ds_read_b64_tr_b16 a[0:1], v109\n\t"
      "ds_read_b64_tr_b16 a[2:3], v109 offset:1024\n\t"
      "ds_read_b64_tr_b16 a[4:5], v109 offset:8\n\t"
      "ds_read_b64_tr_b16 a[6:7], v109 offset:1032\n\t"
      "s_waitcnt lgkmcnt(10)\n\t"
      "ds_read_b64_tr_b16 a[8:9], v109 offset:8192\n\t"
      "ds_read_b64_tr_b16 a[10:11], v109 offset:9216\n\t"
      "ds_read_b64_tr_b16 a[12:13], v109 offset:8200\n\t"
      "ds_read_b64_tr_b16 a[14:15], v109 offset:9224\n\t"
      "s_waitcnt lgkmcnt(10)\n\t"
      "ds_read_b64_tr_b16 a[16:17], v109 offset:16384\n\t"
      "ds_read_b64_tr_b16 a[18:19], v109 offset:17408\n\t"
      "ds_read_b64_tr_b16 a[20:21], v109 offset:16392\n\t"
      "ds_read_b64_tr_b16 a[22:23], v109 offset:17416\n\t"
      "s_waitcnt lgkmcnt(10)\n\t"
      "ds_read_b64_tr_b16 a[24:25], v109 offset:24576\n\t"
      "ds_read_b64_tr_b16 a[26:27], v109 offset:25600\n\t"
      "ds_read_b64_tr_b16 a[28:29], v109 offset:24584\n\t"
      "ds_read_b64_tr_b16 a[30:31], v109 offset:25608\n\t"
      "v_lshlrev_b32 v106, 8, v96\n\t"
      "s_mov_b32 s79, 37120\n\t"
      "s_mov_b32 s81, 0x40000\n\t"
      "v_add_u32 v98, 131072, v97\n\t"
      "v_readlane_b32 s73, v102, 0\n\t"
      "v_readlane_b32 s75, v103, 0\n\t"
      "s_mov_b64 s[64:65], s[40:41]\n\t"
      "s_lshl_b32 s74, %1, 10\n\t"
      "v_readlane_b32 s72, v101, 1\n\t"
      "s_add_u32 s76, s85, 0\n\t"
      "s_waitcnt vmcnt(12)\n\t"
      "s_barrier\n\t"
      "s_waitcnt lgkmcnt(10)\n\t"
      "ds_read_b64_tr_b16 a[32:33], v109 offset:32768\n\t"
      "ds_read_b64_tr_b16 a[34:35], v109 offset:33792\n\t"
      "ds_read_b64_tr_b16 a[36:37], v109 offset:32776\n\t"
      "ds_read_b64_tr_b16 a[38:39], v109 offset:33800\n\t"
      "s_waitcnt lgkmcnt(10)\n\t"
      "ds_read_b64_tr_b16 a[40:41], v109 offset:40960\n\t"
      "ds_read_b64_tr_b16 a[42:43], v109 offset:41984\n\t"
      "ds_read_b64_tr_b16 a[44:45], v109 offset:40968\n\t"
      "ds_read_b64_tr_b16 a[46:47], v109 offset:41992\n\t"
      "s_waitcnt lgkmcnt(10)\n\t"
      "ds_read_b64_tr_b16 a[48:49], v109 offset:49152\n\t"
      "ds_read_b64_tr_b16 a[50:51], v109 offset:50176\n\t"
      "ds_read_b64_tr_b16 a[52:53], v109 offset:49160\n\t"
      "ds_read_b64_tr_b16 a[54:55], v109 offset:50184\n\t"
      "s_waitcnt lgkmcnt(10)\n\t"
      "ds_read_b64_tr_b16 a[56:57], v109 offset:57344\n\t"
      "ds_read_b64_tr_b16 a[58:59], v109 offset:58368\n\t"
      "ds_read_b64_tr_b16 a[60:61], v109 offset:57352\n\t"
      "ds_read_b64_tr_b16 a[62:63], v109 offset:58376\n\t"
      "v_add_u32 v109, 0x10000, v109\n\t"
      "s_waitcnt lgkmcnt(10)\n\t"
      "ds_read_b64_tr_b16 a[64:65], v109\n\t"
      "ds_read_b64_tr_b16 a[66:67], v109 offset:1024\n\t"
      "ds_read_b64_tr_b16 a[68:69], v109 offset:8\n\t"
      "ds_read_b64_tr_b16 a[70:71], v109 offset:1032\n\t"
      "s_waitcnt lgkmcnt(10)\n\t"
      "ds_read_b64_tr_b16 a[72:73], v109 offset:8192\n\t"
      "ds_read_b64_tr_b16 a[74:75], v109 offset:9216\n\t"
      "ds_read_b64_tr_b16 a[76:77], v109 offset:8200\n\t"
      "ds_read_b64_tr_b16 a[78:79], v109 offset:9224\n\t"
      "s_waitcnt vmcnt(0)\n\t"
      "s_barrier\n\t"
      "buffer_load_dword v128, v106, s[60:63], s79 offen sc1\n\t"
      "buffer_load_dword v129, v106, s[60:63], s79 offen offset:128 sc1\n\t"
      "s_waitcnt lgkmcnt(10)\n\t"
      "ds_read_b64_tr_b16 a[80:81], v109 offset:16384\n\t"
      "ds_read_b64_tr_b16 a[82:83], v109 offset:17408\n\t"
      "ds_read_b64_tr_b16 a[84:85], v109 offset:16392\n\t"
      "ds_read_b64_tr_b16 a[86:87], v109 offset:17416\n\t"
      "s_waitcnt lgkmcnt(10)\n\t"
      "ds_read_b64_tr_b16 a[88:89], v109 offset:24576\n\t"
      "ds_read_b64_tr_b16 a[90:91], v109 offset:25600\n\t"
      "ds_read_b64_tr_b16 a[92:93], v109 offset:24584\n\t"
      "ds_read_b64_tr_b16 a[94:95], v109 offset:25608\n\t"
      "s_waitcnt lgkmcnt(10)\n\t"
      "ds_read_b64_tr_b16 a[96:97], v109 offset:32768\n\t"
      "ds_read_b64_tr_b16 a[98:99], v109 offset:33792\n\t"
      "ds_read_b64_tr_b16 a[100:101], v109 offset:32776\n\t"
      "ds_read_b64_tr_b16 a[102:103], v109 offset:33800\n\t"
      "s_waitcnt lgkmcnt(10)\n\t"
      "ds_read_b64_tr_b16 a[104:105], v109 offset:40960\n\t"
      "ds_read_b64_tr_b16 a[106:107], v109 offset:41984\n\t"
      "ds_read_b64_tr_b16 a[108:109], v109 offset:40968\n\t"
      "ds_read_b64_tr_b16 a[110:111], v109 offset:41992\n\t"
      "s_waitcnt lgkmcnt(10)\n\t"
      "ds_read_b64_tr_b16 a[112:113], v109 offset:49152\n\t"
      "ds_read_b64_tr_b16 a[114:115], v109 offset:50176\n\t"
      "ds_read_b64_tr_b16 a[116:117], v109 offset:49160\n\t"
      "ds_read_b64_tr_b16 a[118:119], v109 offset:50184\n\t"
      "s_waitcnt lgkmcnt(10)\n\t"
      "ds_read_b64_tr_b16 a[120:121], v109 offset:57344\n\t"
      "ds_read_b64_tr_b16 a[122:123], v109 offset:58368\n\t"
      "ds_read_b64_tr_b16 a[124:125], v109 offset:57352\n\t"
      "ds_read_b64_tr_b16 a[126:127], v109 offset:58376\n\t"
      "s_mov_b64 s[88:89], 0\n\t"
      "s_mov_b64 s[38:39], 0\n\t"
      "s_waitcnt lgkmcnt(4)\n\t"
      "GKV%=:\n\t"
      "ds_read_b128 v[0:3], v98\n\t"
      "ds_read_b128 v[4:7], v98 offset:1024\n\t"
      "ds_read_b128 v[8:11], v98 offset:2048\n\t"
      "ds_read_b128 v[12:15], v98 offset:3072\n\t"
      "ds_read_b128 v[16:19], v98 offset:4096\n\t"
      "ds_read_b128 v[20:23], v98 offset:5120\n\t"
      "ds_read_b128 v[24:27], v98 offset:6144\n\t"
      "ds_read_b128 v[28:31], v98 offset:7168\n\t"
      "ds_read_b128 v[32:35], v98 offset:8192\n\t"
      "ds_read_b128 v[36:39], v98 offset:9216\n\t"
      "s_waitcnt lgkmcnt(9)\n\t"
      "v_mfma_f32_16x16x32_bf16 v[64:67], a[0:3], v[0:3], 0\n\t"
      "ds_read_b128 v[40:43], v98 offset:10240\n\t"
      "v_mfma_f32_16x16x32_bf16 v[68:71], a[4:7], v[0:3], 0\n\t"
      "s_waitcnt lgkmcnt(9)\n\t"
      "v_mfma_f32_16x16x32_bf16 v[64:67], a[8:11], v[4:7], v[64:67]\n\t"
      "ds_read_b128 v[44:47], v98 offset:11264\n\t"
      "v_mfma_f32_16x16x32_bf16 v[68:71], a[12:15], v[4:7], v[68:71]\n\t"
      "s_waitcnt lgkmcnt(9)\n\t"
      "v_mfma_f32_16x16x32_bf16 v[64:67], a[16:19], v[8:11], v[64:67]\n\t"
      "ds_read_b128 v[48:51], v98 offset:12288\n\t"
      "v_mfma_f32_16x16x32_bf16 v[68:71], a[20:23], v[8:11], v[68:71]\n\t"
      "s_waitcnt lgkmcnt(9)\n\t"
      "v_mfma_f32_16x16x32_bf16 v[64:67], a[24:27], v[12:15], v[64:67]\n\t"
      "ds_read_b128 v[52:55], v98 offset:13312\n\t"
      "v_mfma_f32_16x16x32_bf16 v[68:71], a[28:31], v[12:15], v[68:71]\n\t"
      "s_waitcnt lgkmcnt(9)\n\t"
      "v_mfma_f32_16x16x32_bf16 v[64:67], a[32:35], v[16:19], v[64:67]\n\t"
      "ds_read_b128 v[56:59], v98 offset:14336\n\t"
      "v_mfma_f32_16x16x32_bf16 v[68:71], a[36:39], v[16:19], v[68:71]\n\t"
      "s_waitcnt lgkmcnt(9)\n\t"
      "v_mfma_f32_16x16x32_bf16 v[64:67], a[40:43], v[20:23], v[64:67]\n\t"
      "ds_read_b128 v[60:63], v98 offset:15360\n\t"
      "v_mfma_f32_16x16x32_bf16 v[68:71], a[44:47], v[20:23], v[68:71]\n\t"
      "s_waitcnt lgkmcnt(9)\n\t"
      "v_mfma_f32_16x16x32_bf16 v[64:67], a[48:51], v[24:27], v[64:67]\n\t"
      "ds_read_b128 v[0:3], v98 offset:16384\n\t"
      "v_mfma_f32_16x16x32_bf16 v[68:71], a[52:55], v[24:27], v[68:71]\n\t"
      "s_waitcnt lgkmcnt(9)\n\t"
      "v_mfma_f32_16x16x32_bf16 v[64:67], a[56:59], v[28:31], v[64:67]\n\t"
      "ds_read_b128 v[4:7], v98 offset:17408\n\t"
      "v_mfma_f32_16x16x32_bf16 v[68:71], a[60:63], v[28:31], v[68:71]\n\t"
      "s_waitcnt lgkmcnt(9)\n\t"
      "v_mfma_f32_16x16x32_bf16 v[64:67], a[64:67], v[32:35], v[64:67]\n\t"
      "ds_read_b128 v[8:11], v98 offset:18432\n\t"
      "v_mfma_f32_16x16x32_bf16 v[68:71], a[68:71], v[32:35], v[68:71]\n\t"
      "s_waitcnt lgkmcnt(9)\n\t"
      "v_mfma_f32_16x16x32_bf16 v[64:67], a[72:75], v[36:39], v[64:67]\n\t"
      "ds_read_b128 v[12:15], v98 offset:19456\n\t"
      "v_mfma_f32_16x16x32_bf16 v[68:71], a[76:79], v[36:39], v[68:71]\n\t"
      "s_waitcnt lgkmcnt(9)\n\t"
      "v_mfma_f32_16x16x32_bf16 v[64:67], a[80:83], v[40:43], v[64:67]\n\t"
      "ds_read_b128 v[16:19], v98 offset:20480\n\t"
      "v_mfma_f32_16x16x32_bf16 v[68:71], a[84:87], v[40:43], v[68:71]\n\t"
      "s_waitcnt lgkmcnt(9)\n\t"
      "v_mfma_f32_16x16x32_bf16 v[64:67], a[88:91], v[44:47], v[64:67]\n\t"
      "ds_read_b128 v[20:23], v98 offset:21504\n\t"
      "v_mfma_f32_16x16x32_bf16 v[68:71], a[92:95], v[44:47], v[68:71]\n\t"
      "s_waitcnt lgkmcnt(9)\n\t"
      "v_mfma_f32_16x16x32_bf16 v[64:67], a[96:99], v[48:51], v[64:67]\n\t"
      "ds_read_b128 v[24:27], v98 offset:22528\n\t"
      "v_mfma_f32_16x16x32_bf16 v[68:71], a[100:103], v[48:51], v[68:71]\n\t"
      "s_cmp_lg_u64 s[88:89], 0\n\t"
      "s_barrier\n\t"
      "s_cbranch_scc1 GKV0%=\n\t"
      "s_waitcnt vmcnt(0)\n\t"
      "v_bitop3_b32 v128, v128, v129, %3 bitop3:0x7e\n\t"
      "v_cmp_ne_u32 vcc, 0, v128\n\t"
      "s_nop 0\n\t"
      "s_cmp_eq_u64 vcc, 0\n\t"
      "s_cselect_b64 s[36:37], -1, 0\n\t"
      "GKV0%=:\n\t"
      "s_waitcnt lgkmcnt(9)\n\t"
      "v_mfma_f32_16x16x32_bf16 v[64:67], a[104:107], v[52:55], v[64:67]\n\t"
      "ds_read_b128 v[28:31], v98 offset:23552\n\t"
      "v_mfma_f32_16x16x32_bf16 v[68:71], a[108:111], v[52:55], v[68:71]\n\t"
      "s_add_u32 m0, s76, 0\n\t"
      "s_mov_b64 exec, s[36:37]\n\t"
      "buffer_load_dwordx4 v97, s[60:63], s72 offen sc1 lds\n\t"
      "s_mov_b64 exec, -1\n\t"
      "s_waitcnt lgkmcnt(9)\n\t"
      "v_mfma_f32_16x16x32_bf16 v[64:67], a[112:115], v[56:59], v[64:67]\n\t"
      "ds_read_b128 v[32:35], v98 offset:24576\n\t"
      "v_mfma_f32_16x16x32_bf16 v[68:71], a[116:119], v[56:59], v[68:71]\n\t"
      "s_waitcnt lgkmcnt(9)\n\t"
      "v_mfma_f32_16x16x32_bf16 v[64:67], a[120:123], v[60:63], v[64:67]\n\t"
      "ds_read_b128 v[36:39], v98 offset:25600\n\t"
      "v_mfma_f32_16x16x32_bf16 v[68:71], a[124:127], v[60:63], v[68:71]\n\t"
      "s_add_u32 m0, s76, 0\n\t"
      "s_mov_b64 exec, s[36:37]\n\t"
      "buffer_load_dwordx4 v97, s[60:63], s72 offen offset:1024 sc1 lds\n\t"
      "s_mov_b64 exec, -1\n\t"
      "s_waitcnt lgkmcnt(9)\n\t"
      "v_mfma_f32_16x16x32_bf16 v[72:75], a[0:3], v[0:3], 0\n\t"
      "ds_read_b128 v[40:43], v98 offset:26624\n\t"
      "v_mfma_f32_16x16x32_bf16 v[76:79], a[4:7], v[0:3], 0\n\t"
      "s_waitcnt lgkmcnt(9)\n\t"
      "v_mfma_f32_16x16x32_bf16 v[72:75], a[8:11], v[4:7], v[72:75]\n\t"
      "ds_read_b128 v[44:47], v98 offset:27648\n\t"
      "v_mfma_f32_16x16x32_bf16 v[76:79], a[12:15], v[4:7], v[76:79]\n\t"
      "s_add_u32 m0, s76, 0\n\t"
      "s_mov_b64 exec, s[36:37]\n\t"
      "buffer_load_dwordx4 v97, s[60:63], s72 offen offset:2048 sc1 lds\n\t"
      "s_mov_b64 exec, -1\n\t"
      "s_waitcnt vmcnt(3)\n\t"
      "s_barrier\n\t"
      "s_mov_b64 exec, s[38:39]\n\t"
      "buffer_store_dword v107, off, s[60:63], s75 sc1\n\t"
      "s_mov_b64 exec, -1\n\t"
      "s_waitcnt lgkmcnt(9)\n\t"
      "v_mfma_f32_16x16x32_bf16 v[72:75], a[16:19], v[8:11], v[72:75]\n\t"
      "ds_read_b128 v[48:51], v98 offset:28672\n\t"
      "v_mfma_f32_16x16x32_bf16 v[76:79], a[20:23], v[8:11], v[76:79]\n\t"
      "s_add_u32 m0, s74, 65536\n\t"
      "s_mov_b32 s78, 0\n\t"
      "s_mov_b64 exec, s[88:89]\n\t"
      "buffer_load_dwordx4 v116, s[64:67], s78 offen lds\n\t"
      "s_mov_b64 exec, -1\n\t"
      "s_waitcnt lgkmcnt(9)\n\t"
      "v_mfma_f32_16x16x32_bf16 v[72:75], a[24:27], v[12:15], v[72:75]\n\t"
      "ds_read_b128 v[52:55], v98 offset:29696\n\t"
      "v_mfma_f32_16x16x32_bf16 v[76:79], a[28:31], v[12:15], v[76:79]\n\t"
      "s_add_u32 m0, s76, 0\n\t"
      "s_mov_b64 exec, s[36:37]\n\t"
      "buffer_load_dwordx4 v97, s[60:63], s72 offen offset:3072 sc1 lds\n\t"
      "s_mov_b64 exec, -1\n\t"
      "s_waitcnt lgkmcnt(9)\n\t"
      "v_mfma_f32_16x16x32_bf16 v[72:75], a[32:35], v[16:19], v[72:75]\n\t"
      "ds_read_b128 v[56:59], v98 offset:30720\n\t"
      "v_mfma_f32_16x16x32_bf16 v[76:79], a[36:39], v[16:19], v[76:79]\n\t"
      "s_add_u32 m0, s74, 69632\n\t"
      "s_mov_b32 s78, 262144\n\t"
      "s_mov_b64 exec, s[88:89]\n\t"
      "buffer_load_dwordx4 v116, s[64:67], s78 offen lds\n\t"
      "s_mov_b64 exec, -1\n\t"
      "s_waitcnt lgkmcnt(9)\n\t"
      "v_mfma_f32_16x16x32_bf16 v[72:75], a[40:43], v[20:23], v[72:75]\n\t"
      "ds_read_b128 v[60:63], v98 offset:31744\n\t"
      "v_mfma_f32_16x16x32_bf16 v[76:79], a[44:47], v[20:23], v[76:79]\n\t"
      "s_add_u32 m0, s76, 4096\n\t"
      "s_add_u32 s72, s72, 4096\n\t"
      "s_mov_b64 exec, s[36:37]\n\t"
      "buffer_load_dwordx4 v97, s[60:63], s72 offen sc1 lds\n\t"
      "s_mov_b64 exec, -1\n\t"
      "v_cvt_pk_bf16_f32 v80, v64, v65\n\t"
      "v_cvt_pk_bf16_f32 v81, v66, v67\n\t"
      "v_cvt_pk_bf16_f32 v82, v68, v69\n\t"
      "v_cvt_pk_bf16_f32 v83, v70, v71\n\t"
      "buffer_store_dwordx4 v[80:83], v97, s[60:63], s73 offen sc1\n\t"
      "s_waitcnt lgkmcnt(9)\n\t"
      "v_mfma_f32_16x16x32_bf16 v[72:75], a[48:51], v[24:27], v[72:75]\n\t"
      "v_mfma_f32_16x16x32_bf16 v[76:79], a[52:55], v[24:27], v[76:79]\n\t"
      "s_add_u32 m0, s74, 73728\n\t"
      "s_mov_b32 s78, 524288\n\t"
      "s_mov_b64 exec, s[88:89]\n\t"
      "buffer_load_dwordx4 v116, s[64:67], s78 offen lds\n\t"
      "s_mov_b64 exec, -1\n\t"
      "s_waitcnt lgkmcnt(8)\n\t"
      "v_mfma_f32_16x16x32_bf16 v[72:75], a[56:59], v[28:31], v[72:75]\n\t"
      "v_mfma_f32_16x16x32_bf16 v[76:79], a[60:63], v[28:31], v[76:79]\n\t"
      "s_add_u32 m0, s76, 4096\n\t"
      "s_mov_b64 exec, s[36:37]\n\t"
      "buffer_load_dwordx4 v97, s[60:63], s72 offen offset:1024 sc1 lds\n\t"
      "s_mov_b64 exec, -1\n\t"
      "s_waitcnt lgkmcnt(7)\n\t"
      "v_mfma_f32_16x16x32_bf16 v[72:75], a[64:67], v[32:35], v[72:75]\n\t"
      "v_mfma_f32_16x16x32_bf16 v[76:79], a[68:71], v[32:35], v[76:79]\n\t"
      "s_add_u32 m0, s74, 77824\n\t"
      "s_mov_b32 s78, 786432\n\t"
      "s_mov_b64 exec, s[88:89]\n\t"
      "buffer_load_dwordx4 v116, s[64:67], s78 offen lds\n\t"
      "s_mov_b64 exec, -1\n\t"
      "s_waitcnt lgkmcnt(6)\n\t"
      "v_mfma_f32_16x16x32_bf16 v[72:75], a[72:75], v[36:39], v[72:75]\n\t"
      "v_mfma_f32_16x16x32_bf16 v[76:79], a[76:79], v[36:39], v[76:79]\n\t"
      "s_add_u32 m0, s76, 4096\n\t"
      "s_mov_b64 exec, s[36:37]\n\t"
      "buffer_load_dwordx4 v97, s[60:63], s72 offen offset:2048 sc1 lds\n\t"
      "s_mov_b64 exec, -1\n\t"
      "s_waitcnt lgkmcnt(5)\n\t"
      "v_mfma_f32_16x16x32_bf16 v[72:75], a[80:83], v[40:43], v[72:75]\n\t"
      "v_mfma_f32_16x16x32_bf16 v[76:79], a[84:87], v[40:43], v[76:79]\n\t"
      "s_add_u32 m0, s74, 81920\n\t"
      "s_mov_b32 s78, 1048576\n\t"
      "s_mov_b64 exec, s[88:89]\n\t"
      "buffer_load_dwordx4 v116, s[64:67], s78 offen lds\n\t"
      "s_mov_b64 exec, -1\n\t"
      "s_waitcnt lgkmcnt(4)\n\t"
      "v_mfma_f32_16x16x32_bf16 v[72:75], a[88:91], v[44:47], v[72:75]\n\t"
      "v_mfma_f32_16x16x32_bf16 v[76:79], a[92:95], v[44:47], v[76:79]\n\t"
      "s_add_u32 m0, s76, 4096\n\t"
      "s_mov_b64 exec, s[36:37]\n\t"
      "buffer_load_dwordx4 v97, s[60:63], s72 offen offset:3072 sc1 lds\n\t"
      "s_mov_b64 exec, -1\n\t"
      "s_waitcnt lgkmcnt(3)\n\t"
      "v_mfma_f32_16x16x32_bf16 v[72:75], a[96:99], v[48:51], v[72:75]\n\t"
      "v_mfma_f32_16x16x32_bf16 v[76:79], a[100:103], v[48:51], v[76:79]\n\t"
      "s_add_u32 m0, s74, 86016\n\t"
      "s_mov_b32 s78, 1310720\n\t"
      "s_mov_b64 exec, s[88:89]\n\t"
      "buffer_load_dwordx4 v116, s[64:67], s78 offen lds\n\t"
      "s_mov_b64 exec, -1\n\t"
      "s_waitcnt lgkmcnt(2)\n\t"
      "v_mfma_f32_16x16x32_bf16 v[72:75], a[104:107], v[52:55], v[72:75]\n\t"
      "v_mfma_f32_16x16x32_bf16 v[76:79], a[108:111], v[52:55], v[76:79]\n\t"
      "s_waitcnt lgkmcnt(1)\n\t"
      "v_mfma_f32_16x16x32_bf16 v[72:75], a[112:115], v[56:59], v[72:75]\n\t"
      "v_mfma_f32_16x16x32_bf16 v[76:79], a[116:119], v[56:59], v[76:79]\n\t"
      "s_add_u32 m0, s74, 90112\n\t"
      "s_mov_b32 s78, 1572864\n\t"
      "s_mov_b64 exec, s[88:89]\n\t"
      "buffer_load_dwordx4 v116, s[64:67], s78 offen lds\n\t"
      "s_mov_b64 exec, -1\n\t"
      "s_waitcnt lgkmcnt(0)\n\t"
      "v_mfma_f32_16x16x32_bf16 v[72:75], a[120:123], v[60:63], v[72:75]\n\t"
      "v_mfma_f32_16x16x32_bf16 v[76:79], a[124:127], v[60:63], v[76:79]\n\t"
      "s_nop 6\n\t"
      "v_cvt_pk_bf16_f32 v84, v72, v73\n\t"
      "v_cvt_pk_bf16_f32 v85, v74, v75\n\t"
      "v_cvt_pk_bf16_f32 v86, v76, v77\n\t"
      "v_cvt_pk_bf16_f32 v87, v78, v79\n\t"
      "buffer_store_dwordx4 v[84:87], v97, s[60:63], s73 offen offset:1024 sc1\n\t"
      "s_cmp_lg_u64 s[88:89], 0\n\t"
      "s_cbranch_scc1 GKV1%=\n\t"
      "s_cmp_lg_u64 s[36:37], 0\n\t"
      "s_cbranch_scc1 GL2%=\n\t"
      "GL0%=:\n\t"
      "buffer_load_dword v128, v106, s[60:63], s79 offen sc1\n\t"
      "buffer_load_dword v129, v106, s[60:63], s79 offen offset:128 sc1\n\t"
      "s_sub_u32 s81, s81, 1\n\t"
      "s_waitcnt vmcnt(0)\n\t"
      "v_bitop3_b32 v128, v128, v129, %3 bitop3:0x7e\n\t"
      "v_cmp_ne_u32 vcc, 0, v128\n\t"
      "s_cmp_eq_u32 s81, 0\n\t"
      "s_cbranch_scc1 GL1%=\n\t"
      "s_cbranch_vccnz GL0%=\n\t"
      "GL1%=:\n\t"
      "v_readlane_b32 s72, v101, 1\n\t"
      "s_add_u32 s76, s85, 0\n\t"
      "s_add_u32 m0, s76, 0\n\t"
      "s_nop 2\n\t"
      "buffer_load_dwordx4 v97, s[60:63], s72 offen sc1 lds\n\t"
      "buffer_load_dwordx4 v97, s[60:63], s72 offen offset:1024 sc1 lds\n\t"
      "buffer_load_dwordx4 v97, s[60:63], s72 offen offset:2048 sc1 lds\n\t"
      "buffer_load_dwordx4 v97, s[60:63], s72 offen offset:3072 sc1 lds\n\t"
      "s_add_u32 m0, s76, 4096\n\t"
      "s_add_u32 s72, s72, 4096\n\t"
      "buffer_load_dwordx4 v97, s[60:63], s72 offen sc1 lds\n\t"
      "buffer_load_dwordx4 v97, s[60:63], s72 offen offset:1024 sc1 lds\n\t"
      "buffer_load_dwordx4 v97, s[60:63], s72 offen offset:2048 sc1 lds\n\t"
      "buffer_load_dwordx4 v97, s[60:63], s72 offen offset:3072 sc1 lds\n\t"
      "s_waitcnt vmcnt(0)\n\t"
      "GL2%=:\n\t"
      "s_mov_b32 s77, s73\n\t"
      "v_readlane_b32 s73, v102, 1\n\t"
      "v_mov_b32 v98, v97\n\t"
      "s_mov_b64 s[88:89], -1\n\t"
      "s_mov_b64 s[38:39], s[90:91]\n\t"
      "s_mov_b64 s[36:37], -1\n\t"
      "v_readlane_b32 s72, v101, 2\n\t"
      "s_add_u32 s76, s85, 32768\n\t"
      "s_waitcnt vmcnt(1)\n\t"
      "s_barrier\n\t"
      "s_branch GKV%=\n\t"
      "GKV1%=:\n\t"
      "v_readlane_b32 s75, v103, 1\n\t"
      "v_add_u32 v117, 0x10000, v117\n\t"
      "v_add_u32 v98, 131072, v97\n\t"
      "s_mov_b32 s68, 0\n\t"
      "s_add_u32 m0, s74, 94208\n\t"
      "s_mov_b32 s78, 1835008\n\t"
      "buffer_load_dwordx4 v116, s[64:67], s78 offen lds\n\t"
      "s_add_u32 m0, s74, 98304\n\t"
      "s_mov_b32 s78, 2097152\n\t"
      "buffer_load_dwordx4 v116, s[64:67], s78 offen lds\n\t"
      "s_add_u32 m0, s74, 102400\n\t"
      "s_mov_b32 s78, 2359296\n\t"
      "buffer_load_dwordx4 v116, s[64:67], s78 offen lds\n\t"
      "s_add_u32 m0, s74, 106496\n\t"
      "s_mov_b32 s78, 2621440\n\t"
      "buffer_load_dwordx4 v116, s[64:67], s78 offen lds\n\t"
      "s_add_u32 m0, s74, 110592\n\t"
      "s_mov_b32 s78, 2883584\n\t"
      "buffer_load_dwordx4 v116, s[64:67], s78 offen lds\n\t"
      "s_add_u32 m0, s74, 114688\n\t"
      "s_mov_b32 s78, 3145728\n\t"
      "buffer_load_dwordx4 v116, s[64:67], s78 offen lds\n\t"
      "s_add_u32 m0, s74, 118784\n\t"
      "s_mov_b32 s78, 3407872\n\t"
      "buffer_load_dwordx4 v116, s[64:67], s78 offen lds\n\t"
      "s_add_u32 m0, s74, 122880\n\t"
      "s_mov_b32 s78, 3670016\n\t"
      "buffer_load_dwordx4 v116, s[64:67], s78 offen lds\n\t"
      "s_add_u32 m0, s74, 126976\n\t"
      "s_mov_b32 s78, 3932160\n\t"
      "buffer_load_dwordx4 v116, s[64:67], s78 offen lds\n\t"
      "s_waitcnt vmcnt(9)\n\t"
      "s_barrier\n\t"
      "s_mov_b64 exec, s[90:91]\n\t"
      "buffer_store_dword v107, off, s[60:63], s75 sc1\n\t"
      "s_mov_b64 exec, -1\n\t"
      "s_waitcnt vmcnt(0)\n\t"
      "s_barrier\n\t"
      "s_waitcnt lgkmcnt(12)\n\t"
      "ds_read_b64_tr_b16 a[128:129], v117\n\t"
      "ds_read_b64_tr_b16 a[130:131], v117 offset:512\n\t"
      "s_waitcnt lgkmcnt(12)\n\t"
      "ds_read_b64_tr_b16 a[132:133], v117 offset:4096\n\t"
      "ds_read_b64_tr_b16 a[134:135], v117 offset:4608\n\t"
      "s_waitcnt lgkmcnt(12)\n\t"
      "ds_read_b64_tr_b16 a[136:137], v117 offset:8192\n\t"
      "ds_read_b64_tr_b16 a[138:139], v117 offset:8704\n\t"
      "s_waitcnt lgkmcnt(12)\n\t"
      "ds_read_b64_tr_b16 a[140:141], v117 offset:12288\n\t"
      "ds_read_b64_tr_b16 a[142:143], v117 offset:12800\n\t"
      "s_waitcnt lgkmcnt(12)\n\t"
      "ds_read_b64_tr_b16 a[144:145], v117 offset:16384\n\t"
      "ds_read_b64_tr_b16 a[146:147], v117 offset:16896\n\t"
      "s_waitcnt lgkmcnt(12)\n\t"
      "ds_read_b64_tr_b16 a[148:149], v117 offset:20480\n\t"
      "ds_read_b64_tr_b16 a[150:151], v117 offset:20992\n\t"
      "s_waitcnt lgkmcnt(12)\n\t"
      "ds_read_b64_tr_b16 a[152:153], v117 offset:24576\n\t"
      "ds_read_b64_tr_b16 a[154:155], v117 offset:25088\n\t"
      "s_waitcnt lgkmcnt(12)\n\t"
      "ds_read_b64_tr_b16 a[156:157], v117 offset:28672\n\t"
      "ds_read_b64_tr_b16 a[158:159], v117 offset:29184\n\t"
      "s_waitcnt lgkmcnt(12)\n\t"
      "ds_read_b64_tr_b16 a[160:161], v117 offset:32768\n\t"
      "ds_read_b64_tr_b16 a[162:163], v117 offset:33280\n\t"
      "s_waitcnt lgkmcnt(12)\n\t"
      "ds_read_b64_tr_b16 a[164:165], v117 offset:36864\n\t"
      "ds_read_b64_tr_b16 a[166:167], v117 offset:37376\n\t"
      "s_waitcnt lgkmcnt(12)\n\t"
      "ds_read_b64_tr_b16 a[168:169], v117 offset:40960\n\t"
      "ds_read_b64_tr_b16 a[170:171], v117 offset:41472\n\t"
      "s_waitcnt lgkmcnt(12)\n\t"
      "ds_read_b64_tr_b16 a[172:173], v117 offset:45056\n\t"
      "ds_read_b64_tr_b16 a[174:175], v117 offset:45568\n\t"
      "s_waitcnt lgkmcnt(12)\n\t"
      "ds_read_b64_tr_b16 a[176:177], v117 offset:49152\n\t"
      "ds_read_b64_tr_b16 a[178:179], v117 offset:49664\n\t"
      "s_waitcnt lgkmcnt(12)\n\t"
      "ds_read_b64_tr_b16 a[180:181], v117 offset:53248\n\t"
      "ds_read_b64_tr_b16 a[182:183], v117 offset:53760\n\t"
      "s_waitcnt lgkmcnt(12)\n\t"
      "ds_read_b64_tr_b16 a[184:185], v117 offset:57344\n\t"
      "ds_read_b64_tr_b16 a[186:187], v117 offset:57856\n\t"
      "s_waitcnt lgkmcnt(12)\n\t"
      "ds_read_b64_tr_b16 a[188:189], v117 offset:61440\n\t"
      "ds_read_b64_tr_b16 a[190:191], v117 offset:61952\n\t"
      "s_waitcnt lgkmcnt(4)\n\t"
      "GQ%=:\n\t"
      "ds_read_b128 v[0:3], v98\n\t"
      "ds_read_b128 v[4:7], v98 offset:1024\n\t"
      "ds_read_b128 v[8:11], v98 offset:2048\n\t"
      "ds_read_b128 v[12:15], v98 offset:3072\n\t"
      "ds_read_b128 v[16:19], v98 offset:4096\n\t"
      "ds_read_b128 v[20:23], v98 offset:5120\n\t"
      "ds_read_b128 v[24:27], v98 offset:6144\n\t"
      "ds_read_b128 v[28:31], v98 offset:7168\n\t"
      "ds_read_b128 v[32:35], v98 offset:8192\n\t"
      "ds_read_b128 v[36:39], v98 offset:9216\n\t"
      "s_waitcnt lgkmcnt(9)\n\t"
      "v_mfma_f32_16x16x32_bf16 v[88:91], a[128:131], v[0:3], 0\n\t"
      "ds_read_b128 v[40:43], v98 offset:10240\n\t"
      "s_waitcnt lgkmcnt(9)\n\t"
      "v_mfma_f32_16x16x32_bf16 v[88:91], a[132:135], v[4:7], v[88:91]\n\t"
      "ds_read_b128 v[44:47], v98 offset:11264\n\t"
      "s_waitcnt lgkmcnt(9)\n\t"
      "v_mfma_f32_16x16x32_bf16 v[88:91], a[136:139], v[8:11], v[88:91]\n\t"
      "ds_read_b128 v[48:51], v98 offset:12288\n\t"
      "s_waitcnt lgkmcnt(9)\n\t"
      "v_mfma_f32_16x16x32_bf16 v[88:91], a[140:143], v[12:15], v[88:91]\n\t"
      "ds_read_b128 v[52:55], v98 offset:13312\n\t"
      "s_waitcnt lgkmcnt(9)\n\t"
      "v_mfma_f32_16x16x32_bf16 v[88:91], a[144:147], v[16:19], v[88:91]\n\t"
      "ds_read_b128 v[56:59], v98 offset:14336\n\t"
      "s_waitcnt lgkmcnt(9)\n\t"
      "v_mfma_f32_16x16x32_bf16 v[88:91], a[148:151], v[20:23], v[88:91]\n\t"
      "ds_read_b128 v[60:63], v98 offset:15360\n\t"
      "s_waitcnt lgkmcnt(9)\n\t"
      "v_mfma_f32_16x16x32_bf16 v[88:91], a[152:155], v[24:27], v[88:91]\n\t"
      "ds_read_b128 v[0:3], v98 offset:16384\n\t"
      "s_waitcnt lgkmcnt(9)\n\t"
      "v_mfma_f32_16x16x32_bf16 v[88:91], a[156:159], v[28:31], v[88:91]\n\t"
      "ds_read_b128 v[4:7], v98 offset:17408\n\t"
      "s_waitcnt lgkmcnt(9)\n\t"
      "v_mfma_f32_16x16x32_bf16 v[88:91], a[160:163], v[32:35], v[88:91]\n\t"
      "ds_read_b128 v[8:11], v98 offset:18432\n\t"
      "s_waitcnt lgkmcnt(9)\n\t"
      "v_mfma_f32_16x16x32_bf16 v[88:91], a[164:167], v[36:39], v[88:91]\n\t"
      "ds_read_b128 v[12:15], v98 offset:19456\n\t"
      "s_waitcnt lgkmcnt(9)\n\t"
      "v_mfma_f32_16x16x32_bf16 v[88:91], a[168:171], v[40:43], v[88:91]\n\t"
      "ds_read_b128 v[16:19], v98 offset:20480\n\t"
      "s_waitcnt lgkmcnt(9)\n\t"
      "v_mfma_f32_16x16x32_bf16 v[88:91], a[172:175], v[44:47], v[88:91]\n\t"
      "ds_read_b128 v[20:23], v98 offset:21504\n\t"
      "s_waitcnt lgkmcnt(9)\n\t"
      "v_mfma_f32_16x16x32_bf16 v[88:91], a[176:179], v[48:51], v[88:91]\n\t"
      "ds_read_b128 v[24:27], v98 offset:22528\n\t"
      "s_waitcnt lgkmcnt(9)\n\t"
      "v_mfma_f32_16x16x32_bf16 v[88:91], a[180:183], v[52:55], v[88:91]\n\t"
      "ds_read_b128 v[28:31], v98 offset:23552\n\t"
      "s_waitcnt lgkmcnt(9)\n\t"
      "v_mfma_f32_16x16x32_bf16 v[88:91], a[184:187], v[56:59], v[88:91]\n\t"
      "ds_read_b128 v[32:35], v98 offset:24576\n\t"
      "s_waitcnt lgkmcnt(9)\n\t"
      "v_mfma_f32_16x16x32_bf16 v[88:91], a[188:191], v[60:63], v[88:91]\n\t"
      "ds_read_b128 v[36:39], v98 offset:25600\n\t"
      "s_waitcnt lgkmcnt(9)\n\t"
      "v_mfma_f32_16x16x32_bf16 v[92:95], a[128:131], v[0:3], 0\n\t"
      "ds_read_b128 v[40:43], v98 offset:26624\n\t"
      "s_waitcnt lgkmcnt(9)\n\t"
      "v_mfma_f32_16x16x32_bf16 v[92:95], a[132:135], v[4:7], v[92:95]\n\t"
      "ds_read_b128 v[44:47], v98 offset:27648\n\t"
      "s_waitcnt lgkmcnt(9)\n\t"
      "v_mfma_f32_16x16x32_bf16 v[92:95], a[136:139], v[8:11], v[92:95]\n\t"
      "ds_read_b128 v[48:51], v98 offset:28672\n\t"
      "s_waitcnt lgkmcnt(9)\n\t"
      "v_mfma_f32_16x16x32_bf16 v[92:95], a[140:143], v[12:15], v[92:95]\n\t"
      "ds_read_b128 v[52:55], v98 offset:29696\n\t"
      "s_waitcnt lgkmcnt(9)\n\t"
      "v_mfma_f32_16x16x32_bf16 v[92:95], a[144:147], v[16:19], v[92:95]\n\t"
      "ds_read_b128 v[56:59], v98 offset:30720\n\t"
      "s_waitcnt lgkmcnt(9)\n\t"
      "v_mfma_f32_16x16x32_bf16 v[92:95], a[148:151], v[20:23], v[92:95]\n\t"
      "ds_read_b128 v[60:63], v98 offset:31744\n\t"
      "s_waitcnt lgkmcnt(9)\n\t"
      "v_mfma_f32_16x16x32_bf16 v[92:95], a[152:155], v[24:27], v[92:95]\n\t"
      "s_waitcnt lgkmcnt(8)\n\t"
      "v_mfma_f32_16x16x32_bf16 v[92:95], a[156:159], v[28:31], v[92:95]\n\t"
      "s_waitcnt lgkmcnt(7)\n\t"
      "v_mfma_f32_16x16x32_bf16 v[92:95], a[160:163], v[32:35], v[92:95]\n\t"
      "s_waitcnt lgkmcnt(6)\n\t"
      "v_mfma_f32_16x16x32_bf16 v[92:95], a[164:167], v[36:39], v[92:95]\n\t"
      "s_waitcnt lgkmcnt(5)\n\t"
      "v_mfma_f32_16x16x32_bf16 v[92:95], a[168:171], v[40:43], v[92:95]\n\t"
      "s_waitcnt lgkmcnt(4)\n\t"
      "v_mfma_f32_16x16x32_bf16 v[92:95], a[172:175], v[44:47], v[92:95]\n\t"
      "s_waitcnt lgkmcnt(3)\n\t"
      "v_mfma_f32_16x16x32_bf16 v[92:95], a[176:179], v[48:51], v[92:95]\n\t"
      "s_waitcnt lgkmcnt(2)\n\t"
      "v_mfma_f32_16x16x32_bf16 v[92:95], a[180:183], v[52:55], v[92:95]\n\t"
      "s_waitcnt lgkmcnt(1)\n\t"
      "v_mfma_f32_16x16x32_bf16 v[92:95], a[184:187], v[56:59], v[92:95]\n\t"
      "s_waitcnt lgkmcnt(0)\n\t"
      "v_mfma_f32_16x16x32_bf16 v[92:95], a[188:191], v[60:63], v[92:95]\n\t"
      "v_cvt_pk_bf16_f32 v112, v88, v89\n\t"
      "v_cvt_pk_bf16_f32 v113, v90, v91\n\t"
      "s_cmp_lg_u32 s68, 0\n\t"
      "s_barrier\n\t"
      "s_cbranch_scc1 GQ1%=\n\t"
      "s_nop 3\n\t"
      "v_cvt_pk_bf16_f32 v114, v92, v93\n\t"
      "v_cvt_pk_bf16_f32 v115, v94, v95\n\t"
      "s_nop 0\n\t"
      "v_permlane16_swap_b32 v112, v114\n\t"
      "v_permlane16_swap_b32 v113, v115\n\t"
      "buffer_store_dwordx4 v[112:115], v111, s[60:63], s77 offen sc1\n\t"
      "v_readlane_b32 s72, v101, 3\n\t"
      "s_add_u32 s76, s85, 65536\n\t"
      "s_add_u32 m0, s76, 0\n\t"
      "s_nop 2\n\t"
      "buffer_load_dwordx4 v97, s[60:63], s72 offen sc1 lds\n\t"
      "buffer_load_dwordx4 v97, s[60:63], s72 offen offset:1024 sc1 lds\n\t"
      "buffer_load_dwordx4 v97, s[60:63], s72 offen offset:2048 sc1 lds\n\t"
      "buffer_load_dwordx4 v97, s[60:63], s72 offen offset:3072 sc1 lds\n\t"
      "s_add_u32 m0, s76, 4096\n\t"
      "s_add_u32 s72, s72, 4096\n\t"
      "buffer_load_dwordx4 v97, s[60:63], s72 offen sc1 lds\n\t"
      "buffer_load_dwordx4 v97, s[60:63], s72 offen offset:1024 sc1 lds\n\t"
      "buffer_load_dwordx4 v97, s[60:63], s72 offen offset:2048 sc1 lds\n\t"
      "buffer_load_dwordx4 v97, s[60:63], s72 offen offset:3072 sc1 lds\n\t"
      "v_readlane_b32 s72, v101, 4\n\t"
      "s_add_u32 s76, s85, 98304\n\t"
      "s_add_u32 m0, s76, 0\n\t"
      "s_nop 2\n\t"
      "buffer_load_dwordx4 v97, s[60:63], s72 offen sc1 lds\n\t"
      "buffer_load_dwordx4 v97, s[60:63], s72 offen offset:1024 sc1 lds\n\t"
      "buffer_load_dwordx4 v97, s[60:63], s72 offen offset:2048 sc1 lds\n\t"
      "buffer_load_dwordx4 v97, s[60:63], s72 offen offset:3072 sc1 lds\n\t"
      "s_add_u32 m0, s76, 4096\n\t"
      "s_add_u32 s72, s72, 4096\n\t"
      "buffer_load_dwordx4 v97, s[60:63], s72 offen sc1 lds\n\t"
      "buffer_load_dwordx4 v97, s[60:63], s72 offen offset:1024 sc1 lds\n\t"
      "buffer_load_dwordx4 v97, s[60:63], s72 offen offset:2048 sc1 lds\n\t"
      "buffer_load_dwordx4 v97, s[60:63], s72 offen offset:3072 sc1 lds\n\t"
      "v_mov_b32 v98, v97\n\t"
      "s_mov_b32 s68, 2\n\t"
      "s_branch GQ%=\n\t"
      "GQ1%=:\n\t"
      "s_mov_b32 s69, 5\n\t"
      "s_mov_b32 s70, 63\n\t"
      "s_waitcnt vmcnt(8)\n\t"
      "s_mov_b32 s77, 0x8000\n\t"
      "s_mov_b32 s71, 3\n\t"
      "s_getpc_b64 s[88:89]\n\t"
      "GPC%=:\n\t"
      "s_add_u32 s88, s88, GJOBS%=-GPC%=\n\t"
      "s_addc_u32 s89, s89, 0\n\t"
      "v_add_u32 v98, s77, v97\n\t"
      "s_mov_b32 s76, s85\n\t"
      "ds_read_b128 v[0:3], v98\n\t"
      "ds_read_b128 v[4:7], v98 offset:1024\n\t"
      "ds_read_b128 v[8:11], v98 offset:2048\n\t"
      "ds_read_b128 v[12:15], v98 offset:3072\n\t"
      "ds_read_b128 v[16:19], v98 offset:4096\n\t"
      "ds_read_b128 v[20:23], v98 offset:5120\n\t"
      "ds_read_b128 v[24:27], v98 offset:6144\n\t"
      "ds_read_b128 v[28:31], v98 offset:7168\n\t"
      "ds_read_b128 v[32:35], v98 offset:8192\n\t"
      "ds_read_b128 v[36:39], v98 offset:9216\n\t"
      "GU%=:\n\t"
      "s_cmp_eq_u32 s68, s71\n\t"
      "s_cbranch_scc1 GCALL%=\n\t"
      "GBACK%=:\n\t"
      "s_waitcnt lgkmcnt(9)\n\t"
      "v_mfma_f32_16x16x32_bf16 v[64:67], a[0:3], v[0:3], 0\n\t"
      "ds_read_b128 v[40:43], v98 offset:10240\n\t"
      "v_mfma_f32_16x16x32_bf16 v[68:71], a[4:7], v[0:3], 0\n\t"
      "v_readlane_b32 s72, v101, s69\n\t"
      "v_mfma_f32_16x16x32_bf16 v[88:91], a[128:131], v[0:3], 0\n\t"
      "s_waitcnt lgkmcnt(9)\n\t"
      "v_mfma_f32_16x16x32_bf16 v[64:67], a[8:11], v[4:7], v[64:67]\n\t"
      "ds_read_b128 v[44:47], v98 offset:11264\n\t"
      "v_mfma_f32_16x16x32_bf16 v[68:71], a[12:15], v[4:7], v[68:71]\n\t"
      "v_readlane_b32 s75, v103, s70\n\t"
      "v_mfma_f32_16x16x32_bf16 v[88:91], a[132:135], v[4:7], v[88:91]\n\t"
      "s_waitcnt lgkmcnt(9)\n\t"
      "v_mfma_f32_16x16x32_bf16 v[64:67], a[16:19], v[8:11], v[64:67]\n\t"
      "ds_read_b128 v[48:51], v98 offset:12288\n\t"
      "v_mfma_f32_16x16x32_bf16 v[68:71], a[20:23], v[8:11], v[68:71]\n\t"
      "v_cvt_pk_bf16_f32 v84, v72, v73\n\t"
      "v_cvt_pk_bf16_f32 v85, v74, v75\n\t"
      "v_mfma_f32_16x16x32_bf16 v[88:91], a[136:139], v[8:11], v[88:91]\n\t"
      "v_cvt_pk_bf16_f32 v114, v92, v93\n\t"
      "v_cvt_pk_bf16_f32 v115, v94, v95\n\t"
      "s_waitcnt lgkmcnt(9)\n\t"
      "v_mfma_f32_16x16x32_bf16 v[64:67], a[24:27], v[12:15], v[64:67]\n\t"
      "ds_read_b128 v[52:55], v98 offset:13312\n\t"
      "v_mfma_f32_16x16x32_bf16 v[68:71], a[28:31], v[12:15], v[68:71]\n\t"
      "v_cvt_pk_bf16_f32 v86, v76, v77\n\t"
      "v_cvt_pk_bf16_f32 v87, v78, v79\n\t"
      "v_mfma_f32_16x16x32_bf16 v[88:91], a[140:143], v[12:15], v[88:91]\n\t"
      "v_permlane16_swap_b32 v112, v114\n\t"
      "v_permlane16_swap_b32 v113, v115\n\t"
      "s_waitcnt lgkmcnt(9)\n\t"
      "v_mfma_f32_16x16x32_bf16 v[64:67], a[32:35], v[16:19], v[64:67]\n\t"
      "ds_read_b128 v[56:59], v98 offset:14336\n\t"
      "v_mfma_f32_16x16x32_bf16 v[68:71], a[36:39], v[16:19], v[68:71]\n\t"
      "buffer_store_dwordx4 v[84:87], v97, s[60:63], s73 offen offset:1024 sc1\n\t"
      "buffer_store_dwordx4 v[112:115], v111, s[60:63], s73 offen sc1\n\t"
      "v_mfma_f32_16x16x32_bf16 v[88:91], a[144:147], v[16:19], v[88:91]\n\t"
      "s_add_u32 m0, s76, 0\n\t"
      "buffer_load_dwordx4 v97, s[60:63], s72 offen sc1 lds\n\t"
      "s_waitcnt lgkmcnt(9)\n\t"
      "v_mfma_f32_16x16x32_bf16 v[64:67], a[40:43], v[20:23], v[64:67]\n\t"
      "ds_read_b128 v[60:63], v98 offset:15360\n\t"
      "v_mfma_f32_16x16x32_bf16 v[68:71], a[44:47], v[20:23], v[68:71]\n\t"
      "v_mfma_f32_16x16x32_bf16 v[88:91], a[148:151], v[20:23], v[88:91]\n\t"
      "s_waitcnt lgkmcnt(9)\n\t"
      "v_mfma_f32_16x16x32_bf16 v[64:67], a[48:51], v[24:27], v[64:67]\n\t"
      "ds_read_b128 v[0:3], v98 offset:16384\n\t"
      "v_mfma_f32_16x16x32_bf16 v[68:71], a[52:55], v[24:27], v[68:71]\n\t"
      "v_mfma_f32_16x16x32_bf16 v[88:91], a[152:155], v[24:27], v[88:91]\n\t"
      "s_waitcnt lgkmcnt(9)\n\t"
      "v_mfma_f32_16x16x32_bf16 v[64:67], a[56:59], v[28:31], v[64:67]\n\t"
      "ds_read_b128 v[4:7], v98 offset:17408\n\t"
      "v_mfma_f32_16x16x32_bf16 v[68:71], a[60:63], v[28:31], v[68:71]\n\t"
      "buffer_load_dwordx4 v97, s[60:63], s72 offen offset:1024 sc1 lds\n\t"
      "v_mfma_f32_16x16x32_bf16 v[88:91], a[156:159], v[28:31], v[88:91]\n\t"
      "s_waitcnt lgkmcnt(9)\n\t"
      "v_mfma_f32_16x16x32_bf16 v[64:67], a[64:67], v[32:35], v[64:67]\n\t"
      "ds_read_b128 v[8:11], v98 offset:18432\n\t"
      "v_mfma_f32_16x16x32_bf16 v[68:71], a[68:71], v[32:35], v[68:71]\n\t"
      "v_mfma_f32_16x16x32_bf16 v[88:91], a[160:163], v[32:35], v[88:91]\n\t"
      "s_waitcnt lgkmcnt(9)\n\t"
      "v_mfma_f32_16x16x32_bf16 v[64:67], a[72:75], v[36:39], v[64:67]\n\t"
      "ds_read_b128 v[12:15], v98 offset:19456\n\t"
      "v_mfma_f32_16x16x32_bf16 v[68:71], a[76:79], v[36:39], v[68:71]\n\t"
      "v_readlane_b32 s73, v102, s68\n\t"
      "v_mfma_f32_16x16x32_bf16 v[88:91], a[164:167], v[36:39], v[88:91]\n\t"
      "s_waitcnt lgkmcnt(9)\n\t"
      "v_mfma_f32_16x16x32_bf16 v[64:67], a[80:83], v[40:43], v[64:67]\n\t"
      "ds_read_b128 v[16:19], v98 offset:20480\n\t"
      "v_mfma_f32_16x16x32_bf16 v[68:71], a[84:87], v[40:43], v[68:71]\n\t"
      "buffer_load_dwordx4 v97, s[60:63], s72 offen offset:2048 sc1 lds\n\t"
      "v_mfma_f32_16x16x32_bf16 v[88:91], a[168:171], v[40:43], v[88:91]\n\t"
      "s_waitcnt lgkmcnt(9)\n\t"
      "v_mfma_f32_16x16x32_bf16 v[64:67], a[88:91], v[44:47], v[64:67]\n\t"
      "ds_read_b128 v[20:23], v98 offset:21504\n\t"
      "v_mfma_f32_16x16x32_bf16 v[68:71], a[92:95], v[44:47], v[68:71]\n\t"
      "v_mfma_f32_16x16x32_bf16 v[88:91], a[172:175], v[44:47], v[88:91]\n\t"
      "s_waitcnt lgkmcnt(9)\n\t"
      "v_mfma_f32_16x16x32_bf16 v[64:67], a[96:99], v[48:51], v[64:67]\n\t"
      "ds_read_b128 v[24:27], v98 offset:22528\n\t"
      "v_mfma_f32_16x16x32_bf16 v[68:71], a[100:103], v[48:51], v[68:71]\n\t"
      "v_mfma_f32_16x16x32_bf16 v[88:91], a[176:179], v[48:51], v[88:91]\n\t"
      "s_waitcnt lgkmcnt(9)\n\t"
      "v_mfma_f32_16x16x32_bf16 v[64:67], a[104:107], v[52:55], v[64:67]\n\t"
      "ds_read_b128 v[28:31], v98 offset:23552\n\t"
      "v_mfma_f32_16x16x32_bf16 v[68:71], a[108:111], v[52:55], v[68:71]\n\t"
      "buffer_load_dwordx4 v97, s[60:63], s72 offen offset:3072 sc1 lds\n\t"
      "v_mfma_f32_16x16x32_bf16 v[88:91], a[180:183], v[52:55], v[88:91]\n\t"
      "s_waitcnt lgkmcnt(9)\n\t"
      "v_mfma_f32_16x16x32_bf16 v[64:67], a[112:115], v[56:59], v[64:67]\n\t"
      "ds_read_b128 v[32:35], v98 offset:24576\n\t"
      "v_mfma_f32_16x16x32_bf16 v[68:71], a[116:119], v[56:59], v[68:71]\n\t"
      "v_mfma_f32_16x16x32_bf16 v[88:91], a[184:187], v[56:59], v[88:91]\n\t"
      "s_waitcnt lgkmcnt(9)\n\t"
      "v_mfma_f32_16x16x32_bf16 v[64:67], a[120:123], v[60:63], v[64:67]\n\t"
      "ds_read_b128 v[36:39], v98 offset:25600\n\t"
      "v_mfma_f32_16x16x32_bf16 v[68:71], a[124:127], v[60:63], v[68:71]\n\t"
      "s_add_u32 s68, s68, 1\n\t"
      "v_mfma_f32_16x16x32_bf16 v[88:91], a[188:191], v[60:63], v[88:91]\n\t"
      "s_waitcnt lgkmcnt(9)\n\t"
      "v_mfma_f32_16x16x32_bf16 v[72:75], a[0:3], v[0:3], 0\n\t"
      "ds_read_b128 v[40:43], v98 offset:26624\n\t"
      "v_mfma_f32_16x16x32_bf16 v[76:79], a[4:7], v[0:3], 0\n\t"
      "s_add_u32 m0, s76, 4096\n\t"
      "s_add_u32 s72, s72, 4096\n\t"
      "buffer_load_dwordx4 v97, s[60:63], s72 offen sc1 lds\n\t"
      "v_mfma_f32_16x16x32_bf16 v[92:95], a[128:131], v[0:3], 0\n\t"
      "s_add_u32 s69, s69, 1\n\t"
      "s_add_u32 s70, s70, 1\n\t"
      "s_waitcnt lgkmcnt(9)\n\t"
      "v_mfma_f32_16x16x32_bf16 v[72:75], a[8:11], v[4:7], v[72:75]\n\t"
      "ds_read_b128 v[44:47], v98 offset:27648\n\t"
      "v_mfma_f32_16x16x32_bf16 v[76:79], a[12:15], v[4:7], v[76:79]\n\t"
      "s_and_b32 s70, s70, 63\n\t"
      "s_add_u32 s76, s77, s85\n\t"
      "v_mfma_f32_16x16x32_bf16 v[92:95], a[132:135], v[4:7], v[92:95]\n\t"
      "s_add_u32 s77, s77, 0x8000\n\t"
      "s_and_b32 s77, s77, 0x1ffff\n\t"
      "s_waitcnt lgkmcnt(9)\n\t"
      "v_mfma_f32_16x16x32_bf16 v[72:75], a[16:19], v[8:11], v[72:75]\n\t"
      "ds_read_b128 v[48:51], v98 offset:28672\n\t"
      "v_mfma_f32_16x16x32_bf16 v[76:79], a[20:23], v[8:11], v[76:79]\n\t"
      "v_cvt_pk_bf16_f32 v80, v64, v65\n\t"
      "v_cvt_pk_bf16_f32 v81, v66, v67\n\t"
      "v_mfma_f32_16x16x32_bf16 v[92:95], a[136:139], v[8:11], v[92:95]\n\t"
      "v_cvt_pk_bf16_f32 v112, v88, v89\n\t"
      "s_waitcnt lgkmcnt(9)\n\t"
      "v_mfma_f32_16x16x32_bf16 v[72:75], a[24:27], v[12:15], v[72:75]\n\t"
      "ds_read_b128 v[52:55], v98 offset:29696\n\t"
      "v_mfma_f32_16x16x32_bf16 v[76:79], a[28:31], v[12:15], v[76:79]\n\t"
      "buffer_load_dwordx4 v97, s[60:63], s72 offen offset:1024 sc1 lds\n\t"
      "v_cvt_pk_bf16_f32 v82, v68, v69\n\t"
      "v_mfma_f32_16x16x32_bf16 v[92:95], a[140:143], v[12:15], v[92:95]\n\t"
      "v_cvt_pk_bf16_f32 v83, v70, v71\n\t"
      "v_cvt_pk_bf16_f32 v113, v90, v91\n\t"
      "s_waitcnt lgkmcnt(9)\n\t"
      "v_mfma_f32_16x16x32_bf16 v[72:75], a[32:35], v[16:19], v[72:75]\n\t"
      "ds_read_b128 v[56:59], v98 offset:30720\n\t"
      "v_mfma_f32_16x16x32_bf16 v[76:79], a[36:39], v[16:19], v[76:79]\n\t"
      "buffer_store_dwordx4 v[80:83], v97, s[60:63], s73 offen sc1\n\t"
      "v_mfma_f32_16x16x32_bf16 v[92:95], a[144:147], v[16:19], v[92:95]\n\t"
      "s_waitcnt lgkmcnt(9)\n\t"
      "v_mfma_f32_16x16x32_bf16 v[72:75], a[40:43], v[20:23], v[72:75]\n\t"
      "ds_read_b128 v[60:63], v98 offset:31744\n\t"
      "v_mfma_f32_16x16x32_bf16 v[76:79], a[44:47], v[20:23], v[76:79]\n\t"
      "v_mfma_f32_16x16x32_bf16 v[92:95], a[148:151], v[20:23], v[92:95]\n\t"
      "s_waitcnt lgkmcnt(9)\n\t"
      "v_mfma_f32_16x16x32_bf16 v[72:75], a[48:51], v[24:27], v[72:75]\n\t"
      "v_mfma_f32_16x16x32_bf16 v[76:79], a[52:55], v[24:27], v[76:79]\n\t"
      "buffer_load_dwordx4 v97, s[60:63], s72 offen offset:2048 sc1 lds\n\t"
      "v_add_u32 v98, s77, v97\n\t"
      "v_mfma_f32_16x16x32_bf16 v[92:95], a[152:155], v[24:27], v[92:95]\n\t"
      "s_cmp_lg_u32 s75, 0\n\t"
      "s_waitcnt lgkmcnt(8)\n\t"
      "v_mfma_f32_16x16x32_bf16 v[72:75], a[56:59], v[28:31], v[72:75]\n\t"
      "v_mfma_f32_16x16x32_bf16 v[76:79], a[60:63], v[28:31], v[76:79]\n\t"
      "s_cselect_b64 vcc, s[90:91], 0\n\t"
      "v_mfma_f32_16x16x32_bf16 v[92:95], a[156:159], v[28:31], v[92:95]\n\t"
      "s_waitcnt lgkmcnt(7)\n\t"
      "v_mfma_f32_16x16x32_bf16 v[72:75], a[64:67], v[32:35], v[72:75]\n\t"
      "v_mfma_f32_16x16x32_bf16 v[76:79], a[68:71], v[32:35], v[76:79]\n\t"
      "buffer_load_dwordx4 v97, s[60:63], s72 offen offset:3072 sc1 lds\n\t"
      "v_mfma_f32_16x16x32_bf16 v[92:95], a[160:163], v[32:35], v[92:95]\n\t"
      "s_waitcnt lgkmcnt(6)\n\t"
      "v_mfma_f32_16x16x32_bf16 v[72:75], a[72:75], v[36:39], v[72:75]\n\t"
      "v_mfma_f32_16x16x32_bf16 v[76:79], a[76:79], v[36:39], v[76:79]\n\t"
      "v_mfma_f32_16x16x32_bf16 v[92:95], a[164:167], v[36:39], v[92:95]\n\t"
      "s_waitcnt vmcnt(22) lgkmcnt(0)\n\t"
      "s_barrier\n\t"
      "v_mfma_f32_16x16x32_bf16 v[72:75], a[80:83], v[40:43], v[72:75]\n\t"
      "ds_read_b128 v[0:3], v98\n\t"
      "v_mfma_f32_16x16x32_bf16 v[76:79], a[84:87], v[40:43], v[76:79]\n\t"
      "ds_read_b128 v[4:7], v98 offset:1024\n\t"
      "v_mfma_f32_16x16x32_bf16 v[92:95], a[168:171], v[40:43], v[92:95]\n\t"
      "v_mfma_f32_16x16x32_bf16 v[72:75], a[88:91], v[44:47], v[72:75]\n\t"
      "ds_read_b128 v[8:11], v98 offset:2048\n\t"
      "v_mfma_f32_16x16x32_bf16 v[76:79], a[92:95], v[44:47], v[76:79]\n\t"
      "ds_read_b128 v[12:15], v98 offset:3072\n\t"
      "v_mfma_f32_16x16x32_bf16 v[92:95], a[172:175], v[44:47], v[92:95]\n\t"
      "v_mfma_f32_16x16x32_bf16 v[72:75], a[96:99], v[48:51], v[72:75]\n\t"
      "ds_read_b128 v[16:19], v98 offset:4096\n\t"
      "v_mfma_f32_16x16x32_bf16 v[76:79], a[100:103], v[48:51], v[76:79]\n\t"
      "ds_read_b128 v[20:23], v98 offset:5120\n\t"
      "v_mfma_f32_16x16x32_bf16 v[92:95], a[176:179], v[48:51], v[92:95]\n\t"
      "v_mfma_f32_16x16x32_bf16 v[72:75], a[104:107], v[52:55], v[72:75]\n\t"
      "ds_read_b128 v[24:27], v98 offset:6144\n\t"
      "v_mfma_f32_16x16x32_bf16 v[76:79], a[108:111], v[52:55], v[76:79]\n\t"
      "ds_read_b128 v[28:31], v98 offset:7168\n\t"
      "v_mfma_f32_16x16x32_bf16 v[92:95], a[180:183], v[52:55], v[92:95]\n\t"
      "v_mfma_f32_16x16x32_bf16 v[72:75], a[112:115], v[56:59], v[72:75]\n\t"
      "ds_read_b128 v[32:35], v98 offset:8192\n\t"
      "v_mfma_f32_16x16x32_bf16 v[76:79], a[116:119], v[56:59], v[76:79]\n\t"
      "ds_read_b128 v[36:39], v98 offset:9216\n\t"
      "v_mfma_f32_16x16x32_bf16 v[92:95], a[184:187], v[56:59], v[92:95]\n\t"
      "s_cbranch_vccnz GFLAG%=\n\t"
      "GFB%=:\n\t"
      "v_mfma_f32_16x16x32_bf16 v[72:75], a[120:123], v[60:63], v[72:75]\n\t"
      "v_mfma_f32_16x16x32_bf16 v[76:79], a[124:127], v[60:63], v[76:79]\n\t"
      "v_mfma_f32_16x16x32_bf16 v[92:95], a[188:191], v[60:63], v[92:95]\n\t"
      "s_branch GU%=\n\t"
      "GFLAG%=:\n\t"
      "s_mov_b64 exec, 1\n\t"
      "buffer_store_dword v107, off, s[60:63], s75 sc1\n\t"
      "s_mov_b64 exec, -1\n\t"
      "s_branch GFB%=\n\t"
      "GCALL%=:\n\t"
      "s_setpc_b64 s[88:89]\n\t"
      "GJOBS%=:\n\t"
      "s_nop 7\n\t"
      "v_cvt_pk_bf16_f32 v84, v72, v73\n\t"
      "v_cvt_pk_bf16_f32 v85, v74, v75\n\t"
      "v_cvt_pk_bf16_f32 v86, v76, v77\n\t"
      "v_cvt_pk_bf16_f32 v87, v78, v79\n\t"
      "buffer_store_dwordx4 v[84:87], v97, s[60:63], s73 offen offset:1024 sc1\n\t"
      "v_readlane_b32 s75, v103, 2\n\t"
      "s_waitcnt vmcnt(0)\n\t"
      "s_barrier\n\t"
      "s_mov_b64 exec, s[90:91]\n\t"
      "s_nop 1\n\t"
      "buffer_store_dword v107, off, s[60:63], s75 sc1\n\t"
      "s_mov_b64 exec, -1\n\t"
      "s_mov_b32 s71, 5\n\t"
      "s_getpc_b64 s[88:89]\n\t"
      "s_add_u32 s88, s88, 12\n\t"
      "s_addc_u32 s89, s89, 0\n\t"
      "s_branch GBACK%=\n\t"
      "s_nop 7\n\t"
      "v_readlane_b32 s75, v103, 3\n\t"
      "s_waitcnt vmcnt(9)\n\t"
      "s_barrier\n\t"
      "s_mov_b64 exec, s[90:91]\n\t"
      "s_nop 1\n\t"
      "buffer_store_dword v107, off, s[60:63], s75 sc1\n\t"
      "s_mov_b64 exec, -1\n\t"
      "s_mov_b32 s71, 6\n\t"
      "s_getpc_b64 s[88:89]\n\t"
      "s_add_u32 s88, s88, 12\n\t"
      "s_addc_u32 s89, s89, 0\n\t"
      "s_branch GBACK%=\n\t"
      "s_bitcmp0_b32 %0, 1\n\t"
      "s_cselect_b64 s[36:37], -1, 0\n\t"
      "s_cmp_lt_u32 s82, 28\n\t"
      "s_cselect_b64 s[38:39], -1, 0\n\t"
      "s_min_u32 s74, s82, 27\n\t"
      "v_lshrrev_b32 v104, 3, v96\n\t"
      "v_and_b32 v105, 7, v96\n\t"
      "s_mov_b32 s78, 3584\n\t"
      "v_mul_u32_u24 v104, s78, v104\n\t"
      "v_lshl_add_u32 v104, v105, 4, v104\n\t"
      "s_lshr_b32 s78, %0, 5\n\t"
      "s_lshl_b32 s78, s78, 2\n\t"
      "s_add_u32 s78, s78, %1\n\t"
      "s_mul_i32 s78, s78, 114688\n\t"
      "s_lshl_b32 s80, s74, 7\n\t"
      "s_add_u32 s80, s80, s78\n\t"
      "s_mov_b64 s[64:65], s[50:51]\n\t"
      "s_add_u32 s78, s80, 0\n\t"
      "buffer_load_dwordx4 v[128:131], v104, s[64:67], s78 offen \n\t"
      "s_add_u32 s78, s80, 28672\n\t"
      "buffer_load_dwordx4 v[132:135], v104, s[64:67], s78 offen \n\t"
      "s_add_u32 s78, s80, 57344\n\t"
      "buffer_load_dwordx4 v[136:139], v104, s[64:67], s78 offen \n\t"
      "s_add_u32 s71, s71, 1\n\t"
      "s_getpc_b64 s[88:89]\n\t"
      "s_add_u32 s88, s88, 12\n\t"
      "s_addc_u32 s89, s89, 0\n\t"
      "s_branch GBACK%=\n\t"
      "s_add_u32 s78, s80, 86016\n\t"
      "buffer_load_dwordx4 v[140:143], v104, s[64:67], s78 offen \n\t"
      "s_mov_b64 s[64:65], s[52:53]\n\t"
      "s_add_u32 s78, s80, 0\n\t"
      "buffer_load_dwordx4 v[144:147], v104, s[64:67], s78 offen \n\t"
      "s_add_u32 s78, s80, 28672\n\t"
      "buffer_load_dwordx4 v[148:151], v104, s[64:67], s78 offen \n\t"
      "s_add_u32 s71, s71, 1\n\t"
      "s_getpc_b64 s[88:89]\n\t"
      "s_add_u32 s88, s88, 12\n\t"
      "s_addc_u32 s89, s89, 0\n\t"
      "s_branch GBACK%=\n\t"
      "s_add_u32 s78, s80, 57344\n\t"
      "buffer_load_dwordx4 v[152:155], v104, s[64:67], s78 offen \n\t"
      "s_add_u32 s78, s80, 86016\n\t"
      "buffer_load_dwordx4 v[156:159], v104, s[64:67], s78 offen \n\t"
      "v_mov_b32 v120, -1\n\t"
      "v_mov_b32 v121, -1\n\t"
      "v_mov_b32 v122, -1\n\t"
      "v_mov_b32 v123, -1\n\t"
      "s_lshr_b32 s78, %0, 5\n\t"
      "s_lshl_b32 s78, s78, 2\n\t"
      "s_add_u32 s78, s78, %1\n\t"
      "s_lshr_b32 s79, s78, 1\n\t"
      "s_lshl_b32 s79, s79, 16\n\t"
      "s_lshl_b32 s80, s82, 11\n\t"
      "s_add_u32 s79, s79, s80\n\t"
      "s_and_b32 s78, s78, 1\n\t"
      "s_lshl_b32 s78, s78, 10\n\t"
      "s_add_u32 s79, s79, s78\n\t"
      "s_add_u32 s79, s79, 66813952\n\t"
      "buffer_store_dwordx4 v[120:123], v97, s[60:63], s79 offen sc1\n\t"
      "s_add_u32 s71, s71, 1\n\t"
      "s_getpc_b64 s[88:89]\n\t"
      "s_add_u32 s88, s88, 12\n\t"
      "s_addc_u32 s89, s89, 0\n\t"
      "s_branch GBACK%=\n\t"
      "s_sub_u32 s79, s79, 4194304\n\t"
      "buffer_store_dwordx4 v[120:123], v97, s[60:63], s79 offen sc1\n\t"
      "s_add_u32 s79, s79, 3670016\n\t"
      "buffer_store_dwordx4 v[120:123], v97, s[60:63], s79 offen sc1\n\t"
      "s_sub_u32 s79, s79, 4194304\n\t"
      "buffer_store_dwordx4 v[120:123], v97, s[60:63], s79 offen sc1\n\t"
      "s_add_u32 s71, s71, 1\n\t"
      "s_getpc_b64 s[88:89]\n\t"
      "s_add_u32 s88, s88, 12\n\t"
      "s_addc_u32 s89, s89, 0\n\t"
      "s_branch GBACK%=\n\t"
      "s_add_u32 s79, s79, 3670016\n\t"
      "buffer_store_dwordx4 v[120:123], v97, s[60:63], s79 offen sc1\n\t"
      "s_sub_u32 s79, s79, 4194304\n\t"
      "buffer_store_dwordx4 v[120:123], v97, s[60:63], s79 offen sc1\n\t"
      "s_mov_b64 s[64:65], s[54:55]\n\t"
      "v_lshrrev_b32 v104, 5, v96\n\t"
      "v_and_b32 v105, 31, v96\n\t"
      "v_lshlrev_b32 v104, 10, v104\n\t"
      "v_lshl_add_u32 v104, v105, 4, v104\n\t"
      "s_andn2_b32 s78, %0, 3\n\t"
      "s_add_u32 s78, s78, %1\n\t"
      "s_mul_i32 s78, s78, 14336\n\t"
      "s_and_b32 s79, %0, 1\n\t"
      "s_lshl_b32 s79, s79, 9\n\t"
      "s_add_u32 s78, s78, s79\n\t"
      "s_mov_b64 exec, s[36:37]\n\t"
      "buffer_load_dwordx4 v[160:163], v104, s[64:67], s78 offen \n\t"
      "s_mov_b64 exec, -1\n\t"
      "s_add_u32 s71, s71, 1\n\t"
      "s_getpc_b64 s[88:89]\n\t"
      "s_add_u32 s88, s88, 12\n\t"
      "s_addc_u32 s89, s89, 0\n\t"
      "s_branch GBACK%=\n\t"
      "s_mov_b64 exec, s[36:37]\n\t"
      "buffer_load_dwordx4 v[164:167], v104, s[64:67], s78 offen offset:2048 \n\t"
      "s_add_u32 s78, s78, 4096\n\t"
      "buffer_load_dwordx4 v[168:171], v104, s[64:67], s78 offen \n\t"
      "buffer_load_dwordx4 v[172:175], v104, s[64:67], s78 offen offset:2048 \n\t"
      "s_mov_b64 exec, -1\n\t"
      "s_add_u32 s71, s71, 1\n\t"
      "s_getpc_b64 s[88:89]\n\t"
      "s_add_u32 s88, s88, 12\n\t"
      "s_addc_u32 s89, s89, 0\n\t"
      "s_branch GBACK%=\n\t"
      "s_mov_b64 exec, s[36:37]\n\t"
      "s_add_u32 s78, s78, 4096\n\t"
      "buffer_load_dwordx4 v[176:179], v104, s[64:67], s78 offen \n\t"
      "buffer_load_dwordx4 v[180:183], v104, s[64:67], s78 offen offset:2048 \n\t"
      "s_add_u32 s78, s78, 4096\n\t"
      "buffer_load_dwordx4 v[184:187], v104, s[64:67], s78 offen \n\t"
      "s_mov_b64 exec, -1\n\t"
      "s_add_u32 s71, s71, 1\n\t"
      "s_getpc_b64 s[88:89]\n\t"
      "s_add_u32 s88, s88, 12\n\t"
      "s_addc_u32 s89, s89, 0\n\t"
      "s_branch GBACK%=\n\t"
      "s_mov_b64 exec, s[36:37]\n\t"
      "s_mov_b64 exec, -1\n\t"
      "v_and_b32 v106, 3, v96\n\t"
      "v_mul_u32_u24 v106, 24, v106\n\t"
      "v_lshlrev_b32 v104, 1, v100\n\t"
      "v_sub_u32 v106, v104, v106\n\t"
      "v_add_u32 v104, s76, v97\n\t"
      "v_add_u32 v105, s76, v106\n\t"
      "s_waitcnt vmcnt(30) lgkmcnt(0)\n\t"
      "ds_write_b128 v104, v[128:131]\n\t"
      "ds_write_b128 v104, v[132:135] offset:1024\n\t"
      "ds_write_b128 v104, v[136:139] offset:2048\n\t"
      "ds_write_b128 v104, v[140:143] offset:3072\n\t"
      "ds_write_b128 v104, v[144:147] offset:4096\n\t"
      "ds_write_b128 v104, v[148:151] offset:5120\n\t"
      "ds_write_b128 v104, v[152:155] offset:6144\n\t"
      "ds_write_b128 v104, v[156:159] offset:7168\n\t"
      "s_lshr_b32 s78, %0, 5\n\t"
      "s_lshl_b32 s78, s78, 2\n\t"
      "s_add_u32 s78, s78, %1\n\t"
      "s_lshl_b32 s78, s78, 10\n\t"
      "s_lshl_b32 s79, s74, 17\n\t"
      "s_add_u32 s78, s78, s79\n\t"
      "s_add_u32 s78, s78, 105086976\n\t"
      "s_waitcnt lgkmcnt(6)\n\t"
      "ds_read_b64_tr_b16 v[128:129], v105\n\t"
      "ds_read_b64_tr_b16 v[130:131], v105 offset:512\n\t"
      "ds_read_b64_tr_b16 v[132:133], v105 offset:4096\n\t"
      "ds_read_b64_tr_b16 v[134:135], v105 offset:4608\n\t"
      "ds_read_b64_tr_b16 v[136:137], v105 offset:32\n\t"
      "ds_read_b64_tr_b16 v[138:139], v105 offset:544\n\t"
      "ds_read_b64_tr_b16 v[140:141], v105 offset:4128\n\t"
      "ds_read_b64_tr_b16 v[142:143], v105 offset:4640\n\t"
      "s_waitcnt lgkmcnt(6)\n\t"
      "ds_read_b64_tr_b16 v[144:145], v105 offset:64\n\t"
      "ds_read_b64_tr_b16 v[146:147], v105 offset:576\n\t"
      "ds_read_b64_tr_b16 v[148:149], v105 offset:4160\n\t"
      "ds_read_b64_tr_b16 v[150:151], v105 offset:4672\n\t"
      "ds_read_b64_tr_b16 v[152:153], v105 offset:96\n\t"
      "ds_read_b64_tr_b16 v[154:155], v105 offset:608\n\t"
      "ds_read_b64_tr_b16 v[156:157], v105 offset:4192\n\t"
      "ds_read_b64_tr_b16 v[158:159], v105 offset:4704\n\t"
      "s_waitcnt lgkmcnt(0)\n\t"
      "s_mov_b64 exec, s[38:39]\n\t"
      "buffer_store_dwordx4 v[128:131], v97, s[60:63], s78 offen sc1\n\t"
      "s_add_u32 s78, s78, 16384\n\t"
      "buffer_store_dwordx4 v[132:135], v97, s[60:63], s78 offen sc1\n\t"
      "s_add_u32 s78, s78, 16384\n\t"
      "buffer_store_dwordx4 v[136:139], v97, s[60:63], s78 offen sc1\n\t"
      "s_mov_b64 exec, -1\n\t"
      "s_add_u32 s71, s71, 1\n\t"
      "s_getpc_b64 s[88:89]\n\t"
      "s_add_u32 s88, s88, 12\n\t"
      "s_addc_u32 s89, s89, 0\n\t"
      "s_branch GBACK%=\n\t"
      "s_mov_b64 exec, s[38:39]\n\t"
      "s_add_u32 s78, s78, 16384\n\t"
      "buffer_store_dwordx4 v[140:143], v97, s[60:63], s78 offen sc1\n\t"
      "s_add_u32 s78, s78, 16384\n\t"
      "buffer_store_dwordx4 v[144:147], v97, s[60:63], s78 offen sc1\n\t"
      "s_add_u32 s78, s78, 16384\n\t"
      "buffer_store_dwordx4 v[148:151], v97, s[60:63], s78 offen sc1\n\t"
      "s_mov_b64 exec, -1\n\t"
      "s_add_u32 s71, s71, 1\n\t"
      "s_getpc_b64 s[88:89]\n\t"
      "s_add_u32 s88, s88, 12\n\t"
      "s_addc_u32 s89, s89, 0\n\t"
      "s_branch GBACK%=\n\t"
      "s_mov_b64 exec, s[38:39]\n\t"
      "s_add_u32 s78, s78, 16384\n\t"
      "buffer_store_dwordx4 v[152:155], v97, s[60:63], s78 offen sc1\n\t"
      "s_add_u32 s78, s78, 16384\n\t"
      "buffer_store_dwordx4 v[156:159], v97, s[60:63], s78 offen sc1\n\t"
      "s_add_u32 s78, s78, 16384\n\t"
      "s_mov_b64 exec, -1\n\t"
      "v_lshlrev_b32 v104, 10, v96\n\t"
      "s_and_b32 s78, %0, 1\n\t"
      "s_lshl_b32 s78, s78, 5\n\t"
      "s_lshr_b32 s79, %0, 2\n\t"
      "s_add_u32 s78, s78, s79\n\t"
      "s_lshl_b32 s78, s78, 4\n\t"
      "s_lshl_b32 s79, %1, 16\n\t"
      "s_add_u32 s78, s78, s79\n\t"
      "s_lshl_b32 s79, %1, 10\n\t"
      "s_mov_b64 exec, s[36:37]\n\t"
      "s_add_u32 m0, s79, 131072\n\t"
      "s_nop 0\n\t"
      "buffer_load_dwordx4 v104, s[64:67], s78 offen lds\n\t"
      "s_mov_b64 exec, -1\n\t"
      "s_add_u32 s71, s71, 1\n\t"
      "s_getpc_b64 s[88:89]\n\t"
      "s_add_u32 s88, s88, 12\n\t"
      "s_addc_u32 s89, s89, 0\n\t"
      "s_branch GBACK%=\n\t"
      "s_mov_b64 exec, s[36:37]\n\t"
      "s_add_u32 s78, s78, 0x40000\n\t"
      "s_add_u32 m0, s79, 135168\n\t"
      "s_nop 0\n\t"
      "buffer_load_dwordx4 v104, s[64:67], s78 offen lds\n\t"
      "s_add_u32 s78, s78, 0x40000\n\t"
      "s_add_u32 m0, s79, 139264\n\t"
      "s_nop 0\n\t"
      "buffer_load_dwordx4 v104, s[64:67], s78 offen lds\n\t"
      "s_add_u32 s78, s78, 0x40000\n\t"
      "s_add_u32 m0, s79, 143360\n\t"
      "s_nop 0\n\t"
      "buffer_load_dwordx4 v104, s[64:67], s78 offen lds\n\t"
      "s_mov_b64 exec, -1\n\t"
      "s_add_u32 s71, s71, 1\n\t"
      "s_getpc_b64 s[88:89]\n\t"
      "s_add_u32 s88, s88, 12\n\t"
      "s_addc_u32 s89, s89, 0\n\t"
      "s_branch GBACK%=\n\t"
      "s_mov_b64 exec, s[36:37]\n\t"
      "s_add_u32 s78, s78, 0x40000\n\t"
      "s_add_u32 m0, s79, 147456\n\t"
      "s_nop 0\n\t"
      "buffer_load_dwordx4 v104, s[64:67], s78 offen lds\n\t"
      "s_add_u32 s78, s78, 0x40000\n\t"
      "s_add_u32 m0, s79, 151552\n\t"
      "s_nop 0\n\t"
      "buffer_load_dwordx4 v104, s[64:67], s78 offen lds\n\t"
      "s_add_u32 s78, s78, 0x40000\n\t"
      "s_add_u32 m0, s79, 155648\n\t"
      "s_nop 0\n\t"
      "buffer_load_dwordx4 v104, s[64:67], s78 offen lds\n\t"
      "s_mov_b64 exec, -1\n\t"
      "s_add_u32 s71, s71, 1\n\t"
      "s_getpc_b64 s[88:89]\n\t"
      "s_add_u32 s88, s88, 12\n\t"
      "s_addc_u32 s89, s89, 0\n\t"
      "s_branch GBACK%=\n\t"
      "s_mov_b64 exec, s[36:37]\n\t"
      "s_add_u32 s78, s78, 0x40000\n\t"
      "s_mov_b64 exec, -1\n\t"
      "s_mov_b64 s[64:65], s[46:47]\n\t"
      "v_lshrrev_b32 v99, 2, v96\n\t"
      "v_and_b32 v106, 3, v96\n\t"
      "v_lshlrev_b32 v99, 10, v99\n\t"
      "v_lshl_add_u32 v99, v106, 4, v99\n\t"
      "s_lshr_b32 s78, %0, 4\n\t"
      "s_lshl_b32 s78, s78, 19\n\t"
      "s_bfe_u32 s79, %0, 0x20002\n\t"
      "s_lshl_b32 s79, s79, 8\n\t"
      "s_add_u32 s78, s78, s79\n\t"
      "s_lshl_b32 s79, %1, 6\n\t"
      "s_add_u32 s78, s78, s79\n\t"
      "v_add_u32 v99, s78, v99\n\t"
      "s_mov_b32 s78, 262144\n\t"
      "buffer_load_dwordx4 v[192:195], v99, s[64:67], s78 offen \n\t"
      "s_mov_b32 s78, 278528\n\t"
      "buffer_load_dwordx4 v[196:199], v99, s[64:67], s78 offen \n\t"
      "s_mov_b32 s78, 294912\n\t"
      "buffer_load_dwordx4 v[200:203], v99, s[64:67], s78 offen \n\t"
      "s_add_u32 s71, s71, 1\n\t"
      "s_getpc_b64 s[88:89]\n\t"
      "s_add_u32 s88, s88, 12\n\t"
      "s_addc_u32 s89, s89, 0\n\t"
      "s_branch GBACK%=\n\t"
      "s_mov_b32 s78, 311296\n\t"
      "buffer_load_dwordx4 v[204:207], v99, s[64:67], s78 offen \n\t"
      "s_mov_b32 s78, 327680\n\t"
      "buffer_load_dwordx4 v[208:211], v99, s[64:67], s78 offen \n\t"
      "s_mov_b32 s78, 344064\n\t"
      "buffer_load_dwordx4 v[212:215], v99, s[64:67], s78 offen \n\t"
      "s_add_u32 s71, s71, 1\n\t"
      "s_getpc_b64 s[88:89]\n\t"
      "s_add_u32 s88, s88, 12\n\t"
      "s_addc_u32 s89, s89, 0\n\t"
      "s_branch GBACK%=\n\t"
      "s_mov_b32 s78, 360448\n\t"
      "buffer_load_dwordx4 v[216:219], v99, s[64:67], s78 offen \n\t"
      "s_mov_b32 s78, 376832\n\t"
      "buffer_load_dwordx4 v[220:223], v99, s[64:67], s78 offen \n\t"
      "s_mov_b32 s78, 393216\n\t"
      "buffer_load_dwordx4 v[224:227], v99, s[64:67], s78 offen \n\t"
      "s_add_u32 s71, s71, 1\n\t"
      "s_getpc_b64 s[88:89]\n\t"
      "s_add_u32 s88, s88, 12\n\t"
      "s_addc_u32 s89, s89, 0\n\t"
      "s_branch GBACK%=\n\t"
      "s_mov_b32 s78, 409600\n\t"
      "buffer_load_dwordx4 v[228:231], v99, s[64:67], s78 offen \n\t"
      "s_mov_b32 s78, 425984\n\t"
      "buffer_load_dwordx4 v[232:235], v99, s[64:67], s78 offen \n\t"
      "s_mov_b32 s78, 442368\n\t"
      "buffer_load_dwordx4 v[236:239], v99, s[64:67], s78 offen \n\t"
      "s_add_u32 s71, s71, 1\n\t"
      "s_getpc_b64 s[88:89]\n\t"
      "s_add_u32 s88, s88, 12\n\t"
      "s_addc_u32 s89, s89, 0\n\t"
      "s_branch GBACK%=\n\t"
      "s_mov_b32 s78, 458752\n\t"
      "buffer_load_dwordx4 v[240:243], v99, s[64:67], s78 offen \n\t"
      "s_mov_b32 s78, 475136\n\t"
      "buffer_load_dwordx4 v[244:247], v99, s[64:67], s78 offen \n\t"
      "s_mov_b32 s78, 491520\n\t"
      "buffer_load_dwordx4 v[248:251], v99, s[64:67], s78 offen \n\t"
      "s_add_u32 s71, s71, 1\n\t"
      "s_getpc_b64 s[88:89]\n\t"
      "s_add_u32 s88, s88, 12\n\t"
      "s_addc_u32 s89, s89, 0\n\t"
      "s_branch GBACK%=\n\t"
      "s_mov_b32 s78, 507904\n\t"
      "buffer_load_dwordx4 v[252:255], v99, s[64:67], s78 offen \n\t"
      "s_mov_b32 s78, 0\n\t"
      "buffer_load_dwordx4 v[128:131], v99, s[64:67], s78 offen \n\t"
      "s_mov_b32 s78, 16384\n\t"
      "buffer_load_dwordx4 v[132:135], v99, s[64:67], s78 offen \n\t"
      "s_add_u32 s71, s71, 1\n\t"
      "s_getpc_b64 s[88:89]\n\t"
      "s_add_u32 s88, s88, 12\n\t"
      "s_addc_u32 s89, s89, 0\n\t"
      "s_branch GBACK%=\n\t"
      "s_mov_b32 s78, 32768\n\t"
      "buffer_load_dwordx4 v[136:139], v99, s[64:67], s78 offen \n\t"
      "s_mov_b32 s78, 49152\n\t"
      "buffer_load_dwordx4 v[140:143], v99, s[64:67], s78 offen \n\t"
      "s_mov_b32 s78, 65536\n\t"
      "buffer_load_dwordx4 v[144:147], v99, s[64:67], s78 offen \n\t"
      "s_add_u32 s71, s71, 1\n\t"
      "s_getpc_b64 s[88:89]\n\t"
      "s_add_u32 s88, s88, 12\n\t"
      "s_addc_u32 s89, s89, 0\n\t"
      "s_branch GBACK%=\n\t"
      "s_mov_b32 s78, 81920\n\t"
      "buffer_load_dwordx4 v[148:151], v99, s[64:67], s78 offen \n\t"
      "s_mov_b32 s78, 98304\n\t"
      "buffer_load_dwordx4 v[152:155], v99, s[64:67], s78 offen \n\t"
      "s_mov_b32 s78, 114688\n\t"
      "buffer_load_dwordx4 v[156:159], v99, s[64:67], s78 offen \n\t"
      "s_add_u32 s71, s71, 1\n\t"
      "s_getpc_b64 s[88:89]\n\t"
      "s_add_u32 s88, s88, 12\n\t"
      "s_addc_u32 s89, s89, 0\n\t"
      "s_branch GBACK%=\n\t"
      "s_mov_b32 s78, 131072\n\t"
      "buffer_load_dwordx4 v[160:163], v99, s[64:67], s78 offen \n\t"
      "s_mov_b32 s78, 147456\n\t"
      "buffer_load_dwordx4 v[164:167], v99, s[64:67], s78 offen \n\t"
      "s_mov_b32 s78, 163840\n\t"
      "buffer_load_dwordx4 v[168:171], v99, s[64:67], s78 offen \n\t"
      "s_add_u32 s71, s71, 1\n\t"
      "s_getpc_b64 s[88:89]\n\t"
      "s_add_u32 s88, s88, 12\n\t"
      "s_addc_u32 s89, s89, 0\n\t"
      "s_branch GBACK%=\n\t"
      "s_mov_b32 s78, 180224\n\t"
      "buffer_load_dwordx4 v[172:175], v99, s[64:67], s78 offen \n\t"
      "s_mov_b32 s78, 196608\n\t"
      "buffer_load_dwordx4 v[176:179], v99, s[64:67], s78 offen \n\t"
      "s_mov_b32 s78, 212992\n\t"
      "buffer_load_dwordx4 v[180:183], v99, s[64:67], s78 offen \n\t"
      "s_add_u32 s71, s71, 1\n\t"
      "s_getpc_b64 s[88:89]\n\t"
      "s_add_u32 s88, s88, 12\n\t"
      "s_addc_u32 s89, s89, 0\n\t"
      "s_branch GBACK%=\n\t"
      "s_mov_b32 s78, 229376\n\t"
      "buffer_load_dwordx4 v[184:187], v99, s[64:67], s78 offen \n\t"
      "s_mov_b32 s78, 245760\n\t"
      "buffer_load_dwordx4 v[188:191], v99, s[64:67], s78 offen \n\t"
      "s_mov_b32 s71, 32\n\t"
      "s_getpc_b64 s[88:89]\n\t"
      "s_add_u32 s88, s88, 12\n\t"
      "s_addc_u32 s89, s89, 0\n\t"
      "s_branch GBACK%=\n\t"
      "s_nop 7\n\t"
      "v_add_u32 v104, s76, v97\n\t"
      "v_add_u32 v105, s76, v100\n\t"
      "s_sub_u32 s68, s68, 1\n\t"
      "s_waitcnt vmcnt(11) lgkmcnt(0)\n\t"
      "v_cvt_pk_bf16_f32 v84, v72, v73\n\t"
      "v_cvt_pk_bf16_f32 v85, v74, v75\n\t"
      "v_cvt_pk_bf16_f32 v86, v76, v77\n\t"
      "v_cvt_pk_bf16_f32 v87, v78, v79\n\t"
      "buffer_store_dwordx4 v[84:87], v97, s[60:63], s73 offen offset:1024 sc1\n\t"
      "v_cvt_pk_bf16_f32 v114, v92, v93\n\t"
      "v_cvt_pk_bf16_f32 v115, v94, v95\n\t"
      "s_nop 0\n\t"
      "v_permlane16_swap_b32 v112, v114\n\t"
      "v_permlane16_swap_b32 v113, v115\n\t"
      "buffer_store_dwordx4 v[112:115], v111, s[60:63], s73 offen sc1\n\t"
      "v_readlane_b32 s75, v103, s68\n\t"
      "s_waitcnt lgkmcnt(6)\n\t"
      "ds_write_b128 v104, v[128:131]\n\t"
      "ds_write_b128 v104, v[132:135] offset:1024\n\t"
      "ds_read_b64_tr_b16 a[0:1], v105\n\t"
      "ds_read_b64_tr_b16 a[2:3], v105 offset:256\n\t"
      "ds_read_b64_tr_b16 a[64:65], v105 offset:8\n\t"
      "ds_read_b64_tr_b16 a[66:67], v105 offset:264\n\t"
      "s_waitcnt lgkmcnt(6)\n\t"
      "ds_write_b128 v104, v[136:139] offset:2048\n\t"
      "ds_write_b128 v104, v[140:143] offset:3072\n\t"
      "ds_read_b64_tr_b16 a[4:5], v105 offset:2048\n\t"
      "ds_read_b64_tr_b16 a[6:7], v105 offset:2304\n\t"
      "ds_read_b64_tr_b16 a[68:69], v105 offset:2056\n\t"
      "ds_read_b64_tr_b16 a[70:71], v105 offset:2312\n\t"
      "s_waitcnt lgkmcnt(6)\n\t"
      "ds_write_b128 v104, v[144:147] offset:4096\n\t"
      "ds_write_b128 v104, v[148:151] offset:5120\n\t"
      "ds_read_b64_tr_b16 a[8:9], v105 offset:4096\n\t"
      "ds_read_b64_tr_b16 a[10:11], v105 offset:4352\n\t"
      "ds_read_b64_tr_b16 a[72:73], v105 offset:4104\n\t"
      "ds_read_b64_tr_b16 a[74:75], v105 offset:4360\n\t"
      "s_waitcnt lgkmcnt(6)\n\t"
      "ds_write_b128 v104, v[152:155] offset:6144\n\t"
      "ds_write_b128 v104, v[156:159] offset:7168\n\t"
      "ds_read_b64_tr_b16 a[12:13], v105 offset:6144\n\t"
      "ds_read_b64_tr_b16 a[14:15], v105 offset:6400\n\t"
      "ds_read_b64_tr_b16 a[76:77], v105 offset:6152\n\t"
      "ds_read_b64_tr_b16 a[78:79], v105 offset:6408\n\t"
      "s_waitcnt lgkmcnt(6)\n\t"
      "ds_write_b128 v104, v[160:163]\n\t"
      "ds_write_b128 v104, v[164:167] offset:1024\n\t"
      "ds_read_b64_tr_b16 a[16:17], v105\n\t"
      "ds_read_b64_tr_b16 a[18:19], v105 offset:256\n\t"
      "ds_read_b64_tr_b16 a[80:81], v105 offset:8\n\t"
      "ds_read_b64_tr_b16 a[82:83], v105 offset:264\n\t"
      "s_waitcnt lgkmcnt(6)\n\t"
      "ds_write_b128 v104, v[168:171] offset:2048\n\t"
      "ds_write_b128 v104, v[172:175] offset:3072\n\t"
      "ds_read_b64_tr_b16 a[20:21], v105 offset:2048\n\t"
      "ds_read_b64_tr_b16 a[22:23], v105 offset:2304\n\t"
      "ds_read_b64_tr_b16 a[84:85], v105 offset:2056\n\t"
      "ds_read_b64_tr_b16 a[86:87], v105 offset:2312\n\t"
      "s_waitcnt lgkmcnt(6)\n\t"
      "ds_write_b128 v104, v[176:179] offset:4096\n\t"
      "ds_write_b128 v104, v[180:183] offset:5120\n\t"
      "ds_read_b64_tr_b16 a[24:25], v105 offset:4096\n\t"
      "ds_read_b64_tr_b16 a[26:27], v105 offset:4352\n\t"
      "ds_read_b64_tr_b16 a[88:89], v105 offset:4104\n\t"
      "ds_read_b64_tr_b16 a[90:91], v105 offset:4360\n\t"
      "s_waitcnt lgkmcnt(6)\n\t"
      "ds_write_b128 v104, v[184:187] offset:6144\n\t"
      "ds_write_b128 v104, v[188:191] offset:7168\n\t"
      "ds_read_b64_tr_b16 a[28:29], v105 offset:6144\n\t"
      "ds_read_b64_tr_b16 a[30:31], v105 offset:6400\n\t"
      "ds_read_b64_tr_b16 a[92:93], v105 offset:6152\n\t"
      "ds_read_b64_tr_b16 a[94:95], v105 offset:6408\n\t"
      "s_waitcnt lgkmcnt(6)\n\t"
      "ds_write_b128 v104, v[192:195]\n\t"
      "ds_write_b128 v104, v[196:199] offset:1024\n\t"
      "ds_read_b64_tr_b16 a[32:33], v105\n\t"
      "ds_read_b64_tr_b16 a[34:35], v105 offset:256\n\t"
      "ds_read_b64_tr_b16 a[96:97], v105 offset:8\n\t"
      "ds_read_b64_tr_b16 a[98:99], v105 offset:264\n\t"
      "s_waitcnt lgkmcnt(6)\n\t"
      "ds_write_b128 v104, v[200:203] offset:2048\n\t"
      "ds_write_b128 v104, v[204:207] offset:3072\n\t"
      "ds_read_b64_tr_b16 a[36:37], v105 offset:2048\n\t"
      "ds_read_b64_tr_b16 a[38:39], v105 offset:2304\n\t"
      "ds_read_b64_tr_b16 a[100:101], v105 offset:2056\n\t"
      "ds_read_b64_tr_b16 a[102:103], v105 offset:2312\n\t"
      "s_waitcnt lgkmcnt(6)\n\t"
      "ds_write_b128 v104, v[208:211] offset:4096\n\t"
      "ds_write_b128 v104, v[212:215] offset:5120\n\t"
      "ds_read_b64_tr_b16 a[40:41], v105 offset:4096\n\t"
      "ds_read_b64_tr_b16 a[42:43], v105 offset:4352\n\t"
      "ds_read_b64_tr_b16 a[104:105], v105 offset:4104\n\t"
      "ds_read_b64_tr_b16 a[106:107], v105 offset:4360\n\t"
      "s_waitcnt lgkmcnt(6)\n\t"
      "ds_write_b128 v104, v[216:219] offset:6144\n\t"
      "ds_write_b128 v104, v[220:223] offset:7168\n\t"
      "ds_read_b64_tr_b16 a[44:45], v105 offset:6144\n\t"
      "ds_read_b64_tr_b16 a[46:47], v105 offset:6400\n\t"
      "ds_read_b64_tr_b16 a[108:109], v105 offset:6152\n\t"
      "ds_read_b64_tr_b16 a[110:111], v105 offset:6408\n\t"
      "s_waitcnt lgkmcnt(6)\n\t"
      "ds_write_b128 v104, v[224:227]\n\t"
      "ds_write_b128 v104, v[228:231] offset:1024\n\t"
      "ds_read_b64_tr_b16 a[48:49], v105\n\t"
      "ds_read_b64_tr_b16 a[50:51], v105 offset:256\n\t"
      "ds_read_b64_tr_b16 a[112:113], v105 offset:8\n\t"
      "ds_read_b64_tr_b16 a[114:115], v105 offset:264\n\t"
      "s_waitcnt lgkmcnt(6)\n\t"
      "ds_write_b128 v104, v[232:235] offset:2048\n\t"
      "ds_write_b128 v104, v[236:239] offset:3072\n\t"
      "ds_read_b64_tr_b16 a[52:53], v105 offset:2048\n\t"
      "ds_read_b64_tr_b16 a[54:55], v105 offset:2304\n\t"
      "ds_read_b64_tr_b16 a[116:117], v105 offset:2056\n\t"
      "ds_read_b64_tr_b16 a[118:119], v105 offset:2312\n\t"
      "s_waitcnt lgkmcnt(6)\n\t"
      "ds_write_b128 v104, v[240:243] offset:4096\n\t"
      "ds_write_b128 v104, v[244:247] offset:5120\n\t"
      "ds_read_b64_tr_b16 a[56:57], v105 offset:4096\n\t"
      "ds_read_b64_tr_b16 a[58:59], v105 offset:4352\n\t"
      "ds_read_b64_tr_b16 a[120:121], v105 offset:4104\n\t"
      "ds_read_b64_tr_b16 a[122:123], v105 offset:4360\n\t"
      "s_waitcnt lgkmcnt(6)\n\t"
      "ds_write_b128 v104, v[248:251] offset:6144\n\t"
      "ds_write_b128 v104, v[252:255] offset:7168\n\t"
      "ds_read_b64_tr_b16 a[60:61], v105 offset:6144\n\t"
      "ds_read_b64_tr_b16 a[62:63], v105 offset:6400\n\t"
      "ds_read_b64_tr_b16 a[124:125], v105 offset:6152\n\t"
      "ds_read_b64_tr_b16 a[126:127], v105 offset:6408\n\t"
      "s_waitcnt vmcnt(0) lgkmcnt(0)\n\t"
      "s_barrier\n\t"
      "s_mov_b64 exec, s[90:91]\n\t"
      "buffer_store_dword v107, off, s[60:63], s75 sc1\n\t"
      "s_sub_u32 s75, s75, 32\n\t"
      "buffer_store_dword v107, off, s[60:63], s75 sc1\n\t"
      "s_sub_u32 s75, s75, 2016\n\t"
      "buffer_store_dword v107, off, s[60:63], s75 sc1\n\t"
      "s_mov_b64 exec, -1\n\t"
      : "+{s34}"(w), "+{s33}"(wv), "+{s[0:1]}"(A), "=&s"(tag), "={a[0:31]}"(wo.a), "={a[32:63]}"(wo.b), "={a[64:95]}"(wo.c), "={a[96:127]}"(wo.d)
      :
      : "memory", "vcc", "scc", "v0", "v1", "v2", "v3", "v4", "v5", "v6", "v7", "v8", "v9", "v10", "v11", "v12", "v13", "v14", "v15", "v16", "v17", "v18", "v19", "v20", "v21", "v22", "v23", "v24", "v25", "v26", "v27", "v28", "v29", "v30", "v31", "v32", "v33", "v34", "v35", "v36", "v37", "v38", "v39", "v40", "v41", "v42", "v43", "v44", "v45", "v46", "v47", "v48", "v49", "v50", "v51", "v52", "v53", "v54", "v55", "v56", "v57", "v58", "v59", "v60", "v61", "v62", "v63", "v64", "v65", "v66", "v67", "v68", "v69", "v70", "v71", "v72", "v73", "v74", "v75", "v76", "v77", "v78", "v79", "v80", "v81", "v82", "v83", "v84", "v85", "v86", "v87", "v88", "v89", "v90", "v91", "v92", "v93", "v94", "v95", "v96", "v97", "v98", "v99", "v100", "v101", "v102", "v103", "v104", "v105", "v106", "v107", "v108", "v109", "v110", "v111", "v112", "v113", "v114", "v115", "v116", "v117", "v118", "v119", "v120", "v121", "v122", "v123", "v124", "v125", "v126", "v127", "v128", "v129", "v130", "v131", "v132", "v133", "v134", "v135", "v136", "v137", "v138", "v139", "v140", "v141", "v142", "v143", "v144", "v145", "v146", "v147", "v148", "v149", "v150", "v151", "v152", "v153", "v154", "v155", "v156", "v157", "v158", "v159", "v160", "v161", "v162", "v163", "v164", "v165", "v166", "v167", "v168", "v169", "v170", "v171", "v172", "v173", "v174", "v175", "v176", "v177", "v178", "v179", "v180", "v181", "v182", "v183", "v184", "v185", "v186", "v187", "v188", "v189", "v190", "v191", "v192", "v193", "v194", "v195", "v196", "v197", "v198", "v199", "v200", "v201", "v202", "v203", "v204", "v205", "v206", "v207", "v208", "v209", "v210", "v211", "v212", "v213", "v214", "v215", "v216", "v217", "v218", "v219", "v220", "v221", "v222", "v223", "v224", "v225", "v226", "v227", "v228", "v229", "v230", "v231", "v232", "v233", "v234", "v235", "v236", "v237", "v238", "v239", "v240", "v241", "v242", "v243", "v244", "v245", "v246", "v247", "v248", "v249", "v250", "v251", "v252", "v253", "v254", "v255", "a128", "a129", "a130", "a131", "a132", "a133", "a134", "a135", "a136", "a137", "a138", "a139", "a140", "a141", "a142", "a143", "a144", "a145", "a146", "a147", "a148", "a149", "a150", "a151", "a152", "a153", "a154", "a155", "a156", "a157", "a158", "a159", "a160", "a161", "a162", "a163", "a164", "a165", "a166", "a167", "a168", "a169", "a170", "a171", "a172", "a173", "a174", "a175", "a176", "a177", "a178", "a179", "a180", "a181", "a182", "a183", "a184", "a185", "a186", "a187", "a188", "a189", "a190", "a191", "a192", "a193", "a194", "a195", "a196", "a197", "a198", "a199", "a200", "a201", "a202", "a203", "a204", "a205", "a206", "a207", "a208", "a209", "a210", "a211", "a212", "a213", "a214", "a215", "a216", "a217", "a218", "a219", "a220", "a221", "a222", "a223", "a224", "a225", "a226", "a227", "a228", "a229", "a230", "a231", "a232", "a233", "a234", "a235", "a236", "a237", "a238", "a239", "a240", "a241", "a242", "a243", "a244", "a245", "a246", "a247", "a248", "a249", "a250", "a251", "a252", "a253", "a254", "a255", "s36", "s37", "s38", "s39", "s40", "s41", "s42", "s43", "s44", "s45", "s46", "s47", "s48", "s49", "s50", "s51", "s52", "s53", "s54", "s55", "s56", "s57", "s58", "s59", "s60", "s61", "s62", "s63", "s64", "s65", "s66", "s67", "s68", "s69", "s70", "s71", "s72", "s73", "s74", "s75", "s76", "s77", "s78", "s79", "s80", "s81", "s82", "s83", "s84", "s85", "s86", "s87", "s88", "s89", "s90", "s91", "s92", "s93", "s94", "s95");
  return tag;
}

__device__ __forceinline__ void worker_tasks(char* lds, int& w, int& wv, KArgs& A, unsigned tag, WoRegs& wo) {
  asm volatile("" : "+s"(w), "+s"(wv), "+s"(A));
  const int lane = fresh_lane(), l15 = lane & 15, l4 = lane >> 4;
  const int team = w & 3, j = w >> 2, g = j >> 2, c = j & 3, h = g * 4 + wv;
  const u64 wsa = (u64)A->ws, xa = (u64)A->x, oa = (u64)A->out;
  u32x16 c0 = {(unsigned)wsa, (unsigned)(wsa >> 32) & 0xffffu, WS_BYTES, 0x00020000u, (unsigned)xa, (unsigned)(xa >> 32) & 0xffffu, S_ * DM * 2, 0x00020000u,
               (unsigned)oa, (unsigned)(oa >> 32) & 0xffffu, S_ * DM * 2, 0x00020000u, O_STAMPS + (310000 + j * 4 + wv) * 8, (unsigned)(l15 - l4 * 4),
               (unsigned)(team + 4 * wv + 48), (unsigned)j};
  const int r0 = 0 ? h : j;
  u32x16 c1 = {(unsigned)team, (unsigned)(team + 4 * (0 ? c : wv)), O_QKV + (team * 32 + h) * 24576, O_KVM + (team * 32 + h) * 2048, O_VTM + (team * 32 + h) * 2048,
               O_PART + (team * 8 + g) * 32768, O_PART + r0 * 1024, O_PFLAG + team * 16384, O_KVSTEP + h * 64, O_RBUF, O_LNH, O_HBUF, (unsigned)r0,
               (unsigned)wv * 8192, tag, O_PFLAG + team * 16384 + (j * 4 + wv) * 128};
  u32x8 c4 = {(unsigned)lane, (unsigned)lane, (unsigned)lane, (unsigned)lane, O_TFLAG + (6 * 32 + h) * 64 + 32 + (l15 & 1) * 16, O_TFLAG + (15 * 32 + h) * 64 + 32, O_WM + lane * 256 + 4, (unsigned)lane};
  const int rc = 8 * (l15 + 16 * (l4 >> 1)) + 4 * (l4 & 1);
  const u32x2 w0 = *(const u32x2*)(A->w_r + rc), w1 = *(const u32x2*)(A->w_r + 256 + rc);
  u32x16 c2 = {w0[0] << 16, w0[0] & 0xffff0000u, w0[1] << 16, w0[1] & 0xffff0000u, w1[0] << 16, w1[0] & 0xffff0000u, w1[1] << 16, w1[1] & 0xffff0000u,
               (unsigned)lane * 16, (unsigned)rc * 2, (unsigned)(lane ^ ((l4 & 1) * 8)) * 16,
               (unsigned)(wv * 8192 + (l15 & 3) * 256 + (((l4 * 4 + (l15 >> 2)) ^ ((l15 & 1) * 8)) << 4)),
               (unsigned)(l4 * 128 + (l15 & 7) * 16), (unsigned)(lane & 31) * 16,
               (unsigned)(65536 + wv * 8192 + lane * 16), (unsigned)(65536 + lane * 16)};
  unsigned c3 = (unsigned)(l15 * DM + c * 128 + wv * 32 + l4 * 8) * 2;
  asm volatile(
      "s_nop 1\n\t"
      "v_readfirstlane_b32 s36, v0\n\t"
      "v_readfirstlane_b32 s37, v1\n\t"
      "v_readfirstlane_b32 s38, v2\n\t"
      "v_readfirstlane_b32 s39, v3\n\t"
      "v_readfirstlane_b32 s40, v4\n\t"
      "v_readfirstlane_b32 s41, v5\n\t"
      "v_readfirstlane_b32 s42, v6\n\t"
      "v_readfirstlane_b32 s43, v7\n\t"
      "v_readfirstlane_b32 s44, v8\n\t"
      "v_readfirstlane_b32 s45, v9\n\t"
      "v_readfirstlane_b32 s46, v10\n\t"
      "v_readfirstlane_b32 s47, v11\n\t"
      "v_readfirstlane_b32 s48, v16\n\t"
      "v_readfirstlane_b32 s49, v17\n\t"
      "v_readfirstlane_b32 s50, v18\n\t"
      "v_readfirstlane_b32 s51, v19\n\t"
      "v_readfirstlane_b32 s52, v20\n\t"
      "v_readfirstlane_b32 s53, v21\n\t"
      "v_readfirstlane_b32 s54, v22\n\t"
      "v_readfirstlane_b32 s55, v23\n\t"
      "v_readfirstlane_b32 s56, v24\n\t"
      "v_readfirstlane_b32 s57, v25\n\t"
      "v_readfirstlane_b32 s58, v26\n\t"
      "v_readfirstlane_b32 s59, v27\n\t"
      "v_readfirstlane_b32 s60, v28\n\t"
      "v_readfirstlane_b32 s61, v29\n\t"
      "v_readfirstlane_b32 s62, v30\n\t"
      "v_readfirstlane_b32 s63, v31\n\t"
      "v_readfirstlane_b32 s75, v12\n\t"
      "v_readfirstlane_b32 s93, v14\n\t"
      "v_readfirstlane_b32 s94, v15\n\t"
      "v_cmp_ge_i32 s[76:77], v13, 0\n\t"
      "v_cmp_ge_i32 s[78:79], v13, 1\n\t"
      "v_cmp_ge_i32 s[80:81], v13, 2\n\t"
      "v_cmp_ge_i32 s[82:83], v13, 3\n\t"
      "s_nop 4\n\t"
      "v_readfirstlane_b32 s99, v37\n\t"
      "s_add_u32 s98, s53, 14680064\n\t"
      "v_lshrrev_b32 v40, 4, v248\n\t"
      "v_and_b32 v41, 15, v40\n\t"
      "v_lshrrev_b32 v40, 1, v40\n\t"
      "v_and_b32 v40, 24, v40\n\t"
      "v_sub_u32 v41, v41, v40\n\t"
      "s_mov_b32 s69, 0x3f80\n\t"
      "s_mov_b32 s70, 0x3f800000\n\t"
      "v_mov_b32 v42, 0\n\t"
      "v_cmp_eq_i32 vcc, 0, v41\n\t"
      "v_mov_b32 v43, s69\n\t"
      "s_nop 0\n\t"
      "v_cndmask_b32 v42, v42, v43\n\t"
      "v_cmp_eq_i32 vcc, 1, v41\n\t"
      "v_mov_b32 v43, s70\n\t"
      "s_nop 0\n\t"
      "v_cndmask_b32 v42, v42, v43\n\t"
      "v_accvgpr_write_b32 a224, v42\n\t"
      "v_mov_b32 v42, 0\n\t"
      "v_cmp_eq_i32 vcc, 2, v41\n\t"
      "v_mov_b32 v43, s69\n\t"
      "s_nop 0\n\t"
      "v_cndmask_b32 v42, v42, v43\n\t"
      "v_cmp_eq_i32 vcc, 3, v41\n\t"
      "v_mov_b32 v43, s70\n\t"
      "s_nop 0\n\t"
      "v_cndmask_b32 v42, v42, v43\n\t"
      "v_accvgpr_write_b32 a225, v42\n\t"
      "v_mov_b32 v42, 0\n\t"
      "v_cmp_eq_i32 vcc, 4, v41\n\t"
      "v_mov_b32 v43, s69\n\t"
      "s_nop 0\n\t"
      "v_cndmask_b32 v42, v42, v43\n\t"
      "v_cmp_eq_i32 vcc, 5, v41\n\t"
      "v_mov_b32 v43, s70\n\t"
      "s_nop 0\n\t"
      "v_cndmask_b32 v42, v42, v43\n\t"
      "v_accvgpr_write_b32 a226, v42\n\t"
      "v_mov_b32 v42, 0\n\t"
      "v_cmp_eq_i32 vcc, 6, v41\n\t"
      "v_mov_b32 v43, s69\n\t"
      "s_nop 0\n\t"
      "v_cndmask_b32 v42, v42, v43\n\t"
      "v_cmp_eq_i32 vcc, 7, v41\n\t"
      "v_mov_b32 v43, s70\n\t"
      "s_nop 0\n\t"
      "v_cndmask_b32 v42, v42, v43\n\t"
      "v_accvgpr_write_b32 a227, v42\n\t"
      "v_mov_b32 v42, 0\n\t"
      "v_cmp_eq_i32 vcc, -16, v41\n\t"
      "v_mov_b32 v43, s69\n\t"
      "s_nop 0\n\t"
      "v_cndmask_b32 v42, v42, v43\n\t"
      "v_cmp_eq_i32 vcc, -15, v41\n\t"
      "v_mov_b32 v43, s70\n\t"
      "s_nop 0\n\t"
      "v_cndmask_b32 v42, v42, v43\n\t"
      "v_accvgpr_write_b32 a228, v42\n\t"
      "v_mov_b32 v42, 0\n\t"
      "v_cmp_eq_i32 vcc, -14, v41\n\t"
      "v_mov_b32 v43, s69\n\t"
      "s_nop 0\n\t"
      "v_cndmask_b32 v42, v42, v43\n\t"
      "v_cmp_eq_i32 vcc, -13, v41\n\t"
      "v_mov_b32 v43, s70\n\t"
      "s_nop 0\n\t"
      "v_cndmask_b32 v42, v42, v43\n\t"
      "v_accvgpr_write_b32 a229, v42\n\t"
      "v_mov_b32 v42, 0\n\t"
      "v_cmp_eq_i32 vcc, -12, v41\n\t"
      "v_mov_b32 v43, s69\n\t"
      "s_nop 0\n\t"
      "v_cndmask_b32 v42, v42, v43\n\t"
      "v_cmp_eq_i32 vcc, -11, v41\n\t"
      "v_mov_b32 v43, s70\n\t"
      "s_nop 0\n\t"
      "v_cndmask_b32 v42, v42, v43\n\t"
      "v_accvgpr_write_b32 a230, v42\n\t"
      "v_mov_b32 v42, 0\n\t"
      "v_cmp_eq_i32 vcc, -10, v41\n\t"
      "v_mov_b32 v43, s69\n\t"
      "s_nop 0\n\t"
      "v_cndmask_b32 v42, v42, v43\n\t"
      "v_cmp_eq_i32 vcc, -9, v41\n\t"
      "v_mov_b32 v43, s70\n\t"
      "s_nop 0\n\t"
      "v_cndmask_b32 v42, v42, v43\n\t"
      "v_accvgpr_write_b32 a231, v42\n\t"
      "s_mov_b32 s72, 0x40000\n\t"
      "WE0%=:\n\t"
      "buffer_load_dwordx4 v[40:43], v36, s[36:39], 0 offen sc1\n\t"
      "buffer_load_dword v48, v38, s[36:39], 0 offen sc1\n\t"
      "buffer_load_dword v49, v38, s[36:39], 0 offen offset:128 sc1\n\t"
      "s_waitcnt vmcnt(0)\n\t"
      "v_bitop3_b32 v40, v40, v41, s62 bitop3:0x7e\n\t"
      "v_bitop3_b32 v48, v48, v49, s62 bitop3:0x7e\n\t"
      "v_or_b32 v40, v40, v48\n\t"
      "v_cmp_ne_u32 vcc, 0, v40\n\t"
      "s_nop 0\n\t"
      "s_cbranch_vccz WE1%=\n\t"
      "s_sleep 8\n\t"
      "s_sub_u32 s72, s72, 1\n\t"
      "s_cmp_lg_u32 s72, 0\n\t"
      "s_cbranch_scc1 WE0%=\n\t"
      "WE1%=:\n\t"
      "buffer_load_dwordx4 a[128:131], v248, s[36:39], s50 offen  sc1\n\t"
      "buffer_load_dwordx4 a[132:135], v248, s[36:39], s50 offen offset:1024  sc1\n\t"
      "buffer_load_dwordx4 a[136:139], v248, s[36:39], s50 offen offset:2048  sc1\n\t"
      "buffer_load_dwordx4 a[140:143], v248, s[36:39], s50 offen offset:3072  sc1\n\t"
      "s_add_u32 s69, s50, 4096\n\t"
      "buffer_load_dwordx4 a[144:147], v248, s[36:39], s69 offen  sc1\n\t"
      "buffer_load_dwordx4 a[148:151], v248, s[36:39], s69 offen offset:1024  sc1\n\t"
      "buffer_load_dwordx4 a[152:155], v248, s[36:39], s69 offen offset:2048  sc1\n\t"
      "buffer_load_dwordx4 a[156:159], v248, s[36:39], s69 offen offset:3072  sc1\n\t"
      "s_add_u32 s69, s50, 8192\n\t"
      "s_add_u32 m0, s61, 0\n\t"
      "s_nop 0\n\t"
      "buffer_load_dwordx4 v250, s[36:39], s69 offen sc1 lds\n\t"
      "buffer_load_dwordx4 v250, s[36:39], s69 offen offset:1024 sc1 lds\n\t"
      "buffer_load_dwordx4 v250, s[36:39], s69 offen offset:2048 sc1 lds\n\t"
      "buffer_load_dwordx4 v250, s[36:39], s69 offen offset:3072 sc1 lds\n\t"
      "s_add_u32 s69, s50, 12288\n\t"
      "s_add_u32 m0, s61, 4096\n\t"
      "s_nop 0\n\t"
      "buffer_load_dwordx4 v250, s[36:39], s69 offen sc1 lds\n\t"
      "buffer_load_dwordx4 v250, s[36:39], s69 offen offset:1024 sc1 lds\n\t"
      "buffer_load_dwordx4 v250, s[36:39], s69 offen offset:2048 sc1 lds\n\t"
      "buffer_load_dwordx4 v250, s[36:39], s69 offen offset:3072 sc1 lds\n\t"
      "s_add_u32 s69, s50, 16384\n\t"
      "buffer_load_dwordx4 a[160:163], v248, s[36:39], s69 offen  sc1\n\t"
      "buffer_load_dwordx4 a[164:167], v248, s[36:39], s69 offen offset:1024  sc1\n\t"
      "buffer_load_dwordx4 a[168:171], v248, s[36:39], s69 offen offset:2048  sc1\n\t"
      "buffer_load_dwordx4 a[172:175], v248, s[36:39], s69 offen offset:3072  sc1\n\t"
      "s_add_u32 s69, s50, 20480\n\t"
      "buffer_load_dwordx4 a[176:179], v248, s[36:39], s69 offen  sc1\n\t"
      "buffer_load_dwordx4 a[180:183], v248, s[36:39], s69 offen offset:1024  sc1\n\t"
      "buffer_load_dwordx4 a[184:187], v248, s[36:39], s69 offen offset:2048  sc1\n\t"
      "buffer_load_dwordx4 a[188:191], v248, s[36:39], s69 offen offset:3072  sc1\n\t"
      "s_add_u32 s50, s50, 3145728\n\t"
      "s_mov_b32 s64, 0\n\t"
      "s_mov_b32 s65, 0\n\t"
      "s_mov_b32 s66, 0\n\t"
      "s_mov_b32 s96, 0\n\t"
      "s_mov_b32 s97, 0\n\t"
      "s_add_u32 s89, s49, 16\n\t"
      "s_sub_u32 s95, s94, s60\n\t"
      "s_lshl_b32 s95, s95, 10\n\t"
      "s_add_u32 s95, s95, s54\n\t"
      "s_mov_b32 s67, -1\n\t"
      "s_mov_b32 s74, 0xffff0000\n\t"
      "s_mov_b32 s84, 0x3db504f3\n\t"
      "s_mov_b32 s86, 0x3fb8aa3b\n\t"
      "s_mov_b32 s85, 0x3db504f3\n\t"
      "s_mov_b32 s87, 0x3fb8aa3b\n\t"
      "v_mov_b32 v239, 0xff61b1e6\n\t"
      "s_waitcnt vmcnt(0) lgkmcnt(0)\n\t"
      "s_branch WA%=\n\t"
      "WTOP%=:\n\t"
      "s_cmp_ge_u32 s48, 64\n\t"
      "s_cbranch_scc1 WFIN%=\n\t"
      "s_lshr_b32 s69, s48, 2\n\t"
      "s_cmp_eq_u32 s69, 5\n\t"
      "s_cbranch_scc1 WH2%=\n\t"
      "WT0%=:\n\t"
      "s_waitcnt vmcnt(17)\n\t"
      "s_cmp_eq_u32 s24, 9\n\t"
      "s_cbranch_scc1 WT1%=\n\t"
      "s_waitcnt vmcnt(10)\n\t"
      "s_cmp_lg_u32 s97, 0\n\t"
      "s_cbranch_scc1 WT1%=\n\t"
      "s_waitcnt vmcnt(9)\n\t"
      "s_cmp_eq_u32 s24, 8\n\t"
      "s_cbranch_scc1 WT1%=\n\t"
      "s_waitcnt vmcnt(2)\n\t"
      "WT1%=:\n\t"
      "v_min_u32 v228, v228, v229\n\t"
      "v_min3_u32 v228, v228, v230, v231\n\t"
      "v_min_u32 v232, v232, v233\n\t"
      "s_cmp_lg_u32 s65, 0\n\t"
      "s_cselect_b32 s70, s89, s49\n\t"
      "v_readfirstlane_b32 s88, v228\n\t"
      "s_memrealtime s[90:91]\n\t"
      "v_cmp_ge_u32 vcc, s70, v232\n\t"
      "s_lshr_b32 s69, s88, 16\n\t"
      "s_cmp_gt_u32 s69, s48\n\t"
      "s_cselect_b32 s64, 1, 0\n\t"
      "s_cmp_eq_u64 vcc, 0\n\t"
      "s_cselect_b32 s66, 1, 0\n\t"
      "s_cmp_eq_u32 s65, 2\n\t"
      "s_cselect_b32 s66, 0, s66\n\t"
      "s_add_u32 s70, s48, 4\n\t"
      "s_cmp_gt_u32 s69, s70\n\t"
      "s_cselect_b32 s96, 1, 0\n\t"
      "s_or_b32 s64, s64, s97\n\t"
      "s_cmp_lg_u32 s97, 0\n\t"
      "s_cbranch_scc1 WA%=\n\t"
      "s_cmp_lg_u32 s64, 0\n\t"
      "s_cbranch_scc1 WT2%=\n\t"
      "s_sub_u32 s69, s48, s69\n\t"
      "s_mul_i32 s69, s69, 170\n\t"
      "s_waitcnt lgkmcnt(0)\n\t"
      "s_sub_u32 s70, s90, s88\n\t"
      "s_add_u32 s70, s70, 200\n\t"
      "s_and_b32 s70, s70, 0xffff\n\t"
      "s_add_u32 s69, s69, 70\n\t"
      "s_cmp_ge_u32 s70, s69\n\t"
      "s_cselect_b32 s69, s48, 0\n\t"
      "s_cmp_ge_u32 s69, 40\n\t"
      "s_cselect_b32 s64, 2, 0\n\t"
      "s_cmp_eq_u32 s64, 0\n\t"
      "s_cbranch_scc1 WA%=\n\t"
      "WT2%=:\n\t"
      "buffer_load_dwordx4 v[128:131], v252, s[36:39], s51 offen sc1\n\t"
      "buffer_load_dwordx4 v[132:135], v252, s[36:39], s51 offen offset:512 sc1\n\t"
      "buffer_load_dwordx4 v[136:139], v252, s[36:39], s51 offen offset:1024 sc1\n\t"
      "buffer_load_dwordx4 v[140:143], v252, s[36:39], s51 offen offset:1536 sc1\n\t"
      "buffer_load_dwordx4 v[144:147], v253, s[36:39], s52 offen sc1\n\t"
      "buffer_load_dwordx4 v[148:151], v253, s[36:39], s52 offen offset:512 sc1\n\t"
      "buffer_load_dwordx4 v[152:155], v253, s[36:39], s52 offen offset:1024 sc1\n\t"
      "buffer_load_dwordx4 v[156:159], v253, s[36:39], s52 offen offset:1536 sc1\n\t"
      "WA%=:\n\t"
      "v_mfma_f32_16x16x32_bf16 v[0:3], a[128:131], a[160:163], 0\n\t"
      "ds_read_b64_tr_b16 a[192:193], v251\n\t"
      "ds_read_b64_tr_b16 a[194:195], v251 offset:1024\n\t"
      "v_mfma_f32_16x16x32_bf16 v[4:7], a[128:131], a[164:167], 0\n\t"
      "ds_read_b64_tr_b16 a[196:197], v251 offset:8\n\t"
      "ds_read_b64_tr_b16 a[198:199], v251 offset:1032\n\t"
      "v_mfma_f32_16x16x32_bf16 v[8:11], a[132:135], a[164:167], 0\n\t"
      "ds_read_b64_tr_b16 a[200:201], v251 offset:2048\n\t"
      "ds_read_b64_tr_b16 a[202:203], v251 offset:3072\n\t"
      "v_mfma_f32_16x16x32_bf16 v[0:3], a[136:139], a[168:171], v[0:3]\n\t"
      "ds_read_b64_tr_b16 a[204:205], v251 offset:2056\n\t"
      "ds_read_b64_tr_b16 a[206:207], v251 offset:3080\n\t"
      "v_mfma_f32_16x16x32_bf16 v[4:7], a[136:139], a[172:175], v[4:7]\n\t"
      "v_mfma_f32_16x16x32_bf16 v[8:11], a[140:143], a[172:175], v[8:11]\n\t"
      "s_waitcnt lgkmcnt(0)\n\t"
      "ds_read_b64_tr_b16 a[208:209], v251 offset:4096\n\t"
      "ds_read_b64_tr_b16 a[210:211], v251 offset:5120\n\t"
      "ds_read_b64_tr_b16 a[212:213], v251 offset:4104\n\t"
      "ds_read_b64_tr_b16 a[214:215], v251 offset:5128\n\t"
      "ds_read_b64_tr_b16 a[216:217], v251 offset:6144\n\t"
      "ds_read_b64_tr_b16 a[218:219], v251 offset:7168\n\t"
      "ds_read_b64_tr_b16 a[220:221], v251 offset:6152\n\t"
      "ds_read_b64_tr_b16 a[222:223], v251 offset:7176\n\t"
      "v_mfma_f32_16x16x32_bf16 v[0:3], a[144:147], a[176:179], v[0:3]\n\t"
      "v_mfma_f32_16x16x32_bf16 v[4:7], a[144:147], a[180:183], v[4:7]\n\t"
      "v_mfma_f32_16x16x32_bf16 v[8:11], a[148:151], a[180:183], v[8:11]\n\t"
      "v_mfma_f32_16x16x32_bf16 v[0:3], a[152:155], a[184:187], v[0:3]\n\t"
      "v_mfma_f32_16x16x32_bf16 v[4:7], a[152:155], a[188:191], v[4:7]\n\t"
      "v_mfma_f32_16x16x32_bf16 v[8:11], a[156:159], a[188:191], v[8:11]\n\t"
      "s_waitcnt lgkmcnt(0)\n\t"
      "buffer_load_dwordx4 a[128:131], v248, s[36:39], s50 offen  sc1\n\t"
      "s_nop 3\n\t"
      "v_cvt_pk_bf16_f32 v0, v0, v1\n\t"
      "v_cvt_pk_bf16_f32 v2, v2, v3\n\t"
      "v_cvt_pk_bf16_f32 v4, v4, v5\n\t"
      "v_cvt_pk_bf16_f32 v6, v6, v7\n\t"
      "v_cvt_pk_bf16_f32 v8, v8, v9\n\t"
      "v_cvt_pk_bf16_f32 v10, v10, v11\n\t"
      "v_and_b32 v1, s74, v0\n\t"
      "v_lshlrev_b32 v0, 16, v0\n\t"
      "v_and_b32 v3, s74, v2\n\t"
      "v_lshlrev_b32 v2, 16, v2\n\t"
      "v_and_b32 v5, s74, v4\n\t"
      "v_lshlrev_b32 v4, 16, v4\n\t"
      "v_and_b32 v7, s74, v6\n\t"
      "v_lshlrev_b32 v6, 16, v6\n\t"
      "v_and_b32 v9, s74, v8\n\t"
      "v_lshlrev_b32 v8, 16, v8\n\t"
      "v_and_b32 v11, s74, v10\n\t"
      "v_lshlrev_b32 v10, 16, v10\n\t"
      "v_pk_mul_f32 v[0:1], v[0:1], s[84:85]\n\t"
      "v_pk_mul_f32 v[2:3], v[2:3], s[84:85]\n\t"
      "v_pk_mul_f32 v[4:5], v[4:5], s[84:85]\n\t"
      "buffer_load_dwordx4 a[132:135], v248, s[36:39], s50 offen offset:1024  sc1\n\t"
      "v_pk_mul_f32 v[6:7], v[6:7], s[84:85]\n\t"
      "v_pk_mul_f32 v[8:9], v[8:9], s[84:85]\n\t"
      "v_pk_mul_f32 v[10:11], v[10:11], s[84:85]\n\t"
      "v_cvt_pk_bf16_f32 v0, v0, v1\n\t"
      "v_cvt_pk_bf16_f32 v2, v2, v3\n\t"
      "v_cvt_pk_bf16_f32 v4, v4, v5\n\t"
      "v_cvt_pk_bf16_f32 v6, v6, v7\n\t"
      "v_cvt_pk_bf16_f32 v8, v8, v9\n\t"
      "v_cvt_pk_bf16_f32 v10, v10, v11\n\t"
      "v_and_b32 v1, s74, v0\n\t"
      "v_lshlrev_b32 v0, 16, v0\n\t"
      "v_and_b32 v3, s74, v2\n\t"
      "v_lshlrev_b32 v2, 16, v2\n\t"
      "v_and_b32 v5, s74, v4\n\t"
      "v_lshlrev_b32 v4, 16, v4\n\t"
      "v_and_b32 v7, s74, v6\n\t"
      "v_lshlrev_b32 v6, 16, v6\n\t"
      "v_and_b32 v9, s74, v8\n\t"
      "v_lshlrev_b32 v8, 16, v8\n\t"
      "v_and_b32 v11, s74, v10\n\t"
      "buffer_load_dwordx4 a[136:139], v248, s[36:39], s50 offen offset:2048  sc1\n\t"
      "v_lshlrev_b32 v10, 16, v10\n\t"
      "v_cndmask_b32_e64 v0, v239, v0, s[76:77]\n\t"
      "v_cndmask_b32_e64 v8, v239, v8, s[76:77]\n\t"
      "v_cndmask_b32_e64 v1, v239, v1, s[78:79]\n\t"
      "v_cndmask_b32_e64 v9, v239, v9, s[78:79]\n\t"
      "v_cndmask_b32_e64 v2, v239, v2, s[80:81]\n\t"
      "v_cndmask_b32_e64 v10, v239, v10, s[80:81]\n\t"
      "v_cndmask_b32_e64 v3, v239, v3, s[82:83]\n\t"
      "v_cndmask_b32_e64 v11, v239, v11, s[82:83]\n\t"
      "v_max3_f32 v234, v0, v1, v2\n\t"
      "v_max3_f32 v235, v4, v5, v6\n\t"
      "v_max_f32 v234, v234, v3\n\t"
      "v_max3_f32 v235, v235, v7, v8\n\t"
      "v_max3_f32 v235, v235, v9, v10\n\t"
      "v_max_f32 v235, v235, v11\n\t"
      "s_nop 1\n\t"
      "v_permlane16_swap_b32 v234, v235\n\t"
      "v_max_f32 v234, v234, v235\n\t"
      "v_mov_b32 v235, v234\n\t"
      "s_nop 1\n\t"
      "v_permlane32_swap_b32 v234, v235\n\t"
      "v_max_f32 v234, v234, v235\n\t"
      "v_mov_b32 v235, v234\n\t"
      "buffer_load_dwordx4 a[140:143], v248, s[36:39], s50 offen offset:3072  sc1\n\t"
      "s_nop 0\n\t"
      "v_permlane16_swap_b32 v234, v235\n\t"
      "v_pk_mul_f32 v[12:13], v[234:235], s[86:87]\n\t"
      "v_pk_fma_f32 v[0:1], v[0:1], s[86:87], v[12:13] op_sel:[0,0,0] op_sel_hi:[1,1,0] neg_lo:[0,0,1] neg_hi:[0,0,1]\n\t"
      "v_pk_fma_f32 v[2:3], v[2:3], s[86:87], v[12:13] op_sel:[0,0,0] op_sel_hi:[1,1,0] neg_lo:[0,0,1] neg_hi:[0,0,1]\n\t"
      "v_pk_fma_f32 v[4:5], v[4:5], s[86:87], v[12:13] op_sel:[0,0,1] op_sel_hi:[1,1,1] neg_lo:[0,0,1] neg_hi:[0,0,1]\n\t"
      "v_pk_fma_f32 v[6:7], v[6:7], s[86:87], v[12:13] op_sel:[0,0,1] op_sel_hi:[1,1,1] neg_lo:[0,0,1] neg_hi:[0,0,1]\n\t"
      "v_pk_fma_f32 v[8:9], v[8:9], s[86:87], v[12:13] op_sel:[0,0,1] op_sel_hi:[1,1,1] neg_lo:[0,0,1] neg_hi:[0,0,1]\n\t"
      "v_pk_fma_f32 v[10:11], v[10:11], s[86:87], v[12:13] op_sel:[0,0,1] op_sel_hi:[1,1,1] neg_lo:[0,0,1] neg_hi:[0,0,1]\n\t"
      "v_exp_f32 v0, v0\n\t"
      "v_exp_f32 v1, v1\n\t"
      "v_exp_f32 v2, v2\n\t"
      "v_exp_f32 v3, v3\n\t"
      "v_exp_f32 v4, v4\n\t"
      "v_exp_f32 v5, v5\n\t"
      "v_exp_f32 v6, v6\n\t"
      "s_add_u32 s69, s50, 4096\n\t"
      "buffer_load_dwordx4 a[144:147], v248, s[36:39], s69 offen  sc1\n\t"
      "v_exp_f32 v7, v7\n\t"
      "v_exp_f32 v8, v8\n\t"
      "v_exp_f32 v9, v9\n\t"
      "v_exp_f32 v10, v10\n\t"
      "v_exp_f32 v11, v11\n\t"
      "v_mov_b64 v[18:19], 0\n\t"
      "v_cvt_pk_bf16_f32 v16, v0, v1\n\t"
      "v_cvt_pk_bf16_f32 v17, v2, v3\n\t"
      "v_cvt_pk_bf16_f32 v20, v4, v5\n\t"
      "v_cvt_pk_bf16_f32 v21, v6, v7\n\t"
      "v_cvt_pk_bf16_f32 v22, v8, v9\n\t"
      "v_cvt_pk_bf16_f32 v23, v10, v11\n\t"
      "v_mfma_f32_16x16x32_bf16 v[64:67], a[192:195], v[16:19], 0\n\t"
      "buffer_load_dwordx4 a[148:151], v248, s[36:39], s69 offen offset:1024  sc1\n\t"
      "v_pk_add_f32 v[0:1], v[0:1], v[2:3]\n\t"
      "v_mfma_f32_16x16x32_bf16 v[96:99], a[192:195], v[20:23], 0\n\t"
      "v_pk_add_f32 v[4:5], v[4:5], v[6:7]\n\t"
      "v_mfma_f32_16x16x32_bf16 v[68:71], a[196:199], v[16:19], 0\n\t"
      "v_pk_add_f32 v[8:9], v[8:9], v[10:11]\n\t"
      "v_mfma_f32_16x16x32_bf16 v[100:103], a[196:199], v[20:23], 0\n\t"
      "v_add_f32 v236, v0, v1\n\t"
      "v_mfma_f32_16x16x32_bf16 v[72:75], a[200:203], v[16:19], 0\n\t"
      "v_pk_add_f32 v[4:5], v[4:5], v[8:9]\n\t"
      "v_mfma_f32_16x16x32_bf16 v[104:107], a[200:203], v[20:23], 0\n\t"
      "buffer_load_dwordx4 a[152:155], v248, s[36:39], s69 offen offset:2048  sc1\n\t"
      "v_add_f32 v237, v4, v5\n\t"
      "v_mfma_f32_16x16x32_bf16 v[76:79], a[204:207], v[16:19], 0\n\t"
      "v_mfma_f32_16x16x32_bf16 v[108:111], a[204:207], v[20:23], 0\n\t"
      "v_mfma_f32_16x16x32_bf16 v[80:83], a[208:211], v[16:19], 0\n\t"
      "v_mfma_f32_16x16x32_bf16 v[112:115], a[208:211], v[20:23], 0\n\t"
      "v_mfma_f32_16x16x32_bf16 v[84:87], a[212:215], v[16:19], 0\n\t"
      "buffer_load_dwordx4 a[156:159], v248, s[36:39], s69 offen offset:3072  sc1\n\t"
      "v_mfma_f32_16x16x32_bf16 v[116:119], a[212:215], v[20:23], 0\n\t"
      "v_mfma_f32_16x16x32_bf16 v[88:91], a[216:219], v[16:19], 0\n\t"
      "v_mfma_f32_16x16x32_bf16 v[120:123], a[216:219], v[20:23], 0\n\t"
      "v_mfma_f32_16x16x32_bf16 v[92:95], a[220:223], v[16:19], 0\n\t"
      "v_mfma_f32_16x16x32_bf16 v[124:127], a[220:223], v[20:23], 0\n\t"
      "s_waitcnt vmcnt(8)\n\t"
      "s_cmp_lt_u32 s48, 4\n\t"
      "s_cbranch_scc1 WGO%=\n\t"
      "v_mov_b32 v0, s48\n\t"
      "s_mov_b64 exec, 1\n\t"
      "buffer_store_dword v0, off, s[36:39], s63 sc1\n\t"
      "s_mov_b64 exec, -1\n\t"
      "s_cmp_lt_i32 s67, 0\n\t"
      "s_cbranch_scc1 WPB1%=\n\t"
      "v_mov_b32 v8, s68\n\t"
      "s_mov_b64 exec, 1\n\t"
      "buffer_store_dword v8, off, s[36:39], s67 sc1\n\t"
      "s_mov_b64 exec, -1\n\t"
      "s_mov_b32 s67, -1\n\t"
      "WPB1%=:\n\t"
      "WGO%=:\n\t"
      "s_cmp_eq_u32 s64, 1\n\t"
      "s_cbranch_scc1 WKV%=\n\t"
      "s_cmp_lg_u32 s65, 0\n\t"
      "s_cbranch_scc1 WR2%=\n\t"
      "WGO2%=:\n\t"
      "s_cmp_lt_u32 s48, 40\n\t"
      "s_cselect_b32 s71, 1, -1\n\t"
      "s_add_u32 s71, s71, s48\n\t"
      "s_mov_b32 s72, 0x40000\n\t"
      "WLOOP%=:\n\t"
      "s_cmp_ge_u32 s49, 56\n\t"
      "s_cselect_b32 s69, s49, 99\n\t"
      "s_cmp_lt_u32 s69, s48\n\t"
      "s_cbranch_scc1 WRW%=\n\t"
      "s_cmp_eq_u32 s64, 2\n\t"
      "s_cbranch_scc1 WKC%=\n\t"
      "v_lshlrev_b32 v6, 4, v248\n\t"
      "v_mov_b64 v[4:5], 0\n\t"
      "buffer_load_dwordx4 v[0:3], off, s[36:39], s56 sc1\n\t"
      "s_cmp_ge_u32 s49, s48\n\t"
      "s_cbranch_scc1 WL1%=\n\t"
      "buffer_load_dword v4, v6, s[36:39], s55 offen sc1\n\t"
      "buffer_load_dword v5, v6, s[36:39], s55 offen offset:128 sc1\n\t"
      "WL1%=:\n\t"
      "s_waitcnt vmcnt(0)\n\t"
      "s_cmp_lt_i32 s67, 0\n\t"
      "s_cbranch_scc1 WL2%=\n\t"
      "v_mov_b32 v8, s68\n\t"
      "s_mov_b64 exec, 1\n\t"
      "buffer_store_dword v8, off, s[36:39], s67 sc1\n\t"
      "s_mov_b64 exec, -1\n\t"
      "s_mov_b32 s67, -1\n\t"
      "WL2%=:\n\t"
      "v_min_u32 v0, v0, v1\n\t"
      "v_min3_u32 v0, v0, v2, v3\n\t"
      "v_min_u32 v4, v4, v5\n\t"
      "v_readfirstlane_b32 s88, v0\n\t"
      "v_cmp_ge_u32 vcc, s49, v4\n\t"
      "s_lshr_b32 s69, s88, 16\n\t"
      "s_max_u32 s70, s71, s48\n\t"
      "s_cmp_ge_u32 s69, s70\n\t"
      "s_cbranch_scc1 WKL%=\n\t"
      "s_cmp_ge_u32 s69, s71\n\t"
      "s_cbranch_scc1 WKF%=\n\t"
      "s_cbranch_vccz WRW%=\n\t"
      "s_sleep 6\n\t"
      "s_cmp_lt_u32 s49, s48\n\t"
      "s_cbranch_scc1 WKM%=\n\t"
      "s_sub_u32 s69, s71, s69\n\t"
      "s_min_u32 s69, s69, 9\n\t"
      "WKN%=:\n\t"
      "s_sub_u32 s69, s69, 1\n\t"
      "s_cmp_eq_u32 s69, 0\n\t"
      "s_cbranch_scc1 WKM%=\n\t"
      "s_sleep 48\n\t"
      "s_branch WKN%=\n\t"
      "WKM%=:\n\t"
      "s_sub_u32 s72, s72, 1\n\t"
      "s_cmp_gt_i32 s72, 0\n\t"
      "s_cbranch_scc1 WLOOP%=\n\t"
      "s_branch WKL%=\n\t"
      "WFIN%=:\n\t"
      "s_mov_b32 s64, 0\n\t"
      "s_mov_b32 s72, 0x40000\n\t"
      "s_waitcnt vmcnt(2)\n\t"
      "WFIN1%=:\n\t"
      "s_cmp_lt_i32 s67, 0\n\t"
      "s_cbranch_scc1 WF1%=\n\t"
      "v_mov_b32 v8, s68\n\t"
      "s_mov_b64 exec, 1\n\t"
      "buffer_store_dword v8, off, s[36:39], s67 sc1\n\t"
      "s_mov_b64 exec, -1\n\t"
      "s_mov_b32 s67, -1\n\t"
      "WF1%=:\n\t"
      "s_cmp_lg_u32 s65, 0\n\t"
      "s_cbranch_scc1 WRC%=\n\t"
      "s_cmp_ge_u32 s49, 64\n\t"
      "s_cbranch_scc1 WEND%=\n\t"
      "s_cmp_ge_u32 s49, 56\n\t"
      "s_cbranch_scc1 WRS%=\n\t"
      "v_lshlrev_b32 v6, 4, v248\n\t"
      "WRF%=:\n\t"
      "buffer_load_dword v4, v6, s[36:39], s55 offen sc1\n\t"
      "buffer_load_dword v5, v6, s[36:39], s55 offen offset:128 sc1\n\t"
      "s_waitcnt vmcnt(0)\n\t"
      "v_min_u32 v4, v4, v5\n\t"
      "v_cmp_ge_u32 vcc, s49, v4\n\t"
      "s_nop 0\n\t"
      "s_cbranch_vccz WRW%=\n\t"
      "s_sleep 8\n\t"
      "s_sub_u32 s72, s72, 1\n\t"
      "s_cmp_gt_i32 s72, 0\n\t"
      "s_cbranch_scc1 WRF%=\n\t"
      "WRS%=:\n\t"
      "s_waitcnt vmcnt(0)\n\t"
      "WRW%=:\n\t"
      "s_lshl_b32 s70, s49, 5\n\t"
      "s_add_u32 s70, s70, s60\n\t"
      "s_lshl_b32 s70, s70, 10\n\t"
      "buffer_load_dwordx2 v[224:225], v249, s[40:43], s70 offen \n\t"
      "buffer_load_dwordx2 v[226:227], v249, s[40:43], s70 offen offset:512 \n\t"
      "WRL%=:\n\t"
      "s_lshl_b32 s69, s49, 18\n\t"
      "s_add_u32 s69, s69, s54\n\t"
      "buffer_load_dwordx4 v[160:163], v248, s[36:39], s69 offen sc1\n\t"
      "s_add_u32 s69, s69, 0x8000\n\t"
      "buffer_load_dwordx4 v[164:167], v248, s[36:39], s69 offen sc1\n\t"
      "s_add_u32 s69, s69, 0x8000\n\t"
      "buffer_load_dwordx4 v[168:171], v248, s[36:39], s69 offen sc1\n\t"
      "s_add_u32 s69, s69, 0x8000\n\t"
      "buffer_load_dwordx4 v[172:175], v248, s[36:39], s69 offen sc1\n\t"
      "s_add_u32 s69, s69, 0x8000\n\t"
      "buffer_load_dwordx4 v[176:179], v248, s[36:39], s69 offen sc1\n\t"
      "s_add_u32 s69, s69, 0x8000\n\t"
      "buffer_load_dwordx4 v[180:183], v248, s[36:39], s69 offen sc1\n\t"
      "s_add_u32 s69, s69, 0x8000\n\t"
      "buffer_load_dwordx4 v[184:187], v248, s[36:39], s69 offen sc1\n\t"
      "s_add_u32 s69, s69, 0x8000\n\t"
      "buffer_load_dwordx4 v[188:191], v248, s[36:39], s69 offen sc1\n\t"
      "s_waitcnt vmcnt(0)\n\t"
      "s_cmp_lt_i32 s67, 0\n\t"
      "s_cbranch_scc1 WF2%=\n\t"
      "v_mov_b32 v8, s68\n\t"
      "s_mov_b64 exec, 1\n\t"
      "buffer_store_dword v8, off, s[36:39], s67 sc1\n\t"
      "s_mov_b64 exec, -1\n\t"
      "s_mov_b32 s67, -1\n\t"
      "WF2%=:\n\t"
      "WRC%=:\n\t"
      "v_mfma_f32_16x16x32_bf16 v[16:19], a[224:227], v[160:163], 0\n\t"
      "v_mfma_f32_16x16x32_bf16 v[20:23], a[228:231], v[160:163], 0\n\t"
      "v_and_b32 v193, s74, v224\n\t"
      "v_lshlrev_b32 v192, 16, v224\n\t"
      "v_mfma_f32_16x16x32_bf16 v[16:19], a[224:227], v[164:167], v[16:19]\n\t"
      "v_mfma_f32_16x16x32_bf16 v[20:23], a[228:231], v[164:167], v[20:23]\n\t"
      "v_and_b32 v195, s74, v225\n\t"
      "v_lshlrev_b32 v194, 16, v225\n\t"
      "v_mfma_f32_16x16x32_bf16 v[16:19], a[224:227], v[168:171], v[16:19]\n\t"
      "v_mfma_f32_16x16x32_bf16 v[20:23], a[228:231], v[168:171], v[20:23]\n\t"
      "v_and_b32 v197, s74, v226\n\t"
      "v_lshlrev_b32 v196, 16, v226\n\t"
      "v_mfma_f32_16x16x32_bf16 v[16:19], a[224:227], v[172:175], v[16:19]\n\t"
      "v_mfma_f32_16x16x32_bf16 v[20:23], a[228:231], v[172:175], v[20:23]\n\t"
      "v_and_b32 v199, s74, v227\n\t"
      "v_lshlrev_b32 v198, 16, v227\n\t"
      "v_mfma_f32_16x16x32_bf16 v[16:19], a[224:227], v[176:179], v[16:19]\n\t"
      "v_mfma_f32_16x16x32_bf16 v[20:23], a[228:231], v[176:179], v[20:23]\n\t"
      "v_mfma_f32_16x16x32_bf16 v[16:19], a[224:227], v[180:183], v[16:19]\n\t"
      "v_mfma_f32_16x16x32_bf16 v[20:23], a[228:231], v[180:183], v[20:23]\n\t"
      "v_mfma_f32_16x16x32_bf16 v[16:19], a[224:227], v[184:187], v[16:19]\n\t"
      "v_mfma_f32_16x16x32_bf16 v[20:23], a[228:231], v[184:187], v[20:23]\n\t"
      "v_mfma_f32_16x16x32_bf16 v[16:19], a[224:227], v[188:191], v[16:19]\n\t"
      "v_mfma_f32_16x16x32_bf16 v[20:23], a[228:231], v[188:191], v[20:23]\n\t"
      "s_nop 6\n\t"
      "v_cvt_pk_bf16_f32 v32, v16, v17\n\t"
      "v_cvt_pk_bf16_f32 v34, v18, v19\n\t"
      "v_cvt_pk_bf16_f32 v36, v20, v21\n\t"
      "v_cvt_pk_bf16_f32 v38, v22, v23\n\t"
      "v_and_b32 v33, s74, v32\n\t"
      "v_lshlrev_b32 v32, 16, v32\n\t"
      "v_and_b32 v35, s74, v34\n\t"
      "v_lshlrev_b32 v34, 16, v34\n\t"
      "v_and_b32 v37, s74, v36\n\t"
      "v_lshlrev_b32 v36, 16, v36\n\t"
      "v_and_b32 v39, s74, v38\n\t"
      "v_lshlrev_b32 v38, 16, v38\n\t"
      "v_pk_add_f32 v[192:193], v[192:193], v[32:33]\n\t"
      "v_pk_add_f32 v[194:195], v[194:195], v[34:35]\n\t"
      "v_pk_add_f32 v[196:197], v[196:197], v[36:37]\n\t"
      "v_pk_add_f32 v[198:199], v[198:199], v[38:39]\n\t"
      "v_cvt_pk_bf16_f32 v200, v192, v193\n\t"
      "v_cvt_pk_bf16_f32 v201, v194, v195\n\t"
      "v_cvt_pk_bf16_f32 v202, v196, v197\n\t"
      "v_cvt_pk_bf16_f32 v203, v198, v199\n\t"
      "v_and_b32 v193, s74, v200\n\t"
      "v_lshlrev_b32 v192, 16, v200\n\t"
      "v_and_b32 v195, s74, v201\n\t"
      "v_lshlrev_b32 v194, 16, v201\n\t"
      "v_and_b32 v197, s74, v202\n\t"
      "v_lshlrev_b32 v196, 16, v202\n\t"
      "v_and_b32 v199, s74, v203\n\t"
      "v_lshlrev_b32 v198, 16, v203\n\t"
      "v_pk_add_f32 v[32:33], v[192:193], v[196:197]\n\t"
      "v_pk_add_f32 v[34:35], v[194:195], v[198:199]\n\t"
      "v_pk_add_f32 v[32:33], v[32:33], v[34:35]\n\t"
      "v_add_f32 v32, v32, v33\n\t"
      "s_nop 1\n\t"
      "v_add_f32_dpp v32, v32, v32 quad_perm:[1,0,3,2] row_mask:0xf bank_mask:0xf\n\t"
      "s_nop 1\n\t"
      "v_add_f32_dpp v32, v32, v32 quad_perm:[2,3,0,1] row_mask:0xf bank_mask:0xf\n\t"
      "s_nop 1\n\t"
      "v_add_f32_dpp v32, v32, v32 row_ror:4 row_mask:0xf bank_mask:0xf\n\t"
      "s_nop 1\n\t"
      "v_add_f32_dpp v32, v32, v32 row_ror:8 row_mask:0xf bank_mask:0xf\n\t"
      "v_mov_b32 v33, v32\n\t"
      "s_nop 1\n\t"
      "v_permlane16_swap_b32 v32, v33\n\t"
      "v_add_f32 v32, v32, v33\n\t"
      "v_mov_b32 v33, v32\n\t"
      "s_nop 1\n\t"
      "v_permlane32_swap_b32 v32, v33\n\t"
      "v_add_f32 v32, v32, v33\n\t"
      "v_cmp_u_f32 vcc, v32, v32\n\t"
      "v_mul_f32 v204, 0x3b000000, v32\n\t"
      "s_cbranch_vccnz WRN%=\n\t"
      "WRD%=:\n\t"
      "v_pk_add_f32 v[40:41], v[192:193], v[204:205] op_sel_hi:[1,0] neg_lo:[0,1] neg_hi:[0,1]\n\t"
      "v_pk_add_f32 v[42:43], v[194:195], v[204:205] op_sel_hi:[1,0] neg_lo:[0,1] neg_hi:[0,1]\n\t"
      "v_pk_add_f32 v[44:45], v[196:197], v[204:205] op_sel_hi:[1,0] neg_lo:[0,1] neg_hi:[0,1]\n\t"
      "v_pk_add_f32 v[46:47], v[198:199], v[204:205] op_sel_hi:[1,0] neg_lo:[0,1] neg_hi:[0,1]\n\t"
      "v_pk_mul_f32 v[32:33], v[40:41], v[40:41]\n\t"
      "v_pk_fma_f32 v[32:33], v[42:43], v[42:43], v[32:33]\n\t"
      "v_pk_fma_f32 v[32:33], v[44:45], v[44:45], v[32:33]\n\t"
      "v_pk_fma_f32 v[32:33], v[46:47], v[46:47], v[32:33]\n\t"
      "v_add_f32 v32, v32, v33\n\t"
      "s_nop 1\n\t"
      "v_add_f32_dpp v32, v32, v32 quad_perm:[1,0,3,2] row_mask:0xf bank_mask:0xf\n\t"
      "s_nop 1\n\t"
      "v_add_f32_dpp v32, v32, v32 quad_perm:[2,3,0,1] row_mask:0xf bank_mask:0xf\n\t"
      "s_nop 1\n\t"
      "v_add_f32_dpp v32, v32, v32 row_ror:4 row_mask:0xf bank_mask:0xf\n\t"
      "s_nop 1\n\t"
      "v_add_f32_dpp v32, v32, v32 row_ror:8 row_mask:0xf bank_mask:0xf\n\t"
      "v_mov_b32 v33, v32\n\t"
      "s_nop 1\n\t"
      "v_permlane16_swap_b32 v32, v33\n\t"
      "v_add_f32 v32, v32, v33\n\t"
      "v_mov_b32 v33, v32\n\t"
      "s_nop 1\n\t"
      "v_permlane32_swap_b32 v32, v33\n\t"
      "v_add_f32 v32, v32, v33\n\t"
      "v_mul_f32 v205, 0x3b000000, v32\n\t"
      "v_cvt_pk_bf16_f32 v204, v204, v205\n\t"
      "v_and_b32 v205, s74, v204\n\t"
      "v_lshlrev_b32 v204, 16, v204\n\t"
      "v_add_f32 v205, 0x3727c5ac, v205\n\t"
      "v_pk_add_f32 v[40:41], v[192:193], v[204:205] op_sel_hi:[1,0] neg_lo:[0,1] neg_hi:[0,1]\n\t"
      "v_pk_add_f32 v[42:43], v[194:195], v[204:205] op_sel_hi:[1,0] neg_lo:[0,1] neg_hi:[0,1]\n\t"
      "v_pk_add_f32 v[44:45], v[196:197], v[204:205] op_sel_hi:[1,0] neg_lo:[0,1] neg_hi:[0,1]\n\t"
      "v_pk_add_f32 v[46:47], v[198:199], v[204:205] op_sel_hi:[1,0] neg_lo:[0,1] neg_hi:[0,1]\n\t"
      "v_cvt_pk_bf16_f32 v205, v205, v205\n\t"
      "v_and_b32 v205, s74, v205\n\t"
      "v_sqrt_f32 v205, v205\n\t"
      "v_cvt_pk_bf16_f32 v40, v40, v41\n\t"
      "v_cvt_pk_bf16_f32 v42, v42, v43\n\t"
      "v_cvt_pk_bf16_f32 v44, v44, v45\n\t"
      "v_cvt_pk_bf16_f32 v46, v46, v47\n\t"
      "v_cvt_pk_bf16_f32 v205, v205, v205\n\t"
      "v_and_b32 v205, s74, v205\n\t"
      "v_rcp_f32 v204, v205\n\t"
      "v_and_b32 v41, s74, v40\n\t"
      "v_lshlrev_b32 v40, 16, v40\n\t"
      "v_and_b32 v43, s74, v42\n\t"
      "v_lshlrev_b32 v42, 16, v42\n\t"
      "v_and_b32 v45, s74, v44\n\t"
      "v_lshlrev_b32 v44, 16, v44\n\t"
      "v_and_b32 v47, s74, v46\n\t"
      "v_lshlrev_b32 v46, 16, v46\n\t"
      "v_pk_mul_f32 v[40:41], v[40:41], v[204:205] op_sel_hi:[1,0]\n\t"
      "v_pk_mul_f32 v[42:43], v[42:43], v[204:205] op_sel_hi:[1,0]\n\t"
      "v_pk_mul_f32 v[44:45], v[44:45], v[204:205] op_sel_hi:[1,0]\n\t"
      "v_pk_mul_f32 v[46:47], v[46:47], v[204:205] op_sel_hi:[1,0]\n\t"
      "v_cvt_pk_bf16_f32 v52, v40, v41\n\t"
      "v_cvt_pk_bf16_f32 v53, v42, v43\n\t"
      "v_cvt_pk_bf16_f32 v54, v44, v45\n\t"
      "v_cvt_pk_bf16_f32 v55, v46, v47\n\t"
      "v_and_b32 v33, s74, v52\n\t"
      "v_lshlrev_b32 v32, 16, v52\n\t"
      "v_and_b32 v35, s74, v53\n\t"
      "v_lshlrev_b32 v34, 16, v53\n\t"
      "v_and_b32 v37, s74, v54\n\t"
      "v_lshlrev_b32 v36, 16, v54\n\t"
      "v_and_b32 v39, s74, v55\n\t"
      "v_lshlrev_b32 v38, 16, v55\n\t"
      "v_pk_mul_f32 v[40:41], v[32:33], v[240:241]\n\t"
      "v_pk_fma_f32 v[40:41], v[34:35], v[242:243], v[40:41]\n\t"
      "v_pk_fma_f32 v[40:41], v[36:37], v[244:245], v[40:41]\n\t"
      "v_pk_fma_f32 v[40:41], v[38:39], v[246:247], v[40:41]\n\t"
      "v_add_f32 v40, v40, v41\n\t"
      "s_lshl_b32 s73, s49, 5\n\t"
      "s_add_u32 s73, s73, s60\n\t"
      "s_lshl_b32 s69, s73, 2\n\t"
      "s_add_u32 s69, s69, s57\n\t"
      "s_lshl_b32 s70, s73, 7\n\t"
      "s_add_u32 s70, s70, s57\n\t"
      "s_sub_u32 s70, s70, 212992\n\t"
      "s_cmp_ge_u32 s49, 56\n\t"
      "s_cselect_b32 s70, s70, s69\n\t"
      "v_add_f32_dpp v40, v40, v40 quad_perm:[1,0,3,2] row_mask:0xf bank_mask:0xf\n\t"
      "s_nop 1\n\t"
      "v_add_f32_dpp v40, v40, v40 quad_perm:[2,3,0,1] row_mask:0xf bank_mask:0xf\n\t"
      "s_nop 1\n\t"
      "v_add_f32_dpp v40, v40, v40 row_ror:4 row_mask:0xf bank_mask:0xf\n\t"
      "s_nop 1\n\t"
      "v_add_f32_dpp v40, v40, v40 row_ror:8 row_mask:0xf bank_mask:0xf\n\t"
      "v_mov_b32 v41, v40\n\t"
      "s_nop 1\n\t"
      "v_permlane16_swap_b32 v40, v41\n\t"
      "v_add_f32 v40, v40, v41\n\t"
      "v_mov_b32 v41, v40\n\t"
      "s_nop 1\n\t"
      "v_permlane32_swap_b32 v40, v41\n\t"
      "v_add_f32 v40, v40, v41\n\t"
      "v_cvt_pk_bf16_f32 v40, v40, v40\n\t"
      "v_and_b32 v40, s74, v40\n\t"
      "s_mov_b64 exec, 1\n\t"
      "buffer_store_dword v40, off, s[36:39], s70 sc1\n\t"
      "s_mov_b64 exec, -1\n\t"
      "v_pk_add_f32 v[192:193], v[192:193], v[32:33]\n\t"
      "v_pk_add_f32 v[194:195], v[194:195], v[34:35]\n\t"
      "v_pk_add_f32 v[196:197], v[196:197], v[36:37]\n\t"
      "v_pk_add_f32 v[198:199], v[198:199], v[38:39]\n\t"
      "v_readfirstlane_b32 s68, v40\n\t"
      "v_cvt_pk_bf16_f32 v56, v192, v193\n\t"
      "v_cvt_pk_bf16_f32 v57, v194, v195\n\t"
      "v_cvt_pk_bf16_f32 v58, v196, v197\n\t"
      "v_cvt_pk_bf16_f32 v59, v198, v199\n\t"
      "s_add_u32 s67, s69, 8192\n\t"
      "s_lshl_b32 s73, s73, 10\n\t"
      "s_add_u32 s69, s73, s58\n\t"
      "s_add_u32 s70, s73, s59\n\t"
      "buffer_store_dwordx2 v[56:57], v249, s[44:47], s73 offen sc1\n\t"
      "buffer_store_dwordx2 v[52:53], v249, s[36:39], s69 offen sc1\n\t"
      "buffer_store_dwordx2 v[200:201], v249, s[36:39], s70 offen sc1\n\t"
      "buffer_store_dwordx2 v[58:59], v249, s[44:47], s73 offen offset:512 sc1\n\t"
      "buffer_store_dwordx2 v[54:55], v249, s[36:39], s69 offen offset:512 sc1\n\t"
      "buffer_store_dwordx2 v[202:203], v249, s[36:39], s70 offen offset:512 sc1\n\t"
      "s_mov_b32 s49, s89\n\t"
      "s_add_u32 s89, s89, 16\n\t"
      "s_lshr_b32 s69, s89, 4\n\t"
      "s_cmp_eq_u32 s69, 3\n\t"
      "s_cselect_b32 s89, s93, s89\n\t"
      "s_cmp_ge_u32 s49, 48\n\t"
      "s_cselect_b32 s60, s94, s60\n\t"
      "s_cselect_b32 s54, s95, s54\n\t"
      "s_cmp_ge_u32 s48, 64\n\t"
      "s_cbranch_scc1 WRX%=\n\t"
      "s_cmp_lg_u32 s65, 0\n\t"
      "s_mov_b32 s65, 0\n\t"
      "s_cbranch_scc1 WGO%=\n\t"
      "s_mov_b32 s66, 0\n\t"
      "s_branch WLOOP%=\n\t"
      "WRX%=:\n\t"
      "s_mov_b32 s65, 0\n\t"
      "s_waitcnt vmcnt(0)\n\t"
      "s_branch WFIN1%=\n\t"
      "WRN%=:\n\t"
      "v_max3_u32 v40, v160, v161, v162\n\t"
      "v_max3_u32 v40, v40, v163, v164\n\t"
      "v_max3_u32 v40, v40, v165, v166\n\t"
      "v_max3_u32 v40, v40, v167, v168\n\t"
      "v_max3_u32 v40, v40, v169, v170\n\t"
      "v_max3_u32 v40, v40, v171, v172\n\t"
      "v_max3_u32 v40, v40, v173, v174\n\t"
      "v_max3_u32 v40, v40, v175, v176\n\t"
      "v_max3_u32 v40, v40, v177, v178\n\t"
      "v_max3_u32 v40, v40, v179, v180\n\t"
      "v_max3_u32 v40, v40, v181, v182\n\t"
      "v_max3_u32 v40, v40, v183, v184\n\t"
      "v_max3_u32 v40, v40, v185, v186\n\t"
      "v_max3_u32 v40, v40, v187, v188\n\t"
      "v_max3_u32 v40, v40, v189, v190\n\t"
      "v_max_u32 v40, v40, v191\n\t"
      "v_cmp_eq_u32 vcc, -1, v40\n\t"
      "s_nop 0\n\t"
      "s_cbranch_vccz WRD%=\n\t"
      "s_cmp_lg_u32 s65, 0\n\t"
      "s_cbranch_scc1 WRD%=\n\t"
      "s_sub_u32 s72, s72, 1\n\t"
      "s_cmp_gt_i32 s72, 0\n\t"
      "s_cbranch_scc1 WRL%=\n\t"
      "s_branch WRD%=\n\t"
      "WH2%=:\n\t"
      "s_mov_b32 s72, 0x40000\n\t"
      "WH3%=:\n\t"
      "buffer_load_dwordx2 v[0:1], off, s[36:39], s99 sc1\n\t"
      "buffer_load_dwordx2 v[2:3], off, s[36:39], s99 offset:16 sc1\n\t"
      "s_sub_u32 s72, s72, 1\n\t"
      "s_waitcnt vmcnt(0)\n\t"
      "v_bitop3_b32 v0, v0, v1, s62 bitop3:0x7e\n\t"
      "v_bitop3_b32 v2, v2, v3, s62 bitop3:0x7e\n\t"
      "v_or_b32 v0, v0, v2\n\t"
      "v_cmp_ne_u32 vcc, 0, v0\n\t"
      "s_cmp_eq_u32 s72, 0\n\t"
      "s_cbranch_scc1 WT0%=\n\t"
      "s_cbranch_vccnz WH3%=\n\t"
      "s_branch WT0%=\n\t"
      "WKV1%=:\n\t"
      "s_cmp_eq_u32 s66, 0\n\t"
      "s_cbranch_scc1 WB1%=\n\t"
      "WB2%=:\n\t"
      "s_mov_b32 s65, 1\n\t"
      "v_mfma_f32_16x16x32_bf16 v[0:3], v[128:131], a[160:163], 0\n\t"
      "v_mfma_f32_16x16x32_bf16 v[4:7], v[128:131], a[164:167], 0\n\t"
      "v_mfma_f32_16x16x32_bf16 v[0:3], v[132:135], a[168:171], v[0:3]\n\t"
      "v_mfma_f32_16x16x32_bf16 v[4:7], v[132:135], a[172:175], v[4:7]\n\t"
      "v_mfma_f32_16x16x32_bf16 v[0:3], v[136:139], a[176:179], v[0:3]\n\t"
      "v_mfma_f32_16x16x32_bf16 v[4:7], v[136:139], a[180:183], v[4:7]\n\t"
      "v_mfma_f32_16x16x32_bf16 v[0:3], v[140:143], a[184:187], v[0:3]\n\t"
      "v_mfma_f32_16x16x32_bf16 v[4:7], v[140:143], a[188:191], v[4:7]\n\t"
      "s_add_u32 s51, s51, 262144\n\t"
      "s_add_u32 s52, s52, 262144\n\t"
      "s_lshl_b32 s70, s49, 5\n\t"
      "s_add_u32 s70, s70, s60\n\t"
      "s_lshl_b32 s70, s70, 10\n\t"
      "buffer_load_dwordx2 v[224:225], v249, s[40:43], s70 offen \n\t"
      "s_nop 0\n\t"
      "v_cvt_pk_bf16_f32 v0, v0, v1\n\t"
      "v_cvt_pk_bf16_f32 v2, v2, v3\n\t"
      "v_cvt_pk_bf16_f32 v4, v4, v5\n\t"
      "v_cvt_pk_bf16_f32 v6, v6, v7\n\t"
      "v_and_b32 v1, s74, v0\n\t"
      "v_lshlrev_b32 v0, 16, v0\n\t"
      "v_and_b32 v3, s74, v2\n\t"
      "buffer_load_dwordx2 v[226:227], v249, s[40:43], s70 offen offset:512 \n\t"
      "v_lshlrev_b32 v2, 16, v2\n\t"
      "v_and_b32 v5, s74, v4\n\t"
      "v_lshlrev_b32 v4, 16, v4\n\t"
      "v_and_b32 v7, s74, v6\n\t"
      "v_lshlrev_b32 v6, 16, v6\n\t"
      "v_pk_mul_f32 v[0:1], v[0:1], s[84:85]\n\t"
      "v_pk_mul_f32 v[2:3], v[2:3], s[84:85]\n\t"
      "s_lshl_b32 s69, s49, 18\n\t"
      "s_add_u32 s69, s69, s54\n\t"
      "buffer_load_dwordx4 v[160:163], v248, s[36:39], s69 offen sc1\n\t"
      "v_pk_mul_f32 v[4:5], v[4:5], s[84:85]\n\t"
      "v_pk_mul_f32 v[6:7], v[6:7], s[84:85]\n\t"
      "v_cvt_pk_bf16_f32 v0, v0, v1\n\t"
      "v_cvt_pk_bf16_f32 v2, v2, v3\n\t"
      "v_cvt_pk_bf16_f32 v4, v4, v5\n\t"
      "v_cvt_pk_bf16_f32 v6, v6, v7\n\t"
      "v_and_b32 v1, s74, v0\n\t"
      "s_add_u32 s69, s69, 0x8000\n\t"
      "buffer_load_dwordx4 v[164:167], v248, s[36:39], s69 offen sc1\n\t"
      "v_lshlrev_b32 v0, 16, v0\n\t"
      "v_and_b32 v3, s74, v2\n\t"
      "v_lshlrev_b32 v2, 16, v2\n\t"
      "v_and_b32 v5, s74, v4\n\t"
      "v_lshlrev_b32 v4, 16, v4\n\t"
      "v_and_b32 v7, s74, v6\n\t"
      "v_lshlrev_b32 v6, 16, v6\n\t"
      "s_add_u32 s69, s69, 0x8000\n\t"
      "buffer_load_dwordx4 v[168:171], v248, s[36:39], s69 offen sc1\n\t"
      "v_max3_f32 v8, v0, v1, v2\n\t"
      "v_max3_f32 v8, v8, v3, v234\n\t"
      "v_max3_f32 v9, v4, v5, v6\n\t"
      "v_max3_f32 v9, v9, v7, v235\n\t"
      "s_nop 1\n\t"
      "v_permlane16_swap_b32 v8, v9\n\t"
      "v_max_f32 v8, v8, v9\n\t"
      "v_mov_b32 v9, v8\n\t"
      "s_add_u32 s69, s69, 0x8000\n\t"
      "buffer_load_dwordx4 v[172:175], v248, s[36:39], s69 offen sc1\n\t"
      "v_permlane16_swap_b32 v8, v9\n\t"
      "v_pk_mul_f32 v[8:9], v[8:9], s[86:87]\n\t"
      "v_pk_fma_f32 v[10:11], v[234:235], s[86:87], v[8:9] neg_lo:[0,0,1] neg_hi:[0,0,1]\n\t"
      "v_pk_fma_f32 v[0:1], v[0:1], s[86:87], v[8:9] op_sel:[0,0,0] op_sel_hi:[1,1,0] neg_lo:[0,0,1] neg_hi:[0,0,1]\n\t"
      "v_pk_fma_f32 v[2:3], v[2:3], s[86:87], v[8:9] op_sel:[0,0,0] op_sel_hi:[1,1,0] neg_lo:[0,0,1] neg_hi:[0,0,1]\n\t"
      "v_pk_fma_f32 v[4:5], v[4:5], s[86:87], v[8:9] op_sel:[0,0,1] op_sel_hi:[1,1,1] neg_lo:[0,0,1] neg_hi:[0,0,1]\n\t"
      "v_pk_fma_f32 v[6:7], v[6:7], s[86:87], v[8:9] op_sel:[0,0,1] op_sel_hi:[1,1,1] neg_lo:[0,0,1] neg_hi:[0,0,1]\n\t"
      "s_add_u32 s69, s69, 0x8000\n\t"
      "buffer_load_dwordx4 v[176:179], v248, s[36:39], s69 offen sc1\n\t"
      "v_exp_f32 v10, v10\n\t"
      "v_exp_f32 v11, v11\n\t"
      "v_exp_f32 v0, v0\n\t"
      "v_exp_f32 v1, v1\n\t"
      "s_add_u32 s69, s69, 0x8000\n\t"
      "buffer_load_dwordx4 v[180:183], v248, s[36:39], s69 offen sc1\n\t"
      "v_exp_f32 v2, v2\n\t"
      "v_exp_f32 v3, v3\n\t"
      "v_exp_f32 v4, v4\n\t"
      "v_exp_f32 v5, v5\n\t"
      "s_add_u32 s69, s69, 0x8000\n\t"
      "buffer_load_dwordx4 v[184:187], v248, s[36:39], s69 offen sc1\n\t"
      "v_exp_f32 v6, v6\n\t"
      "v_exp_f32 v7, v7\n\t"
      "v_pk_mul_f32 v[12:13], v[236:237], v[10:11]\n\t"
      "v_pk_add_f32 v[14:15], v[0:1], v[2:3]\n\t"
      "s_add_u32 s69, s69, 0x8000\n\t"
      "buffer_load_dwordx4 v[188:191], v248, s[36:39], s69 offen sc1\n\t"
      "v_pk_add_f32 v[16:17], v[4:5], v[6:7]\n\t"
      "v_add_f32 v14, v14, v15\n\t"
      "v_add_f32 v15, v16, v17\n\t"
      "v_fma_f32 v12, v14, 0.5, v12\n\t"
      "v_fma_f32 v13, v15, 0.5, v13\n\t"
      "s_nop 1\n\t"
      "v_permlane16_swap_b32 v12, v13\n\t"
      "v_add_f32 v12, v12, v13\n\t"
      "s_add_u32 s69, s50, 8192\n\t"
      "s_add_u32 m0, s61, 0\n\t"
      "s_nop 0\n\t"
      "buffer_load_dwordx4 v250, s[36:39], s69 offen sc1 lds\n\t"
      "v_mov_b32 v13, v12\n\t"
      "s_nop 1\n\t"
      "v_permlane32_swap_b32 v12, v13\n\t"
      "v_add_f32 v12, v12, v13\n\t"
      "v_mov_b32 v13, v12\n\t"
      "s_nop 1\n\t"
      "v_permlane16_swap_b32 v12, v13\n\t"
      "v_rcp_f32 v12, v12\n\t"
      "buffer_load_dwordx4 v250, s[36:39], s69 offen offset:1024 sc1 lds\n\t"
      "v_rcp_f32 v13, v13\n\t"
      "s_nop 0\n\t"
      "v_pk_mul_f32 v[10:11], v[10:11], v[12:13]\n\t"
      "v_pk_mul_f32 v[12:13], v[12:13], 0.5 op_sel_hi:[1,0]\n\t"
      "v_pk_mul_f32 v[0:1], v[0:1], v[12:13] op_sel:[0,0] op_sel_hi:[1,0]\n\t"
      "v_pk_mul_f32 v[2:3], v[2:3], v[12:13] op_sel:[0,0] op_sel_hi:[1,0]\n\t"
      "v_pk_mul_f32 v[4:5], v[4:5], v[12:13] op_sel:[0,1] op_sel_hi:[1,1]\n\t"
      "buffer_load_dwordx4 v250, s[36:39], s69 offen offset:2048 sc1 lds\n\t"
      "v_pk_mul_f32 v[6:7], v[6:7], v[12:13] op_sel:[0,1] op_sel_hi:[1,1]\n\t"
      "v_cvt_pk_bf16_f32 v16, v0, v1\n\t"
      "v_cvt_pk_bf16_f32 v17, v2, v3\n\t"
      "v_cvt_pk_bf16_f32 v18, v4, v5\n\t"
      "v_cvt_pk_bf16_f32 v19, v6, v7\n\t"
      "v_pk_mul_f32 v[64:65], v[64:65], v[10:11] op_sel:[0,0] op_sel_hi:[1,0]\n\t"
      "v_pk_mul_f32 v[66:67], v[66:67], v[10:11] op_sel:[0,0] op_sel_hi:[1,0]\n\t"
      "buffer_load_dwordx4 v250, s[36:39], s69 offen offset:3072 sc1 lds\n\t"
      "v_pk_mul_f32 v[68:69], v[68:69], v[10:11] op_sel:[0,0] op_sel_hi:[1,0]\n\t"
      "v_pk_mul_f32 v[70:71], v[70:71], v[10:11] op_sel:[0,0] op_sel_hi:[1,0]\n\t"
      "v_mfma_f32_16x16x16_bf16 v[64:67], v[144:145], v[16:17], v[64:67]\n\t"
      "v_pk_mul_f32 v[72:73], v[72:73], v[10:11] op_sel:[0,0] op_sel_hi:[1,0]\n\t"
      "v_pk_mul_f32 v[74:75], v[74:75], v[10:11] op_sel:[0,0] op_sel_hi:[1,0]\n\t"
      "v_mfma_f32_16x16x16_bf16 v[68:71], v[146:147], v[16:17], v[68:71]\n\t"
      "s_add_u32 s69, s50, 12288\n\t"
      "s_add_u32 m0, s61, 4096\n\t"
      "s_nop 0\n\t"
      "buffer_load_dwordx4 v250, s[36:39], s69 offen sc1 lds\n\t"
      "v_pk_mul_f32 v[76:77], v[76:77], v[10:11] op_sel:[0,0] op_sel_hi:[1,0]\n\t"
      "v_pk_mul_f32 v[78:79], v[78:79], v[10:11] op_sel:[0,0] op_sel_hi:[1,0]\n\t"
      "v_mfma_f32_16x16x16_bf16 v[72:75], v[148:149], v[16:17], v[72:75]\n\t"
      "v_pk_mul_f32 v[80:81], v[80:81], v[10:11] op_sel:[0,0] op_sel_hi:[1,0]\n\t"
      "v_pk_mul_f32 v[82:83], v[82:83], v[10:11] op_sel:[0,0] op_sel_hi:[1,0]\n\t"
      "buffer_load_dwordx4 v250, s[36:39], s69 offen offset:1024 sc1 lds\n\t"
      "v_mfma_f32_16x16x16_bf16 v[76:79], v[150:151], v[16:17], v[76:79]\n\t"
      "v_pk_mul_f32 v[84:85], v[84:85], v[10:11] op_sel:[0,0] op_sel_hi:[1,0]\n\t"
      "v_pk_mul_f32 v[86:87], v[86:87], v[10:11] op_sel:[0,0] op_sel_hi:[1,0]\n\t"
      "v_mfma_f32_16x16x16_bf16 v[80:83], v[152:153], v[16:17], v[80:83]\n\t"
      "v_pk_mul_f32 v[88:89], v[88:89], v[10:11] op_sel:[0,0] op_sel_hi:[1,0]\n\t"
      "v_pk_mul_f32 v[90:91], v[90:91], v[10:11] op_sel:[0,0] op_sel_hi:[1,0]\n\t"
      "buffer_load_dwordx4 v250, s[36:39], s69 offen offset:2048 sc1 lds\n\t"
      "v_mfma_f32_16x16x16_bf16 v[84:87], v[154:155], v[16:17], v[84:87]\n\t"
      "v_pk_mul_f32 v[92:93], v[92:93], v[10:11] op_sel:[0,0] op_sel_hi:[1,0]\n\t"
      "v_pk_mul_f32 v[94:95], v[94:95], v[10:11] op_sel:[0,0] op_sel_hi:[1,0]\n\t"
      "v_cvt_pk_bf16_f32 v24, v64, v65\n\t"
      "v_cvt_pk_bf16_f32 v25, v66, v67\n\t"
      "v_cvt_pk_bf16_f32 v26, v68, v69\n\t"
      "buffer_load_dwordx4 v250, s[36:39], s69 offen offset:3072 sc1 lds\n\t"
      "v_cvt_pk_bf16_f32 v27, v70, v71\n\t"
      "ds_write_b128 v254, v[24:27]\n\t"
      "v_mfma_f32_16x16x16_bf16 v[88:91], v[156:157], v[16:17], v[88:91]\n\t"
      "v_pk_mul_f32 v[96:97], v[96:97], v[10:11] op_sel:[0,1] op_sel_hi:[1,1]\n\t"
      "v_pk_mul_f32 v[98:99], v[98:99], v[10:11] op_sel:[0,1] op_sel_hi:[1,1]\n\t"
      "v_mfma_f32_16x16x16_bf16 v[92:95], v[158:159], v[16:17], v[92:95]\n\t"
      "s_add_u32 s69, s50, 16384\n\t"
      "buffer_load_dwordx4 a[160:163], v248, s[36:39], s69 offen  sc1\n\t"
      "v_pk_mul_f32 v[100:101], v[100:101], v[10:11] op_sel:[0,1] op_sel_hi:[1,1]\n\t"
      "v_pk_mul_f32 v[102:103], v[102:103], v[10:11] op_sel:[0,1] op_sel_hi:[1,1]\n\t"
      "v_cvt_pk_bf16_f32 v28, v72, v73\n\t"
      "v_cvt_pk_bf16_f32 v29, v74, v75\n\t"
      "v_cvt_pk_bf16_f32 v30, v76, v77\n\t"
      "v_cvt_pk_bf16_f32 v31, v78, v79\n\t"
      "buffer_load_dwordx4 a[164:167], v248, s[36:39], s69 offen offset:1024  sc1\n\t"
      "ds_write_b128 v254, v[28:31] offset:2048\n\t"
      "v_mfma_f32_16x16x16_bf16 v[96:99], v[144:145], v[18:19], v[96:99]\n\t"
      "v_pk_mul_f32 v[104:105], v[104:105], v[10:11] op_sel:[0,1] op_sel_hi:[1,1]\n\t"
      "v_pk_mul_f32 v[106:107], v[106:107], v[10:11] op_sel:[0,1] op_sel_hi:[1,1]\n\t"
      "v_mfma_f32_16x16x16_bf16 v[100:103], v[146:147], v[18:19], v[100:103]\n\t"
      "buffer_load_dwordx4 a[168:171], v248, s[36:39], s69 offen offset:2048  sc1\n\t"
      "v_pk_mul_f32 v[108:109], v[108:109], v[10:11] op_sel:[0,1] op_sel_hi:[1,1]\n\t"
      "v_pk_mul_f32 v[110:111], v[110:111], v[10:11] op_sel:[0,1] op_sel_hi:[1,1]\n\t"
      "v_cvt_pk_bf16_f32 v24, v80, v81\n\t"
      "v_cvt_pk_bf16_f32 v25, v82, v83\n\t"
      "v_cvt_pk_bf16_f32 v26, v84, v85\n\t"
      "v_cvt_pk_bf16_f32 v27, v86, v87\n\t"
      "ds_write_b128 v254, v[24:27] offset:4096\n\t"
      "buffer_load_dwordx4 a[172:175], v248, s[36:39], s69 offen offset:3072  sc1\n\t"
      "v_mfma_f32_16x16x16_bf16 v[104:107], v[148:149], v[18:19], v[104:107]\n\t"
      "v_pk_mul_f32 v[112:113], v[112:113], v[10:11] op_sel:[0,1] op_sel_hi:[1,1]\n\t"
      "v_pk_mul_f32 v[114:115], v[114:115], v[10:11] op_sel:[0,1] op_sel_hi:[1,1]\n\t"
      "v_mfma_f32_16x16x16_bf16 v[108:111], v[150:151], v[18:19], v[108:111]\n\t"
      "v_pk_mul_f32 v[116:117], v[116:117], v[10:11] op_sel:[0,1] op_sel_hi:[1,1]\n\t"
      "s_add_u32 s69, s50, 20480\n\t"
      "buffer_load_dwordx4 a[176:179], v248, s[36:39], s69 offen  sc1\n\t"
      "v_pk_mul_f32 v[118:119], v[118:119], v[10:11] op_sel:[0,1] op_sel_hi:[1,1]\n\t"
      "v_cvt_pk_bf16_f32 v28, v88, v89\n\t"
      "v_cvt_pk_bf16_f32 v29, v90, v91\n\t"
      "v_cvt_pk_bf16_f32 v30, v92, v93\n\t"
      "v_cvt_pk_bf16_f32 v31, v94, v95\n\t"
      "ds_write_b128 v254, v[28:31] offset:6144\n\t"
      "v_mfma_f32_16x16x16_bf16 v[112:115], v[152:153], v[18:19], v[112:115]\n\t"
      "buffer_load_dwordx4 a[180:183], v248, s[36:39], s69 offen offset:1024  sc1\n\t"
      "v_pk_mul_f32 v[120:121], v[120:121], v[10:11] op_sel:[0,1] op_sel_hi:[1,1]\n\t"
      "v_pk_mul_f32 v[122:123], v[122:123], v[10:11] op_sel:[0,1] op_sel_hi:[1,1]\n\t"
      "v_mfma_f32_16x16x16_bf16 v[116:119], v[154:155], v[18:19], v[116:119]\n\t"
      "v_pk_mul_f32 v[124:125], v[124:125], v[10:11] op_sel:[0,1] op_sel_hi:[1,1]\n\t"
      "v_pk_mul_f32 v[126:127], v[126:127], v[10:11] op_sel:[0,1] op_sel_hi:[1,1]\n\t"
      "buffer_load_dwordx4 a[184:187], v248, s[36:39], s69 offen offset:2048  sc1\n\t"
      "v_cvt_pk_bf16_f32 v24, v96, v97\n\t"
      "v_cvt_pk_bf16_f32 v25, v98, v99\n\t"
      "v_cvt_pk_bf16_f32 v26, v100, v101\n\t"
      "v_cvt_pk_bf16_f32 v27, v102, v103\n\t"
      "ds_write_b128 v254, v[24:27] offset:1024\n\t"
      "v_mfma_f32_16x16x16_bf16 v[120:123], v[156:157], v[18:19], v[120:123]\n\t"
      "buffer_load_dwordx4 a[188:191], v248, s[36:39], s69 offen offset:3072  sc1\n\t"
      "v_mfma_f32_16x16x16_bf16 v[124:127], v[158:159], v[18:19], v[124:127]\n\t"
      "v_cvt_pk_bf16_f32 v28, v104, v105\n\t"
      "v_cvt_pk_bf16_f32 v29, v106, v107\n\t"
      "v_cvt_pk_bf16_f32 v30, v108, v109\n\t"
      "v_cvt_pk_bf16_f32 v31, v110, v111\n\t"
      "ds_write_b128 v254, v[28:31] offset:3072\n\t"
      "v_cvt_pk_bf16_f32 v24, v112, v113\n\t"
      "v_cvt_pk_bf16_f32 v25, v114, v115\n\t"
      "v_cvt_pk_bf16_f32 v26, v116, v117\n\t"
      "v_cvt_pk_bf16_f32 v27, v118, v119\n\t"
      "ds_write_b128 v254, v[24:27] offset:5120\n\t"
      "v_cvt_pk_bf16_f32 v28, v120, v121\n\t"
      "v_cvt_pk_bf16_f32 v29, v122, v123\n\t"
      "v_cvt_pk_bf16_f32 v30, v124, v125\n\t"
      "v_cvt_pk_bf16_f32 v31, v126, v127\n\t"
      "ds_write_b128 v254, v[28:31] offset:7168\n\t"
      "s_waitcnt vmcnt(40) lgkmcnt(0)\n\t"
      "s_barrier\n\t"
      "s_branch WO%=\n\t"
      "WB1%=:\n\t"
      "v_mfma_f32_16x16x32_bf16 v[0:3], v[128:131], a[160:163], 0\n\t"
      "v_mfma_f32_16x16x32_bf16 v[4:7], v[128:131], a[164:167], 0\n\t"
      "v_mfma_f32_16x16x32_bf16 v[0:3], v[132:135], a[168:171], v[0:3]\n\t"
      "v_mfma_f32_16x16x32_bf16 v[4:7], v[132:135], a[172:175], v[4:7]\n\t"
      "v_mfma_f32_16x16x32_bf16 v[0:3], v[136:139], a[176:179], v[0:3]\n\t"
      "v_mfma_f32_16x16x32_bf16 v[4:7], v[136:139], a[180:183], v[4:7]\n\t"
      "v_mfma_f32_16x16x32_bf16 v[0:3], v[140:143], a[184:187], v[0:3]\n\t"
      "v_mfma_f32_16x16x32_bf16 v[4:7], v[140:143], a[188:191], v[4:7]\n\t"
      "s_add_u32 s51, s51, 262144\n\t"
      "s_add_u32 s52, s52, 262144\n\t"
      "s_add_u32 s69, s50, 8192\n\t"
      "s_add_u32 m0, s61, 0\n\t"
      "s_nop 0\n\t"
      "buffer_load_dwordx4 v250, s[36:39], s69 offen sc1 lds\n\t"
      "s_nop 0\n\t"
      "v_cvt_pk_bf16_f32 v0, v0, v1\n\t"
      "v_cvt_pk_bf16_f32 v2, v2, v3\n\t"
      "v_cvt_pk_bf16_f32 v4, v4, v5\n\t"
      "v_cvt_pk_bf16_f32 v6, v6, v7\n\t"
      "v_and_b32 v1, s74, v0\n\t"
      "v_lshlrev_b32 v0, 16, v0\n\t"
      "v_and_b32 v3, s74, v2\n\t"
      "v_lshlrev_b32 v2, 16, v2\n\t"
      "v_and_b32 v5, s74, v4\n\t"
      "v_lshlrev_b32 v4, 16, v4\n\t"
      "v_and_b32 v7, s74, v6\n\t"
      "v_lshlrev_b32 v6, 16, v6\n\t"
      "buffer_load_dwordx4 v250, s[36:39], s69 offen offset:1024 sc1 lds\n\t"
      "v_pk_mul_f32 v[0:1], v[0:1], s[84:85]\n\t"
      "v_pk_mul_f32 v[2:3], v[2:3], s[84:85]\n\t"
      "v_pk_mul_f32 v[4:5], v[4:5], s[84:85]\n\t"
      "v_pk_mul_f32 v[6:7], v[6:7], s[84:85]\n\t"
      "v_cvt_pk_bf16_f32 v0, v0, v1\n\t"
      "v_cvt_pk_bf16_f32 v2, v2, v3\n\t"
      "v_cvt_pk_bf16_f32 v4, v4, v5\n\t"
      "v_cvt_pk_bf16_f32 v6, v6, v7\n\t"
      "v_and_b32 v1, s74, v0\n\t"
      "v_lshlrev_b32 v0, 16, v0\n\t"
      "v_and_b32 v3, s74, v2\n\t"
      "buffer_load_dwordx4 v250, s[36:39], s69 offen offset:2048 sc1 lds\n\t"
      "v_lshlrev_b32 v2, 16, v2\n\t"
      "v_and_b32 v5, s74, v4\n\t"
      "v_lshlrev_b32 v4, 16, v4\n\t"
      "v_and_b32 v7, s74, v6\n\t"
      "v_lshlrev_b32 v6, 16, v6\n\t"
      "v_max3_f32 v8, v0, v1, v2\n\t"
      "v_max3_f32 v8, v8, v3, v234\n\t"
      "v_max3_f32 v9, v4, v5, v6\n\t"
      "v_max3_f32 v9, v9, v7, v235\n\t"
      "s_nop 1\n\t"
      "v_permlane16_swap_b32 v8, v9\n\t"
      "v_max_f32 v8, v8, v9\n\t"
      "buffer_load_dwordx4 v250, s[36:39], s69 offen offset:3072 sc1 lds\n\t"
      "v_mov_b32 v9, v8\n\t"
      "s_nop 1\n\t"
      "v_permlane16_swap_b32 v8, v9\n\t"
      "v_pk_mul_f32 v[8:9], v[8:9], s[86:87]\n\t"
      "v_pk_fma_f32 v[10:11], v[234:235], s[86:87], v[8:9] neg_lo:[0,0,1] neg_hi:[0,0,1]\n\t"
      "v_pk_fma_f32 v[0:1], v[0:1], s[86:87], v[8:9] op_sel:[0,0,0] op_sel_hi:[1,1,0] neg_lo:[0,0,1] neg_hi:[0,0,1]\n\t"
      "v_pk_fma_f32 v[2:3], v[2:3], s[86:87], v[8:9] op_sel:[0,0,0] op_sel_hi:[1,1,0] neg_lo:[0,0,1] neg_hi:[0,0,1]\n\t"
      "v_pk_fma_f32 v[4:5], v[4:5], s[86:87], v[8:9] op_sel:[0,0,1] op_sel_hi:[1,1,1] neg_lo:[0,0,1] neg_hi:[0,0,1]\n\t"
      "v_pk_fma_f32 v[6:7], v[6:7], s[86:87], v[8:9] op_sel:[0,0,1] op_sel_hi:[1,1,1] neg_lo:[0,0,1] neg_hi:[0,0,1]\n\t"
      "v_exp_f32 v10, v10\n\t"
      "v_exp_f32 v11, v11\n\t"
      "s_add_u32 s69, s50, 12288\n\t"
      "s_add_u32 m0, s61, 4096\n\t"
      "s_nop 0\n\t"
      "buffer_load_dwordx4 v250, s[36:39], s69 offen sc1 lds\n\t"
      "v_exp_f32 v0, v0\n\t"
      "v_exp_f32 v1, v1\n\t"
      "v_exp_f32 v2, v2\n\t"
      "v_exp_f32 v3, v3\n\t"
      "v_exp_f32 v4, v4\n\t"
      "v_exp_f32 v5, v5\n\t"
      "buffer_load_dwordx4 v250, s[36:39], s69 offen offset:1024 sc1 lds\n\t"
      "v_exp_f32 v6, v6\n\t"
      "v_exp_f32 v7, v7\n\t"
      "v_pk_mul_f32 v[12:13], v[236:237], v[10:11]\n\t"
      "v_pk_add_f32 v[14:15], v[0:1], v[2:3]\n\t"
      "v_pk_add_f32 v[16:17], v[4:5], v[6:7]\n\t"
      "v_add_f32 v14, v14, v15\n\t"
      "v_add_f32 v15, v16, v17\n\t"
      "v_fma_f32 v12, v14, 0.5, v12\n\t"
      "v_fma_f32 v13, v15, 0.5, v13\n\t"
      "buffer_load_dwordx4 v250, s[36:39], s69 offen offset:2048 sc1 lds\n\t"
      "s_nop 0\n\t"
      "v_permlane16_swap_b32 v12, v13\n\t"
      "v_add_f32 v12, v12, v13\n\t"
      "v_mov_b32 v13, v12\n\t"
      "s_nop 1\n\t"
      "v_permlane32_swap_b32 v12, v13\n\t"
      "v_add_f32 v12, v12, v13\n\t"
      "v_mov_b32 v13, v12\n\t"
      "s_nop 1\n\t"
      "v_permlane16_swap_b32 v12, v13\n\t"
      "v_rcp_f32 v12, v12\n\t"
      "v_rcp_f32 v13, v13\n\t"
      "s_nop 0\n\t"
      "v_pk_mul_f32 v[10:11], v[10:11], v[12:13]\n\t"
      "buffer_load_dwordx4 v250, s[36:39], s69 offen offset:3072 sc1 lds\n\t"
      "v_pk_mul_f32 v[12:13], v[12:13], 0.5 op_sel_hi:[1,0]\n\t"
      "v_pk_mul_f32 v[0:1], v[0:1], v[12:13] op_sel:[0,0] op_sel_hi:[1,0]\n\t"
      "v_pk_mul_f32 v[2:3], v[2:3], v[12:13] op_sel:[0,0] op_sel_hi:[1,0]\n\t"
      "v_pk_mul_f32 v[4:5], v[4:5], v[12:13] op_sel:[0,1] op_sel_hi:[1,1]\n\t"
      "v_pk_mul_f32 v[6:7], v[6:7], v[12:13] op_sel:[0,1] op_sel_hi:[1,1]\n\t"
      "v_cvt_pk_bf16_f32 v16, v0, v1\n\t"
      "v_cvt_pk_bf16_f32 v17, v2, v3\n\t"
      "v_cvt_pk_bf16_f32 v18, v4, v5\n\t"
      "v_cvt_pk_bf16_f32 v19, v6, v7\n\t"
      "v_pk_mul_f32 v[64:65], v[64:65], v[10:11] op_sel:[0,0] op_sel_hi:[1,0]\n\t"
      "v_pk_mul_f32 v[66:67], v[66:67], v[10:11] op_sel:[0,0] op_sel_hi:[1,0]\n\t"
      "s_add_u32 s69, s50, 16384\n\t"
      "buffer_load_dwordx4 a[160:163], v248, s[36:39], s69 offen  sc1\n\t"
      "v_pk_mul_f32 v[68:69], v[68:69], v[10:11] op_sel:[0,0] op_sel_hi:[1,0]\n\t"
      "v_pk_mul_f32 v[70:71], v[70:71], v[10:11] op_sel:[0,0] op_sel_hi:[1,0]\n\t"
      "v_mfma_f32_16x16x16_bf16 v[64:67], v[144:145], v[16:17], v[64:67]\n\t"
      "v_pk_mul_f32 v[72:73], v[72:73], v[10:11] op_sel:[0,0] op_sel_hi:[1,0]\n\t"
      "v_pk_mul_f32 v[74:75], v[74:75], v[10:11] op_sel:[0,0] op_sel_hi:[1,0]\n\t"
      "v_mfma_f32_16x16x16_bf16 v[68:71], v[146:147], v[16:17], v[68:71]\n\t"
      "v_pk_mul_f32 v[76:77], v[76:77], v[10:11] op_sel:[0,0] op_sel_hi:[1,0]\n\t"
      "v_pk_mul_f32 v[78:79], v[78:79], v[10:11] op_sel:[0,0] op_sel_hi:[1,0]\n\t"
      "v_mfma_f32_16x16x16_bf16 v[72:75], v[148:149], v[16:17], v[72:75]\n\t"
      "buffer_load_dwordx4 a[164:167], v248, s[36:39], s69 offen offset:1024  sc1\n\t"
      "v_pk_mul_f32 v[80:81], v[80:81], v[10:11] op_sel:[0,0] op_sel_hi:[1,0]\n\t"
      "v_pk_mul_f32 v[82:83], v[82:83], v[10:11] op_sel:[0,0] op_sel_hi:[1,0]\n\t"
      "v_mfma_f32_16x16x16_bf16 v[76:79], v[150:151], v[16:17], v[76:79]\n\t"
      "v_pk_mul_f32 v[84:85], v[84:85], v[10:11] op_sel:[0,0] op_sel_hi:[1,0]\n\t"
      "v_pk_mul_f32 v[86:87], v[86:87], v[10:11] op_sel:[0,0] op_sel_hi:[1,0]\n\t"
      "v_mfma_f32_16x16x16_bf16 v[80:83], v[152:153], v[16:17], v[80:83]\n\t"
      "v_pk_mul_f32 v[88:89], v[88:89], v[10:11] op_sel:[0,0] op_sel_hi:[1,0]\n\t"
      "v_pk_mul_f32 v[90:91], v[90:91], v[10:11] op_sel:[0,0] op_sel_hi:[1,0]\n\t"
      "v_mfma_f32_16x16x16_bf16 v[84:87], v[154:155], v[16:17], v[84:87]\n\t"
      "buffer_load_dwordx4 a[168:171], v248, s[36:39], s69 offen offset:2048  sc1\n\t"
      "v_pk_mul_f32 v[92:93], v[92:93], v[10:11] op_sel:[0,0] op_sel_hi:[1,0]\n\t"
      "v_pk_mul_f32 v[94:95], v[94:95], v[10:11] op_sel:[0,0] op_sel_hi:[1,0]\n\t"
      "v_cvt_pk_bf16_f32 v24, v64, v65\n\t"
      "v_cvt_pk_bf16_f32 v25, v66, v67\n\t"
      "v_cvt_pk_bf16_f32 v26, v68, v69\n\t"
      "v_cvt_pk_bf16_f32 v27, v70, v71\n\t"
      "ds_write_b128 v254, v[24:27]\n\t"
      "v_mfma_f32_16x16x16_bf16 v[88:91], v[156:157], v[16:17], v[88:91]\n\t"
      "v_pk_mul_f32 v[96:97], v[96:97], v[10:11] op_sel:[0,1] op_sel_hi:[1,1]\n\t"
      "v_pk_mul_f32 v[98:99], v[98:99], v[10:11] op_sel:[0,1] op_sel_hi:[1,1]\n\t"
      "buffer_load_dwordx4 a[172:175], v248, s[36:39], s69 offen offset:3072  sc1\n\t"
      "v_mfma_f32_16x16x16_bf16 v[92:95], v[158:159], v[16:17], v[92:95]\n\t"
      "v_pk_mul_f32 v[100:101], v[100:101], v[10:11] op_sel:[0,1] op_sel_hi:[1,1]\n\t"
      "v_pk_mul_f32 v[102:103], v[102:103], v[10:11] op_sel:[0,1] op_sel_hi:[1,1]\n\t"
      "v_cvt_pk_bf16_f32 v28, v72, v73\n\t"
      "v_cvt_pk_bf16_f32 v29, v74, v75\n\t"
      "v_cvt_pk_bf16_f32 v30, v76, v77\n\t"
      "v_cvt_pk_bf16_f32 v31, v78, v79\n\t"
      "ds_write_b128 v254, v[28:31] offset:2048\n\t"
      "v_mfma_f32_16x16x16_bf16 v[96:99], v[144:145], v[18:19], v[96:99]\n\t"
      "s_add_u32 s69, s50, 20480\n\t"
      "buffer_load_dwordx4 a[176:179], v248, s[36:39], s69 offen  sc1\n\t"
      "v_pk_mul_f32 v[104:105], v[104:105], v[10:11] op_sel:[0,1] op_sel_hi:[1,1]\n\t"
      "v_pk_mul_f32 v[106:107], v[106:107], v[10:11] op_sel:[0,1] op_sel_hi:[1,1]\n\t"
      "v_mfma_f32_16x16x16_bf16 v[100:103], v[146:147], v[18:19], v[100:103]\n\t"
      "v_pk_mul_f32 v[108:109], v[108:109], v[10:11] op_sel:[0,1] op_sel_hi:[1,1]\n\t"
      "v_pk_mul_f32 v[110:111], v[110:111], v[10:11] op_sel:[0,1] op_sel_hi:[1,1]\n\t"
      "v_cvt_pk_bf16_f32 v24, v80, v81\n\t"
      "v_cvt_pk_bf16_f32 v25, v82, v83\n\t"
      "v_cvt_pk_bf16_f32 v26, v84, v85\n\t"
      "v_cvt_pk_bf16_f32 v27, v86, v87\n\t"
      "ds_write_b128 v254, v[24:27] offset:4096\n\t"
      "v_mfma_f32_16x16x16_bf16 v[104:107], v[148:149], v[18:19], v[104:107]\n\t"
      "buffer_load_dwordx4 a[180:183], v248, s[36:39], s69 offen offset:1024  sc1\n\t"
      "v_pk_mul_f32 v[112:113], v[112:113], v[10:11] op_sel:[0,1] op_sel_hi:[1,1]\n\t"
      "v_pk_mul_f32 v[114:115], v[114:115], v[10:11] op_sel:[0,1] op_sel_hi:[1,1]\n\t"
      "v_mfma_f32_16x16x16_bf16 v[108:111], v[150:151], v[18:19], v[108:111]\n\t"
      "v_pk_mul_f32 v[116:117], v[116:117], v[10:11] op_sel:[0,1] op_sel_hi:[1,1]\n\t"
      "v_pk_mul_f32 v[118:119], v[118:119], v[10:11] op_sel:[0,1] op_sel_hi:[1,1]\n\t"
      "v_cvt_pk_bf16_f32 v28, v88, v89\n\t"
      "v_cvt_pk_bf16_f32 v29, v90, v91\n\t"
      "v_cvt_pk_bf16_f32 v30, v92, v93\n\t"
      "v_cvt_pk_bf16_f32 v31, v94, v95\n\t"
      "buffer_load_dwordx4 a[184:187], v248, s[36:39], s69 offen offset:2048  sc1\n\t"
      "ds_write_b128 v254, v[28:31] offset:6144\n\t"
      "v_mfma_f32_16x16x16_bf16 v[112:115], v[152:153], v[18:19], v[112:115]\n\t"
      "v_pk_mul_f32 v[120:121], v[120:121], v[10:11] op_sel:[0,1] op_sel_hi:[1,1]\n\t"
      "v_pk_mul_f32 v[122:123], v[122:123], v[10:11] op_sel:[0,1] op_sel_hi:[1,1]\n\t"
      "v_mfma_f32_16x16x16_bf16 v[116:119], v[154:155], v[18:19], v[116:119]\n\t"
      "v_pk_mul_f32 v[124:125], v[124:125], v[10:11] op_sel:[0,1] op_sel_hi:[1,1]\n\t"
      "v_pk_mul_f32 v[126:127], v[126:127], v[10:11] op_sel:[0,1] op_sel_hi:[1,1]\n\t"
      "v_cvt_pk_bf16_f32 v24, v96, v97\n\t"
      "v_cvt_pk_bf16_f32 v25, v98, v99\n\t"
      "v_cvt_pk_bf16_f32 v26, v100, v101\n\t"
      "buffer_load_dwordx4 a[188:191], v248, s[36:39], s69 offen offset:3072  sc1\n\t"
      "v_cvt_pk_bf16_f32 v27, v102, v103\n\t"
      "ds_write_b128 v254, v[24:27] offset:1024\n\t"
      "v_mfma_f32_16x16x16_bf16 v[120:123], v[156:157], v[18:19], v[120:123]\n\t"
      "v_mfma_f32_16x16x16_bf16 v[124:127], v[158:159], v[18:19], v[124:127]\n\t"
      "v_cvt_pk_bf16_f32 v28, v104, v105\n\t"
      "v_cvt_pk_bf16_f32 v29, v106, v107\n\t"
      "v_cvt_pk_bf16_f32 v30, v108, v109\n\t"
      "v_cvt_pk_bf16_f32 v31, v110, v111\n\t"
      "ds_write_b128 v254, v[28:31] offset:3072\n\t"
      "v_cvt_pk_bf16_f32 v24, v112, v113\n\t"
      "v_cvt_pk_bf16_f32 v25, v114, v115\n\t"
      "v_cvt_pk_bf16_f32 v26, v116, v117\n\t"
      "v_cvt_pk_bf16_f32 v27, v118, v119\n\t"
      "ds_write_b128 v254, v[24:27] offset:5120\n\t"
      "v_cvt_pk_bf16_f32 v28, v120, v121\n\t"
      "v_cvt_pk_bf16_f32 v29, v122, v123\n\t"
      "v_cvt_pk_bf16_f32 v30, v124, v125\n\t"
      "v_cvt_pk_bf16_f32 v31, v126, v127\n\t"
      "ds_write_b128 v254, v[28:31] offset:7168\n\t"
      "s_waitcnt vmcnt(40) lgkmcnt(0)\n\t"
      "s_barrier\n\t"
      "s_branch WO%=\n\t"
      "WOL1%=:\n\t"
      "buffer_load_dwordx4 v[128:131], v252, s[36:39], s51 offen sc1\n\t"
      "s_waitcnt lgkmcnt(11)\n\t"
      "v_mfma_f32_16x16x32_bf16 v[0:3], a[0:3], v[64:67], 0\n\t"
      "v_mfma_f32_16x16x32_bf16 v[4:7], a[64:67], v[64:67], 0\n\t"
      "ds_read_b128 v[112:115], v255 offset:12288\n\t"
      "s_waitcnt lgkmcnt(11)\n\t"
      "v_mfma_f32_16x16x32_bf16 v[8:11], a[0:3], v[68:71], 0\n\t"
      "v_mfma_f32_16x16x32_bf16 v[12:15], a[64:67], v[68:71], 0\n\t"
      "ds_read_b128 v[116:119], v255 offset:13312\n\t"
      "s_waitcnt lgkmcnt(11)\n\t"
      "v_mfma_f32_16x16x32_bf16 v[0:3], a[4:7], v[72:75], v[0:3]\n\t"
      "v_mfma_f32_16x16x32_bf16 v[4:7], a[68:71], v[72:75], v[4:7]\n\t"
      "ds_read_b128 v[120:123], v255 offset:14336\n\t"
      "s_waitcnt lgkmcnt(11)\n\t"
      "v_mfma_f32_16x16x32_bf16 v[8:11], a[4:7], v[76:79], v[8:11]\n\t"
      "v_mfma_f32_16x16x32_bf16 v[12:15], a[68:71], v[76:79], v[12:15]\n\t"
      "buffer_load_dwordx4 v[132:135], v252, s[36:39], s51 offen offset:512 sc1\n\t"
      "ds_read_b128 v[124:127], v255 offset:15360\n\t"
      "s_waitcnt lgkmcnt(11)\n\t"
      "v_mfma_f32_16x16x32_bf16 v[0:3], a[8:11], v[80:83], v[0:3]\n\t"
      "v_mfma_f32_16x16x32_bf16 v[4:7], a[72:75], v[80:83], v[4:7]\n\t"
      "ds_read_b128 v[64:67], v255 offset:16384\n\t"
      "s_waitcnt lgkmcnt(11)\n\t"
      "v_mfma_f32_16x16x32_bf16 v[8:11], a[8:11], v[84:87], v[8:11]\n\t"
      "v_mfma_f32_16x16x32_bf16 v[12:15], a[72:75], v[84:87], v[12:15]\n\t"
      "ds_read_b128 v[68:71], v255 offset:17408\n\t"
      "s_waitcnt lgkmcnt(11)\n\t"
      "v_mfma_f32_16x16x32_bf16 v[0:3], a[12:15], v[88:91], v[0:3]\n\t"
      "v_mfma_f32_16x16x32_bf16 v[4:7], a[76:79], v[88:91], v[4:7]\n\t"
      "ds_read_b128 v[72:75], v255 offset:18432\n\t"
      "s_waitcnt lgkmcnt(11)\n\t"
      "v_mfma_f32_16x16x32_bf16 v[8:11], a[12:15], v[92:95], v[8:11]\n\t"
      "v_mfma_f32_16x16x32_bf16 v[12:15], a[76:79], v[92:95], v[12:15]\n\t"
      "buffer_load_dwordx4 v[136:139], v252, s[36:39], s51 offen offset:1024 sc1\n\t"
      "ds_read_b128 v[76:79], v255 offset:19456\n\t"
      "s_waitcnt lgkmcnt(11)\n\t"
      "v_mfma_f32_16x16x32_bf16 v[0:3], a[16:19], v[96:99], v[0:3]\n\t"
      "v_mfma_f32_16x16x32_bf16 v[4:7], a[80:83], v[96:99], v[4:7]\n\t"
      "ds_read_b128 v[80:83], v255 offset:20480\n\t"
      "s_waitcnt lgkmcnt(11)\n\t"
      "v_mfma_f32_16x16x32_bf16 v[8:11], a[16:19], v[100:103], v[8:11]\n\t"
      "v_mfma_f32_16x16x32_bf16 v[12:15], a[80:83], v[100:103], v[12:15]\n\t"
      "ds_read_b128 v[84:87], v255 offset:21504\n\t"
      "s_waitcnt lgkmcnt(11)\n\t"
      "v_mfma_f32_16x16x32_bf16 v[0:3], a[20:23], v[104:107], v[0:3]\n\t"
      "v_mfma_f32_16x16x32_bf16 v[4:7], a[84:87], v[104:107], v[4:7]\n\t"
      "ds_read_b128 v[88:91], v255 offset:22528\n\t"
      "s_waitcnt lgkmcnt(11)\n\t"
      "v_mfma_f32_16x16x32_bf16 v[8:11], a[20:23], v[108:111], v[8:11]\n\t"
      "v_mfma_f32_16x16x32_bf16 v[12:15], a[84:87], v[108:111], v[12:15]\n\t"
      "buffer_load_dwordx4 v[140:143], v252, s[36:39], s51 offen offset:1536 sc1\n\t"
      "ds_read_b128 v[92:95], v255 offset:23552\n\t"
      "s_waitcnt lgkmcnt(11)\n\t"
      "v_mfma_f32_16x16x32_bf16 v[0:3], a[24:27], v[112:115], v[0:3]\n\t"
      "v_mfma_f32_16x16x32_bf16 v[4:7], a[88:91], v[112:115], v[4:7]\n\t"
      "ds_read_b128 v[96:99], v255 offset:24576\n\t"
      "s_waitcnt lgkmcnt(11)\n\t"
      "v_mfma_f32_16x16x32_bf16 v[8:11], a[24:27], v[116:119], v[8:11]\n\t"
      "v_mfma_f32_16x16x32_bf16 v[12:15], a[88:91], v[116:119], v[12:15]\n\t"
      "ds_read_b128 v[100:103], v255 offset:25600\n\t"
      "s_waitcnt lgkmcnt(11)\n\t"
      "v_mfma_f32_16x16x32_bf16 v[0:3], a[28:31], v[120:123], v[0:3]\n\t"
      "v_mfma_f32_16x16x32_bf16 v[4:7], a[92:95], v[120:123], v[4:7]\n\t"
      "ds_read_b128 v[104:107], v255 offset:26624\n\t"
      "s_waitcnt lgkmcnt(11)\n\t"
      "v_mfma_f32_16x16x32_bf16 v[8:11], a[28:31], v[124:127], v[8:11]\n\t"
      "buffer_load_dwordx4 v[144:147], v253, s[36:39], s52 offen sc1\n\t"
      "v_mfma_f32_16x16x32_bf16 v[12:15], a[92:95], v[124:127], v[12:15]\n\t"
      "ds_read_b128 v[108:111], v255 offset:27648\n\t"
      "s_waitcnt lgkmcnt(11)\n\t"
      "v_mfma_f32_16x16x32_bf16 v[0:3], a[32:35], v[64:67], v[0:3]\n\t"
      "v_mfma_f32_16x16x32_bf16 v[4:7], a[96:99], v[64:67], v[4:7]\n\t"
      "ds_read_b128 v[112:115], v255 offset:28672\n\t"
      "s_waitcnt lgkmcnt(11)\n\t"
      "v_mfma_f32_16x16x32_bf16 v[8:11], a[32:35], v[68:71], v[8:11]\n\t"
      "v_mfma_f32_16x16x32_bf16 v[12:15], a[96:99], v[68:71], v[12:15]\n\t"
      "ds_read_b128 v[116:119], v255 offset:29696\n\t"
      "s_waitcnt lgkmcnt(11)\n\t"
      "v_mfma_f32_16x16x32_bf16 v[0:3], a[36:39], v[72:75], v[0:3]\n\t"
      "v_mfma_f32_16x16x32_bf16 v[4:7], a[100:103], v[72:75], v[4:7]\n\t"
      "ds_read_b128 v[120:123], v255 offset:30720\n\t"
      "s_waitcnt lgkmcnt(11)\n\t"
      "v_mfma_f32_16x16x32_bf16 v[8:11], a[36:39], v[76:79], v[8:11]\n\t"
      "buffer_load_dwordx4 v[148:151], v253, s[36:39], s52 offen offset:512 sc1\n\t"
      "v_mfma_f32_16x16x32_bf16 v[12:15], a[100:103], v[76:79], v[12:15]\n\t"
      "ds_read_b128 v[124:127], v255 offset:31744\n\t"
      "s_waitcnt lgkmcnt(11)\n\t"
      "v_mfma_f32_16x16x32_bf16 v[0:3], a[40:43], v[80:83], v[0:3]\n\t"
      "v_mfma_f32_16x16x32_bf16 v[4:7], a[104:107], v[80:83], v[4:7]\n\t"
      "s_waitcnt lgkmcnt(10)\n\t"
      "v_mfma_f32_16x16x32_bf16 v[8:11], a[40:43], v[84:87], v[8:11]\n\t"
      "v_mfma_f32_16x16x32_bf16 v[12:15], a[104:107], v[84:87], v[12:15]\n\t"
      "s_waitcnt lgkmcnt(9)\n\t"
      "v_mfma_f32_16x16x32_bf16 v[0:3], a[44:47], v[88:91], v[0:3]\n\t"
      "v_mfma_f32_16x16x32_bf16 v[4:7], a[108:111], v[88:91], v[4:7]\n\t"
      "s_waitcnt lgkmcnt(8)\n\t"
      "v_mfma_f32_16x16x32_bf16 v[8:11], a[44:47], v[92:95], v[8:11]\n\t"
      "buffer_load_dwordx4 v[152:155], v253, s[36:39], s52 offen offset:1024 sc1\n\t"
      "v_mfma_f32_16x16x32_bf16 v[12:15], a[108:111], v[92:95], v[12:15]\n\t"
      "s_waitcnt lgkmcnt(7)\n\t"
      "v_mfma_f32_16x16x32_bf16 v[0:3], a[48:51], v[96:99], v[0:3]\n\t"
      "v_mfma_f32_16x16x32_bf16 v[4:7], a[112:115], v[96:99], v[4:7]\n\t"
      "s_waitcnt lgkmcnt(6)\n\t"
      "v_mfma_f32_16x16x32_bf16 v[8:11], a[48:51], v[100:103], v[8:11]\n\t"
      "v_mfma_f32_16x16x32_bf16 v[12:15], a[112:115], v[100:103], v[12:15]\n\t"
      "s_waitcnt lgkmcnt(5)\n\t"
      "v_mfma_f32_16x16x32_bf16 v[0:3], a[52:55], v[104:107], v[0:3]\n\t"
      "v_mfma_f32_16x16x32_bf16 v[4:7], a[116:119], v[104:107], v[4:7]\n\t"
      "s_waitcnt lgkmcnt(4)\n\t"
      "v_mfma_f32_16x16x32_bf16 v[8:11], a[52:55], v[108:111], v[8:11]\n\t"
      "v_mfma_f32_16x16x32_bf16 v[12:15], a[116:119], v[108:111], v[12:15]\n\t"
      "buffer_load_dwordx4 v[156:159], v253, s[36:39], s52 offen offset:1536 sc1\n\t"
      "s_waitcnt lgkmcnt(3)\n\t"
      "v_mfma_f32_16x16x32_bf16 v[0:3], a[56:59], v[112:115], v[0:3]\n\t"
      "v_mfma_f32_16x16x32_bf16 v[4:7], a[120:123], v[112:115], v[4:7]\n\t"
      "s_waitcnt lgkmcnt(2)\n\t"
      "v_mfma_f32_16x16x32_bf16 v[8:11], a[56:59], v[116:119], v[8:11]\n\t"
      "v_mfma_f32_16x16x32_bf16 v[12:15], a[120:123], v[116:119], v[12:15]\n\t"
      "s_waitcnt lgkmcnt(1)\n\t"
      "v_mfma_f32_16x16x32_bf16 v[0:3], a[60:63], v[120:123], v[0:3]\n\t"
      "v_mfma_f32_16x16x32_bf16 v[4:7], a[124:127], v[120:123], v[4:7]\n\t"
      "s_waitcnt lgkmcnt(0)\n\t"
      "v_mfma_f32_16x16x32_bf16 v[8:11], a[60:63], v[124:127], v[8:11]\n\t"
      "v_mfma_f32_16x16x32_bf16 v[12:15], a[124:127], v[124:127], v[12:15]\n\t"
      "v_xor_b32 v254, 0x8000, v254\n\t"
      "v_xor_b32 v255, 0x8000, v255\n\t"
      "s_add_u32 s69, s53, 0x4000\n\t"
      "s_add_u32 s48, s48, 4\n\t"
      "s_waitcnt vmcnt(40)\n\t"
      "v_cvt_pk_bf16_f32 v0, v0, v1\n\t"
      "v_cvt_pk_bf16_f32 v1, v2, v3\n\t"
      "v_cvt_pk_bf16_f32 v2, v4, v5\n\t"
      "v_cvt_pk_bf16_f32 v3, v6, v7\n\t"
      "v_min_u32 v0, -2, v0\n\t"
      "v_min_u32 v1, -2, v1\n\t"
      "v_min_u32 v2, -2, v2\n\t"
      "v_min_u32 v3, -2, v3\n\t"
      "buffer_store_dwordx4 v[0:3], v238, s[36:39], s53 offen sc1\n\t"
      "v_cvt_pk_bf16_f32 v8, v8, v9\n\t"
      "v_cvt_pk_bf16_f32 v9, v10, v11\n\t"
      "v_cvt_pk_bf16_f32 v10, v12, v13\n\t"
      "v_cvt_pk_bf16_f32 v11, v14, v15\n\t"
      "v_min_u32 v8, -2, v8\n\t"
      "v_min_u32 v9, -2, v9\n\t"
      "v_min_u32 v10, -2, v10\n\t"
      "v_min_u32 v11, -2, v11\n\t"
      "buffer_store_dwordx4 v[8:11], v238, s[36:39], s69 offen sc1\n\t"
      "s_add_u32 s53, s53, 1048576\n\t"
      "s_nop 1\n\t"
      "s_branch WTOP%=\n\t"
      "WR2%=:\n\t"
      "v_cvt_pk_bf16_f32 v204, v204, v205\n\t"
      "v_and_b32 v205, s74, v204\n\t"
      "v_lshlrev_b32 v204, 16, v204\n\t"
      "v_add_f32 v205, 0x3727c5ac, v205\n\t"
      "v_pk_add_f32 v[40:41], v[192:193], v[204:205] op_sel_hi:[1,0] neg_lo:[0,1] neg_hi:[0,1]\n\t"
      "v_pk_add_f32 v[42:43], v[194:195], v[204:205] op_sel_hi:[1,0] neg_lo:[0,1] neg_hi:[0,1]\n\t"
      "v_pk_add_f32 v[44:45], v[196:197], v[204:205] op_sel_hi:[1,0] neg_lo:[0,1] neg_hi:[0,1]\n\t"
      "v_pk_add_f32 v[46:47], v[198:199], v[204:205] op_sel_hi:[1,0] neg_lo:[0,1] neg_hi:[0,1]\n\t"
      "v_cvt_pk_bf16_f32 v205, v205, v205\n\t"
      "v_and_b32 v205, s74, v205\n\t"
      "v_sqrt_f32 v205, v205\n\t"
      "v_cvt_pk_bf16_f32 v40, v40, v41\n\t"
      "v_cvt_pk_bf16_f32 v42, v42, v43\n\t"
      "v_cvt_pk_bf16_f32 v44, v44, v45\n\t"
      "v_cvt_pk_bf16_f32 v46, v46, v47\n\t"
      "v_cvt_pk_bf16_f32 v205, v205, v205\n\t"
      "v_and_b32 v205, s74, v205\n\t"
      "v_rcp_f32 v204, v205\n\t"
      "v_and_b32 v41, s74, v40\n\t"
      "v_lshlrev_b32 v40, 16, v40\n\t"
      "v_and_b32 v43, s74, v42\n\t"
      "v_lshlrev_b32 v42, 16, v42\n\t"
      "v_and_b32 v45, s74, v44\n\t"
      "v_lshlrev_b32 v44, 16, v44\n\t"
      "v_and_b32 v47, s74, v46\n\t"
      "v_lshlrev_b32 v46, 16, v46\n\t"
      "v_pk_mul_f32 v[40:41], v[40:41], v[204:205] op_sel_hi:[1,0]\n\t"
      "v_pk_mul_f32 v[42:43], v[42:43], v[204:205] op_sel_hi:[1,0]\n\t"
      "v_pk_mul_f32 v[44:45], v[44:45], v[204:205] op_sel_hi:[1,0]\n\t"
      "v_pk_mul_f32 v[46:47], v[46:47], v[204:205] op_sel_hi:[1,0]\n\t"
      "v_cvt_pk_bf16_f32 v52, v40, v41\n\t"
      "v_cvt_pk_bf16_f32 v53, v42, v43\n\t"
      "v_cvt_pk_bf16_f32 v54, v44, v45\n\t"
      "v_cvt_pk_bf16_f32 v55, v46, v47\n\t"
      "v_and_b32 v33, s74, v52\n\t"
      "v_lshlrev_b32 v32, 16, v52\n\t"
      "v_and_b32 v35, s74, v53\n\t"
      "v_lshlrev_b32 v34, 16, v53\n\t"
      "v_and_b32 v37, s74, v54\n\t"
      "v_lshlrev_b32 v36, 16, v54\n\t"
      "v_and_b32 v39, s74, v55\n\t"
      "v_lshlrev_b32 v38, 16, v55\n\t"
      "v_pk_mul_f32 v[40:41], v[32:33], v[240:241]\n\t"
      "v_pk_fma_f32 v[40:41], v[34:35], v[242:243], v[40:41]\n\t"
      "v_pk_fma_f32 v[40:41], v[36:37], v[244:245], v[40:41]\n\t"
      "v_pk_fma_f32 v[40:41], v[38:39], v[246:247], v[40:41]\n\t"
      "v_add_f32 v40, v40, v41\n\t"
      "s_lshl_b32 s73, s49, 5\n\t"
      "s_add_u32 s73, s73, s60\n\t"
      "s_lshl_b32 s69, s73, 2\n\t"
      "s_add_u32 s69, s69, s57\n\t"
      "s_lshl_b32 s70, s73, 7\n\t"
      "s_add_u32 s70, s70, s57\n\t"
      "s_sub_u32 s70, s70, 212992\n\t"
      "s_cmp_ge_u32 s49, 56\n\t"
      "s_cselect_b32 s70, s70, s69\n\t"
      "v_add_f32_dpp v40, v40, v40 quad_perm:[1,0,3,2] row_mask:0xf bank_mask:0xf\n\t"
      "s_nop 1\n\t"
      "v_add_f32_dpp v40, v40, v40 quad_perm:[2,3,0,1] row_mask:0xf bank_mask:0xf\n\t"
      "s_nop 1\n\t"
      "v_add_f32_dpp v40, v40, v40 row_ror:4 row_mask:0xf bank_mask:0xf\n\t"
      "s_nop 1\n\t"
      "v_add_f32_dpp v40, v40, v40 row_ror:8 row_mask:0xf bank_mask:0xf\n\t"
      "v_mov_b32 v41, v40\n\t"
      "s_nop 1\n\t"
      "v_permlane16_swap_b32 v40, v41\n\t"
      "v_add_f32 v40, v40, v41\n\t"
      "v_mov_b32 v41, v40\n\t"
      "s_nop 1\n\t"
      "v_permlane32_swap_b32 v40, v41\n\t"
      "v_add_f32 v40, v40, v41\n\t"
      "v_cvt_pk_bf16_f32 v40, v40, v40\n\t"
      "v_and_b32 v40, s74, v40\n\t"
      "s_mov_b64 exec, 1\n\t"
      "buffer_store_dword v40, off, s[36:39], s70 sc1\n\t"
      "s_mov_b64 exec, -1\n\t"
      "v_pk_add_f32 v[192:193], v[192:193], v[32:33]\n\t"
      "v_pk_add_f32 v[194:195], v[194:195], v[34:35]\n\t"
      "v_pk_add_f32 v[196:197], v[196:197], v[36:37]\n\t"
      "v_pk_add_f32 v[198:199], v[198:199], v[38:39]\n\t"
      "v_readfirstlane_b32 s68, v40\n\t"
      "v_cvt_pk_bf16_f32 v56, v192, v193\n\t"
      "v_cvt_pk_bf16_f32 v57, v194, v195\n\t"
      "v_cvt_pk_bf16_f32 v58, v196, v197\n\t"
      "v_cvt_pk_bf16_f32 v59, v198, v199\n\t"
      "s_add_u32 s67, s69, 8192\n\t"
      "s_lshl_b32 s73, s73, 10\n\t"
      "s_add_u32 s69, s73, s58\n\t"
      "s_add_u32 s70, s73, s59\n\t"
      "buffer_store_dwordx2 v[56:57], v249, s[44:47], s73 offen sc1\n\t"
      "buffer_store_dwordx2 v[52:53], v249, s[36:39], s69 offen sc1\n\t"
      "buffer_store_dwordx2 v[200:201], v249, s[36:39], s70 offen sc1\n\t"
      "buffer_store_dwordx2 v[58:59], v249, s[44:47], s73 offen offset:512 sc1\n\t"
      "buffer_store_dwordx2 v[54:55], v249, s[36:39], s69 offen offset:512 sc1\n\t"
      "buffer_store_dwordx2 v[202:203], v249, s[36:39], s70 offen offset:512 sc1\n\t"
      "s_mov_b32 s49, s89\n\t"
      "s_add_u32 s89, s89, 16\n\t"
      "s_lshr_b32 s69, s89, 4\n\t"
      "s_cmp_eq_u32 s69, 3\n\t"
      "s_cselect_b32 s89, s93, s89\n\t"
      "s_cmp_ge_u32 s49, 48\n\t"
      "s_cselect_b32 s60, s94, s60\n\t"
      "s_cselect_b32 s54, s95, s54\n\t"
      "s_mov_b32 s65, 0\n\t"
      "s_branch WGO2%=\n\t"
      "WOM1%=:\n\t"
      "s_mov_b32 s65, 2\n\t"
      "s_cmp_lg_u32 s96, 0\n\t"
      "s_cbranch_scc1 WOL3%=\n\t"
      "WOL2%=:\n\t"
      "s_waitcnt lgkmcnt(11)\n\t"
      "v_mfma_f32_16x16x32_bf16 v[0:3], a[0:3], v[64:67], 0\n\t"
      "v_mfma_f32_16x16x32_bf16 v[16:19], a[224:227], v[160:163], 0\n\t"
      "v_mfma_f32_16x16x32_bf16 v[4:7], a[64:67], v[64:67], 0\n\t"
      "v_mfma_f32_16x16x32_bf16 v[20:23], a[228:231], v[160:163], 0\n\t"
      "v_and_b32 v193, s74, v224\n\t"
      "v_lshlrev_b32 v192, 16, v224\n\t"
      "ds_read_b128 v[112:115], v255 offset:12288\n\t"
      "s_waitcnt lgkmcnt(11)\n\t"
      "v_mfma_f32_16x16x32_bf16 v[8:11], a[0:3], v[68:71], 0\n\t"
      "v_mfma_f32_16x16x32_bf16 v[16:19], a[224:227], v[164:167], v[16:19]\n\t"
      "v_mfma_f32_16x16x32_bf16 v[12:15], a[64:67], v[68:71], 0\n\t"
      "v_mfma_f32_16x16x32_bf16 v[20:23], a[228:231], v[164:167], v[20:23]\n\t"
      "v_and_b32 v195, s74, v225\n\t"
      "v_lshlrev_b32 v194, 16, v225\n\t"
      "ds_read_b128 v[116:119], v255 offset:13312\n\t"
      "s_waitcnt lgkmcnt(11)\n\t"
      "v_mfma_f32_16x16x32_bf16 v[0:3], a[4:7], v[72:75], v[0:3]\n\t"
      "v_mfma_f32_16x16x32_bf16 v[16:19], a[224:227], v[168:171], v[16:19]\n\t"
      "v_mfma_f32_16x16x32_bf16 v[4:7], a[68:71], v[72:75], v[4:7]\n\t"
      "v_mfma_f32_16x16x32_bf16 v[20:23], a[228:231], v[168:171], v[20:23]\n\t"
      "v_and_b32 v197, s74, v226\n\t"
      "v_lshlrev_b32 v196, 16, v226\n\t"
      "ds_read_b128 v[120:123], v255 offset:14336\n\t"
      "s_waitcnt lgkmcnt(11)\n\t"
      "v_mfma_f32_16x16x32_bf16 v[8:11], a[4:7], v[76:79], v[8:11]\n\t"
      "v_mfma_f32_16x16x32_bf16 v[16:19], a[224:227], v[172:175], v[16:19]\n\t"
      "v_mfma_f32_16x16x32_bf16 v[12:15], a[68:71], v[76:79], v[12:15]\n\t"
      "v_mfma_f32_16x16x32_bf16 v[20:23], a[228:231], v[172:175], v[20:23]\n\t"
      "v_and_b32 v199, s74, v227\n\t"
      "v_lshlrev_b32 v198, 16, v227\n\t"
      "ds_read_b128 v[124:127], v255 offset:15360\n\t"
      "s_waitcnt lgkmcnt(11)\n\t"
      "v_mfma_f32_16x16x32_bf16 v[0:3], a[8:11], v[80:83], v[0:3]\n\t"
      "v_mfma_f32_16x16x32_bf16 v[16:19], a[224:227], v[176:179], v[16:19]\n\t"
      "v_mfma_f32_16x16x32_bf16 v[4:7], a[72:75], v[80:83], v[4:7]\n\t"
      "v_mfma_f32_16x16x32_bf16 v[20:23], a[228:231], v[176:179], v[20:23]\n\t"
      "ds_read_b128 v[64:67], v255 offset:16384\n\t"
      "s_waitcnt lgkmcnt(11)\n\t"
      "v_mfma_f32_16x16x32_bf16 v[8:11], a[8:11], v[84:87], v[8:11]\n\t"
      "v_mfma_f32_16x16x32_bf16 v[16:19], a[224:227], v[180:183], v[16:19]\n\t"
      "v_mfma_f32_16x16x32_bf16 v[12:15], a[72:75], v[84:87], v[12:15]\n\t"
      "v_mfma_f32_16x16x32_bf16 v[20:23], a[228:231], v[180:183], v[20:23]\n\t"
      "ds_read_b128 v[68:71], v255 offset:17408\n\t"
      "s_waitcnt lgkmcnt(11)\n\t"
      "v_mfma_f32_16x16x32_bf16 v[0:3], a[12:15], v[88:91], v[0:3]\n\t"
      "v_mfma_f32_16x16x32_bf16 v[16:19], a[224:227], v[184:187], v[16:19]\n\t"
      "v_mfma_f32_16x16x32_bf16 v[4:7], a[76:79], v[88:91], v[4:7]\n\t"
      "v_mfma_f32_16x16x32_bf16 v[20:23], a[228:231], v[184:187], v[20:23]\n\t"
      "ds_read_b128 v[72:75], v255 offset:18432\n\t"
      "s_waitcnt lgkmcnt(11)\n\t"
      "v_mfma_f32_16x16x32_bf16 v[8:11], a[12:15], v[92:95], v[8:11]\n\t"
      "v_mfma_f32_16x16x32_bf16 v[16:19], a[224:227], v[188:191], v[16:19]\n\t"
      "v_mfma_f32_16x16x32_bf16 v[12:15], a[76:79], v[92:95], v[12:15]\n\t"
      "v_mfma_f32_16x16x32_bf16 v[20:23], a[228:231], v[188:191], v[20:23]\n\t"
      "ds_read_b128 v[76:79], v255 offset:19456\n\t"
      "s_waitcnt lgkmcnt(11)\n\t"
      "v_mfma_f32_16x16x32_bf16 v[0:3], a[16:19], v[96:99], v[0:3]\n\t"
      "v_cvt_pk_bf16_f32 v32, v16, v17\n\t"
      "v_cvt_pk_bf16_f32 v34, v18, v19\n\t"
      "v_mfma_f32_16x16x32_bf16 v[4:7], a[80:83], v[96:99], v[4:7]\n\t"
      "ds_read_b128 v[80:83], v255 offset:20480\n\t"
      "s_waitcnt lgkmcnt(11)\n\t"
      "v_mfma_f32_16x16x32_bf16 v[8:11], a[16:19], v[100:103], v[8:11]\n\t"
      "v_cvt_pk_bf16_f32 v36, v20, v21\n\t"
      "v_cvt_pk_bf16_f32 v38, v22, v23\n\t"
      "v_mfma_f32_16x16x32_bf16 v[12:15], a[80:83], v[100:103], v[12:15]\n\t"
      "v_and_b32 v33, s74, v32\n\t"
      "v_lshlrev_b32 v32, 16, v32\n\t"
      "ds_read_b128 v[84:87], v255 offset:21504\n\t"
      "s_waitcnt lgkmcnt(11)\n\t"
      "v_mfma_f32_16x16x32_bf16 v[0:3], a[20:23], v[104:107], v[0:3]\n\t"
      "v_and_b32 v35, s74, v34\n\t"
      "v_lshlrev_b32 v34, 16, v34\n\t"
      "v_mfma_f32_16x16x32_bf16 v[4:7], a[84:87], v[104:107], v[4:7]\n\t"
      "v_and_b32 v37, s74, v36\n\t"
      "v_lshlrev_b32 v36, 16, v36\n\t"
      "ds_read_b128 v[88:91], v255 offset:22528\n\t"
      "s_waitcnt lgkmcnt(11)\n\t"
      "v_mfma_f32_16x16x32_bf16 v[8:11], a[20:23], v[108:111], v[8:11]\n\t"
      "v_and_b32 v39, s74, v38\n\t"
      "v_lshlrev_b32 v38, 16, v38\n\t"
      "v_mfma_f32_16x16x32_bf16 v[12:15], a[84:87], v[108:111], v[12:15]\n\t"
      "v_add_f32 v192, v192, v32\n\t"
      "v_add_f32 v193, v193, v33\n\t"
      "ds_read_b128 v[92:95], v255 offset:23552\n\t"
      "s_waitcnt lgkmcnt(11)\n\t"
      "v_mfma_f32_16x16x32_bf16 v[0:3], a[24:27], v[112:115], v[0:3]\n\t"
      "v_add_f32 v194, v194, v34\n\t"
      "v_add_f32 v195, v195, v35\n\t"
      "v_mfma_f32_16x16x32_bf16 v[4:7], a[88:91], v[112:115], v[4:7]\n\t"
      "v_add_f32 v196, v196, v36\n\t"
      "v_add_f32 v197, v197, v37\n\t"
      "ds_read_b128 v[96:99], v255 offset:24576\n\t"
      "s_waitcnt lgkmcnt(11)\n\t"
      "v_mfma_f32_16x16x32_bf16 v[8:11], a[24:27], v[116:119], v[8:11]\n\t"
      "v_add_f32 v198, v198, v38\n\t"
      "v_add_f32 v199, v199, v39\n\t"
      "v_mfma_f32_16x16x32_bf16 v[12:15], a[88:91], v[116:119], v[12:15]\n\t"
      "v_cvt_pk_bf16_f32 v200, v192, v193\n\t"
      "v_cvt_pk_bf16_f32 v201, v194, v195\n\t"
      "ds_read_b128 v[100:103], v255 offset:25600\n\t"
      "s_waitcnt lgkmcnt(11)\n\t"
      "v_mfma_f32_16x16x32_bf16 v[0:3], a[28:31], v[120:123], v[0:3]\n\t"
      "v_cvt_pk_bf16_f32 v202, v196, v197\n\t"
      "v_cvt_pk_bf16_f32 v203, v198, v199\n\t"
      "v_mfma_f32_16x16x32_bf16 v[4:7], a[92:95], v[120:123], v[4:7]\n\t"
      "v_and_b32 v193, s74, v200\n\t"
      "v_lshlrev_b32 v192, 16, v200\n\t"
      "ds_read_b128 v[104:107], v255 offset:26624\n\t"
      "s_waitcnt lgkmcnt(11)\n\t"
      "v_mfma_f32_16x16x32_bf16 v[8:11], a[28:31], v[124:127], v[8:11]\n\t"
      "v_and_b32 v195, s74, v201\n\t"
      "v_lshlrev_b32 v194, 16, v201\n\t"
      "v_mfma_f32_16x16x32_bf16 v[12:15], a[92:95], v[124:127], v[12:15]\n\t"
      "v_and_b32 v197, s74, v202\n\t"
      "v_lshlrev_b32 v196, 16, v202\n\t"
      "ds_read_b128 v[108:111], v255 offset:27648\n\t"
      "s_waitcnt lgkmcnt(11)\n\t"
      "v_mfma_f32_16x16x32_bf16 v[0:3], a[32:35], v[64:67], v[0:3]\n\t"
      "v_and_b32 v199, s74, v203\n\t"
      "v_lshlrev_b32 v198, 16, v203\n\t"
      "v_mfma_f32_16x16x32_bf16 v[4:7], a[96:99], v[64:67], v[4:7]\n\t"
      "v_add_f32 v32, v192, v196\n\t"
      "v_add_f32 v33, v193, v197\n\t"
      "ds_read_b128 v[112:115], v255 offset:28672\n\t"
      "s_waitcnt lgkmcnt(11)\n\t"
      "v_mfma_f32_16x16x32_bf16 v[8:11], a[32:35], v[68:71], v[8:11]\n\t"
      "v_add_f32 v34, v194, v198\n\t"
      "v_add_f32 v35, v195, v199\n\t"
      "v_mfma_f32_16x16x32_bf16 v[12:15], a[96:99], v[68:71], v[12:15]\n\t"
      "v_add_f32 v32, v32, v34\n\t"
      "v_add_f32 v33, v33, v35\n\t"
      "ds_read_b128 v[116:119], v255 offset:29696\n\t"
      "s_waitcnt lgkmcnt(11)\n\t"
      "v_mfma_f32_16x16x32_bf16 v[0:3], a[36:39], v[72:75], v[0:3]\n\t"
      "v_add_f32 v32, v32, v33\n\t"
      "v_mfma_f32_16x16x32_bf16 v[4:7], a[100:103], v[72:75], v[4:7]\n\t"
      "ds_read_b128 v[120:123], v255 offset:30720\n\t"
      "s_waitcnt lgkmcnt(11)\n\t"
      "v_mfma_f32_16x16x32_bf16 v[8:11], a[36:39], v[76:79], v[8:11]\n\t"
      "v_add_f32_dpp v32, v32, v32 quad_perm:[1,0,3,2] row_mask:0xf bank_mask:0xf\n\t"
      "v_mfma_f32_16x16x32_bf16 v[12:15], a[100:103], v[76:79], v[12:15]\n\t"
      "ds_read_b128 v[124:127], v255 offset:31744\n\t"
      "s_waitcnt lgkmcnt(11)\n\t"
      "v_mfma_f32_16x16x32_bf16 v[0:3], a[40:43], v[80:83], v[0:3]\n\t"
      "v_add_f32_dpp v32, v32, v32 quad_perm:[2,3,0,1] row_mask:0xf bank_mask:0xf\n\t"
      "v_mfma_f32_16x16x32_bf16 v[4:7], a[104:107], v[80:83], v[4:7]\n\t"
      "s_waitcnt lgkmcnt(10)\n\t"
      "v_mfma_f32_16x16x32_bf16 v[8:11], a[40:43], v[84:87], v[8:11]\n\t"
      "v_add_f32_dpp v32, v32, v32 row_ror:4 row_mask:0xf bank_mask:0xf\n\t"
      "v_mfma_f32_16x16x32_bf16 v[12:15], a[104:107], v[84:87], v[12:15]\n\t"
      "s_waitcnt lgkmcnt(9)\n\t"
      "v_mfma_f32_16x16x32_bf16 v[0:3], a[44:47], v[88:91], v[0:3]\n\t"
      "v_add_f32_dpp v32, v32, v32 row_ror:8 row_mask:0xf bank_mask:0xf\n\t"
      "v_mov_b32 v33, v32\n\t"
      "v_mfma_f32_16x16x32_bf16 v[4:7], a[108:111], v[88:91], v[4:7]\n\t"
      "s_waitcnt lgkmcnt(8)\n\t"
      "v_mfma_f32_16x16x32_bf16 v[8:11], a[44:47], v[92:95], v[8:11]\n\t"
      "v_permlane16_swap_b32 v32, v33\n\t"
      "v_add_f32 v32, v32, v33\n\t"
      "v_mfma_f32_16x16x32_bf16 v[12:15], a[108:111], v[92:95], v[12:15]\n\t"
      "v_mov_b32 v33, v32\n\t"
      "s_waitcnt lgkmcnt(7)\n\t"
      "v_mfma_f32_16x16x32_bf16 v[0:3], a[48:51], v[96:99], v[0:3]\n\t"
      "v_permlane32_swap_b32 v32, v33\n\t"
      "v_add_f32 v32, v32, v33\n\t"
      "v_mfma_f32_16x16x32_bf16 v[4:7], a[112:115], v[96:99], v[4:7]\n\t"
      "v_mul_f32 v204, 0x3b000000, v32\n\t"
      "v_sub_f32 v40, v192, v204\n\t"
      "s_waitcnt lgkmcnt(6)\n\t"
      "v_mfma_f32_16x16x32_bf16 v[8:11], a[48:51], v[100:103], v[8:11]\n\t"
      "v_sub_f32 v41, v193, v204\n\t"
      "v_sub_f32 v42, v194, v204\n\t"
      "v_mfma_f32_16x16x32_bf16 v[12:15], a[112:115], v[100:103], v[12:15]\n\t"
      "v_sub_f32 v43, v195, v204\n\t"
      "v_sub_f32 v44, v196, v204\n\t"
      "s_waitcnt lgkmcnt(5)\n\t"
      "v_mfma_f32_16x16x32_bf16 v[0:3], a[52:55], v[104:107], v[0:3]\n\t"
      "v_sub_f32 v45, v197, v204\n\t"
      "v_sub_f32 v46, v198, v204\n\t"
      "v_mfma_f32_16x16x32_bf16 v[4:7], a[116:119], v[104:107], v[4:7]\n\t"
      "v_sub_f32 v47, v199, v204\n\t"
      "v_mul_f32 v32, v40, v40\n\t"
      "s_waitcnt lgkmcnt(4)\n\t"
      "v_mfma_f32_16x16x32_bf16 v[8:11], a[52:55], v[108:111], v[8:11]\n\t"
      "v_mul_f32 v33, v41, v41\n\t"
      "v_fma_f32 v32, v42, v42, v32\n\t"
      "v_mfma_f32_16x16x32_bf16 v[12:15], a[116:119], v[108:111], v[12:15]\n\t"
      "v_fma_f32 v33, v43, v43, v33\n\t"
      "v_fma_f32 v32, v44, v44, v32\n\t"
      "s_waitcnt lgkmcnt(3)\n\t"
      "v_mfma_f32_16x16x32_bf16 v[0:3], a[56:59], v[112:115], v[0:3]\n\t"
      "v_fma_f32 v33, v45, v45, v33\n\t"
      "v_fma_f32 v32, v46, v46, v32\n\t"
      "v_mfma_f32_16x16x32_bf16 v[4:7], a[120:123], v[112:115], v[4:7]\n\t"
      "v_fma_f32 v33, v47, v47, v33\n\t"
      "v_add_f32 v32, v32, v33\n\t"
      "s_waitcnt lgkmcnt(2)\n\t"
      "v_mfma_f32_16x16x32_bf16 v[8:11], a[56:59], v[116:119], v[8:11]\n\t"
      "v_add_f32_dpp v32, v32, v32 quad_perm:[1,0,3,2] row_mask:0xf bank_mask:0xf\n\t"
      "v_mfma_f32_16x16x32_bf16 v[12:15], a[120:123], v[116:119], v[12:15]\n\t"
      "s_waitcnt lgkmcnt(1)\n\t"
      "v_mfma_f32_16x16x32_bf16 v[0:3], a[60:63], v[120:123], v[0:3]\n\t"
      "v_add_f32_dpp v32, v32, v32 quad_perm:[2,3,0,1] row_mask:0xf bank_mask:0xf\n\t"
      "v_mfma_f32_16x16x32_bf16 v[4:7], a[124:127], v[120:123], v[4:7]\n\t"
      "s_waitcnt lgkmcnt(0)\n\t"
      "v_mfma_f32_16x16x32_bf16 v[8:11], a[60:63], v[124:127], v[8:11]\n\t"
      "v_add_f32_dpp v32, v32, v32 row_ror:4 row_mask:0xf bank_mask:0xf\n\t"
      "v_mfma_f32_16x16x32_bf16 v[12:15], a[124:127], v[124:127], v[12:15]\n\t"
      "s_nop 0\n\t"
      "v_add_f32_dpp v32, v32, v32 row_ror:8 row_mask:0xf bank_mask:0xf\n\t"
      "v_mov_b32 v33, v32\n\t"
      "s_nop 1\n\t"
      "v_permlane16_swap_b32 v32, v33\n\t"
      "v_add_f32 v32, v32, v33\n\t"
      "v_mov_b32 v33, v32\n\t"
      "s_nop 1\n\t"
      "v_permlane32_swap_b32 v32, v33\n\t"
      "v_add_f32 v32, v32, v33\n\t"
      "v_mul_f32 v205, 0x3b000000, v32\n\t"
      "v_xor_b32 v254, 0x8000, v254\n\t"
      "v_xor_b32 v255, 0x8000, v255\n\t"
      "s_add_u32 s69, s53, 0x4000\n\t"
      "s_add_u32 s48, s48, 4\n\t"
      "s_waitcnt vmcnt(40)\n\t"
      "v_cvt_pk_bf16_f32 v0, v0, v1\n\t"
      "v_cvt_pk_bf16_f32 v1, v2, v3\n\t"
      "v_cvt_pk_bf16_f32 v2, v4, v5\n\t"
      "v_cvt_pk_bf16_f32 v3, v6, v7\n\t"
      "v_min_u32 v0, -2, v0\n\t"
      "v_min_u32 v1, -2, v1\n\t"
      "v_min_u32 v2, -2, v2\n\t"
      "v_min_u32 v3, -2, v3\n\t"
      "buffer_store_dwordx4 v[0:3], v238, s[36:39], s53 offen sc1\n\t"
      "v_cvt_pk_bf16_f32 v8, v8, v9\n\t"
      "v_cvt_pk_bf16_f32 v9, v10, v11\n\t"
      "v_cvt_pk_bf16_f32 v10, v12, v13\n\t"
      "v_cvt_pk_bf16_f32 v11, v14, v15\n\t"
      "v_min_u32 v8, -2, v8\n\t"
      "v_min_u32 v9, -2, v9\n\t"
      "v_min_u32 v10, -2, v10\n\t"
      "v_min_u32 v11, -2, v11\n\t"
      "buffer_store_dwordx4 v[8:11], v238, s[36:39], s69 offen sc1\n\t"
      "s_add_u32 s53, s53, 1048576\n\t"
      "s_nop 1\n\t"
      "s_branch WTOP%=\n\t"
      "WOL3%=:\n\t"
      "buffer_load_dwordx4 v[128:131], v252, s[36:39], s51 offen sc1\n\t"
      "s_waitcnt lgkmcnt(11)\n\t"
      "v_mfma_f32_16x16x32_bf16 v[0:3], a[0:3], v[64:67], 0\n\t"
      "v_mfma_f32_16x16x32_bf16 v[16:19], a[224:227], v[160:163], 0\n\t"
      "v_mfma_f32_16x16x32_bf16 v[4:7], a[64:67], v[64:67], 0\n\t"
      "v_mfma_f32_16x16x32_bf16 v[20:23], a[228:231], v[160:163], 0\n\t"
      "v_and_b32 v193, s74, v224\n\t"
      "v_lshlrev_b32 v192, 16, v224\n\t"
      "ds_read_b128 v[112:115], v255 offset:12288\n\t"
      "s_waitcnt lgkmcnt(11)\n\t"
      "v_mfma_f32_16x16x32_bf16 v[8:11], a[0:3], v[68:71], 0\n\t"
      "v_mfma_f32_16x16x32_bf16 v[16:19], a[224:227], v[164:167], v[16:19]\n\t"
      "v_mfma_f32_16x16x32_bf16 v[12:15], a[64:67], v[68:71], 0\n\t"
      "v_mfma_f32_16x16x32_bf16 v[20:23], a[228:231], v[164:167], v[20:23]\n\t"
      "v_and_b32 v195, s74, v225\n\t"
      "v_lshlrev_b32 v194, 16, v225\n\t"
      "ds_read_b128 v[116:119], v255 offset:13312\n\t"
      "s_waitcnt lgkmcnt(11)\n\t"
      "v_mfma_f32_16x16x32_bf16 v[0:3], a[4:7], v[72:75], v[0:3]\n\t"
      "v_mfma_f32_16x16x32_bf16 v[16:19], a[224:227], v[168:171], v[16:19]\n\t"
      "v_mfma_f32_16x16x32_bf16 v[4:7], a[68:71], v[72:75], v[4:7]\n\t"
      "v_mfma_f32_16x16x32_bf16 v[20:23], a[228:231], v[168:171], v[20:23]\n\t"
      "v_and_b32 v197, s74, v226\n\t"
      "v_lshlrev_b32 v196, 16, v226\n\t"
      "ds_read_b128 v[120:123], v255 offset:14336\n\t"
      "s_waitcnt lgkmcnt(11)\n\t"
      "v_mfma_f32_16x16x32_bf16 v[8:11], a[4:7], v[76:79], v[8:11]\n\t"
      "v_mfma_f32_16x16x32_bf16 v[16:19], a[224:227], v[172:175], v[16:19]\n\t"
      "v_mfma_f32_16x16x32_bf16 v[12:15], a[68:71], v[76:79], v[12:15]\n\t"
      "buffer_load_dwordx4 v[132:135], v252, s[36:39], s51 offen offset:512 sc1\n\t"
      "ds_read_b128 v[124:127], v255 offset:15360\n\t"
      "s_waitcnt lgkmcnt(11)\n\t"
      "v_mfma_f32_16x16x32_bf16 v[0:3], a[8:11], v[80:83], v[0:3]\n\t"
      "v_mfma_f32_16x16x32_bf16 v[20:23], a[228:231], v[172:175], v[20:23]\n\t"
      "v_and_b32 v199, s74, v227\n\t"
      "v_lshlrev_b32 v198, 16, v227\n\t"
      "v_mfma_f32_16x16x32_bf16 v[4:7], a[72:75], v[80:83], v[4:7]\n\t"
      "v_mfma_f32_16x16x32_bf16 v[16:19], a[224:227], v[176:179], v[16:19]\n\t"
      "ds_read_b128 v[64:67], v255 offset:16384\n\t"
      "s_waitcnt lgkmcnt(11)\n\t"
      "v_mfma_f32_16x16x32_bf16 v[8:11], a[8:11], v[84:87], v[8:11]\n\t"
      "v_mfma_f32_16x16x32_bf16 v[20:23], a[228:231], v[176:179], v[20:23]\n\t"
      "v_mfma_f32_16x16x32_bf16 v[12:15], a[72:75], v[84:87], v[12:15]\n\t"
      "v_mfma_f32_16x16x32_bf16 v[16:19], a[224:227], v[180:183], v[16:19]\n\t"
      "ds_read_b128 v[68:71], v255 offset:17408\n\t"
      "s_waitcnt lgkmcnt(11)\n\t"
      "v_mfma_f32_16x16x32_bf16 v[0:3], a[12:15], v[88:91], v[0:3]\n\t"
      "v_mfma_f32_16x16x32_bf16 v[20:23], a[228:231], v[180:183], v[20:23]\n\t"
      "v_mfma_f32_16x16x32_bf16 v[4:7], a[76:79], v[88:91], v[4:7]\n\t"
      "v_mfma_f32_16x16x32_bf16 v[16:19], a[224:227], v[184:187], v[16:19]\n\t"
      "ds_read_b128 v[72:75], v255 offset:18432\n\t"
      "s_waitcnt lgkmcnt(11)\n\t"
      "v_mfma_f32_16x16x32_bf16 v[8:11], a[12:15], v[92:95], v[8:11]\n\t"
      "v_mfma_f32_16x16x32_bf16 v[20:23], a[228:231], v[184:187], v[20:23]\n\t"
      "v_mfma_f32_16x16x32_bf16 v[12:15], a[76:79], v[92:95], v[12:15]\n\t"
      "buffer_load_dwordx4 v[136:139], v252, s[36:39], s51 offen offset:1024 sc1\n\t"
      "ds_read_b128 v[76:79], v255 offset:19456\n\t"
      "s_waitcnt lgkmcnt(11)\n\t"
      "v_mfma_f32_16x16x32_bf16 v[0:3], a[16:19], v[96:99], v[0:3]\n\t"
      "v_mfma_f32_16x16x32_bf16 v[16:19], a[224:227], v[188:191], v[16:19]\n\t"
      "v_mfma_f32_16x16x32_bf16 v[4:7], a[80:83], v[96:99], v[4:7]\n\t"
      "v_mfma_f32_16x16x32_bf16 v[20:23], a[228:231], v[188:191], v[20:23]\n\t"
      "ds_read_b128 v[80:83], v255 offset:20480\n\t"
      "s_waitcnt lgkmcnt(11)\n\t"
      "v_mfma_f32_16x16x32_bf16 v[8:11], a[16:19], v[100:103], v[8:11]\n\t"
      "v_cvt_pk_bf16_f32 v32, v16, v17\n\t"
      "v_cvt_pk_bf16_f32 v34, v18, v19\n\t"
      "v_mfma_f32_16x16x32_bf16 v[12:15], a[80:83], v[100:103], v[12:15]\n\t"
      "ds_read_b128 v[84:87], v255 offset:21504\n\t"
      "s_waitcnt lgkmcnt(11)\n\t"
      "v_mfma_f32_16x16x32_bf16 v[0:3], a[20:23], v[104:107], v[0:3]\n\t"
      "v_cvt_pk_bf16_f32 v36, v20, v21\n\t"
      "v_cvt_pk_bf16_f32 v38, v22, v23\n\t"
      "v_mfma_f32_16x16x32_bf16 v[4:7], a[84:87], v[104:107], v[4:7]\n\t"
      "v_and_b32 v33, s74, v32\n\t"
      "v_lshlrev_b32 v32, 16, v32\n\t"
      "ds_read_b128 v[88:91], v255 offset:22528\n\t"
      "s_waitcnt lgkmcnt(11)\n\t"
      "v_mfma_f32_16x16x32_bf16 v[8:11], a[20:23], v[108:111], v[8:11]\n\t"
      "v_and_b32 v35, s74, v34\n\t"
      "v_lshlrev_b32 v34, 16, v34\n\t"
      "v_mfma_f32_16x16x32_bf16 v[12:15], a[84:87], v[108:111], v[12:15]\n\t"
      "buffer_load_dwordx4 v[140:143], v252, s[36:39], s51 offen offset:1536 sc1\n\t"
      "ds_read_b128 v[92:95], v255 offset:23552\n\t"
      "s_waitcnt lgkmcnt(11)\n\t"
      "v_mfma_f32_16x16x32_bf16 v[0:3], a[24:27], v[112:115], v[0:3]\n\t"
      "v_and_b32 v37, s74, v36\n\t"
      "v_lshlrev_b32 v36, 16, v36\n\t"
      "v_mfma_f32_16x16x32_bf16 v[4:7], a[88:91], v[112:115], v[4:7]\n\t"
      "v_and_b32 v39, s74, v38\n\t"
      "v_lshlrev_b32 v38, 16, v38\n\t"
      "ds_read_b128 v[96:99], v255 offset:24576\n\t"
      "s_waitcnt lgkmcnt(11)\n\t"
      "v_mfma_f32_16x16x32_bf16 v[8:11], a[24:27], v[116:119], v[8:11]\n\t"
      "v_add_f32 v192, v192, v32\n\t"
      "v_add_f32 v193, v193, v33\n\t"
      "v_mfma_f32_16x16x32_bf16 v[12:15], a[88:91], v[116:119], v[12:15]\n\t"
      "v_add_f32 v194, v194, v34\n\t"
      "v_add_f32 v195, v195, v35\n\t"
      "ds_read_b128 v[100:103], v255 offset:25600\n\t"
      "s_waitcnt lgkmcnt(11)\n\t"
      "v_mfma_f32_16x16x32_bf16 v[0:3], a[28:31], v[120:123], v[0:3]\n\t"
      "v_add_f32 v196, v196, v36\n\t"
      "v_add_f32 v197, v197, v37\n\t"
      "v_mfma_f32_16x16x32_bf16 v[4:7], a[92:95], v[120:123], v[4:7]\n\t"
      "v_add_f32 v198, v198, v38\n\t"
      "v_add_f32 v199, v199, v39\n\t"
      "ds_read_b128 v[104:107], v255 offset:26624\n\t"
      "s_waitcnt lgkmcnt(11)\n\t"
      "v_mfma_f32_16x16x32_bf16 v[8:11], a[28:31], v[124:127], v[8:11]\n\t"
      "buffer_load_dwordx4 v[144:147], v253, s[36:39], s52 offen sc1\n\t"
      "v_mfma_f32_16x16x32_bf16 v[12:15], a[92:95], v[124:127], v[12:15]\n\t"
      "v_cvt_pk_bf16_f32 v200, v192, v193\n\t"
      "v_cvt_pk_bf16_f32 v201, v194, v195\n\t"
      "ds_read_b128 v[108:111], v255 offset:27648\n\t"
      "s_waitcnt lgkmcnt(11)\n\t"
      "v_mfma_f32_16x16x32_bf16 v[0:3], a[32:35], v[64:67], v[0:3]\n\t"
      "v_cvt_pk_bf16_f32 v202, v196, v197\n\t"
      "v_cvt_pk_bf16_f32 v203, v198, v199\n\t"
      "v_mfma_f32_16x16x32_bf16 v[4:7], a[96:99], v[64:67], v[4:7]\n\t"
      "v_and_b32 v193, s74, v200\n\t"
      "v_lshlrev_b32 v192, 16, v200\n\t"
      "ds_read_b128 v[112:115], v255 offset:28672\n\t"
      "s_waitcnt lgkmcnt(11)\n\t"
      "v_mfma_f32_16x16x32_bf16 v[8:11], a[32:35], v[68:71], v[8:11]\n\t"
      "v_and_b32 v195, s74, v201\n\t"
      "v_lshlrev_b32 v194, 16, v201\n\t"
      "v_mfma_f32_16x16x32_bf16 v[12:15], a[96:99], v[68:71], v[12:15]\n\t"
      "v_and_b32 v197, s74, v202\n\t"
      "v_lshlrev_b32 v196, 16, v202\n\t"
      "ds_read_b128 v[116:119], v255 offset:29696\n\t"
      "s_waitcnt lgkmcnt(11)\n\t"
      "v_mfma_f32_16x16x32_bf16 v[0:3], a[36:39], v[72:75], v[0:3]\n\t"
      "v_and_b32 v199, s74, v203\n\t"
      "v_lshlrev_b32 v198, 16, v203\n\t"
      "v_mfma_f32_16x16x32_bf16 v[4:7], a[100:103], v[72:75], v[4:7]\n\t"
      "v_add_f32 v32, v192, v196\n\t"
      "v_add_f32 v33, v193, v197\n\t"
      "ds_read_b128 v[120:123], v255 offset:30720\n\t"
      "s_waitcnt lgkmcnt(11)\n\t"
      "v_mfma_f32_16x16x32_bf16 v[8:11], a[36:39], v[76:79], v[8:11]\n\t"
      "buffer_load_dwordx4 v[148:151], v253, s[36:39], s52 offen offset:512 sc1\n\t"
      "v_mfma_f32_16x16x32_bf16 v[12:15], a[100:103], v[76:79], v[12:15]\n\t"
      "v_add_f32 v34, v194, v198\n\t"
      "v_add_f32 v35, v195, v199\n\t"
      "ds_read_b128 v[124:127], v255 offset:31744\n\t"
      "s_waitcnt lgkmcnt(11)\n\t"
      "v_mfma_f32_16x16x32_bf16 v[0:3], a[40:43], v[80:83], v[0:3]\n\t"
      "v_add_f32 v32, v32, v34\n\t"
      "v_add_f32 v33, v33, v35\n\t"
      "v_mfma_f32_16x16x32_bf16 v[4:7], a[104:107], v[80:83], v[4:7]\n\t"
      "v_add_f32 v32, v32, v33\n\t"
      "s_waitcnt lgkmcnt(10)\n\t"
      "v_mfma_f32_16x16x32_bf16 v[8:11], a[40:43], v[84:87], v[8:11]\n\t"
      "v_add_f32_dpp v32, v32, v32 quad_perm:[1,0,3,2] row_mask:0xf bank_mask:0xf\n\t"
      "v_mfma_f32_16x16x32_bf16 v[12:15], a[104:107], v[84:87], v[12:15]\n\t"
      "s_waitcnt lgkmcnt(9)\n\t"
      "v_mfma_f32_16x16x32_bf16 v[0:3], a[44:47], v[88:91], v[0:3]\n\t"
      "v_add_f32_dpp v32, v32, v32 quad_perm:[2,3,0,1] row_mask:0xf bank_mask:0xf\n\t"
      "v_mfma_f32_16x16x32_bf16 v[4:7], a[108:111], v[88:91], v[4:7]\n\t"
      "s_waitcnt lgkmcnt(8)\n\t"
      "v_mfma_f32_16x16x32_bf16 v[8:11], a[44:47], v[92:95], v[8:11]\n\t"
      "buffer_load_dwordx4 v[152:155], v253, s[36:39], s52 offen offset:1024 sc1\n\t"
      "v_mfma_f32_16x16x32_bf16 v[12:15], a[108:111], v[92:95], v[12:15]\n\t"
      "v_add_f32_dpp v32, v32, v32 row_ror:4 row_mask:0xf bank_mask:0xf\n\t"
      "s_waitcnt lgkmcnt(7)\n\t"
      "v_mfma_f32_16x16x32_bf16 v[0:3], a[48:51], v[96:99], v[0:3]\n\t"
      "v_add_f32_dpp v32, v32, v32 row_ror:8 row_mask:0xf bank_mask:0xf\n\t"
      "v_mov_b32 v33, v32\n\t"
      "v_mfma_f32_16x16x32_bf16 v[4:7], a[112:115], v[96:99], v[4:7]\n\t"
      "s_waitcnt lgkmcnt(6)\n\t"
      "v_mfma_f32_16x16x32_bf16 v[8:11], a[48:51], v[100:103], v[8:11]\n\t"
      "v_permlane16_swap_b32 v32, v33\n\t"
      "v_add_f32 v32, v32, v33\n\t"
      "v_mfma_f32_16x16x32_bf16 v[12:15], a[112:115], v[100:103], v[12:15]\n\t"
      "v_mov_b32 v33, v32\n\t"
      "s_waitcnt lgkmcnt(5)\n\t"
      "v_mfma_f32_16x16x32_bf16 v[0:3], a[52:55], v[104:107], v[0:3]\n\t"
      "v_permlane32_swap_b32 v32, v33\n\t"
      "v_add_f32 v32, v32, v33\n\t"
      "v_mfma_f32_16x16x32_bf16 v[4:7], a[116:119], v[104:107], v[4:7]\n\t"
      "v_mul_f32 v204, 0x3b000000, v32\n\t"
      "v_sub_f32 v40, v192, v204\n\t"
      "s_waitcnt lgkmcnt(4)\n\t"
      "v_mfma_f32_16x16x32_bf16 v[8:11], a[52:55], v[108:111], v[8:11]\n\t"
      "v_sub_f32 v41, v193, v204\n\t"
      "v_sub_f32 v42, v194, v204\n\t"
      "v_mfma_f32_16x16x32_bf16 v[12:15], a[116:119], v[108:111], v[12:15]\n\t"
      "buffer_load_dwordx4 v[156:159], v253, s[36:39], s52 offen offset:1536 sc1\n\t"
      "s_waitcnt lgkmcnt(3)\n\t"
      "v_mfma_f32_16x16x32_bf16 v[0:3], a[56:59], v[112:115], v[0:3]\n\t"
      "v_sub_f32 v43, v195, v204\n\t"
      "v_sub_f32 v44, v196, v204\n\t"
      "v_mfma_f32_16x16x32_bf16 v[4:7], a[120:123], v[112:115], v[4:7]\n\t"
      "v_sub_f32 v45, v197, v204\n\t"
      "v_sub_f32 v46, v198, v204\n\t"
      "s_waitcnt lgkmcnt(2)\n\t"
      "v_mfma_f32_16x16x32_bf16 v[8:11], a[56:59], v[116:119], v[8:11]\n\t"
      "v_sub_f32 v47, v199, v204\n\t"
      "v_mul_f32 v32, v40, v40\n\t"
      "v_mfma_f32_16x16x32_bf16 v[12:15], a[120:123], v[116:119], v[12:15]\n\t"
      "v_mul_f32 v33, v41, v41\n\t"
      "v_fma_f32 v32, v42, v42, v32\n\t"
      "s_waitcnt lgkmcnt(1)\n\t"
      "v_mfma_f32_16x16x32_bf16 v[0:3], a[60:63], v[120:123], v[0:3]\n\t"
      "v_fma_f32 v33, v43, v43, v33\n\t"
      "v_fma_f32 v32, v44, v44, v32\n\t"
      "v_mfma_f32_16x16x32_bf16 v[4:7], a[124:127], v[120:123], v[4:7]\n\t"
      "v_fma_f32 v33, v45, v45, v33\n\t"
      "v_fma_f32 v32, v46, v46, v32\n\t"
      "s_waitcnt lgkmcnt(0)\n\t"
      "v_mfma_f32_16x16x32_bf16 v[8:11], a[60:63], v[124:127], v[8:11]\n\t"
      "v_fma_f32 v33, v47, v47, v33\n\t"
      "v_add_f32 v32, v32, v33\n\t"
      "v_mfma_f32_16x16x32_bf16 v[12:15], a[124:127], v[124:127], v[12:15]\n\t"
      "s_nop 0\n\t"
      "v_add_f32_dpp v32, v32, v32 quad_perm:[1,0,3,2] row_mask:0xf bank_mask:0xf\n\t"
      "s_nop 1\n\t"
      "v_add_f32_dpp v32, v32, v32 quad_perm:[2,3,0,1] row_mask:0xf bank_mask:0xf\n\t"
      "s_nop 1\n\t"
      "v_add_f32_dpp v32, v32, v32 row_ror:4 row_mask:0xf bank_mask:0xf\n\t"
      "s_nop 1\n\t"
      "v_add_f32_dpp v32, v32, v32 row_ror:8 row_mask:0xf bank_mask:0xf\n\t"
      "v_mov_b32 v33, v32\n\t"
      "s_nop 1\n\t"
      "v_permlane16_swap_b32 v32, v33\n\t"
      "v_add_f32 v32, v32, v33\n\t"
      "v_mov_b32 v33, v32\n\t"
      "s_nop 1\n\t"
      "v_permlane32_swap_b32 v32, v33\n\t"
      "v_add_f32 v32, v32, v33\n\t"
      "v_mul_f32 v205, 0x3b000000, v32\n\t"
      "v_xor_b32 v254, 0x8000, v254\n\t"
      "v_xor_b32 v255, 0x8000, v255\n\t"
      "s_add_u32 s69, s53, 0x4000\n\t"
      "s_add_u32 s48, s48, 4\n\t"
      "s_waitcnt vmcnt(40)\n\t"
      "v_cvt_pk_bf16_f32 v0, v0, v1\n\t"
      "v_cvt_pk_bf16_f32 v1, v2, v3\n\t"
      "v_cvt_pk_bf16_f32 v2, v4, v5\n\t"
      "v_cvt_pk_bf16_f32 v3, v6, v7\n\t"
      "v_min_u32 v0, -2, v0\n\t"
      "v_min_u32 v1, -2, v1\n\t"
      "v_min_u32 v2, -2, v2\n\t"
      "v_min_u32 v3, -2, v3\n\t"
      "buffer_store_dwordx4 v[0:3], v238, s[36:39], s53 offen sc1\n\t"
      "v_cvt_pk_bf16_f32 v8, v8, v9\n\t"
      "v_cvt_pk_bf16_f32 v9, v10, v11\n\t"
      "v_cvt_pk_bf16_f32 v10, v12, v13\n\t"
      "v_cvt_pk_bf16_f32 v11, v14, v15\n\t"
      "v_min_u32 v8, -2, v8\n\t"
      "v_min_u32 v9, -2, v9\n\t"
      "v_min_u32 v10, -2, v10\n\t"
      "v_min_u32 v11, -2, v11\n\t"
      "buffer_store_dwordx4 v[8:11], v238, s[36:39], s69 offen sc1\n\t"
      "s_add_u32 s53, s53, 1048576\n\t"
      "s_nop 1\n\t"
      "s_branch WTOP%=\n\t"
      "WOM2%=:\n\t"
      "s_mov_b32 s65, 0\n\t"
      "s_add_u32 s24, s96, 8\n\t"
      "s_cmp_lg_u32 s96, 0\n\t"
      "s_cbranch_scc1 WOL5%=\n\t"
      "WOL4%=:\n\t"
      "s_waitcnt lgkmcnt(11)\n\t"
      "v_mfma_f32_16x16x32_bf16 v[0:3], a[0:3], v[64:67], 0\n\t"
      "v_cvt_pk_bf16_f32 v204, v204, v205\n\t"
      "v_and_b32 v205, s74, v204\n\t"
      "v_mfma_f32_16x16x32_bf16 v[4:7], a[64:67], v[64:67], 0\n\t"
      "v_lshlrev_b32 v204, 16, v204\n\t"
      "v_add_f32 v205, 0x3727c5ac, v205\n\t"
      "ds_read_b128 v[112:115], v255 offset:12288\n\t"
      "s_waitcnt lgkmcnt(11)\n\t"
      "v_mfma_f32_16x16x32_bf16 v[8:11], a[0:3], v[68:71], 0\n\t"
      "v_sub_f32 v40, v192, v204\n\t"
      "v_sub_f32 v41, v193, v204\n\t"
      "v_mfma_f32_16x16x32_bf16 v[12:15], a[64:67], v[68:71], 0\n\t"
      "v_sub_f32 v42, v194, v204\n\t"
      "v_sub_f32 v43, v195, v204\n\t"
      "ds_read_b128 v[116:119], v255 offset:13312\n\t"
      "s_waitcnt lgkmcnt(11)\n\t"
      "v_mfma_f32_16x16x32_bf16 v[0:3], a[4:7], v[72:75], v[0:3]\n\t"
      "v_sub_f32 v44, v196, v204\n\t"
      "v_sub_f32 v45, v197, v204\n\t"
      "v_mfma_f32_16x16x32_bf16 v[4:7], a[68:71], v[72:75], v[4:7]\n\t"
      "v_sub_f32 v46, v198, v204\n\t"
      "v_sub_f32 v47, v199, v204\n\t"
      "ds_read_b128 v[120:123], v255 offset:14336\n\t"
      "s_waitcnt lgkmcnt(11)\n\t"
      "v_mfma_f32_16x16x32_bf16 v[8:11], a[4:7], v[76:79], v[8:11]\n\t"
      "v_cvt_pk_bf16_f32 v205, v205, v205\n\t"
      "v_and_b32 v205, s74, v205\n\t"
      "v_mfma_f32_16x16x32_bf16 v[12:15], a[68:71], v[76:79], v[12:15]\n\t"
      "v_sqrt_f32 v205, v205\n\t"
      "ds_read_b128 v[124:127], v255 offset:15360\n\t"
      "s_waitcnt lgkmcnt(11)\n\t"
      "v_mfma_f32_16x16x32_bf16 v[0:3], a[8:11], v[80:83], v[0:3]\n\t"
      "v_cvt_pk_bf16_f32 v40, v40, v41\n\t"
      "v_cvt_pk_bf16_f32 v42, v42, v43\n\t"
      "v_mfma_f32_16x16x32_bf16 v[4:7], a[72:75], v[80:83], v[4:7]\n\t"
      "v_cvt_pk_bf16_f32 v44, v44, v45\n\t"
      "v_cvt_pk_bf16_f32 v46, v46, v47\n\t"
      "ds_read_b128 v[64:67], v255 offset:16384\n\t"
      "s_waitcnt lgkmcnt(11)\n\t"
      "v_mfma_f32_16x16x32_bf16 v[8:11], a[8:11], v[84:87], v[8:11]\n\t"
      "v_cvt_pk_bf16_f32 v205, v205, v205\n\t"
      "v_and_b32 v205, s74, v205\n\t"
      "v_mfma_f32_16x16x32_bf16 v[12:15], a[72:75], v[84:87], v[12:15]\n\t"
      "v_rcp_f32 v204, v205\n\t"
      "ds_read_b128 v[68:71], v255 offset:17408\n\t"
      "s_waitcnt lgkmcnt(11)\n\t"
      "v_mfma_f32_16x16x32_bf16 v[0:3], a[12:15], v[88:91], v[0:3]\n\t"
      "v_and_b32 v41, s74, v40\n\t"
      "v_lshlrev_b32 v40, 16, v40\n\t"
      "v_mfma_f32_16x16x32_bf16 v[4:7], a[76:79], v[88:91], v[4:7]\n\t"
      "v_and_b32 v43, s74, v42\n\t"
      "v_lshlrev_b32 v42, 16, v42\n\t"
      "ds_read_b128 v[72:75], v255 offset:18432\n\t"
      "s_waitcnt lgkmcnt(11)\n\t"
      "v_mfma_f32_16x16x32_bf16 v[8:11], a[12:15], v[92:95], v[8:11]\n\t"
      "v_and_b32 v45, s74, v44\n\t"
      "v_lshlrev_b32 v44, 16, v44\n\t"
      "v_mfma_f32_16x16x32_bf16 v[12:15], a[76:79], v[92:95], v[12:15]\n\t"
      "v_and_b32 v47, s74, v46\n\t"
      "v_lshlrev_b32 v46, 16, v46\n\t"
      "ds_read_b128 v[76:79], v255 offset:19456\n\t"
      "s_waitcnt lgkmcnt(11)\n\t"
      "v_mfma_f32_16x16x32_bf16 v[0:3], a[16:19], v[96:99], v[0:3]\n\t"
      "v_mul_f32 v40, v40, v204\n\t"
      "v_mul_f32 v41, v41, v204\n\t"
      "v_mfma_f32_16x16x32_bf16 v[4:7], a[80:83], v[96:99], v[4:7]\n\t"
      "v_mul_f32 v42, v42, v204\n\t"
      "v_mul_f32 v43, v43, v204\n\t"
      "ds_read_b128 v[80:83], v255 offset:20480\n\t"
      "s_waitcnt lgkmcnt(11)\n\t"
      "v_mfma_f32_16x16x32_bf16 v[8:11], a[16:19], v[100:103], v[8:11]\n\t"
      "v_mul_f32 v44, v44, v204\n\t"
      "v_mul_f32 v45, v45, v204\n\t"
      "v_mfma_f32_16x16x32_bf16 v[12:15], a[80:83], v[100:103], v[12:15]\n\t"
      "v_mul_f32 v46, v46, v204\n\t"
      "v_mul_f32 v47, v47, v204\n\t"
      "ds_read_b128 v[84:87], v255 offset:21504\n\t"
      "s_waitcnt lgkmcnt(11)\n\t"
      "v_mfma_f32_16x16x32_bf16 v[0:3], a[20:23], v[104:107], v[0:3]\n\t"
      "v_cvt_pk_bf16_f32 v52, v40, v41\n\t"
      "v_cvt_pk_bf16_f32 v53, v42, v43\n\t"
      "v_mfma_f32_16x16x32_bf16 v[4:7], a[84:87], v[104:107], v[4:7]\n\t"
      "v_cvt_pk_bf16_f32 v54, v44, v45\n\t"
      "v_cvt_pk_bf16_f32 v55, v46, v47\n\t"
      "ds_read_b128 v[88:91], v255 offset:22528\n\t"
      "s_waitcnt lgkmcnt(11)\n\t"
      "v_mfma_f32_16x16x32_bf16 v[8:11], a[20:23], v[108:111], v[8:11]\n\t"
      "v_and_b32 v33, s74, v52\n\t"
      "v_lshlrev_b32 v32, 16, v52\n\t"
      "v_mfma_f32_16x16x32_bf16 v[12:15], a[84:87], v[108:111], v[12:15]\n\t"
      "v_and_b32 v35, s74, v53\n\t"
      "v_lshlrev_b32 v34, 16, v53\n\t"
      "ds_read_b128 v[92:95], v255 offset:23552\n\t"
      "s_waitcnt lgkmcnt(11)\n\t"
      "v_mfma_f32_16x16x32_bf16 v[0:3], a[24:27], v[112:115], v[0:3]\n\t"
      "v_and_b32 v37, s74, v54\n\t"
      "v_lshlrev_b32 v36, 16, v54\n\t"
      "v_mfma_f32_16x16x32_bf16 v[4:7], a[88:91], v[112:115], v[4:7]\n\t"
      "v_and_b32 v39, s74, v55\n\t"
      "v_lshlrev_b32 v38, 16, v55\n\t"
      "ds_read_b128 v[96:99], v255 offset:24576\n\t"
      "s_waitcnt lgkmcnt(11)\n\t"
      "v_mfma_f32_16x16x32_bf16 v[8:11], a[24:27], v[116:119], v[8:11]\n\t"
      "v_mul_f32 v40, v32, v240\n\t"
      "v_mul_f32 v41, v33, v241\n\t"
      "v_mfma_f32_16x16x32_bf16 v[12:15], a[88:91], v[116:119], v[12:15]\n\t"
      "v_fma_f32 v40, v34, v242, v40\n\t"
      "v_fma_f32 v41, v35, v243, v41\n\t"
      "ds_read_b128 v[100:103], v255 offset:25600\n\t"
      "s_waitcnt lgkmcnt(11)\n\t"
      "v_mfma_f32_16x16x32_bf16 v[0:3], a[28:31], v[120:123], v[0:3]\n\t"
      "v_fma_f32 v40, v36, v244, v40\n\t"
      "v_fma_f32 v41, v37, v245, v41\n\t"
      "v_mfma_f32_16x16x32_bf16 v[4:7], a[92:95], v[120:123], v[4:7]\n\t"
      "v_fma_f32 v40, v38, v246, v40\n\t"
      "v_fma_f32 v41, v39, v247, v41\n\t"
      "ds_read_b128 v[104:107], v255 offset:26624\n\t"
      "s_waitcnt lgkmcnt(11)\n\t"
      "v_mfma_f32_16x16x32_bf16 v[8:11], a[28:31], v[124:127], v[8:11]\n\t"
      "v_add_f32 v40, v40, v41\n\t"
      "s_lshl_b32 s73, s49, 5\n\t"
      "s_add_u32 s73, s73, s60\n\t"
      "s_lshl_b32 s69, s73, 2\n\t"
      "s_add_u32 s69, s69, s57\n\t"
      "s_lshl_b32 s70, s73, 7\n\t"
      "s_add_u32 s70, s70, s57\n\t"
      "s_sub_u32 s70, s70, 212992\n\t"
      "s_cmp_ge_u32 s49, 56\n\t"
      "s_cselect_b32 s70, s70, s69\n\t"
      "v_add_f32_dpp v40, v40, v40 quad_perm:[1,0,3,2] row_mask:0xf bank_mask:0xf\n\t"
      "v_mfma_f32_16x16x32_bf16 v[12:15], a[92:95], v[124:127], v[12:15]\n\t"
      "ds_read_b128 v[108:111], v255 offset:27648\n\t"
      "s_waitcnt lgkmcnt(11)\n\t"
      "v_mfma_f32_16x16x32_bf16 v[0:3], a[32:35], v[64:67], v[0:3]\n\t"
      "v_add_f32_dpp v40, v40, v40 quad_perm:[2,3,0,1] row_mask:0xf bank_mask:0xf\n\t"
      "v_mfma_f32_16x16x32_bf16 v[4:7], a[96:99], v[64:67], v[4:7]\n\t"
      "ds_read_b128 v[112:115], v255 offset:28672\n\t"
      "s_waitcnt lgkmcnt(11)\n\t"
      "v_mfma_f32_16x16x32_bf16 v[8:11], a[32:35], v[68:71], v[8:11]\n\t"
      "v_add_f32_dpp v40, v40, v40 row_ror:4 row_mask:0xf bank_mask:0xf\n\t"
      "v_mfma_f32_16x16x32_bf16 v[12:15], a[96:99], v[68:71], v[12:15]\n\t"
      "ds_read_b128 v[116:119], v255 offset:29696\n\t"
      "s_waitcnt lgkmcnt(11)\n\t"
      "v_mfma_f32_16x16x32_bf16 v[0:3], a[36:39], v[72:75], v[0:3]\n\t"
      "v_add_f32_dpp v40, v40, v40 row_ror:8 row_mask:0xf bank_mask:0xf\n\t"
      "v_mov_b32 v41, v40\n\t"
      "v_mfma_f32_16x16x32_bf16 v[4:7], a[100:103], v[72:75], v[4:7]\n\t"
      "ds_read_b128 v[120:123], v255 offset:30720\n\t"
      "s_waitcnt lgkmcnt(11)\n\t"
      "v_mfma_f32_16x16x32_bf16 v[8:11], a[36:39], v[76:79], v[8:11]\n\t"
      "v_permlane16_swap_b32 v40, v41\n\t"
      "v_add_f32 v40, v40, v41\n\t"
      "v_mfma_f32_16x16x32_bf16 v[12:15], a[100:103], v[76:79], v[12:15]\n\t"
      "v_mov_b32 v41, v40\n\t"
      "ds_read_b128 v[124:127], v255 offset:31744\n\t"
      "s_waitcnt lgkmcnt(11)\n\t"
      "v_mfma_f32_16x16x32_bf16 v[0:3], a[40:43], v[80:83], v[0:3]\n\t"
      "v_permlane32_swap_b32 v40, v41\n\t"
      "v_add_f32 v40, v40, v41\n\t"
      "v_mfma_f32_16x16x32_bf16 v[4:7], a[104:107], v[80:83], v[4:7]\n\t"
      "v_cvt_pk_bf16_f32 v40, v40, v40\n\t"
      "v_and_b32 v40, s74, v40\n\t"
      "s_waitcnt lgkmcnt(10)\n\t"
      "v_mfma_f32_16x16x32_bf16 v[8:11], a[40:43], v[84:87], v[8:11]\n\t"
      "s_mov_b64 exec, 1\n\t"
      "buffer_store_dword v40, off, s[36:39], s70 sc1\n\t"
      "s_mov_b64 exec, -1\n\t"
      "v_add_f32 v192, v192, v32\n\t"
      "v_add_f32 v193, v193, v33\n\t"
      "v_mfma_f32_16x16x32_bf16 v[12:15], a[104:107], v[84:87], v[12:15]\n\t"
      "v_add_f32 v194, v194, v34\n\t"
      "v_add_f32 v195, v195, v35\n\t"
      "s_waitcnt lgkmcnt(9)\n\t"
      "v_mfma_f32_16x16x32_bf16 v[0:3], a[44:47], v[88:91], v[0:3]\n\t"
      "v_add_f32 v196, v196, v36\n\t"
      "v_add_f32 v197, v197, v37\n\t"
      "v_mfma_f32_16x16x32_bf16 v[4:7], a[108:111], v[88:91], v[4:7]\n\t"
      "v_add_f32 v198, v198, v38\n\t"
      "v_add_f32 v199, v199, v39\n\t"
      "s_waitcnt lgkmcnt(8)\n\t"
      "v_mfma_f32_16x16x32_bf16 v[8:11], a[44:47], v[92:95], v[8:11]\n\t"
      "v_readfirstlane_b32 s68, v40\n\t"
      "v_cvt_pk_bf16_f32 v56, v192, v193\n\t"
      "v_mfma_f32_16x16x32_bf16 v[12:15], a[108:111], v[92:95], v[12:15]\n\t"
      "v_cvt_pk_bf16_f32 v57, v194, v195\n\t"
      "v_cvt_pk_bf16_f32 v58, v196, v197\n\t"
      "s_waitcnt lgkmcnt(7)\n\t"
      "v_mfma_f32_16x16x32_bf16 v[0:3], a[48:51], v[96:99], v[0:3]\n\t"
      "v_cvt_pk_bf16_f32 v59, v198, v199\n\t"
      "s_add_u32 s67, s69, 8192\n\t"
      "s_lshl_b32 s73, s73, 10\n\t"
      "s_add_u32 s69, s73, s58\n\t"
      "s_add_u32 s70, s73, s59\n\t"
      "buffer_store_dwordx2 v[56:57], v249, s[44:47], s73 offen sc1\n\t"
      "v_mfma_f32_16x16x32_bf16 v[4:7], a[112:115], v[96:99], v[4:7]\n\t"
      "buffer_store_dwordx2 v[52:53], v249, s[36:39], s69 offen sc1\n\t"
      "buffer_store_dwordx2 v[200:201], v249, s[36:39], s70 offen sc1\n\t"
      "s_waitcnt lgkmcnt(6)\n\t"
      "v_mfma_f32_16x16x32_bf16 v[8:11], a[48:51], v[100:103], v[8:11]\n\t"
      "buffer_store_dwordx2 v[58:59], v249, s[44:47], s73 offen offset:512 sc1\n\t"
      "buffer_store_dwordx2 v[54:55], v249, s[36:39], s69 offen offset:512 sc1\n\t"
      "v_mfma_f32_16x16x32_bf16 v[12:15], a[112:115], v[100:103], v[12:15]\n\t"
      "buffer_store_dwordx2 v[202:203], v249, s[36:39], s70 offen offset:512 sc1\n\t"
      "s_mov_b32 s49, s89\n\t"
      "s_add_u32 s89, s89, 16\n\t"
      "s_lshr_b32 s69, s89, 4\n\t"
      "s_cmp_eq_u32 s69, 3\n\t"
      "s_cselect_b32 s89, s93, s89\n\t"
      "s_cmp_ge_u32 s49, 48\n\t"
      "s_cselect_b32 s60, s94, s60\n\t"
      "s_cselect_b32 s54, s95, s54\n\t"
      "s_waitcnt lgkmcnt(5)\n\t"
      "v_mfma_f32_16x16x32_bf16 v[0:3], a[52:55], v[104:107], v[0:3]\n\t"
      "v_mfma_f32_16x16x32_bf16 v[4:7], a[116:119], v[104:107], v[4:7]\n\t"
      "s_waitcnt lgkmcnt(4)\n\t"
      "v_mfma_f32_16x16x32_bf16 v[8:11], a[52:55], v[108:111], v[8:11]\n\t"
      "v_mfma_f32_16x16x32_bf16 v[12:15], a[116:119], v[108:111], v[12:15]\n\t"
      "s_waitcnt lgkmcnt(3)\n\t"
      "v_mfma_f32_16x16x32_bf16 v[0:3], a[56:59], v[112:115], v[0:3]\n\t"
      "v_mfma_f32_16x16x32_bf16 v[4:7], a[120:123], v[112:115], v[4:7]\n\t"
      "s_waitcnt lgkmcnt(2)\n\t"
      "v_mfma_f32_16x16x32_bf16 v[8:11], a[56:59], v[116:119], v[8:11]\n\t"
      "v_mfma_f32_16x16x32_bf16 v[12:15], a[120:123], v[116:119], v[12:15]\n\t"
      "s_waitcnt lgkmcnt(1)\n\t"
      "v_mfma_f32_16x16x32_bf16 v[0:3], a[60:63], v[120:123], v[0:3]\n\t"
      "v_mfma_f32_16x16x32_bf16 v[4:7], a[124:127], v[120:123], v[4:7]\n\t"
      "s_waitcnt lgkmcnt(0)\n\t"
      "v_mfma_f32_16x16x32_bf16 v[8:11], a[60:63], v[124:127], v[8:11]\n\t"
      "v_mfma_f32_16x16x32_bf16 v[12:15], a[124:127], v[124:127], v[12:15]\n\t"
      "v_xor_b32 v254, 0x8000, v254\n\t"
      "v_xor_b32 v255, 0x8000, v255\n\t"
      "s_add_u32 s69, s53, 0x4000\n\t"
      "s_add_u32 s48, s48, 4\n\t"
      "s_waitcnt vmcnt(40)\n\t"
      "v_cvt_pk_bf16_f32 v0, v0, v1\n\t"
      "v_cvt_pk_bf16_f32 v1, v2, v3\n\t"
      "v_cvt_pk_bf16_f32 v2, v4, v5\n\t"
      "v_cvt_pk_bf16_f32 v3, v6, v7\n\t"
      "v_min_u32 v0, -2, v0\n\t"
      "v_min_u32 v1, -2, v1\n\t"
      "v_min_u32 v2, -2, v2\n\t"
      "v_min_u32 v3, -2, v3\n\t"
      "buffer_store_dwordx4 v[0:3], v238, s[36:39], s53 offen sc1\n\t"
      "v_cvt_pk_bf16_f32 v8, v8, v9\n\t"
      "v_cvt_pk_bf16_f32 v9, v10, v11\n\t"
      "v_cvt_pk_bf16_f32 v10, v12, v13\n\t"
      "v_cvt_pk_bf16_f32 v11, v14, v15\n\t"
      "v_min_u32 v8, -2, v8\n\t"
      "v_min_u32 v9, -2, v9\n\t"
      "v_min_u32 v10, -2, v10\n\t"
      "v_min_u32 v11, -2, v11\n\t"
      "buffer_store_dwordx4 v[8:11], v238, s[36:39], s69 offen sc1\n\t"
      "s_add_u32 s53, s53, 1048576\n\t"
      "s_nop 1\n\t"
      "s_branch WTOP%=\n\t"
      "WOL5%=:\n\t"
      "buffer_load_dwordx4 v[128:131], v252, s[36:39], s51 offen sc1\n\t"
      "s_waitcnt lgkmcnt(11)\n\t"
      "v_mfma_f32_16x16x32_bf16 v[0:3], a[0:3], v[64:67], 0\n\t"
      "v_cvt_pk_bf16_f32 v204, v204, v205\n\t"
      "v_and_b32 v205, s74, v204\n\t"
      "v_mfma_f32_16x16x32_bf16 v[4:7], a[64:67], v[64:67], 0\n\t"
      "v_lshlrev_b32 v204, 16, v204\n\t"
      "v_add_f32 v205, 0x3727c5ac, v205\n\t"
      "ds_read_b128 v[112:115], v255 offset:12288\n\t"
      "s_waitcnt lgkmcnt(11)\n\t"
      "v_mfma_f32_16x16x32_bf16 v[8:11], a[0:3], v[68:71], 0\n\t"
      "v_sub_f32 v40, v192, v204\n\t"
      "v_sub_f32 v41, v193, v204\n\t"
      "v_mfma_f32_16x16x32_bf16 v[12:15], a[64:67], v[68:71], 0\n\t"
      "v_sub_f32 v42, v194, v204\n\t"
      "v_sub_f32 v43, v195, v204\n\t"
      "ds_read_b128 v[116:119], v255 offset:13312\n\t"
      "s_waitcnt lgkmcnt(11)\n\t"
      "v_mfma_f32_16x16x32_bf16 v[0:3], a[4:7], v[72:75], v[0:3]\n\t"
      "v_sub_f32 v44, v196, v204\n\t"
      "v_sub_f32 v45, v197, v204\n\t"
      "v_mfma_f32_16x16x32_bf16 v[4:7], a[68:71], v[72:75], v[4:7]\n\t"
      "v_sub_f32 v46, v198, v204\n\t"
      "v_sub_f32 v47, v199, v204\n\t"
      "ds_read_b128 v[120:123], v255 offset:14336\n\t"
      "s_waitcnt lgkmcnt(11)\n\t"
      "v_mfma_f32_16x16x32_bf16 v[8:11], a[4:7], v[76:79], v[8:11]\n\t"
      "v_cvt_pk_bf16_f32 v205, v205, v205\n\t"
      "v_and_b32 v205, s74, v205\n\t"
      "v_mfma_f32_16x16x32_bf16 v[12:15], a[68:71], v[76:79], v[12:15]\n\t"
      "buffer_load_dwordx4 v[132:135], v252, s[36:39], s51 offen offset:512 sc1\n\t"
      "ds_read_b128 v[124:127], v255 offset:15360\n\t"
      "s_waitcnt lgkmcnt(11)\n\t"
      "v_mfma_f32_16x16x32_bf16 v[0:3], a[8:11], v[80:83], v[0:3]\n\t"
      "v_sqrt_f32 v205, v205\n\t"
      "v_mfma_f32_16x16x32_bf16 v[4:7], a[72:75], v[80:83], v[4:7]\n\t"
      "v_cvt_pk_bf16_f32 v40, v40, v41\n\t"
      "v_cvt_pk_bf16_f32 v42, v42, v43\n\t"
      "ds_read_b128 v[64:67], v255 offset:16384\n\t"
      "s_waitcnt lgkmcnt(11)\n\t"
      "v_mfma_f32_16x16x32_bf16 v[8:11], a[8:11], v[84:87], v[8:11]\n\t"
      "v_cvt_pk_bf16_f32 v44, v44, v45\n\t"
      "v_cvt_pk_bf16_f32 v46, v46, v47\n\t"
      "v_mfma_f32_16x16x32_bf16 v[12:15], a[72:75], v[84:87], v[12:15]\n\t"
      "v_cvt_pk_bf16_f32 v205, v205, v205\n\t"
      "v_and_b32 v205, s74, v205\n\t"
      "ds_read_b128 v[68:71], v255 offset:17408\n\t"
      "s_waitcnt lgkmcnt(11)\n\t"
      "v_mfma_f32_16x16x32_bf16 v[0:3], a[12:15], v[88:91], v[0:3]\n\t"
      "v_rcp_f32 v204, v205\n\t"
      "v_mfma_f32_16x16x32_bf16 v[4:7], a[76:79], v[88:91], v[4:7]\n\t"
      "v_and_b32 v41, s74, v40\n\t"
      "v_lshlrev_b32 v40, 16, v40\n\t"
      "ds_read_b128 v[72:75], v255 offset:18432\n\t"
      "s_waitcnt lgkmcnt(11)\n\t"
      "v_mfma_f32_16x16x32_bf16 v[8:11], a[12:15], v[92:95], v[8:11]\n\t"
      "v_and_b32 v43, s74, v42\n\t"
      "v_lshlrev_b32 v42, 16, v42\n\t"
      "v_mfma_f32_16x16x32_bf16 v[12:15], a[76:79], v[92:95], v[12:15]\n\t"
      "buffer_load_dwordx4 v[136:139], v252, s[36:39], s51 offen offset:1024 sc1\n\t"
      "ds_read_b128 v[76:79], v255 offset:19456\n\t"
      "s_waitcnt lgkmcnt(11)\n\t"
      "v_mfma_f32_16x16x32_bf16 v[0:3], a[16:19], v[96:99], v[0:3]\n\t"
      "v_and_b32 v45, s74, v44\n\t"
      "v_lshlrev_b32 v44, 16, v44\n\t"
      "v_mfma_f32_16x16x32_bf16 v[4:7], a[80:83], v[96:99], v[4:7]\n\t"
      "v_and_b32 v47, s74, v46\n\t"
      "v_lshlrev_b32 v46, 16, v46\n\t"
      "ds_read_b128 v[80:83], v255 offset:20480\n\t"
      "s_waitcnt lgkmcnt(11)\n\t"
      "v_mfma_f32_16x16x32_bf16 v[8:11], a[16:19], v[100:103], v[8:11]\n\t"
      "v_mul_f32 v40, v40, v204\n\t"
      "v_mul_f32 v41, v41, v204\n\t"
      "v_mfma_f32_16x16x32_bf16 v[12:15], a[80:83], v[100:103], v[12:15]\n\t"
      "v_mul_f32 v42, v42, v204\n\t"
      "v_mul_f32 v43, v43, v204\n\t"
      "ds_read_b128 v[84:87], v255 offset:21504\n\t"
      "s_waitcnt lgkmcnt(11)\n\t"
      "v_mfma_f32_16x16x32_bf16 v[0:3], a[20:23], v[104:107], v[0:3]\n\t"
      "v_mul_f32 v44, v44, v204\n\t"
      "v_mul_f32 v45, v45, v204\n\t"
      "v_mfma_f32_16x16x32_bf16 v[4:7], a[84:87], v[104:107], v[4:7]\n\t"
      "v_mul_f32 v46, v46, v204\n\t"
      "v_mul_f32 v47, v47, v204\n\t"
      "ds_read_b128 v[88:91], v255 offset:22528\n\t"
      "s_waitcnt lgkmcnt(11)\n\t"
      "v_mfma_f32_16x16x32_bf16 v[8:11], a[20:23], v[108:111], v[8:11]\n\t"
      "v_cvt_pk_bf16_f32 v52, v40, v41\n\t"
      "v_cvt_pk_bf16_f32 v53, v42, v43\n\t"
      "v_mfma_f32_16x16x32_bf16 v[12:15], a[84:87], v[108:111], v[12:15]\n\t"
      "buffer_load_dwordx4 v[140:143], v252, s[36:39], s51 offen offset:1536 sc1\n\t"
      "ds_read_b128 v[92:95], v255 offset:23552\n\t"
      "s_waitcnt lgkmcnt(11)\n\t"
      "v_mfma_f32_16x16x32_bf16 v[0:3], a[24:27], v[112:115], v[0:3]\n\t"
      "v_cvt_pk_bf16_f32 v54, v44, v45\n\t"
      "v_cvt_pk_bf16_f32 v55, v46, v47\n\t"
      "v_mfma_f32_16x16x32_bf16 v[4:7], a[88:91], v[112:115], v[4:7]\n\t"
      "v_and_b32 v33, s74, v52\n\t"
      "v_lshlrev_b32 v32, 16, v52\n\t"
      "ds_read_b128 v[96:99], v255 offset:24576\n\t"
      "s_waitcnt lgkmcnt(11)\n\t"
      "v_mfma_f32_16x16x32_bf16 v[8:11], a[24:27], v[116:119], v[8:11]\n\t"
      "v_and_b32 v35, s74, v53\n\t"
      "v_lshlrev_b32 v34, 16, v53\n\t"
      "v_mfma_f32_16x16x32_bf16 v[12:15], a[88:91], v[116:119], v[12:15]\n\t"
      "v_and_b32 v37, s74, v54\n\t"
      "v_lshlrev_b32 v36, 16, v54\n\t"
      "ds_read_b128 v[100:103], v255 offset:25600\n\t"
      "s_waitcnt lgkmcnt(11)\n\t"
      "v_mfma_f32_16x16x32_bf16 v[0:3], a[28:31], v[120:123], v[0:3]\n\t"
      "v_and_b32 v39, s74, v55\n\t"
      "v_lshlrev_b32 v38, 16, v55\n\t"
      "v_mfma_f32_16x16x32_bf16 v[4:7], a[92:95], v[120:123], v[4:7]\n\t"
      "v_mul_f32 v40, v32, v240\n\t"
      "v_mul_f32 v41, v33, v241\n\t"
      "ds_read_b128 v[104:107], v255 offset:26624\n\t"
      "s_waitcnt lgkmcnt(11)\n\t"
      "v_mfma_f32_16x16x32_bf16 v[8:11], a[28:31], v[124:127], v[8:11]\n\t"
      "buffer_load_dwordx4 v[144:147], v253, s[36:39], s52 offen sc1\n\t"
      "v_mfma_f32_16x16x32_bf16 v[12:15], a[92:95], v[124:127], v[12:15]\n\t"
      "v_fma_f32 v40, v34, v242, v40\n\t"
      "v_fma_f32 v41, v35, v243, v41\n\t"
      "ds_read_b128 v[108:111], v255 offset:27648\n\t"
      "s_waitcnt lgkmcnt(11)\n\t"
      "v_mfma_f32_16x16x32_bf16 v[0:3], a[32:35], v[64:67], v[0:3]\n\t"
      "v_fma_f32 v40, v36, v244, v40\n\t"
      "v_fma_f32 v41, v37, v245, v41\n\t"
      "v_mfma_f32_16x16x32_bf16 v[4:7], a[96:99], v[64:67], v[4:7]\n\t"
      "v_fma_f32 v40, v38, v246, v40\n\t"
      "v_fma_f32 v41, v39, v247, v41\n\t"
      "ds_read_b128 v[112:115], v255 offset:28672\n\t"
      "s_waitcnt lgkmcnt(11)\n\t"
      "v_mfma_f32_16x16x32_bf16 v[8:11], a[32:35], v[68:71], v[8:11]\n\t"
      "v_add_f32 v40, v40, v41\n\t"
      "s_lshl_b32 s73, s49, 5\n\t"
      "s_add_u32 s73, s73, s60\n\t"
      "s_lshl_b32 s69, s73, 2\n\t"
      "s_add_u32 s69, s69, s57\n\t"
      "s_lshl_b32 s70, s73, 7\n\t"
      "s_add_u32 s70, s70, s57\n\t"
      "s_sub_u32 s70, s70, 212992\n\t"
      "s_cmp_ge_u32 s49, 56\n\t"
      "s_cselect_b32 s70, s70, s69\n\t"
      "v_add_f32_dpp v40, v40, v40 quad_perm:[1,0,3,2] row_mask:0xf bank_mask:0xf\n\t"
      "v_mfma_f32_16x16x32_bf16 v[12:15], a[96:99], v[68:71], v[12:15]\n\t"
      "ds_read_b128 v[116:119], v255 offset:29696\n\t"
      "s_waitcnt lgkmcnt(11)\n\t"
      "v_mfma_f32_16x16x32_bf16 v[0:3], a[36:39], v[72:75], v[0:3]\n\t"
      "v_add_f32_dpp v40, v40, v40 quad_perm:[2,3,0,1] row_mask:0xf bank_mask:0xf\n\t"
      "v_mfma_f32_16x16x32_bf16 v[4:7], a[100:103], v[72:75], v[4:7]\n\t"
      "ds_read_b128 v[120:123], v255 offset:30720\n\t"
      "s_waitcnt lgkmcnt(11)\n\t"
      "v_mfma_f32_16x16x32_bf16 v[8:11], a[36:39], v[76:79], v[8:11]\n\t"
      "buffer_load_dwordx4 v[148:151], v253, s[36:39], s52 offen offset:512 sc1\n\t"
      "v_mfma_f32_16x16x32_bf16 v[12:15], a[100:103], v[76:79], v[12:15]\n\t"
      "v_add_f32_dpp v40, v40, v40 row_ror:4 row_mask:0xf bank_mask:0xf\n\t"
      "ds_read_b128 v[124:127], v255 offset:31744\n\t"
      "s_waitcnt lgkmcnt(11)\n\t"
      "v_mfma_f32_16x16x32_bf16 v[0:3], a[40:43], v[80:83], v[0:3]\n\t"
      "v_add_f32_dpp v40, v40, v40 row_ror:8 row_mask:0xf bank_mask:0xf\n\t"
      "v_mov_b32 v41, v40\n\t"
      "v_mfma_f32_16x16x32_bf16 v[4:7], a[104:107], v[80:83], v[4:7]\n\t"
      "s_waitcnt lgkmcnt(10)\n\t"
      "v_mfma_f32_16x16x32_bf16 v[8:11], a[40:43], v[84:87], v[8:11]\n\t"
      "v_permlane16_swap_b32 v40, v41\n\t"
      "v_add_f32 v40, v40, v41\n\t"
      "v_mfma_f32_16x16x32_bf16 v[12:15], a[104:107], v[84:87], v[12:15]\n\t"
      "v_mov_b32 v41, v40\n\t"
      "s_waitcnt lgkmcnt(9)\n\t"
      "v_mfma_f32_16x16x32_bf16 v[0:3], a[44:47], v[88:91], v[0:3]\n\t"
      "v_permlane32_swap_b32 v40, v41\n\t"
      "v_add_f32 v40, v40, v41\n\t"
      "v_mfma_f32_16x16x32_bf16 v[4:7], a[108:111], v[88:91], v[4:7]\n\t"
      "v_cvt_pk_bf16_f32 v40, v40, v40\n\t"
      "v_and_b32 v40, s74, v40\n\t"
      "s_waitcnt lgkmcnt(8)\n\t"
      "v_mfma_f32_16x16x32_bf16 v[8:11], a[44:47], v[92:95], v[8:11]\n\t"
      "buffer_load_dwordx4 v[152:155], v253, s[36:39], s52 offen offset:1024 sc1\n\t"
      "v_mfma_f32_16x16x32_bf16 v[12:15], a[108:111], v[92:95], v[12:15]\n\t"
      "s_mov_b64 exec, 1\n\t"
      "buffer_store_dword v40, off, s[36:39], s70 sc1\n\t"
      "s_mov_b64 exec, -1\n\t"
      "v_add_f32 v192, v192, v32\n\t"
      "v_add_f32 v193, v193, v33\n\t"
      "s_waitcnt lgkmcnt(7)\n\t"
      "v_mfma_f32_16x16x32_bf16 v[0:3], a[48:51], v[96:99], v[0:3]\n\t"
      "v_add_f32 v194, v194, v34\n\t"
      "v_add_f32 v195, v195, v35\n\t"
      "v_mfma_f32_16x16x32_bf16 v[4:7], a[112:115], v[96:99], v[4:7]\n\t"
      "v_add_f32 v196, v196, v36\n\t"
      "v_add_f32 v197, v197, v37\n\t"
      "s_waitcnt lgkmcnt(6)\n\t"
      "v_mfma_f32_16x16x32_bf16 v[8:11], a[48:51], v[100:103], v[8:11]\n\t"
      "v_add_f32 v198, v198, v38\n\t"
      "v_add_f32 v199, v199, v39\n\t"
      "v_mfma_f32_16x16x32_bf16 v[12:15], a[112:115], v[100:103], v[12:15]\n\t"
      "v_readfirstlane_b32 s68, v40\n\t"
      "v_cvt_pk_bf16_f32 v56, v192, v193\n\t"
      "s_waitcnt lgkmcnt(5)\n\t"
      "v_mfma_f32_16x16x32_bf16 v[0:3], a[52:55], v[104:107], v[0:3]\n\t"
      "v_cvt_pk_bf16_f32 v57, v194, v195\n\t"
      "v_cvt_pk_bf16_f32 v58, v196, v197\n\t"
      "v_mfma_f32_16x16x32_bf16 v[4:7], a[116:119], v[104:107], v[4:7]\n\t"
      "v_cvt_pk_bf16_f32 v59, v198, v199\n\t"
      "s_add_u32 s67, s69, 8192\n\t"
      "s_lshl_b32 s73, s73, 10\n\t"
      "s_add_u32 s69, s73, s58\n\t"
      "s_add_u32 s70, s73, s59\n\t"
      "buffer_store_dwordx2 v[56:57], v249, s[44:47], s73 offen sc1\n\t"
      "s_waitcnt lgkmcnt(4)\n\t"
      "v_mfma_f32_16x16x32_bf16 v[8:11], a[52:55], v[108:111], v[8:11]\n\t"
      "buffer_store_dwordx2 v[52:53], v249, s[36:39], s69 offen sc1\n\t"
      "buffer_store_dwordx2 v[200:201], v249, s[36:39], s70 offen sc1\n\t"
      "v_mfma_f32_16x16x32_bf16 v[12:15], a[116:119], v[108:111], v[12:15]\n\t"
      "buffer_load_dwordx4 v[156:159], v253, s[36:39], s52 offen offset:1536 sc1\n\t"
      "s_waitcnt lgkmcnt(3)\n\t"
      "v_mfma_f32_16x16x32_bf16 v[0:3], a[56:59], v[112:115], v[0:3]\n\t"
      "buffer_store_dwordx2 v[58:59], v249, s[44:47], s73 offen offset:512 sc1\n\t"
      "buffer_store_dwordx2 v[54:55], v249, s[36:39], s69 offen offset:512 sc1\n\t"
      "v_mfma_f32_16x16x32_bf16 v[4:7], a[120:123], v[112:115], v[4:7]\n\t"
      "buffer_store_dwordx2 v[202:203], v249, s[36:39], s70 offen offset:512 sc1\n\t"
      "s_mov_b32 s49, s89\n\t"
      "s_add_u32 s89, s89, 16\n\t"
      "s_lshr_b32 s69, s89, 4\n\t"
      "s_cmp_eq_u32 s69, 3\n\t"
      "s_cselect_b32 s89, s93, s89\n\t"
      "s_cmp_ge_u32 s49, 48\n\t"
      "s_cselect_b32 s60, s94, s60\n\t"
      "s_cselect_b32 s54, s95, s54\n\t"
      "s_waitcnt lgkmcnt(2)\n\t"
      "v_mfma_f32_16x16x32_bf16 v[8:11], a[56:59], v[116:119], v[8:11]\n\t"
      "v_mfma_f32_16x16x32_bf16 v[12:15], a[120:123], v[116:119], v[12:15]\n\t"
      "s_waitcnt lgkmcnt(1)\n\t"
      "v_mfma_f32_16x16x32_bf16 v[0:3], a[60:63], v[120:123], v[0:3]\n\t"
      "v_mfma_f32_16x16x32_bf16 v[4:7], a[124:127], v[120:123], v[4:7]\n\t"
      "s_waitcnt lgkmcnt(0)\n\t"
      "v_mfma_f32_16x16x32_bf16 v[8:11], a[60:63], v[124:127], v[8:11]\n\t"
      "v_mfma_f32_16x16x32_bf16 v[12:15], a[124:127], v[124:127], v[12:15]\n\t"
      "v_xor_b32 v254, 0x8000, v254\n\t"
      "v_xor_b32 v255, 0x8000, v255\n\t"
      "s_add_u32 s69, s53, 0x4000\n\t"
      "s_add_u32 s48, s48, 4\n\t"
      "s_waitcnt vmcnt(40)\n\t"
      "v_cvt_pk_bf16_f32 v0, v0, v1\n\t"
      "v_cvt_pk_bf16_f32 v1, v2, v3\n\t"
      "v_cvt_pk_bf16_f32 v2, v4, v5\n\t"
      "v_cvt_pk_bf16_f32 v3, v6, v7\n\t"
      "v_min_u32 v0, -2, v0\n\t"
      "v_min_u32 v1, -2, v1\n\t"
      "v_min_u32 v2, -2, v2\n\t"
      "v_min_u32 v3, -2, v3\n\t"
      "buffer_store_dwordx4 v[0:3], v238, s[36:39], s53 offen sc1\n\t"
      "v_cvt_pk_bf16_f32 v8, v8, v9\n\t"
      "v_cvt_pk_bf16_f32 v9, v10, v11\n\t"
      "v_cvt_pk_bf16_f32 v10, v12, v13\n\t"
      "v_cvt_pk_bf16_f32 v11, v14, v15\n\t"
      "v_min_u32 v8, -2, v8\n\t"
      "v_min_u32 v9, -2, v9\n\t"
      "v_min_u32 v10, -2, v10\n\t"
      "v_min_u32 v11, -2, v11\n\t"
      "buffer_store_dwordx4 v[8:11], v238, s[36:39], s69 offen sc1\n\t"
      "s_add_u32 s53, s53, 1048576\n\t"
      "s_nop 1\n\t"
      "s_branch WTOP%=\n\t"
      "WKF%=:\n\t"
      "s_cmp_eq_u32 s48, 63\n\t"
      "s_cselect_b32 s69, 0, 46\n\t"
      "s_add_u32 s88, s88, s69\n\t"
      "s_add_u32 s88, s88, -6\n\t"
      "WKT%=:\n\t"
      "s_memrealtime s[90:91]\n\t"
      "s_waitcnt lgkmcnt(0)\n\t"
      "s_sub_u32 s69, s88, s90\n\t"
      "s_and_b32 s69, s69, 0xffff\n\t"
      "s_cmp_lt_u32 s69, 400\n\t"
      "s_cbranch_scc1 WKT%=\n\t"
      "WKL%=:\n\t"
      "buffer_load_dwordx4 v[128:131], v252, s[36:39], s51 offen sc1\n\t"
      "buffer_load_dwordx4 v[132:135], v252, s[36:39], s51 offen offset:512 sc1\n\t"
      "buffer_load_dwordx4 v[136:139], v252, s[36:39], s51 offen offset:1024 sc1\n\t"
      "buffer_load_dwordx4 v[140:143], v252, s[36:39], s51 offen offset:1536 sc1\n\t"
      "buffer_load_dwordx4 v[144:147], v253, s[36:39], s52 offen sc1\n\t"
      "buffer_load_dwordx4 v[148:151], v253, s[36:39], s52 offen offset:512 sc1\n\t"
      "buffer_load_dwordx4 v[152:155], v253, s[36:39], s52 offen offset:1024 sc1\n\t"
      "buffer_load_dwordx4 v[156:159], v253, s[36:39], s52 offen offset:1536 sc1\n\t"
      "v_lshlrev_b32 v6, 4, v248\n\t"
      "v_mov_b64 v[4:5], 0\n\t"
      "s_cmp_ge_u32 s49, s48\n\t"
      "s_cbranch_scc1 WKL1%=\n\t"
      "buffer_load_dword v4, v6, s[36:39], s55 offen sc1\n\t"
      "buffer_load_dword v5, v6, s[36:39], s55 offen offset:128 sc1\n\t"
      "WKL1%=:\n\t"
      "s_waitcnt vmcnt(0)\n\t"
      "v_min_u32 v4, v4, v5\n\t"
      "v_cmp_ge_u32 vcc, s49, v4\n\t"
      "s_nop 0\n\t"
      "s_cmp_eq_u64 vcc, 0\n\t"
      "s_cselect_b32 s66, 1, 0\n\t"
      "s_cmp_lt_u32 s48, 40\n\t"
      "s_cbranch_scc1 WKV%=\n\t"
      "WKC%=:\n\t"
      "v_max3_u32 v0, v128, v129, v130\n\t"
      "v_max3_u32 v0, v0, v131, v132\n\t"
      "v_max3_u32 v0, v0, v133, v134\n\t"
      "v_max3_u32 v0, v0, v135, v136\n\t"
      "v_max3_u32 v0, v0, v137, v138\n\t"
      "v_max3_u32 v0, v0, v139, v140\n\t"
      "v_max3_u32 v0, v0, v141, v142\n\t"
      "v_max3_u32 v0, v0, v143, v144\n\t"
      "v_max3_u32 v0, v0, v145, v146\n\t"
      "v_max3_u32 v0, v0, v147, v148\n\t"
      "v_max3_u32 v0, v0, v149, v150\n\t"
      "v_max3_u32 v0, v0, v151, v152\n\t"
      "v_max3_u32 v0, v0, v153, v154\n\t"
      "v_max3_u32 v0, v0, v155, v156\n\t"
      "v_max3_u32 v0, v0, v157, v158\n\t"
      "v_max_u32 v0, v0, v159\n\t"
      "v_cmp_eq_u32 vcc, -1, v0\n\t"
      "s_nop 0\n\t"
      "s_cbranch_vccz WKV%=\n\t"
      "s_sleep 2\n\t"
      "s_sub_u32 s72, s72, 1\n\t"
      "s_cmp_gt_i32 s72, 0\n\t"
      "s_cbranch_scc1 WKL%=\n\t"
      "WKV%=:\n\t"
      "s_cmp_lt_u32 s48, 60\n\t"
      "s_cbranch_scc1 WKV1%=\n\t"
      "s_cmp_eq_u32 s66, 0\n\t"
      "s_cbranch_scc1 WB0%=\n\t"
      "s_lshl_b32 s70, s49, 5\n\t"
      "s_add_u32 s70, s70, s60\n\t"
      "s_lshl_b32 s70, s70, 10\n\t"
      "buffer_load_dwordx2 v[224:225], v249, s[40:43], s70 offen \n\t"
      "buffer_load_dwordx2 v[226:227], v249, s[40:43], s70 offen offset:512 \n\t"
      "s_lshl_b32 s69, s49, 18\n\t"
      "s_add_u32 s69, s69, s54\n\t"
      "buffer_load_dwordx4 v[160:163], v248, s[36:39], s69 offen sc1\n\t"
      "s_add_u32 s69, s69, 0x8000\n\t"
      "buffer_load_dwordx4 v[164:167], v248, s[36:39], s69 offen sc1\n\t"
      "s_add_u32 s69, s69, 0x8000\n\t"
      "buffer_load_dwordx4 v[168:171], v248, s[36:39], s69 offen sc1\n\t"
      "s_add_u32 s69, s69, 0x8000\n\t"
      "buffer_load_dwordx4 v[172:175], v248, s[36:39], s69 offen sc1\n\t"
      "s_add_u32 s69, s69, 0x8000\n\t"
      "buffer_load_dwordx4 v[176:179], v248, s[36:39], s69 offen sc1\n\t"
      "s_add_u32 s69, s69, 0x8000\n\t"
      "buffer_load_dwordx4 v[180:183], v248, s[36:39], s69 offen sc1\n\t"
      "s_add_u32 s69, s69, 0x8000\n\t"
      "buffer_load_dwordx4 v[184:187], v248, s[36:39], s69 offen sc1\n\t"
      "s_add_u32 s69, s69, 0x8000\n\t"
      "buffer_load_dwordx4 v[188:191], v248, s[36:39], s69 offen sc1\n\t"
      "s_mov_b32 s65, 1\n\t"
      "WB0%=:\n\t"
      "v_mfma_f32_16x16x32_bf16 v[0:3], v[128:131], a[160:163], 0\n\t"
      "v_mfma_f32_16x16x32_bf16 v[4:7], v[128:131], a[164:167], 0\n\t"
      "v_mfma_f32_16x16x32_bf16 v[0:3], v[132:135], a[168:171], v[0:3]\n\t"
      "v_mfma_f32_16x16x32_bf16 v[4:7], v[132:135], a[172:175], v[4:7]\n\t"
      "v_mfma_f32_16x16x32_bf16 v[0:3], v[136:139], a[176:179], v[0:3]\n\t"
      "v_mfma_f32_16x16x32_bf16 v[4:7], v[136:139], a[180:183], v[4:7]\n\t"
      "v_mfma_f32_16x16x32_bf16 v[0:3], v[140:143], a[184:187], v[0:3]\n\t"
      "v_mfma_f32_16x16x32_bf16 v[4:7], v[140:143], a[188:191], v[4:7]\n\t"
      "s_add_u32 s51, s51, 262144\n\t"
      "s_add_u32 s52, s52, 262144\n\t"
      "s_nop 4\n\t"
      "v_cvt_pk_bf16_f32 v0, v0, v1\n\t"
      "v_cvt_pk_bf16_f32 v2, v2, v3\n\t"
      "v_cvt_pk_bf16_f32 v4, v4, v5\n\t"
      "v_cvt_pk_bf16_f32 v6, v6, v7\n\t"
      "v_and_b32 v1, s74, v0\n\t"
      "v_lshlrev_b32 v0, 16, v0\n\t"
      "v_and_b32 v3, s74, v2\n\t"
      "v_lshlrev_b32 v2, 16, v2\n\t"
      "v_and_b32 v5, s74, v4\n\t"
      "v_lshlrev_b32 v4, 16, v4\n\t"
      "v_and_b32 v7, s74, v6\n\t"
      "v_lshlrev_b32 v6, 16, v6\n\t"
      "v_pk_mul_f32 v[0:1], v[0:1], s[84:85]\n\t"
      "v_pk_mul_f32 v[2:3], v[2:3], s[84:85]\n\t"
      "v_pk_mul_f32 v[4:5], v[4:5], s[84:85]\n\t"
      "v_pk_mul_f32 v[6:7], v[6:7], s[84:85]\n\t"
      "v_cvt_pk_bf16_f32 v0, v0, v1\n\t"
      "v_cvt_pk_bf16_f32 v2, v2, v3\n\t"
      "v_cvt_pk_bf16_f32 v4, v4, v5\n\t"
      "v_cvt_pk_bf16_f32 v6, v6, v7\n\t"
      "v_and_b32 v1, s74, v0\n\t"
      "v_lshlrev_b32 v0, 16, v0\n\t"
      "v_and_b32 v3, s74, v2\n\t"
      "v_lshlrev_b32 v2, 16, v2\n\t"
      "v_and_b32 v5, s74, v4\n\t"
      "v_lshlrev_b32 v4, 16, v4\n\t"
      "v_and_b32 v7, s74, v6\n\t"
      "v_lshlrev_b32 v6, 16, v6\n\t"
      "v_max3_f32 v8, v0, v1, v2\n\t"
      "v_max3_f32 v8, v8, v3, v234\n\t"
      "v_max3_f32 v9, v4, v5, v6\n\t"
      "v_max3_f32 v9, v9, v7, v235\n\t"
      "s_nop 1\n\t"
      "v_permlane16_swap_b32 v8, v9\n\t"
      "v_max_f32 v8, v8, v9\n\t"
      "v_mov_b32 v9, v8\n\t"
      "s_nop 1\n\t"
      "v_permlane16_swap_b32 v8, v9\n\t"
      "v_pk_mul_f32 v[8:9], v[8:9], s[86:87]\n\t"
      "v_pk_fma_f32 v[10:11], v[234:235], s[86:87], v[8:9] neg_lo:[0,0,1] neg_hi:[0,0,1]\n\t"
      "v_pk_fma_f32 v[0:1], v[0:1], s[86:87], v[8:9] op_sel:[0,0,0] op_sel_hi:[1,1,0] neg_lo:[0,0,1] neg_hi:[0,0,1]\n\t"
      "v_pk_fma_f32 v[2:3], v[2:3], s[86:87], v[8:9] op_sel:[0,0,0] op_sel_hi:[1,1,0] neg_lo:[0,0,1] neg_hi:[0,0,1]\n\t"
      "v_pk_fma_f32 v[4:5], v[4:5], s[86:87], v[8:9] op_sel:[0,0,1] op_sel_hi:[1,1,1] neg_lo:[0,0,1] neg_hi:[0,0,1]\n\t"
      "v_pk_fma_f32 v[6:7], v[6:7], s[86:87], v[8:9] op_sel:[0,0,1] op_sel_hi:[1,1,1] neg_lo:[0,0,1] neg_hi:[0,0,1]\n\t"
      "v_exp_f32 v10, v10\n\t"
      "v_exp_f32 v11, v11\n\t"
      "v_exp_f32 v0, v0\n\t"
      "v_exp_f32 v1, v1\n\t"
      "v_exp_f32 v2, v2\n\t"
      "v_exp_f32 v3, v3\n\t"
      "v_exp_f32 v4, v4\n\t"
      "v_exp_f32 v5, v5\n\t"
      "v_exp_f32 v6, v6\n\t"
      "v_exp_f32 v7, v7\n\t"
      "v_pk_mul_f32 v[12:13], v[236:237], v[10:11]\n\t"
      "v_pk_add_f32 v[14:15], v[0:1], v[2:3]\n\t"
      "v_pk_add_f32 v[16:17], v[4:5], v[6:7]\n\t"
      "v_add_f32 v14, v14, v15\n\t"
      "v_add_f32 v15, v16, v17\n\t"
      "v_fma_f32 v12, v14, 0.5, v12\n\t"
      "v_fma_f32 v13, v15, 0.5, v13\n\t"
      "s_nop 1\n\t"
      "v_permlane16_swap_b32 v12, v13\n\t"
      "v_add_f32 v12, v12, v13\n\t"
      "v_mov_b32 v13, v12\n\t"
      "s_nop 1\n\t"
      "v_permlane32_swap_b32 v12, v13\n\t"
      "v_add_f32 v12, v12, v13\n\t"
      "v_mov_b32 v13, v12\n\t"
      "s_nop 1\n\t"
      "v_permlane16_swap_b32 v12, v13\n\t"
      "v_rcp_f32 v12, v12\n\t"
      "v_rcp_f32 v13, v13\n\t"
      "s_nop 0\n\t"
      "v_pk_mul_f32 v[10:11], v[10:11], v[12:13]\n\t"
      "v_pk_mul_f32 v[12:13], v[12:13], 0.5 op_sel_hi:[1,0]\n\t"
      "v_pk_mul_f32 v[0:1], v[0:1], v[12:13] op_sel:[0,0] op_sel_hi:[1,0]\n\t"
      "v_pk_mul_f32 v[2:3], v[2:3], v[12:13] op_sel:[0,0] op_sel_hi:[1,0]\n\t"
      "v_pk_mul_f32 v[4:5], v[4:5], v[12:13] op_sel:[0,1] op_sel_hi:[1,1]\n\t"
      "v_pk_mul_f32 v[6:7], v[6:7], v[12:13] op_sel:[0,1] op_sel_hi:[1,1]\n\t"
      "v_cvt_pk_bf16_f32 v16, v0, v1\n\t"
      "v_cvt_pk_bf16_f32 v17, v2, v3\n\t"
      "v_cvt_pk_bf16_f32 v18, v4, v5\n\t"
      "v_cvt_pk_bf16_f32 v19, v6, v7\n\t"
      "v_pk_mul_f32 v[64:65], v[64:65], v[10:11] op_sel:[0,0] op_sel_hi:[1,0]\n\t"
      "v_pk_mul_f32 v[66:67], v[66:67], v[10:11] op_sel:[0,0] op_sel_hi:[1,0]\n\t"
      "v_pk_mul_f32 v[68:69], v[68:69], v[10:11] op_sel:[0,0] op_sel_hi:[1,0]\n\t"
      "v_pk_mul_f32 v[70:71], v[70:71], v[10:11] op_sel:[0,0] op_sel_hi:[1,0]\n\t"
      "v_mfma_f32_16x16x16_bf16 v[64:67], v[144:145], v[16:17], v[64:67]\n\t"
      "v_pk_mul_f32 v[72:73], v[72:73], v[10:11] op_sel:[0,0] op_sel_hi:[1,0]\n\t"
      "v_pk_mul_f32 v[74:75], v[74:75], v[10:11] op_sel:[0,0] op_sel_hi:[1,0]\n\t"
      "v_mfma_f32_16x16x16_bf16 v[68:71], v[146:147], v[16:17], v[68:71]\n\t"
      "v_pk_mul_f32 v[76:77], v[76:77], v[10:11] op_sel:[0,0] op_sel_hi:[1,0]\n\t"
      "v_pk_mul_f32 v[78:79], v[78:79], v[10:11] op_sel:[0,0] op_sel_hi:[1,0]\n\t"
      "v_mfma_f32_16x16x16_bf16 v[72:75], v[148:149], v[16:17], v[72:75]\n\t"
      "v_pk_mul_f32 v[80:81], v[80:81], v[10:11] op_sel:[0,0] op_sel_hi:[1,0]\n\t"
      "v_pk_mul_f32 v[82:83], v[82:83], v[10:11] op_sel:[0,0] op_sel_hi:[1,0]\n\t"
      "v_mfma_f32_16x16x16_bf16 v[76:79], v[150:151], v[16:17], v[76:79]\n\t"
      "v_pk_mul_f32 v[84:85], v[84:85], v[10:11] op_sel:[0,0] op_sel_hi:[1,0]\n\t"
      "v_pk_mul_f32 v[86:87], v[86:87], v[10:11] op_sel:[0,0] op_sel_hi:[1,0]\n\t"
      "v_mfma_f32_16x16x16_bf16 v[80:83], v[152:153], v[16:17], v[80:83]\n\t"
      "v_pk_mul_f32 v[88:89], v[88:89], v[10:11] op_sel:[0,0] op_sel_hi:[1,0]\n\t"
      "v_pk_mul_f32 v[90:91], v[90:91], v[10:11] op_sel:[0,0] op_sel_hi:[1,0]\n\t"
      "v_mfma_f32_16x16x16_bf16 v[84:87], v[154:155], v[16:17], v[84:87]\n\t"
      "v_pk_mul_f32 v[92:93], v[92:93], v[10:11] op_sel:[0,0] op_sel_hi:[1,0]\n\t"
      "v_pk_mul_f32 v[94:95], v[94:95], v[10:11] op_sel:[0,0] op_sel_hi:[1,0]\n\t"
      "v_cvt_pk_bf16_f32 v24, v64, v65\n\t"
      "v_cvt_pk_bf16_f32 v25, v66, v67\n\t"
      "v_cvt_pk_bf16_f32 v26, v68, v69\n\t"
      "v_cvt_pk_bf16_f32 v27, v70, v71\n\t"
      "ds_write_b128 v254, v[24:27]\n\t"
      "v_mfma_f32_16x16x16_bf16 v[88:91], v[156:157], v[16:17], v[88:91]\n\t"
      "v_pk_mul_f32 v[96:97], v[96:97], v[10:11] op_sel:[0,1] op_sel_hi:[1,1]\n\t"
      "v_pk_mul_f32 v[98:99], v[98:99], v[10:11] op_sel:[0,1] op_sel_hi:[1,1]\n\t"
      "v_mfma_f32_16x16x16_bf16 v[92:95], v[158:159], v[16:17], v[92:95]\n\t"
      "v_pk_mul_f32 v[100:101], v[100:101], v[10:11] op_sel:[0,1] op_sel_hi:[1,1]\n\t"
      "v_pk_mul_f32 v[102:103], v[102:103], v[10:11] op_sel:[0,1] op_sel_hi:[1,1]\n\t"
      "v_cvt_pk_bf16_f32 v28, v72, v73\n\t"
      "v_cvt_pk_bf16_f32 v29, v74, v75\n\t"
      "v_cvt_pk_bf16_f32 v30, v76, v77\n\t"
      "v_cvt_pk_bf16_f32 v31, v78, v79\n\t"
      "ds_write_b128 v254, v[28:31] offset:2048\n\t"
      "v_mfma_f32_16x16x16_bf16 v[96:99], v[144:145], v[18:19], v[96:99]\n\t"
      "v_pk_mul_f32 v[104:105], v[104:105], v[10:11] op_sel:[0,1] op_sel_hi:[1,1]\n\t"
      "v_pk_mul_f32 v[106:107], v[106:107], v[10:11] op_sel:[0,1] op_sel_hi:[1,1]\n\t"
      "v_mfma_f32_16x16x16_bf16 v[100:103], v[146:147], v[18:19], v[100:103]\n\t"
      "v_pk_mul_f32 v[108:109], v[108:109], v[10:11] op_sel:[0,1] op_sel_hi:[1,1]\n\t"
      "v_pk_mul_f32 v[110:111], v[110:111], v[10:11] op_sel:[0,1] op_sel_hi:[1,1]\n\t"
      "v_cvt_pk_bf16_f32 v24, v80, v81\n\t"
      "v_cvt_pk_bf16_f32 v25, v82, v83\n\t"
      "v_cvt_pk_bf16_f32 v26, v84, v85\n\t"
      "v_cvt_pk_bf16_f32 v27, v86, v87\n\t"
      "ds_write_b128 v254, v[24:27] offset:4096\n\t"
      "v_mfma_f32_16x16x16_bf16 v[104:107], v[148:149], v[18:19], v[104:107]\n\t"
      "v_pk_mul_f32 v[112:113], v[112:113], v[10:11] op_sel:[0,1] op_sel_hi:[1,1]\n\t"
      "v_pk_mul_f32 v[114:115], v[114:115], v[10:11] op_sel:[0,1] op_sel_hi:[1,1]\n\t"
      "v_mfma_f32_16x16x16_bf16 v[108:111], v[150:151], v[18:19], v[108:111]\n\t"
      "v_pk_mul_f32 v[116:117], v[116:117], v[10:11] op_sel:[0,1] op_sel_hi:[1,1]\n\t"
      "v_pk_mul_f32 v[118:119], v[118:119], v[10:11] op_sel:[0,1] op_sel_hi:[1,1]\n\t"
      "v_cvt_pk_bf16_f32 v28, v88, v89\n\t"
      "v_cvt_pk_bf16_f32 v29, v90, v91\n\t"
      "v_cvt_pk_bf16_f32 v30, v92, v93\n\t"
      "v_cvt_pk_bf16_f32 v31, v94, v95\n\t"
      "ds_write_b128 v254, v[28:31] offset:6144\n\t"
      "v_mfma_f32_16x16x16_bf16 v[112:115], v[152:153], v[18:19], v[112:115]\n\t"
      "v_pk_mul_f32 v[120:121], v[120:121], v[10:11] op_sel:[0,1] op_sel_hi:[1,1]\n\t"
      "v_pk_mul_f32 v[122:123], v[122:123], v[10:11] op_sel:[0,1] op_sel_hi:[1,1]\n\t"
      "v_mfma_f32_16x16x16_bf16 v[116:119], v[154:155], v[18:19], v[116:119]\n\t"
      "v_pk_mul_f32 v[124:125], v[124:125], v[10:11] op_sel:[0,1] op_sel_hi:[1,1]\n\t"
      "v_pk_mul_f32 v[126:127], v[126:127], v[10:11] op_sel:[0,1] op_sel_hi:[1,1]\n\t"
      "v_cvt_pk_bf16_f32 v24, v96, v97\n\t"
      "v_cvt_pk_bf16_f32 v25, v98, v99\n\t"
      "v_cvt_pk_bf16_f32 v26, v100, v101\n\t"
      "v_cvt_pk_bf16_f32 v27, v102, v103\n\t"
      "ds_write_b128 v254, v[24:27] offset:1024\n\t"
      "v_mfma_f32_16x16x16_bf16 v[120:123], v[156:157], v[18:19], v[120:123]\n\t"
      "v_mfma_f32_16x16x16_bf16 v[124:127], v[158:159], v[18:19], v[124:127]\n\t"
      "v_cvt_pk_bf16_f32 v28, v104, v105\n\t"
      "v_cvt_pk_bf16_f32 v29, v106, v107\n\t"
      "v_cvt_pk_bf16_f32 v30, v108, v109\n\t"
      "v_cvt_pk_bf16_f32 v31, v110, v111\n\t"
      "ds_write_b128 v254, v[28:31] offset:3072\n\t"
      "v_cvt_pk_bf16_f32 v24, v112, v113\n\t"
      "v_cvt_pk_bf16_f32 v25, v114, v115\n\t"
      "v_cvt_pk_bf16_f32 v26, v116, v117\n\t"
      "v_cvt_pk_bf16_f32 v27, v118, v119\n\t"
      "ds_write_b128 v254, v[24:27] offset:5120\n\t"
      "v_cvt_pk_bf16_f32 v28, v120, v121\n\t"
      "v_cvt_pk_bf16_f32 v29, v122, v123\n\t"
      "v_cvt_pk_bf16_f32 v30, v124, v125\n\t"
      "v_cvt_pk_bf16_f32 v31, v126, v127\n\t"
      "ds_write_b128 v254, v[28:31] offset:7168\n\t"
      "s_waitcnt vmcnt(40) lgkmcnt(0)\n\t"
      "s_barrier\n\t"
      "WO%=:\n\t"
      "s_waitcnt vmcnt(16)\n\t"
      "ds_read_b128 v[64:67], v255\n\t"
      "ds_read_b128 v[68:71], v255 offset:1024\n\t"
      "ds_read_b128 v[72:75], v255 offset:2048\n\t"
      "ds_read_b128 v[76:79], v255 offset:3072\n\t"
      "ds_read_b128 v[80:83], v255 offset:4096\n\t"
      "ds_read_b128 v[84:87], v255 offset:5120\n\t"
      "ds_read_b128 v[88:91], v255 offset:6144\n\t"
      "ds_read_b128 v[92:95], v255 offset:7168\n\t"
      "ds_read_b128 v[96:99], v255 offset:8192\n\t"
      "ds_read_b128 v[100:103], v255 offset:9216\n\t"
      "ds_read_b128 v[104:107], v255 offset:10240\n\t"
      "ds_read_b128 v[108:111], v255 offset:11264\n\t"
      "s_cmp_ge_u32 s48, 16\n\t"
      "s_cbranch_scc1 WO3%=\n\t"
      "v_mov_b64 v[20:21], -1\n\t"
      "v_mov_b64 v[22:23], -1\n\t"
      "buffer_store_dwordx4 v[20:23], v238, s[36:39], s98 offen sc1\n\t"
      "s_bitcmp0_b32 s48, 2\n\t"
      "s_cselect_b32 s69, 1, 63\n\t"
      "s_lshl_b32 s69, s69, 14\n\t"
      "s_add_u32 s98, s98, s69\n\t"
      "WO3%=:\n\t"
      "v_lshlrev_b32 v16, 4, v248\n\t"
      "s_cmp_ge_u32 s48, 60\n\t"
      "s_cbranch_scc1 WO1%=\n\t"
      "buffer_load_dwordx4 v[228:231], off, s[36:39], s56 sc1\n\t"
      "s_mov_b32 s97, s96\n\t"
      "v_mov_b64 v[232:233], 0\n\t"
      "s_cmp_lg_u32 s65, 0\n\t"
      "s_cselect_b32 s70, s89, s49\n\t"
      "s_cmp_ge_u32 s70, s48\n\t"
      "s_cbranch_scc1 WO1%=\n\t"
      "buffer_load_dword v232, v16, s[36:39], s55 offen sc1\n\t"
      "buffer_load_dword v233, v16, s[36:39], s55 offen offset:128 sc1\n\t"
      "WO1%=:\n\t"
      "s_cmp_lt_u32 s48, 56\n\t"
      "s_cselect_b32 s69, 3145728, 0\n\t"
      "s_add_u32 s50, s50, s69\n\t"
      "s_mov_b32 s24, 0\n\t"
      "s_cmp_eq_u32 s65, 2\n\t"
      "s_cbranch_scc1 WOM2%=\n\t"
      "s_cmp_lt_u32 s48, 60\n\t"
      "s_cselect_b32 s69, s65, 0\n\t"
      "s_cmp_lg_u32 s69, 0\n\t"
      "s_cbranch_scc1 WOM1%=\n\t"
      "s_cmp_lg_u32 s96, 0\n\t"
      "s_cbranch_scc1 WOL1%=\n\t"
      "WOL0%=:\n\t"
      "s_waitcnt lgkmcnt(11)\n\t"
      "v_mfma_f32_16x16x32_bf16 v[0:3], a[0:3], v[64:67], 0\n\t"
      "v_mfma_f32_16x16x32_bf16 v[4:7], a[64:67], v[64:67], 0\n\t"
      "ds_read_b128 v[112:115], v255 offset:12288\n\t"
      "s_waitcnt lgkmcnt(11)\n\t"
      "v_mfma_f32_16x16x32_bf16 v[8:11], a[0:3], v[68:71], 0\n\t"
      "v_mfma_f32_16x16x32_bf16 v[12:15], a[64:67], v[68:71], 0\n\t"
      "ds_read_b128 v[116:119], v255 offset:13312\n\t"
      "s_waitcnt lgkmcnt(11)\n\t"
      "v_mfma_f32_16x16x32_bf16 v[0:3], a[4:7], v[72:75], v[0:3]\n\t"
      "v_mfma_f32_16x16x32_bf16 v[4:7], a[68:71], v[72:75], v[4:7]\n\t"
      "ds_read_b128 v[120:123], v255 offset:14336\n\t"
      "s_waitcnt lgkmcnt(11)\n\t"
      "v_mfma_f32_16x16x32_bf16 v[8:11], a[4:7], v[76:79], v[8:11]\n\t"
      "v_mfma_f32_16x16x32_bf16 v[12:15], a[68:71], v[76:79], v[12:15]\n\t"
      "ds_read_b128 v[124:127], v255 offset:15360\n\t"
      "s_waitcnt lgkmcnt(11)\n\t"
      "v_mfma_f32_16x16x32_bf16 v[0:3], a[8:11], v[80:83], v[0:3]\n\t"
      "v_mfma_f32_16x16x32_bf16 v[4:7], a[72:75], v[80:83], v[4:7]\n\t"
      "ds_read_b128 v[64:67], v255 offset:16384\n\t"
      "s_waitcnt lgkmcnt(11)\n\t"
      "v_mfma_f32_16x16x32_bf16 v[8:11], a[8:11], v[84:87], v[8:11]\n\t"
      "v_mfma_f32_16x16x32_bf16 v[12:15], a[72:75], v[84:87], v[12:15]\n\t"
      "ds_read_b128 v[68:71], v255 offset:17408\n\t"
      "s_waitcnt lgkmcnt(11)\n\t"
      "v_mfma_f32_16x16x32_bf16 v[0:3], a[12:15], v[88:91], v[0:3]\n\t"
      "v_mfma_f32_16x16x32_bf16 v[4:7], a[76:79], v[88:91], v[4:7]\n\t"
      "ds_read_b128 v[72:75], v255 offset:18432\n\t"
      "s_waitcnt lgkmcnt(11)\n\t"
      "v_mfma_f32_16x16x32_bf16 v[8:11], a[12:15], v[92:95], v[8:11]\n\t"
      "v_mfma_f32_16x16x32_bf16 v[12:15], a[76:79], v[92:95], v[12:15]\n\t"
      "ds_read_b128 v[76:79], v255 offset:19456\n\t"
      "s_waitcnt lgkmcnt(11)\n\t"
      "v_mfma_f32_16x16x32_bf16 v[0:3], a[16:19], v[96:99], v[0:3]\n\t"
      "v_mfma_f32_16x16x32_bf16 v[4:7], a[80:83], v[96:99], v[4:7]\n\t"
      "ds_read_b128 v[80:83], v255 offset:20480\n\t"
      "s_waitcnt lgkmcnt(11)\n\t"
      "v_mfma_f32_16x16x32_bf16 v[8:11], a[16:19], v[100:103], v[8:11]\n\t"
      "v_mfma_f32_16x16x32_bf16 v[12:15], a[80:83], v[100:103], v[12:15]\n\t"
      "ds_read_b128 v[84:87], v255 offset:21504\n\t"
      "s_waitcnt lgkmcnt(11)\n\t"
      "v_mfma_f32_16x16x32_bf16 v[0:3], a[20:23], v[104:107], v[0:3]\n\t"
      "v_mfma_f32_16x16x32_bf16 v[4:7], a[84:87], v[104:107], v[4:7]\n\t"
      "ds_read_b128 v[88:91], v255 offset:22528\n\t"
      "s_waitcnt lgkmcnt(11)\n\t"
      "v_mfma_f32_16x16x32_bf16 v[8:11], a[20:23], v[108:111], v[8:11]\n\t"
      "v_mfma_f32_16x16x32_bf16 v[12:15], a[84:87], v[108:111], v[12:15]\n\t"
      "ds_read_b128 v[92:95], v255 offset:23552\n\t"
      "s_waitcnt lgkmcnt(11)\n\t"
      "v_mfma_f32_16x16x32_bf16 v[0:3], a[24:27], v[112:115], v[0:3]\n\t"
      "v_mfma_f32_16x16x32_bf16 v[4:7], a[88:91], v[112:115], v[4:7]\n\t"
      "ds_read_b128 v[96:99], v255 offset:24576\n\t"
      "s_waitcnt lgkmcnt(11)\n\t"
      "v_mfma_f32_16x16x32_bf16 v[8:11], a[24:27], v[116:119], v[8:11]\n\t"
      "v_mfma_f32_16x16x32_bf16 v[12:15], a[88:91], v[116:119], v[12:15]\n\t"
      "ds_read_b128 v[100:103], v255 offset:25600\n\t"
      "s_waitcnt lgkmcnt(11)\n\t"
      "v_mfma_f32_16x16x32_bf16 v[0:3], a[28:31], v[120:123], v[0:3]\n\t"
      "v_mfma_f32_16x16x32_bf16 v[4:7], a[92:95], v[120:123], v[4:7]\n\t"
      "ds_read_b128 v[104:107], v255 offset:26624\n\t"
      "s_waitcnt lgkmcnt(11)\n\t"
      "v_mfma_f32_16x16x32_bf16 v[8:11], a[28:31], v[124:127], v[8:11]\n\t"
      "v_mfma_f32_16x16x32_bf16 v[12:15], a[92:95], v[124:127], v[12:15]\n\t"
      "ds_read_b128 v[108:111], v255 offset:27648\n\t"
      "s_waitcnt lgkmcnt(11)\n\t"
      "v_mfma_f32_16x16x32_bf16 v[0:3], a[32:35], v[64:67], v[0:3]\n\t"
      "v_mfma_f32_16x16x32_bf16 v[4:7], a[96:99], v[64:67], v[4:7]\n\t"
      "ds_read_b128 v[112:115], v255 offset:28672\n\t"
      "s_waitcnt lgkmcnt(11)\n\t"
      "v_mfma_f32_16x16x32_bf16 v[8:11], a[32:35], v[68:71], v[8:11]\n\t"
      "v_mfma_f32_16x16x32_bf16 v[12:15], a[96:99], v[68:71], v[12:15]\n\t"
      "ds_read_b128 v[116:119], v255 offset:29696\n\t"
      "s_waitcnt lgkmcnt(11)\n\t"
      "v_mfma_f32_16x16x32_bf16 v[0:3], a[36:39], v[72:75], v[0:3]\n\t"
      "v_mfma_f32_16x16x32_bf16 v[4:7], a[100:103], v[72:75], v[4:7]\n\t"
      "ds_read_b128 v[120:123], v255 offset:30720\n\t"
      "s_waitcnt lgkmcnt(11)\n\t"
      "v_mfma_f32_16x16x32_bf16 v[8:11], a[36:39], v[76:79], v[8:11]\n\t"
      "v_mfma_f32_16x16x32_bf16 v[12:15], a[100:103], v[76:79], v[12:15]\n\t"
      "ds_read_b128 v[124:127], v255 offset:31744\n\t"
      "s_waitcnt lgkmcnt(11)\n\t"
      "v_mfma_f32_16x16x32_bf16 v[0:3], a[40:43], v[80:83], v[0:3]\n\t"
      "v_mfma_f32_16x16x32_bf16 v[4:7], a[104:107], v[80:83], v[4:7]\n\t"
      "s_waitcnt lgkmcnt(10)\n\t"
      "v_mfma_f32_16x16x32_bf16 v[8:11], a[40:43], v[84:87], v[8:11]\n\t"
      "v_mfma_f32_16x16x32_bf16 v[12:15], a[104:107], v[84:87], v[12:15]\n\t"
      "s_waitcnt lgkmcnt(9)\n\t"
      "v_mfma_f32_16x16x32_bf16 v[0:3], a[44:47], v[88:91], v[0:3]\n\t"
      "v_mfma_f32_16x16x32_bf16 v[4:7], a[108:111], v[88:91], v[4:7]\n\t"
      "s_waitcnt lgkmcnt(8)\n\t"
      "v_mfma_f32_16x16x32_bf16 v[8:11], a[44:47], v[92:95], v[8:11]\n\t"
      "v_mfma_f32_16x16x32_bf16 v[12:15], a[108:111], v[92:95], v[12:15]\n\t"
      "s_waitcnt lgkmcnt(7)\n\t"
      "v_mfma_f32_16x16x32_bf16 v[0:3], a[48:51], v[96:99], v[0:3]\n\t"
      "v_mfma_f32_16x16x32_bf16 v[4:7], a[112:115], v[96:99], v[4:7]\n\t"
      "s_waitcnt lgkmcnt(6)\n\t"
      "v_mfma_f32_16x16x32_bf16 v[8:11], a[48:51], v[100:103], v[8:11]\n\t"
      "v_mfma_f32_16x16x32_bf16 v[12:15], a[112:115], v[100:103], v[12:15]\n\t"
      "s_waitcnt lgkmcnt(5)\n\t"
      "v_mfma_f32_16x16x32_bf16 v[0:3], a[52:55], v[104:107], v[0:3]\n\t"
      "v_mfma_f32_16x16x32_bf16 v[4:7], a[116:119], v[104:107], v[4:7]\n\t"
      "s_waitcnt lgkmcnt(4)\n\t"
      "v_mfma_f32_16x16x32_bf16 v[8:11], a[52:55], v[108:111], v[8:11]\n\t"
      "v_mfma_f32_16x16x32_bf16 v[12:15], a[116:119], v[108:111], v[12:15]\n\t"
      "s_waitcnt lgkmcnt(3)\n\t"
      "v_mfma_f32_16x16x32_bf16 v[0:3], a[56:59], v[112:115], v[0:3]\n\t"
      "v_mfma_f32_16x16x32_bf16 v[4:7], a[120:123], v[112:115], v[4:7]\n\t"
      "s_waitcnt lgkmcnt(2)\n\t"
      "v_mfma_f32_16x16x32_bf16 v[8:11], a[56:59], v[116:119], v[8:11]\n\t"
      "v_mfma_f32_16x16x32_bf16 v[12:15], a[120:123], v[116:119], v[12:15]\n\t"
      "s_waitcnt lgkmcnt(1)\n\t"
      "v_mfma_f32_16x16x32_bf16 v[0:3], a[60:63], v[120:123], v[0:3]\n\t"
      "v_mfma_f32_16x16x32_bf16 v[4:7], a[124:127], v[120:123], v[4:7]\n\t"
      "s_waitcnt lgkmcnt(0)\n\t"
      "v_mfma_f32_16x16x32_bf16 v[8:11], a[60:63], v[124:127], v[8:11]\n\t"
      "v_mfma_f32_16x16x32_bf16 v[12:15], a[124:127], v[124:127], v[12:15]\n\t"
      "v_xor_b32 v254, 0x8000, v254\n\t"
      "v_xor_b32 v255, 0x8000, v255\n\t"
      "s_add_u32 s69, s53, 0x4000\n\t"
      "s_add_u32 s48, s48, 4\n\t"
      "s_waitcnt vmcnt(40)\n\t"
      "v_cvt_pk_bf16_f32 v0, v0, v1\n\t"
      "v_cvt_pk_bf16_f32 v1, v2, v3\n\t"
      "v_cvt_pk_bf16_f32 v2, v4, v5\n\t"
      "v_cvt_pk_bf16_f32 v3, v6, v7\n\t"
      "v_min_u32 v0, -2, v0\n\t"
      "v_min_u32 v1, -2, v1\n\t"
      "v_min_u32 v2, -2, v2\n\t"
      "v_min_u32 v3, -2, v3\n\t"
      "buffer_store_dwordx4 v[0:3], v238, s[36:39], s53 offen sc1\n\t"
      "v_cvt_pk_bf16_f32 v8, v8, v9\n\t"
      "v_cvt_pk_bf16_f32 v9, v10, v11\n\t"
      "v_cvt_pk_bf16_f32 v10, v12, v13\n\t"
      "v_cvt_pk_bf16_f32 v11, v14, v15\n\t"
      "v_min_u32 v8, -2, v8\n\t"
      "v_min_u32 v9, -2, v9\n\t"
      "v_min_u32 v10, -2, v10\n\t"
      "v_min_u32 v11, -2, v11\n\t"
      "buffer_store_dwordx4 v[8:11], v238, s[36:39], s69 offen sc1\n\t"
      "s_add_u32 s53, s53, 1048576\n\t"
      "s_nop 1\n\t"
      "s_branch WTOP%=\n\t"
      "WEND%=:\n\t"
      : "+{v[0:15]}"(c0), "+{v[16:31]}"(c1), "+{v[240:255]}"(c2), "+{v238}"(c3), "+{v[32:39]}"(c4), "+s"(w), "+s"(wv), "+s"(A),
        "+{a[0:31]}"(wo.a), "+{a[32:63]}"(wo.b), "+{a[64:95]}"(wo.c), "+{a[96:127]}"(wo.d)
      :
      : "memory", "vcc", "scc", "v40", "v41", "v42", "v43", "v44", "v45", "v46", "v47", "v48", "v49", "v50", "v51", "v52", "v53", "v54", "v55", "v56", "v57", "v58", "v59", "v60", "v61", "v62", "v63", "v64", "v65", "v66", "v67", "v68", "v69", "v70", "v71", "v72", "v73", "v74", "v75", "v76", "v77", "v78", "v79", "v80", "v81", "v82", "v83", "v84", "v85", "v86", "v87", "v88", "v89", "v90", "v91", "v92", "v93", "v94", "v95", "v96", "v97", "v98", "v99", "v100", "v101", "v102", "v103", "v104", "v105", "v106", "v107", "v108", "v109", "v110", "v111", "v112", "v113", "v114", "v115", "v116", "v117", "v118", "v119", "v120", "v121", "v122", "v123", "v124", "v125", "v126", "v127", "v128", "v129", "v130", "v131", "v132", "v133", "v134", "v135", "v136", "v137", "v138", "v139", "v140", "v141", "v142", "v143", "v144", "v145", "v146", "v147", "v148", "v149", "v150", "v151", "v152", "v153", "v154", "v155", "v156", "v157", "v158", "v159", "v160", "v161", "v162", "v163", "v164", "v165", "v166", "v167", "v168", "v169", "v170", "v171", "v172", "v173", "v174", "v175", "v176", "v177", "v178", "v179", "v180", "v181", "v182", "v183", "v184", "v185", "v186", "v187", "v188", "v189", "v190", "v191", "v192", "v193", "v194", "v195", "v196", "v197", "v198", "v199", "v200", "v201", "v202", "v203", "v204", "v205", "v206", "v207", "v208", "v209", "v210", "v211", "v212", "v213", "v214", "v215", "v216", "v217", "v218", "v219", "v220", "v221", "v222", "v223", "v224", "v225", "v226", "v227", "v228", "v229", "v230", "v231", "v232", "v233", "v234", "v235", "v236", "v237", "v239", "a128", "a129", "a130", "a131", "a132", "a133", "a134", "a135", "a136", "a137", "a138", "a139", "a140", "a141", "a142", "a143", "a144", "a145", "a146", "a147", "a148", "a149", "a150", "a151", "a152", "a153", "a154", "a155", "a156", "a157", "a158", "a159", "a160", "a161", "a162", "a163", "a164", "a165", "a166", "a167", "a168", "a169", "a170", "a171", "a172", "a173", "a174", "a175", "a176", "a177", "a178", "a179", "a180", "a181", "a182", "a183", "a184", "a185", "a186", "a187", "a188", "a189", "a190", "a191", "a192", "a193", "a194", "a195", "a196", "a197", "a198", "a199", "a200", "a201", "a202", "a203", "a204", "a205", "a206", "a207", "a208", "a209", "a210", "a211", "a212", "a213", "a214", "a215", "a216", "a217", "a218", "a219", "a220", "a221", "a222", "a223", "a224", "a225", "a226", "a227", "a228", "a229", "a230", "a231", "a232", "a233", "a234", "a235", "a236", "a237", "a238", "a239", "a240", "a241", "a242", "a243", "a244", "a245", "a246", "a247", "a248", "a249", "a250", "a251", "a252", "a253", "a254", "a255", "s24", "s36", "s37", "s38", "s39", "s40", "s41", "s42", "s43", "s44", "s45", "s46", "s47", "s48", "s49", "s50", "s51", "s52", "s53", "s54", "s55", "s56", "s57", "s58", "s59", "s60", "s61", "s62", "s63", "s64", "s65", "s66", "s67", "s68", "s69", "s70", "s71", "s72", "s73", "s74", "s75", "s76", "s77", "s78", "s79", "s80", "s81", "s82", "s83", "s84", "s85", "s86", "s87", "s88", "s89", "s90", "s91", "s92", "s93", "s94", "s95", "s96", "s97", "s98", "s99");
}

__global__ void __launch_bounds__(256, 1) mega_kernel(MegaParams P) {
  __shared__ __attribute__((aligned(16))) char lds[LDS_BYTES];
  int b, wv;
  KArgs A = (KArgs)__builtin_amdgcn_kernarg_segment_ptr();
  u32x16 ka; u32x8 kb;
  asm volatile("s_load_dwordx16 s[36:51], %4, 0\n\t"
               "s_load_dwordx8 s[52:59], %4, 64\n\t"
               "s_getpc_b64 s[60:61]\n"
               "KPC:\n\t"
               "v_lshlrev_b32 v254, 7, %6\n\t"
               "global_load_dword v255, v254, s[60:61]\n\t"
               "v_add_u32 v254, 0x8000, v254\n\t"
               "v_min_u32 v254, KENDW-KPC, v254\n\t"
               "global_load_dword v255, v254, s[60:61]\n\t"
               "v_readfirstlane_b32 %1, %6\n\t"
               "s_lshr_b32 %0, %5, 1\n\t"
               "s_andn2_b32 %0, %0, 3\n\t"
               "s_and_b32 s60, %5, 3\n\t"
               "s_or_b32 %0, %0, s60\n\t"
               "s_lshr_b32 %1, %1, 6\n\t"
               "s_bitcmp1_b32 %5, 2\n\t"
               "s_cbranch_scc1 GSTART\n\t"
               "s_waitcnt lgkmcnt(0)"
               : "=&{s34}"(b), "=&{s33}"(wv), "=&{s[36:51]}"(ka), "=&{s[52:59]}"(kb), "+{s[0:1]}"(A)
               : "s"(blockIdx.x), "v"(threadIdx.x) : "v254", "v255", "s60", "s61", "scc");
  static_assert(XS == 0, "");
  if (b >= NREC) {
    int w = b, wv2 = wv;
    WoRegs wo;
    const unsigned tag = worker_role(w, wv2, A, wo);
    worker_tasks(lds, w, wv2, A, tag, wo);
    if (w == 0 && wv2 == 0) {
      const int lane = fresh_lane();
#pragma unroll 1
      for (unsigned spins = 0; spins < (1u << 16); ++spins) {
        const u32x2 sel = top2_wave((const float*)(A->ws + O_RBUF) + S_, lane, spins == 0, spins < 8);
        if (spins && sel[1]) {
          if (lane < 8) __hip_atomic_store((gu64*)(A->ws + O_CNT + lane * 64), (u64)sel[0] | ((u64)sel[1] << 32), RLX_AGENT);
          break;
        }
        __builtin_amdgcn_s_sleep(SSLEEP);
        if (spins == 0 && SWAIT) {
          unsigned f = 0;
#pragma unroll 1
          for (unsigned n = 0; n < 4096; ++n) {
            f = __builtin_amdgcn_readfirstlane(__hip_atomic_load((gu32*)(A->ws + O_KVSTEP), RLX_AGENT));
            if ((f >> 16) >= 61) break;
            __builtin_amdgcn_s_sleep(4);
          }
          f += SWAIT - ((f >> 16) - 61) * 156;
#pragma unroll 1
          while (((f - (unsigned)__builtin_amdgcn_s_memrealtime()) & 0xffffu) < 400u) __builtin_amdgcn_s_sleep(1);
        }
      }
    }
    __syncthreads();
    if ((w & 2) == 0) mlp2_role(A, lds, (w & 1) * 32 + (w >> 2), wv2 * 64 + fresh_lane());
    asm volatile("KENDW:");
    __builtin_amdgcn_endpgm();
    __builtin_unreachable();
  } else {
    KArgs kp = A;
    rec_role(lds, kp, b, wv, ka, kb);
    const int tid = wv * 64 + fresh_lane();
    __syncthreads();
    if ((b & 31) < M1_WGS / 4) mlp1_role(kp, lds, (b & 31) * 4 + (b >> 5), tid);
    else if (SW2 && b == M1_WGS / 4 + 1) {
      if (wv == 0) {
        const unsigned t0 = (unsigned)__builtin_amdgcn_s_memrealtime();
#pragma unroll 1
        for (unsigned spins = 0; spins < (1u << 16); ++spins) {
          const u32x2 sel = top2_wave((const float*)(kp->ws + O_RBUF) + S_, tid, spins == 0, false);
          if (spins && sel[1]) {
            if (tid < 32) __hip_atomic_store((gu64*)(kp->ws + O_SEL2 + tid * 128), (u64)sel[0] | ((u64)sel[1] << 32), RLX_AGENT);
            break;
          }
#pragma unroll 1
          while ((unsigned)__builtin_amdgcn_s_memrealtime() - t0 < SW2) __builtin_amdgcn_s_sleep(1);
        }
      }
    }
    else if (b == M1_WGS / 4) {
      if (tid == 64) {
        gu32* e = (gu32*)(kp->ws + O_EPOCH);
        __hip_atomic_store(e, mktag(__hip_atomic_load(e, RLX_AGENT)), RLX_AGENT);
      }
    }
    __builtin_amdgcn_endpgm();
    __builtin_unreachable();
  }
}

extern "C" void kernel_launch(void* const* d_in, const int* in_sizes, int n_in, void* d_out, int out_size, void* d_ws, size_t ws_size,
                              hipStream_t stream) {
  MegaParams P;
  P.x = (const u16*)d_in[0]; P.mem0 = (const u16*)d_in[1];
  P.Wq = (const u16*)d_in[2]; P.Wk = (const u16*)d_in[3]; P.Wv = (const u16*)d_in[4]; P.Wo = (const u16*)d_in[5];
  P.w_r = (const u16*)d_in[8]; P.wg = (const u16*)d_in[9]; P.wu = (const u16*)d_in[10]; P.wd = (const u16*)d_in[11];
  P.out = (u16*)d_out; P.ws = (char*)d_ws;
  mega_kernel<<<256, 256, 0, stream>>>(P);
}
```

```cpp
#include <hip/hip_runtime.h>
#include <hip/hip_bf16.h>
#define SWAIT 0
#define SW2 0
#define ASLEEP 32
#define ASALL 0
#define PSLEEP 8
#define FSLEEP 6
#define RDLY 250
#define RDL2 100
#define ADLY 130
#define CPF 1
#define TCH 0

typedef unsigned short u16;
typedef unsigned long long u64;
typedef __attribute__((ext_vector_type(8))) short bf16x8;
typedef __attribute__((ext_vector_type(4))) float f32x4;
typedef __attribute__((ext_vector_type(4))) unsigned u32x4;
#define GAS __attribute__((address_space(1)))
typedef GAS u64 gu64;
typedef GAS unsigned gu32;
#define RLX_AGENT __ATOMIC_RELAXED, __HIP_MEMORY_SCOPE_AGENT

constexpr int S_ = 2048, DM = 512, NH = 32, DH = 128, MM = 8, SEG = 32, NSEG = 64;
constexpr int HD = NH * DH;
constexpr int QKVN = 3 * HD;
constexpr int FF = 1792;

__device__ __forceinline__ float bf2f(u16 v) { return __builtin_bit_cast(float, (unsigned)v << 16); }
__device__ __forceinline__ u16 f2bf(float f) { unsigned u = __builtin_bit_cast(unsigned, f); return (u16)((u + 0x7fffu + ((u >> 16) & 1u)) >> 16); }
__device__ __forceinline__ float rbf(float f) { return bf2f(f2bf(f)); }

typedef __attribute__((ext_vector_type(2))) float f32x2;
typedef __attribute__((ext_vector_type(2))) __bf16 bf16x2_t;
typedef __attribute__((ext_vector_type(2))) unsigned u32x2;
__device__ __forceinline__ unsigned pk2(float a, float b) { return __builtin_bit_cast(unsigned, __builtin_convertvector((f32x2){a, b}, bf16x2_t)); }
__device__ __forceinline__ unsigned f2u(float f) { return __builtin_bit_cast(unsigned, f); }
__device__ __forceinline__ float u2f(unsigned u) { return __builtin_bit_cast(float, u); }
__device__ __forceinline__ float lo2f(unsigned u) { return __builtin_bit_cast(float, u << 16); }
__device__ __forceinline__ float hi2f(unsigned u) { return __builtin_bit_cast(float, u & 0xffff0000u); }
__device__ __forceinline__ void unpack8(const u32x4 v, float (&f)[8]) {
#pragma unroll
  for (int i = 0; i < 4; ++i) { f[2 * i] = lo2f(v[i]); f[2 * i + 1] = hi2f(v[i]); }
}

constexpr unsigned O_EPOCH = 0;
constexpr unsigned O_TFLAG = 256;
constexpr unsigned O_MFLAG = O_TFLAG + 16 * 32 * 64;
constexpr unsigned O_LFLAG = O_MFLAG + 32 * 128;
constexpr unsigned O_WM = O_LFLAG + 128 * 128;
constexpr unsigned O_KVSTEP = O_WM + 128 * 128;
constexpr unsigned O_CNT = O_KVSTEP + 2048;
constexpr unsigned O_PFLAG = O_CNT + 2048;
constexpr unsigned O_SEL2 = O_PFLAG + 4 * 128 * 128;
constexpr unsigned O_ACT = O_SEL2 + 32 * 128;
constexpr unsigned O_RBUF = O_ACT + 3 * 112 * 128;
constexpr unsigned O_INIT_END = O_RBUF + 2 * S_ * 4 + 256 * 128;
constexpr unsigned O_STAMPS = 196608 + 65 * 32768;
constexpr unsigned O_XN = O_STAMPS + (4 << 20);
constexpr unsigned O_QKV = O_XN + S_ * DM * 2;
constexpr unsigned O_KVM = O_QKV + S_ * QKVN * 2;
constexpr unsigned O_VTM = O_KVM + NSEG * 8 * HD * 2;
constexpr unsigned O_PART = O_VTM + NSEG * 8 * HD * 2;
constexpr unsigned O_HBUF = O_PART + S_ * 8 * DM * 4;
constexpr unsigned O_LNH = O_HBUF + S_ * DM * 2;
constexpr unsigned O_MFRAG = O_LNH + S_ * DM * 2;
constexpr unsigned O_SLOTS = (O_MFRAG + 2 * DM * FF * 2 + 0x1fffff) & ~0x1fffffu;
constexpr unsigned WS_BYTES = O_SLOTS + 65 * 32768;
#define QO(i) (((i) & 1) * 16384 + ((i) >> 1) * 8192)
#define CAL(b) (O_SLOTS + 128 + ((b) >> 3) * 16384 + ((b) & 7) * 1024)
static_assert(O_INIT_END <= O_STAMPS && (O_INIT_END - O_KVSTEP) / 16 <= 128 * 128, "layout");
static_assert(O_TFLAG == 256 && O_MFLAG == 33024 && O_LFLAG == 37120 && O_STAMPS == 2326528 && O_XN == 6520832 && O_QKV == 8617984 && O_KVM == 58949632 && O_VTM == 63143936 && O_PART == 67338240 && O_MFRAG == 105086976 && O_SLOTS == 109051904 && O_CNT + 1024 == 72960 && O_INIT_END <= 262144 && O_WM == 53504, "layout");
constexpr int L_BS = 520, L_QN = 49152, L_TB = 98304, L_VT = 32768;
struct MegaParams {
  const u16* x; const u16* mem0; const u16* Wq; const u16* Wk; const u16* Wv; const u16* Wo;
  const u16* w_r; const u16* wg; const u16* wu; const u16* wd;
  u16* out;
  char* ws;
};
__device__ __forceinline__ unsigned mktag(unsigned e) { return e * 0x9e3779b1u + 0x7f4a7c15u; }

typedef __attribute__((ext_vector_type(4))) short s16x4;
#define LAS __attribute__((address_space(3)))
constexpr int LDS_BYTES = 163840;
#define SB() __builtin_amdgcn_sched_barrier(0)
#define LDS_BARRIER() asm volatile("s_waitcnt lgkmcnt(0)\n\ts_barrier" ::: "memory")
#define VM_DRAIN() asm volatile("s_waitcnt vmcnt(0)" ::: "memory")
__device__ __forceinline__ float xmax16_32(float m) {
  u32x2 p = __builtin_amdgcn_permlane16_swap(f2u(m), f2u(m), false, false);
  m = fmaxf(u2f(p[0]), u2f(p[1]));
  p = __builtin_amdgcn_permlane32_swap(f2u(m), f2u(m), false, false);
  return fmaxf(u2f(p[0]), u2f(p[1]));
}
__device__ __forceinline__ float xsum16_32(float m) {
  u32x2 p = __builtin_amdgcn_permlane16_swap(f2u(m), f2u(m), false, false);
  m = u2f(p[0]) + u2f(p[1]);
  p = __builtin_amdgcn_permlane32_swap(f2u(m), f2u(m), false, false);
  return u2f(p[0]) + u2f(p[1]);
}
__device__ __forceinline__ unsigned wave_max_u32(unsigned v) {
  v = max(v, (unsigned)__builtin_amdgcn_mov_dpp(v, 0xb1, 0xf, 0xf, true));
  v = max(v, (unsigned)__builtin_amdgcn_mov_dpp(v, 0x4e, 0xf, 0xf, true));
  v = max(v, (unsigned)__builtin_amdgcn_mov_dpp(v, 0x124, 0xf, 0xf, true));
  v = max(v, (unsigned)__builtin_amdgcn_mov_dpp(v, 0x128, 0xf, 0xf, true));
  u32x2 p = __builtin_amdgcn_permlane16_swap(v, v, false, false);
  v = max(p[0], p[1]);
  p = __builtin_amdgcn_permlane32_swap(v, v, false, false);
  return max(p[0], p[1]);
}
__device__ __forceinline__ u32x4 ld_sc1(__amdgpu_buffer_rsrc_t rs, unsigned off) { return __builtin_amdgcn_raw_buffer_load_b128(rs, off, 0, 16); }
__device__ __forceinline__ u32x2 ld8_sc1(__amdgpu_buffer_rsrc_t rs, unsigned off) { return __builtin_amdgcn_raw_buffer_load_b64(rs, off, 0, 16); }

#define XS 0
#define VBX __builtin_amdgcn_readfirstlane((int)(((blockIdx.x & 4) << 5) + (blockIdx.x >> 3) * 4 + (blockIdx.x & 3)))
constexpr int NREC = 128, NWORK = 128;
typedef __attribute__((ext_vector_type(8))) unsigned u32x8;
typedef __attribute__((ext_vector_type(16))) unsigned u32x16;
typedef __attribute__((ext_vector_type(32))) unsigned u32x32;
#define RSRC(p, n) __builtin_amdgcn_make_buffer_rsrc((void*)(p), 0, (n), 0x00020000)

__device__ __forceinline__ int fresh_lane() {
  int l;
  asm volatile("v_mbcnt_lo_u32_b32 %0, -1, 0\n\tv_mbcnt_hi_u32_b32 %0, -1, %0" : "=v"(l));
  return l;
}
typedef const __attribute__((address_space(4))) MegaParams* KArgs;
#define ARGS() KArgs A = (KArgs)__builtin_amdgcn_kernarg_segment_ptr(); asm volatile("" : "+s"(A))
#define MS(k)
__device__ __forceinline__ void rec_role(char* lds, KArgs& A, int& b, int& wv, const u32x16 ka, const u32x8 kb) {
#define HMAP 0
  const int h = HMAP == 1 ? (b & 3) * 8 + ((b >> 2) & 7) : (HMAP == 2 ? (b & 1) * 16 + (b & 2) / 2 + ((b >> 2) & 7) * 2 : b & 31), part = b >> 5;
  const int lane = fresh_lane(), l15 = lane & 15, l4 = lane >> 4;
  const int b1 = (l15 >> 1) & 1, mrow = 2 * (l15 >> 2) + (l15 & 1);
  const int r4 = l15 >> 2, c4 = l15 & 3;
#define KA(v, i) ((u64)v[i] | (u64)v[(i) + 1] << 32)
  const u64 wsa = KA(kb, 6), woa = KA(ka, 10), wva = KA(ka, 8), wqa = KA(ka, 4), wka = KA(ka, 6), m0a = KA(ka, 2);
  const unsigned awv = (unsigned)((8 * wv + l4) * 8192 + h * 256 + ((l15 ^ (l4 | (wv << 2))) << 4)), rot = 1 ? part : 0;
  const int sig = 1 ? (h >> 2) & 1 : 0;
  unsigned tch;
  asm volatile(
      "s_lshl_b32 s91, %5, 4\n\t"
      "s_add_u32 m0, %5, 0\n\t"
      "s_xor_b32 s90, s91, 0\n\t"
      "buffer_load_dwordx4 %1, %3, s90 offen lds\n\t"
      "s_add_u32 m0, %5, 1024\n\t"
      "s_xor_b32 s90, s91, 16384\n\t"
      "buffer_load_dwordx4 %1, %3, s90 offen lds\n\t"
      "s_add_u32 m0, %5, 8192\n\t"
      "s_xor_b32 s90, s91, 32768\n\t"
      "buffer_load_dwordx4 %1, %3, s90 offen lds\n\t"
      "s_add_u32 m0, %5, 9216\n\t"
      "s_xor_b32 s90, s91, 49152\n\t"
      "buffer_load_dwordx4 %1, %3, s90 offen lds\n\t"
      "s_add_u32 m0, %5, 16384\n\t"
      "s_xor_b32 s90, s91, 65536\n\t"
      "buffer_load_dwordx4 %1, %3, s90 offen lds\n\t"
      "s_add_u32 m0, %5, 17408\n\t"
      "s_xor_b32 s90, s91, 81920\n\t"
      "buffer_load_dwordx4 %1, %3, s90 offen lds\n\t"
      "s_add_u32 m0, %5, 24576\n\t"
      "s_xor_b32 s90, s91, 98304\n\t"
      "buffer_load_dwordx4 %1, %3, s90 offen lds\n\t"
      "s_add_u32 m0, %5, 25600\n\t"
      "s_xor_b32 s90, s91, 114688\n\t"
      "buffer_load_dwordx4 %1, %3, s90 offen lds\n\t"
      : "=&{s87}"(tch)
      : "v"((unsigned)((h * DH + (lane >> 2)) * DM + 128 * part + 32 * wv + 8 * (2 * (lane & 1) + (((lane >> 1) & 1) ^ sig))) * 2), "v"(awv), "s"(RSRC(woa, DM * HD * 2)), "s"(RSRC(wva, DM * HD * 2)),
        "s"(wv * 2048), "s"(rot << 19), "s"(wva), "s"(wqa), "s"(wka), "s"(wsa), "s"(m0a)
      : "memory", "scc", "s90", "s91", "s92");
  SB();
  const unsigned i0 = 1024 + b * 512 + wv * 64 + lane, o1 = O_KVSTEP + ((wv >> 1) * 8192 + b * 64 + lane) * 16;
  const unsigned oz = wv == 2 ? CAL(b ^ 1) + (lane >> 5) * 8192 + ((lane >> 3) & 3) * 256 + (lane & 7) * 16 : ((wv & 1) && o1 < O_INIT_END ? o1 : 0xffffff00u);
  const int sl = lane ^ ((l4 & 1) << 1), su = ((l15 >> 3) * 16 + c4 * 4 + l4) ^ ((l15 >> 3) << 1);
  const int jt = wv ^ rot;
  u32x8 cc = {oz, (unsigned)lane, (unsigned)lane,
              wv == 2 || o1 < O_ACT ? 0u : ~0u,
              (unsigned)(QO(jt >> 1) + (((jt & 1) * 2 + (l4 >> 1)) * 8 + (r4 & 1) * 2 + (l15 >> 3)) * 256 + (l15 & 1) * 64 + (2 * b1 + (l4 & 1)) * 16),
              (unsigned)(h * 2048 + (2 * (part & 1) + (l4 >> 1)) * 512 + (2 * b1 + (l4 & 1)) * 128 + mrow * 16),
              (unsigned)(h * 2048 + part * 512 + (l4 >> 1) * 256 + (8 * wv + 4 * (l15 >> 3) + c4) * 16 + (r4 & 1) * 8 + (l4 & 1) * 4),
              (unsigned)((2 * (wv >> 1) + (l4 >> 1)) * 2048 + ((2 * (wv & 1) + (l4 & 1)) * 16 + l15) * 16)};
  u32x8 c3 = {(unsigned)lane, O_SLOTS + (i0 >> 3) * 256 + (i0 & 7) * 16,
              (unsigned)(wv * 2048 + (8 * l4 + r4) * 64 + c4 * 16),
              (unsigned)(wv * 2048 + (r4 >> 1) * 1024 + (l4 * 16 + 8 * (c4 >> 1) + 4 * (r4 & 1) + (c4 & 1)) * 16),
              (unsigned)(L_VT + (8 * r4 + c4) * 256 + (((4 * wv + l4) ^ l15) << 4)),
              awv,
              (unsigned)(L_VT + ((wv & 1) * 32 + 8 * l4 + r4) * 256 + (((4 * part + 2 * (wv >> 1) + (c4 >> 1)) ^ (r4 | (l4 << 2))) << 4) + (c4 & 1) * 8),
              (unsigned)(QO(part) + h * 256 + (lane & 7) * 16)};
  const int L = (h & 7) * 8 + (lane & 7);
  const u64 mp = (u64)((const u16*)m0a + (2 * ((L >> 2) & 3) + ((L >> 4) & 1)) * DM + 128 * part + 32 * (h >> 3) + 8 * (L >> 5) + 16 * (L & 1) + (L & 2));
  u32x8 c0 = {(unsigned)((8 * wv + l4) * 8192 + h * 256 + ((l15 ^ (2 * l4 + 8 * (wv & 1))) << 4)),
              (unsigned)(L_TB + (8 * l4 + r4) * 256 + (((4 * wv + (c4 >> 1)) ^ (2 * r4 + 8 * (l4 & 1))) << 4) + (c4 & 1) * 8),
              (unsigned)mp, (unsigned)(mp >> 32), (unsigned)(CAL(b) + wv * 256 + ((wv ^ (b >> 2)) & 1) * 8192) & (unsigned)((b - 32) >> 31) & (unsigned)((1 - wv) >> 31), (unsigned)(QO(part) + sig * 1024),
              b1 ? 0xEEEEu : 0x4444u, (unsigned)((wv * 8 + (lane >> 5)) * 256 + (lane & 31) * 4)};
  u32x4 c1 = {(unsigned)(sl * 8),
                    (unsigned)(L_QN + ((wv >> 1) * 2 + (r4 & 1)) * L_BS + ((wv & 1) * 32 + su) * 8),
                    (unsigned)lane, (unsigned)lane * 16};
  const unsigned k0 = O_QKV + h * 24576, v0 = O_QKV + 8192 + SEG * QKVN * 2 + h * 24576 + wv * 2048;
  const unsigned role = wv < 2 ? wv : (wv == 3 ? 4 : (part < 2 ? 2 + part : 5));
  u32x16 sc = {(unsigned)lane, (unsigned)wv, role, O_SLOTS, k0, O_MFRAG + (min(h, 27) * 4 + part) * 32768 + wv * 4096, v0, rot << 19, O_KVM, O_VTM,
                     O_TFLAG + h * 64, O_KVSTEP + h * 64 + part * 4, O_STAMPS + (b * 4 + wv) * 65 * 64,
                     O_EPOCH, O_MFLAG + h * 128, (unsigned)wv * 2048};
  u32x16 s2 = {(unsigned)woa, (unsigned)(woa >> 32) & 0xffffu, DM * HD * 2, 0x00020000u, (unsigned)wva, (unsigned)(wva >> 32) & 0xffffu, O_WM, O_WM + b * 128,
               (unsigned)wsa, (unsigned)(wsa >> 32) & 0xffffu, WS_BYTES, 0x00020000u,
               (unsigned)wqa, (unsigned)(wqa >> 32) & 0xffffu, (unsigned)wka, (unsigned)(wka >> 32) & 0xffffu};
  asm volatile(
      "s_nop 1\n\t"
      "v_readfirstlane_b32 s40, v0\n\t"
      "v_readfirstlane_b32 s41, v1\n\t"
      "v_readfirstlane_b32 s42, v2\n\t"
      "v_readfirstlane_b32 s43, v3\n\t"
      "v_readfirstlane_b32 s44, v4\n\t"
      "v_readfirstlane_b32 s45, v5\n\t"
      "v_readfirstlane_b32 s46, v6\n\t"
      "v_readfirstlane_b32 s47, v7\n\t"
      "v_readfirstlane_b32 s48, v8\n\t"
      "v_readfirstlane_b32 s49, v9\n\t"
      "v_readfirstlane_b32 s50, v10\n\t"
      "v_readfirstlane_b32 s51, v11\n\t"
      "v_readfirstlane_b32 s52, v12\n\t"
      "v_readfirstlane_b32 s53, v13\n\t"
      "v_readfirstlane_b32 s54, v14\n\t"
      "v_readfirstlane_b32 s55, v15\n\t"
      "v_readfirstlane_b32 s88, v16\n\t"
      "v_readfirstlane_b32 s89, v17\n\t"
      "v_readfirstlane_b32 s90, v18\n\t"
      "v_readfirstlane_b32 s91, v19\n\t"
      "v_readfirstlane_b32 s92, v20\n\t"
      "v_readfirstlane_b32 s93, v21\n\t"
      "v_readfirstlane_b32 s94, v22\n\t"
      "v_readfirstlane_b32 s95, v23\n\t"
      "v_readfirstlane_b32 s36, v24\n\t"
      "v_readfirstlane_b32 s37, v25\n\t"
      "v_readfirstlane_b32 s38, v26\n\t"
      "v_readfirstlane_b32 s39, v27\n\t"
      "v_readfirstlane_b32 s96, v28\n\t"
      "v_readfirstlane_b32 s97, v29\n\t"
      "v_readfirstlane_b32 s98, v30\n\t"
      "v_readfirstlane_b32 s99, v31\n\t"
      "s_nop 4\n\t"
      "v_readfirstlane_b32 s86, v120\n\t"
      "v_add_u32 v100, s55, v127\n\t"
      "s_mov_b64 s[88:89], s[92:93]\n\t"
      "s_add_u32 m0, s55, 32768\n\t"
      "s_xor_b32 s61, s47, 0\n\t"
      "buffer_load_dwordx4 v105, s[88:91], s61 offen lds\n\t"
      "s_add_u32 m0, s55, 33792\n\t"
      "s_xor_b32 s61, s47, 32768\n\t"
      "buffer_load_dwordx4 v105, s[88:91], s61 offen lds\n\t"
      "s_add_u32 m0, s55, 40960\n\t"
      "s_xor_b32 s61, s47, 262144\n\t"
      "buffer_load_dwordx4 v105, s[88:91], s61 offen lds\n\t"
      "s_add_u32 m0, s55, 41984\n\t"
      "s_xor_b32 s61, s47, 294912\n\t"
      "buffer_load_dwordx4 v105, s[88:91], s61 offen lds\n\t"
      "s_add_u32 m0, s55, 49152\n\t"
      "s_xor_b32 s61, s47, 524288\n\t"
      "buffer_load_dwordx4 v105, s[88:91], s61 offen lds\n\t"
      "s_add_u32 m0, s55, 50176\n\t"
      "s_xor_b32 s61, s47, 557056\n\t"
      "buffer_load_dwordx4 v105, s[88:91], s61 offen lds\n\t"
      "s_add_u32 m0, s55, 57344\n\t"
      "s_xor_b32 s61, s47, 786432\n\t"
      "buffer_load_dwordx4 v105, s[88:91], s61 offen lds\n\t"
      "s_add_u32 m0, s55, 58368\n\t"
      "s_xor_b32 s61, s47, 819200\n\t"
      "buffer_load_dwordx4 v105, s[88:91], s61 offen lds\n\t"
      "buffer_load_dword v126, off, s[36:39], s53 sc1\n\t"
      "global_load_ushort v96, v[118:119], off offset:0\n\t"
      "global_load_ushort v97, v[118:119], off offset:2\n\t"
      "global_load_ushort v98, v[118:119], off offset:8\n\t"
      "global_load_ushort v99, v[118:119], off offset:10\n\t"
      "s_xor_b32 s61, s86, 0x2000\n\t"
      "s_buffer_load_dword s87, s[36:39], s61\n\t"
      "s_waitcnt vmcnt(13)\n\t"
      "s_waitcnt lgkmcnt(0)\n\t"
      "ds_read_b64_tr_b16 v[64:65], v102\n\t"
      "ds_read_b64_tr_b16 v[66:67], v102 offset:256\n\t"
      "ds_read_b64_tr_b16 v[68:69], v102 offset:8\n\t"
      "ds_read_b64_tr_b16 v[70:71], v102 offset:264\n\t"
      "ds_read_b64_tr_b16 v[72:73], v102 offset:8192\n\t"
      "ds_read_b64_tr_b16 v[74:75], v102 offset:8448\n\t"
      "ds_read_b64_tr_b16 v[76:77], v102 offset:8200\n\t"
      "ds_read_b64_tr_b16 v[78:79], v102 offset:8456\n\t"
      "ds_read_b64_tr_b16 v[80:81], v102 offset:16384\n\t"
      "ds_read_b64_tr_b16 v[82:83], v102 offset:16640\n\t"
      "ds_read_b64_tr_b16 v[84:85], v102 offset:16392\n\t"
      "ds_read_b64_tr_b16 v[86:87], v102 offset:16648\n\t"
      "s_waitcnt lgkmcnt(4)\n\t"
      "ds_read_b64_tr_b16 v[88:89], v102 offset:24576\n\t"
      "ds_read_b64_tr_b16 v[90:91], v102 offset:24832\n\t"
      "ds_read_b64_tr_b16 v[92:93], v102 offset:24584\n\t"
      "ds_read_b64_tr_b16 v[94:95], v102 offset:24840\n\t"
      "s_mov_b64 s[88:89], s[96:97]\n\t"
      "s_add_u32 m0, s55, 98304\n\t"
      "s_xor_b32 s61, s47, 0\n\t"
      "buffer_load_dwordx4 v116, s[88:91], s61 offen lds\n\t"
      "s_add_u32 m0, s55, 99328\n\t"
      "s_xor_b32 s61, s47, 32768\n\t"
      "buffer_load_dwordx4 v116, s[88:91], s61 offen lds\n\t"
      "s_add_u32 m0, s55, 106496\n\t"
      "s_xor_b32 s61, s47, 262144\n\t"
      "buffer_load_dwordx4 v116, s[88:91], s61 offen lds\n\t"
      "s_add_u32 m0, s55, 107520\n\t"
      "s_xor_b32 s61, s47, 294912\n\t"
      "buffer_load_dwordx4 v116, s[88:91], s61 offen lds\n\t"
      "s_waitcnt lgkmcnt(0)\n\t"
      "ds_write_b128 v103, v[64:67]\n\t"
      "ds_write_b128 v103, v[68:71] offset:32\n\t"
      "ds_write_b128 v103, v[72:75] offset:8192\n\t"
      "ds_write_b128 v103, v[76:79] offset:8224\n\t"
      "ds_write_b128 v103, v[80:83] offset:16384\n\t"
      "ds_write_b128 v103, v[84:87] offset:16416\n\t"
      "ds_write_b128 v103, v[88:91] offset:24576\n\t"
      "ds_write_b128 v103, v[92:95] offset:24608\n\t"
      "s_waitcnt lgkmcnt(4)\n\t"
      "ds_read_b128 v[32:35], v100\n\t"
      "ds_read_b128 v[36:39], v100 offset:1024\n\t"
      "ds_read_b128 v[40:43], v100 offset:8192\n\t"
      "ds_read_b128 v[44:47], v100 offset:9216\n\t"
      "ds_read_b128 v[48:51], v100 offset:16384\n\t"
      "ds_read_b128 v[52:55], v100 offset:17408\n\t"
      "ds_read_b128 v[56:59], v100 offset:24576\n\t"
      "ds_read_b128 v[60:63], v100 offset:25600\n\t"
      "v_xor_b32 v118, 32, v117\n\t"
      "v_xor_b32 v119, 64, v104\n\t"
      "v_xor_b32 v102, 128, v104\n\t"
      "v_xor_b32 v103, 192, v104\n\t"
      "s_mov_b64 s[88:89], s[92:93]\n\t"
      "s_add_u32 m0, s55, 65536\n\t"
      "s_xor_b32 s61, s47, 1048576\n\t"
      "buffer_load_dwordx4 v105, s[88:91], s61 offen lds\n\t"
      "s_add_u32 m0, s55, 66560\n\t"
      "s_xor_b32 s61, s47, 1081344\n\t"
      "buffer_load_dwordx4 v105, s[88:91], s61 offen lds\n\t"
      "s_mov_b64 s[88:89], s[96:97]\n\t"
      "s_add_u32 m0, s55, 114688\n\t"
      "s_xor_b32 s61, s47, 524288\n\t"
      "buffer_load_dwordx4 v116, s[88:91], s61 offen lds\n\t"
      "s_add_u32 m0, s55, 115712\n\t"
      "s_xor_b32 s61, s47, 557056\n\t"
      "buffer_load_dwordx4 v116, s[88:91], s61 offen lds\n\t"
      "s_add_u32 m0, s55, 122880\n\t"
      "s_xor_b32 s61, s47, 786432\n\t"
      "buffer_load_dwordx4 v116, s[88:91], s61 offen lds\n\t"
      "s_add_u32 m0, s55, 123904\n\t"
      "s_xor_b32 s61, s47, 819200\n\t"
      "buffer_load_dwordx4 v116, s[88:91], s61 offen lds\n\t"
      "s_waitcnt lgkmcnt(0)\n\t"
      "s_waitcnt vmcnt(21)\n\t"
      "s_barrier\n\t"
      "ds_read_b128 v[0:3], v104\n\t"
      "ds_read_b128 v[4:7], v119\n\t"
      "ds_read_b128 v[8:11], v102\n\t"
      "ds_read_b128 v[12:15], v103\n\t"
      "ds_read_b128 v[64:67], v104 offset:1024\n\t"
      "ds_read_b128 v[68:71], v119 offset:1024\n\t"
      "ds_read_b128 v[72:75], v102 offset:1024\n\t"
      "ds_read_b128 v[76:79], v103 offset:1024\n\t"
      "s_mov_b64 s[88:89], s[92:93]\n\t"
      "s_add_u32 m0, s55, 73728\n\t"
      "s_xor_b32 s61, s47, 1310720\n\t"
      "buffer_load_dwordx4 v105, s[88:91], s61 offen lds\n\t"
      "s_add_u32 m0, s55, 74752\n\t"
      "s_xor_b32 s61, s47, 1343488\n\t"
      "buffer_load_dwordx4 v105, s[88:91], s61 offen lds\n\t"
      "s_mov_b64 s[88:89], s[96:97]\n\t"
      "s_add_u32 m0, s55, 131072\n\t"
      "s_xor_b32 s61, s47, 1048576\n\t"
      "buffer_load_dwordx4 v116, s[88:91], s61 offen lds\n\t"
      "s_add_u32 m0, s55, 132096\n\t"
      "s_xor_b32 s61, s47, 1081344\n\t"
      "buffer_load_dwordx4 v116, s[88:91], s61 offen lds\n\t"
      "s_add_u32 m0, s55, 139264\n\t"
      "s_xor_b32 s61, s47, 1310720\n\t"
      "buffer_load_dwordx4 v116, s[88:91], s61 offen lds\n\t"
      "s_add_u32 m0, s55, 140288\n\t"
      "s_xor_b32 s61, s47, 1343488\n\t"
      "buffer_load_dwordx4 v116, s[88:91], s61 offen lds\n\t"
      "s_mov_b64 s[88:89], s[92:93]\n\t"
      "s_add_u32 m0, s55, 81920\n\t"
      "s_xor_b32 s61, s47, 1572864\n\t"
      "buffer_load_dwordx4 v105, s[88:91], s61 offen lds\n\t"
      "s_add_u32 m0, s55, 82944\n\t"
      "s_xor_b32 s61, s47, 1605632\n\t"
      "buffer_load_dwordx4 v105, s[88:91], s61 offen lds\n\t"
      "s_waitcnt vmcnt(27)\n\t"
      "s_barrier\n\t"
      "s_waitcnt lgkmcnt(7)\n\t"
      "v_mfma_f32_16x16x32_bf16 v[16:19], v[0:3], v[32:35], 0\n\t"
      "ds_read_b128 v[80:83], v104 offset:8192\n\t"
      "v_mfma_f32_16x16x32_bf16 v[20:23], v[0:3], v[36:39], 0\n\t"
      "s_mov_b64 s[88:89], s[96:97]\n\t"
      "s_add_u32 m0, s55, 147456\n\t"
      "s_xor_b32 s61, s47, 1572864\n\t"
      "buffer_load_dwordx4 v116, s[88:91], s61 offen lds\n\t"
      "ds_read_b64_tr_b16 a[176:177], v106\n\t"
      "s_waitcnt lgkmcnt(8)\n\t"
      "v_mfma_f32_16x16x32_bf16 v[16:19], v[4:7], v[40:43], v[16:19]\n\t"
      "ds_read_b128 v[84:87], v119 offset:8192\n\t"
      "v_mfma_f32_16x16x32_bf16 v[20:23], v[4:7], v[44:47], v[20:23]\n\t"
      "s_add_u32 m0, s55, 148480\n\t"
      "s_xor_b32 s61, s47, 1605632\n\t"
      "buffer_load_dwordx4 v116, s[88:91], s61 offen lds\n\t"
      "ds_read_b64_tr_b16 a[178:179], v106 offset:1024\n\t"
      "s_waitcnt lgkmcnt(9)\n\t"
      "v_mfma_f32_16x16x32_bf16 v[16:19], v[8:11], v[48:51], v[16:19]\n\t"
      "ds_read_b128 v[88:91], v102 offset:8192\n\t"
      "v_mfma_f32_16x16x32_bf16 v[20:23], v[8:11], v[52:55], v[20:23]\n\t"
      "s_add_u32 m0, s55, 155648\n\t"
      "s_xor_b32 s61, s47, 1835008\n\t"
      "buffer_load_dwordx4 v116, s[88:91], s61 offen lds\n\t"
      "s_add_u32 m0, s55, 156672\n\t"
      "s_xor_b32 s61, s47, 1867776\n\t"
      "buffer_load_dwordx4 v116, s[88:91], s61 offen lds\n\t"
      "s_waitcnt lgkmcnt(9)\n\t"
      "v_mfma_f32_16x16x32_bf16 v[16:19], v[12:15], v[56:59], v[16:19]\n\t"
      "ds_read_b128 v[92:95], v103 offset:8192\n\t"
      "v_mfma_f32_16x16x32_bf16 v[20:23], v[12:15], v[60:63], v[20:23]\n\t"
      "s_mov_b64 s[88:89], s[92:93]\n\t"
      "s_add_u32 m0, s55, 90112\n\t"
      "s_xor_b32 s61, s47, 1835008\n\t"
      "buffer_load_dwordx4 v105, s[88:91], s61 offen lds\n\t"
      "s_add_u32 m0, s55, 91136\n\t"
      "s_xor_b32 s61, s47, 1867776\n\t"
      "buffer_load_dwordx4 v105, s[88:91], s61 offen lds\n\t"
      "s_waitcnt lgkmcnt(9)\n\t"
      "v_mfma_f32_16x16x32_bf16 v[24:27], v[64:67], v[32:35], 0\n\t"
      "ds_read_b128 v[0:3], v104 offset:9216\n\t"
      "v_mfma_f32_16x16x32_bf16 v[28:31], v[64:67], v[36:39], 0\n\t"
      "s_waitcnt lgkmcnt(9)\n\t"
      "v_mfma_f32_16x16x32_bf16 v[24:27], v[68:71], v[40:43], v[24:27]\n\t"
      "ds_read_b128 v[4:7], v119 offset:9216\n\t"
      "v_cvt_pk_bf16_f32 v128, v16, v17\n\t"
      "v_mfma_f32_16x16x32_bf16 v[28:31], v[68:71], v[44:47], v[28:31]\n\t"
      "v_cvt_pk_bf16_f32 v129, v18, v19\n\t"
      "s_waitcnt lgkmcnt(9)\n\t"
      "v_mfma_f32_16x16x32_bf16 v[24:27], v[72:75], v[48:51], v[24:27]\n\t"
      "ds_read_b128 v[8:11], v102 offset:9216\n\t"
      "v_cvt_pk_bf16_f32 v192, v20, v21\n\t"
      "v_mfma_f32_16x16x32_bf16 v[28:31], v[72:75], v[52:55], v[28:31]\n\t"
      "v_cvt_pk_bf16_f32 v193, v22, v23\n\t"
      "s_waitcnt lgkmcnt(9)\n\t"
      "v_mfma_f32_16x16x32_bf16 v[24:27], v[76:79], v[56:59], v[24:27]\n\t"
      "ds_read_b128 v[12:15], v103 offset:9216\n\t"
      "v_mfma_f32_16x16x32_bf16 v[28:31], v[76:79], v[60:63], v[28:31]\n\t"
      "s_waitcnt vmcnt(31)\n\t"
      "s_waitcnt lgkmcnt(6)\n\t"
      "s_barrier\n\t"
      "v_mfma_f32_16x16x32_bf16 v[16:19], v[80:83], v[32:35], 0\n\t"
      "ds_read_b128 v[64:67], v104 offset:16384\n\t"
      "v_mfma_f32_16x16x32_bf16 v[20:23], v[80:83], v[36:39], 0\n\t"
      "s_add_u32 m0, s55, 32768\n\t"
      "s_xor_b32 s61, s47, 2097152\n\t"
      "buffer_load_dwordx4 v105, s[88:91], s61 offen lds\n\t"
      "s_add_u32 m0, s55, 33792\n\t"
      "s_xor_b32 s61, s47, 2129920\n\t"
      "buffer_load_dwordx4 v105, s[88:91], s61 offen lds\n\t"
      "v_mfma_f32_16x16x32_bf16 v[16:19], v[84:87], v[40:43], v[16:19]\n\t"
      "ds_read_b128 v[68:71], v119 offset:16384\n\t"
      "v_cvt_pk_bf16_f32 v130, v24, v25\n\t"
      "v_mfma_f32_16x16x32_bf16 v[20:23], v[84:87], v[44:47], v[20:23]\n\t"
      "v_cvt_pk_bf16_f32 v131, v26, v27\n\t"
      "s_waitcnt lgkmcnt(7)\n\t"
      "v_mfma_f32_16x16x32_bf16 v[16:19], v[88:91], v[48:51], v[16:19]\n\t"
      "ds_read_b128 v[72:75], v102 offset:16384\n\t"
      "v_cvt_pk_bf16_f32 v194, v28, v29\n\t"
      "v_mfma_f32_16x16x32_bf16 v[20:23], v[88:91], v[52:55], v[20:23]\n\t"
      "v_cvt_pk_bf16_f32 v195, v30, v31\n\t"
      "s_waitcnt lgkmcnt(7)\n\t"
      "v_mfma_f32_16x16x32_bf16 v[16:19], v[92:95], v[56:59], v[16:19]\n\t"
      "ds_read_b128 v[76:79], v103 offset:16384\n\t"
      "v_mfma_f32_16x16x32_bf16 v[20:23], v[92:95], v[60:63], v[20:23]\n\t"
      "v_mfma_f32_16x16x32_bf16 a[168:171], v[128:131], v[128:131], 0\n\t"
      "s_waitcnt lgkmcnt(7)\n\t"
      "v_mfma_f32_16x16x32_bf16 v[24:27], v[0:3], v[32:35], 0\n\t"
      "ds_read_b128 v[80:83], v104 offset:17408\n\t"
      "v_mfma_f32_16x16x32_bf16 v[28:31], v[0:3], v[36:39], 0\n\t"
      "s_waitcnt lgkmcnt(7)\n\t"
      "v_mfma_f32_16x16x32_bf16 v[24:27], v[4:7], v[40:43], v[24:27]\n\t"
      "ds_read_b128 v[84:87], v119 offset:17408\n\t"
      "v_cvt_pk_bf16_f32 v132, v16, v17\n\t"
      "v_mfma_f32_16x16x32_bf16 v[28:31], v[4:7], v[44:47], v[28:31]\n\t"
      "v_cvt_pk_bf16_f32 v133, v18, v19\n\t"
      "s_waitcnt lgkmcnt(7)\n\t"
      "v_mfma_f32_16x16x32_bf16 v[24:27], v[8:11], v[48:51], v[24:27]\n\t"
      "ds_read_b128 v[88:91], v102 offset:17408\n\t"
      "v_cvt_pk_bf16_f32 v196, v20, v21\n\t"
      "v_mfma_f32_16x16x32_bf16 v[28:31], v[8:11], v[52:55], v[28:31]\n\t"
      "v_cvt_pk_bf16_f32 v197, v22, v23\n\t"
      "s_waitcnt lgkmcnt(7)\n\t"
      "v_mfma_f32_16x16x32_bf16 v[24:27], v[12:15], v[56:59], v[24:27]\n\t"
      "ds_read_b128 v[92:95], v103 offset:17408\n\t"
      "v_mfma_f32_16x16x32_bf16 v[28:31], v[12:15], v[60:63], v[28:31]\n\t"
      "v_mfma_f32_16x16x32_bf16 a[232:235], v[192:195], v[192:195], 0\n\t"
      "s_waitcnt vmcnt(31)\n\t"
      "s_waitcnt vmcnt(22)\n\t"
      "s_barrier\n\t"
      "s_waitcnt lgkmcnt(7)\n\t"
      "v_mfma_f32_16x16x32_bf16 v[16:19], v[64:67], v[32:35], 0\n\t"
      "ds_read_b128 v[0:3], v104 offset:24576\n\t"
      "v_mfma_f32_16x16x32_bf16 v[20:23], v[64:67], v[36:39], 0\n\t"
      "s_add_u32 m0, s55, 40960\n\t"
      "s_xor_b32 s61, s47, 2359296\n\t"
      "buffer_load_dwordx4 v105, s[88:91], s61 offen lds\n\t"
      "ds_read_b64_tr_b16 a[180:181], v106 offset:16384\n\t"
      "s_waitcnt lgkmcnt(8)\n\t"
      "v_mfma_f32_16x16x32_bf16 v[16:19], v[68:71], v[40:43], v[16:19]\n\t"
      "ds_read_b128 v[4:7], v119 offset:24576\n\t"
      "v_cvt_pk_bf16_f32 v134, v24, v25\n\t"
      "v_mfma_f32_16x16x32_bf16 v[20:23], v[68:71], v[44:47], v[20:23]\n\t"
      "s_add_u32 m0, s55, 41984\n\t"
      "s_xor_b32 s61, s47, 2392064\n\t"
      "buffer_load_dwordx4 v105, s[88:91], s61 offen lds\n\t"
      "ds_read_b64_tr_b16 a[182:183], v106 offset:17408\n\t"
      "v_cvt_pk_bf16_f32 v135, v26, v27\n\t"
      "s_waitcnt lgkmcnt(9)\n\t"
      "v_mfma_f32_16x16x32_bf16 v[16:19], v[72:75], v[48:51], v[16:19]\n\t"
      "ds_read_b128 v[8:11], v102 offset:24576\n\t"
      "v_cvt_pk_bf16_f32 v198, v28, v29\n\t"
      "v_mfma_f32_16x16x32_bf16 v[20:23], v[72:75], v[52:55], v[20:23]\n\t"
      "ds_read_b64_tr_b16 a[0:1], v117\n\t"
      "ds_read_b64_tr_b16 a[2:3], v117 offset:1024\n\t"
      "v_cvt_pk_bf16_f32 v199, v30, v31\n\t"
      "s_waitcnt lgkmcnt(11)\n\t"
      "v_mfma_f32_16x16x32_bf16 v[16:19], v[76:79], v[56:59], v[16:19]\n\t"
      "ds_read_b128 v[12:15], v103 offset:24576\n\t"
      "v_mfma_f32_16x16x32_bf16 v[20:23], v[76:79], v[60:63], v[20:23]\n\t"
      "ds_read_b64_tr_b16 a[64:65], v118\n\t"
      "ds_read_b64_tr_b16 a[66:67], v118 offset:1024\n\t"
      "v_mfma_f32_16x16x32_bf16 a[168:171], v[132:135], v[132:135], a[168:171]\n\t"
      "s_waitcnt lgkmcnt(13)\n\t"
      "v_mfma_f32_16x16x32_bf16 v[24:27], v[80:83], v[32:35], 0\n\t"
      "ds_read_b128 v[64:67], v104 offset:25600\n\t"
      "v_mfma_f32_16x16x32_bf16 v[28:31], v[80:83], v[36:39], 0\n\t"
      "ds_read_b64_tr_b16 a[4:5], v117 offset:8192\n\t"
      "s_waitcnt lgkmcnt(12)\n\t"
      "ds_read_b64_tr_b16 a[6:7], v117 offset:9216\n\t"
      "v_mfma_f32_16x16x32_bf16 v[24:27], v[84:87], v[40:43], v[24:27]\n\t"
      "ds_read_b128 v[68:71], v119 offset:25600\n\t"
      "v_cvt_pk_bf16_f32 v136, v16, v17\n\t"
      "v_mfma_f32_16x16x32_bf16 v[28:31], v[84:87], v[44:47], v[28:31]\n\t"
      "ds_read_b64_tr_b16 a[68:69], v118 offset:8192\n\t"
      "s_waitcnt lgkmcnt(12)\n\t"
      "ds_read_b64_tr_b16 a[70:71], v118 offset:9216\n\t"
      "v_cvt_pk_bf16_f32 v137, v18, v19\n\t"
      "v_mfma_f32_16x16x32_bf16 v[24:27], v[88:91], v[48:51], v[24:27]\n\t"
      "ds_read_b128 v[72:75], v102 offset:25600\n\t"
      "v_cvt_pk_bf16_f32 v200, v20, v21\n\t"
      "v_mfma_f32_16x16x32_bf16 v[28:31], v[88:91], v[52:55], v[28:31]\n\t"
      "v_cvt_pk_bf16_f32 v201, v22, v23\n\t"
      "v_mfma_f32_16x16x32_bf16 v[24:27], v[92:95], v[56:59], v[24:27]\n\t"
      "ds_read_b128 v[76:79], v103 offset:25600\n\t"
      "v_mfma_f32_16x16x32_bf16 v[28:31], v[92:95], v[60:63], v[28:31]\n\t"
      "v_mfma_f32_16x16x32_bf16 a[232:235], v[196:199], v[196:199], a[232:235]\n\t"
      "s_waitcnt vmcnt(22)\n\t"
      "s_waitcnt vmcnt(18)\n\t"
      "s_waitcnt lgkmcnt(2)\n\t"
      "s_barrier\n\t"
      "v_mfma_f32_16x16x32_bf16 v[16:19], v[0:3], v[32:35], 0\n\t"
      "ds_read_b128 v[80:83], v104 offset:32768\n\t"
      "v_mfma_f32_16x16x32_bf16 v[20:23], v[0:3], v[36:39], 0\n\t"
      "s_add_u32 m0, s55, 49152\n\t"
      "s_xor_b32 s61, s47, 2621440\n\t"
      "buffer_load_dwordx4 v105, s[88:91], s61 offen lds\n\t"
      "ds_read_b64_tr_b16 a[8:9], v117 offset:16384\n\t"
      "v_mfma_f32_16x16x32_bf16 v[16:19], v[4:7], v[40:43], v[16:19]\n\t"
      "ds_read_b128 v[84:87], v119 offset:32768\n\t"
      "v_cvt_pk_bf16_f32 v138, v24, v25\n\t"
      "v_mfma_f32_16x16x32_bf16 v[20:23], v[4:7], v[44:47], v[20:23]\n\t"
      "s_add_u32 m0, s55, 50176\n\t"
      "s_xor_b32 s61, s47, 2654208\n\t"
      "buffer_load_dwordx4 v105, s[88:91], s61 offen lds\n\t"
      "ds_read_b64_tr_b16 a[10:11], v117 offset:17408\n\t"
      "v_cvt_pk_bf16_f32 v139, v26, v27\n\t"
      "v_mfma_f32_16x16x32_bf16 v[16:19], v[8:11], v[48:51], v[16:19]\n\t"
      "ds_read_b128 v[88:91], v102 offset:32768\n\t"
      "v_cvt_pk_bf16_f32 v202, v28, v29\n\t"
      "v_mfma_f32_16x16x32_bf16 v[20:23], v[8:11], v[52:55], v[20:23]\n\t"
      "s_mov_b64 s[88:89], s[96:97]\n\t"
      "s_add_u32 m0, s55, 98304\n\t"
      "s_xor_b32 s61, s47, 2097152\n\t"
      "buffer_load_dwordx4 v116, s[88:91], s61 offen lds\n\t"
      "ds_read_b64_tr_b16 a[72:73], v118 offset:16384\n\t"
      "v_cvt_pk_bf16_f32 v203, v30, v31\n\t"
      "v_mfma_f32_16x16x32_bf16 v[16:19], v[12:15], v[56:59], v[16:19]\n\t"
      "ds_read_b128 v[92:95], v103 offset:32768\n\t"
      "v_mfma_f32_16x16x32_bf16 v[20:23], v[12:15], v[60:63], v[20:23]\n\t"
      "s_add_u32 m0, s55, 99328\n\t"
      "s_xor_b32 s61, s47, 2129920\n\t"
      "buffer_load_dwordx4 v116, s[88:91], s61 offen lds\n\t"
      "ds_read_b64_tr_b16 a[74:75], v118 offset:17408\n\t"
      "v_mfma_f32_16x16x32_bf16 a[168:171], v[136:139], v[136:139], a[168:171]\n\t"
      "v_mfma_f32_16x16x32_bf16 v[24:27], v[64:67], v[32:35], 0\n\t"
      "ds_read_b128 v[0:3], v104 offset:33792\n\t"
      "v_mfma_f32_16x16x32_bf16 v[28:31], v[64:67], v[36:39], 0\n\t"
      "s_add_u32 m0, s55, 106496\n\t"
      "s_xor_b32 s61, s47, 2359296\n\t"
      "buffer_load_dwordx4 v116, s[88:91], s61 offen lds\n\t"
      "ds_read_b64_tr_b16 a[12:13], v117 offset:24576\n\t"
      "v_mfma_f32_16x16x32_bf16 v[24:27], v[68:71], v[40:43], v[24:27]\n\t"
      "ds_read_b128 v[4:7], v119 offset:33792\n\t"
      "v_cvt_pk_bf16_f32 v140, v16, v17\n\t"
      "v_mfma_f32_16x16x32_bf16 v[28:31], v[68:71], v[44:47], v[28:31]\n\t"
      "s_add_u32 m0, s55, 107520\n\t"
      "s_xor_b32 s61, s47, 2392064\n\t"
      "buffer_load_dwordx4 v116, s[88:91], s61 offen lds\n\t"
      "ds_read_b64_tr_b16 a[14:15], v117 offset:25600\n\t"
      "v_cvt_pk_bf16_f32 v141, v18, v19\n\t"
      "s_waitcnt lgkmcnt(13)\n\t"
      "v_mfma_f32_16x16x32_bf16 v[24:27], v[72:75], v[48:51], v[24:27]\n\t"
      "ds_read_b128 v[8:11], v102 offset:33792\n\t"
      "v_cvt_pk_bf16_f32 v204, v20, v21\n\t"
      "v_mfma_f32_16x16x32_bf16 v[28:31], v[72:75], v[52:55], v[28:31]\n\t"
      "ds_read_b64_tr_b16 a[76:77], v118 offset:24576\n\t"
      "s_waitcnt lgkmcnt(12)\n\t"
      "ds_read_b64_tr_b16 a[78:79], v118 offset:25600\n\t"
      "v_cvt_pk_bf16_f32 v205, v22, v23\n\t"
      "v_mfma_f32_16x16x32_bf16 v[24:27], v[76:79], v[56:59], v[24:27]\n\t"
      "ds_read_b128 v[12:15], v103 offset:33792\n\t"
      "v_mfma_f32_16x16x32_bf16 v[28:31], v[76:79], v[60:63], v[28:31]\n\t"
      "v_mfma_f32_16x16x32_bf16 a[232:235], v[200:203], v[200:203], a[232:235]\n\t"
      "s_waitcnt vmcnt(22)\n\t"
      "s_waitcnt vmcnt(18)\n\t"
      "s_waitcnt lgkmcnt(1)\n\t"
      "s_barrier\n\t"
      "v_mfma_f32_16x16x32_bf16 v[16:19], v[80:83], v[32:35], 0\n\t"
      "ds_read_b128 v[64:67], v104 offset:40960\n\t"
      "v_mfma_f32_16x16x32_bf16 v[20:23], v[80:83], v[36:39], 0\n\t"
      "s_mov_b64 s[88:89], s[92:93]\n\t"
      "s_add_u32 m0, s55, 57344\n\t"
      "s_xor_b32 s61, s47, 2883584\n\t"
      "buffer_load_dwordx4 v105, s[88:91], s61 offen lds\n\t"
      "ds_read_b64_tr_b16 a[184:185], v106 offset:32768\n\t"
      "v_mfma_f32_16x16x32_bf16 v[16:19], v[84:87], v[40:43], v[16:19]\n\t"
      "ds_read_b128 v[68:71], v119 offset:40960\n\t"
      "v_cvt_pk_bf16_f32 v142, v24, v25\n\t"
      "v_mfma_f32_16x16x32_bf16 v[20:23], v[84:87], v[44:47], v[20:23]\n\t"
      "s_add_u32 m0, s55, 58368\n\t"
      "s_xor_b32 s61, s47, 2916352\n\t"
      "buffer_load_dwordx4 v105, s[88:91], s61 offen lds\n\t"
      "ds_read_b64_tr_b16 a[186:187], v106 offset:33792\n\t"
      "v_cvt_pk_bf16_f32 v143, v26, v27\n\t"
      "v_mfma_f32_16x16x32_bf16 v[16:19], v[88:91], v[48:51], v[16:19]\n\t"
      "ds_read_b128 v[72:75], v102 offset:40960\n\t"
      "v_cvt_pk_bf16_f32 v206, v28, v29\n\t"
      "v_mfma_f32_16x16x32_bf16 v[20:23], v[88:91], v[52:55], v[20:23]\n\t"
      "s_mov_b64 s[88:89], s[96:97]\n\t"
      "s_add_u32 m0, s55, 114688\n\t"
      "s_xor_b32 s61, s47, 2621440\n\t"
      "buffer_load_dwordx4 v116, s[88:91], s61 offen lds\n\t"
      "ds_read_b64_tr_b16 a[16:17], v117 offset:32768\n\t"
      "v_cvt_pk_bf16_f32 v207, v30, v31\n\t"
      "v_mfma_f32_16x16x32_bf16 v[16:19], v[92:95], v[56:59], v[16:19]\n\t"
      "ds_read_b128 v[76:79], v103 offset:40960\n\t"
      "v_mfma_f32_16x16x32_bf16 v[20:23], v[92:95], v[60:63], v[20:23]\n\t"
      "s_add_u32 m0, s55, 115712\n\t"
      "s_xor_b32 s61, s47, 2654208\n\t"
      "buffer_load_dwordx4 v116, s[88:91], s61 offen lds\n\t"
      "ds_read_b64_tr_b16 a[18:19], v117 offset:33792\n\t"
      "v_mfma_f32_16x16x32_bf16 a[168:171], v[140:143], v[140:143], a[168:171]\n\t"
      "v_mfma_f32_16x16x32_bf16 v[24:27], v[0:3], v[32:35], 0\n\t"
      "ds_read_b128 v[80:83], v104 offset:41984\n\t"
      "v_mfma_f32_16x16x32_bf16 v[28:31], v[0:3], v[36:39], 0\n\t"
      "s_add_u32 m0, s55, 122880\n\t"
      "s_xor_b32 s61, s47, 2883584\n\t"
      "buffer_load_dwordx4 v116, s[88:91], s61 offen lds\n\t"
      "ds_read_b64_tr_b16 a[80:81], v118 offset:32768\n\t"
      "v_mfma_f32_16x16x32_bf16 v[24:27], v[4:7], v[40:43], v[24:27]\n\t"
      "ds_read_b128 v[84:87], v119 offset:41984\n\t"
      "v_cvt_pk_bf16_f32 v144, v16, v17\n\t"
      "v_mfma_f32_16x16x32_bf16 v[28:31], v[4:7], v[44:47], v[28:31]\n\t"
      "s_add_u32 m0, s55, 123904\n\t"
      "s_xor_b32 s61, s47, 2916352\n\t"
      "buffer_load_dwordx4 v116, s[88:91], s61 offen lds\n\t"
      "ds_read_b64_tr_b16 a[82:83], v118 offset:33792\n\t"
      "v_cvt_pk_bf16_f32 v145, v18, v19\n\t"
      "v_mfma_f32_16x16x32_bf16 v[24:27], v[8:11], v[48:51], v[24:27]\n\t"
      "ds_read_b128 v[88:91], v102 offset:41984\n\t"
      "v_cvt_pk_bf16_f32 v208, v20, v21\n\t"
      "v_mfma_f32_16x16x32_bf16 v[28:31], v[8:11], v[52:55], v[28:31]\n\t"
      "ds_read_b64_tr_b16 a[20:21], v117 offset:40960\n\t"
      "s_waitcnt lgkmcnt(12)\n\t"
      "ds_read_b64_tr_b16 a[22:23], v117 offset:41984\n\t"
      "v_cvt_pk_bf16_f32 v209, v22, v23\n\t"
      "v_mfma_f32_16x16x32_bf16 v[24:27], v[12:15], v[56:59], v[24:27]\n\t"
      "ds_read_b128 v[92:95], v103 offset:41984\n\t"
      "v_mfma_f32_16x16x32_bf16 v[28:31], v[12:15], v[60:63], v[28:31]\n\t"
      "ds_read_b64_tr_b16 a[84:85], v118 offset:40960\n\t"
      "s_waitcnt lgkmcnt(12)\n\t"
      "ds_read_b64_tr_b16 a[86:87], v118 offset:41984\n\t"
      "v_mfma_f32_16x16x32_bf16 a[232:235], v[204:207], v[204:207], a[232:235]\n\t"
      "s_waitcnt vmcnt(22)\n\t"
      "s_waitcnt vmcnt(18)\n\t"
      "s_waitcnt lgkmcnt(0)\n\t"
      "s_barrier\n\t"
      "v_mfma_f32_16x16x32_bf16 v[16:19], v[64:67], v[32:35], 0\n\t"
      "ds_read_b128 v[0:3], v104 offset:49152\n\t"
      "v_mfma_f32_16x16x32_bf16 v[20:23], v[64:67], v[36:39], 0\n\t"
      "s_mov_b64 s[88:89], s[92:93]\n\t"
      "s_add_u32 m0, s55, 65536\n\t"
      "s_xor_b32 s61, s47, 3145728\n\t"
      "buffer_load_dwordx4 v105, s[88:91], s61 offen lds\n\t"
      "ds_read_b64_tr_b16 a[24:25], v117 offset:49152\n\t"
      "v_mfma_f32_16x16x32_bf16 v[16:19], v[68:71], v[40:43], v[16:19]\n\t"
      "ds_read_b128 v[4:7], v119 offset:49152\n\t"
      "v_cvt_pk_bf16_f32 v146, v24, v25\n\t"
      "v_mfma_f32_16x16x32_bf16 v[20:23], v[68:71], v[44:47], v[20:23]\n\t"
      "s_add_u32 m0, s55, 66560\n\t"
      "s_xor_b32 s61, s47, 3178496\n\t"
      "buffer_load_dwordx4 v105, s[88:91], s61 offen lds\n\t"
      "ds_read_b64_tr_b16 a[26:27], v117 offset:50176\n\t"
      "v_cvt_pk_bf16_f32 v147, v26, v27\n\t"
      "v_mfma_f32_16x16x32_bf16 v[16:19], v[72:75], v[48:51], v[16:19]\n\t"
      "ds_read_b128 v[8:11], v102 offset:49152\n\t"
      "v_cvt_pk_bf16_f32 v210, v28, v29\n\t"
      "v_mfma_f32_16x16x32_bf16 v[20:23], v[72:75], v[52:55], v[20:23]\n\t"
      "s_mov_b64 s[88:89], s[96:97]\n\t"
      "s_add_u32 m0, s55, 131072\n\t"
      "s_xor_b32 s61, s47, 3145728\n\t"
      "buffer_load_dwordx4 v116, s[88:91], s61 offen lds\n\t"
      "ds_read_b64_tr_b16 a[88:89], v118 offset:49152\n\t"
      "v_cvt_pk_bf16_f32 v211, v30, v31\n\t"
      "v_mfma_f32_16x16x32_bf16 v[16:19], v[76:79], v[56:59], v[16:19]\n\t"
      "ds_read_b128 v[12:15], v103 offset:49152\n\t"
      "v_mfma_f32_16x16x32_bf16 v[20:23], v[76:79], v[60:63], v[20:23]\n\t"
      "s_add_u32 m0, s55, 132096\n\t"
      "s_xor_b32 s61, s47, 3178496\n\t"
      "buffer_load_dwordx4 v116, s[88:91], s61 offen lds\n\t"
      "ds_read_b64_tr_b16 a[90:91], v118 offset:50176\n\t"
      "v_mfma_f32_16x16x32_bf16 a[168:171], v[144:147], v[144:147], a[168:171]\n\t"
      "v_mfma_f32_16x16x32_bf16 v[24:27], v[80:83], v[32:35], 0\n\t"
      "ds_read_b128 v[64:67], v104 offset:50176\n\t"
      "v_mfma_f32_16x16x32_bf16 v[28:31], v[80:83], v[36:39], 0\n\t"
      "s_add_u32 m0, s55, 139264\n\t"
      "s_xor_b32 s61, s47, 3407872\n\t"
      "buffer_load_dwordx4 v116, s[88:91], s61 offen lds\n\t"
      "ds_read_b64_tr_b16 a[28:29], v117 offset:57344\n\t"
      "v_mfma_f32_16x16x32_bf16 v[24:27], v[84:87], v[40:43], v[24:27]\n\t"
      "ds_read_b128 v[68:71], v119 offset:50176\n\t"
      "v_cvt_pk_bf16_f32 v148, v16, v17\n\t"
      "v_mfma_f32_16x16x32_bf16 v[28:31], v[84:87], v[44:47], v[28:31]\n\t"
      "s_add_u32 m0, s55, 140288\n\t"
      "s_xor_b32 s61, s47, 3440640\n\t"
      "buffer_load_dwordx4 v116, s[88:91], s61 offen lds\n\t"
      "ds_read_b64_tr_b16 a[30:31], v117 offset:58368\n\t"
      "v_cvt_pk_bf16_f32 v149, v18, v19\n\t"
      "v_mfma_f32_16x16x32_bf16 v[24:27], v[88:91], v[48:51], v[24:27]\n\t"
      "ds_read_b128 v[72:75], v102 offset:50176\n\t"
      "v_cvt_pk_bf16_f32 v212, v20, v21\n\t"
      "v_mfma_f32_16x16x32_bf16 v[28:31], v[88:91], v[52:55], v[28:31]\n\t"
      "ds_read_b64_tr_b16 a[92:93], v118 offset:57344\n\t"
      "ds_read_b64_tr_b16 a[94:95], v118 offset:58368\n\t"
      "v_cvt_pk_bf16_f32 v213, v22, v23\n\t"
      "v_mfma_f32_16x16x32_bf16 v[24:27], v[92:95], v[56:59], v[24:27]\n\t"
      "s_waitcnt lgkmcnt(12)\n\t"
      "ds_read_b128 v[76:79], v103 offset:50176\n\t"
      "v_mfma_f32_16x16x32_bf16 v[28:31], v[92:95], v[60:63], v[28:31]\n\t"
      "v_mfma_f32_16x16x32_bf16 a[232:235], v[208:211], v[208:211], a[232:235]\n\t"
      "s_waitcnt vmcnt(22)\n\t"
      "s_waitcnt vmcnt(12)\n\t"
      "s_waitcnt lgkmcnt(1)\n\t"
      "s_barrier\n\t"
      "v_mfma_f32_16x16x32_bf16 v[16:19], v[0:3], v[32:35], 0\n\t"
      "ds_read_b128 v[80:83], v104 offset:57344\n\t"
      "v_mfma_f32_16x16x32_bf16 v[20:23], v[0:3], v[36:39], 0\n\t"
      "s_mov_b64 s[88:89], s[92:93]\n\t"
      "s_add_u32 m0, s55, 73728\n\t"
      "s_xor_b32 s61, s47, 3407872\n\t"
      "buffer_load_dwordx4 v105, s[88:91], s61 offen lds\n\t"
      "ds_read_b64_tr_b16 a[188:189], v106 offset:49152\n\t"
      "v_mfma_f32_16x16x32_bf16 v[16:19], v[4:7], v[40:43], v[16:19]\n\t"
      "ds_read_b128 v[84:87], v119 offset:57344\n\t"
      "v_cvt_pk_bf16_f32 v150, v24, v25\n\t"
      "v_mfma_f32_16x16x32_bf16 v[20:23], v[4:7], v[44:47], v[20:23]\n\t"
      "s_add_u32 m0, s55, 74752\n\t"
      "s_xor_b32 s61, s47, 3440640\n\t"
      "buffer_load_dwordx4 v105, s[88:91], s61 offen lds\n\t"
      "ds_read_b64_tr_b16 a[190:191], v106 offset:50176\n\t"
      "v_cvt_pk_bf16_f32 v151, v26, v27\n\t"
      "v_mfma_f32_16x16x32_bf16 v[16:19], v[8:11], v[48:51], v[16:19]\n\t"
      "ds_read_b128 v[88:91], v102 offset:57344\n\t"
      "v_cvt_pk_bf16_f32 v214, v28, v29\n\t"
      "v_mfma_f32_16x16x32_bf16 v[20:23], v[8:11], v[52:55], v[20:23]\n\t"
      "s_mov_b64 s[88:89], s[96:97]\n\t"
      "s_add_u32 m0, s55, 147456\n\t"
      "s_xor_b32 s61, s47, 3670016\n\t"
      "buffer_load_dwordx4 v116, s[88:91], s61 offen lds\n\t"
      "ds_read_b64_tr_b16 a[32:33], v117\n\t"
      "v_cvt_pk_bf16_f32 v215, v30, v31\n\t"
      "v_mfma_f32_16x16x32_bf16 v[16:19], v[12:15], v[56:59], v[16:19]\n\t"
      "ds_read_b128 v[92:95], v103 offset:57344\n\t"
      "v_mfma_f32_16x16x32_bf16 v[20:23], v[12:15], v[60:63], v[20:23]\n\t"
      "s_add_u32 m0, s55, 148480\n\t"
      "s_xor_b32 s61, s47, 3702784\n\t"
      "buffer_load_dwordx4 v116, s[88:91], s61 offen lds\n\t"
      "ds_read_b64_tr_b16 a[34:35], v117 offset:1024\n\t"
      "v_mfma_f32_16x16x32_bf16 a[168:171], v[148:151], v[148:151], a[168:171]\n\t"
      "v_readfirstlane_b32 s53, v126\n\t"
      "v_lshl_or_b32 v96, v96, 16, 1\n\t"
      "v_lshl_or_b32 v97, v97, 16, 1\n\t"
      "v_lshl_or_b32 v98, v98, 16, 1\n\t"
      "v_lshl_or_b32 v99, v99, 16, 1\n\t"
      "s_mov_b64 exec, 0xff\n\t"
      "buffer_store_dwordx4 v[96:99], v107, s[36:39], s43 offen sc1\n\t"
      "s_mov_b64 exec, -1\n\t"
      "s_mul_i32 s53, s53, 0x9e3779b1\n\t"
      "s_add_u32 s53, s53, 0x7f4a7c15\n\t"
      "v_mov_b32 v96, 0x20000000\n\t"
      "v_mov_b32 v97, 0x20000000\n\t"
      "v_mov_b32 v98, 0x20000000\n\t"
      "v_mov_b32 v99, 0x20000000\n\t"
      "s_mov_b32 s61, 8192\n\t"
      "buffer_store_dwordx4 v[96:99], v101, s[36:39], 0 offen sc1\n\t"
      "buffer_store_dwordx4 v[96:99], v101, s[36:39], s61 offen sc1\n\t"
      "s_nop 1\n\t"
      "v_mov_b32 v96, v111\n\t"
      "v_mov_b32 v97, v111\n\t"
      "v_mov_b32 v98, v111\n\t"
      "v_mov_b32 v99, v111\n\t"
      "buffer_store_dwordx4 v[96:99], v108, s[36:39], 0 offen sc1\n\t"
      "v_mfma_f32_16x16x32_bf16 v[24:27], v[64:67], v[32:35], 0\n\t"
      "ds_read_b128 v[0:3], v104 offset:58368\n\t"
      "v_mfma_f32_16x16x32_bf16 v[28:31], v[64:67], v[36:39], 0\n\t"
      "s_add_u32 m0, s55, 155648\n\t"
      "s_xor_b32 s61, s47, 3932160\n\t"
      "buffer_load_dwordx4 v116, s[88:91], s61 offen lds\n\t"
      "ds_read_b64_tr_b16 a[96:97], v118\n\t"
      "v_mfma_f32_16x16x32_bf16 v[24:27], v[68:71], v[40:43], v[24:27]\n\t"
      "ds_read_b128 v[4:7], v119 offset:58368\n\t"
      "v_cvt_pk_bf16_f32 v152, v16, v17\n\t"
      "v_mfma_f32_16x16x32_bf16 v[28:31], v[68:71], v[44:47], v[28:31]\n\t"
      "s_add_u32 m0, s55, 156672\n\t"
      "s_xor_b32 s61, s47, 3964928\n\t"
      "buffer_load_dwordx4 v116, s[88:91], s61 offen lds\n\t"
      "ds_read_b64_tr_b16 a[98:99], v118 offset:1024\n\t"
      "v_cvt_pk_bf16_f32 v153, v18, v19\n\t"
      "v_mfma_f32_16x16x32_bf16 v[24:27], v[72:75], v[48:51], v[24:27]\n\t"
      "ds_read_b128 v[8:11], v102 offset:58368\n\t"
      "v_cvt_pk_bf16_f32 v216, v20, v21\n\t"
      "v_mfma_f32_16x16x32_bf16 v[28:31], v[72:75], v[52:55], v[28:31]\n\t"
      "ds_read_b64_tr_b16 a[36:37], v117 offset:8192\n\t"
      "s_waitcnt lgkmcnt(12)\n\t"
      "ds_read_b64_tr_b16 a[38:39], v117 offset:9216\n\t"
      "v_cvt_pk_bf16_f32 v217, v22, v23\n\t"
      "v_mfma_f32_16x16x32_bf16 v[24:27], v[76:79], v[56:59], v[24:27]\n\t"
      "ds_read_b128 v[12:15], v103 offset:58368\n\t"
      "v_mfma_f32_16x16x32_bf16 v[28:31], v[76:79], v[60:63], v[28:31]\n\t"
      "ds_read_b64_tr_b16 a[100:101], v118 offset:8192\n\t"
      "s_waitcnt lgkmcnt(12)\n\t"
      "ds_read_b64_tr_b16 a[102:103], v118 offset:9216\n\t"
      "v_mfma_f32_16x16x32_bf16 a[232:235], v[212:215], v[212:215], a[232:235]\n\t"
      "s_waitcnt vmcnt(16)\n\t"
      "s_waitcnt lgkmcnt(0)\n\t"
      "s_barrier\n\t"
      "v_mfma_f32_16x16x32_bf16 v[16:19], v[80:83], v[32:35], 0\n\t"
      "ds_read_b128 v[64:67], v104\n\t"
      "v_mfma_f32_16x16x32_bf16 v[20:23], v[80:83], v[36:39], 0\n\t"
      "s_mov_b64 s[88:89], s[92:93]\n\t"
      "s_add_u32 m0, s55, 81920\n\t"
      "s_xor_b32 s61, s47, 3670016\n\t"
      "buffer_load_dwordx4 v105, s[88:91], s61 offen lds\n\t"
      "ds_read_b64_tr_b16 a[40:41], v117 offset:16384\n\t"
      "v_mfma_f32_16x16x32_bf16 v[16:19], v[84:87], v[40:43], v[16:19]\n\t"
      "ds_read_b128 v[68:71], v119\n\t"
      "v_cvt_pk_bf16_f32 v154, v24, v25\n\t"
      "v_mfma_f32_16x16x32_bf16 v[20:23], v[84:87], v[44:47], v[20:23]\n\t"
      "s_add_u32 m0, s55, 82944\n\t"
      "s_xor_b32 s61, s47, 3702784\n\t"
      "buffer_load_dwordx4 v105, s[88:91], s61 offen lds\n\t"
      "ds_read_b64_tr_b16 a[42:43], v117 offset:17408\n\t"
      "v_cvt_pk_bf16_f32 v155, v26, v27\n\t"
      "v_mfma_f32_16x16x32_bf16 v[16:19], v[88:91], v[48:51], v[16:19]\n\t"
      "ds_read_b128 v[72:75], v102\n\t"
      "v_cvt_pk_bf16_f32 v218, v28, v29\n\t"
      "v_mfma_f32_16x16x32_bf16 v[20:23], v[88:91], v[52:55], v[20:23]\n\t"
      "s_mov_b64 s[88:89], s[98:99]\n\t"
      "s_add_u32 m0, s55, 98304\n\t"
      "s_xor_b32 s61, s47, 0\n\t"
      "buffer_load_dwordx4 v116, s[88:91], s61 offen lds\n\t"
      "ds_read_b64_tr_b16 a[104:105], v118 offset:16384\n\t"
      "v_cvt_pk_bf16_f32 v219, v30, v31\n\t"
      "v_mfma_f32_16x16x32_bf16 v[16:19], v[92:95], v[56:59], v[16:19]\n\t"
      "ds_read_b128 v[76:79], v103\n\t"
      "v_mfma_f32_16x16x32_bf16 v[20:23], v[92:95], v[60:63], v[20:23]\n\t"
      "s_add_u32 m0, s55, 99328\n\t"
      "s_xor_b32 s61, s47, 32768\n\t"
      "buffer_load_dwordx4 v116, s[88:91], s61 offen lds\n\t"
      "ds_read_b64_tr_b16 a[106:107], v118 offset:17408\n\t"
      "v_mfma_f32_16x16x32_bf16 a[168:171], v[152:155], v[152:155], a[168:171]\n\t"
      "v_mfma_f32_16x16x32_bf16 v[24:27], v[0:3], v[32:35], 0\n\t"
      "ds_read_b128 v[80:83], v104 offset:1024\n\t"
      "v_mfma_f32_16x16x32_bf16 v[28:31], v[0:3], v[36:39], 0\n\t"
      "s_add_u32 m0, s55, 106496\n\t"
      "s_xor_b32 s61, s47, 262144\n\t"
      "buffer_load_dwordx4 v116, s[88:91], s61 offen lds\n\t"
      "ds_read_b64_tr_b16 a[44:45], v117 offset:24576\n\t"
      "v_mfma_f32_16x16x32_bf16 v[24:27], v[4:7], v[40:43], v[24:27]\n\t"
      "ds_read_b128 v[84:87], v119 offset:1024\n\t"
      "v_cvt_pk_bf16_f32 v156, v16, v17\n\t"
      "v_mfma_f32_16x16x32_bf16 v[28:31], v[4:7], v[44:47], v[28:31]\n\t"
      "s_add_u32 m0, s55, 107520\n\t"
      "s_xor_b32 s61, s47, 294912\n\t"
      "buffer_load_dwordx4 v116, s[88:91], s61 offen lds\n\t"
      "ds_read_b64_tr_b16 a[46:47], v117 offset:25600\n\t"
      "v_cvt_pk_bf16_f32 v157, v18, v19\n\t"
      "v_mfma_f32_16x16x32_bf16 v[24:27], v[8:11], v[48:51], v[24:27]\n\t"
      "ds_read_b128 v[88:91], v102 offset:1024\n\t"
      "v_cvt_pk_bf16_f32 v220, v20, v21\n\t"
      "v_mfma_f32_16x16x32_bf16 v[28:31], v[8:11], v[52:55], v[28:31]\n\t"
      "ds_read_b64_tr_b16 a[108:109], v118 offset:24576\n\t"
      "ds_read_b64_tr_b16 a[110:111], v118 offset:25600\n\t"
      "v_cvt_pk_bf16_f32 v221, v22, v23\n\t"
      "v_mfma_f32_16x16x32_bf16 v[24:27], v[12:15], v[56:59], v[24:27]\n\t"
      "s_waitcnt lgkmcnt(12)\n\t"
      "ds_read_b128 v[92:95], v103 offset:1024\n\t"
      "v_mfma_f32_16x16x32_bf16 v[28:31], v[12:15], v[60:63], v[28:31]\n\t"
      "v_mfma_f32_16x16x32_bf16 a[232:235], v[216:219], v[216:219], a[232:235]\n\t"
      "s_waitcnt vmcnt(16)\n\t"
      "s_waitcnt lgkmcnt(1)\n\t"
      "s_barrier\n\t"
      "v_mfma_f32_16x16x32_bf16 v[16:19], v[64:67], v[32:35], 0\n\t"
      "ds_read_b128 v[0:3], v104 offset:8192\n\t"
      "v_mfma_f32_16x16x32_bf16 v[20:23], v[64:67], v[36:39], 0\n\t"
      "s_mov_b64 s[88:89], s[92:93]\n\t"
      "s_add_u32 m0, s55, 90112\n\t"
      "s_xor_b32 s61, s47, 3932160\n\t"
      "buffer_load_dwordx4 v105, s[88:91], s61 offen lds\n\t"
      "ds_read_b64_tr_b16 a[240:241], v106\n\t"
      "v_mfma_f32_16x16x32_bf16 v[16:19], v[68:71], v[40:43], v[16:19]\n\t"
      "ds_read_b128 v[4:7], v119 offset:8192\n\t"
      "v_cvt_pk_bf16_f32 v158, v24, v25\n\t"
      "v_mfma_f32_16x16x32_bf16 v[20:23], v[68:71], v[44:47], v[20:23]\n\t"
      "s_add_u32 m0, s55, 91136\n\t"
      "s_xor_b32 s61, s47, 3964928\n\t"
      "buffer_load_dwordx4 v105, s[88:91], s61 offen lds\n\t"
      "ds_read_b64_tr_b16 a[242:243], v106 offset:1024\n\t"
      "v_cvt_pk_bf16_f32 v159, v26, v27\n\t"
      "v_mfma_f32_16x16x32_bf16 v[16:19], v[72:75], v[48:51], v[16:19]\n\t"
      "ds_read_b128 v[8:11], v102 offset:8192\n\t"
      "v_cvt_pk_bf16_f32 v222, v28, v29\n\t"
      "v_mfma_f32_16x16x32_bf16 v[20:23], v[72:75], v[52:55], v[20:23]\n\t"
      "s_mov_b64 s[88:89], s[98:99]\n\t"
      "s_add_u32 m0, s55, 114688\n\t"
      "s_xor_b32 s61, s47, 524288\n\t"
      "buffer_load_dwordx4 v116, s[88:91], s61 offen lds\n\t"
      "ds_read_b64_tr_b16 a[48:49], v117 offset:32768\n\t"
      "v_cvt_pk_bf16_f32 v223, v30, v31\n\t"
      "v_mfma_f32_16x16x32_bf16 v[16:19], v[76:79], v[56:59], v[16:19]\n\t"
      "ds_read_b128 v[12:15], v103 offset:8192\n\t"
      "v_mfma_f32_16x16x32_bf16 v[20:23], v[76:79], v[60:63], v[20:23]\n\t"
      "s_add_u32 m0, s55, 115712\n\t"
      "s_xor_b32 s61, s47, 557056\n\t"
      "buffer_load_dwordx4 v116, s[88:91], s61 offen lds\n\t"
      "ds_read_b64_tr_b16 a[50:51], v117 offset:33792\n\t"
      "v_mfma_f32_16x16x32_bf16 a[168:171], v[156:159], v[156:159], a[168:171]\n\t"
      "v_mfma_f32_16x16x32_bf16 v[24:27], v[80:83], v[32:35], 0\n\t"
      "ds_read_b128 v[64:67], v104 offset:9216\n\t"
      "v_mfma_f32_16x16x32_bf16 v[28:31], v[80:83], v[36:39], 0\n\t"
      "s_add_u32 m0, s55, 122880\n\t"
      "s_xor_b32 s61, s47, 786432\n\t"
      "buffer_load_dwordx4 v116, s[88:91], s61 offen lds\n\t"
      "ds_read_b64_tr_b16 a[112:113], v118 offset:32768\n\t"
      "v_mfma_f32_16x16x32_bf16 v[24:27], v[84:87], v[40:43], v[24:27]\n\t"
      "ds_read_b128 v[68:71], v119 offset:9216\n\t"
      "v_cvt_pk_bf16_f32 v160, v16, v17\n\t"
      "v_mfma_f32_16x16x32_bf16 v[28:31], v[84:87], v[44:47], v[28:31]\n\t"
      "s_add_u32 m0, s55, 123904\n\t"
      "s_xor_b32 s61, s47, 819200\n\t"
      "buffer_load_dwordx4 v116, s[88:91], s61 offen lds\n\t"
      "ds_read_b64_tr_b16 a[114:115], v118 offset:33792\n\t"
      "v_cvt_pk_bf16_f32 v161, v18, v19\n\t"
      "v_mfma_f32_16x16x32_bf16 v[24:27], v[88:91], v[48:51], v[24:27]\n\t"
      "ds_read_b128 v[72:75], v102 offset:9216\n\t"
      "v_cvt_pk_bf16_f32 v224, v20, v21\n\t"
      "v_mfma_f32_16x16x32_bf16 v[28:31], v[88:91], v[52:55], v[28:31]\n\t"
      "ds_read_b64_tr_b16 a[52:53], v117 offset:40960\n\t"
      "s_waitcnt lgkmcnt(12)\n\t"
      "ds_read_b64_tr_b16 a[54:55], v117 offset:41984\n\t"
      "v_cvt_pk_bf16_f32 v225, v22, v23\n\t"
      "v_mfma_f32_16x16x32_bf16 v[24:27], v[92:95], v[56:59], v[24:27]\n\t"
      "ds_read_b128 v[76:79], v103 offset:9216\n\t"
      "v_mfma_f32_16x16x32_bf16 v[28:31], v[92:95], v[60:63], v[28:31]\n\t"
      "ds_read_b64_tr_b16 a[116:117], v118 offset:40960\n\t"
      "s_waitcnt lgkmcnt(12)\n\t"
      "ds_read_b64_tr_b16 a[118:119], v118 offset:41984\n\t"
      "v_mfma_f32_16x16x32_bf16 a[232:235], v[220:223], v[220:223], a[232:235]\n\t"
      "s_waitcnt vmcnt(12)\n\t"
      "s_waitcnt lgkmcnt(0)\n\t"
      "s_barrier\n\t"
      "v_mfma_f32_16x16x32_bf16 v[16:19], v[0:3], v[32:35], 0\n\t"
      "ds_read_b128 v[80:83], v104 offset:16384\n\t"
      "v_mfma_f32_16x16x32_bf16 v[20:23], v[0:3], v[36:39], 0\n\t"
      "s_add_u32 m0, s55, 131072\n\t"
      "s_xor_b32 s61, s47, 1048576\n\t"
      "buffer_load_dwordx4 v116, s[88:91], s61 offen lds\n\t"
      "ds_read_b64_tr_b16 a[56:57], v117 offset:49152\n\t"
      "v_mfma_f32_16x16x32_bf16 v[16:19], v[4:7], v[40:43], v[16:19]\n\t"
      "ds_read_b128 v[84:87], v119 offset:16384\n\t"
      "v_cvt_pk_bf16_f32 v162, v24, v25\n\t"
      "v_mfma_f32_16x16x32_bf16 v[20:23], v[4:7], v[44:47], v[20:23]\n\t"
      "s_add_u32 m0, s55, 132096\n\t"
      "s_xor_b32 s61, s47, 1081344\n\t"
      "buffer_load_dwordx4 v116, s[88:91], s61 offen lds\n\t"
      "ds_read_b64_tr_b16 a[58:59], v117 offset:50176\n\t"
      "v_cvt_pk_bf16_f32 v163, v26, v27\n\t"
      "v_mfma_f32_16x16x32_bf16 v[16:19], v[8:11], v[48:51], v[16:19]\n\t"
      "ds_read_b128 v[88:91], v102 offset:16384\n\t"
      "v_cvt_pk_bf16_f32 v226, v28, v29\n\t"
      "v_mfma_f32_16x16x32_bf16 v[20:23], v[8:11], v[52:55], v[20:23]\n\t"
      "s_add_u32 m0, s55, 139264\n\t"
      "s_xor_b32 s61, s47, 1310720\n\t"
      "buffer_load_dwordx4 v116, s[88:91], s61 offen lds\n\t"
      "ds_read_b64_tr_b16 a[120:121], v118 offset:49152\n\t"
      "v_cvt_pk_bf16_f32 v227, v30, v31\n\t"
      "v_mfma_f32_16x16x32_bf16 v[16:19], v[12:15], v[56:59], v[16:19]\n\t"
      "ds_read_b128 v[92:95], v103 offset:16384\n\t"
      "v_mfma_f32_16x16x32_bf16 v[20:23], v[12:15], v[60:63], v[20:23]\n\t"
      "s_add_u32 m0, s55, 140288\n\t"
      "s_xor_b32 s61, s47, 1343488\n\t"
      "buffer_load_dwordx4 v116, s[88:91], s61 offen lds\n\t"
      "ds_read_b64_tr_b16 a[122:123], v118 offset:50176\n\t"
      "v_mfma_f32_16x16x32_bf16 a[168:171], v[160:163], v[160:163], a[168:171]\n\t"
      "v_mfma_f32_16x16x32_bf16 v[24:27], v[64:67], v[32:35], 0\n\t"
      "ds_read_b128 v[0:3], v104 offset:17408\n\t"
      "v_mfma_f32_16x16x32_bf16 v[28:31], v[64:67], v[36:39], 0\n\t"
      "ds_read_b64_tr_b16 a[60:61], v117 offset:57344\n\t"
      "ds_read_b64_tr_b16 a[62:63], v117 offset:58368\n\t"
      "v_mfma_f32_16x16x32_bf16 v[24:27], v[68:71], v[40:43], v[24:27]\n\t"
      "ds_read_b128 v[4:7], v119 offset:17408\n\t"
      "v_cvt_pk_bf16_f32 v164, v16, v17\n\t"
      "v_mfma_f32_16x16x32_bf16 v[28:31], v[68:71], v[44:47], v[28:31]\n\t"
      "ds_read_b64_tr_b16 a[124:125], v118 offset:57344\n\t"
      "ds_read_b64_tr_b16 a[126:127], v118 offset:58368\n\t"
      "v_cvt_pk_bf16_f32 v165, v18, v19\n\t"
      "v_mfma_f32_16x16x32_bf16 v[24:27], v[72:75], v[48:51], v[24:27]\n\t"
      "ds_read_b128 v[8:11], v102 offset:17408\n\t"
      "v_cvt_pk_bf16_f32 v228, v20, v21\n\t"
      "v_mfma_f32_16x16x32_bf16 v[28:31], v[72:75], v[52:55], v[28:31]\n\t"
      "v_cvt_pk_bf16_f32 v229, v22, v23\n\t"
      "v_mfma_f32_16x16x32_bf16 v[24:27], v[76:79], v[56:59], v[24:27]\n\t"
      "s_waitcnt lgkmcnt(12)\n\t"
      "ds_read_b128 v[12:15], v103 offset:17408\n\t"
      "v_mfma_f32_16x16x32_bf16 v[28:31], v[76:79], v[60:63], v[28:31]\n\t"
      "v_mfma_f32_16x16x32_bf16 a[232:235], v[224:227], v[224:227], a[232:235]\n\t"
      "s_waitcnt vmcnt(10)\n\t"
      "s_waitcnt lgkmcnt(2)\n\t"
      "s_barrier\n\t"
      "v_mfma_f32_16x16x32_bf16 v[16:19], v[80:83], v[32:35], 0\n\t"
      "ds_read_b128 v[64:67], v104 offset:24576\n\t"
      "v_mfma_f32_16x16x32_bf16 v[20:23], v[80:83], v[36:39], 0\n\t"
      "s_add_u32 m0, s55, 147456\n\t"
      "s_xor_b32 s61, s47, 1572864\n\t"
      "buffer_load_dwordx4 v116, s[88:91], s61 offen lds\n\t"
      "ds_read_b64_tr_b16 a[244:245], v106 offset:16384\n\t"
      "v_mfma_f32_16x16x32_bf16 v[16:19], v[84:87], v[40:43], v[16:19]\n\t"
      "ds_read_b128 v[68:71], v119 offset:24576\n\t"
      "v_cvt_pk_bf16_f32 v166, v24, v25\n\t"
      "v_mfma_f32_16x16x32_bf16 v[20:23], v[84:87], v[44:47], v[20:23]\n\t"
      "s_add_u32 m0, s55, 148480\n\t"
      "s_xor_b32 s61, s47, 1605632\n\t"
      "buffer_load_dwordx4 v116, s[88:91], s61 offen lds\n\t"
      "ds_read_b64_tr_b16 a[246:247], v106 offset:17408\n\t"
      "v_cvt_pk_bf16_f32 v167, v26, v27\n\t"
      "v_mfma_f32_16x16x32_bf16 v[16:19], v[88:91], v[48:51], v[16:19]\n\t"
      "ds_read_b128 v[72:75], v102 offset:24576\n\t"
      "v_cvt_pk_bf16_f32 v230, v28, v29\n\t"
      "v_mfma_f32_16x16x32_bf16 v[20:23], v[88:91], v[52:55], v[20:23]\n\t"
      "s_add_u32 m0, s55, 155648\n\t"
      "s_xor_b32 s61, s47, 1835008\n\t"
      "buffer_load_dwordx4 v116, s[88:91], s61 offen lds\n\t"
      "ds_read_b64_tr_b16 a[128:129], v117\n\t"
      "v_cvt_pk_bf16_f32 v231, v30, v31\n\t"
      "v_mfma_f32_16x16x32_bf16 v[16:19], v[92:95], v[56:59], v[16:19]\n\t"
      "ds_read_b128 v[76:79], v103 offset:24576\n\t"
      "v_mfma_f32_16x16x32_bf16 v[20:23], v[92:95], v[60:63], v[20:23]\n\t"
      "s_add_u32 m0, s55, 156672\n\t"
      "s_xor_b32 s61, s47, 1867776\n\t"
      "buffer_load_dwordx4 v116, s[88:91], s61 offen lds\n\t"
      "ds_read_b64_tr_b16 a[130:131], v117 offset:1024\n\t"
      "v_mfma_f32_16x16x32_bf16 a[168:171], v[164:167], v[164:167], a[168:171]\n\t"
      "v_mfma_f32_16x16x32_bf16 v[24:27], v[0:3], v[32:35], 0\n\t"
      "ds_read_b128 v[80:83], v104 offset:25600\n\t"
      "v_mfma_f32_16x16x32_bf16 v[28:31], v[0:3], v[36:39], 0\n\t"
      "ds_read_b64_tr_b16 a[192:193], v118\n\t"
      "ds_read_b64_tr_b16 a[194:195], v118 offset:1024\n\t"
      "v_mfma_f32_16x16x32_bf16 v[24:27], v[4:7], v[40:43], v[24:27]\n\t"
      "ds_read_b128 v[84:87], v119 offset:25600\n\t"
      "v_cvt_pk_bf16_f32 v168, v16, v17\n\t"
      "v_mfma_f32_16x16x32_bf16 v[28:31], v[4:7], v[44:47], v[28:31]\n\t"
      "ds_read_b64_tr_b16 a[132:133], v117 offset:8192\n\t"
      "s_waitcnt lgkmcnt(12)\n\t"
      "ds_read_b64_tr_b16 a[134:135], v117 offset:9216\n\t"
      "v_cvt_pk_bf16_f32 v169, v18, v19\n\t"
      "v_mfma_f32_16x16x32_bf16 v[24:27], v[8:11], v[48:51], v[24:27]\n\t"
      "ds_read_b128 v[88:91], v102 offset:25600\n\t"
      "v_cvt_pk_bf16_f32 v232, v20, v21\n\t"
      "v_mfma_f32_16x16x32_bf16 v[28:31], v[8:11], v[52:55], v[28:31]\n\t"
      "ds_read_b64_tr_b16 a[196:197], v118 offset:8192\n\t"
      "s_waitcnt lgkmcnt(12)\n\t"
      "ds_read_b64_tr_b16 a[198:199], v118 offset:9216\n\t"
      "v_cvt_pk_bf16_f32 v233, v22, v23\n\t"
      "v_mfma_f32_16x16x32_bf16 v[24:27], v[12:15], v[56:59], v[24:27]\n\t"
      "ds_read_b128 v[92:95], v103 offset:25600\n\t"
      "v_mfma_f32_16x16x32_bf16 v[28:31], v[12:15], v[60:63], v[28:31]\n\t"
      "v_mfma_f32_16x16x32_bf16 a[232:235], v[228:231], v[228:231], a[232:235]\n\t"
      "s_waitcnt vmcnt(8)\n\t"
      "s_waitcnt lgkmcnt(1)\n\t"
      "s_barrier\n\t"
      "v_mfma_f32_16x16x32_bf16 v[16:19], v[64:67], v[32:35], 0\n\t"
      "ds_read_b128 v[0:3], v104 offset:32768\n\t"
      "v_mfma_f32_16x16x32_bf16 v[20:23], v[64:67], v[36:39], 0\n\t"
      "s_add_u32 m0, s55, 98304\n\t"
      "s_xor_b32 s61, s47, 2097152\n\t"
      "buffer_load_dwordx4 v116, s[88:91], s61 offen lds\n\t"
      "ds_read_b64_tr_b16 a[136:137], v117 offset:16384\n\t"
      "v_mfma_f32_16x16x32_bf16 v[16:19], v[68:71], v[40:43], v[16:19]\n\t"
      "ds_read_b128 v[4:7], v119 offset:32768\n\t"
      "v_cvt_pk_bf16_f32 v170, v24, v25\n\t"
      "v_mfma_f32_16x16x32_bf16 v[20:23], v[68:71], v[44:47], v[20:23]\n\t"
      "s_add_u32 m0, s55, 99328\n\t"
      "s_xor_b32 s61, s47, 2129920\n\t"
      "buffer_load_dwordx4 v116, s[88:91], s61 offen lds\n\t"
      "ds_read_b64_tr_b16 a[138:139], v117 offset:17408\n\t"
      "v_cvt_pk_bf16_f32 v171, v26, v27\n\t"
      "v_mfma_f32_16x16x32_bf16 v[16:19], v[72:75], v[48:51], v[16:19]\n\t"
      "ds_read_b128 v[8:11], v102 offset:32768\n\t"
      "v_cvt_pk_bf16_f32 v234, v28, v29\n\t"
      "v_mfma_f32_16x16x32_bf16 v[20:23], v[72:75], v[52:55], v[20:23]\n\t"
      "s_add_u32 m0, s55, 106496\n\t"
      "s_xor_b32 s61, s47, 2359296\n\t"
      "buffer_load_dwordx4 v116, s[88:91], s61 offen lds\n\t"
      "ds_read_b64_tr_b16 a[200:201], v118 offset:16384\n\t"
      "v_cvt_pk_bf16_f32 v235, v30, v31\n\t"
      "v_mfma_f32_16x16x32_bf16 v[16:19], v[76:79], v[56:59], v[16:19]\n\t"
      "ds_read_b128 v[12:15], v103 offset:32768\n\t"
      "v_mfma_f32_16x16x32_bf16 v[20:23], v[76:79], v[60:63], v[20:23]\n\t"
      "s_add_u32 m0, s55, 107520\n\t"
      "s_xor_b32 s61, s47, 2392064\n\t"
      "buffer_load_dwordx4 v116, s[88:91], s61 offen lds\n\t"
      "ds_read_b64_tr_b16 a[202:203], v118 offset:17408\n\t"
      "v_mfma_f32_16x16x32_bf16 a[168:171], v[168:171], v[168:171], a[168:171]\n\t"
      "v_mfma_f32_16x16x32_bf16 v[24:27], v[80:83], v[32:35], 0\n\t"
      "ds_read_b128 v[64:67], v104 offset:33792\n\t"
      "v_mfma_f32_16x16x32_bf16 v[28:31], v[80:83], v[36:39], 0\n\t"
      "ds_read_b64_tr_b16 a[140:141], v117 offset:24576\n\t"
      "ds_read_b64_tr_b16 a[142:143], v117 offset:25600\n\t"
      "v_mfma_f32_16x16x32_bf16 v[24:27], v[84:87], v[40:43], v[24:27]\n\t"
      "ds_read_b128 v[68:71], v119 offset:33792\n\t"
      "v_cvt_pk_bf16_f32 v172, v16, v17\n\t"
      "v_mfma_f32_16x16x32_bf16 v[28:31], v[84:87], v[44:47], v[28:31]\n\t"
      "ds_read_b64_tr_b16 a[204:205], v118 offset:24576\n\t"
      "ds_read_b64_tr_b16 a[206:207], v118 offset:25600\n\t"
      "v_cvt_pk_bf16_f32 v173, v18, v19\n\t"
      "v_mfma_f32_16x16x32_bf16 v[24:27], v[88:91], v[48:51], v[24:27]\n\t"
      "s_waitcnt lgkmcnt(12)\n\t"
      "ds_read_b128 v[72:75], v102 offset:33792\n\t"
      "v_cvt_pk_bf16_f32 v236, v20, v21\n\t"
      "v_mfma_f32_16x16x32_bf16 v[28:31], v[88:91], v[52:55], v[28:31]\n\t"
      "v_cvt_pk_bf16_f32 v237, v22, v23\n\t"
      "v_mfma_f32_16x16x32_bf16 v[24:27], v[92:95], v[56:59], v[24:27]\n\t"
      "ds_read_b128 v[76:79], v103 offset:33792\n\t"
      "v_mfma_f32_16x16x32_bf16 v[28:31], v[92:95], v[60:63], v[28:31]\n\t"
      "v_mfma_f32_16x16x32_bf16 a[232:235], v[232:235], v[232:235], a[232:235]\n\t"
      "s_waitcnt vmcnt(8)\n\t"
      "s_waitcnt lgkmcnt(2)\n\t"
      "s_barrier\n\t"
      "v_mfma_f32_16x16x32_bf16 v[16:19], v[0:3], v[32:35], 0\n\t"
      "ds_read_b128 v[80:83], v104 offset:40960\n\t"
      "v_mfma_f32_16x16x32_bf16 v[20:23], v[0:3], v[36:39], 0\n\t"
      "s_add_u32 m0, s55, 114688\n\t"
      "s_xor_b32 s61, s47, 2621440\n\t"
      "buffer_load_dwordx4 v116, s[88:91], s61 offen lds\n\t"
      "ds_read_b64_tr_b16 a[248:249], v106 offset:32768\n\t"
      "v_mfma_f32_16x16x32_bf16 v[16:19], v[4:7], v[40:43], v[16:19]\n\t"
      "ds_read_b128 v[84:87], v119 offset:40960\n\t"
      "v_cvt_pk_bf16_f32 v174, v24, v25\n\t"
      "v_mfma_f32_16x16x32_bf16 v[20:23], v[4:7], v[44:47], v[20:23]\n\t"
      "s_add_u32 m0, s55, 115712\n\t"
      "s_xor_b32 s61, s47, 2654208\n\t"
      "buffer_load_dwordx4 v116, s[88:91], s61 offen lds\n\t"
      "ds_read_b64_tr_b16 a[250:251], v106 offset:33792\n\t"
      "v_cvt_pk_bf16_f32 v175, v26, v27\n\t"
      "v_mfma_f32_16x16x32_bf16 v[16:19], v[8:11], v[48:51], v[16:19]\n\t"
      "ds_read_b128 v[88:91], v102 offset:40960\n\t"
      "v_cvt_pk_bf16_f32 v238, v28, v29\n\t"
      "v_mfma_f32_16x16x32_bf16 v[20:23], v[8:11], v[52:55], v[20:23]\n\t"
      "s_add_u32 m0, s55, 122880\n\t"
      "s_xor_b32 s61, s47, 2883584\n\t"
      "buffer_load_dwordx4 v116, s[88:91], s61 offen lds\n\t"
      "ds_read_b64_tr_b16 a[144:145], v117 offset:32768\n\t"
      "v_cvt_pk_bf16_f32 v239, v30, v31\n\t"
      "v_mfma_f32_16x16x32_bf16 v[16:19], v[12:15], v[56:59], v[16:19]\n\t"
      "ds_read_b128 v[92:95], v103 offset:40960\n\t"
      "v_mfma_f32_16x16x32_bf16 v[20:23], v[12:15], v[60:63], v[20:23]\n\t"
      "s_add_u32 m0, s55, 123904\n\t"
      "s_xor_b32 s61, s47, 2916352\n\t"
      "buffer_load_dwordx4 v116, s[88:91], s61 offen lds\n\t"
      "ds_read_b64_tr_b16 a[146:147], v117 offset:33792\n\t"
      "v_mfma_f32_16x16x32_bf16 a[168:171], v[172:175], v[172:175], a[168:171]\n\t"
      "v_mfma_f32_16x16x32_bf16 v[24:27], v[64:67], v[32:35], 0\n\t"
      "ds_read_b128 v[0:3], v104 offset:41984\n\t"
      "v_mfma_f32_16x16x32_bf16 v[28:31], v[64:67], v[36:39], 0\n\t"
      "ds_read_b64_tr_b16 a[208:209], v118 offset:32768\n\t"
      "ds_read_b64_tr_b16 a[210:211], v118 offset:33792\n\t"
      "v_mfma_f32_16x16x32_bf16 v[24:27], v[68:71], v[40:43], v[24:27]\n\t"
      "ds_read_b128 v[4:7], v119 offset:41984\n\t"
      "v_cvt_pk_bf16_f32 v176, v16, v17\n\t"
      "v_mfma_f32_16x16x32_bf16 v[28:31], v[68:71], v[44:47], v[28:31]\n\t"
      "ds_read_b64_tr_b16 a[148:149], v117 offset:40960\n\t"
      "s_waitcnt lgkmcnt(12)\n\t"
      "ds_read_b64_tr_b16 a[150:151], v117 offset:41984\n\t"
      "v_cvt_pk_bf16_f32 v177, v18, v19\n\t"
      "v_mfma_f32_16x16x32_bf16 v[24:27], v[72:75], v[48:51], v[24:27]\n\t"
      "ds_read_b128 v[8:11], v102 offset:41984\n\t"
      "v_cvt_pk_bf16_f32 v240, v20, v21\n\t"
      "v_mfma_f32_16x16x32_bf16 v[28:31], v[72:75], v[52:55], v[28:31]\n\t"
      "ds_read_b64_tr_b16 a[212:213], v118 offset:40960\n\t"
      "s_waitcnt lgkmcnt(12)\n\t"
      "ds_read_b64_tr_b16 a[214:215], v118 offset:41984\n\t"
      "v_cvt_pk_bf16_f32 v241, v22, v23\n\t"
      "v_mfma_f32_16x16x32_bf16 v[24:27], v[76:79], v[56:59], v[24:27]\n\t"
      "ds_read_b128 v[12:15], v103 offset:41984\n\t"
      "v_mfma_f32_16x16x32_bf16 v[28:31], v[76:79], v[60:63], v[28:31]\n\t"
      "v_mfma_f32_16x16x32_bf16 a[232:235], v[236:239], v[236:239], a[232:235]\n\t"
      "s_waitcnt vmcnt(8)\n\t"
      "s_waitcnt lgkmcnt(1)\n\t"
      "s_barrier\n\t"
      "v_mfma_f32_16x16x32_bf16 v[16:19], v[80:83], v[32:35], 0\n\t"
      "ds_read_b128 v[64:67], v104 offset:49152\n\t"
      "v_mfma_f32_16x16x32_bf16 v[20:23], v[80:83], v[36:39], 0\n\t"
      "s_add_u32 m0, s55, 131072\n\t"
      "s_xor_b32 s61, s47, 3145728\n\t"
      "buffer_load_dwordx4 v116, s[88:91], s61 offen lds\n\t"
      "ds_read_b64_tr_b16 a[152:153], v117 offset:49152\n\t"
      "v_mfma_f32_16x16x32_bf16 v[16:19], v[84:87], v[40:43], v[16:19]\n\t"
      "ds_read_b128 v[68:71], v119 offset:49152\n\t"
      "v_cvt_pk_bf16_f32 v178, v24, v25\n\t"
      "v_mfma_f32_16x16x32_bf16 v[20:23], v[84:87], v[44:47], v[20:23]\n\t"
      "s_add_u32 m0, s55, 132096\n\t"
      "s_xor_b32 s61, s47, 3178496\n\t"
      "buffer_load_dwordx4 v116, s[88:91], s61 offen lds\n\t"
      "ds_read_b64_tr_b16 a[154:155], v117 offset:50176\n\t"
      "v_cvt_pk_bf16_f32 v179, v26, v27\n\t"
      "v_mfma_f32_16x16x32_bf16 v[16:19], v[88:91], v[48:51], v[16:19]\n\t"
      "ds_read_b128 v[72:75], v102 offset:49152\n\t"
      "v_cvt_pk_bf16_f32 v242, v28, v29\n\t"
      "v_mfma_f32_16x16x32_bf16 v[20:23], v[88:91], v[52:55], v[20:23]\n\t"
      "s_add_u32 m0, s55, 139264\n\t"
      "s_xor_b32 s61, s47, 3407872\n\t"
      "buffer_load_dwordx4 v116, s[88:91], s61 offen lds\n\t"
      "ds_read_b64_tr_b16 a[216:217], v118 offset:49152\n\t"
      "v_cvt_pk_bf16_f32 v243, v30, v31\n\t"
      "v_mfma_f32_16x16x32_bf16 v[16:19], v[92:95], v[56:59], v[16:19]\n\t"
      "ds_read_b128 v[76:79], v103 offset:49152\n\t"
      "v_mfma_f32_16x16x32_bf16 v[20:23], v[92:95], v[60:63], v[20:23]\n\t"
      "s_add_u32 m0, s55, 140288\n\t"
      "s_xor_b32 s61, s47, 3440640\n\t"
      "buffer_load_dwordx4 v116, s[88:91], s61 offen lds\n\t"
      "ds_read_b64_tr_b16 a[218:219], v118 offset:50176\n\t"
      "v_mfma_f32_16x16x32_bf16 a[168:171], v[176:179], v[176:179], a[168:171]\n\t"
      "v_mfma_f32_16x16x32_bf16 v[24:27], v[0:3], v[32:35], 0\n\t"
      "ds_read_b128 v[80:83], v104 offset:50176\n\t"
      "v_mfma_f32_16x16x32_bf16 v[28:31], v[0:3], v[36:39], 0\n\t"
      "ds_read_b64_tr_b16 a[156:157], v117 offset:57344\n\t"
      "ds_read_b64_tr_b16 a[158:159], v117 offset:58368\n\t"
      "v_mfma_f32_16x16x32_bf16 v[24:27], v[4:7], v[40:43], v[24:27]\n\t"
      "ds_read_b128 v[84:87], v119 offset:50176\n\t"
      "v_cvt_pk_bf16_f32 v180, v16, v17\n\t"
      "v_mfma_f32_16x16x32_bf16 v[28:31], v[4:7], v[44:47], v[28:31]\n\t"
      "ds_read_b64_tr_b16 a[220:221], v118 offset:57344\n\t"
      "ds_read_b64_tr_b16 a[222:223], v118 offset:58368\n\t"
      "v_cvt_pk_bf16_f32 v181, v18, v19\n\t"
      "v_mfma_f32_16x16x32_bf16 v[24:27], v[8:11], v[48:51], v[24:27]\n\t"
      "s_waitcnt lgkmcnt(12)\n\t"
      "ds_read_b128 v[88:91], v102 offset:50176\n\t"
      "v_cvt_pk_bf16_f32 v244, v20, v21\n\t"
      "v_mfma_f32_16x16x32_bf16 v[28:31], v[8:11], v[52:55], v[28:31]\n\t"
      "v_cvt_pk_bf16_f32 v245, v22, v23\n\t"
      "v_mfma_f32_16x16x32_bf16 v[24:27], v[12:15], v[56:59], v[24:27]\n\t"
      "ds_read_b128 v[92:95], v103 offset:50176\n\t"
      "v_mfma_f32_16x16x32_bf16 v[28:31], v[12:15], v[60:63], v[28:31]\n\t"
      "v_mfma_f32_16x16x32_bf16 a[232:235], v[240:243], v[240:243], a[232:235]\n\t"
      "s_waitcnt vmcnt(8)\n\t"
      "s_waitcnt lgkmcnt(2)\n\t"
      "s_barrier\n\t"
      "v_mfma_f32_16x16x32_bf16 v[16:19], v[64:67], v[32:35], 0\n\t"
      "ds_read_b128 v[0:3], v104 offset:57344\n\t"
      "v_mfma_f32_16x16x32_bf16 v[20:23], v[64:67], v[36:39], 0\n\t"
      "s_add_u32 m0, s55, 147456\n\t"
      "s_xor_b32 s61, s47, 3670016\n\t"
      "buffer_load_dwordx4 v116, s[88:91], s61 offen lds\n\t"
      "ds_read_b64_tr_b16 a[252:253], v106 offset:49152\n\t"
      "v_mfma_f32_16x16x32_bf16 v[16:19], v[68:71], v[40:43], v[16:19]\n\t"
      "ds_read_b128 v[4:7], v119 offset:57344\n\t"
      "v_cvt_pk_bf16_f32 v182, v24, v25\n\t"
      "v_mfma_f32_16x16x32_bf16 v[20:23], v[68:71], v[44:47], v[20:23]\n\t"
      "s_add_u32 m0, s55, 148480\n\t"
      "s_xor_b32 s61, s47, 3702784\n\t"
      "buffer_load_dwordx4 v116, s[88:91], s61 offen lds\n\t"
      "ds_read_b64_tr_b16 a[254:255], v106 offset:50176\n\t"
      "v_cvt_pk_bf16_f32 v183, v26, v27\n\t"
      "v_mfma_f32_16x16x32_bf16 v[16:19], v[72:75], v[48:51], v[16:19]\n\t"
      "ds_read_b128 v[8:11], v102 offset:57344\n\t"
      "v_cvt_pk_bf16_f32 v246, v28, v29\n\t"
      "v_mfma_f32_16x16x32_bf16 v[20:23], v[72:75], v[52:55], v[20:23]\n\t"
      "s_add_u32 m0, s55, 155648\n\t"
      "s_xor_b32 s61, s47, 3932160\n\t"
      "buffer_load_dwordx4 v116, s[88:91], s61 offen lds\n\t"
      "ds_read_b64_tr_b16 a[160:161], v117\n\t"
      "v_cvt_pk_bf16_f32 v247, v30, v31\n\t"
      "v_mfma_f32_16x16x32_bf16 v[16:19], v[76:79], v[56:59], v[16:19]\n\t"
      "ds_read_b128 v[12:15], v103 offset:57344\n\t"
      "v_mfma_f32_16x16x32_bf16 v[20:23], v[76:79], v[60:63], v[20:23]\n\t"
      "s_add_u32 m0, s55, 156672\n\t"
      "s_xor_b32 s61, s47, 3964928\n\t"
      "buffer_load_dwordx4 v116, s[88:91], s61 offen lds\n\t"
      "ds_read_b64_tr_b16 a[162:163], v117 offset:1024\n\t"
      "v_mfma_f32_16x16x32_bf16 a[168:171], v[180:183], v[180:183], a[168:171]\n\t"
      "v_mfma_f32_16x16x32_bf16 v[24:27], v[80:83], v[32:35], 0\n\t"
      "ds_read_b128 v[64:67], v104 offset:58368\n\t"
      "v_mfma_f32_16x16x32_bf16 v[28:31], v[80:83], v[36:39], 0\n\t"
      "ds_read_b64_tr_b16 a[224:225], v118\n\t"
      "ds_read_b64_tr_b16 a[226:227], v118 offset:1024\n\t"
      "v_mfma_f32_16x16x32_bf16 v[24:27], v[84:87], v[40:43], v[24:27]\n\t"
      "ds_read_b128 v[68:71], v119 offset:58368\n\t"
      "v_cvt_pk_bf16_f32 v184, v16, v17\n\t"
      "v_mfma_f32_16x16x32_bf16 v[28:31], v[84:87], v[44:47], v[28:31]\n\t"
      "ds_read_b64_tr_b16 a[164:165], v117 offset:8192\n\t"
      "s_waitcnt lgkmcnt(12)\n\t"
      "ds_read_b64_tr_b16 a[166:167], v117 offset:9216\n\t"
      "v_cvt_pk_bf16_f32 v185, v18, v19\n\t"
      "v_mfma_f32_16x16x32_bf16 v[24:27], v[88:91], v[48:51], v[24:27]\n\t"
      "ds_read_b128 v[72:75], v102 offset:58368\n\t"
      "v_cvt_pk_bf16_f32 v248, v20, v21\n\t"
      "v_mfma_f32_16x16x32_bf16 v[28:31], v[88:91], v[52:55], v[28:31]\n\t"
      "ds_read_b64_tr_b16 a[228:229], v118 offset:8192\n\t"
      "s_waitcnt lgkmcnt(12)\n\t"
      "ds_read_b64_tr_b16 a[230:231], v118 offset:9216\n\t"
      "v_cvt_pk_bf16_f32 v249, v22, v23\n\t"
      "v_mfma_f32_16x16x32_bf16 v[24:27], v[92:95], v[56:59], v[24:27]\n\t"
      "ds_read_b128 v[76:79], v103 offset:58368\n\t"
      "v_mfma_f32_16x16x32_bf16 v[28:31], v[92:95], v[60:63], v[28:31]\n\t"
      "v_mfma_f32_16x16x32_bf16 a[232:235], v[244:247], v[244:247], a[232:235]\n\t"
      "v_mfma_f32_16x16x32_bf16 v[16:19], v[0:3], v[32:35], 0\n\t"
      "v_mfma_f32_16x16x32_bf16 v[20:23], v[0:3], v[36:39], 0\n\t"
      "v_mfma_f32_16x16x32_bf16 v[16:19], v[4:7], v[40:43], v[16:19]\n\t"
      "v_cvt_pk_bf16_f32 v186, v24, v25\n\t"
      "v_mfma_f32_16x16x32_bf16 v[20:23], v[4:7], v[44:47], v[20:23]\n\t"
      "v_cvt_pk_bf16_f32 v187, v26, v27\n\t"
      "s_waitcnt lgkmcnt(13)\n\t"
      "v_mfma_f32_16x16x32_bf16 v[16:19], v[8:11], v[48:51], v[16:19]\n\t"
      "v_cvt_pk_bf16_f32 v250, v28, v29\n\t"
      "v_mfma_f32_16x16x32_bf16 v[20:23], v[8:11], v[52:55], v[20:23]\n\t"
      "v_cvt_pk_bf16_f32 v251, v30, v31\n\t"
      "s_waitcnt lgkmcnt(11)\n\t"
      "v_mfma_f32_16x16x32_bf16 v[16:19], v[12:15], v[56:59], v[16:19]\n\t"
      "v_mfma_f32_16x16x32_bf16 v[20:23], v[12:15], v[60:63], v[20:23]\n\t"
      "v_mfma_f32_16x16x32_bf16 a[168:171], v[184:187], v[184:187], a[168:171]\n\t"
      "buffer_load_dwordx4 v[96:99], off, s[36:39], s54 sc1\n\t"
      "s_waitcnt lgkmcnt(9)\n\t"
      "v_mfma_f32_16x16x32_bf16 v[24:27], v[64:67], v[32:35], 0\n\t"
      "v_mfma_f32_16x16x32_bf16 v[28:31], v[64:67], v[36:39], 0\n\t"
      "s_waitcnt lgkmcnt(6)\n\t"
      "v_mfma_f32_16x16x32_bf16 v[24:27], v[68:71], v[40:43], v[24:27]\n\t"
      "v_cvt_pk_bf16_f32 v188, v16, v17\n\t"
      "v_mfma_f32_16x16x32_bf16 v[28:31], v[68:71], v[44:47], v[28:31]\n\t"
      "v_cvt_pk_bf16_f32 v189, v18, v19\n\t"
      "s_waitcnt lgkmcnt(3)\n\t"
      "v_mfma_f32_16x16x32_bf16 v[24:27], v[72:75], v[48:51], v[24:27]\n\t"
      "v_cvt_pk_bf16_f32 v252, v20, v21\n\t"
      "v_mfma_f32_16x16x32_bf16 v[28:31], v[72:75], v[52:55], v[28:31]\n\t"
      "v_cvt_pk_bf16_f32 v253, v22, v23\n\t"
      "s_waitcnt lgkmcnt(0)\n\t"
      "v_mfma_f32_16x16x32_bf16 v[24:27], v[76:79], v[56:59], v[24:27]\n\t"
      "v_mfma_f32_16x16x32_bf16 v[28:31], v[76:79], v[60:63], v[28:31]\n\t"
      "v_mfma_f32_16x16x32_bf16 a[232:235], v[248:251], v[248:251], a[232:235]\n\t"
      "v_lshl_add_u32 v8, s41, 10, v127\n\t"
      "v_add_u32 v8, 65536, v8\n\t"
      "v_mov_b32 v12, 0\n\t"
      "v_mov_b32 v9, s41\n\t"
      "v_lshlrev_b32 v9, 2, v9\n\t"
      "v_cvt_pk_bf16_f32 v190, v24, v25\n\t"
      "v_cvt_pk_bf16_f32 v191, v26, v27\n\t"
      "v_cvt_pk_bf16_f32 v254, v28, v29\n\t"
      "v_cvt_pk_bf16_f32 v255, v30, v31\n\t"
      "s_nop 1\n\t"
      "v_mfma_f32_16x16x32_bf16 a[168:171], v[188:191], v[188:191], a[168:171]\n\t"
      "v_mfma_f32_16x16x32_bf16 a[232:235], v[252:255], v[252:255], a[232:235]\n\t"
      "s_sub_u32 s64, s46, 786432\n\t"
      "s_mov_b32 s71, 98304\n\t"
      "s_mov_b32 s63, 0x10000\n\t"
      "s_nop 4\n\t"
      "v_accvgpr_read_b32 v0, a168\n\t"
      "v_accvgpr_read_b32 v1, a169\n\t"
      "v_accvgpr_read_b32 v2, a170\n\t"
      "v_accvgpr_read_b32 v3, a171\n\t"
      "v_accvgpr_read_b32 v4, a232\n\t"
      "v_accvgpr_read_b32 v5, a233\n\t"
      "v_accvgpr_read_b32 v6, a234\n\t"
      "v_accvgpr_read_b32 v7, a235\n\t"
      "v_and_b32 v0, 0x7fffffff, v0\n\t"
      "v_and_b32 v1, 0x7fffffff, v1\n\t"
      "v_and_b32 v2, 0x7fffffff, v2\n\t"
      "v_and_b32 v3, 0x7fffffff, v3\n\t"
      "v_and_b32 v4, 0x7fffffff, v4\n\t"
      "v_and_b32 v5, 0x7fffffff, v5\n\t"
      "v_and_b32 v6, 0x7fffffff, v6\n\t"
      "v_and_b32 v7, 0x7fffffff, v7\n\t"
      "v_max3_u32 v0, v0, v1, v2\n\t"
      "v_max3_u32 v3, v3, v4, v5\n\t"
      "v_max3_u32 v0, v0, v6, v7\n\t"
      "v_max_u32 v0, v0, v3\n\t"
      "s_nop 1\n\t"
      "v_max_u32_dpp v0, v0, v0 quad_perm:[1,0,3,2] row_mask:0xf bank_mask:0xf\n\t"
      "s_nop 1\n\t"
      "v_max_u32_dpp v0, v0, v0 quad_perm:[2,3,0,1] row_mask:0xf bank_mask:0xf\n\t"
      "s_nop 1\n\t"
      "v_max_u32_dpp v0, v0, v0 row_ror:4 row_mask:0xf bank_mask:0xf\n\t"
      "s_nop 1\n\t"
      "v_max_u32_dpp v0, v0, v0 row_ror:8 row_mask:0xf bank_mask:0xf\n\t"
      "v_mov_b32 v1, v0\n\t"
      "s_nop 1\n\t"
      "v_permlane16_swap_b32 v0, v1\n\t"
      "v_max_u32 v0, v0, v1\n\t"
      "v_mov_b32 v1, v0\n\t"
      "s_nop 1\n\t"
      "v_permlane32_swap_b32 v0, v1\n\t"
      "v_max_u32 v0, v0, v1\n\t"
      "ds_write_b32 v9, v0 offset:32768\n\t"
      "s_waitcnt lgkmcnt(0)\n\t"
      "s_barrier\n\t"
      "ds_read_b128 v[4:7], v12 offset:32768\n\t"
      "s_waitcnt lgkmcnt(0)\n\t"
      "v_max_u32 v4, v4, v5\n\t"
      "v_max3_u32 v4, v4, v6, v7\n\t"
      "v_mov_b32 v5, s53\n\t"
      "s_memrealtime s[92:93]\n\t"
      "v_add_u32 v4, 0x10000, v4\n\t"
      "s_mov_b64 exec, 1\n\t"
      "s_mov_b32 s61, 0xffff\n\t"
      "s_waitcnt lgkmcnt(0)\n\t"
      "v_mov_b32 v6, s92\n\t"
      "v_bfi_b32 v4, s61, v6, v4\n\t"
      "buffer_store_dwordx2 v[4:5], off, s[36:39], s95 sc1\n\t"
      "s_mov_b64 exec, -1\n\t"
      "s_waitcnt vmcnt(1)\n\t"
      "v_bitop3_b32 v96, v96, v97, s53 bitop3:0x7e\n\t"
      "v_bitop3_b32 v98, v98, v99, s53 bitop3:0x7e\n\t"
      "v_or_b32 v96, v96, v98\n\t"
      "s_add_u32 m0, s55, 98304\n\t"
      "v_readfirstlane_b32 s56, v96\n\t"
      "s_nop 1\n\t"
      "s_cmp_eq_u32 s56, 0\n\t"
      "s_cbranch_scc0 LF2%=\n\t"
      "buffer_load_dwordx4 v127, s[36:39], s64 offen sc1 lds\n\t"
      "buffer_load_dwordx4 v127, s[36:39], s64 offen offset:1024 sc1 lds\n\t"
      "LF2%=:\n\t"
      "buffer_load_dwordx4 v[96:99], off, s[36:39], s54 sc1\n\t"
      "ds_write_b128 v8, a[176:179]\n\t"
      "ds_write_b128 v8, a[180:183] offset:4096\n\t"
      "ds_write_b128 v8, a[184:187] offset:8192\n\t"
      "ds_write_b128 v8, a[188:191] offset:12288\n\t"
      "ds_write_b128 v8, a[240:243] offset:16384\n\t"
      "ds_write_b128 v8, a[244:247] offset:20480\n\t"
      "ds_write_b128 v8, a[248:251] offset:24576\n\t"
      "ds_write_b128 v8, a[252:255] offset:28672\n\t"
      "ds_write_b128 v100, v[112:115] offset:57344\n\t"
      "s_cbranch_scc1 LF3%=\n\t"
      "LF0%=:\n\t"
      "s_waitcnt vmcnt(0)\n\t"
      "v_bitop3_b32 v96, v96, v97, s53 bitop3:0x7e\n\t"
      "v_bitop3_b32 v98, v98, v99, s53 bitop3:0x7e\n\t"
      "v_or_b32 v96, v96, v98\n\t"
      "s_add_u32 m0, s55, 98304\n\t"
      "v_readfirstlane_b32 s56, v96\n\t"
      "s_nop 1\n\t"
      "s_cmp_eq_u32 s56, 0\n\t"
      "s_cbranch_scc1 LF1%=\n\t"
      "buffer_load_dwordx4 v[96:99], off, s[36:39], s54 sc1\n\t"
      "s_sub_u32 s63, s63, 1\n\t"
      "s_cmp_lg_u32 s63, 0\n\t"
      "s_cbranch_scc1 LF0%=\n\t"
      "LF1%=:\n\t"
      "buffer_load_dwordx4 v127, s[36:39], s64 offen sc1 lds\n\t"
      "buffer_load_dwordx4 v127, s[36:39], s64 offen offset:1024 sc1 lds\n\t"
      "LF3%=:\n\t"
      "v_lshlrev_b32 v91, 4, v127\n\t"
      "s_cmp_eq_u32 s86, 0\n\t"
      "s_cbranch_scc1 LC9%=\n\t"
      "s_waitcnt lgkmcnt(0)\n\t"
      "s_memtime s[96:97]\n\t"
      "s_waitcnt lgkmcnt(0)\n\t"
      "s_buffer_load_dword s87, s[36:39], s86\n\t"
      "s_waitcnt lgkmcnt(0)\n\t"
      "s_memtime s[98:99]\n\t"
      "s_waitcnt lgkmcnt(0)\n\t"
      "LC9%=:\n\t"
      "s_sub_u32 s61, s98, s96\n\t"
      "s_lshr_b32 s61, s61, 2\n\t"
      "s_min_u32 s61, s61, 511\n\t"
      "s_sub_u32 s62, 0, s61\n\t"
      "s_bitcmp1_b32 s86, 13\n\t"
      "s_cselect_b32 s61, s62, s61\n\t"
      "s_add_u32 s65, s61, 1049088\n\t"
      "v_readfirstlane_b32 s97, v121\n\t"
      "v_mov_b32 v65, v115\n\t"
      "v_mov_b32 v64, 0\n\t"
      "v_mov_b32 v66, 0\n\t"
      "v_mov_b32 v67, 0\n\t"
      "v_mov_b32 v109, 0\n\t"
      "v_mov_b32 v110, 0\n\t"
      "v_mov_b32 v111, 0\n\t"
      "s_mov_b32 s40, -1\n\t"
      "s_add_u32 s45, s55, 32768\n\t"
      "v_mov_b32 v126, 0\n\t"
      "s_mov_b32 s56, 0\n\t"
      "s_mov_b32 s58, 1\n\t"
      "s_mov_b32 s59, 0x3e0293ee\n\t"
      "s_mov_b32 s96, 0x40820000\n\t"
      "s_mov_b32 s57, 1.0\n\t"
      "s_add_u32 s68, s43, 8192\n\t"
      "s_add_u32 s97, s97, s43\n\t"
      "s_add_u32 s97, s97, 0x8000\n\t"
      "s_mov_b32 s99, 0\n\t"
      "s_mov_b32 s26, 2460\n\t"
      "s_mov_b32 s25, 0\n\t"
      "s_memtime s[78:79]\n\t"
      "s_waitcnt lgkmcnt(0)\n\t"
      "s_sub_u32 s78, s78, 4000\n\t"
      "s_mov_b32 s84, s78\n\t"
      "s_mov_b32 s88, s78\n\t"
      "s_add_u32 s50, s50, 32\n\t"
      "s_mov_b32 s72, 0xcccccccc\n\t"
      "s_mov_b32 s73, 0xcccccccc\n\t"
      "s_waitcnt vmcnt(0) lgkmcnt(0)\n\t"
      "s_barrier\n\t"
      "s_mov_b32 m0, s45\n\t"
      "s_sub_u32 s61, 204800, s71\n\t"
      "buffer_load_dwordx4 v65, s[36:39], s44 offen sc1 lds\n\t"
      "buffer_load_dwordx4 v65, s[36:39], s44 offen offset:1024 sc1 lds\n\t"
      "s_add_u32 m0, s55, s61\n\t"
      "s_nop 0\n\t"
      "buffer_load_dwordx4 v127, s[36:39], s46 offen sc1 lds\n\t"
      "buffer_load_dwordx4 v127, s[36:39], s46 offen offset:1024 sc1 lds\n\t"
      "buffer_load_dwordx2 v[92:93], v91, s[36:39], s94 offen sc1\n\t"
      "buffer_load_dwordx2 v[94:95], v91, s[36:39], s94 offen offset:128 sc1\n\t"
      "ds_read_b64_tr_b16 a[168:169], v117 offset:16384\n\t"
      "ds_read_b64_tr_b16 a[170:171], v117 offset:17408\n\t"
      "ds_read_b64_tr_b16 a[232:233], v118 offset:16384\n\t"
      "ds_read_b64_tr_b16 a[234:235], v118 offset:17408\n\t"
      "ds_read_b64_tr_b16 a[172:173], v117 offset:24576\n\t"
      "ds_read_b64_tr_b16 a[174:175], v117 offset:25600\n\t"
      "ds_read_b64_tr_b16 a[236:237], v118 offset:24576\n\t"
      "ds_read_b64_tr_b16 a[238:239], v118 offset:25600\n\t"
      "ds_read_b64_tr_b16 a[176:177], v117 offset:32768\n\t"
      "ds_read_b64_tr_b16 a[178:179], v117 offset:33792\n\t"
      "ds_read_b64_tr_b16 a[240:241], v118 offset:32768\n\t"
      "ds_read_b64_tr_b16 a[242:243], v118 offset:33792\n\t"
      "ds_read_b64_tr_b16 a[180:181], v117 offset:40960\n\t"
      "ds_read_b64_tr_b16 a[182:183], v117 offset:41984\n\t"
      "ds_read_b64_tr_b16 a[244:245], v118 offset:40960\n\t"
      "s_waitcnt lgkmcnt(12)\n\t"
      "ds_read_b64_tr_b16 a[246:247], v118 offset:41984\n\t"
      "ds_read_b64_tr_b16 a[184:185], v117 offset:49152\n\t"
      "ds_read_b64_tr_b16 a[186:187], v117 offset:50176\n\t"
      "s_waitcnt lgkmcnt(12)\n\t"
      "ds_read_b64_tr_b16 a[248:249], v118 offset:49152\n\t"
      "ds_read_b64_tr_b16 a[250:251], v118 offset:50176\n\t"
      "ds_read_b64_tr_b16 a[188:189], v117 offset:57344\n\t"
      "s_waitcnt lgkmcnt(12)\n\t"
      "ds_read_b64_tr_b16 a[190:191], v117 offset:58368\n\t"
      "ds_read_b64_tr_b16 a[252:253], v118 offset:57344\n\t"
      "ds_read_b64_tr_b16 a[254:255], v118 offset:58368\n\t"
      "s_waitcnt lgkmcnt(6)\n\t"
      "s_cmp_eq_u32 s41, 1\n\t"
      "s_cbranch_scc1 LWIN_1%=\n\t"
      "s_cmp_eq_u32 s41, 2\n\t"
      "s_cbranch_scc1 LWIN_2%=\n\t"
      "s_cmp_eq_u32 s41, 3\n\t"
      "s_cbranch_scc1 LWIN_3%=\n\t"
      "s_branch LWIN_0%=\n\t"
      ".p2align 6\n\t.fill 16, 4, 0xbf800000\n\t"
      "LPOLL_0%=:\n\t"
      "buffer_load_dwordx4 v[84:87], v60, s[36:39], s43 offen sc1\n\t"
      "buffer_load_dwordx4 v[88:91], v60, s[36:39], s43 offen offset:1024 sc1\n\t"
      "s_waitcnt lgkmcnt(4)\n\t"
      "v_mfma_f32_16x16x32_bf16 v[32:35], v[0:3], v[8:11], v[32:35]\n\t"
      "v_mfma_f32_16x16x32_bf16 v[36:39], v[4:7], v[8:11], v[36:39]\n\t"
      "v_mfma_f32_16x16x32_bf16 v[40:43], v[0:3], v[12:15], v[40:43]\n\t"
      "v_mfma_f32_16x16x32_bf16 v[44:47], v[4:7], v[12:15], v[44:47]\n\t"
      "buffer_load_dwordx4 v[92:95], v60, s[36:39], s68 offen sc1\n\t"
      "buffer_load_dwordx4 v[96:99], v60, s[36:39], s68 offen offset:1024 sc1\n\t"
      "ds_read_b64 v[54:55], v124 offset:53312\n\t"
      "s_xor_b32 s69, s97, 1024\n\t"
      "s_waitcnt lgkmcnt(1)\n\t"
      "v_mfma_f32_16x16x32_bf16 v[32:35], v[76:79], v[112:115], v[32:35]\n\t"
      "v_mfma_f32_16x16x32_bf16 v[36:39], v[80:83], v[112:115], v[36:39]\n\t"
      "v_mfma_f32_16x16x32_bf16 v[40:43], v[76:79], v[116:119], v[40:43]\n\t"
      "v_mfma_f32_16x16x32_bf16 v[44:47], v[80:83], v[116:119], v[44:47]\n\t"
      "s_waitcnt lgkmcnt(0)\n\t"
      "v_add_f32 v48, v48, v54\n\t"
      "v_add_f32_e64 v49, v49, v55\n\t"
      "v_cvt_pk_bf16_f32 v48, v48, v49\n\t"
      "buffer_store_dword v48, v62, s[36:39], s28 offen sc1\n\t"
      "s_mov_b64 exec, 1\n\t"
      "ds_write_b64 v109, v[110:111] offset:55392\n\t"
      "s_mov_b64 exec, -1\n\t"
      "s_nop 4\n\t"
      "v_cvt_pk_bf16_f32 v68, v32, v33\n\t"
      "v_cvt_pk_bf16_f32 v69, v34, v35\n\t"
      "v_cvt_pk_bf16_f32 v70, v36, v37\n\t"
      "v_cvt_pk_bf16_f32 v71, v38, v39\n\t"
      "v_cvt_pk_bf16_f32 v74, v40, v41\n\t"
      "v_cvt_pk_bf16_f32 v75, v42, v43\n\t"
      "v_cvt_pk_bf16_f32 v72, v44, v45\n\t"
      "v_cvt_pk_bf16_f32 v73, v46, v47\n\t"
      "v_mov_b64 v[32:33], 0\n\t"
      "v_mov_b64 v[34:35], 0\n\t"
      "v_mov_b64 v[36:37], 0\n\t"
      "v_mov_b64 v[38:39], 0\n\t"
      "v_mov_b64 v[40:41], 0\n\t"
      "v_mov_b64 v[42:43], 0\n\t"
      "v_mov_b64 v[44:45], 0\n\t"
      "v_mov_b64 v[46:47], 0\n\t"
      "v_mov_b64 v[48:49], 0\n\t"
      "v_mov_b64 v[50:51], 0\n\t"
      "v_mov_b64 v[52:53], 0\n\t"
      "v_mov_b64 v[54:55], 0\n\t"
      "s_nop 0\n\t"
      "s_nop 0\n\t"
      "s_nop 0\n\t"
      "s_nop 0\n\t"
      "s_nop 0\n\t"
      "s_nop 0\n\t"
      "s_nop 0\n\t"
      "s_nop 0\n\t"
      "s_waitcnt vmcnt(4)\n\t"
      "v_bitop3_b32 v64, v84, v85, v86 bitop3:0x80\n\t"
      "v_pk_fma_f32 v[6:7], v[86:87], s[56:57], v[126:127] op_sel:[0,1,0] op_sel_hi:[1,1,0]\n\t"
      "v_pk_fma_f32 v[4:5], v[84:85], s[56:57], v[126:127] op_sel:[0,1,0] op_sel_hi:[1,1,0]\n\t"
      "v_cvt_pk_bf16_f32 v0, v4, v5\n\t"
      "v_cvt_pk_bf16_f32 v1, v6, v7\n\t"
      "ds_write_b64 v127, v[0:1] offset:40960\n\t"
      "s_waitcnt vmcnt(3)\n\t"
      "v_pk_fma_f32 v[8:9], v[88:89], s[56:57], v[126:127] op_sel:[0,1,0] op_sel_hi:[1,1,0]\n\t"
      "v_cvt_pk_bf16_f32 v2, v8, v9\n\t"
      "s_nop 0\n\t"
      "v_pk_fma_f32 v[10:11], v[90:91], s[56:57], v[126:127] op_sel:[0,1,0] op_sel_hi:[1,1,0]\n\t"
      "v_cvt_pk_bf16_f32 v3, v10, v11\n\t"
      "ds_write_b64 v127, v[2:3] offset:40968\n\t"
      "v_bitop3_b32 v64, v64, v87, v88 bitop3:0x80\n\t"
      "v_bitop3_b32 v64, v64, v89, v90 bitop3:0x80\n\t"
      "v_bitop3_b32 v64, v64, v91, s58 bitop3:0x80\n\t"
      "v_cmp_eq_u32_e64 vcc, 0, v64\n\t"
      "v_smfmac_f32_16x16x64_bf16 v[32:35], v[0:3], a[0:7], v122\n\t"
      "v_smfmac_f32_16x16x64_bf16 v[48:51], v[0:3], v[128:135], v122\n\t"
      "s_cbranch_vccnz LR0_0%=\n\t"
      "LC0_0%=:\n\t"
      "v_smfmac_f32_16x16x64_bf16 v[52:55], v[0:3], v[192:199], v122\n\t"
      "s_waitcnt lgkmcnt(0)\n\t"
      "v_smfmac_f32_16x16x64_bf16 v[44:47], v[0:3], a[192:199], v122\n\t"
      "s_barrier\n\t"
      "ds_read_b128 v[4:7], v127 offset:41984\n\t"
      "v_smfmac_f32_16x16x64_bf16 v[36:39], v[0:3], a[64:71], v122\n\t"
      "ds_read_b128 v[12:15], v127 offset:44032\n\t"
      "v_smfmac_f32_16x16x64_bf16 v[40:43], v[0:3], a[128:135], v122\n\t"
      "ds_read_b128 v[8:11], v127 offset:43008\n\t"
      "v_bitop3_b32 v120, v120, v121, s53 bitop3:0x7e\n\t"
      "v_mfma_f32_16x16x32_bf16 v[56:59], v[0:3], v[0:3], 0\n\t"
      "s_waitcnt lgkmcnt(2)\n\t"
      "v_smfmac_f32_16x16x64_bf16 v[40:43], v[4:7], a[136:143], v122\n\t"
      "s_waitcnt vmcnt(2)\n\t"
      "v_fma_f32 v23, v95, s57, v126\n\t"
      "v_fma_f32 v22, v94, s57, v126\n\t"
      "v_smfmac_f32_16x16x64_bf16 v[32:35], v[4:7], a[8:15], v122\n\t"
      "s_nop 0\n\t"
      "v_fma_f32 v20, v92, s57, v126\n\t"
      "v_bitop3_b32 v64, v92, v93, v94 bitop3:0x80\n\t"
      "v_fma_f32 v21, v93, s57, v126\n\t"
      "v_cvt_pk_bf16_f32 v16, v20, v21\n\t"
      "v_smfmac_f32_16x16x64_bf16 v[36:39], v[4:7], a[72:79], v122\n\t"
      "v_cvt_pk_bf16_f32 v17, v22, v23\n\t"
      "v_smfmac_f32_16x16x64_bf16 v[44:47], v[4:7], a[200:207], v122\n\t"
      "s_waitcnt vmcnt(1)\n\t"
      "s_waitcnt lgkmcnt(0)\n\t"
      "v_fma_f32 v25, v97, s57, v126\n\t"
      "v_fma_f32 v27, v99, s57, v126\n\t"
      "v_fma_f32 v24, v96, s57, v126\n\t"
      "v_smfmac_f32_16x16x64_bf16 v[36:39], v[8:11], a[80:87], v122\n\t"
      "ds_write_b64 v127, v[16:17] offset:45056\n\t"
      "v_cvt_pk_bf16_f32 v18, v24, v25\n\t"
      "v_fma_f32 v26, v98, s57, v126\n\t"
      "v_cvt_pk_bf16_f32 v19, v26, v27\n\t"
      "v_smfmac_f32_16x16x64_bf16 v[36:39], v[12:15], a[88:95], v122\n\t"
      "ds_write_b64 v127, v[18:19] offset:45064\n\t"
      "v_smfmac_f32_16x16x64_bf16 v[44:47], v[8:11], a[208:215], v122\n\t"
      "v_bitop3_b32 v64, v64, v95, v96 bitop3:0x80\n\t"
      "v_smfmac_f32_16x16x64_bf16 v[40:43], v[8:11], a[144:151], v122\n\t"
      "v_bitop3_b32 v64, v64, v97, v98 bitop3:0x80\n\t"
      "v_bitop3_b32 v64, v64, v99, s60 bitop3:0x80\n\t"
      "v_smfmac_f32_16x16x64_bf16 v[44:47], v[12:15], a[216:223], v122\n\t"
      "v_cmp_eq_u32_e64 vcc, 0, v64\n\t"
      "v_smfmac_f32_16x16x64_bf16 v[32:35], v[8:11], a[16:23], v122\n\t"
      "s_waitcnt lgkmcnt(0)\n\t"
      "v_smfmac_f32_16x16x64_bf16 v[40:43], v[12:15], a[152:159], v122\n\t"
      "s_cbranch_vccnz LR1_0%=\n\t"
      "LC1_0%=:\n\t"
      "v_mfma_f32_16x16x32_bf16 v[56:59], v[16:19], v[16:19], v[56:59]\n\t"
      "s_barrier\n\t"
      "v_smfmac_f32_16x16x64_bf16 v[40:43], v[16:19], a[160:167], v122\n\t"
      "ds_read_b128 v[20:23], v127 offset:46080\n\t"
      "v_readfirstlane_b32 s56, v120\n\t"
      "v_smfmac_f32_16x16x64_bf16 v[36:39], v[16:19], a[96:103], v122\n\t"
      "ds_read_b128 v[24:27], v127 offset:47104\n\t"
      "v_smfmac_f32_16x16x64_bf16 v[32:35], v[12:15], a[24:31], v122\n\t"
      "s_add_u32 s68, s68, 0x8000\n\t"
      "ds_read_b128 v[28:31], v127 offset:48128\n\t"
      "v_max3_f32 v56, v56, v57, v58\n\t"
      "v_smfmac_f32_16x16x64_bf16 v[32:35], v[16:19], a[32:39], v122\n\t"
      "ds_read_b128 v[60:63], v127 offset:37888\n\t"
      "v_smfmac_f32_16x16x64_bf16 v[44:47], v[16:19], a[224:231], v122\n\t"
      "s_waitcnt lgkmcnt(3)\n\t"
      "v_max_f32 v56, v56, v59\n\t"
      "v_smfmac_f32_16x16x64_bf16 v[32:35], v[20:23], a[40:47], v122\n\t"
      "s_waitcnt lgkmcnt(2)\n\t"
      "ds_read_b128 v[88:91], v127 offset:39936\n\t"
      "v_smfmac_f32_16x16x64_bf16 v[32:35], v[24:27], a[48:55], v122\n\t"
      "s_waitcnt lgkmcnt(2)\n\t"
      "v_smfmac_f32_16x16x64_bf16 v[32:35], v[28:31], a[56:63], v122\n\t"
      "ds_read_b128 v[80:83], v127 offset:35840\n\t"
      "v_max_f32_dpp v56, v56, v56 quad_perm:[1,0,3,2] row_mask:0xf bank_mask:0xf\n\t"
      "v_smfmac_f32_16x16x64_bf16 v[36:39], v[20:23], a[104:111], v122\n\t"
      "ds_read_b128 v[84:87], v127 offset:36864\n\t"
      "v_max_f32_dpp v56, v56, v56 quad_perm:[2,3,0,1] row_mask:0xf bank_mask:0xf\n\t"
      "v_smfmac_f32_16x16x64_bf16 v[36:39], v[24:27], a[112:119], v122\n\t"
      "ds_read_b128 v[76:79], v127 offset:32768\n\t"
      "v_add_f32_e64 v33, v33, v35\n\t"
      "v_smfmac_f32_16x16x64_bf16 v[36:39], v[28:31], a[120:127], v122\n\t"
      "s_mov_b32 exec_lo, 0x100001\n\t"
      "s_mov_b32 exec_hi, 0x10000100\n\t"
      "v_smfmac_f32_16x16x64_bf16 v[44:47], v[20:23], a[232:239], v122\n\t"
      "ds_max_i32 v109, v56 offset:55392\n\t"
      "v_smfmac_f32_16x16x64_bf16 v[44:47], v[24:27], a[240:247], v122\n\t"
      "s_mov_b64 exec, -1\n\t"
      "v_add_f32 v32, v32, v34\n\t"
      "v_add_f32 v37, v37, v39\n\t"
      "v_mul_f32 v37, s59, v37\n\t"
      "v_smfmac_f32_16x16x64_bf16 v[44:47], v[28:31], a[248:255], v122\n\t"
      "ds_read_b128 v[92:95], v127 offset:34816\n\t"
      "v_add_f32_e64 v36, v36, v38\n\t"
      "v_smfmac_f32_16x16x64_bf16 v[40:43], v[20:23], a[168:175], v122\n\t"
      "ds_read_b128 v[56:59], v127 offset:38912\n\t"
      "v_mul_f32 v32, s59, v32\n\t"
      "v_mul_f32 v33, s59, v33\n\t"
      "v_cvt_pk_bf16_f32 v112, v32, v33\n\t"
      "v_smfmac_f32_16x16x64_bf16 v[40:43], v[24:27], a[176:183], v122\n\t"
      "ds_read_b128 v[96:99], v127 offset:33792\n\t"
      "v_mul_f32_e64 v36, s59, v36\n\t"
      "v_cvt_pk_bf16_f32 v113, v36, v37\n\t"
      "v_smfmac_f32_16x16x64_bf16 v[40:43], v[28:31], a[184:191], v122\n\t"
      "ds_write_b64 v125, v[112:113]\n\t"
      "v_smfmac_f32_16x16x64_bf16 v[48:51], v[4:7], v[136:143], v122\n\t"
      "s_mov_b32 s58, 0x10000000\n\t"
      "v_add_f32 v45, v45, v47\n\t"
      "v_add_f32 v44, v44, v46\n\t"
      "v_cvt_pk_bf16_f32 v121, v44, v45\n\t"
      "v_add_f32 v41, v41, v43\n\t"
      "v_add_f32 v40, v40, v42\n\t"
      "v_cvt_pk_bf16_f32 v120, v40, v41\n\t"
      "v_smfmac_f32_16x16x64_bf16 v[48:51], v[8:11], v[144:151], v122\n\t"
      "ds_write_b64 v125, v[120:121] offset:2080\n\t"
      "s_waitcnt lgkmcnt(1)\n\t"
      "v_smfmac_f32_16x16x64_bf16 v[48:51], v[12:15], v[152:159], v122\n\t"
      "s_barrier\n\t"
      "ds_read_b32 v108, v109 offset:55392\n\t"
      "v_smfmac_f32_16x16x64_bf16 v[48:51], v[16:19], v[160:167], v122\n\t"
      "ds_read_b64_tr_b16 v[32:33], v124 offset:49152\n\t"
      "ds_read_b64_tr_b16 v[34:35], v124 offset:49672\n\t"
      "v_smfmac_f32_16x16x64_bf16 v[48:51], v[20:23], v[168:175], v122\n\t"
      "ds_read_b64_tr_b16 v[38:39], v124 offset:50712\n\t"
      "s_waitcnt vmcnt(0) lgkmcnt(4)\n\t"
      "ds_read_b64_tr_b16 v[36:37], v124 offset:50192\n\t"
      "v_smfmac_f32_16x16x64_bf16 v[52:55], v[4:7], v[200:207], v122\n\t"
      "s_barrier\n\t"
      "ds_read_b64_tr_b16 v[46:47], v124 offset:52792\n\t"
      "v_smfmac_f32_16x16x64_bf16 v[48:51], v[24:27], v[176:183], v122\n\t"
      "s_waitcnt lgkmcnt(5)\n\t"
      "ds_read_b64_tr_b16 v[42:43], v124 offset:51752\n\t"
      "ds_read_b64_tr_b16 v[44:45], v124 offset:52272\n\t"
      "v_readfirstlane_b32 s61, v108\n\t"
      "v_smfmac_f32_16x16x64_bf16 v[48:51], v[28:31], v[184:191], v122\n\t"
      "s_waitcnt lgkmcnt(5)\n\t"
      "ds_read_b64_tr_b16 v[40:41], v124 offset:51232\n\t"
      "v_mov_b32_dpp v103, v35 quad_perm:[2,3,0,1] row_mask:0xf bank_mask:0xf\n\t"
      "s_waitcnt lgkmcnt(4)\n\t"
      "v_mfma_f32_16x16x32_bf16 v[116:119], v[80:83], v[32:35], 0\n\t"
      "s_lshr_b32 s61, s61, 23\n\t"
      "s_add_u32 s61, s61, s67\n\t"
      "v_mov_b32_dpp v107, v39 quad_perm:[2,3,0,1] row_mask:0xf bank_mask:0xf\n\t"
      "v_mfma_f32_16x16x32_bf16 v[112:115], v[76:79], v[32:35], 0\n\t"
      "s_max_u32 s61, s61, s66\n\t"
      "s_lshr_b32 s61, s61, 1\n\t"
      "v_mov_b32_dpp v106, v38 quad_perm:[2,3,0,1] row_mask:0xf bank_mask:0xf\n\t"
      "v_mfma_f32_16x16x32_bf16 v[112:115], v[84:87], v[36:39], v[112:115]\n\t"
      "s_min_u32 s61, s61, 200\n\t"
      "v_mov_b32_dpp v104, v36 quad_perm:[2,3,0,1] row_mask:0xf bank_mask:0xf\n\t"
      "v_mov_b32_dpp v101, v33 quad_perm:[2,3,0,1] row_mask:0xf bank_mask:0xf\n\t"
      "s_waitcnt lgkmcnt(0)\n\t"
      "v_mfma_f32_16x16x32_bf16 v[76:79], v[40:43], v[32:35], 0\n\t"
      "buffer_load_dwordx4 v127, s[36:39], s46 offen sc1 lds\n\t"
      "v_mfma_f32_16x16x32_bf16 v[76:79], v[44:47], v[36:39], v[76:79]\n\t"
      "s_lshl_b32 s61, s61, 23\n\t"
      "v_mov_b32_dpp v105, v37 quad_perm:[2,3,0,1] row_mask:0xf bank_mask:0xf\n\t"
      "s_add_u32 s65, s61, 33554432\n\t"
      "v_add_f32_e64 v49, v49, v51\n\t"
      "v_mov_b32_dpp v102, v34 quad_perm:[2,3,0,1] row_mask:0xf bank_mask:0xf\n\t"
      "v_mfma_f32_16x16x32_bf16 v[116:119], v[88:91], v[36:39], v[116:119]\n\t"
      "s_pack_hh_b32_b16 s75, s65, s65\n\t"
      "v_add_f32 v48, v48, v50\n\t"
      "v_cvt_pk_bf16_f32 v64, v48, v49\n\t"
      "v_mov_b32_dpp v100, v32 quad_perm:[2,3,0,1] row_mask:0xf bank_mask:0xf\n\t"
      "v_cndmask_b32_e64 v120, v76, v78, s[72:73]\n\t"
      "v_cndmask_b32_e64 v121, v77, v79, s[72:73]\n\t"
      "v_mfma_f32_16x16x32_bf16 v[112:115], v[92:95], v[100:103], v[112:115]\n\t"
      "buffer_load_dwordx4 v127, s[36:39], s46 offen offset:1024 sc1 lds\n\t"
      "v_mfma_f32_16x16x32_bf16 v[116:119], v[96:99], v[100:103], v[116:119]\n\t"
      "v_add_f32_dpp v121, v121, v121 quad_perm:[2,3,0,1] row_mask:0xf bank_mask:0xf\n\t"
      "v_add_f32_dpp v120, v120, v120 quad_perm:[2,3,0,1] row_mask:0xf bank_mask:0xf\n\t"
      "v_mfma_f32_16x16x32_bf16 v[116:119], v[60:63], v[104:107], v[116:119]\n\t"
      "s_mov_b32 m0, s45\n\t"
      "v_exp_f32 v80, v120\n\t"
      "v_exp_f32_e64 v81, v121\n\t"
      "v_mfma_f32_16x16x32_bf16 v[112:115], v[56:59], v[104:107], v[112:115]\n\t"
      "s_pack_hh_b32_b16 s74, s65, s65\n\t"
      "v_mov_b64 v[90:91], s[74:75]\n\t"
      "v_cvt_pk_bf16_f32 v108, v80, v81\n\t"
      "v_smfmac_f32_16x16x64_bf16 v[52:55], v[8:11], v[208:215], v122\n\t"
      "buffer_load_dwordx4 v65, s[36:39], s44 offen offset:1024 sc1 lds\n\t"
      "v_mfma_f32_16x16x32_bf16 v[84:87], v[108:111], v[64:67], 0\n\t"
      "v_cndmask_b32_e64 v115, v115, v119, s[72:73]\n\t"
      "v_cndmask_b32_e64 v113, v113, v117, s[72:73]\n\t"
      "v_cndmask_b32_e64 v114, v114, v118, s[72:73]\n\t"
      "v_mov_b64 v[88:89], s[74:75]\n\t"
      "v_exp_f32 v58, v114\n\t"
      "v_exp_f32 v59, v115\n\t"
      "v_exp_f32 v57, v113\n\t"
      "v_cndmask_b32_e64 v112, v112, v116, s[72:73]\n\t"
      "v_exp_f32_e64 v56, v112\n\t"
      "v_cvt_pk_bf16_f32 v97, v58, v59\n\t"
      "v_cvt_pk_bf16_f32 v96, v56, v57\n\t"
      "v_mfma_f32_16x16x32_bf16 v[48:51], v[108:111], v[88:91], 0\n\t"
      "v_mov_b32_dpp v99, v97 quad_perm:[2,3,0,1] row_mask:0xf bank_mask:0xf\n\t"
      "v_mov_b32_dpp v98, v96 quad_perm:[2,3,0,1] row_mask:0xf bank_mask:0xf\n\t"
      "v_max3_f32 v83, |v115|, |v120|, |v121|\n\t"
      "v_max3_f32 v82, |v112|, |v113|, |v114|\n\t"
      "v_mfma_f32_16x16x32_bf16 v[48:51], v[96:99], v[88:91], v[48:51]\n\t"
      "buffer_load_dwordx4 v65, s[36:39], s44 offen sc1 lds\n\t"
      "v_mfma_f32_16x16x32_bf16 v[84:87], v[96:99], v[68:71], v[84:87]\n\t"
      "v_max_f32_e64 v82, v82, v83\n\t"
      "v_mfma_f32_16x16x32_bf16 v[92:95], v[96:99], v[72:75], 0\n\t"
      "s_add_u32 s43, s43, 0x8000\n\t"
      "v_cmp_nge_f32 vcc, 0x42000000, v82\n\t"
      "v_rcp_f32 v48, v48\n\t"
      "v_rcp_f32 v49, v49\n\t"
      "v_fma_f32 v84, v84, v48, s96\n\t"
      "v_fma_f32 v85, v85, v49, s96\n\t"
      "s_cbranch_vccnz LMX_0%=\n\t"
      "buffer_atomic_add v85, v123, s[36:39], s97 offen offset:512\n\t"
      "buffer_atomic_add v84, v123, s[36:39], s97 offen\n\t"
      "v_smfmac_f32_16x16x64_bf16 v[52:55], v[12:15], v[216:223], v122\n\t"
      "v_smfmac_f32_16x16x64_bf16 v[52:55], v[16:19], v[224:231], v122\n\t"
      "v_smfmac_f32_16x16x64_bf16 v[52:55], v[20:23], v[232:239], v122\n\t"
      "v_smfmac_f32_16x16x64_bf16 v[52:55], v[24:27], v[240:247], v122\n\t"
      "s_nop 0\n\t"
      "v_smfmac_f32_16x16x64_bf16 v[52:55], v[28:31], v[248:255], v122\n\t"
      "s_nop 7\n\t"
      "v_add_f32 v53, v53, v55\n\t"
      "v_add_f32_e64 v52, v52, v54\n\t"
      "v_cvt_pk_bf16_f32 v66, v52, v53\n\t"
      "s_nop 1\n\t"
      "v_mfma_f32_16x16x32_bf16 v[92:95], v[108:111], v[66:69], v[92:95]\n\t"
      "s_nop 7\n\t"
      "v_fma_f32 v94, v94, v48, s96\n\t"
      "buffer_atomic_add v94, v123, s[36:39], s69 offen\n\t"
      "s_add_u32 s62, s61, 0x83400000\n\t"
      "v_fma_f32 v95, v95, v49, s96\n\t"
      "buffer_atomic_add v95, v123, s[36:39], s69 offen offset:512\n\t"
      "s_add_u32 s57, s61, 310378496\n\t"
      "s_add_u32 s97, s97, 0x8000\n\t"
      "v_mov_b32 v126, s62\n\t"
      "LWIN_0%=:\n\t"
      "v_add_u32 v100, s55, v127\n\t"
      "ds_read_b128 v[60:63], v100 offset:57344\n\t"
      "s_cmp_eq_u32 s56, 0\n\t"
      "s_cbranch_scc0 LSLOW_0%=\n\t"
      "LWIN1_0%=:\n\t"
      "buffer_load_dwordx2 v[120:121], off, s[36:39], s50 sc1\n\t"
      "s_mov_b32 s64, s50\n\t"
      "s_cmp_lt_i32 s40, 2\n\t"
      "s_cbranch_scc1 LEARLY_0%=\n\t"
      "LROLE_0%=:\n\t"
      "v_add_u32 v52, 65536, v127\n\t"
      "ds_read_b128 v[88:91], v52\n\t"
      "ds_read_b128 v[92:95], v52 offset:1024\n\t"
      "ds_read_b128 v[32:35], v52 offset:4096\n\t"
      "ds_read_b128 v[36:39], v52 offset:5120\n\t"
      "ds_read_b128 v[40:43], v52 offset:8192\n\t"
      "ds_read_b128 v[44:47], v52 offset:9216\n\t"
      "v_mov_b64 v[48:49], 0\n\t"
      "v_mov_b64 v[50:51], 0\n\t"
      "v_add_u32 v104, s71, v100\n\t"
      "ds_read_b128 v[76:79], v104\n\t"
      "ds_read_b128 v[80:83], v104 offset:1024\n\t"
      "ds_read_b128 v[112:115], v100\n\t"
      "ds_read_b128 v[116:119], v100 offset:1024\n\t"
      "v_xor_b32 v101, 2048, v104\n\t"
      "v_xor_b32 v102, 4096, v104\n\t"
      "v_xor_b32 v103, 6144, v104\n\t"
      "s_waitcnt lgkmcnt(8)\n\t"
      "v_smfmac_f32_16x16x64_bf16 v[48:51], v[0:3], v[88:95], v122\n\t"
      "ds_read_b128 v[88:91], v52 offset:12288\n\t"
      "ds_read_b128 v[92:95], v52 offset:13312\n\t"
      "s_add_u32 m0, s55, s71\n\t"
      "s_sub_u32 s71, 204800, s71\n\t"
      "s_waitcnt lgkmcnt(8)\n\t"
      "v_smfmac_f32_16x16x64_bf16 v[48:51], v[4:7], v[32:39], v122\n\t"
      "ds_read_b128 v[16:19], v101\n\t"
      "ds_read_b128 v[20:23], v101 offset:1024\n\t"
      "s_add_u32 s40, s40, 1\n\t"
      "s_cmp_lt_u32 s40, 63\n\t"
      "s_waitcnt lgkmcnt(8)\n\t"
      "v_smfmac_f32_16x16x64_bf16 v[48:51], v[8:11], v[40:47], v122\n\t"
      "ds_read_b128 v[24:27], v100 offset:8192\n\t"
      "ds_read_b128 v[28:31], v100 offset:9216\n\t"
      "s_cselect_b32 s61, 786432, 0\n\t"
      "s_cselect_b32 s60, s58, 0\n\t"
      "s_waitcnt lgkmcnt(4)\n\t"
      "v_smfmac_f32_16x16x64_bf16 v[48:51], v[12:15], v[88:95], v122\n\t"
      "s_add_u32 s44, s44, s61\n\t"
      "s_cmp_lt_u32 s40, 62\n\t"
      "ds_read_b128 v[0:3], v102\n\t"
      "ds_read_b128 v[4:7], v102 offset:1024\n\t"
      "ds_read_b128 v[8:11], v100 offset:16384\n\t"
      "ds_read_b128 v[12:15], v100 offset:17408\n\t"
      "s_cselect_b32 s61, 786432, 0\n\t"
      "s_add_u32 s46, s46, s61\n\t"
      "s_add_u32 s61, s40, 3\n\t"
      "s_and_b32 s62, s61, 3\n\t"
      "s_cselect_b32 s62, 16, 2000\n\t"
      "s_cmp_lt_u32 s61, 64\n\t"
      "s_cselect_b32 s62, s62, 0\n\t"
      "s_add_u32 s50, s50, s62\n\t"
      "s_mov_b32 s63, 0x2000\n\t"
      "s_mov_b32 s28, s49\n\t"
      "s_add_u32 s49, s49, 65536\n\t"
      "s_cmp_eq_u32 s40, 64\n\t"
      "v_add_f32 v48, v48, v50\n\t"
      "v_add_f32 v49, v49, v51\n\t"
      "s_cbranch_scc1 LFIN_0%=\n\t"
      "v_mfma_f32_16x16x32_bf16 v[32:35], v[76:79], v[112:115], 0\n\t"
      "v_mfma_f32_16x16x32_bf16 v[36:39], v[80:83], v[112:115], 0\n\t"
      "v_mfma_f32_16x16x32_bf16 v[40:43], v[76:79], v[116:119], 0\n\t"
      "v_mfma_f32_16x16x32_bf16 v[44:47], v[80:83], v[116:119], 0\n\t"
      "ds_read_b128 v[76:79], v103\n\t"
      "ds_read_b128 v[80:83], v103 offset:1024\n\t"
      "ds_read_b128 v[112:115], v100 offset:24576\n\t"
      "ds_read_b128 v[116:119], v100 offset:25600\n\t"
      "s_waitcnt lgkmcnt(8)\n\t"
      "v_mfma_f32_16x16x32_bf16 v[32:35], v[16:19], v[24:27], v[32:35]\n\t"
      "v_mfma_f32_16x16x32_bf16 v[36:39], v[20:23], v[24:27], v[36:39]\n\t"
      "v_mfma_f32_16x16x32_bf16 v[40:43], v[16:19], v[28:31], v[40:43]\n\t"
      "v_mfma_f32_16x16x32_bf16 v[44:47], v[20:23], v[28:31], v[44:47]\n\t"
      "LGATE_0%=:\n\t"
      "LBAR_0%=:\n\t"
      "s_cmp_gt_u32 s40, 1\n\t"
      "s_barrier\n\t"
      "s_cbranch_scc1 LPOLL_0%=\n\t"
      "s_branch LFIRST_0%=\n\t"
      ".p2align 6\n\t.fill 16, 4, 0xbf800000\n\t"
      "LPOLL_1%=:\n\t"
      "buffer_load_dwordx4 v[84:87], v60, s[36:39], s43 offen sc1\n\t"
      "buffer_load_dwordx4 v[88:91], v60, s[36:39], s43 offen offset:1024 sc1\n\t"
      "s_waitcnt lgkmcnt(4)\n\t"
      "v_mfma_f32_16x16x32_bf16 v[32:35], v[0:3], v[8:11], v[32:35]\n\t"
      "v_mfma_f32_16x16x32_bf16 v[36:39], v[4:7], v[8:11], v[36:39]\n\t"
      "v_mfma_f32_16x16x32_bf16 v[40:43], v[0:3], v[12:15], v[40:43]\n\t"
      "v_mfma_f32_16x16x32_bf16 v[44:47], v[4:7], v[12:15], v[44:47]\n\t"
      "buffer_load_dwordx4 v[92:95], v60, s[36:39], s68 offen sc1\n\t"
      "buffer_load_dwordx4 v[96:99], v60, s[36:39], s68 offen offset:1024 sc1\n\t"
      "ds_read_b64 v[54:55], v124 offset:53832\n\t"
      "s_xor_b32 s69, s97, 1024\n\t"
      "s_waitcnt lgkmcnt(1)\n\t"
      "v_mfma_f32_16x16x32_bf16 v[32:35], v[76:79], v[112:115], v[32:35]\n\t"
      "v_mfma_f32_16x16x32_bf16 v[36:39], v[80:83], v[112:115], v[36:39]\n\t"
      "v_mfma_f32_16x16x32_bf16 v[40:43], v[76:79], v[116:119], v[40:43]\n\t"
      "v_mfma_f32_16x16x32_bf16 v[44:47], v[80:83], v[116:119], v[44:47]\n\t"
      "s_waitcnt lgkmcnt(0)\n\t"
      "v_add_f32 v48, v48, v54\n\t"
      "v_add_f32_e64 v49, v49, v55\n\t"
      "v_cvt_pk_bf16_f32 v48, v48, v49\n\t"
      "buffer_store_dword v48, v62, s[36:39], s28 offen sc1\n\t"
      "s_mov_b64 exec, 1\n\t"
      "ds_write_b64 v109, v[110:111] offset:55392\n\t"
      "s_mov_b64 exec, -1\n\t"
      "s_nop 4\n\t"
      "v_cvt_pk_bf16_f32 v68, v32, v33\n\t"
      "v_cvt_pk_bf16_f32 v69, v34, v35\n\t"
      "v_cvt_pk_bf16_f32 v70, v36, v37\n\t"
      "v_cvt_pk_bf16_f32 v71, v38, v39\n\t"
      "v_cvt_pk_bf16_f32 v74, v40, v41\n\t"
      "v_cvt_pk_bf16_f32 v75, v42, v43\n\t"
      "v_cvt_pk_bf16_f32 v72, v44, v45\n\t"
      "v_cvt_pk_bf16_f32 v73, v46, v47\n\t"
      "v_mov_b64 v[32:33], 0\n\t"
      "v_mov_b64 v[34:35], 0\n\t"
      "v_mov_b64 v[36:37], 0\n\t"
      "v_mov_b64 v[38:39], 0\n\t"
      "v_mov_b64 v[40:41], 0\n\t"
      "v_mov_b64 v[42:43], 0\n\t"
      "v_mov_b64 v[44:45], 0\n\t"
      "v_mov_b64 v[46:47], 0\n\t"
      "v_mov_b64 v[48:49], 0\n\t"
      "v_mov_b64 v[50:51], 0\n\t"
      "v_mov_b64 v[52:53], 0\n\t"
      "v_mov_b64 v[54:55], 0\n\t"
      "s_nop 0\n\t"
      "s_nop 0\n\t"
      "s_nop 0\n\t"
      "s_nop 0\n\t"
      "s_nop 0\n\t"
      "s_nop 0\n\t"
      "s_nop 0\n\t"
      "s_nop 0\n\t"
      "s_waitcnt vmcnt(4)\n\t"
      "v_bitop3_b32 v64, v84, v85, v86 bitop3:0x80\n\t"
      "v_pk_fma_f32 v[2:3], v[86:87], s[56:57], v[126:127] op_sel:[0,1,0] op_sel_hi:[1,1,0]\n\t"
      "v_pk_fma_f32 v[0:1], v[84:85], s[56:57], v[126:127] op_sel:[0,1,0] op_sel_hi:[1,1,0]\n\t"
      "v_cvt_pk_bf16_f32 v4, v0, v1\n\t"
      "v_cvt_pk_bf16_f32 v5, v2, v3\n\t"
      "ds_write_b64 v127, v[4:5] offset:41984\n\t"
      "s_waitcnt vmcnt(3)\n\t"
      "v_pk_fma_f32 v[12:13], v[88:89], s[56:57], v[126:127] op_sel:[0,1,0] op_sel_hi:[1,1,0]\n\t"
      "v_cvt_pk_bf16_f32 v6, v12, v13\n\t"
      "s_nop 0\n\t"
      "v_pk_fma_f32 v[14:15], v[90:91], s[56:57], v[126:127] op_sel:[0,1,0] op_sel_hi:[1,1,0]\n\t"
      "v_cvt_pk_bf16_f32 v7, v14, v15\n\t"
      "ds_write_b64 v127, v[6:7] offset:41992\n\t"
      "v_bitop3_b32 v64, v64, v87, v88 bitop3:0x80\n\t"
      "v_bitop3_b32 v64, v64, v89, v90 bitop3:0x80\n\t"
      "v_bitop3_b32 v64, v64, v91, s58 bitop3:0x80\n\t"
      "v_cmp_eq_u32_e64 vcc, 0, v64\n\t"
      "v_smfmac_f32_16x16x64_bf16 v[32:35], v[4:7], a[8:15], v122\n\t"
      "v_smfmac_f32_16x16x64_bf16 v[48:51], v[4:7], v[136:143], v122\n\t"
      "s_cbranch_vccnz LR0_1%=\n\t"
      "LC0_1%=:\n\t"
      "v_smfmac_f32_16x16x64_bf16 v[52:55], v[4:7], v[200:207], v122\n\t"
      "s_waitcnt lgkmcnt(0)\n\t"
      "v_smfmac_f32_16x16x64_bf16 v[44:47], v[4:7], a[200:207], v122\n\t"
      "s_barrier\n\t"
      "ds_read_b128 v[0:3], v127 offset:40960\n\t"
      "v_smfmac_f32_16x16x64_bf16 v[36:39], v[4:7], a[72:79], v122\n\t"
      "ds_read_b128 v[8:11], v127 offset:43008\n\t"
      "v_smfmac_f32_16x16x64_bf16 v[40:43], v[4:7], a[136:143], v122\n\t"
      "ds_read_b128 v[12:15], v127 offset:44032\n\t"
      "v_bitop3_b32 v120, v120, v121, s53 bitop3:0x7e\n\t"
      "v_mfma_f32_16x16x32_bf16 v[56:59], v[4:7], v[4:7], 0\n\t"
      "s_waitcnt lgkmcnt(2)\n\t"
      "v_smfmac_f32_16x16x64_bf16 v[40:43], v[0:3], a[128:135], v122\n\t"
      "s_waitcnt vmcnt(2)\n\t"
      "v_fma_f32 v19, v95, s57, v126\n\t"
      "v_fma_f32 v18, v94, s57, v126\n\t"
      "v_smfmac_f32_16x16x64_bf16 v[32:35], v[0:3], a[0:7], v122\n\t"
      "s_nop 0\n\t"
      "v_fma_f32 v16, v92, s57, v126\n\t"
      "v_bitop3_b32 v64, v92, v93, v94 bitop3:0x80\n\t"
      "v_fma_f32 v17, v93, s57, v126\n\t"
      "v_cvt_pk_bf16_f32 v20, v16, v17\n\t"
      "v_smfmac_f32_16x16x64_bf16 v[36:39], v[0:3], a[64:71], v122\n\t"
      "v_cvt_pk_bf16_f32 v21, v18, v19\n\t"
      "v_smfmac_f32_16x16x64_bf16 v[44:47], v[0:3], a[192:199], v122\n\t"
      "s_waitcnt vmcnt(1)\n\t"
      "s_waitcnt lgkmcnt(0)\n\t"
      "v_fma_f32 v29, v97, s57, v126\n\t"
      "v_fma_f32 v31, v99, s57, v126\n\t"
      "v_fma_f32 v28, v96, s57, v126\n\t"
      "v_smfmac_f32_16x16x64_bf16 v[36:39], v[12:15], a[88:95], v122\n\t"
      "ds_write_b64 v127, v[20:21] offset:46080\n\t"
      "v_cvt_pk_bf16_f32 v22, v28, v29\n\t"
      "v_fma_f32 v30, v98, s57, v126\n\t"
      "v_cvt_pk_bf16_f32 v23, v30, v31\n\t"
      "v_smfmac_f32_16x16x64_bf16 v[36:39], v[8:11], a[80:87], v122\n\t"
      "ds_write_b64 v127, v[22:23] offset:46088\n\t"
      "v_smfmac_f32_16x16x64_bf16 v[44:47], v[12:15], a[216:223], v122\n\t"
      "v_bitop3_b32 v64, v64, v95, v96 bitop3:0x80\n\t"
      "v_smfmac_f32_16x16x64_bf16 v[40:43], v[12:15], a[152:159], v122\n\t"
      "v_bitop3_b32 v64, v64, v97, v98 bitop3:0x80\n\t"
      "v_bitop3_b32 v64, v64, v99, s60 bitop3:0x80\n\t"
      "v_smfmac_f32_16x16x64_bf16 v[44:47], v[8:11], a[208:215], v122\n\t"
      "v_cmp_eq_u32_e64 vcc, 0, v64\n\t"
      "v_smfmac_f32_16x16x64_bf16 v[32:35], v[12:15], a[24:31], v122\n\t"
      "s_waitcnt lgkmcnt(0)\n\t"
      "v_smfmac_f32_16x16x64_bf16 v[40:43], v[8:11], a[144:151], v122\n\t"
      "s_cbranch_vccnz LR1_1%=\n\t"
      "LC1_1%=:\n\t"
      "v_mfma_f32_16x16x32_bf16 v[56:59], v[20:23], v[20:23], v[56:59]\n\t"
      "s_barrier\n\t"
      "v_smfmac_f32_16x16x64_bf16 v[40:43], v[20:23], a[168:175], v122\n\t"
      "ds_read_b128 v[16:19], v127 offset:45056\n\t"
      "v_readfirstlane_b32 s56, v120\n\t"
      "v_smfmac_f32_16x16x64_bf16 v[36:39], v[20:23], a[104:111], v122\n\t"
      "ds_read_b128 v[28:31], v127 offset:48128\n\t"
      "v_smfmac_f32_16x16x64_bf16 v[32:35], v[8:11], a[16:23], v122\n\t"
      "s_add_u32 s68, s68, 0x8000\n\t"
      "ds_read_b128 v[24:27], v127 offset:47104\n\t"
      "v_max3_f32 v56, v56, v57, v58\n\t"
      "v_smfmac_f32_16x16x64_bf16 v[32:35], v[20:23], a[40:47], v122\n\t"
      "ds_read_b128 v[60:63], v127 offset:37888\n\t"
      "v_smfmac_f32_16x16x64_bf16 v[44:47], v[20:23], a[232:239], v122\n\t"
      "s_waitcnt lgkmcnt(3)\n\t"
      "v_max_f32 v56, v56, v59\n\t"
      "v_smfmac_f32_16x16x64_bf16 v[32:35], v[16:19], a[32:39], v122\n\t"
      "s_waitcnt lgkmcnt(2)\n\t"
      "ds_read_b128 v[88:91], v127 offset:39936\n\t"
      "v_smfmac_f32_16x16x64_bf16 v[32:35], v[28:31], a[56:63], v122\n\t"
      "s_waitcnt lgkmcnt(2)\n\t"
      "v_smfmac_f32_16x16x64_bf16 v[32:35], v[24:27], a[48:55], v122\n\t"
      "ds_read_b128 v[80:83], v127 offset:35840\n\t"
      "v_max_f32_dpp v56, v56, v56 quad_perm:[1,0,3,2] row_mask:0xf bank_mask:0xf\n\t"
      "v_smfmac_f32_16x16x64_bf16 v[36:39], v[16:19], a[96:103], v122\n\t"
      "ds_read_b128 v[84:87], v127 offset:36864\n\t"
      "v_max_f32_dpp v56, v56, v56 quad_perm:[2,3,0,1] row_mask:0xf bank_mask:0xf\n\t"
      "v_smfmac_f32_16x16x64_bf16 v[36:39], v[28:31], a[120:127], v122\n\t"
      "ds_read_b128 v[76:79], v127 offset:32768\n\t"
      "v_add_f32_e64 v33, v33, v35\n\t"
      "v_smfmac_f32_16x16x64_bf16 v[36:39], v[24:27], a[112:119], v122\n\t"
      "s_mov_b32 exec_lo, 0x100001\n\t"
      "s_mov_b32 exec_hi, 0x10000100\n\t"
      "v_smfmac_f32_16x16x64_bf16 v[44:47], v[16:19], a[224:231], v122\n\t"
      "ds_max_i32 v109, v56 offset:55392\n\t"
      "v_smfmac_f32_16x16x64_bf16 v[44:47], v[28:31], a[248:255], v122\n\t"
      "s_mov_b64 exec, -1\n\t"
      "v_add_f32 v32, v32, v34\n\t"
      "v_add_f32 v37, v37, v39\n\t"
      "v_mul_f32 v37, s59, v37\n\t"
      "v_smfmac_f32_16x16x64_bf16 v[44:47], v[24:27], a[240:247], v122\n\t"
      "ds_read_b128 v[92:95], v127 offset:34816\n\t"
      "v_add_f32_e64 v36, v36, v38\n\t"
      "v_smfmac_f32_16x16x64_bf16 v[40:43], v[16:19], a[160:167], v122\n\t"
      "ds_read_b128 v[56:59], v127 offset:38912\n\t"
      "v_mul_f32 v32, s59, v32\n\t"
      "v_mul_f32 v33, s59, v33\n\t"
      "v_cvt_pk_bf16_f32 v112, v32, v33\n\t"
      "v_smfmac_f32_16x16x64_bf16 v[40:43], v[28:31], a[184:191], v122\n\t"
      "ds_read_b128 v[96:99], v127 offset:33792\n\t"
      "v_mul_f32_e64 v36, s59, v36\n\t"
      "v_cvt_pk_bf16_f32 v113, v36, v37\n\t"
      "v_smfmac_f32_16x16x64_bf16 v[40:43], v[24:27], a[176:183], v122\n\t"
      "ds_write_b64 v125, v[112:113]\n\t"
      "v_smfmac_f32_16x16x64_bf16 v[48:51], v[0:3], v[128:135], v122\n\t"
      "s_mov_b32 s58, 0x10000000\n\t"
      "v_add_f32 v45, v45, v47\n\t"
      "v_add_f32 v44, v44, v46\n\t"
      "v_cvt_pk_bf16_f32 v121, v44, v45\n\t"
      "v_add_f32 v41, v41, v43\n\t"
      "v_add_f32 v40, v40, v42\n\t"
      "v_cvt_pk_bf16_f32 v120, v40, v41\n\t"
      "v_smfmac_f32_16x16x64_bf16 v[48:51], v[12:15], v[152:159], v122\n\t"
      "ds_write_b64 v125, v[120:121] offset:2080\n\t"
      "s_waitcnt lgkmcnt(1)\n\t"
      "v_smfmac_f32_16x16x64_bf16 v[48:51], v[8:11], v[144:151], v122\n\t"
      "s_barrier\n\t"
      "ds_read_b32 v108, v109 offset:55392\n\t"
      "v_smfmac_f32_16x16x64_bf16 v[48:51], v[20:23], v[168:175], v122\n\t"
      "ds_read_b64_tr_b16 v[32:33], v124 offset:49152\n\t"
      "ds_read_b64_tr_b16 v[34:35], v124 offset:49672\n\t"
      "v_smfmac_f32_16x16x64_bf16 v[48:51], v[16:19], v[160:167], v122\n\t"
      "ds_read_b64_tr_b16 v[38:39], v124 offset:50712\n\t"
      "s_waitcnt vmcnt(0) lgkmcnt(4)\n\t"
      "ds_read_b64_tr_b16 v[36:37], v124 offset:50192\n\t"
      "v_smfmac_f32_16x16x64_bf16 v[52:55], v[0:3], v[192:199], v122\n\t"
      "s_barrier\n\t"
      "ds_read_b64_tr_b16 v[46:47], v124 offset:52792\n\t"
      "v_smfmac_f32_16x16x64_bf16 v[48:51], v[28:31], v[184:191], v122\n\t"
      "s_waitcnt lgkmcnt(5)\n\t"
      "ds_read_b64_tr_b16 v[42:43], v124 offset:51752\n\t"
      "ds_read_b64_tr_b16 v[44:45], v124 offset:52272\n\t"
      "v_readfirstlane_b32 s61, v108\n\t"
      "v_smfmac_f32_16x16x64_bf16 v[48:51], v[24:27], v[176:183], v122\n\t"
      "s_waitcnt lgkmcnt(5)\n\t"
      "ds_read_b64_tr_b16 v[40:41], v124 offset:51232\n\t"
      "v_mov_b32_dpp v103, v35 quad_perm:[2,3,0,1] row_mask:0xf bank_mask:0xf\n\t"
      "s_waitcnt lgkmcnt(4)\n\t"
      "v_mfma_f32_16x16x32_bf16 v[116:119], v[80:83], v[32:35], 0\n\t"
      "s_lshr_b32 s61, s61, 23\n\t"
      "s_add_u32 s61, s61, s67\n\t"
      "v_mov_b32_dpp v107, v39 quad_perm:[2,3,0,1] row_mask:0xf bank_mask:0xf\n\t"
      "v_mfma_f32_16x16x32_bf16 v[112:115], v[76:79], v[32:35], 0\n\t"
      "s_max_u32 s61, s61, s66\n\t"
      "s_lshr_b32 s61, s61, 1\n\t"
      "v_mov_b32_dpp v106, v38 quad_perm:[2,3,0,1] row_mask:0xf bank_mask:0xf\n\t"
      "v_mfma_f32_16x16x32_bf16 v[112:115], v[84:87], v[36:39], v[112:115]\n\t"
      "s_min_u32 s61, s61, 200\n\t"
      "v_mov_b32_dpp v104, v36 quad_perm:[2,3,0,1] row_mask:0xf bank_mask:0xf\n\t"
      "v_mov_b32_dpp v101, v33 quad_perm:[2,3,0,1] row_mask:0xf bank_mask:0xf\n\t"
      "s_waitcnt lgkmcnt(0)\n\t"
      "v_mfma_f32_16x16x32_bf16 v[76:79], v[40:43], v[32:35], 0\n\t"
      "buffer_load_dwordx4 v127, s[36:39], s46 offen sc1 lds\n\t"
      "v_mfma_f32_16x16x32_bf16 v[76:79], v[44:47], v[36:39], v[76:79]\n\t"
      "s_lshl_b32 s61, s61, 23\n\t"
      "v_mov_b32_dpp v105, v37 quad_perm:[2,3,0,1] row_mask:0xf bank_mask:0xf\n\t"
      "s_add_u32 s65, s61, 33554432\n\t"
      "v_add_f32_e64 v49, v49, v51\n\t"
      "v_mov_b32_dpp v102, v34 quad_perm:[2,3,0,1] row_mask:0xf bank_mask:0xf\n\t"
      "v_mfma_f32_16x16x32_bf16 v[116:119], v[88:91], v[36:39], v[116:119]\n\t"
      "s_pack_hh_b32_b16 s75, s65, s65\n\t"
      "v_add_f32 v48, v48, v50\n\t"
      "v_cvt_pk_bf16_f32 v64, v48, v49\n\t"
      "v_mov_b32_dpp v100, v32 quad_perm:[2,3,0,1] row_mask:0xf bank_mask:0xf\n\t"
      "v_cndmask_b32_e64 v120, v76, v78, s[72:73]\n\t"
      "v_cndmask_b32_e64 v121, v77, v79, s[72:73]\n\t"
      "v_mfma_f32_16x16x32_bf16 v[112:115], v[92:95], v[100:103], v[112:115]\n\t"
      "buffer_load_dwordx4 v127, s[36:39], s46 offen offset:1024 sc1 lds\n\t"
      "v_mfma_f32_16x16x32_bf16 v[116:119], v[96:99], v[100:103], v[116:119]\n\t"
      "v_add_f32_dpp v121, v121, v121 quad_perm:[2,3,0,1] row_mask:0xf bank_mask:0xf\n\t"
      "v_add_f32_dpp v120, v120, v120 quad_perm:[2,3,0,1] row_mask:0xf bank_mask:0xf\n\t"
      "v_mfma_f32_16x16x32_bf16 v[116:119], v[60:63], v[104:107], v[116:119]\n\t"
      "s_mov_b32 m0, s45\n\t"
      "v_exp_f32 v80, v120\n\t"
      "v_exp_f32_e64 v81, v121\n\t"
      "v_mfma_f32_16x16x32_bf16 v[112:115], v[56:59], v[104:107], v[112:115]\n\t"
      "s_pack_hh_b32_b16 s74, s65, s65\n\t"
      "v_mov_b64 v[90:91], s[74:75]\n\t"
      "v_cvt_pk_bf16_f32 v108, v80, v81\n\t"
      "v_smfmac_f32_16x16x64_bf16 v[52:55], v[12:15], v[216:223], v122\n\t"
      "buffer_load_dwordx4 v65, s[36:39], s44 offen offset:1024 sc1 lds\n\t"
      "v_mfma_f32_16x16x32_bf16 v[84:87], v[108:111], v[64:67], 0\n\t"
      "v_cndmask_b32_e64 v115, v115, v119, s[72:73]\n\t"
      "v_cndmask_b32_e64 v113, v113, v117, s[72:73]\n\t"
      "v_cndmask_b32_e64 v114, v114, v118, s[72:73]\n\t"
      "v_mov_b64 v[88:89], s[74:75]\n\t"
      "v_exp_f32 v58, v114\n\t"
      "v_exp_f32 v59, v115\n\t"
      "v_exp_f32 v57, v113\n\t"
      "v_cndmask_b32_e64 v112, v112, v116, s[72:73]\n\t"
      "v_exp_f32_e64 v56, v112\n\t"
      "v_cvt_pk_bf16_f32 v97, v58, v59\n\t"
      "v_cvt_pk_bf16_f32 v96, v56, v57\n\t"
      "v_mfma_f32_16x16x32_bf16 v[48:51], v[108:111], v[88:91], 0\n\t"
      "v_mov_b32_dpp v99, v97 quad_perm:[2,3,0,1] row_mask:0xf bank_mask:0xf\n\t"
      "v_mov_b32_dpp v98, v96 quad_perm:[2,3,0,1] row_mask:0xf bank_mask:0xf\n\t"
      "v_max3_f32 v83, |v115|, |v120|, |v121|\n\t"
      "v_max3_f32 v82, |v112|, |v113|, |v114|\n\t"
      "v_mfma_f32_16x16x32_bf16 v[48:51], v[96:99], v[88:91], v[48:51]\n\t"
      "buffer_load_dwordx4 v65, s[36:39], s44 offen sc1 lds\n\t"
      "v_mfma_f32_16x16x32_bf16 v[84:87], v[96:99], v[68:71], v[84:87]\n\t"
      "v_max_f32_e64 v82, v82, v83\n\t"
      "v_mfma_f32_16x16x32_bf16 v[92:95], v[96:99], v[72:75], 0\n\t"
      "s_add_u32 s43, s43, 0x8000\n\t"
      "v_cmp_nge_f32 vcc, 0x42000000, v82\n\t"
      "v_rcp_f32 v48, v48\n\t"
      "v_rcp_f32 v49, v49\n\t"
      "v_fma_f32 v84, v84, v48, s96\n\t"
      "v_fma_f32 v85, v85, v49, s96\n\t"
      "s_cbranch_vccnz LMX_1%=\n\t"
      "buffer_atomic_add v85, v123, s[36:39], s97 offen offset:512\n\t"
      "buffer_atomic_add v84, v123, s[36:39], s97 offen\n\t"
      "v_smfmac_f32_16x16x64_bf16 v[52:55], v[8:11], v[208:215], v122\n\t"
      "v_smfmac_f32_16x16x64_bf16 v[52:55], v[20:23], v[232:239], v122\n\t"
      "v_smfmac_f32_16x16x64_bf16 v[52:55], v[16:19], v[224:231], v122\n\t"
      "v_smfmac_f32_16x16x64_bf16 v[52:55], v[28:31], v[248:255], v122\n\t"
      "s_nop 0\n\t"
      "v_smfmac_f32_16x16x64_bf16 v[52:55], v[24:27], v[240:247], v122\n\t"
      "s_nop 7\n\t"
      "v_add_f32 v53, v53, v55\n\t"
      "v_add_f32_e64 v52, v52, v54\n\t"
      "v_cvt_pk_bf16_f32 v66, v52, v53\n\t"
      "s_nop 1\n\t"
      "v_mfma_f32_16x16x32_bf16 v[92:95], v[108:111], v[66:69], v[92:95]\n\t"
      "s_nop 7\n\t"
      "v_fma_f32 v94, v94, v48, s96\n\t"
      "buffer_atomic_add v94, v123, s[36:39], s69 offen\n\t"
      "s_add_u32 s62, s61, 0x83400000\n\t"
      "v_fma_f32 v95, v95, v49, s96\n\t"
      "buffer_atomic_add v95, v123, s[36:39], s69 offen offset:512\n\t"
      "s_add_u32 s57, s61, 310378496\n\t"
      "s_add_u32 s97, s97, 0x8000\n\t"
      "v_mov_b32 v126, s62\n\t"
      "LWIN_1%=:\n\t"
      "v_add_u32 v100, s55, v127\n\t"
      "ds_read_b128 v[60:63], v100 offset:57344\n\t"
      "s_cmp_eq_u32 s56, 0\n\t"
      "s_cbranch_scc0 LSLOW_1%=\n\t"
      "LWIN1_1%=:\n\t"
      "buffer_load_dwordx2 v[120:121], off, s[36:39], s50 sc1\n\t"
      "s_mov_b32 s64, s50\n\t"
      "s_cmp_lt_i32 s40, 2\n\t"
      "s_cbranch_scc1 LEARLY_1%=\n\t"
      "LROLE_1%=:\n\t"
      "v_add_u32 v52, 67584, v127\n\t"
      "ds_read_b128 v[88:91], v52\n\t"
      "ds_read_b128 v[92:95], v52 offset:1024\n\t"
      "ds_read_b128 v[32:35], v52 offset:4096\n\t"
      "ds_read_b128 v[36:39], v52 offset:5120\n\t"
      "ds_read_b128 v[40:43], v52 offset:8192\n\t"
      "ds_read_b128 v[44:47], v52 offset:9216\n\t"
      "v_mov_b64 v[48:49], 0\n\t"
      "v_mov_b64 v[50:51], 0\n\t"
      "v_add_u32 v104, s71, v100\n\t"
      "ds_read_b128 v[76:79], v104\n\t"
      "ds_read_b128 v[80:83], v104 offset:1024\n\t"
      "ds_read_b128 v[112:115], v100\n\t"
      "ds_read_b128 v[116:119], v100 offset:1024\n\t"
      "v_xor_b32 v101, 2048, v104\n\t"
      "v_xor_b32 v102, 4096, v104\n\t"
      "v_xor_b32 v103, 6144, v104\n\t"
      "s_waitcnt lgkmcnt(8)\n\t"
      "v_smfmac_f32_16x16x64_bf16 v[48:51], v[0:3], v[88:95], v122\n\t"
      "ds_read_b128 v[88:91], v52 offset:12288\n\t"
      "ds_read_b128 v[92:95], v52 offset:13312\n\t"
      "s_add_u32 m0, s55, s71\n\t"
      "s_sub_u32 s71, 204800, s71\n\t"
      "s_waitcnt lgkmcnt(8)\n\t"
      "v_smfmac_f32_16x16x64_bf16 v[48:51], v[4:7], v[32:39], v122\n\t"
      "ds_read_b128 v[16:19], v101\n\t"
      "ds_read_b128 v[20:23], v101 offset:1024\n\t"
      "s_add_u32 s40, s40, 1\n\t"
      "s_cmp_lt_u32 s40, 63\n\t"
      "s_waitcnt lgkmcnt(8)\n\t"
      "v_smfmac_f32_16x16x64_bf16 v[48:51], v[8:11], v[40:47], v122\n\t"
      "ds_read_b128 v[24:27], v100 offset:8192\n\t"
      "ds_read_b128 v[28:31], v100 offset:9216\n\t"
      "s_cselect_b32 s61, 786432, 0\n\t"
      "s_cselect_b32 s60, s58, 0\n\t"
      "s_waitcnt lgkmcnt(4)\n\t"
      "v_smfmac_f32_16x16x64_bf16 v[48:51], v[12:15], v[88:95], v122\n\t"
      "s_add_u32 s44, s44, s61\n\t"
      "s_cmp_lt_u32 s40, 62\n\t"
      "ds_read_b128 v[0:3], v102\n\t"
      "ds_read_b128 v[4:7], v102 offset:1024\n\t"
      "ds_read_b128 v[8:11], v100 offset:16384\n\t"
      "ds_read_b128 v[12:15], v100 offset:17408\n\t"
      "s_cselect_b32 s61, 786432, 0\n\t"
      "s_add_u32 s46, s46, s61\n\t"
      "s_add_u32 s61, s40, 3\n\t"
      "s_and_b32 s62, s61, 3\n\t"
      "s_cselect_b32 s62, 16, 2000\n\t"
      "s_cmp_lt_u32 s61, 64\n\t"
      "s_cselect_b32 s62, s62, 0\n\t"
      "s_add_u32 s50, s50, s62\n\t"
      "s_mov_b32 s63, 0x2000\n\t"
      "s_mov_b32 s28, s49\n\t"
      "s_add_u32 s49, s49, 65536\n\t"
      "s_cmp_eq_u32 s40, 64\n\t"
      "v_add_f32 v48, v48, v50\n\t"
      "v_add_f32 v49, v49, v51\n\t"
      "s_cbranch_scc1 LFIN_1%=\n\t"
      "v_mfma_f32_16x16x32_bf16 v[32:35], v[76:79], v[112:115], 0\n\t"
      "v_mfma_f32_16x16x32_bf16 v[36:39], v[80:83], v[112:115], 0\n\t"
      "v_mfma_f32_16x16x32_bf16 v[40:43], v[76:79], v[116:119], 0\n\t"
      "v_mfma_f32_16x16x32_bf16 v[44:47], v[80:83], v[116:119], 0\n\t"
      "ds_read_b128 v[76:79], v103\n\t"
      "ds_read_b128 v[80:83], v103 offset:1024\n\t"
      "ds_read_b128 v[112:115], v100 offset:24576\n\t"
      "ds_read_b128 v[116:119], v100 offset:25600\n\t"
      "s_waitcnt lgkmcnt(8)\n\t"
      "v_mfma_f32_16x16x32_bf16 v[32:35], v[16:19], v[24:27], v[32:35]\n\t"
      "v_mfma_f32_16x16x32_bf16 v[36:39], v[20:23], v[24:27], v[36:39]\n\t"
      "v_mfma_f32_16x16x32_bf16 v[40:43], v[16:19], v[28:31], v[40:43]\n\t"
      "v_mfma_f32_16x16x32_bf16 v[44:47], v[20:23], v[28:31], v[44:47]\n\t"
      "LGATE_1%=:\n\t"
      "LBAR_1%=:\n\t"
      "s_cmp_gt_u32 s40, 1\n\t"
      "s_barrier\n\t"
      "s_cbranch_scc1 LPOLL_1%=\n\t"
      "s_branch LFIRST_1%=\n\t"
      ".p2align 6\n\t.fill 16, 4, 0xbf800000\n\t"
      "LPOLL_2%=:\n\t"
      "buffer_load_dwordx4 v[84:87], v60, s[36:39], s43 offen sc1\n\t"
      "buffer_load_dwordx4 v[88:91], v60, s[36:39], s43 offen offset:1024 sc1\n\t"
      "s_waitcnt lgkmcnt(8)\n\t"
      "v_mfma_f32_16x16x32_bf16 v[32:35], v[0:3], v[8:11], v[32:35]\n\t"
      "v_mfma_f32_16x16x32_bf16 v[36:39], v[4:7], v[8:11], v[36:39]\n\t"
      "v_mfma_f32_16x16x32_bf16 v[40:43], v[0:3], v[12:15], v[40:43]\n\t"
      "v_mfma_f32_16x16x32_bf16 v[44:47], v[4:7], v[12:15], v[44:47]\n\t"
      "buffer_load_dwordx4 v[92:95], v60, s[36:39], s68 offen sc1\n\t"
      "buffer_load_dwordx4 v[96:99], v60, s[36:39], s68 offen offset:1024 sc1\n\t"
      "v_mov_b64 v[48:49], 0\n\t"
      "v_mov_b64 v[50:51], 0\n\t"
      "v_mov_b64 v[52:53], 0\n\t"
      "v_mov_b64 v[54:55], 0\n\t"
      "s_xor_b32 s69, s97, 1024\n\t"
      "s_waitcnt lgkmcnt(4)\n\t"
      "v_mfma_f32_16x16x32_bf16 v[32:35], v[16:19], v[24:27], v[32:35]\n\t"
      "v_mfma_f32_16x16x32_bf16 v[36:39], v[20:23], v[24:27], v[36:39]\n\t"
      "v_mfma_f32_16x16x32_bf16 v[40:43], v[16:19], v[28:31], v[40:43]\n\t"
      "v_mfma_f32_16x16x32_bf16 v[44:47], v[20:23], v[28:31], v[44:47]\n\t"
      "s_waitcnt lgkmcnt(0)\n\t"
      "v_mfma_f32_16x16x32_bf16 v[32:35], v[76:79], v[112:115], v[32:35]\n\t"
      "v_mfma_f32_16x16x32_bf16 v[36:39], v[80:83], v[112:115], v[36:39]\n\t"
      "v_mfma_f32_16x16x32_bf16 v[40:43], v[76:79], v[116:119], v[40:43]\n\t"
      "v_mfma_f32_16x16x32_bf16 v[44:47], v[80:83], v[116:119], v[44:47]\n\t"
      "s_mov_b64 exec, 1\n\t"
      "ds_write_b64 v109, v[110:111] offset:55392\n\t"
      "s_mov_b64 exec, -1\n\t"
      "s_nop 4\n\t"
      "v_cvt_pk_bf16_f32 v68, v32, v33\n\t"
      "v_cvt_pk_bf16_f32 v69, v34, v35\n\t"
      "v_cvt_pk_bf16_f32 v70, v36, v37\n\t"
      "v_cvt_pk_bf16_f32 v71, v38, v39\n\t"
      "v_cvt_pk_bf16_f32 v74, v40, v41\n\t"
      "v_cvt_pk_bf16_f32 v75, v42, v43\n\t"
      "v_cvt_pk_bf16_f32 v72, v44, v45\n\t"
      "v_cvt_pk_bf16_f32 v73, v46, v47\n\t"
      "v_mov_b64 v[32:33], 0\n\t"
      "v_mov_b64 v[34:35], 0\n\t"
      "v_mov_b64 v[36:37], 0\n\t"
      "v_mov_b64 v[38:39], 0\n\t"
      "v_mov_b64 v[40:41], 0\n\t"
      "v_mov_b64 v[42:43], 0\n\t"
      "v_mov_b64 v[44:45], 0\n\t"
      "v_mov_b64 v[46:47], 0\n\t"
      "s_nop 0\n\t"
      "s_nop 0\n\t"
      "s_nop 0\n\t"
      "s_nop 0\n\t"
      "s_nop 0\n\t"
      "s_nop 0\n\t"
      "s_nop 0\n\t"
      "s_nop 0\n\t"
      "s_nop 0\n\t"
      "s_waitcnt vmcnt(3)\n\t"
      "v_bitop3_b32 v64, v84, v85, v86 bitop3:0x80\n\t"
      "v_pk_fma_f32 v[14:15], v[86:87], s[56:57], v[126:127] op_sel:[0,1,0] op_sel_hi:[1,1,0]\n\t"
      "v_pk_fma_f32 v[12:13], v[84:85], s[56:57], v[126:127] op_sel:[0,1,0] op_sel_hi:[1,1,0]\n\t"
      "v_cvt_pk_bf16_f32 v8, v12, v13\n\t"
      "v_cvt_pk_bf16_f32 v9, v14, v15\n\t"
      "ds_write_b64 v127, v[8:9] offset:43008\n\t"
      "s_waitcnt vmcnt(2)\n\t"
      "v_pk_fma_f32 v[0:1], v[88:89], s[56:57], v[126:127] op_sel:[0,1,0] op_sel_hi:[1,1,0]\n\t"
      "v_cvt_pk_bf16_f32 v10, v0, v1\n\t"
      "s_nop 0\n\t"
      "v_pk_fma_f32 v[2:3], v[90:91], s[56:57], v[126:127] op_sel:[0,1,0] op_sel_hi:[1,1,0]\n\t"
      "v_cvt_pk_bf16_f32 v11, v2, v3\n\t"
      "ds_write_b64 v127, v[10:11] offset:43016\n\t"
      "v_bitop3_b32 v64, v64, v87, v88 bitop3:0x80\n\t"
      "v_bitop3_b32 v64, v64, v89, v90 bitop3:0x80\n\t"
      "v_bitop3_b32 v64, v64, v91, s58 bitop3:0x80\n\t"
      "v_cmp_eq_u32_e64 vcc, 0, v64\n\t"
      "v_smfmac_f32_16x16x64_bf16 v[32:35], v[8:11], a[16:23], v122\n\t"
      "v_smfmac_f32_16x16x64_bf16 v[48:51], v[8:11], v[144:151], v122\n\t"
      "s_cbranch_vccnz LR0_2%=\n\t"
      "LC0_2%=:\n\t"
      "v_smfmac_f32_16x16x64_bf16 v[52:55], v[8:11], v[208:215], v122\n\t"
      "s_waitcnt lgkmcnt(0)\n\t"
      "v_smfmac_f32_16x16x64_bf16 v[44:47], v[8:11], a[208:215], v122\n\t"
      "s_barrier\n\t"
      "ds_read_b128 v[12:15], v127 offset:44032\n\t"
      "v_smfmac_f32_16x16x64_bf16 v[36:39], v[8:11], a[80:87], v122\n\t"
      "ds_read_b128 v[4:7], v127 offset:41984\n\t"
      "v_smfmac_f32_16x16x64_bf16 v[40:43], v[8:11], a[144:151], v122\n\t"
      "ds_read_b128 v[0:3], v127 offset:40960\n\t"
      "v_bitop3_b32 v120, v120, v121, s53 bitop3:0x7e\n\t"
      "v_mfma_f32_16x16x32_bf16 v[56:59], v[8:11], v[8:11], 0\n\t"
      "s_waitcnt lgkmcnt(2)\n\t"
      "v_smfmac_f32_16x16x64_bf16 v[40:43], v[12:15], a[152:159], v122\n\t"
      "s_waitcnt vmcnt(1)\n\t"
      "v_fma_f32 v31, v95, s57, v126\n\t"
      "v_fma_f32 v30, v94, s57, v126\n\t"
      "v_smfmac_f32_16x16x64_bf16 v[32:35], v[12:15], a[24:31], v122\n\t"
      "s_nop 0\n\t"
      "v_fma_f32 v28, v92, s57, v126\n\t"
      "v_bitop3_b32 v64, v92, v93, v94 bitop3:0x80\n\t"
      "v_fma_f32 v29, v93, s57, v126\n\t"
      "v_cvt_pk_bf16_f32 v24, v28, v29\n\t"
      "v_smfmac_f32_16x16x64_bf16 v[36:39], v[12:15], a[88:95], v122\n\t"
      "v_cvt_pk_bf16_f32 v25, v30, v31\n\t"
      "v_smfmac_f32_16x16x64_bf16 v[44:47], v[12:15], a[216:223], v122\n\t"
      "s_waitcnt vmcnt(0)\n\t"
      "s_waitcnt lgkmcnt(0)\n\t"
      "v_fma_f32 v17, v97, s57, v126\n\t"
      "v_fma_f32 v19, v99, s57, v126\n\t"
      "v_fma_f32 v16, v96, s57, v126\n\t"
      "v_smfmac_f32_16x16x64_bf16 v[36:39], v[0:3], a[64:71], v122\n\t"
      "ds_write_b64 v127, v[24:25] offset:47104\n\t"
      "v_cvt_pk_bf16_f32 v26, v16, v17\n\t"
      "v_fma_f32 v18, v98, s57, v126\n\t"
      "v_cvt_pk_bf16_f32 v27, v18, v19\n\t"
      "v_smfmac_f32_16x16x64_bf16 v[36:39], v[4:7], a[72:79], v122\n\t"
      "ds_write_b64 v127, v[26:27] offset:47112\n\t"
      "v_smfmac_f32_16x16x64_bf16 v[44:47], v[0:3], a[192:199], v122\n\t"
      "v_bitop3_b32 v64, v64, v95, v96 bitop3:0x80\n\t"
      "v_smfmac_f32_16x16x64_bf16 v[40:43], v[0:3], a[128:135], v122\n\t"
      "v_bitop3_b32 v64, v64, v97, v98 bitop3:0x80\n\t"
      "v_bitop3_b32 v64, v64, v99, s60 bitop3:0x80\n\t"
      "v_smfmac_f32_16x16x64_bf16 v[44:47], v[4:7], a[200:207], v122\n\t"
      "v_cmp_eq_u32_e64 vcc, 0, v64\n\t"
      "v_smfmac_f32_16x16x64_bf16 v[32:35], v[0:3], a[0:7], v122\n\t"
      "s_waitcnt lgkmcnt(0)\n\t"
      "v_smfmac_f32_16x16x64_bf16 v[40:43], v[4:7], a[136:143], v122\n\t"
      "s_cbranch_vccnz LR1_2%=\n\t"
      "LC1_2%=:\n\t"
      "v_mfma_f32_16x16x32_bf16 v[56:59], v[24:27], v[24:27], v[56:59]\n\t"
      "s_barrier\n\t"
      "v_smfmac_f32_16x16x64_bf16 v[40:43], v[24:27], a[176:183], v122\n\t"
      "ds_read_b128 v[28:31], v127 offset:48128\n\t"
      "v_readfirstlane_b32 s56, v120\n\t"
      "v_smfmac_f32_16x16x64_bf16 v[36:39], v[24:27], a[112:119], v122\n\t"
      "ds_read_b128 v[16:19], v127 offset:45056\n\t"
      "v_smfmac_f32_16x16x64_bf16 v[32:35], v[4:7], a[8:15], v122\n\t"
      "s_add_u32 s68, s68, 0x8000\n\t"
      "ds_read_b128 v[20:23], v127 offset:46080\n\t"
      "v_max3_f32 v56, v56, v57, v58\n\t"
      "v_smfmac_f32_16x16x64_bf16 v[32:35], v[24:27], a[48:55], v122\n\t"
      "ds_read_b128 v[60:63], v127 offset:37888\n\t"
      "v_smfmac_f32_16x16x64_bf16 v[44:47], v[24:27], a[240:247], v122\n\t"
      "s_waitcnt lgkmcnt(3)\n\t"
      "v_max_f32 v56, v56, v59\n\t"
      "v_smfmac_f32_16x16x64_bf16 v[32:35], v[28:31], a[56:63], v122\n\t"
      "s_waitcnt lgkmcnt(2)\n\t"
      "ds_read_b128 v[88:91], v127 offset:39936\n\t"
      "v_smfmac_f32_16x16x64_bf16 v[32:35], v[16:19], a[32:39], v122\n\t"
      "s_waitcnt lgkmcnt(2)\n\t"
      "v_smfmac_f32_16x16x64_bf16 v[32:35], v[20:23], a[40:47], v122\n\t"
      "ds_read_b128 v[80:83], v127 offset:35840\n\t"
      "v_max_f32_dpp v56, v56, v56 quad_perm:[1,0,3,2] row_mask:0xf bank_mask:0xf\n\t"
      "v_smfmac_f32_16x16x64_bf16 v[36:39], v[28:31], a[120:127], v122\n\t"
      "ds_read_b128 v[84:87], v127 offset:36864\n\t"
      "v_max_f32_dpp v56, v56, v56 quad_perm:[2,3,0,1] row_mask:0xf bank_mask:0xf\n\t"
      "v_smfmac_f32_16x16x64_bf16 v[36:39], v[16:19], a[96:103], v122\n\t"
      "ds_read_b128 v[76:79], v127 offset:32768\n\t"
      "v_add_f32_e64 v33, v33, v35\n\t"
      "v_smfmac_f32_16x16x64_bf16 v[36:39], v[20:23], a[104:111], v122\n\t"
      "s_mov_b32 exec_lo, 0x100001\n\t"
      "s_mov_b32 exec_hi, 0x10000100\n\t"
      "v_smfmac_f32_16x16x64_bf16 v[44:47], v[28:31], a[248:255], v122\n\t"
      "ds_max_i32 v109, v56 offset:55392\n\t"
      "v_smfmac_f32_16x16x64_bf16 v[44:47], v[16:19], a[224:231], v122\n\t"
      "s_mov_b64 exec, -1\n\t"
      "v_add_f32 v32, v32, v34\n\t"
      "v_add_f32 v37, v37, v39\n\t"
      "v_mul_f32 v37, s59, v37\n\t"
      "v_smfmac_f32_16x16x64_bf16 v[44:47], v[20:23], a[232:239], v122\n\t"
      "ds_read_b128 v[92:95], v127 offset:34816\n\t"
      "v_add_f32_e64 v36, v36, v38\n\t"
      "v_smfmac_f32_16x16x64_bf16 v[40:43], v[28:31], a[184:191], v122\n\t"
      "ds_read_b128 v[56:59], v127 offset:38912\n\t"
      "v_mul_f32 v32, s59, v32\n\t"
      "v_mul_f32 v33, s59, v33\n\t"
      "v_cvt_pk_bf16_f32 v112, v32, v33\n\t"
      "v_smfmac_f32_16x16x64_bf16 v[40:43], v[16:19], a[160:167], v122\n\t"
      "ds_read_b128 v[96:99], v127 offset:33792\n\t"
      "v_mul_f32_e64 v36, s59, v36\n\t"
      "v_cvt_pk_bf16_f32 v113, v36, v37\n\t"
      "v_smfmac_f32_16x16x64_bf16 v[40:43], v[20:23], a[168:175], v122\n\t"
      "ds_write_b64 v125, v[112:113]\n\t"
      "v_smfmac_f32_16x16x64_bf16 v[48:51], v[12:15], v[152:159], v122\n\t"
      "s_mov_b32 s58, 0x10000000\n\t"
      "v_add_f32 v45, v45, v47\n\t"
      "v_add_f32 v44, v44, v46\n\t"
      "v_cvt_pk_bf16_f32 v121, v44, v45\n\t"
      "v_add_f32 v41, v41, v43\n\t"
      "v_add_f32 v40, v40, v42\n\t"
      "v_cvt_pk_bf16_f32 v120, v40, v41\n\t"
      "v_smfmac_f32_16x16x64_bf16 v[48:51], v[0:3], v[128:135], v122\n\t"
      "ds_write_b64 v125, v[120:121] offset:2080\n\t"
      "s_waitcnt lgkmcnt(1)\n\t"
      "v_smfmac_f32_16x16x64_bf16 v[48:51], v[4:7], v[136:143], v122\n\t"
      "s_barrier\n\t"
      "ds_read_b32 v108, v109 offset:55392\n\t"
      "v_smfmac_f32_16x16x64_bf16 v[48:51], v[24:27], v[176:183], v122\n\t"
      "ds_read_b64_tr_b16 v[32:33], v124 offset:49152\n\t"
      "ds_read_b64_tr_b16 v[34:35], v124 offset:49672\n\t"
      "v_smfmac_f32_16x16x64_bf16 v[48:51], v[28:31], v[184:191], v122\n\t"
      "ds_read_b64_tr_b16 v[38:39], v124 offset:50712\n\t"
      "s_waitcnt lgkmcnt(4)\n\t"
      "ds_read_b64_tr_b16 v[36:37], v124 offset:50192\n\t"
      "v_smfmac_f32_16x16x64_bf16 v[52:55], v[12:15], v[216:223], v122\n\t"
      "s_barrier\n\t"
      "ds_read_b64_tr_b16 v[46:47], v124 offset:52792\n\t"
      "v_smfmac_f32_16x16x64_bf16 v[48:51], v[16:19], v[160:167], v122\n\t"
      "s_waitcnt lgkmcnt(5)\n\t"
      "ds_read_b64_tr_b16 v[42:43], v124 offset:51752\n\t"
      "ds_read_b64_tr_b16 v[44:45], v124 offset:52272\n\t"
      "v_readfirstlane_b32 s61, v108\n\t"
      "v_smfmac_f32_16x16x64_bf16 v[48:51], v[20:23], v[168:175], v122\n\t"
      "s_waitcnt lgkmcnt(5)\n\t"
      "ds_read_b64_tr_b16 v[40:41], v124 offset:51232\n\t"
      "v_mov_b32_dpp v103, v35 quad_perm:[2,3,0,1] row_mask:0xf bank_mask:0xf\n\t"
      "s_waitcnt lgkmcnt(4)\n\t"
      "v_mfma_f32_16x16x32_bf16 v[116:119], v[80:83], v[32:35], 0\n\t"
      "s_lshr_b32 s61, s61, 23\n\t"
      "s_add_u32 s61, s61, s67\n\t"
      "v_mov_b32_dpp v107, v39 quad_perm:[2,3,0,1] row_mask:0xf bank_mask:0xf\n\t"
      "v_mfma_f32_16x16x32_bf16 v[112:115], v[76:79], v[32:35], 0\n\t"
      "s_max_u32 s61, s61, s66\n\t"
      "s_lshr_b32 s61, s61, 1\n\t"
      "v_mov_b32_dpp v106, v38 quad_perm:[2,3,0,1] row_mask:0xf bank_mask:0xf\n\t"
      "v_mfma_f32_16x16x32_bf16 v[112:115], v[84:87], v[36:39], v[112:115]\n\t"
      "s_min_u32 s61, s61, 200\n\t"
      "v_mov_b32_dpp v104, v36 quad_perm:[2,3,0,1] row_mask:0xf bank_mask:0xf\n\t"
      "v_mov_b32_dpp v101, v33 quad_perm:[2,3,0,1] row_mask:0xf bank_mask:0xf\n\t"
      "s_waitcnt lgkmcnt(0)\n\t"
      "v_mfma_f32_16x16x32_bf16 v[76:79], v[40:43], v[32:35], 0\n\t"
      "buffer_load_dwordx4 v127, s[36:39], s46 offen sc1 lds\n\t"
      "v_mfma_f32_16x16x32_bf16 v[76:79], v[44:47], v[36:39], v[76:79]\n\t"
      "s_lshl_b32 s61, s61, 23\n\t"
      "v_mov_b32_dpp v105, v37 quad_perm:[2,3,0,1] row_mask:0xf bank_mask:0xf\n\t"
      "s_add_u32 s65, s61, 33554432\n\t"
      "v_add_f32_e64 v49, v49, v51\n\t"
      "v_mov_b32_dpp v102, v34 quad_perm:[2,3,0,1] row_mask:0xf bank_mask:0xf\n\t"
      "v_mfma_f32_16x16x32_bf16 v[116:119], v[88:91], v[36:39], v[116:119]\n\t"
      "s_pack_hh_b32_b16 s75, s65, s65\n\t"
      "v_add_f32 v48, v48, v50\n\t"
      "v_cvt_pk_bf16_f32 v64, v48, v49\n\t"
      "v_mov_b32_dpp v100, v32 quad_perm:[2,3,0,1] row_mask:0xf bank_mask:0xf\n\t"
      "v_cndmask_b32_e64 v120, v76, v78, s[72:73]\n\t"
      "v_cndmask_b32_e64 v121, v77, v79, s[72:73]\n\t"
      "v_mfma_f32_16x16x32_bf16 v[112:115], v[92:95], v[100:103], v[112:115]\n\t"
      "buffer_load_dwordx4 v127, s[36:39], s46 offen offset:1024 sc1 lds\n\t"
      "v_mfma_f32_16x16x32_bf16 v[116:119], v[96:99], v[100:103], v[116:119]\n\t"
      "v_add_f32_dpp v121, v121, v121 quad_perm:[2,3,0,1] row_mask:0xf bank_mask:0xf\n\t"
      "v_add_f32_dpp v120, v120, v120 quad_perm:[2,3,0,1] row_mask:0xf bank_mask:0xf\n\t"
      "v_mfma_f32_16x16x32_bf16 v[116:119], v[60:63], v[104:107], v[116:119]\n\t"
      "s_mov_b32 m0, s45\n\t"
      "v_exp_f32 v80, v120\n\t"
      "v_exp_f32_e64 v81, v121\n\t"
      "v_mfma_f32_16x16x32_bf16 v[112:115], v[56:59], v[104:107], v[112:115]\n\t"
      "s_pack_hh_b32_b16 s74, s65, s65\n\t"
      "v_mov_b64 v[90:91], s[74:75]\n\t"
      "v_cvt_pk_bf16_f32 v108, v80, v81\n\t"
      "v_smfmac_f32_16x16x64_bf16 v[52:55], v[0:3], v[192:199], v122\n\t"
      "buffer_load_dwordx4 v65, s[36:39], s44 offen offset:1024 sc1 lds\n\t"
      "v_mfma_f32_16x16x32_bf16 v[84:87], v[108:111], v[64:67], 0\n\t"
      "v_cndmask_b32_e64 v115, v115, v119, s[72:73]\n\t"
      "v_cndmask_b32_e64 v113, v113, v117, s[72:73]\n\t"
      "v_cndmask_b32_e64 v114, v114, v118, s[72:73]\n\t"
      "v_mov_b64 v[88:89], s[74:75]\n\t"
      "v_exp_f32 v58, v114\n\t"
      "v_exp_f32 v59, v115\n\t"
      "v_exp_f32 v57, v113\n\t"
      "v_cndmask_b32_e64 v112, v112, v116, s[72:73]\n\t"
      "v_exp_f32_e64 v56, v112\n\t"
      "v_cvt_pk_bf16_f32 v97, v58, v59\n\t"
      "v_cvt_pk_bf16_f32 v96, v56, v57\n\t"
      "v_mfma_f32_16x16x32_bf16 v[48:51], v[108:111], v[88:91], 0\n\t"
      "v_mov_b32_dpp v99, v97 quad_perm:[2,3,0,1] row_mask:0xf bank_mask:0xf\n\t"
      "v_mov_b32_dpp v98, v96 quad_perm:[2,3,0,1] row_mask:0xf bank_mask:0xf\n\t"
      "v_max3_f32 v83, |v115|, |v120|, |v121|\n\t"
      "v_max3_f32 v82, |v112|, |v113|, |v114|\n\t"
      "v_mfma_f32_16x16x32_bf16 v[48:51], v[96:99], v[88:91], v[48:51]\n\t"
      "buffer_load_dwordx4 v65, s[36:39], s44 offen sc1 lds\n\t"
      "v_mfma_f32_16x16x32_bf16 v[84:87], v[96:99], v[68:71], v[84:87]\n\t"
      "v_max_f32_e64 v82, v82, v83\n\t"
      "v_mfma_f32_16x16x32_bf16 v[92:95], v[96:99], v[72:75], 0\n\t"
      "s_add_u32 s43, s43, 0x8000\n\t"
      "v_cmp_nge_f32 vcc, 0x42000000, v82\n\t"
      "v_rcp_f32 v48, v48\n\t"
      "v_rcp_f32 v49, v49\n\t"
      "v_fma_f32 v84, v84, v48, s96\n\t"
      "v_fma_f32 v85, v85, v49, s96\n\t"
      "s_cbranch_vccnz LMX_2%=\n\t"
      "buffer_atomic_add v85, v123, s[36:39], s97 offen offset:512\n\t"
      "buffer_atomic_add v84, v123, s[36:39], s97 offen\n\t"
      "v_smfmac_f32_16x16x64_bf16 v[52:55], v[4:7], v[200:207], v122\n\t"
      "v_smfmac_f32_16x16x64_bf16 v[52:55], v[24:27], v[240:247], v122\n\t"
      "v_smfmac_f32_16x16x64_bf16 v[52:55], v[28:31], v[248:255], v122\n\t"
      "v_smfmac_f32_16x16x64_bf16 v[52:55], v[16:19], v[224:231], v122\n\t"
      "s_nop 0\n\t"
      "v_smfmac_f32_16x16x64_bf16 v[52:55], v[20:23], v[232:239], v122\n\t"
      "s_nop 7\n\t"
      "v_add_f32 v53, v53, v55\n\t"
      "v_add_f32_e64 v52, v52, v54\n\t"
      "v_cvt_pk_bf16_f32 v66, v52, v53\n\t"
      "s_nop 1\n\t"
      "v_mfma_f32_16x16x32_bf16 v[92:95], v[108:111], v[66:69], v[92:95]\n\t"
      "s_nop 7\n\t"
      "v_fma_f32 v94, v94, v48, s96\n\t"
      "buffer_atomic_add v94, v123, s[36:39], s69 offen\n\t"
      "s_add_u32 s62, s61, 0x83400000\n\t"
      "v_fma_f32 v95, v95, v49, s96\n\t"
      "buffer_atomic_add v95, v123, s[36:39], s69 offen offset:512\n\t"
      "s_add_u32 s57, s61, 310378496\n\t"
      "s_add_u32 s97, s97, 0x8000\n\t"
      "v_mov_b32 v126, s62\n\t"
      "LWIN_2%=:\n\t"
      "v_add_u32 v100, s55, v127\n\t"
      "ds_read_b128 v[60:63], v100 offset:57344\n\t"
      "s_cmp_eq_u32 s56, 0\n\t"
      "s_cbranch_scc0 LSLOW_2%=\n\t"
      "LWIN1_2%=:\n\t"
      "buffer_load_dwordx2 v[120:121], off, s[36:39], s50 sc1\n\t"
      "s_mov_b32 s64, s50\n\t"
      "s_cmp_lt_i32 s40, 2\n\t"
      "s_cbranch_scc1 LEARLY_2%=\n\t"
      "LROLE_2%=:\n\t"
      "v_add_u32 v52, 65536, v127\n\t"
      "ds_read_b128 v[88:91], v52 offset:16384\n\t"
      "ds_read_b128 v[92:95], v52 offset:17408\n\t"
      "ds_read_b128 v[32:35], v52 offset:20480\n\t"
      "ds_read_b128 v[36:39], v52 offset:21504\n\t"
      "s_cmp_eq_u32 s42, 3\n\t"
      "s_waitcnt lgkmcnt(4)\n\t"
      "s_cbranch_scc1 LWK1_2%=\n\t"
      "s_cmp_eq_u32 s42, 2\n\t"
      "s_cbranch_scc0 LW4_2%=\n\t"
      "buffer_store_dwordx4 v[40:43], v61, s[36:39], s48 offen sc1\n\t"
      "LW4_2%=:\n\t"
      "s_add_u32 s48, s48, 65536\n\t"
      "s_nop 0\n\t"
      "ds_read_b128 v[40:43], v52 offset:24576\n\t"
      "ds_read_b128 v[44:47], v52 offset:25600\n\t"
      "v_mov_b64 v[48:49], 0\n\t"
      "v_mov_b64 v[50:51], 0\n\t"
      "s_memrealtime s[92:93]\n\t"
      "s_waitcnt lgkmcnt(0)\n\t"
      "s_sub_u32 s61, s92, s95\n\t"
      "s_mov_b32 s95, s92\n\t"
      "s_min_u32 s61, s61, 190\n\t"
      "s_add_u32 s61, s61, s92\n\t"
      "s_pack_ll_b32_b16 s61, s61, s40\n\t"
      "v_mov_b32 v63, s61\n\t"
      "s_mov_b64 exec, 1\n\t"
      "buffer_store_dword v63, off, s[36:39], s51 sc1\n\t"
      "s_mov_b64 exec, -1\n\t"
      "v_add_u32 v104, s71, v100\n\t"
      "ds_read_b128 v[76:79], v104\n\t"
      "ds_read_b128 v[80:83], v104 offset:1024\n\t"
      "ds_read_b128 v[112:115], v100\n\t"
      "ds_read_b128 v[116:119], v100 offset:1024\n\t"
      "v_xor_b32 v101, 2048, v104\n\t"
      "v_xor_b32 v102, 4096, v104\n\t"
      "v_xor_b32 v103, 6144, v104\n\t"
      "v_smfmac_f32_16x16x64_bf16 v[48:51], v[16:19], v[88:95], v122\n\t"
      "ds_read_b128 v[88:91], v52 offset:28672\n\t"
      "ds_read_b128 v[92:95], v52 offset:29696\n\t"
      "s_add_u32 m0, s55, s71\n\t"
      "s_sub_u32 s71, 204800, s71\n\t"
      "v_smfmac_f32_16x16x64_bf16 v[48:51], v[20:23], v[32:39], v122\n\t"
      "ds_read_b128 v[0:3], v101\n\t"
      "ds_read_b128 v[4:7], v101 offset:1024\n\t"
      "s_add_u32 s40, s40, 1\n\t"
      "s_cmp_lt_u32 s40, 63\n\t"
      "v_smfmac_f32_16x16x64_bf16 v[48:51], v[24:27], v[40:47], v122\n\t"
      "ds_read_b128 v[8:11], v100 offset:8192\n\t"
      "ds_read_b128 v[12:15], v100 offset:9216\n\t"
      "s_cselect_b32 s61, 786432, 0\n\t"
      "s_cselect_b32 s60, s58, 0\n\t"
      "s_waitcnt lgkmcnt(4)\n\t"
      "v_smfmac_f32_16x16x64_bf16 v[48:51], v[28:31], v[88:95], v122\n\t"
      "s_add_u32 s44, s44, s61\n\t"
      "s_cmp_lt_u32 s40, 62\n\t"
      "ds_read_b128 v[16:19], v102\n\t"
      "ds_read_b128 v[20:23], v102 offset:1024\n\t"
      "ds_read_b128 v[24:27], v100 offset:16384\n\t"
      "ds_read_b128 v[28:31], v100 offset:17408\n\t"
      "s_cselect_b32 s61, 786432, 0\n\t"
      "s_add_u32 s46, s46, s61\n\t"
      "s_add_u32 s61, s40, 3\n\t"
      "s_and_b32 s62, s61, 3\n\t"
      "s_cselect_b32 s62, 16, 2000\n\t"
      "s_cmp_lt_u32 s61, 64\n\t"
      "s_cselect_b32 s62, s62, 0\n\t"
      "s_add_u32 s50, s50, s62\n\t"
      "s_mov_b32 s63, 0x2000\n\t"
      "s_cmp_eq_u32 s40, 64\n\t"
      "v_add_f32 v48, v48, v50\n\t"
      "v_add_f32 v49, v49, v51\n\t"
      "ds_write_b64 v124, v[48:49] offset:53312\n\t"
      "s_cbranch_scc1 LFIN_2%=\n\t"
      "v_mfma_f32_16x16x32_bf16 v[32:35], v[76:79], v[112:115], 0\n\t"
      "v_mfma_f32_16x16x32_bf16 v[36:39], v[80:83], v[112:115], 0\n\t"
      "v_mfma_f32_16x16x32_bf16 v[40:43], v[76:79], v[116:119], 0\n\t"
      "v_mfma_f32_16x16x32_bf16 v[44:47], v[80:83], v[116:119], 0\n\t"
      "ds_read_b128 v[76:79], v103\n\t"
      "ds_read_b128 v[80:83], v103 offset:1024\n\t"
      "ds_read_b128 v[112:115], v100 offset:24576\n\t"
      "ds_read_b128 v[116:119], v100 offset:25600\n\t"
      "LGATE_2%=:\n\t"
      "LBAR_2%=:\n\t"
      "s_cmp_gt_u32 s40, 1\n\t"
      "s_waitcnt lgkmcnt(0)\n\t"
      "s_barrier\n\t"
      "s_cbranch_scc1 LPOLL_2%=\n\t"
      "s_branch LFIRST_2%=\n\t"
      ".p2align 6\n\t.fill 16, 4, 0xbf800000\n\t"
      "LPOLL_3%=:\n\t"
      "s_memtime s[78:79]\n\t"
      "buffer_load_dwordx4 v[84:87], v60, s[36:39], s43 offen sc1\n\t"
      "buffer_load_dwordx4 v[88:91], v60, s[36:39], s43 offen offset:1024 sc1\n\t"
      "s_waitcnt lgkmcnt(8)\n\t"
      "v_mfma_f32_16x16x32_bf16 v[32:35], v[0:3], v[8:11], v[32:35]\n\t"
      "v_mfma_f32_16x16x32_bf16 v[36:39], v[4:7], v[8:11], v[36:39]\n\t"
      "v_mfma_f32_16x16x32_bf16 v[40:43], v[0:3], v[12:15], v[40:43]\n\t"
      "v_mfma_f32_16x16x32_bf16 v[44:47], v[4:7], v[12:15], v[44:47]\n\t"
      "buffer_load_dwordx4 v[92:95], v60, s[36:39], s68 offen sc1\n\t"
      "buffer_load_dwordx4 v[96:99], v60, s[36:39], s68 offen offset:1024 sc1\n\t"
      "v_mov_b64 v[48:49], 0\n\t"
      "v_mov_b64 v[50:51], 0\n\t"
      "v_mov_b64 v[52:53], 0\n\t"
      "v_mov_b64 v[54:55], 0\n\t"
      "s_xor_b32 s69, s97, 1024\n\t"
      "s_waitcnt lgkmcnt(4)\n\t"
      "v_mfma_f32_16x16x32_bf16 v[32:35], v[16:19], v[24:27], v[32:35]\n\t"
      "v_mfma_f32_16x16x32_bf16 v[36:39], v[20:23], v[24:27], v[36:39]\n\t"
      "v_mfma_f32_16x16x32_bf16 v[40:43], v[16:19], v[28:31], v[40:43]\n\t"
      "v_mfma_f32_16x16x32_bf16 v[44:47], v[20:23], v[28:31], v[44:47]\n\t"
      "s_waitcnt lgkmcnt(0)\n\t"
      "v_mfma_f32_16x16x32_bf16 v[32:35], v[76:79], v[112:115], v[32:35]\n\t"
      "v_mfma_f32_16x16x32_bf16 v[36:39], v[80:83], v[112:115], v[36:39]\n\t"
      "v_mfma_f32_16x16x32_bf16 v[40:43], v[76:79], v[116:119], v[40:43]\n\t"
      "v_mfma_f32_16x16x32_bf16 v[44:47], v[80:83], v[116:119], v[44:47]\n\t"
      "s_mov_b64 exec, 1\n\t"
      "ds_write_b64 v109, v[110:111] offset:55392\n\t"
      "s_mov_b64 exec, -1\n\t"
      "s_nop 4\n\t"
      "v_cvt_pk_bf16_f32 v68, v32, v33\n\t"
      "v_cvt_pk_bf16_f32 v69, v34, v35\n\t"
      "v_cvt_pk_bf16_f32 v70, v36, v37\n\t"
      "v_cvt_pk_bf16_f32 v71, v38, v39\n\t"
      "v_cvt_pk_bf16_f32 v74, v40, v41\n\t"
      "v_cvt_pk_bf16_f32 v75, v42, v43\n\t"
      "v_cvt_pk_bf16_f32 v72, v44, v45\n\t"
      "v_cvt_pk_bf16_f32 v73, v46, v47\n\t"
      "v_mov_b64 v[32:33], 0\n\t"
      "v_mov_b64 v[34:35], 0\n\t"
      "v_mov_b64 v[36:37], 0\n\t"
      "v_mov_b64 v[38:39], 0\n\t"
      "v_mov_b64 v[40:41], 0\n\t"
      "v_mov_b64 v[42:43], 0\n\t"
      "v_mov_b64 v[44:45], 0\n\t"
      "v_mov_b64 v[46:47], 0\n\t"
      "s_nop 0\n\t"
      "s_nop 0\n\t"
      "s_nop 0\n\t"
      "s_nop 0\n\t"
      "s_nop 0\n\t"
      "s_waitcnt vmcnt(3)\n\t"
      "s_memtime s[88:89]\n\t"
      "v_bitop3_b32 v64, v84, v85, v86 bitop3:0x80\n\t"
      "v_pk_fma_f32 v[10:11], v[86:87], s[56:57], v[126:127] op_sel:[0,1,0] op_sel_hi:[1,1,0]\n\t"
      "v_pk_fma_f32 v[8:9], v[84:85], s[56:57], v[126:127] op_sel:[0,1,0] op_sel_hi:[1,1,0]\n\t"
      "v_cvt_pk_bf16_f32 v12, v8, v9\n\t"
      "v_cvt_pk_bf16_f32 v13, v10, v11\n\t"
      "ds_write_b64 v127, v[12:13] offset:44032\n\t"
      "s_waitcnt vmcnt(2)\n\t"
      "v_pk_fma_f32 v[4:5], v[88:89], s[56:57], v[126:127] op_sel:[0,1,0] op_sel_hi:[1,1,0]\n\t"
      "v_cvt_pk_bf16_f32 v14, v4, v5\n\t"
      "s_nop 0\n\t"
      "v_pk_fma_f32 v[6:7], v[90:91], s[56:57], v[126:127] op_sel:[0,1,0] op_sel_hi:[1,1,0]\n\t"
      "v_cvt_pk_bf16_f32 v15, v6, v7\n\t"
      "ds_write_b64 v127, v[14:15] offset:44040\n\t"
      "v_bitop3_b32 v64, v64, v87, v88 bitop3:0x80\n\t"
      "v_bitop3_b32 v64, v64, v89, v90 bitop3:0x80\n\t"
      "v_bitop3_b32 v64, v64, v91, s58 bitop3:0x80\n\t"
      "v_cmp_eq_u32_e64 vcc, 0, v64\n\t"
      "v_smfmac_f32_16x16x64_bf16 v[32:35], v[12:15], a[24:31], v122\n\t"
      "v_smfmac_f32_16x16x64_bf16 v[48:51], v[12:15], v[152:159], v122\n\t"
      "s_cbranch_vccnz LR0_3%=\n\t"
      "LC0_3%=:\n\t"
      "v_smfmac_f32_16x16x64_bf16 v[52:55], v[12:15], v[216:223], v122\n\t"
      "s_waitcnt lgkmcnt(0)\n\t"
      "v_smfmac_f32_16x16x64_bf16 v[44:47], v[12:15], a[216:223], v122\n\t"
      "s_barrier\n\t"
      "ds_read_b128 v[8:11], v127 offset:43008\n\t"
      "v_smfmac_f32_16x16x64_bf16 v[36:39], v[12:15], a[88:95], v122\n\t"
      "ds_read_b128 v[0:3], v127 offset:40960\n\t"
      "v_smfmac_f32_16x16x64_bf16 v[40:43], v[12:15], a[152:159], v122\n\t"
      "ds_read_b128 v[4:7], v127 offset:41984\n\t"
      "v_bitop3_b32 v120, v120, v121, s53 bitop3:0x7e\n\t"
      "v_mfma_f32_16x16x32_bf16 v[56:59], v[12:15], v[12:15], 0\n\t"
      "s_waitcnt lgkmcnt(2)\n\t"
      "v_smfmac_f32_16x16x64_bf16 v[40:43], v[8:11], a[144:151], v122\n\t"
      "s_waitcnt vmcnt(1)\n\t"
      "v_fma_f32 v27, v95, s57, v126\n\t"
      "v_fma_f32 v26, v94, s57, v126\n\t"
      "v_smfmac_f32_16x16x64_bf16 v[32:35], v[8:11], a[16:23], v122\n\t"
      "s_nop 0\n\t"
      "v_fma_f32 v24, v92, s57, v126\n\t"
      "v_bitop3_b32 v64, v92, v93, v94 bitop3:0x80\n\t"
      "v_fma_f32 v25, v93, s57, v126\n\t"
      "v_cvt_pk_bf16_f32 v28, v24, v25\n\t"
      "v_smfmac_f32_16x16x64_bf16 v[36:39], v[8:11], a[80:87], v122\n\t"
      "v_cvt_pk_bf16_f32 v29, v26, v27\n\t"
      "v_smfmac_f32_16x16x64_bf16 v[44:47], v[8:11], a[208:215], v122\n\t"
      "s_waitcnt vmcnt(0)\n\t"
      "s_waitcnt lgkmcnt(0)\n\t"
      "v_fma_f32 v21, v97, s57, v126\n\t"
      "v_fma_f32 v23, v99, s57, v126\n\t"
      "v_fma_f32 v20, v96, s57, v126\n\t"
      "v_smfmac_f32_16x16x64_bf16 v[36:39], v[4:7], a[72:79], v122\n\t"
      "ds_write_b64 v127, v[28:29] offset:48128\n\t"
      "v_cvt_pk_bf16_f32 v30, v20, v21\n\t"
      "v_fma_f32 v22, v98, s57, v126\n\t"
      "v_cvt_pk_bf16_f32 v31, v22, v23\n\t"
      "v_smfmac_f32_16x16x64_bf16 v[36:39], v[0:3], a[64:71], v122\n\t"
      "ds_write_b64 v127, v[30:31] offset:48136\n\t"
      "v_smfmac_f32_16x16x64_bf16 v[44:47], v[4:7], a[200:207], v122\n\t"
      "v_bitop3_b32 v64, v64, v95, v96 bitop3:0x80\n\t"
      "v_smfmac_f32_16x16x64_bf16 v[40:43], v[4:7], a[136:143], v122\n\t"
      "v_bitop3_b32 v64, v64, v97, v98 bitop3:0x80\n\t"
      "v_bitop3_b32 v64, v64, v99, s60 bitop3:0x80\n\t"
      "v_smfmac_f32_16x16x64_bf16 v[44:47], v[0:3], a[192:199], v122\n\t"
      "v_cmp_eq_u32_e64 vcc, 0, v64\n\t"
      "v_smfmac_f32_16x16x64_bf16 v[32:35], v[4:7], a[8:15], v122\n\t"
      "s_waitcnt lgkmcnt(0)\n\t"
      "v_smfmac_f32_16x16x64_bf16 v[40:43], v[0:3], a[128:135], v122\n\t"
      "s_cbranch_vccnz LR1_3%=\n\t"
      "LC1_3%=:\n\t"
      "v_mfma_f32_16x16x32_bf16 v[56:59], v[28:31], v[28:31], v[56:59]\n\t"
      "s_barrier\n\t"
      "v_smfmac_f32_16x16x64_bf16 v[40:43], v[28:31], a[184:191], v122\n\t"
      "ds_read_b128 v[24:27], v127 offset:47104\n\t"
      "v_readfirstlane_b32 s56, v120\n\t"
      "v_smfmac_f32_16x16x64_bf16 v[36:39], v[28:31], a[120:127], v122\n\t"
      "ds_read_b128 v[20:23], v127 offset:46080\n\t"
      "v_smfmac_f32_16x16x64_bf16 v[32:35], v[0:3], a[0:7], v122\n\t"
      "s_add_u32 s68, s68, 0x8000\n\t"
      "ds_read_b128 v[16:19], v127 offset:45056\n\t"
      "v_max3_f32 v56, v56, v57, v58\n\t"
      "v_smfmac_f32_16x16x64_bf16 v[32:35], v[28:31], a[56:63], v122\n\t"
      "ds_read_b128 v[60:63], v127 offset:37888\n\t"
      "v_smfmac_f32_16x16x64_bf16 v[44:47], v[28:31], a[248:255], v122\n\t"
      "s_waitcnt lgkmcnt(3)\n\t"
      "v_max_f32 v56, v56, v59\n\t"
      "v_smfmac_f32_16x16x64_bf16 v[32:35], v[24:27], a[48:55], v122\n\t"
      "s_waitcnt lgkmcnt(2)\n\t"
      "ds_read_b128 v[88:91], v127 offset:39936\n\t"
      "v_smfmac_f32_16x16x64_bf16 v[32:35], v[20:23], a[40:47], v122\n\t"
      "s_waitcnt lgkmcnt(2)\n\t"
      "v_smfmac_f32_16x16x64_bf16 v[32:35], v[16:19], a[32:39], v122\n\t"
      "ds_read_b128 v[80:83], v127 offset:35840\n\t"
      "v_max_f32_dpp v56, v56, v56 quad_perm:[1,0,3,2] row_mask:0xf bank_mask:0xf\n\t"
      "v_smfmac_f32_16x16x64_bf16 v[36:39], v[24:27], a[112:119], v122\n\t"
      "ds_read_b128 v[84:87], v127 offset:36864\n\t"
      "v_max_f32_dpp v56, v56, v56 quad_perm:[2,3,0,1] row_mask:0xf bank_mask:0xf\n\t"
      "v_smfmac_f32_16x16x64_bf16 v[36:39], v[20:23], a[104:111], v122\n\t"
      "ds_read_b128 v[76:79], v127 offset:32768\n\t"
      "v_add_f32_e64 v33, v33, v35\n\t"
      "v_smfmac_f32_16x16x64_bf16 v[36:39], v[16:19], a[96:103], v122\n\t"
      "s_mov_b32 exec_lo, 0x100001\n\t"
      "s_mov_b32 exec_hi, 0x10000100\n\t"
      "v_smfmac_f32_16x16x64_bf16 v[44:47], v[24:27], a[240:247], v122\n\t"
      "ds_max_i32 v109, v56 offset:55392\n\t"
      "v_smfmac_f32_16x16x64_bf16 v[44:47], v[20:23], a[232:239], v122\n\t"
      "s_mov_b64 exec, -1\n\t"
      "v_add_f32 v32, v32, v34\n\t"
      "v_add_f32 v37, v37, v39\n\t"
      "v_mul_f32 v37, s59, v37\n\t"
      "v_smfmac_f32_16x16x64_bf16 v[44:47], v[16:19], a[224:231], v122\n\t"
      "ds_read_b128 v[92:95], v127 offset:34816\n\t"
      "v_add_f32_e64 v36, v36, v38\n\t"
      "v_smfmac_f32_16x16x64_bf16 v[40:43], v[24:27], a[176:183], v122\n\t"
      "ds_read_b128 v[56:59], v127 offset:38912\n\t"
      "v_mul_f32 v32, s59, v32\n\t"
      "v_mul_f32 v33, s59, v33\n\t"
      "v_cvt_pk_bf16_f32 v112, v32, v33\n\t"
      "v_smfmac_f32_16x16x64_bf16 v[40:43], v[20:23], a[168:175], v122\n\t"
      "ds_read_b128 v[96:99], v127 offset:33792\n\t"
      "v_mul_f32_e64 v36, s59, v36\n\t"
      "v_cvt_pk_bf16_f32 v113, v36, v37\n\t"
      "v_smfmac_f32_16x16x64_bf16 v[40:43], v[16:19], a[160:167], v122\n\t"
      "ds_write_b64 v125, v[112:113]\n\t"
      "v_smfmac_f32_16x16x64_bf16 v[48:51], v[8:11], v[144:151], v122\n\t"
      "s_mov_b32 s58, 0x10000000\n\t"
      "v_add_f32 v45, v45, v47\n\t"
      "v_add_f32 v44, v44, v46\n\t"
      "v_cvt_pk_bf16_f32 v121, v44, v45\n\t"
      "v_add_f32 v41, v41, v43\n\t"
      "v_add_f32 v40, v40, v42\n\t"
      "v_cvt_pk_bf16_f32 v120, v40, v41\n\t"
      "v_smfmac_f32_16x16x64_bf16 v[48:51], v[4:7], v[136:143], v122\n\t"
      "ds_write_b64 v125, v[120:121] offset:2080\n\t"
      "s_waitcnt lgkmcnt(1)\n\t"
      "v_smfmac_f32_16x16x64_bf16 v[48:51], v[0:3], v[128:135], v122\n\t"
      "s_barrier\n\t"
      "ds_read_b32 v108, v109 offset:55392\n\t"
      "v_smfmac_f32_16x16x64_bf16 v[48:51], v[28:31], v[184:191], v122\n\t"
      "ds_read_b64_tr_b16 v[32:33], v124 offset:49152\n\t"
      "ds_read_b64_tr_b16 v[34:35], v124 offset:49672\n\t"
      "v_smfmac_f32_16x16x64_bf16 v[48:51], v[24:27], v[176:183], v122\n\t"
      "ds_read_b64_tr_b16 v[38:39], v124 offset:50712\n\t"
      "s_waitcnt lgkmcnt(4)\n\t"
      "ds_read_b64_tr_b16 v[36:37], v124 offset:50192\n\t"
      "v_smfmac_f32_16x16x64_bf16 v[52:55], v[8:11], v[208:215], v122\n\t"
      "s_barrier\n\t"
      "ds_read_b64_tr_b16 v[46:47], v124 offset:52792\n\t"
      "v_smfmac_f32_16x16x64_bf16 v[48:51], v[20:23], v[168:175], v122\n\t"
      "s_waitcnt lgkmcnt(5)\n\t"
      "ds_read_b64_tr_b16 v[42:43], v124 offset:51752\n\t"
      "ds_read_b64_tr_b16 v[44:45], v124 offset:52272\n\t"
      "v_readfirstlane_b32 s61, v108\n\t"
      "v_smfmac_f32_16x16x64_bf16 v[48:51], v[16:19], v[160:167], v122\n\t"
      "s_waitcnt lgkmcnt(5)\n\t"
      "ds_read_b64_tr_b16 v[40:41], v124 offset:51232\n\t"
      "v_mov_b32_dpp v103, v35 quad_perm:[2,3,0,1] row_mask:0xf bank_mask:0xf\n\t"
      "s_waitcnt lgkmcnt(4)\n\t"
      "v_mfma_f32_16x16x32_bf16 v[116:119], v[80:83], v[32:35], 0\n\t"
      "s_lshr_b32 s61, s61, 23\n\t"
      "s_add_u32 s61, s61, s67\n\t"
      "v_mov_b32_dpp v107, v39 quad_perm:[2,3,0,1] row_mask:0xf bank_mask:0xf\n\t"
      "v_mfma_f32_16x16x32_bf16 v[112:115], v[76:79], v[32:35], 0\n\t"
      "s_max_u32 s61, s61, s66\n\t"
      "s_lshr_b32 s61, s61, 1\n\t"
      "v_mov_b32_dpp v106, v38 quad_perm:[2,3,0,1] row_mask:0xf bank_mask:0xf\n\t"
      "v_mfma_f32_16x16x32_bf16 v[112:115], v[84:87], v[36:39], v[112:115]\n\t"
      "s_min_u32 s61, s61, 200\n\t"
      "v_mov_b32_dpp v104, v36 quad_perm:[2,3,0,1] row_mask:0xf bank_mask:0xf\n\t"
      "v_mov_b32_dpp v101, v33 quad_perm:[2,3,0,1] row_mask:0xf bank_mask:0xf\n\t"
      "s_waitcnt lgkmcnt(0)\n\t"
      "v_mfma_f32_16x16x32_bf16 v[76:79], v[40:43], v[32:35], 0\n\t"
      "buffer_load_dwordx4 v127, s[36:39], s46 offen sc1 lds\n\t"
      "v_mfma_f32_16x16x32_bf16 v[76:79], v[44:47], v[36:39], v[76:79]\n\t"
      "s_lshl_b32 s61, s61, 23\n\t"
      "v_mov_b32_dpp v105, v37 quad_perm:[2,3,0,1] row_mask:0xf bank_mask:0xf\n\t"
      "s_add_u32 s65, s61, 33554432\n\t"
      "v_add_f32_e64 v49, v49, v51\n\t"
      "v_mov_b32_dpp v102, v34 quad_perm:[2,3,0,1] row_mask:0xf bank_mask:0xf\n\t"
      "v_mfma_f32_16x16x32_bf16 v[116:119], v[88:91], v[36:39], v[116:119]\n\t"
      "s_pack_hh_b32_b16 s75, s65, s65\n\t"
      "v_add_f32 v48, v48, v50\n\t"
      "v_cvt_pk_bf16_f32 v64, v48, v49\n\t"
      "v_mov_b32_dpp v100, v32 quad_perm:[2,3,0,1] row_mask:0xf bank_mask:0xf\n\t"
      "v_cndmask_b32_e64 v120, v76, v78, s[72:73]\n\t"
      "v_cndmask_b32_e64 v121, v77, v79, s[72:73]\n\t"
      "v_mfma_f32_16x16x32_bf16 v[112:115], v[92:95], v[100:103], v[112:115]\n\t"
      "buffer_load_dwordx4 v127, s[36:39], s46 offen offset:1024 sc1 lds\n\t"
      "v_mfma_f32_16x16x32_bf16 v[116:119], v[96:99], v[100:103], v[116:119]\n\t"
      "v_add_f32_dpp v121, v121, v121 quad_perm:[2,3,0,1] row_mask:0xf bank_mask:0xf\n\t"
      "v_add_f32_dpp v120, v120, v120 quad_perm:[2,3,0,1] row_mask:0xf bank_mask:0xf\n\t"
      "v_mfma_f32_16x16x32_bf16 v[116:119], v[60:63], v[104:107], v[116:119]\n\t"
      "s_mov_b32 m0, s45\n\t"
      "v_exp_f32 v80, v120\n\t"
      "v_exp_f32_e64 v81, v121\n\t"
      "v_mfma_f32_16x16x32_bf16 v[112:115], v[56:59], v[104:107], v[112:115]\n\t"
      "s_pack_hh_b32_b16 s74, s65, s65\n\t"
      "v_mov_b64 v[90:91], s[74:75]\n\t"
      "v_cvt_pk_bf16_f32 v108, v80, v81\n\t"
      "v_smfmac_f32_16x16x64_bf16 v[52:55], v[4:7], v[200:207], v122\n\t"
      "buffer_load_dwordx4 v65, s[36:39], s44 offen offset:1024 sc1 lds\n\t"
      "v_mfma_f32_16x16x32_bf16 v[84:87], v[108:111], v[64:67], 0\n\t"
      "v_cndmask_b32_e64 v115, v115, v119, s[72:73]\n\t"
      "v_cndmask_b32_e64 v113, v113, v117, s[72:73]\n\t"
      "v_cndmask_b32_e64 v114, v114, v118, s[72:73]\n\t"
      "v_mov_b64 v[88:89], s[74:75]\n\t"
      "v_exp_f32 v58, v114\n\t"
      "v_exp_f32 v59, v115\n\t"
      "v_exp_f32 v57, v113\n\t"
      "v_cndmask_b32_e64 v112, v112, v116, s[72:73]\n\t"
      "v_exp_f32_e64 v56, v112\n\t"
      "v_cvt_pk_bf16_f32 v97, v58, v59\n\t"
      "v_cvt_pk_bf16_f32 v96, v56, v57\n\t"
      "v_mfma_f32_16x16x32_bf16 v[48:51], v[108:111], v[88:91], 0\n\t"
      "v_mov_b32_dpp v99, v97 quad_perm:[2,3,0,1] row_mask:0xf bank_mask:0xf\n\t"
      "v_mov_b32_dpp v98, v96 quad_perm:[2,3,0,1] row_mask:0xf bank_mask:0xf\n\t"
      "v_max3_f32 v83, |v115|, |v120|, |v121|\n\t"
      "v_max3_f32 v82, |v112|, |v113|, |v114|\n\t"
      "v_mfma_f32_16x16x32_bf16 v[48:51], v[96:99], v[88:91], v[48:51]\n\t"
      "buffer_load_dwordx4 v65, s[36:39], s44 offen sc1 lds\n\t"
      "v_mfma_f32_16x16x32_bf16 v[84:87], v[96:99], v[68:71], v[84:87]\n\t"
      "v_max_f32_e64 v82, v82, v83\n\t"
      "v_mfma_f32_16x16x32_bf16 v[92:95], v[96:99], v[72:75], 0\n\t"
      "s_add_u32 s43, s43, 0x8000\n\t"
      "v_cmp_nge_f32 vcc, 0x42000000, v82\n\t"
      "v_rcp_f32 v48, v48\n\t"
      "v_rcp_f32 v49, v49\n\t"
      "v_fma_f32 v84, v84, v48, s96\n\t"
      "v_fma_f32 v85, v85, v49, s96\n\t"
      "s_cbranch_vccnz LMX_3%=\n\t"
      "buffer_atomic_add v85, v123, s[36:39], s97 offen offset:512\n\t"
      "buffer_atomic_add v84, v123, s[36:39], s97 offen\n\t"
      "v_smfmac_f32_16x16x64_bf16 v[52:55], v[0:3], v[192:199], v122\n\t"
      "v_smfmac_f32_16x16x64_bf16 v[52:55], v[28:31], v[248:255], v122\n\t"
      "v_smfmac_f32_16x16x64_bf16 v[52:55], v[24:27], v[240:247], v122\n\t"
      "v_smfmac_f32_16x16x64_bf16 v[52:55], v[20:23], v[232:239], v122\n\t"
      "s_nop 0\n\t"
      "v_smfmac_f32_16x16x64_bf16 v[52:55], v[16:19], v[224:231], v122\n\t"
      "s_nop 7\n\t"
      "v_add_f32 v53, v53, v55\n\t"
      "v_add_f32_e64 v52, v52, v54\n\t"
      "v_cvt_pk_bf16_f32 v66, v52, v53\n\t"
      "s_nop 1\n\t"
      "v_mfma_f32_16x16x32_bf16 v[92:95], v[108:111], v[66:69], v[92:95]\n\t"
      "s_nop 7\n\t"
      "v_fma_f32 v94, v94, v48, s96\n\t"
      "buffer_atomic_add v94, v123, s[36:39], s69 offen\n\t"
      "s_add_u32 s62, s61, 0x83400000\n\t"
      "v_fma_f32 v95, v95, v49, s96\n\t"
      "buffer_atomic_add v95, v123, s[36:39], s69 offen offset:512\n\t"
      "s_add_u32 s57, s61, 310378496\n\t"
      "s_add_u32 s97, s97, 0x8000\n\t"
      "v_mov_b32 v126, s62\n\t"
      "LWIN_3%=:\n\t"
      "v_add_u32 v100, s55, v127\n\t"
      "ds_read_b128 v[60:63], v100 offset:57344\n\t"
      "s_cmp_eq_u32 s56, 0\n\t"
      "s_cbranch_scc0 LSLOW_3%=\n\t"
      "LWIN1_3%=:\n\t"
      "buffer_load_dwordx2 v[120:121], off, s[36:39], s50 sc1\n\t"
      "s_mov_b32 s64, s50\n\t"
      "s_cmp_lt_i32 s40, 2\n\t"
      "s_cbranch_scc1 LEARLY_3%=\n\t"
      "LROLE_3%=:\n\t"
      "v_add_u32 v52, 67584, v127\n\t"
      "ds_read_b128 v[88:91], v52 offset:16384\n\t"
      "ds_read_b128 v[92:95], v52 offset:17408\n\t"
      "ds_read_b128 v[32:35], v52 offset:20480\n\t"
      "ds_read_b128 v[36:39], v52 offset:21504\n\t"
      "ds_read_b128 v[40:43], v52 offset:24576\n\t"
      "ds_read_b128 v[44:47], v52 offset:25600\n\t"
      "v_mov_b64 v[48:49], 0\n\t"
      "v_mov_b64 v[50:51], 0\n\t"
      "v_add_u32 v104, s71, v100\n\t"
      "ds_read_b128 v[76:79], v104\n\t"
      "ds_read_b128 v[80:83], v104 offset:1024\n\t"
      "ds_read_b128 v[112:115], v100\n\t"
      "ds_read_b128 v[116:119], v100 offset:1024\n\t"
      "v_xor_b32 v101, 2048, v104\n\t"
      "v_xor_b32 v102, 4096, v104\n\t"
      "v_xor_b32 v103, 6144, v104\n\t"
      "s_waitcnt lgkmcnt(8)\n\t"
      "v_smfmac_f32_16x16x64_bf16 v[48:51], v[16:19], v[88:95], v122\n\t"
      "ds_read_b128 v[88:91], v52 offset:28672\n\t"
      "ds_read_b128 v[92:95], v52 offset:29696\n\t"
      "s_sub_u32 s61, s88, s78\n\t"
      "s_sub_i32 s61, s61, 564\n\t"
      "s_waitcnt lgkmcnt(8)\n\t"
      "v_smfmac_f32_16x16x64_bf16 v[48:51], v[20:23], v[32:39], v122\n\t"
      "ds_read_b128 v[0:3], v101\n\t"
      "ds_read_b128 v[4:7], v101 offset:1024\n\t"
      "s_min_i32 s61, s61, 64\n\t"
      "s_max_i32 s61, s61, -96\n\t"
      "s_waitcnt lgkmcnt(8)\n\t"
      "v_smfmac_f32_16x16x64_bf16 v[48:51], v[24:27], v[40:47], v122\n\t"
      "ds_read_b128 v[8:11], v100 offset:8192\n\t"
      "ds_read_b128 v[12:15], v100 offset:9216\n\t"
      "s_ashr_i32 s61, s61, 2\n\t"
      "s_cmp_eq_u32 s63, 0x2000\n\t"
      "s_waitcnt lgkmcnt(4)\n\t"
      "v_smfmac_f32_16x16x64_bf16 v[48:51], v[28:31], v[88:95], v122\n\t"
      "s_cselect_b32 s61, s61, 24\n\t"
      "s_add_u32 s26, s26, s61\n\t"
      "ds_read_b128 v[16:19], v102\n\t"
      "ds_read_b128 v[20:23], v102 offset:1024\n\t"
      "ds_read_b128 v[24:27], v100 offset:16384\n\t"
      "ds_read_b128 v[28:31], v100 offset:17408\n\t"
      "s_max_u32 s26, s26, 2100\n\t"
      "s_min_u32 s26, s26, 2900\n\t"
      "s_add_u32 m0, s55, s71\n\t"
      "s_sub_u32 s71, 204800, s71\n\t"
      "s_add_u32 s40, s40, 1\n\t"
      "s_cmp_lt_u32 s40, 63\n\t"
      "s_cselect_b32 s61, 786432, 0\n\t"
      "s_cselect_b32 s60, s58, 0\n\t"
      "s_add_u32 s44, s44, s61\n\t"
      "s_cmp_lt_u32 s40, 62\n\t"
      "s_cselect_b32 s61, 786432, 0\n\t"
      "s_add_u32 s46, s46, s61\n\t"
      "s_add_u32 s61, s40, 3\n\t"
      "s_and_b32 s62, s61, 3\n\t"
      "s_cselect_b32 s62, 16, 2000\n\t"
      "s_cmp_lt_u32 s61, 64\n\t"
      "s_cselect_b32 s62, s62, 0\n\t"
      "s_add_u32 s50, s50, s62\n\t"
      "s_mov_b32 s63, 0x2000\n\t"
      "s_cmp_eq_u32 s40, 64\n\t"
      "v_add_f32 v48, v48, v50\n\t"
      "v_add_f32 v49, v49, v51\n\t"
      "ds_write_b64 v124, v[48:49] offset:53832\n\t"
      "s_cbranch_scc1 LFIN_3%=\n\t"
      "v_mfma_f32_16x16x32_bf16 v[32:35], v[76:79], v[112:115], 0\n\t"
      "v_mfma_f32_16x16x32_bf16 v[36:39], v[80:83], v[112:115], 0\n\t"
      "v_mfma_f32_16x16x32_bf16 v[40:43], v[76:79], v[116:119], 0\n\t"
      "v_mfma_f32_16x16x32_bf16 v[44:47], v[80:83], v[116:119], 0\n\t"
      "ds_read_b128 v[76:79], v103\n\t"
      "ds_read_b128 v[80:83], v103 offset:1024\n\t"
      "ds_read_b128 v[112:115], v100 offset:24576\n\t"
      "ds_read_b128 v[116:119], v100 offset:25600\n\t"
      "LGATE_3%=:\n\t"
      "s_memtime s[92:93]\n\t"
      "s_waitcnt lgkmcnt(0)\n\t"
      "s_sub_u32 s61, s92, s88\n\t"
      "s_sub_i32 s61, s26, s61\n\t"
      "s_sub_i32 s61, s61, 70\n\t"
      "s_min_i32 s61, s61, 3000\n\t"
      "s_getpc_b64 s[92:93]\n\t"
      "LDPC_3%=:\n\t"
      "s_max_i32 s61, s61, 0\n\t"
      "s_andn2_b32 s62, 28, s61\n\t"
      "s_add_u32 s62, s62, LDSL_3%=-LDPC_3%=\n\t"
      "s_add_u32 s92, s92, s62\n\t"
      "s_addc_u32 s93, s93, 0\n\t"
      "LDLY_3%=:\n\t"
      "s_sub_i32 s61, s61, 32\n\t"
      "s_cmp_ge_i32 s61, 0\n\t"
      "s_cbranch_scc1 LDLY_3%=\n\t"
      "s_setpc_b64 s[92:93]\n\t"
      "LDSL_3%=:\n\t"
      "s_nop 0\n\t"
      "s_nop 0\n\t"
      "s_nop 0\n\t"
      "s_nop 0\n\t"
      "s_nop 0\n\t"
      "s_nop 0\n\t"
      "s_nop 0\n\t"
      "LBAR_3%=:\n\t"
      "s_cmp_gt_u32 s40, 1\n\t"
      "s_barrier\n\t"
      "s_cbranch_scc1 LPOLL_3%=\n\t"
      "s_branch LFIRST_3%=\n\t"
      "LUTOK_0%=:\n\t"
      "v_add_u32 v104, s71, v100\n\t"
      "ds_read_b128 v[76:79], v104\n\t"
      "ds_read_b128 v[80:83], v104 offset:1024\n\t"
      "ds_read_b128 v[112:115], v100\n\t"
      "ds_read_b128 v[116:119], v100 offset:1024\n\t"
      "v_xor_b32 v101, 2048, v104\n\t"
      "v_xor_b32 v102, 4096, v104\n\t"
      "v_xor_b32 v103, 6144, v104\n\t"
      "ds_read_b128 v[16:19], v101\n\t"
      "ds_read_b128 v[20:23], v101 offset:1024\n\t"
      "ds_read_b128 v[24:27], v100 offset:8192\n\t"
      "ds_read_b128 v[28:31], v100 offset:9216\n\t"
      "ds_read_b128 v[0:3], v102\n\t"
      "ds_read_b128 v[4:7], v102 offset:1024\n\t"
      "ds_read_b128 v[8:11], v100 offset:16384\n\t"
      "ds_read_b128 v[12:15], v100 offset:17408\n\t"
      "s_mov_b32 s28, s52\n\t"
      "s_add_u32 m0, s55, s71\n\t"
      "s_sub_u32 s71, 204800, s71\n\t"
      "s_add_u32 s40, s40, 1\n\t"
      "s_cmp_lt_u32 s40, 63\n\t"
      "s_cselect_b32 s61, 786432, 0\n\t"
      "s_cselect_b32 s60, s58, 0\n\t"
      "s_add_u32 s44, s44, s61\n\t"
      "s_cmp_lt_u32 s40, 62\n\t"
      "s_cselect_b32 s61, 786432, 0\n\t"
      "s_add_u32 s46, s46, s61\n\t"
      "s_add_u32 s61, s40, 3\n\t"
      "s_and_b32 s62, s61, 3\n\t"
      "s_cselect_b32 s62, 16, 2000\n\t"
      "s_cmp_lt_u32 s61, 64\n\t"
      "s_cselect_b32 s62, s62, 0\n\t"
      "s_add_u32 s50, s50, s62\n\t"
      "s_mov_b32 s63, 0x2000\n\t"
      "s_waitcnt lgkmcnt(8)\n\t"
      "v_mfma_f32_16x16x32_bf16 v[32:35], v[76:79], v[112:115], 0\n\t"
      "v_mfma_f32_16x16x32_bf16 v[36:39], v[80:83], v[112:115], 0\n\t"
      "v_mfma_f32_16x16x32_bf16 v[40:43], v[76:79], v[116:119], 0\n\t"
      "v_mfma_f32_16x16x32_bf16 v[44:47], v[80:83], v[116:119], 0\n\t"
      "ds_read_b128 v[76:79], v103\n\t"
      "ds_read_b128 v[80:83], v103 offset:1024\n\t"
      "ds_read_b128 v[112:115], v100 offset:24576\n\t"
      "ds_read_b128 v[116:119], v100 offset:25600\n\t"
      "s_waitcnt lgkmcnt(8)\n\t"
      "v_mfma_f32_16x16x32_bf16 v[32:35], v[16:19], v[24:27], v[32:35]\n\t"
      "v_mfma_f32_16x16x32_bf16 v[36:39], v[20:23], v[24:27], v[36:39]\n\t"
      "v_mfma_f32_16x16x32_bf16 v[40:43], v[16:19], v[28:31], v[40:43]\n\t"
      "v_mfma_f32_16x16x32_bf16 v[44:47], v[20:23], v[28:31], v[44:47]\n\t"
      "s_branch LGATE_0%=\n\t"
      "LFIN_0%=:\n\t"
      "s_waitcnt lgkmcnt(0)\n\t"
      "s_barrier\n\t"
      "ds_read_b64 v[54:55], v124 offset:53312\n\t"
      "s_waitcnt lgkmcnt(0)\n\t"
      "v_add_f32 v48, v48, v54\n\t"
      "v_add_f32 v49, v49, v55\n\t"
      "v_cvt_pk_bf16_f32 v48, v48, v49\n\t"
      "buffer_store_dword v48, v62, s[36:39], s28 offen sc1\n\t"
      "s_branch LEND%=\n\t"
      "LFIRST_0%=:\n\t"
      "s_cmp_eq_u32 s40, 0\n\t"
      "s_cbranch_scc1 LWM0_0%=\n\t"
      "LDEC_0%=:\n\t"
      "s_waitcnt vmcnt(0)\n\t"
      "v_readfirstlane_b32 s61, v106\n\t"
      "s_mov_b32 s62, 72960\n\t"
      "s_sub_u32 s63, s63, 1\n\t"
      "buffer_load_dword v106, off, s[36:39], s62 sc1\n\t"
      "s_cmp_eq_u32 s63, 0\n\t"
      "s_cbranch_scc1 LDEC1_0%=\n\t"
      "s_cmp_lt_u32 s61, 67108864\n\t"
      "s_cbranch_scc1 LDEC_0%=\n\t"
      "LDEC1_0%=:\n\t"
      "s_cmp_gt_u32 s61, 67141632\n\t"
      "s_cselect_b32 s99, 0x2000, 0\n\t"
      "s_xor_b32 s97, s97, s99\n\t"
      "s_barrier\n\t"
      "s_mov_b32 s63, 0x2000\n\t"
      "s_branch LPOLL_0%=\n\t"
      "LWM0_0%=:\n\t"
      "s_mov_b32 s63, 0x10000\n\t"
      "LWM_0%=:\n\t"
      "s_waitcnt vmcnt(0) lgkmcnt(0)\n\t"
      "v_bitop3_b32 v93, v93, v95, s53 bitop3:0x7e\n\t"
      "v_max_u32 v92, v92, v94\n\t"
      "v_cmp_ne_u32 vcc, 0, v93\n\t"
      "s_nop 1\n\t"
      "v_max_u32_dpp v92, v92, v92 quad_perm:[1,0,3,2] row_mask:0xf bank_mask:0xf\n\t"
      "s_nop 1\n\t"
      "v_max_u32_dpp v92, v92, v92 quad_perm:[2,3,0,1] row_mask:0xf bank_mask:0xf\n\t"
      "s_nop 1\n\t"
      "v_max_u32_dpp v92, v92, v92 row_ror:4 row_mask:0xf bank_mask:0xf\n\t"
      "s_nop 1\n\t"
      "v_max_u32_dpp v92, v92, v92 row_ror:8 row_mask:0xf bank_mask:0xf\n\t"
      "v_mov_b32 v93, v92\n\t"
      "s_nop 1\n\t"
      "v_permlane16_swap_b32 v92, v93\n\t"
      "v_max_u32 v92, v92, v93\n\t"
      "v_mov_b32 v93, v92\n\t"
      "s_nop 1\n\t"
      "v_permlane32_swap_b32 v92, v93\n\t"
      "v_max_u32 v92, v92, v93\n\t"
      "v_mul_f32 v93, 0x40a00000, v92\n\t"
      "s_nop 0\n\t"
      "v_readfirstlane_b32 s67, v93\n\t"
      "v_mul_f32 v93, 0x43030000, v93\n\t"
      "s_nop 0\n\t"
      "v_readfirstlane_b32 s66, v93\n\t"
      "s_cbranch_vccz LWM1_0%=\n\t"
      "s_sleep 4\n\t"
      "buffer_load_dwordx2 v[92:93], v91, s[36:39], s94 offen sc1\n\t"
      "buffer_load_dwordx2 v[94:95], v91, s[36:39], s94 offen offset:128 sc1\n\t"
      "s_sub_u32 s63, s63, 1\n\t"
      "s_cmp_lg_u32 s63, 0\n\t"
      "s_cbranch_scc1 LWM_0%=\n\t"
      "s_waitcnt vmcnt(0)\n\t"
      "LWM1_0%=:\n\t"
      "s_lshr_b32 s67, s67, 23\n\t"
      "s_add_u32 s67, s67, 5\n\t"
      "s_lshr_b32 s66, s66, 23\n\t"
      "s_add_u32 s66, s66, 129\n\t"
      "s_max_u32 s66, s66, 80\n\t"
      "buffer_load_dwordx2 v[120:121], off, s[36:39], s64 sc1\n\t"
      "s_mov_b32 s63, 0x2000\n\t"
      "s_branch LPOLL_0%=\n\t"
      "LR0_0%=:\n\t"
      "buffer_load_dwordx4 v[84:87], v60, s[36:39], s43 offen sc1\n\t"
      "buffer_load_dwordx4 v[88:91], v60, s[36:39], s43 offen offset:1024 sc1\n\t"
      "s_waitcnt vmcnt(2)\n\t"
      "buffer_load_dwordx4 v[92:95], v60, s[36:39], s68 offen sc1\n\t"
      "buffer_load_dwordx4 v[96:99], v60, s[36:39], s68 offen offset:1024 sc1\n\t"
      "s_sub_u32 s63, s63, 1\n\t"
      "s_mov_b64 exec, 1\n\t"
      "buffer_store_dword v109, off, s[36:39], s52 sc1\n\t"
      "s_mov_b64 exec, -1\n\t"
      "s_waitcnt vmcnt(3) lgkmcnt(0)\n\t"
      "v_bitop3_b32 v64, v84, v85, v86 bitop3:0x80\n\t"
      "v_bitop3_b32 v64, v64, v87, v88 bitop3:0x80\n\t"
      "v_bitop3_b32 v64, v64, v89, v90 bitop3:0x80\n\t"
      "v_bitop3_b32 v64, v64, v91, s58 bitop3:0x80\n\t"
      "v_cmp_eq_u32 vcc, 0, v64\n\t"
      "v_pk_fma_f32 v[4:5], v[84:85], s[56:57], v[126:127] op_sel:[0,1,0] op_sel_hi:[1,1,0]\n\t"
      "v_pk_fma_f32 v[6:7], v[86:87], s[56:57], v[126:127] op_sel:[0,1,0] op_sel_hi:[1,1,0]\n\t"
      "v_cvt_pk_bf16_f32 v0, v4, v5\n\t"
      "v_cvt_pk_bf16_f32 v1, v6, v7\n\t"
      "ds_write_b64 v127, v[0:1] offset:40960\n\t"
      "v_pk_fma_f32 v[8:9], v[88:89], s[56:57], v[126:127] op_sel:[0,1,0] op_sel_hi:[1,1,0]\n\t"
      "v_pk_fma_f32 v[10:11], v[90:91], s[56:57], v[126:127] op_sel:[0,1,0] op_sel_hi:[1,1,0]\n\t"
      "v_cvt_pk_bf16_f32 v2, v8, v9\n\t"
      "v_cvt_pk_bf16_f32 v3, v10, v11\n\t"
      "ds_write_b64 v127, v[2:3] offset:40968\n\t"
      "s_cmp_le_i32 s63, 0\n\t"
      "s_waitcnt lgkmcnt(0)\n\t"
      "s_cbranch_scc1 LRZ_0%=\n\t"
      "s_cbranch_vccnz LR0_0%=\n\t"
      "LRZ_0%=:\n\t"
      "v_mov_b64 v[32:33], 0\n\t"
      "v_mov_b64 v[34:35], 0\n\t"
      "v_mov_b64 v[36:37], 0\n\t"
      "v_mov_b64 v[38:39], 0\n\t"
      "v_mov_b64 v[40:41], 0\n\t"
      "v_mov_b64 v[42:43], 0\n\t"
      "v_mov_b64 v[44:45], 0\n\t"
      "v_mov_b64 v[46:47], 0\n\t"
      "v_mov_b64 v[48:49], 0\n\t"
      "v_mov_b64 v[50:51], 0\n\t"
      "v_mov_b64 v[52:53], 0\n\t"
      "v_mov_b64 v[54:55], 0\n\t"
      "v_smfmac_f32_16x16x64_bf16 v[32:35], v[0:3], a[0:7], v122\n\t"
      "v_smfmac_f32_16x16x64_bf16 v[48:51], v[0:3], v[128:135], v122\n\t"
      "s_branch LC0_0%=\n\t"
      "LR1_0%=:\n\t"
      "v_bitop3_b32 v64, v92, v93, v94 bitop3:0x80\n\t"
      "v_bitop3_b32 v64, v64, v95, v96 bitop3:0x80\n\t"
      "v_bitop3_b32 v64, v64, v97, v98 bitop3:0x80\n\t"
      "v_bitop3_b32 v64, v64, v99, s58 bitop3:0x80\n\t"
      "v_cmp_eq_u32 vcc, 0, v64\n\t"
      "s_nop 0\n\t"
      "s_cbranch_vccz LRX_0%=\n\t"
      "LRY_0%=:\n\t"
      "buffer_load_dwordx4 v[92:95], v60, s[36:39], s68 offen sc1\n\t"
      "buffer_load_dwordx4 v[96:99], v60, s[36:39], s68 offen offset:1024 sc1\n\t"
      "s_sub_u32 s63, s63, 1\n\t"
      "s_waitcnt vmcnt(0) lgkmcnt(0)\n\t"
      "v_bitop3_b32 v64, v92, v93, v94 bitop3:0x80\n\t"
      "v_bitop3_b32 v64, v64, v95, v96 bitop3:0x80\n\t"
      "v_bitop3_b32 v64, v64, v97, v98 bitop3:0x80\n\t"
      "v_bitop3_b32 v64, v64, v99, s58 bitop3:0x80\n\t"
      "v_cmp_eq_u32 vcc, 0, v64\n\t"
      "v_pk_fma_f32 v[20:21], v[92:93], s[56:57], v[126:127] op_sel:[0,1,0] op_sel_hi:[1,1,0]\n\t"
      "v_pk_fma_f32 v[22:23], v[94:95], s[56:57], v[126:127] op_sel:[0,1,0] op_sel_hi:[1,1,0]\n\t"
      "v_cvt_pk_bf16_f32 v16, v20, v21\n\t"
      "v_cvt_pk_bf16_f32 v17, v22, v23\n\t"
      "ds_write_b64 v127, v[16:17] offset:45056\n\t"
      "v_pk_fma_f32 v[24:25], v[96:97], s[56:57], v[126:127] op_sel:[0,1,0] op_sel_hi:[1,1,0]\n\t"
      "v_pk_fma_f32 v[26:27], v[98:99], s[56:57], v[126:127] op_sel:[0,1,0] op_sel_hi:[1,1,0]\n\t"
      "v_cvt_pk_bf16_f32 v18, v24, v25\n\t"
      "v_cvt_pk_bf16_f32 v19, v26, v27\n\t"
      "ds_write_b64 v127, v[18:19] offset:45064\n\t"
      "s_cmp_le_i32 s63, 0\n\t"
      "s_waitcnt lgkmcnt(0)\n\t"
      "s_cbranch_scc1 LRX_0%=\n\t"
      "s_cbranch_vccnz LRY_0%=\n\t"
      "LRX_0%=:\n\t"
      "s_cmp_eq_u32 s40, 63\n\t"
      "s_cbranch_scc1 LLAST_0%=\n\t"
      "s_branch LC1_0%=\n\t"
      "LMX_0%=:\n\t"
      "s_waitcnt lgkmcnt(0)\n\t"
      "s_nop 7\n\t"
      "v_smfmac_f32_16x16x64_bf16 v[52:55], v[12:15], v[216:223], v122\n\t"
      "v_smfmac_f32_16x16x64_bf16 v[52:55], v[16:19], v[224:231], v122\n\t"
      "v_smfmac_f32_16x16x64_bf16 v[52:55], v[20:23], v[232:239], v122\n\t"
      "v_smfmac_f32_16x16x64_bf16 v[52:55], v[24:27], v[240:247], v122\n\t"
      "v_smfmac_f32_16x16x64_bf16 v[52:55], v[28:31], v[248:255], v122\n\t"
      "s_nop 7\n\t"
      "v_add_f32 v53, v53, v55\n\t"
      "v_add_f32_e64 v52, v52, v54\n\t"
      "v_cvt_pk_bf16_f32 v66, v52, v53\n\t"
      "s_add_u32 s62, s61, 0x83400000\n\t"
      "s_add_u32 s57, s61, 310378496\n\t"
      "v_mov_b32 v126, s62\n\t"
      "v_max3_f32 v82, v112, v113, v114\n\t"
      "v_max_f32 v82, v82, v115\n\t"
      "s_nop 1\n\t"
      "v_max_f32_dpp v82, v82, v82 quad_perm:[2,3,0,1] row_mask:0xf bank_mask:0xf\n\t"
      "v_max3_f32 v83, v82, v120, v121\n\t"
      "v_max3_f32 v60, v82, v120, v121\n\t"
      "s_nop 1\n\t"
      "v_permlane16_swap_b32 v83, v60\n\t"
      "v_max_f32 v61, v83, v60\n\t"
      "v_max_f32 v60, v83, v60\n\t"
      "s_nop 1\n\t"
      "v_permlane32_swap_b32 v61, v60\n\t"
      "v_max_f32 v83, v61, v60\n\t"
      "v_sub_f32 v112, v112, v83\n\t"
      "v_sub_f32 v113, v113, v83\n\t"
      "v_sub_f32 v114, v114, v83\n\t"
      "v_sub_f32 v115, v115, v83\n\t"
      "v_sub_f32 v120, v120, v83\n\t"
      "v_sub_f32 v121, v121, v83\n\t"
      "v_exp_f32_e64 v56, v112\n\t"
      "v_exp_f32 v57, v113\n\t"
      "v_exp_f32 v58, v114\n\t"
      "v_exp_f32 v59, v115\n\t"
      "v_exp_f32 v80, v120\n\t"
      "v_exp_f32_e64 v81, v121\n\t"
      "v_cvt_pk_bf16_f32 v96, v56, v57\n\t"
      "v_cvt_pk_bf16_f32 v97, v58, v59\n\t"
      "v_cvt_pk_bf16_f32 v108, v80, v81\n\t"
      "v_mov_b32_dpp v98, v96 quad_perm:[2,3,0,1] row_mask:0xf bank_mask:0xf\n\t"
      "v_mov_b32_dpp v99, v97 quad_perm:[2,3,0,1] row_mask:0xf bank_mask:0xf\n\t"
      "v_mfma_f32_16x16x32_bf16 v[48:51], v[108:111], v[88:91], 0\n\t"
      "s_nop 0\n\t"
      "v_mfma_f32_16x16x32_bf16 v[48:51], v[96:99], v[88:91], v[48:51]\n\t"
      "v_mfma_f32_16x16x32_bf16 v[84:87], v[108:111], v[64:67], 0\n\t"
      "v_mfma_f32_16x16x32_bf16 v[84:87], v[96:99], v[68:71], v[84:87]\n\t"
      "v_mfma_f32_16x16x32_bf16 v[92:95], v[96:99], v[72:75], 0\n\t"
      "v_mfma_f32_16x16x32_bf16 v[92:95], v[108:111], v[66:69], v[92:95]\n\t"
      "s_nop 3\n\t"
      "v_rcp_f32 v48, v48\n\t"
      "v_rcp_f32 v49, v49\n\t"
      "v_fma_f32 v84, v84, v48, s96\n\t"
      "buffer_atomic_add v84, v123, s[36:39], s97 offen\n\t"
      "v_fma_f32 v85, v85, v49, s96\n\t"
      "buffer_atomic_add v85, v123, s[36:39], s97 offen offset:512\n\t"
      "v_fma_f32 v94, v94, v48, s96\n\t"
      "buffer_atomic_add v94, v123, s[36:39], s69 offen\n\t"
      "v_fma_f32 v95, v95, v49, s96\n\t"
      "buffer_atomic_add v95, v123, s[36:39], s69 offen offset:512\n\t"
      "s_add_u32 s97, s97, 0x8000\n\t"
      "s_branch LWIN_0%=\n\t"
      "LEARLY_0%=:\n\t"
      "s_cmp_lt_i32 s40, 0\n\t"
      "s_waitcnt lgkmcnt(0)\n\t"
      "s_cbranch_scc1 LUTOK_0%=\n\t"
      "s_xor_b32 s43, s43, s99\n\t"
      "s_xor_b32 s68, s68, s99\n\t"
      "s_mov_b32 s61, 72960\n\t"
      "buffer_load_dword v106, off, s[36:39], s61 sc1\n\t"
      "s_branch LROLE_0%=\n\t"
      "LSLOW_0%=:\n\t"
      "s_mov_b32 s63, 0x10000\n\t"
      "LSLOW1_0%=:\n\t"
      "buffer_load_dwordx2 v[92:93], off, s[36:39], s64 sc1\n\t"
      "s_waitcnt vmcnt(0)\n\t"
      "v_bitop3_b32 v92, v92, v93, s53 bitop3:0x7e\n\t"
      "s_nop 0\n\t"
      "v_readfirstlane_b32 s56, v92\n\t"
      "s_nop 1\n\t"
      "s_cmp_eq_u32 s56, 0\n\t"
      "s_cbranch_scc1 LSLOW2_0%=\n\t"
      "s_sleep 8\n\t"
      "s_sub_u32 s63, s63, 1\n\t"
      "s_cmp_lg_u32 s63, 0\n\t"
      "s_cbranch_scc1 LSLOW1_0%=\n\t"
      "LSLOW2_0%=:\n\t"
      "s_mov_b32 m0, s45\n\t"
      "s_sub_u32 s61, 204800, s71\n\t"
      "buffer_load_dwordx4 v65, s[36:39], s44 offen sc1 lds\n\t"
      "buffer_load_dwordx4 v65, s[36:39], s44 offen offset:1024 sc1 lds\n\t"
      "s_add_u32 m0, s55, s61\n\t"
      "s_nop 0\n\t"
      "buffer_load_dwordx4 v127, s[36:39], s46 offen sc1 lds\n\t"
      "buffer_load_dwordx4 v127, s[36:39], s46 offen offset:1024 sc1 lds\n\t"
      "s_branch LWIN1_0%=\n\t"
      "LLAST_0%=:\n\t"
      "s_waitcnt vmcnt(0) lgkmcnt(0)\n\t"
      "s_barrier\n\t"
      "ds_read_b128 v[20:23], v127 offset:46080\n\t"
      "ds_read_b128 v[24:27], v127 offset:47104\n\t"
      "ds_read_b128 v[28:31], v127 offset:48128\n\t"
      "v_smfmac_f32_16x16x64_bf16 v[40:43], v[16:19], a[160:167], v122\n\t"
      "v_smfmac_f32_16x16x64_bf16 v[44:47], v[16:19], a[224:231], v122\n\t"
      "s_waitcnt lgkmcnt(2)\n\t"
      "v_smfmac_f32_16x16x64_bf16 v[44:47], v[20:23], a[232:239], v122\n\t"
      "v_smfmac_f32_16x16x64_bf16 v[40:43], v[20:23], a[168:175], v122\n\t"
      "s_waitcnt lgkmcnt(1)\n\t"
      "v_smfmac_f32_16x16x64_bf16 v[44:47], v[24:27], a[240:247], v122\n\t"
      "v_smfmac_f32_16x16x64_bf16 v[40:43], v[24:27], a[176:183], v122\n\t"
      "s_waitcnt lgkmcnt(0)\n\t"
      "v_smfmac_f32_16x16x64_bf16 v[44:47], v[28:31], a[248:255], v122\n\t"
      "v_smfmac_f32_16x16x64_bf16 v[40:43], v[28:31], a[184:191], v122\n\t"
      "s_nop 7\n\t"
      "v_add_f32 v40, v40, v42\n\t"
      "v_add_f32 v41, v41, v43\n\t"
      "v_cvt_pk_bf16_f32 v120, v40, v41\n\t"
      "v_add_f32 v44, v44, v46\n\t"
      "v_add_f32 v45, v45, v47\n\t"
      "v_cvt_pk_bf16_f32 v121, v44, v45\n\t"
      "ds_write_b64 v125, v[120:121] offset:2080\n\t"
      "s_waitcnt lgkmcnt(0)\n\t"
      "s_barrier\n\t"
      "ds_read_b64_tr_b16 v[40:41], v124 offset:51232\n\t"
      "ds_read_b64_tr_b16 v[42:43], v124 offset:51752\n\t"
      "ds_read_b64_tr_b16 v[44:45], v124 offset:52272\n\t"
      "ds_read_b64_tr_b16 v[46:47], v124 offset:52792\n\t"
      "v_add_u32 v100, s55, v127\n\t"
      "ds_read_b128 v[60:63], v100 offset:57344\n\t"
      "s_waitcnt lgkmcnt(0)\n\t"
      "s_branch LROLE_0%=\n\t"
      "LUTOK_1%=:\n\t"
      "v_add_u32 v104, s71, v100\n\t"
      "ds_read_b128 v[76:79], v104\n\t"
      "ds_read_b128 v[80:83], v104 offset:1024\n\t"
      "ds_read_b128 v[112:115], v100\n\t"
      "ds_read_b128 v[116:119], v100 offset:1024\n\t"
      "v_xor_b32 v101, 2048, v104\n\t"
      "v_xor_b32 v102, 4096, v104\n\t"
      "v_xor_b32 v103, 6144, v104\n\t"
      "ds_read_b128 v[16:19], v101\n\t"
      "ds_read_b128 v[20:23], v101 offset:1024\n\t"
      "ds_read_b128 v[24:27], v100 offset:8192\n\t"
      "ds_read_b128 v[28:31], v100 offset:9216\n\t"
      "ds_read_b128 v[0:3], v102\n\t"
      "ds_read_b128 v[4:7], v102 offset:1024\n\t"
      "ds_read_b128 v[8:11], v100 offset:16384\n\t"
      "ds_read_b128 v[12:15], v100 offset:17408\n\t"
      "s_mov_b32 s28, s52\n\t"
      "s_add_u32 m0, s55, s71\n\t"
      "s_sub_u32 s71, 204800, s71\n\t"
      "s_add_u32 s40, s40, 1\n\t"
      "s_cmp_lt_u32 s40, 63\n\t"
      "s_cselect_b32 s61, 786432, 0\n\t"
      "s_cselect_b32 s60, s58, 0\n\t"
      "s_add_u32 s44, s44, s61\n\t"
      "s_cmp_lt_u32 s40, 62\n\t"
      "s_cselect_b32 s61, 786432, 0\n\t"
      "s_add_u32 s46, s46, s61\n\t"
      "s_add_u32 s61, s40, 3\n\t"
      "s_and_b32 s62, s61, 3\n\t"
      "s_cselect_b32 s62, 16, 2000\n\t"
      "s_cmp_lt_u32 s61, 64\n\t"
      "s_cselect_b32 s62, s62, 0\n\t"
      "s_add_u32 s50, s50, s62\n\t"
      "s_mov_b32 s63, 0x2000\n\t"
      "s_waitcnt lgkmcnt(8)\n\t"
      "v_mfma_f32_16x16x32_bf16 v[32:35], v[76:79], v[112:115], 0\n\t"
      "v_mfma_f32_16x16x32_bf16 v[36:39], v[80:83], v[112:115], 0\n\t"
      "v_mfma_f32_16x16x32_bf16 v[40:43], v[76:79], v[116:119], 0\n\t"
      "v_mfma_f32_16x16x32_bf16 v[44:47], v[80:83], v[116:119], 0\n\t"
      "ds_read_b128 v[76:79], v103\n\t"
      "ds_read_b128 v[80:83], v103 offset:1024\n\t"
      "ds_read_b128 v[112:115], v100 offset:24576\n\t"
      "ds_read_b128 v[116:119], v100 offset:25600\n\t"
      "s_waitcnt lgkmcnt(8)\n\t"
      "v_mfma_f32_16x16x32_bf16 v[32:35], v[16:19], v[24:27], v[32:35]\n\t"
      "v_mfma_f32_16x16x32_bf16 v[36:39], v[20:23], v[24:27], v[36:39]\n\t"
      "v_mfma_f32_16x16x32_bf16 v[40:43], v[16:19], v[28:31], v[40:43]\n\t"
      "v_mfma_f32_16x16x32_bf16 v[44:47], v[20:23], v[28:31], v[44:47]\n\t"
      "s_branch LGATE_1%=\n\t"
      "LFIN_1%=:\n\t"
      "s_waitcnt lgkmcnt(0)\n\t"
      "s_barrier\n\t"
      "ds_read_b64 v[54:55], v124 offset:53832\n\t"
      "s_waitcnt lgkmcnt(0)\n\t"
      "v_add_f32 v48, v48, v54\n\t"
      "v_add_f32 v49, v49, v55\n\t"
      "v_cvt_pk_bf16_f32 v48, v48, v49\n\t"
      "buffer_store_dword v48, v62, s[36:39], s28 offen sc1\n\t"
      "s_branch LEND%=\n\t"
      "LFIRST_1%=:\n\t"
      "s_cmp_eq_u32 s40, 0\n\t"
      "s_cbranch_scc1 LWM0_1%=\n\t"
      "LDEC_1%=:\n\t"
      "s_waitcnt vmcnt(0)\n\t"
      "v_readfirstlane_b32 s61, v106\n\t"
      "s_mov_b32 s62, 72960\n\t"
      "s_sub_u32 s63, s63, 1\n\t"
      "buffer_load_dword v106, off, s[36:39], s62 sc1\n\t"
      "s_cmp_eq_u32 s63, 0\n\t"
      "s_cbranch_scc1 LDEC1_1%=\n\t"
      "s_cmp_lt_u32 s61, 67108864\n\t"
      "s_cbranch_scc1 LDEC_1%=\n\t"
      "LDEC1_1%=:\n\t"
      "s_cmp_gt_u32 s61, 67141632\n\t"
      "s_cselect_b32 s99, 0x2000, 0\n\t"
      "s_xor_b32 s97, s97, s99\n\t"
      "s_barrier\n\t"
      "s_mov_b32 s63, 0x2000\n\t"
      "s_branch LPOLL_1%=\n\t"
      "LWM0_1%=:\n\t"
      "s_mov_b32 s63, 0x10000\n\t"
      "LWM_1%=:\n\t"
      "s_waitcnt vmcnt(0) lgkmcnt(0)\n\t"
      "v_bitop3_b32 v93, v93, v95, s53 bitop3:0x7e\n\t"
      "v_max_u32 v92, v92, v94\n\t"
      "v_cmp_ne_u32 vcc, 0, v93\n\t"
      "s_nop 1\n\t"
      "v_max_u32_dpp v92, v92, v92 quad_perm:[1,0,3,2] row_mask:0xf bank_mask:0xf\n\t"
      "s_nop 1\n\t"
      "v_max_u32_dpp v92, v92, v92 quad_perm:[2,3,0,1] row_mask:0xf bank_mask:0xf\n\t"
      "s_nop 1\n\t"
      "v_max_u32_dpp v92, v92, v92 row_ror:4 row_mask:0xf bank_mask:0xf\n\t"
      "s_nop 1\n\t"
      "v_max_u32_dpp v92, v92, v92 row_ror:8 row_mask:0xf bank_mask:0xf\n\t"
      "v_mov_b32 v93, v92\n\t"
      "s_nop 1\n\t"
      "v_permlane16_swap_b32 v92, v93\n\t"
      "v_max_u32 v92, v92, v93\n\t"
      "v_mov_b32 v93, v92\n\t"
      "s_nop 1\n\t"
      "v_permlane32_swap_b32 v92, v93\n\t"
      "v_max_u32 v92, v92, v93\n\t"
      "v_mul_f32 v93, 0x40a00000, v92\n\t"
      "s_nop 0\n\t"
      "v_readfirstlane_b32 s67, v93\n\t"
      "v_mul_f32 v93, 0x43030000, v93\n\t"
      "s_nop 0\n\t"
      "v_readfirstlane_b32 s66, v93\n\t"
      "s_cbranch_vccz LWM1_1%=\n\t"
      "s_sleep 4\n\t"
      "buffer_load_dwordx2 v[92:93], v91, s[36:39], s94 offen sc1\n\t"
      "buffer_load_dwordx2 v[94:95], v91, s[36:39], s94 offen offset:128 sc1\n\t"
      "s_sub_u32 s63, s63, 1\n\t"
      "s_cmp_lg_u32 s63, 0\n\t"
      "s_cbranch_scc1 LWM_1%=\n\t"
      "s_waitcnt vmcnt(0)\n\t"
      "LWM1_1%=:\n\t"
      "s_lshr_b32 s67, s67, 23\n\t"
      "s_add_u32 s67, s67, 5\n\t"
      "s_lshr_b32 s66, s66, 23\n\t"
      "s_add_u32 s66, s66, 129\n\t"
      "s_max_u32 s66, s66, 80\n\t"
      "buffer_load_dwordx2 v[120:121], off, s[36:39], s64 sc1\n\t"
      "s_mov_b32 s63, 0x2000\n\t"
      "s_branch LPOLL_1%=\n\t"
      "LR0_1%=:\n\t"
      "buffer_load_dwordx4 v[84:87], v60, s[36:39], s43 offen sc1\n\t"
      "buffer_load_dwordx4 v[88:91], v60, s[36:39], s43 offen offset:1024 sc1\n\t"
      "s_waitcnt vmcnt(2)\n\t"
      "buffer_load_dwordx4 v[92:95], v60, s[36:39], s68 offen sc1\n\t"
      "buffer_load_dwordx4 v[96:99], v60, s[36:39], s68 offen offset:1024 sc1\n\t"
      "s_sub_u32 s63, s63, 1\n\t"
      "s_mov_b64 exec, 1\n\t"
      "buffer_store_dword v109, off, s[36:39], s52 sc1\n\t"
      "s_mov_b64 exec, -1\n\t"
      "s_waitcnt vmcnt(3) lgkmcnt(0)\n\t"
      "v_bitop3_b32 v64, v84, v85, v86 bitop3:0x80\n\t"
      "v_bitop3_b32 v64, v64, v87, v88 bitop3:0x80\n\t"
      "v_bitop3_b32 v64, v64, v89, v90 bitop3:0x80\n\t"
      "v_bitop3_b32 v64, v64, v91, s58 bitop3:0x80\n\t"
      "v_cmp_eq_u32 vcc, 0, v64\n\t"
      "v_pk_fma_f32 v[0:1], v[84:85], s[56:57], v[126:127] op_sel:[0,1,0] op_sel_hi:[1,1,0]\n\t"
      "v_pk_fma_f32 v[2:3], v[86:87], s[56:57], v[126:127] op_sel:[0,1,0] op_sel_hi:[1,1,0]\n\t"
      "v_cvt_pk_bf16_f32 v4, v0, v1\n\t"
      "v_cvt_pk_bf16_f32 v5, v2, v3\n\t"
      "ds_write_b64 v127, v[4:5] offset:41984\n\t"
      "v_pk_fma_f32 v[12:13], v[88:89], s[56:57], v[126:127] op_sel:[0,1,0] op_sel_hi:[1,1,0]\n\t"
      "v_pk_fma_f32 v[14:15], v[90:91], s[56:57], v[126:127] op_sel:[0,1,0] op_sel_hi:[1,1,0]\n\t"
      "v_cvt_pk_bf16_f32 v6, v12, v13\n\t"
      "v_cvt_pk_bf16_f32 v7, v14, v15\n\t"
      "ds_write_b64 v127, v[6:7] offset:41992\n\t"
      "s_cmp_le_i32 s63, 0\n\t"
      "s_waitcnt lgkmcnt(0)\n\t"
      "s_cbranch_scc1 LRZ_1%=\n\t"
      "s_cbranch_vccnz LR0_1%=\n\t"
      "LRZ_1%=:\n\t"
      "v_mov_b64 v[32:33], 0\n\t"
      "v_mov_b64 v[34:35], 0\n\t"
      "v_mov_b64 v[36:37], 0\n\t"
      "v_mov_b64 v[38:39], 0\n\t"
      "v_mov_b64 v[40:41], 0\n\t"
      "v_mov_b64 v[42:43], 0\n\t"
      "v_mov_b64 v[44:45], 0\n\t"
      "v_mov_b64 v[46:47], 0\n\t"
      "v_mov_b64 v[48:49], 0\n\t"
      "v_mov_b64 v[50:51], 0\n\t"
      "v_mov_b64 v[52:53], 0\n\t"
      "v_mov_b64 v[54:55], 0\n\t"
      "v_smfmac_f32_16x16x64_bf16 v[32:35], v[4:7], a[8:15], v122\n\t"
      "v_smfmac_f32_16x16x64_bf16 v[48:51], v[4:7], v[136:143], v122\n\t"
      "s_branch LC0_1%=\n\t"
      "LR1_1%=:\n\t"
      "v_bitop3_b32 v64, v92, v93, v94 bitop3:0x80\n\t"
      "v_bitop3_b32 v64, v64, v95, v96 bitop3:0x80\n\t"
      "v_bitop3_b32 v64, v64, v97, v98 bitop3:0x80\n\t"
      "v_bitop3_b32 v64, v64, v99, s58 bitop3:0x80\n\t"
      "v_cmp_eq_u32 vcc, 0, v64\n\t"
      "s_nop 0\n\t"
      "s_cbranch_vccz LRX_1%=\n\t"
      "LRY_1%=:\n\t"
      "buffer_load_dwordx4 v[92:95], v60, s[36:39], s68 offen sc1\n\t"
      "buffer_load_dwordx4 v[96:99], v60, s[36:39], s68 offen offset:1024 sc1\n\t"
      "s_sub_u32 s63, s63, 1\n\t"
      "s_waitcnt vmcnt(0) lgkmcnt(0)\n\t"
      "v_bitop3_b32 v64, v92, v93, v94 bitop3:0x80\n\t"
      "v_bitop3_b32 v64, v64, v95, v96 bitop3:0x80\n\t"
      "v_bitop3_b32 v64, v64, v97, v98 bitop3:0x80\n\t"
      "v_bitop3_b32 v64, v64, v99, s58 bitop3:0x80\n\t"
      "v_cmp_eq_u32 vcc, 0, v64\n\t"
      "v_pk_fma_f32 v[16:17], v[92:93], s[56:57], v[126:127] op_sel:[0,1,0] op_sel_hi:[1,1,0]\n\t"
      "v_pk_fma_f32 v[18:19], v[94:95], s[56:57], v[126:127] op_sel:[0,1,0] op_sel_hi:[1,1,0]\n\t"
      "v_cvt_pk_bf16_f32 v20, v16, v17\n\t"
      "v_cvt_pk_bf16_f32 v21, v18, v19\n\t"
      "ds_write_b64 v127, v[20:21] offset:46080\n\t"
      "v_pk_fma_f32 v[28:29], v[96:97], s[56:57], v[126:127] op_sel:[0,1,0] op_sel_hi:[1,1,0]\n\t"
      "v_pk_fma_f32 v[30:31], v[98:99], s[56:57], v[126:127] op_sel:[0,1,0] op_sel_hi:[1,1,0]\n\t"
      "v_cvt_pk_bf16_f32 v22, v28, v29\n\t"
      "v_cvt_pk_bf16_f32 v23, v30, v31\n\t"
      "ds_write_b64 v127, v[22:23] offset:46088\n\t"
      "s_cmp_le_i32 s63, 0\n\t"
      "s_waitcnt lgkmcnt(0)\n\t"
      "s_cbranch_scc1 LRX_1%=\n\t"
      "s_cbranch_vccnz LRY_1%=\n\t"
      "LRX_1%=:\n\t"
      "s_cmp_eq_u32 s40, 63\n\t"
      "s_cbranch_scc1 LLAST_1%=\n\t"
      "s_branch LC1_1%=\n\t"
      "LMX_1%=:\n\t"
      "s_waitcnt lgkmcnt(0)\n\t"
      "s_nop 7\n\t"
      "v_smfmac_f32_16x16x64_bf16 v[52:55], v[8:11], v[208:215], v122\n\t"
      "v_smfmac_f32_16x16x64_bf16 v[52:55], v[20:23], v[232:239], v122\n\t"
      "v_smfmac_f32_16x16x64_bf16 v[52:55], v[16:19], v[224:231], v122\n\t"
      "v_smfmac_f32_16x16x64_bf16 v[52:55], v[28:31], v[248:255], v122\n\t"
      "v_smfmac_f32_16x16x64_bf16 v[52:55], v[24:27], v[240:247], v122\n\t"
      "s_nop 7\n\t"
      "v_add_f32 v53, v53, v55\n\t"
      "v_add_f32_e64 v52, v52, v54\n\t"
      "v_cvt_pk_bf16_f32 v66, v52, v53\n\t"
      "s_add_u32 s62, s61, 0x83400000\n\t"
      "s_add_u32 s57, s61, 310378496\n\t"
      "v_mov_b32 v126, s62\n\t"
      "v_max3_f32 v82, v112, v113, v114\n\t"
      "v_max_f32 v82, v82, v115\n\t"
      "s_nop 1\n\t"
      "v_max_f32_dpp v82, v82, v82 quad_perm:[2,3,0,1] row_mask:0xf bank_mask:0xf\n\t"
      "v_max3_f32 v83, v82, v120, v121\n\t"
      "v_max3_f32 v60, v82, v120, v121\n\t"
      "s_nop 1\n\t"
      "v_permlane16_swap_b32 v83, v60\n\t"
      "v_max_f32 v61, v83, v60\n\t"
      "v_max_f32 v60, v83, v60\n\t"
      "s_nop 1\n\t"
      "v_permlane32_swap_b32 v61, v60\n\t"
      "v_max_f32 v83, v61, v60\n\t"
      "v_sub_f32 v112, v112, v83\n\t"
      "v_sub_f32 v113, v113, v83\n\t"
      "v_sub_f32 v114, v114, v83\n\t"
      "v_sub_f32 v115, v115, v83\n\t"
      "v_sub_f32 v120, v120, v83\n\t"
      "v_sub_f32 v121, v121, v83\n\t"
      "v_exp_f32_e64 v56, v112\n\t"
      "v_exp_f32 v57, v113\n\t"
      "v_exp_f32 v58, v114\n\t"
      "v_exp_f32 v59, v115\n\t"
      "v_exp_f32 v80, v120\n\t"
      "v_exp_f32_e64 v81, v121\n\t"
      "v_cvt_pk_bf16_f32 v96, v56, v57\n\t"
      "v_cvt_pk_bf16_f32 v97, v58, v59\n\t"
      "v_cvt_pk_bf16_f32 v108, v80, v81\n\t"
      "v_mov_b32_dpp v98, v96 quad_perm:[2,3,0,1] row_mask:0xf bank_mask:0xf\n\t"
      "v_mov_b32_dpp v99, v97 quad_perm:[2,3,0,1] row_mask:0xf bank_mask:0xf\n\t"
      "v_mfma_f32_16x16x32_bf16 v[48:51], v[108:111], v[88:91], 0\n\t"
      "s_nop 0\n\t"
      "v_mfma_f32_16x16x32_bf16 v[48:51], v[96:99], v[88:91], v[48:51]\n\t"
      "v_mfma_f32_16x16x32_bf16 v[84:87], v[108:111], v[64:67], 0\n\t"
      "v_mfma_f32_16x16x32_bf16 v[84:87], v[96:99], v[68:71], v[84:87]\n\t"
      "v_mfma_f32_16x16x32_bf16 v[92:95], v[96:99], v[72:75], 0\n\t"
      "v_mfma_f32_16x16x32_bf16 v[92:95], v[108:111], v[66:69], v[92:95]\n\t"
      "s_nop 3\n\t"
      "v_rcp_f32 v48, v48\n\t"
      "v_rcp_f32 v49, v49\n\t"
      "v_fma_f32 v84, v84, v48, s96\n\t"
      "buffer_atomic_add v84, v123, s[36:39], s97 offen\n\t"
      "v_fma_f32 v85, v85, v49, s96\n\t"
      "buffer_atomic_add v85, v123, s[36:39], s97 offen offset:512\n\t"
      "v_fma_f32 v94, v94, v48, s96\n\t"
      "buffer_atomic_add v94, v123, s[36:39], s69 offen\n\t"
      "v_fma_f32 v95, v95, v49, s96\n\t"
      "buffer_atomic_add v95, v123, s[36:39], s69 offen offset:512\n\t"
      "s_add_u32 s97, s97, 0x8000\n\t"
      "s_branch LWIN_1%=\n\t"
      "LEARLY_1%=:\n\t"
      "s_cmp_lt_i32 s40, 0\n\t"
      "s_waitcnt lgkmcnt(0)\n\t"
      "s_cbranch_scc1 LUTOK_1%=\n\t"
      "s_xor_b32 s43, s43, s99\n\t"
      "s_xor_b32 s68, s68, s99\n\t"
      "s_mov_b32 s61, 72960\n\t"
      "buffer_load_dword v106, off, s[36:39], s61 sc1\n\t"
      "s_branch LROLE_1%=\n\t"
      "LSLOW_1%=:\n\t"
      "s_mov_b32 s63, 0x10000\n\t"
      "LSLOW1_1%=:\n\t"
      "buffer_load_dwordx2 v[92:93], off, s[36:39], s64 sc1\n\t"
      "s_waitcnt vmcnt(0)\n\t"
      "v_bitop3_b32 v92, v92, v93, s53 bitop3:0x7e\n\t"
      "s_nop 0\n\t"
      "v_readfirstlane_b32 s56, v92\n\t"
      "s_nop 1\n\t"
      "s_cmp_eq_u32 s56, 0\n\t"
      "s_cbranch_scc1 LSLOW2_1%=\n\t"
      "s_sleep 8\n\t"
      "s_sub_u32 s63, s63, 1\n\t"
      "s_cmp_lg_u32 s63, 0\n\t"
      "s_cbranch_scc1 LSLOW1_1%=\n\t"
      "LSLOW2_1%=:\n\t"
      "s_mov_b32 m0, s45\n\t"
      "s_sub_u32 s61, 204800, s71\n\t"
      "buffer_load_dwordx4 v65, s[36:39], s44 offen sc1 lds\n\t"
      "buffer_load_dwordx4 v65, s[36:39], s44 offen offset:1024 sc1 lds\n\t"
      "s_add_u32 m0, s55, s61\n\t"
      "s_nop 0\n\t"
      "buffer_load_dwordx4 v127, s[36:39], s46 offen sc1 lds\n\t"
      "buffer_load_dwordx4 v127, s[36:39], s46 offen offset:1024 sc1 lds\n\t"
      "s_branch LWIN1_1%=\n\t"
      "LLAST_1%=:\n\t"
      "s_waitcnt vmcnt(0) lgkmcnt(0)\n\t"
      "s_barrier\n\t"
      "ds_read_b128 v[16:19], v127 offset:45056\n\t"
      "ds_read_b128 v[28:31], v127 offset:48128\n\t"
      "ds_read_b128 v[24:27], v127 offset:47104\n\t"
      "v_smfmac_f32_16x16x64_bf16 v[40:43], v[20:23], a[168:175], v122\n\t"
      "v_smfmac_f32_16x16x64_bf16 v[44:47], v[20:23], a[232:239], v122\n\t"
      "s_waitcnt lgkmcnt(2)\n\t"
      "v_smfmac_f32_16x16x64_bf16 v[44:47], v[16:19], a[224:231], v122\n\t"
      "v_smfmac_f32_16x16x64_bf16 v[40:43], v[16:19], a[160:167], v122\n\t"
      "s_waitcnt lgkmcnt(1)\n\t"
      "v_smfmac_f32_16x16x64_bf16 v[44:47], v[28:31], a[248:255], v122\n\t"
      "v_smfmac_f32_16x16x64_bf16 v[40:43], v[28:31], a[184:191], v122\n\t"
      "s_waitcnt lgkmcnt(0)\n\t"
      "v_smfmac_f32_16x16x64_bf16 v[44:47], v[24:27], a[240:247], v122\n\t"
      "v_smfmac_f32_16x16x64_bf16 v[40:43], v[24:27], a[176:183], v122\n\t"
      "s_nop 7\n\t"
      "v_add_f32 v40, v40, v42\n\t"
      "v_add_f32 v41, v41, v43\n\t"
      "v_cvt_pk_bf16_f32 v120, v40, v41\n\t"
      "v_add_f32 v44, v44, v46\n\t"
      "v_add_f32 v45, v45, v47\n\t"
      "v_cvt_pk_bf16_f32 v121, v44, v45\n\t"
      "ds_write_b64 v125, v[120:121] offset:2080\n\t"
      "s_waitcnt lgkmcnt(0)\n\t"
      "s_barrier\n\t"
      "ds_read_b64_tr_b16 v[40:41], v124 offset:51232\n\t"
      "ds_read_b64_tr_b16 v[42:43], v124 offset:51752\n\t"
      "ds_read_b64_tr_b16 v[44:45], v124 offset:52272\n\t"
      "ds_read_b64_tr_b16 v[46:47], v124 offset:52792\n\t"
      "v_add_u32 v100, s55, v127\n\t"
      "ds_read_b128 v[60:63], v100 offset:57344\n\t"
      "s_waitcnt lgkmcnt(0)\n\t"
      "s_branch LROLE_1%=\n\t"
      "LUTOK_2%=:\n\t"
      "v_add_u32 v104, s71, v100\n\t"
      "ds_read_b128 v[76:79], v104\n\t"
      "ds_read_b128 v[80:83], v104 offset:1024\n\t"
      "ds_read_b128 v[112:115], v100\n\t"
      "ds_read_b128 v[116:119], v100 offset:1024\n\t"
      "v_xor_b32 v101, 2048, v104\n\t"
      "v_xor_b32 v102, 4096, v104\n\t"
      "v_xor_b32 v103, 6144, v104\n\t"
      "ds_read_b128 v[0:3], v101\n\t"
      "ds_read_b128 v[4:7], v101 offset:1024\n\t"
      "ds_read_b128 v[8:11], v100 offset:8192\n\t"
      "ds_read_b128 v[12:15], v100 offset:9216\n\t"
      "ds_read_b128 v[16:19], v102\n\t"
      "ds_read_b128 v[20:23], v102 offset:1024\n\t"
      "ds_read_b128 v[24:27], v100 offset:16384\n\t"
      "ds_read_b128 v[28:31], v100 offset:17408\n\t"
      "s_add_u32 m0, s55, s71\n\t"
      "s_sub_u32 s71, 204800, s71\n\t"
      "s_add_u32 s40, s40, 1\n\t"
      "s_cmp_lt_u32 s40, 63\n\t"
      "s_cselect_b32 s61, 786432, 0\n\t"
      "s_cselect_b32 s60, s58, 0\n\t"
      "s_add_u32 s44, s44, s61\n\t"
      "s_cmp_lt_u32 s40, 62\n\t"
      "s_cselect_b32 s61, 786432, 0\n\t"
      "s_add_u32 s46, s46, s61\n\t"
      "s_add_u32 s61, s40, 3\n\t"
      "s_and_b32 s62, s61, 3\n\t"
      "s_cselect_b32 s62, 16, 2000\n\t"
      "s_cmp_lt_u32 s61, 64\n\t"
      "s_cselect_b32 s62, s62, 0\n\t"
      "s_add_u32 s50, s50, s62\n\t"
      "s_mov_b32 s63, 0x2000\n\t"
      "s_waitcnt lgkmcnt(8)\n\t"
      "v_mfma_f32_16x16x32_bf16 v[32:35], v[76:79], v[112:115], 0\n\t"
      "v_mfma_f32_16x16x32_bf16 v[36:39], v[80:83], v[112:115], 0\n\t"
      "v_mfma_f32_16x16x32_bf16 v[40:43], v[76:79], v[116:119], 0\n\t"
      "v_mfma_f32_16x16x32_bf16 v[44:47], v[80:83], v[116:119], 0\n\t"
      "ds_read_b128 v[76:79], v103\n\t"
      "ds_read_b128 v[80:83], v103 offset:1024\n\t"
      "ds_read_b128 v[112:115], v100 offset:24576\n\t"
      "ds_read_b128 v[116:119], v100 offset:25600\n\t"
      "s_branch LGATE_2%=\n\t"
      "LFIN_2%=:\n\t"
      "s_waitcnt lgkmcnt(0)\n\t"
      "s_barrier\n\t"
      "s_branch LEND%=\n\t"
      "LWK1_2%=:\n\t"
      "buffer_store_dwordx4 v[44:47], v61, s[36:39], s48 offen sc1\n\t"
      "s_branch LW4_2%=\n\t"
      "LFIRST_2%=:\n\t"
      "s_cmp_eq_u32 s40, 0\n\t"
      "s_cbranch_scc1 LWM0_2%=\n\t"
      "LDEC_2%=:\n\t"
      "s_waitcnt vmcnt(0)\n\t"
      "v_readfirstlane_b32 s61, v106\n\t"
      "s_mov_b32 s62, 72960\n\t"
      "s_sub_u32 s63, s63, 1\n\t"
      "buffer_load_dword v106, off, s[36:39], s62 sc1\n\t"
      "s_cmp_eq_u32 s63, 0\n\t"
      "s_cbranch_scc1 LDEC1_2%=\n\t"
      "s_cmp_lt_u32 s61, 67108864\n\t"
      "s_cbranch_scc1 LDEC_2%=\n\t"
      "LDEC1_2%=:\n\t"
      "s_cmp_gt_u32 s61, 67141632\n\t"
      "s_cselect_b32 s99, 0x2000, 0\n\t"
      "s_xor_b32 s97, s97, s99\n\t"
      "s_barrier\n\t"
      "s_mov_b32 s63, 0x2000\n\t"
      "s_branch LPOLL_2%=\n\t"
      "LWM0_2%=:\n\t"
      "s_mov_b32 s63, 0x10000\n\t"
      "LWM_2%=:\n\t"
      "s_waitcnt vmcnt(0) lgkmcnt(0)\n\t"
      "v_bitop3_b32 v93, v93, v95, s53 bitop3:0x7e\n\t"
      "v_max_u32 v92, v92, v94\n\t"
      "v_cmp_ne_u32 vcc, 0, v93\n\t"
      "s_nop 1\n\t"
      "v_max_u32_dpp v92, v92, v92 quad_perm:[1,0,3,2] row_mask:0xf bank_mask:0xf\n\t"
      "s_nop 1\n\t"
      "v_max_u32_dpp v92, v92, v92 quad_perm:[2,3,0,1] row_mask:0xf bank_mask:0xf\n\t"
      "s_nop 1\n\t"
      "v_max_u32_dpp v92, v92, v92 row_ror:4 row_mask:0xf bank_mask:0xf\n\t"
      "s_nop 1\n\t"
      "v_max_u32_dpp v92, v92, v92 row_ror:8 row_mask:0xf bank_mask:0xf\n\t"
      "v_mov_b32 v93, v92\n\t"
      "s_nop 1\n\t"
      "v_permlane16_swap_b32 v92, v93\n\t"
      "v_max_u32 v92, v92, v93\n\t"
      "v_mov_b32 v93, v92\n\t"
      "s_nop 1\n\t"
      "v_permlane32_swap_b32 v92, v93\n\t"
      "v_max_u32 v92, v92, v93\n\t"
      "v_mul_f32 v93, 0x40a00000, v92\n\t"
      "s_nop 0\n\t"
      "v_readfirstlane_b32 s67, v93\n\t"
      "v_mul_f32 v93, 0x43030000, v93\n\t"
      "s_nop 0\n\t"
      "v_readfirstlane_b32 s66, v93\n\t"
      "s_cbranch_vccz LWM1_2%=\n\t"
      "s_sleep 4\n\t"
      "buffer_load_dwordx2 v[92:93], v91, s[36:39], s94 offen sc1\n\t"
      "buffer_load_dwordx2 v[94:95], v91, s[36:39], s94 offen offset:128 sc1\n\t"
      "s_sub_u32 s63, s63, 1\n\t"
      "s_cmp_lg_u32 s63, 0\n\t"
      "s_cbranch_scc1 LWM_2%=\n\t"
      "s_waitcnt vmcnt(0)\n\t"
      "LWM1_2%=:\n\t"
      "s_cmp_lg_u32 s86, 0\n\t"
      "v_mov_b32 v92, s65\n\t"
      "s_mov_b32 s62, 72960\n\t"
      "s_cselect_b64 exec, 1, 0\n\t"
      "buffer_atomic_add v92, off, s[36:39], s62\n\t"
      "s_mov_b64 exec, -1\n\t"
      "s_lshr_b32 s67, s67, 23\n\t"
      "s_add_u32 s67, s67, 5\n\t"
      "s_lshr_b32 s66, s66, 23\n\t"
      "s_add_u32 s66, s66, 129\n\t"
      "s_max_u32 s66, s66, 80\n\t"
      "buffer_load_dwordx2 v[120:121], off, s[36:39], s64 sc1\n\t"
      "s_mov_b32 s63, 0x2000\n\t"
      "s_branch LPOLL_2%=\n\t"
      "LR0_2%=:\n\t"
      "buffer_load_dwordx4 v[84:87], v60, s[36:39], s43 offen sc1\n\t"
      "buffer_load_dwordx4 v[88:91], v60, s[36:39], s43 offen offset:1024 sc1\n\t"
      "s_waitcnt vmcnt(2)\n\t"
      "buffer_load_dwordx4 v[92:95], v60, s[36:39], s68 offen sc1\n\t"
      "buffer_load_dwordx4 v[96:99], v60, s[36:39], s68 offen offset:1024 sc1\n\t"
      "s_sub_u32 s63, s63, 1\n\t"
      "s_waitcnt vmcnt(2) lgkmcnt(0)\n\t"
      "v_bitop3_b32 v64, v84, v85, v86 bitop3:0x80\n\t"
      "v_bitop3_b32 v64, v64, v87, v88 bitop3:0x80\n\t"
      "v_bitop3_b32 v64, v64, v89, v90 bitop3:0x80\n\t"
      "v_bitop3_b32 v64, v64, v91, s58 bitop3:0x80\n\t"
      "v_cmp_eq_u32 vcc, 0, v64\n\t"
      "v_pk_fma_f32 v[12:13], v[84:85], s[56:57], v[126:127] op_sel:[0,1,0] op_sel_hi:[1,1,0]\n\t"
      "v_pk_fma_f32 v[14:15], v[86:87], s[56:57], v[126:127] op_sel:[0,1,0] op_sel_hi:[1,1,0]\n\t"
      "v_cvt_pk_bf16_f32 v8, v12, v13\n\t"
      "v_cvt_pk_bf16_f32 v9, v14, v15\n\t"
      "ds_write_b64 v127, v[8:9] offset:43008\n\t"
      "v_pk_fma_f32 v[0:1], v[88:89], s[56:57], v[126:127] op_sel:[0,1,0] op_sel_hi:[1,1,0]\n\t"
      "v_pk_fma_f32 v[2:3], v[90:91], s[56:57], v[126:127] op_sel:[0,1,0] op_sel_hi:[1,1,0]\n\t"
      "v_cvt_pk_bf16_f32 v10, v0, v1\n\t"
      "v_cvt_pk_bf16_f32 v11, v2, v3\n\t"
      "ds_write_b64 v127, v[10:11] offset:43016\n\t"
      "s_cmp_le_i32 s63, 0\n\t"
      "s_waitcnt lgkmcnt(0)\n\t"
      "s_cbranch_scc1 LRZ_2%=\n\t"
      "s_cbranch_vccnz LR0_2%=\n\t"
      "LRZ_2%=:\n\t"
      "v_mov_b64 v[32:33], 0\n\t"
      "v_mov_b64 v[34:35], 0\n\t"
      "v_mov_b64 v[36:37], 0\n\t"
      "v_mov_b64 v[38:39], 0\n\t"
      "v_mov_b64 v[40:41], 0\n\t"
      "v_mov_b64 v[42:43], 0\n\t"
      "v_mov_b64 v[44:45], 0\n\t"
      "v_mov_b64 v[46:47], 0\n\t"
      "v_mov_b64 v[48:49], 0\n\t"
      "v_mov_b64 v[50:51], 0\n\t"
      "v_mov_b64 v[52:53], 0\n\t"
      "v_mov_b64 v[54:55], 0\n\t"
      "v_smfmac_f32_16x16x64_bf16 v[32:35], v[8:11], a[16:23], v122\n\t"
      "v_smfmac_f32_16x16x64_bf16 v[48:51], v[8:11], v[144:151], v122\n\t"
      "s_branch LC0_2%=\n\t"
      "LR1_2%=:\n\t"
      "v_bitop3_b32 v64, v92, v93, v94 bitop3:0x80\n\t"
      "v_bitop3_b32 v64, v64, v95, v96 bitop3:0x80\n\t"
      "v_bitop3_b32 v64, v64, v97, v98 bitop3:0x80\n\t"
      "v_bitop3_b32 v64, v64, v99, s58 bitop3:0x80\n\t"
      "v_cmp_eq_u32 vcc, 0, v64\n\t"
      "s_nop 0\n\t"
      "s_cbranch_vccz LRX_2%=\n\t"
      "LRY_2%=:\n\t"
      "buffer_load_dwordx4 v[92:95], v60, s[36:39], s68 offen sc1\n\t"
      "buffer_load_dwordx4 v[96:99], v60, s[36:39], s68 offen offset:1024 sc1\n\t"
      "s_sub_u32 s63, s63, 1\n\t"
      "s_waitcnt vmcnt(0) lgkmcnt(0)\n\t"
      "v_bitop3_b32 v64, v92, v93, v94 bitop3:0x80\n\t"
      "v_bitop3_b32 v64, v64, v95, v96 bitop3:0x80\n\t"
      "v_bitop3_b32 v64, v64, v97, v98 bitop3:0x80\n\t"
      "v_bitop3_b32 v64, v64, v99, s58 bitop3:0x80\n\t"
      "v_cmp_eq_u32 vcc, 0, v64\n\t"
      "v_pk_fma_f32 v[28:29], v[92:93], s[56:57], v[126:127] op_sel:[0,1,0] op_sel_hi:[1,1,0]\n\t"
      "v_pk_fma_f32 v[30:31], v[94:95], s[56:57], v[126:127] op_sel:[0,1,0] op_sel_hi:[1,1,0]\n\t"
      "v_cvt_pk_bf16_f32 v24, v28, v29\n\t"
      "v_cvt_pk_bf16_f32 v25, v30, v31\n\t"
      "ds_write_b64 v127, v[24:25] offset:47104\n\t"
      "v_pk_fma_f32 v[16:17], v[96:97], s[56:57], v[126:127] op_sel:[0,1,0] op_sel_hi:[1,1,0]\n\t"
      "v_pk_fma_f32 v[18:19], v[98:99], s[56:57], v[126:127] op_sel:[0,1,0] op_sel_hi:[1,1,0]\n\t"
      "v_cvt_pk_bf16_f32 v26, v16, v17\n\t"
      "v_cvt_pk_bf16_f32 v27, v18, v19\n\t"
      "ds_write_b64 v127, v[26:27] offset:47112\n\t"
      "s_cmp_le_i32 s63, 0\n\t"
      "s_waitcnt lgkmcnt(0)\n\t"
      "s_cbranch_scc1 LRX_2%=\n\t"
      "s_cbranch_vccnz LRY_2%=\n\t"
      "LRX_2%=:\n\t"
      "s_cmp_eq_u32 s40, 63\n\t"
      "s_cbranch_scc1 LLAST_2%=\n\t"
      "s_branch LC1_2%=\n\t"
      "LMX_2%=:\n\t"
      "s_waitcnt lgkmcnt(0)\n\t"
      "s_nop 7\n\t"
      "v_smfmac_f32_16x16x64_bf16 v[52:55], v[4:7], v[200:207], v122\n\t"
      "v_smfmac_f32_16x16x64_bf16 v[52:55], v[24:27], v[240:247], v122\n\t"
      "v_smfmac_f32_16x16x64_bf16 v[52:55], v[28:31], v[248:255], v122\n\t"
      "v_smfmac_f32_16x16x64_bf16 v[52:55], v[16:19], v[224:231], v122\n\t"
      "v_smfmac_f32_16x16x64_bf16 v[52:55], v[20:23], v[232:239], v122\n\t"
      "s_nop 7\n\t"
      "v_add_f32 v53, v53, v55\n\t"
      "v_add_f32_e64 v52, v52, v54\n\t"
      "v_cvt_pk_bf16_f32 v66, v52, v53\n\t"
      "s_add_u32 s62, s61, 0x83400000\n\t"
      "s_add_u32 s57, s61, 310378496\n\t"
      "v_mov_b32 v126, s62\n\t"
      "v_max3_f32 v82, v112, v113, v114\n\t"
      "v_max_f32 v82, v82, v115\n\t"
      "s_nop 1\n\t"
      "v_max_f32_dpp v82, v82, v82 quad_perm:[2,3,0,1] row_mask:0xf bank_mask:0xf\n\t"
      "v_max3_f32 v83, v82, v120, v121\n\t"
      "v_max3_f32 v60, v82, v120, v121\n\t"
      "s_nop 1\n\t"
      "v_permlane16_swap_b32 v83, v60\n\t"
      "v_max_f32 v61, v83, v60\n\t"
      "v_max_f32 v60, v83, v60\n\t"
      "s_nop 1\n\t"
      "v_permlane32_swap_b32 v61, v60\n\t"
      "v_max_f32 v83, v61, v60\n\t"
      "v_sub_f32 v112, v112, v83\n\t"
      "v_sub_f32 v113, v113, v83\n\t"
      "v_sub_f32 v114, v114, v83\n\t"
      "v_sub_f32 v115, v115, v83\n\t"
      "v_sub_f32 v120, v120, v83\n\t"
      "v_sub_f32 v121, v121, v83\n\t"
      "v_exp_f32_e64 v56, v112\n\t"
      "v_exp_f32 v57, v113\n\t"
      "v_exp_f32 v58, v114\n\t"
      "v_exp_f32 v59, v115\n\t"
      "v_exp_f32 v80, v120\n\t"
      "v_exp_f32_e64 v81, v121\n\t"
      "v_cvt_pk_bf16_f32 v96, v56, v57\n\t"
      "v_cvt_pk_bf16_f32 v97, v58, v59\n\t"
      "v_cvt_pk_bf16_f32 v108, v80, v81\n\t"
      "v_mov_b32_dpp v98, v96 quad_perm:[2,3,0,1] row_mask:0xf bank_mask:0xf\n\t"
      "v_mov_b32_dpp v99, v97 quad_perm:[2,3,0,1] row_mask:0xf bank_mask:0xf\n\t"
      "v_mfma_f32_16x16x32_bf16 v[48:51], v[108:111], v[88:91], 0\n\t"
      "s_nop 0\n\t"
      "v_mfma_f32_16x16x32_bf16 v[48:51], v[96:99], v[88:91], v[48:51]\n\t"
      "v_mfma_f32_16x16x32_bf16 v[84:87], v[108:111], v[64:67], 0\n\t"
      "v_mfma_f32_16x16x32_bf16 v[84:87], v[96:99], v[68:71], v[84:87]\n\t"
      "v_mfma_f32_16x16x32_bf16 v[92:95], v[96:99], v[72:75], 0\n\t"
      "v_mfma_f32_16x16x32_bf16 v[92:95], v[108:111], v[66:69], v[92:95]\n\t"
      "s_nop 3\n\t"
      "v_rcp_f32 v48, v48\n\t"
      "v_rcp_f32 v49, v49\n\t"
      "v_fma_f32 v84, v84, v48, s96\n\t"
      "buffer_atomic_add v84, v123, s[36:39], s97 offen\n\t"
      "v_fma_f32 v85, v85, v49, s96\n\t"
      "buffer_atomic_add v85, v123, s[36:39], s97 offen offset:512\n\t"
      "v_fma_f32 v94, v94, v48, s96\n\t"
      "buffer_atomic_add v94, v123, s[36:39], s69 offen\n\t"
      "v_fma_f32 v95, v95, v49, s96\n\t"
      "buffer_atomic_add v95, v123, s[36:39], s69 offen offset:512\n\t"
      "s_add_u32 s97, s97, 0x8000\n\t"
      "s_branch LWIN_2%=\n\t"
      "LEARLY_2%=:\n\t"
      "s_cmp_lt_i32 s40, 0\n\t"
      "s_waitcnt lgkmcnt(0)\n\t"
      "s_cbranch_scc1 LUTOK_2%=\n\t"
      "s_xor_b32 s43, s43, s99\n\t"
      "s_xor_b32 s68, s68, s99\n\t"
      "s_mov_b32 s61, 72960\n\t"
      "buffer_load_dword v106, off, s[36:39], s61 sc1\n\t"
      "s_branch LROLE_2%=\n\t"
      "LSLOW_2%=:\n\t"
      "s_mov_b32 s63, 0x10000\n\t"
      "LSLOW1_2%=:\n\t"
      "buffer_load_dwordx2 v[92:93], off, s[36:39], s64 sc1\n\t"
      "s_waitcnt vmcnt(0)\n\t"
      "v_bitop3_b32 v92, v92, v93, s53 bitop3:0x7e\n\t"
      "s_nop 0\n\t"
      "v_readfirstlane_b32 s56, v92\n\t"
      "s_nop 1\n\t"
      "s_cmp_eq_u32 s56, 0\n\t"
      "s_cbranch_scc1 LSLOW2_2%=\n\t"
      "s_sleep 8\n\t"
      "s_sub_u32 s63, s63, 1\n\t"
      "s_cmp_lg_u32 s63, 0\n\t"
      "s_cbranch_scc1 LSLOW1_2%=\n\t"
      "LSLOW2_2%=:\n\t"
      "s_mov_b32 m0, s45\n\t"
      "s_sub_u32 s61, 204800, s71\n\t"
      "buffer_load_dwordx4 v65, s[36:39], s44 offen sc1 lds\n\t"
      "buffer_load_dwordx4 v65, s[36:39], s44 offen offset:1024 sc1 lds\n\t"
      "s_add_u32 m0, s55, s61\n\t"
      "s_nop 0\n\t"
      "buffer_load_dwordx4 v127, s[36:39], s46 offen sc1 lds\n\t"
      "buffer_load_dwordx4 v127, s[36:39], s46 offen offset:1024 sc1 lds\n\t"
      "s_branch LWIN1_2%=\n\t"
      "LLAST_2%=:\n\t"
      "s_waitcnt vmcnt(0) lgkmcnt(0)\n\t"
      "s_barrier\n\t"
      "ds_read_b128 v[28:31], v127 offset:48128\n\t"
      "ds_read_b128 v[16:19], v127 offset:45056\n\t"
      "ds_read_b128 v[20:23], v127 offset:46080\n\t"
      "v_smfmac_f32_16x16x64_bf16 v[40:43], v[24:27], a[176:183], v122\n\t"
      "v_smfmac_f32_16x16x64_bf16 v[44:47], v[24:27], a[240:247], v122\n\t"
      "s_waitcnt lgkmcnt(2)\n\t"
      "v_smfmac_f32_16x16x64_bf16 v[44:47], v[28:31], a[248:255], v122\n\t"
      "v_smfmac_f32_16x16x64_bf16 v[40:43], v[28:31], a[184:191], v122\n\t"
      "s_waitcnt lgkmcnt(1)\n\t"
      "v_smfmac_f32_16x16x64_bf16 v[44:47], v[16:19], a[224:231], v122\n\t"
      "v_smfmac_f32_16x16x64_bf16 v[40:43], v[16:19], a[160:167], v122\n\t"
      "s_waitcnt lgkmcnt(0)\n\t"
      "v_smfmac_f32_16x16x64_bf16 v[44:47], v[20:23], a[232:239], v122\n\t"
      "v_smfmac_f32_16x16x64_bf16 v[40:43], v[20:23], a[168:175], v122\n\t"
      "s_nop 7\n\t"
      "v_add_f32 v40, v40, v42\n\t"
      "v_add_f32 v41, v41, v43\n\t"
      "v_cvt_pk_bf16_f32 v120, v40, v41\n\t"
      "v_add_f32 v44, v44, v46\n\t"
      "v_add_f32 v45, v45, v47\n\t"
      "v_cvt_pk_bf16_f32 v121, v44, v45\n\t"
      "ds_write_b64 v125, v[120:121] offset:2080\n\t"
      "s_waitcnt lgkmcnt(0)\n\t"
      "s_barrier\n\t"
      "ds_read_b64_tr_b16 v[40:41], v124 offset:51232\n\t"
      "ds_read_b64_tr_b16 v[42:43], v124 offset:51752\n\t"
      "ds_read_b64_tr_b16 v[44:45], v124 offset:52272\n\t"
      "ds_read_b64_tr_b16 v[46:47], v124 offset:52792\n\t"
      "v_add_u32 v100, s55, v127\n\t"
      "ds_read_b128 v[60:63], v100 offset:57344\n\t"
      "s_waitcnt lgkmcnt(0)\n\t"
      "s_branch LROLE_2%=\n\t"
      "LUTOK_3%=:\n\t"
      "v_add_u32 v104, s71, v100\n\t"
      "ds_read_b128 v[76:79], v104\n\t"
      "ds_read_b128 v[80:83], v104 offset:1024\n\t"
      "ds_read_b128 v[112:115], v100\n\t"
      "ds_read_b128 v[116:119], v100 offset:1024\n\t"
      "v_xor_b32 v101, 2048, v104\n\t"
      "v_xor_b32 v102, 4096, v104\n\t"
      "v_xor_b32 v103, 6144, v104\n\t"
      "ds_read_b128 v[0:3], v101\n\t"
      "ds_read_b128 v[4:7], v101 offset:1024\n\t"
      "ds_read_b128 v[8:11], v100 offset:8192\n\t"
      "ds_read_b128 v[12:15], v100 offset:9216\n\t"
      "ds_read_b128 v[16:19], v102\n\t"
      "ds_read_b128 v[20:23], v102 offset:1024\n\t"
      "ds_read_b128 v[24:27], v100 offset:16384\n\t"
      "ds_read_b128 v[28:31], v100 offset:17408\n\t"
      "s_sub_u32 s61, s88, s78\n\t"
      "s_sub_i32 s61, s61, 564\n\t"
      "s_min_i32 s61, s61, 64\n\t"
      "s_max_i32 s61, s61, -96\n\t"
      "s_ashr_i32 s61, s61, 2\n\t"
      "s_cmp_eq_u32 s63, 0x2000\n\t"
      "s_cselect_b32 s61, s61, 24\n\t"
      "s_add_u32 s26, s26, s61\n\t"
      "s_max_u32 s26, s26, 2100\n\t"
      "s_min_u32 s26, s26, 2900\n\t"
      "s_add_u32 m0, s55, s71\n\t"
      "s_sub_u32 s71, 204800, s71\n\t"
      "s_add_u32 s40, s40, 1\n\t"
      "s_cmp_lt_u32 s40, 63\n\t"
      "s_cselect_b32 s61, 786432, 0\n\t"
      "s_cselect_b32 s60, s58, 0\n\t"
      "s_add_u32 s44, s44, s61\n\t"
      "s_cmp_lt_u32 s40, 62\n\t"
      "s_cselect_b32 s61, 786432, 0\n\t"
      "s_add_u32 s46, s46, s61\n\t"
      "s_add_u32 s61, s40, 3\n\t"
      "s_and_b32 s62, s61, 3\n\t"
      "s_cselect_b32 s62, 16, 2000\n\t"
      "s_cmp_lt_u32 s61, 64\n\t"
      "s_cselect_b32 s62, s62, 0\n\t"
      "s_add_u32 s50, s50, s62\n\t"
      "s_mov_b32 s63, 0x2000\n\t"
      "s_waitcnt lgkmcnt(8)\n\t"
      "v_mfma_f32_16x16x32_bf16 v[32:35], v[76:79], v[112:115], 0\n\t"
      "v_mfma_f32_16x16x32_bf16 v[36:39], v[80:83], v[112:115], 0\n\t"
      "v_mfma_f32_16x16x32_bf16 v[40:43], v[76:79], v[116:119], 0\n\t"
      "v_mfma_f32_16x16x32_bf16 v[44:47], v[80:83], v[116:119], 0\n\t"
      "ds_read_b128 v[76:79], v103\n\t"
      "ds_read_b128 v[80:83], v103 offset:1024\n\t"
      "ds_read_b128 v[112:115], v100 offset:24576\n\t"
      "ds_read_b128 v[116:119], v100 offset:25600\n\t"
      "s_branch LGATE_3%=\n\t"
      "LFIN_3%=:\n\t"
      "s_waitcnt lgkmcnt(0)\n\t"
      "s_barrier\n\t"
      "s_branch LEND%=\n\t"
      "LFIRST_3%=:\n\t"
      "s_cmp_eq_u32 s40, 0\n\t"
      "s_cbranch_scc1 LWM0_3%=\n\t"
      "LDEC_3%=:\n\t"
      "s_waitcnt vmcnt(0)\n\t"
      "v_readfirstlane_b32 s61, v106\n\t"
      "s_mov_b32 s62, 72960\n\t"
      "s_sub_u32 s63, s63, 1\n\t"
      "buffer_load_dword v106, off, s[36:39], s62 sc1\n\t"
      "s_cmp_eq_u32 s63, 0\n\t"
      "s_cbranch_scc1 LDEC1_3%=\n\t"
      "s_cmp_lt_u32 s61, 67108864\n\t"
      "s_cbranch_scc1 LDEC_3%=\n\t"
      "LDEC1_3%=:\n\t"
      "s_cmp_gt_u32 s61, 67141632\n\t"
      "s_cselect_b32 s99, 0x2000, 0\n\t"
      "s_xor_b32 s97, s97, s99\n\t"
      "s_mov_b32 s62, 64\n\t"
      "LST0_3%=:\n\t"
      "v_bitop3_b32 v57, v57, v59, s53 bitop3:0x7e\n\t"
      "v_cmp_ne_u32 vcc, 0, v57\n\t"
      "v_subrev_u32 v97, s87, v56\n\t"
      "v_subrev_u32 v98, s87, v58\n\t"
      "v_max_i32 v97, v97, v98\n\t"
      "s_cbranch_vccz LST3_3%=\n\t"
      "buffer_load_dwordx2 v[56:57], v96, s[36:39], s86 offen sc1\n\t"
      "buffer_load_dwordx2 v[58:59], v96, s[36:39], s86 offen offset:128 sc1\n\t"
      "s_sub_u32 s62, s62, 1\n\t"
      "s_cmp_eq_u32 s62, 0\n\t"
      "s_waitcnt vmcnt(0)\n\t"
      "s_cbranch_scc0 LST0_3%=\n\t"
      "s_branch LST2_3%=\n\t"
      "LST3_3%=:\n\t"
      "s_nop 1\n\t"
      "v_max_i32_dpp v97, v97, v97 quad_perm:[1,0,3,2] row_mask:0xf bank_mask:0xf\n\t"
      "s_nop 1\n\t"
      "v_max_i32_dpp v97, v97, v97 quad_perm:[2,3,0,1] row_mask:0xf bank_mask:0xf\n\t"
      "s_nop 1\n\t"
      "v_max_i32_dpp v97, v97, v97 row_ror:4 row_mask:0xf bank_mask:0xf\n\t"
      "s_nop 1\n\t"
      "v_max_i32_dpp v97, v97, v97 row_ror:8 row_mask:0xf bank_mask:0xf\n\t"
      "v_mov_b32 v98, v97\n\t"
      "s_nop 1\n\t"
      "v_permlane16_swap_b32 v97, v98\n\t"
      "v_max_i32 v97, v97, v98\n\t"
      "v_mov_b32 v98, v97\n\t"
      "s_nop 1\n\t"
      "v_permlane32_swap_b32 v97, v98\n\t"
      "v_max_i32 v97, v97, v98\n\t"
      "s_nop 0\n\t"
      "v_readfirstlane_b32 s61, v97\n\t"
      "s_nop 1\n\t"
      "s_min_u32 s61, s61, 500\n\t"
      "s_add_u32 s61, s61, 170\n\t"
      "LST1_3%=:\n\t"
      "s_memrealtime s[92:93]\n\t"
      "s_waitcnt lgkmcnt(0)\n\t"
      "s_sub_u32 s62, s92, s87\n\t"
      "s_cmp_lt_u32 s62, s61\n\t"
      "s_cbranch_scc1 LST1_3%=\n\t"
      "LST2_3%=:\n\t"
      "s_barrier\n\t"
      "s_mov_b32 s63, 0x2000\n\t"
      "s_branch LPOLL_3%=\n\t"
      "LWM0_3%=:\n\t"
      "s_mov_b32 s63, 0x10000\n\t"
      "LWM_3%=:\n\t"
      "s_waitcnt vmcnt(0) lgkmcnt(0)\n\t"
      "v_bitop3_b32 v93, v93, v95, s53 bitop3:0x7e\n\t"
      "v_max_u32 v92, v92, v94\n\t"
      "v_cmp_ne_u32 vcc, 0, v93\n\t"
      "s_nop 1\n\t"
      "v_max_u32_dpp v92, v92, v92 quad_perm:[1,0,3,2] row_mask:0xf bank_mask:0xf\n\t"
      "s_nop 1\n\t"
      "v_max_u32_dpp v92, v92, v92 quad_perm:[2,3,0,1] row_mask:0xf bank_mask:0xf\n\t"
      "s_nop 1\n\t"
      "v_max_u32_dpp v92, v92, v92 row_ror:4 row_mask:0xf bank_mask:0xf\n\t"
      "s_nop 1\n\t"
      "v_max_u32_dpp v92, v92, v92 row_ror:8 row_mask:0xf bank_mask:0xf\n\t"
      "v_mov_b32 v93, v92\n\t"
      "s_nop 1\n\t"
      "v_permlane16_swap_b32 v92, v93\n\t"
      "v_max_u32 v92, v92, v93\n\t"
      "v_mov_b32 v93, v92\n\t"
      "s_nop 1\n\t"
      "v_permlane32_swap_b32 v92, v93\n\t"
      "v_max_u32 v92, v92, v93\n\t"
      "v_mul_f32 v93, 0x40a00000, v92\n\t"
      "s_nop 0\n\t"
      "v_readfirstlane_b32 s67, v93\n\t"
      "v_mul_f32 v93, 0x43030000, v93\n\t"
      "s_nop 0\n\t"
      "v_readfirstlane_b32 s66, v93\n\t"
      "s_cbranch_vccz LWM1_3%=\n\t"
      "s_sleep 4\n\t"
      "buffer_load_dwordx2 v[92:93], v91, s[36:39], s94 offen sc1\n\t"
      "buffer_load_dwordx2 v[94:95], v91, s[36:39], s94 offen offset:128 sc1\n\t"
      "s_sub_u32 s63, s63, 1\n\t"
      "s_cmp_lg_u32 s63, 0\n\t"
      "s_cbranch_scc1 LWM_3%=\n\t"
      "s_waitcnt vmcnt(0)\n\t"
      "LWM1_3%=:\n\t"
      "s_cmp_lg_u32 s86, 0\n\t"
      "v_mov_b32 v92, s65\n\t"
      "s_mov_b32 s62, 72960\n\t"
      "s_cselect_b64 exec, 1, 0\n\t"
      "buffer_atomic_add v92, off, s[36:39], s62\n\t"
      "s_mov_b64 exec, -1\n\t"
      "s_lshr_b32 s67, s67, 23\n\t"
      "s_add_u32 s67, s67, 5\n\t"
      "s_lshr_b32 s66, s66, 23\n\t"
      "s_add_u32 s66, s66, 129\n\t"
      "s_max_u32 s66, s66, 80\n\t"
      "s_memrealtime s[92:93]\n\t"
      "s_mov_b64 exec, 1\n\t"
      "v_mov_b32 v89, s53\n\t"
      "s_add_u32 s86, s94, 208640\n\t"
      "s_add_u32 s61, s95, 208640\n\t"
      "s_waitcnt lgkmcnt(0)\n\t"
      "v_mov_b32 v88, s92\n\t"
      "s_mov_b32 s87, s92\n\t"
      "buffer_store_dwordx2 v[88:89], off, s[36:39], s61 sc1\n\t"
      "s_mov_b64 exec, -1\n\t"
      "buffer_load_dwordx2 v[120:121], off, s[36:39], s64 sc1\n\t"
      "s_mov_b32 s63, 0x2000\n\t"
      "s_branch LPOLL_3%=\n\t"
      "LR0_3%=:\n\t"
      "buffer_load_dwordx4 v[84:87], v60, s[36:39], s43 offen sc1\n\t"
      "buffer_load_dwordx4 v[88:91], v60, s[36:39], s43 offen offset:1024 sc1\n\t"
      "s_waitcnt vmcnt(2)\n\t"
      "buffer_load_dwordx4 v[92:95], v60, s[36:39], s68 offen sc1\n\t"
      "buffer_load_dwordx4 v[96:99], v60, s[36:39], s68 offen offset:1024 sc1\n\t"
      "s_sub_u32 s63, s63, 1\n\t"
      "s_waitcnt vmcnt(2) lgkmcnt(0)\n\t"
      "v_bitop3_b32 v64, v84, v85, v86 bitop3:0x80\n\t"
      "v_bitop3_b32 v64, v64, v87, v88 bitop3:0x80\n\t"
      "v_bitop3_b32 v64, v64, v89, v90 bitop3:0x80\n\t"
      "v_bitop3_b32 v64, v64, v91, s58 bitop3:0x80\n\t"
      "v_cmp_eq_u32 vcc, 0, v64\n\t"
      "v_pk_fma_f32 v[8:9], v[84:85], s[56:57], v[126:127] op_sel:[0,1,0] op_sel_hi:[1,1,0]\n\t"
      "v_pk_fma_f32 v[10:11], v[86:87], s[56:57], v[126:127] op_sel:[0,1,0] op_sel_hi:[1,1,0]\n\t"
      "v_cvt_pk_bf16_f32 v12, v8, v9\n\t"
      "v_cvt_pk_bf16_f32 v13, v10, v11\n\t"
      "ds_write_b64 v127, v[12:13] offset:44032\n\t"
      "v_pk_fma_f32 v[4:5], v[88:89], s[56:57], v[126:127] op_sel:[0,1,0] op_sel_hi:[1,1,0]\n\t"
      "v_pk_fma_f32 v[6:7], v[90:91], s[56:57], v[126:127] op_sel:[0,1,0] op_sel_hi:[1,1,0]\n\t"
      "v_cvt_pk_bf16_f32 v14, v4, v5\n\t"
      "v_cvt_pk_bf16_f32 v15, v6, v7\n\t"
      "ds_write_b64 v127, v[14:15] offset:44040\n\t"
      "s_memtime s[88:89]\n\t"
      "s_cmp_le_i32 s63, 0\n\t"
      "s_waitcnt lgkmcnt(0)\n\t"
      "s_cbranch_scc1 LRZ_3%=\n\t"
      "s_cbranch_vccnz LR0_3%=\n\t"
      "LRZ_3%=:\n\t"
      "s_sub_u32 s88, s88, 0\n\t"
      "v_mov_b64 v[32:33], 0\n\t"
      "v_mov_b64 v[34:35], 0\n\t"
      "v_mov_b64 v[36:37], 0\n\t"
      "v_mov_b64 v[38:39], 0\n\t"
      "v_mov_b64 v[40:41], 0\n\t"
      "v_mov_b64 v[42:43], 0\n\t"
      "v_mov_b64 v[44:45], 0\n\t"
      "v_mov_b64 v[46:47], 0\n\t"
      "v_mov_b64 v[48:49], 0\n\t"
      "v_mov_b64 v[50:51], 0\n\t"
      "v_mov_b64 v[52:53], 0\n\t"
      "v_mov_b64 v[54:55], 0\n\t"
      "v_smfmac_f32_16x16x64_bf16 v[32:35], v[12:15], a[24:31], v122\n\t"
      "v_smfmac_f32_16x16x64_bf16 v[48:51], v[12:15], v[152:159], v122\n\t"
      "s_branch LC0_3%=\n\t"
      "LR1_3%=:\n\t"
      "v_bitop3_b32 v64, v92, v93, v94 bitop3:0x80\n\t"
      "v_bitop3_b32 v64, v64, v95, v96 bitop3:0x80\n\t"
      "v_bitop3_b32 v64, v64, v97, v98 bitop3:0x80\n\t"
      "v_bitop3_b32 v64, v64, v99, s58 bitop3:0x80\n\t"
      "v_cmp_eq_u32 vcc, 0, v64\n\t"
      "s_nop 0\n\t"
      "s_cbranch_vccz LRX_3%=\n\t"
      "LRY_3%=:\n\t"
      "buffer_load_dwordx4 v[92:95], v60, s[36:39], s68 offen sc1\n\t"
      "buffer_load_dwordx4 v[96:99], v60, s[36:39], s68 offen offset:1024 sc1\n\t"
      "s_sub_u32 s63, s63, 1\n\t"
      "s_waitcnt vmcnt(0) lgkmcnt(0)\n\t"
      "v_bitop3_b32 v64, v92, v93, v94 bitop3:0x80\n\t"
      "v_bitop3_b32 v64, v64, v95, v96 bitop3:0x80\n\t"
      "v_bitop3_b32 v64, v64, v97, v98 bitop3:0x80\n\t"
      "v_bitop3_b32 v64, v64, v99, s58 bitop3:0x80\n\t"
      "v_cmp_eq_u32 vcc, 0, v64\n\t"
      "v_pk_fma_f32 v[24:25], v[92:93], s[56:57], v[126:127] op_sel:[0,1,0] op_sel_hi:[1,1,0]\n\t"
      "v_pk_fma_f32 v[26:27], v[94:95], s[56:57], v[126:127] op_sel:[0,1,0] op_sel_hi:[1,1,0]\n\t"
      "v_cvt_pk_bf16_f32 v28, v24, v25\n\t"
      "v_cvt_pk_bf16_f32 v29, v26, v27\n\t"
      "ds_write_b64 v127, v[28:29] offset:48128\n\t"
      "v_pk_fma_f32 v[20:21], v[96:97], s[56:57], v[126:127] op_sel:[0,1,0] op_sel_hi:[1,1,0]\n\t"
      "v_pk_fma_f32 v[22:23], v[98:99], s[56:57], v[126:127] op_sel:[0,1,0] op_sel_hi:[1,1,0]\n\t"
      "v_cvt_pk_bf16_f32 v30, v20, v21\n\t"
      "v_cvt_pk_bf16_f32 v31, v22, v23\n\t"
      "ds_write_b64 v127, v[30:31] offset:48136\n\t"
      "s_memtime s[88:89]\n\t"
      "s_cmp_le_i32 s63, 0\n\t"
      "s_waitcnt lgkmcnt(0)\n\t"
      "s_cbranch_scc1 LRX_3%=\n\t"
      "s_cbranch_vccnz LRY_3%=\n\t"
      "s_sub_u32 s88, s88, 336\n\t"
      "LRX_3%=:\n\t"
      "s_cmp_eq_u32 s40, 63\n\t"
      "s_cbranch_scc1 LLAST_3%=\n\t"
      "s_branch LC1_3%=\n\t"
      "LMX_3%=:\n\t"
      "s_waitcnt lgkmcnt(0)\n\t"
      "s_nop 7\n\t"
      "v_smfmac_f32_16x16x64_bf16 v[52:55], v[0:3], v[192:199], v122\n\t"
      "v_smfmac_f32_16x16x64_bf16 v[52:55], v[28:31], v[248:255], v122\n\t"
      "v_smfmac_f32_16x16x64_bf16 v[52:55], v[24:27], v[240:247], v122\n\t"
      "v_smfmac_f32_16x16x64_bf16 v[52:55], v[20:23], v[232:239], v122\n\t"
      "v_smfmac_f32_16x16x64_bf16 v[52:55], v[16:19], v[224:231], v122\n\t"
      "s_nop 7\n\t"
      "v_add_f32 v53, v53, v55\n\t"
      "v_add_f32_e64 v52, v52, v54\n\t"
      "v_cvt_pk_bf16_f32 v66, v52, v53\n\t"
      "s_add_u32 s62, s61, 0x83400000\n\t"
      "s_add_u32 s57, s61, 310378496\n\t"
      "v_mov_b32 v126, s62\n\t"
      "v_max3_f32 v82, v112, v113, v114\n\t"
      "v_max_f32 v82, v82, v115\n\t"
      "s_nop 1\n\t"
      "v_max_f32_dpp v82, v82, v82 quad_perm:[2,3,0,1] row_mask:0xf bank_mask:0xf\n\t"
      "v_max3_f32 v83, v82, v120, v121\n\t"
      "v_max3_f32 v60, v82, v120, v121\n\t"
      "s_nop 1\n\t"
      "v_permlane16_swap_b32 v83, v60\n\t"
      "v_max_f32 v61, v83, v60\n\t"
      "v_max_f32 v60, v83, v60\n\t"
      "s_nop 1\n\t"
      "v_permlane32_swap_b32 v61, v60\n\t"
      "v_max_f32 v83, v61, v60\n\t"
      "v_sub_f32 v112, v112, v83\n\t"
      "v_sub_f32 v113, v113, v83\n\t"
      "v_sub_f32 v114, v114, v83\n\t"
      "v_sub_f32 v115, v115, v83\n\t"
      "v_sub_f32 v120, v120, v83\n\t"
      "v_sub_f32 v121, v121, v83\n\t"
      "v_exp_f32_e64 v56, v112\n\t"
      "v_exp_f32 v57, v113\n\t"
      "v_exp_f32 v58, v114\n\t"
      "v_exp_f32 v59, v115\n\t"
      "v_exp_f32 v80, v120\n\t"
      "v_exp_f32_e64 v81, v121\n\t"
      "v_cvt_pk_bf16_f32 v96, v56, v57\n\t"
      "v_cvt_pk_bf16_f32 v97, v58, v59\n\t"
      "v_cvt_pk_bf16_f32 v108, v80, v81\n\t"
      "v_mov_b32_dpp v98, v96 quad_perm:[2,3,0,1] row_mask:0xf bank_mask:0xf\n\t"
      "v_mov_b32_dpp v99, v97 quad_perm:[2,3,0,1] row_mask:0xf bank_mask:0xf\n\t"
      "v_mfma_f32_16x16x32_bf16 v[48:51], v[108:111], v[88:91], 0\n\t"
      "s_nop 0\n\t"
      "v_mfma_f32_16x16x32_bf16 v[48:51], v[96:99], v[88:91], v[48:51]\n\t"
      "v_mfma_f32_16x16x32_bf16 v[84:87], v[108:111], v[64:67], 0\n\t"
      "v_mfma_f32_16x16x32_bf16 v[84:87], v[96:99], v[68:71], v[84:87]\n\t"
      "v_mfma_f32_16x16x32_bf16 v[92:95], v[96:99], v[72:75], 0\n\t"
      "v_mfma_f32_16x16x32_bf16 v[92:95], v[108:111], v[66:69], v[92:95]\n\t"
      "s_nop 3\n\t"
      "v_rcp_f32 v48, v48\n\t"
      "v_rcp_f32 v49, v49\n\t"
      "v_fma_f32 v84, v84, v48, s96\n\t"
      "buffer_atomic_add v84, v123, s[36:39], s97 offen\n\t"
      "v_fma_f32 v85, v85, v49, s96\n\t"
      "buffer_atomic_add v85, v123, s[36:39], s97 offen offset:512\n\t"
      "v_fma_f32 v94, v94, v48, s96\n\t"
      "buffer_atomic_add v94, v123, s[36:39], s69 offen\n\t"
      "v_fma_f32 v95, v95, v49, s96\n\t"
      "buffer_atomic_add v95, v123, s[36:39], s69 offen offset:512\n\t"
      "s_add_u32 s97, s97, 0x8000\n\t"
      "s_branch LWIN_3%=\n\t"
      "LEARLY_3%=:\n\t"
      "s_cmp_lt_i32 s40, 0\n\t"
      "s_waitcnt lgkmcnt(0)\n\t"
      "s_cbranch_scc1 LUTOK_3%=\n\t"
      "s_xor_b32 s43, s43, s99\n\t"
      "s_xor_b32 s68, s68, s99\n\t"
      "s_mov_b32 s61, 72960\n\t"
      "buffer_load_dword v106, off, s[36:39], s61 sc1\n\t"
      "s_cmp_lg_u32 s40, 0\n\t"
      "s_cbranch_scc1 LROLE_3%=\n\t"
      "v_lshlrev_b32 v96, 4, v127\n\t"
      "buffer_load_dwordx2 v[56:57], v96, s[36:39], s86 offen sc1\n\t"
      "buffer_load_dwordx2 v[58:59], v96, s[36:39], s86 offen offset:128 sc1\n\t"
      "s_branch LROLE_3%=\n\t"
      "LSLOW_3%=:\n\t"
      "s_mov_b32 s63, 0x10000\n\t"
      "LSLOW1_3%=:\n\t"
      "buffer_load_dwordx2 v[92:93], off, s[36:39], s64 sc1\n\t"
      "s_waitcnt vmcnt(0)\n\t"
      "v_bitop3_b32 v92, v92, v93, s53 bitop3:0x7e\n\t"
      "s_nop 0\n\t"
      "v_readfirstlane_b32 s56, v92\n\t"
      "s_nop 1\n\t"
      "s_cmp_eq_u32 s56, 0\n\t"
      "s_cbranch_scc1 LSLOW2_3%=\n\t"
      "s_sleep 8\n\t"
      "s_sub_u32 s63, s63, 1\n\t"
      "s_cmp_lg_u32 s63, 0\n\t"
      "s_cbranch_scc1 LSLOW1_3%=\n\t"
      "LSLOW2_3%=:\n\t"
      "s_mov_b32 m0, s45\n\t"
      "s_sub_u32 s61, 204800, s71\n\t"
      "buffer_load_dwordx4 v65, s[36:39], s44 offen sc1 lds\n\t"
      "buffer_load_dwordx4 v65, s[36:39], s44 offen offset:1024 sc1 lds\n\t"
      "s_add_u32 m0, s55, s61\n\t"
      "s_nop 0\n\t"
      "buffer_load_dwordx4 v127, s[36:39], s46 offen sc1 lds\n\t"
      "buffer_load_dwordx4 v127, s[36:39], s46 offen offset:1024 sc1 lds\n\t"
      "s_branch LWIN1_3%=\n\t"
      "LLAST_3%=:\n\t"
      "s_waitcnt vmcnt(0) lgkmcnt(0)\n\t"
      "s_barrier\n\t"
      "ds_read_b128 v[24:27], v127 offset:47104\n\t"
      "ds_read_b128 v[20:23], v127 offset:46080\n\t"
      "ds_read_b128 v[16:19], v127 offset:45056\n\t"
      "v_smfmac_f32_16x16x64_bf16 v[40:43], v[28:31], a[184:191], v122\n\t"
      "v_smfmac_f32_16x16x64_bf16 v[44:47], v[28:31], a[248:255], v122\n\t"
      "s_waitcnt lgkmcnt(2)\n\t"
      "v_smfmac_f32_16x16x64_bf16 v[44:47], v[24:27], a[240:247], v122\n\t"
      "v_smfmac_f32_16x16x64_bf16 v[40:43], v[24:27], a[176:183], v122\n\t"
      "s_waitcnt lgkmcnt(1)\n\t"
      "v_smfmac_f32_16x16x64_bf16 v[44:47], v[20:23], a[232:239], v122\n\t"
      "v_smfmac_f32_16x16x64_bf16 v[40:43], v[20:23], a[168:175], v122\n\t"
      "s_waitcnt lgkmcnt(0)\n\t"
      "v_smfmac_f32_16x16x64_bf16 v[44:47], v[16:19], a[224:231], v122\n\t"
      "v_smfmac_f32_16x16x64_bf16 v[40:43], v[16:19], a[160:167], v122\n\t"
      "s_nop 7\n\t"
      "v_add_f32 v40, v40, v42\n\t"
      "v_add_f32 v41, v41, v43\n\t"
      "v_cvt_pk_bf16_f32 v120, v40, v41\n\t"
      "v_add_f32 v44, v44, v46\n\t"
      "v_add_f32 v45, v45, v47\n\t"
      "v_cvt_pk_bf16_f32 v121, v44, v45\n\t"
      "ds_write_b64 v125, v[120:121] offset:2080\n\t"
      "s_waitcnt lgkmcnt(0)\n\t"
      "s_barrier\n\t"
      "ds_read_b64_tr_b16 v[40:41], v124 offset:51232\n\t"
      "ds_read_b64_tr_b16 v[42:43], v124 offset:51752\n\t"
      "ds_read_b64_tr_b16 v[44:45], v124 offset:52272\n\t"
      "ds_read_b64_tr_b16 v[46:47], v124 offset:52792\n\t"
      "v_add_u32 v100, s55, v127\n\t"
      "ds_read_b128 v[60:63], v100 offset:57344\n\t"
      "s_waitcnt lgkmcnt(0)\n\t"
      "s_branch LROLE_3%=\n\t"
      "LEND%=:\n\t"
      "s_waitcnt vmcnt(2) lgkmcnt(0)\n\t"
      "s_cmp_lg_u32 s42, 9\n\t"
      "s_cbranch_scc1 LOUT%=\n\t"
      "s_waitcnt vmcnt(0)\n\t"
      "LOUT%=:\n\t"
      : "+{v[116:123]}"(c0), "+{v[124:127]}"(c1), "+{v[0:15]}"(sc), "+{v[16:31]}"(s2), "+{v[100:107]}"(c3), "+{v[108:115]}"(cc), "+s"(A), "+s"(b), "+s"(wv), "+{s87}"(tch)
      :
      : "memory", "vcc", "scc", "v32", "v33", "v34", "v35", "v36", "v37", "v38", "v39", "v40", "v41", "v42", "v43", "v44", "v45", "v46", "v47", "v48", "v49", "v50", "v51", "v52", "v53", "v54", "v55", "v56", "v57", "v58", "v59", "v60", "v61", "v62", "v63", "v64", "v65", "v66", "v67", "v68", "v69", "v70", "v71", "v72", "v73", "v74", "v75", "v76", "v77", "v78", "v79", "v80", "v81", "v82", "v83", "v84", "v85", "v86", "v87", "v88", "v89", "v90", "v91", "v92", "v93", "v94", "v95", "v96", "v97", "v98", "v99", "v128", "v129", "v130", "v131", "v132", "v133", "v134", "v135", "v136", "v137", "v138", "v139", "v140", "v141", "v142", "v143", "v144", "v145", "v146", "v147", "v148", "v149", "v150", "v151", "v152", "v153", "v154", "v155", "v156", "v157", "v158", "v159", "v160", "v161", "v162", "v163", "v164", "v165", "v166", "v167", "v168", "v169", "v170", "v171", "v172", "v173", "v174", "v175", "v176", "v177", "v178", "v179", "v180", "v181", "v182", "v183", "v184", "v185", "v186", "v187", "v188", "v189", "v190", "v191", "v192", "v193", "v194", "v195", "v196", "v197", "v198", "v199", "v200", "v201", "v202", "v203", "v204", "v205", "v206", "v207", "v208", "v209", "v210", "v211", "v212", "v213", "v214", "v215", "v216", "v217", "v218", "v219", "v220", "v221", "v222", "v223", "v224", "v225", "v226", "v227", "v228", "v229", "v230", "v231", "v232", "v233", "v234", "v235", "v236", "v237", "v238", "v239", "v240", "v241", "v242", "v243", "v244", "v245", "v246", "v247", "v248", "v249", "v250", "v251", "v252", "v253", "v254", "v255", "a0", "a1", "a2", "a3", "a4", "a5", "a6", "a7", "a8", "a9", "a10", "a11", "a12", "a13", "a14", "a15", "a16", "a17", "a18", "a19", "a20", "a21", "a22", "a23", "a24", "a25", "a26", "a27", "a28", "a29", "a30", "a31", "a32", "a33", "a34", "a35", "a36", "a37", "a38", "a39", "a40", "a41", "a42", "a43", "a44", "a45", "a46", "a47", "a48", "a49", "a50", "a51", "a52", "a53", "a54", "a55", "a56", "a57", "a58", "a59", "a60", "a61", "a62", "a63", "a64", "a65", "a66", "a67", "a68", "a69", "a70", "a71", "a72", "a73", "a74", "a75", "a76", "a77", "a78", "a79", "a80", "a81", "a82", "a83", "a84", "a85", "a86", "a87", "a88", "a89", "a90", "a91", "a92", "a93", "a94", "a95", "a96", "a97", "a98", "a99", "a100", "a101", "a102", "a103", "a104", "a105", "a106", "a107", "a108", "a109", "a110", "a111", "a112", "a113", "a114", "a115", "a116", "a117", "a118", "a119", "a120", "a121", "a122", "a123", "a124", "a125", "a126", "a127", "a128", "a129", "a130", "a131", "a132", "a133", "a134", "a135", "a136", "a137", "a138", "a139", "a140", "a141", "a142", "a143", "a144", "a145", "a146", "a147", "a148", "a149", "a150", "a151", "a152", "a153", "a154", "a155", "a156", "a157", "a158", "a159", "a160", "a161", "a162", "a163", "a164", "a165", "a166", "a167", "a168", "a169", "a170", "a171", "a172", "a173", "a174", "a175", "a176", "a177", "a178", "a179", "a180", "a181", "a182", "a183", "a184", "a185", "a186", "a187", "a188", "a189", "a190", "a191", "a192", "a193", "a194", "a195", "a196", "a197", "a198", "a199", "a200", "a201", "a202", "a203", "a204", "a205", "a206", "a207", "a208", "a209", "a210", "a211", "a212", "a213", "a214", "a215", "a216", "a217", "a218", "a219", "a220", "a221", "a222", "a223", "a224", "a225", "a226", "a227", "a228", "a229", "a230", "a231", "a232", "a233", "a234", "a235", "a236", "a237", "a238", "a239", "a240", "a241", "a242", "a243", "a244", "a245", "a246", "a247", "a248", "a249", "a250", "a251", "a252", "a253", "a254", "a255", "s22", "s23", "s24", "s25", "s26", "s27", "s28", "s29", "s30", "s31", "s36", "s37", "s38", "s39", "s40", "s41", "s42", "s43", "s44", "s45", "s46", "s47", "s48", "s49", "s50", "s51", "s52", "s53", "s54", "s55", "s56", "s57", "s58", "s59", "s60", "s61", "s62", "s63", "s64", "s65", "s66", "s67", "s68", "s69", "s70", "s71", "s72", "s73", "s74", "s75", "s76", "s77", "s78", "s79", "s80", "s81", "s82", "s83", "s84", "s85", "s86", "s88", "s89", "s90", "s91", "s92", "s93", "s94", "s95", "s96", "s97", "s98", "s99");
}

#define SSLEEP 1
#define SNEED 0
#define TSLEEP 4
__device__ __forceinline__ unsigned mkkey(unsigned w, int idx) {
  unsigned u = w >> 16;
  u = (u & 0x7fffu) == 0 ? 0u : u;
  unsigned o = (u & 0x8000u) ? (~u & 0xffffu) : (u | 0x8000u);
  o = (u & 0x7fffu) > 0x7f80u ? 0u : o;
  return (o << 16) | (0xffffu - (unsigned)idx);
}
template <bool spread>
__device__ __forceinline__ u32x2 top2(const float* rbuf, int tid, int need, unsigned* sk) {
  unsigned key[9];
  const __amdgpu_buffer_rsrc_t rrs = __builtin_amdgcn_make_buffer_rsrc((void*)rbuf, 0, spread ? 2 * S_ * 4 + 256 * 128 : S_ * 4, 0x00020000);
  const int t8 = spread && tid >= 224 ? 0 : tid * 8;
#pragma unroll 1
  for (unsigned spins = 0; spins < (1u << 20); ++spins) {
    asm volatile("" ::: "memory");
    const u32x4 a = ld_sc1(rrs, t8 * 4), b = ld_sc1(rrs, t8 * 4 + 16);
    key[8] = spread ? __builtin_amdgcn_raw_buffer_load_b32(rrs, 2 * S_ * 4 + tid * 128, 0, 16) : a[0];
    unsigned mx = key[8];
#pragma unroll
    for (int k = 0; k < 4; ++k) { key[k] = a[k]; key[4 + k] = b[k]; mx = max(mx, max(a[k], b[k])); }
    if (mx != ~0u || tid * 8 >= need) break;
    __builtin_amdgcn_s_sleep(TSLEEP);
  }
  unsigned m = 0;
#pragma unroll
  for (int k = 0; k < 9; ++k) {
    key[k] = mkkey(key[k], k < 8 ? t8 + k : (spread ? S_ - 256 + tid : t8));
    m = max(m, key[k]);
  }
  m = wave_max_u32(m);
  __syncthreads();
  if ((tid & 63) == 0) sk[tid >> 6] = m;
  __syncthreads();
  const unsigned k0 = max(max(sk[0], sk[1]), max(sk[2], sk[3]));
  m = 0;
#pragma unroll
  for (int k = 0; k < 9; ++k) m = max(m, key[k] == k0 ? 0u : key[k]);
  m = wave_max_u32(m);
  if ((tid & 63) == 0) sk[4 + (tid >> 6)] = m;
  __syncthreads();
  const unsigned k1 = max(max(sk[4], sk[5]), max(sk[6], sk[7]));
  return (u32x2){(unsigned)__builtin_amdgcn_readfirstlane(k0), (unsigned)__builtin_amdgcn_readfirstlane(k1)};
}
__device__ __forceinline__ u32x2 top2_wave(const float* rbuf, int lane, bool dry, bool strict) {
  const __amdgpu_buffer_rsrc_t rrs = __builtin_amdgcn_make_buffer_rsrc((void*)rbuf, 0, S_ * 4, 0x00020000);
  asm volatile("" ::: "memory");
  u32x4 a[8];
#pragma unroll
  for (int i = 0; i < 8; ++i) a[i] = (u32x4){0u, 0u, 0u, 0u};
  if (!dry) {
#pragma unroll
    for (int i = 0; i < 8; ++i) a[i] = ld_sc1(rrs, (i * 64 + lane) * 16);
  }
  if (SNEED) {
    unsigned mx = 0;
#pragma unroll
    for (int i = 0; i < 8; ++i) {
      const unsigned x = max(max(a[i][0], a[i][1]), max(a[i][2], a[i][3]));
      mx = max(mx, i * 8 + (lane >> 3) < SNEED ? x : 0u);
    }
    if (strict && __builtin_amdgcn_ballot_w64(mx == ~0u)) return (u32x2){0u, 0u};
  }
  unsigned m1 = 0, m2 = 0;
#pragma unroll
  for (int i = 0; i < 32; ++i) {
    const unsigned w = a[i >> 2][i & 3], o = w ^ ((unsigned)((int)w >> 31) | 0x80000000u);
    const unsigned k = (o & 0xffff0000u) | ((0xffffu - (unsigned)(((i >> 2) * 64 + lane) * 4 + (i & 3))) & ~w);
    m2 = max(min(m1, k), m2);
    m1 = max(m1, k);
  }
  unsigned k[2];
  k[0] = wave_max_u32(m1);
  k[1] = wave_max_u32(m1 == k[0] ? m2 : m1);
  if (!k[1]) return (u32x2){0u, 0u};
#pragma unroll
  for (int i = 0; i < 2; ++i) k[i] = mkkey((k[i] >> 31) ? k[i] & 0x7fffffffu : ~k[i], 0xffff - (k[i] & 0xffff));
  return (u32x2){max(k[0], k[1]), min(k[0], k[1])};
}
__device__ __forceinline__ bool other_top2a(const float* rbuf, int tid, unsigned k0, unsigned k1) {
  const __amdgpu_buffer_rsrc_t rrs = __builtin_amdgcn_make_buffer_rsrc((void*)rbuf, 0, S_ * 4, 0x00020000);
  const int t8 = tid >= 224 ? 0 : tid * 8;
  bool bad = false;
#pragma unroll 1
  for (unsigned spins = 0; spins < (1u << 20); ++spins) {
    asm volatile("" ::: "memory");
    const u32x4 a = ld_sc1(rrs, t8 * 4), b = ld_sc1(rrs, t8 * 4 + 16);
    unsigned mx = 0;
    bad = false;
#pragma unroll
    for (int k = 0; k < 4; ++k) {
      const unsigned ka = mkkey(a[k], t8 + k), kb = mkkey(b[k], t8 + 4 + k);
      bad |= (ka > k1 && ka != k0) || (kb > k1 && kb != k0);
      mx = max(mx, max(a[k], b[k]));
    }
    if (__builtin_amdgcn_ballot_w64(mx == ~0u) == 0) break;
    __builtin_amdgcn_s_sleep(TSLEEP);
  }
  return __builtin_amdgcn_ballot_w64(bad) != 0;
}
__device__ __forceinline__ bool other_top2b(const float* rbuf, int tid, unsigned k0, unsigned k1, bool bad, unsigned* sk, unsigned& tmp, unsigned f) {
  if (RDLY && (f >> 16) == 63) {
    const unsigned t = f + RDLY - (3 - (tid >> 6)) * RDL2;
#pragma unroll 1
    while (((t - (unsigned)__builtin_amdgcn_s_memrealtime()) & 0xffffu) < 400u) __builtin_amdgcn_s_sleep(1);
  }
  const __amdgpu_buffer_rsrc_t rrs = __builtin_amdgcn_make_buffer_rsrc((void*)rbuf, 0, 2 * S_ * 4 + 256 * 128, 0x00020000);
  unsigned w = RDLY ? __builtin_amdgcn_raw_buffer_load_b32(rrs, 2 * S_ * 4 + tid * 128, 0, 16) : ~0u, n;
  tmp = 0;
  if (!RDLY || __builtin_amdgcn_ballot_w64(w == ~0u)) {
  asm volatile("buffer_load_dword %1, %3, %4, 0 offen sc1\n\ts_sleep %5\n\t"
               "buffer_load_dword %1, %3, %4, 0 offen sc1\n\ts_sleep %5\n\t"
               "s_mov_b32 %2, 0x100000\n"
               "TP%=:\n\t"
               "buffer_load_dword %1, %3, %4, 0 offen sc1\n\t"
               "s_sub_u32 %2, %2, 1\n\t"
               "s_waitcnt vmcnt(2)\n\t"
               "v_mov_b32 %0, %1\n\t"
               "v_cmp_eq_u32 vcc, -1, %0\n\t"
               "s_cbranch_scc1 TQ%=\n\t"
               "s_cbranch_vccnz TP%=\n"
               "TQ%=:"
               : "=&v"(w), "=&v"(tmp), "=&s"(n) : "v"(2 * S_ * 4 + tid * 128), "s"(rrs), "n"(PSLEEP) : "memory", "vcc", "scc");
  }
  const unsigned k = mkkey(w, S_ - 256 + tid);
  bad |= k > k1 && k != k0;
  const unsigned any = __builtin_amdgcn_ballot_w64(bad) != 0;
  if ((tid & 63) == 0) sk[tid >> 6] = any;
  LDS_BARRIER();
  const u32x4 v = *(const u32x4*)sk;
  return __builtin_amdgcn_readfirstlane(v[0] | v[1] | v[2] | v[3]) != 0;
}

constexpr int M1_WGS = FF / 16;
constexpr int L_WD = 131072;
__device__ __forceinline__ void wait_set(gu32* f) {
#pragma unroll 1
  for (unsigned spins = 0; spins < (1u << 18); ++spins) {
    if (__hip_atomic_load(f, RLX_AGENT) != ~0u) return;
    __builtin_amdgcn_s_sleep(4);
  }
}
__device__ __forceinline__ u32x2 wait_selt(char* ws, int b, unsigned& tm) {
  const __amdgpu_buffer_rsrc_t rs = __builtin_amdgcn_make_buffer_rsrc((void*)(ws + O_CNT), 0, 1024, 0x00020000);
  u32x4 v;
#pragma unroll 1
  for (unsigned spins = 0; spins < (1u << 18); ++spins) {
    asm volatile("" ::: "memory");
    v = ld_sc1(rs, (b & 7) * 64);
    if (v[1]) break;
    __builtin_amdgcn_s_sleep(FSLEEP);
  }
  tm = __builtin_amdgcn_readfirstlane(v[2]);
  return (u32x2){(unsigned)__builtin_amdgcn_readfirstlane(v[0]), (unsigned)__builtin_amdgcn_readfirstlane(v[1])};
}
__device__ __forceinline__ u32x2 wait_sel(gu32* f, u64 v = 0) {
#pragma unroll 1
  for (unsigned spins = 0; spins < (1u << 18) && !v; ++spins) {
    v = __hip_atomic_load((gu64*)f, RLX_AGENT);
    if (v) break;
    __builtin_amdgcn_s_sleep(FSLEEP);
  }
  return (u32x2){(unsigned)__builtin_amdgcn_readfirstlane((unsigned)v), (unsigned)__builtin_amdgcn_readfirstlane((unsigned)(v >> 32))};
}
__device__ __forceinline__ bf16x8 as_frag(u32x4 v) { return __builtin_bit_cast(bf16x8, v); }
__device__ __forceinline__ u32x4 tr_frag(char* p0, char* p1) {
  const s16x4 a = __builtin_amdgcn_ds_read_tr16_b64_v4i16((LAS s16x4*)p0), b = __builtin_amdgcn_ds_read_tr16_b64_v4i16((LAS s16x4*)p1);
  const u32x2 x = __builtin_bit_cast(u32x2, a), y = __builtin_bit_cast(u32x2, b);
  return (u32x4){x[0], x[1], y[0], y[1]};
}
__device__ __forceinline__ void mlp1_role(KArgs P, char* lds, int b, int tid) {
  float* part = (float*)lds;
  unsigned* si = (unsigned*)(lds + 4096);
  const int lane = tid & 63, wv = tid >> 6, l15 = lane & 15, l4 = lane >> 4;
  const __amdgpu_buffer_rsrc_t lrs = __builtin_amdgcn_make_buffer_rsrc((void*)(P->ws + O_LNH), 0, S_ * DM * 2, 0x00020000);
  const __amdgpu_buffer_rsrc_t mrs = __builtin_amdgcn_make_buffer_rsrc((void*)(P->ws + O_MFRAG), 0, 2 * DM * FF * 2, 0x00020000);
  u32x4 aw[2][4];
  MS(0);
  const u64 s0 = __hip_atomic_load((gu64*)(P->ws + O_CNT + (b & 7) * 64), RLX_AGENT);
  const unsigned fl = __hip_atomic_load((gu32*)(P->ws + O_KVSTEP + (b >> 2) * 64 + (b & 3) * 4), RLX_AGENT);
#pragma unroll
  for (int i = 0; i < 8; ++i) aw[i >> 2][i & 3] = ld_sc1(mrs, (unsigned)(b * 32768 + (i >> 2) * 16384 + (wv * 4 + (i & 3)) * 1024 + lane * 16));
  u32x2 sel = wait_sel((gu32*)(P->ws + O_CNT) + (b & 7) * 16, s0);
  MS(1);
  int round = 0;
#pragma unroll 1
  for (;;) {
    const int idx = 0xffff - (((l15 & 1) ? sel[1] : sel[0]) & 0xffff);
    if (round == 2) wait_set((gu32*)(P->ws + O_RBUF) + S_ + idx);
    u32x4 bx[4];
#pragma unroll
    for (int i = 0; i < 4; ++i) bx[i] = ld_sc1(lrs, (unsigned)(idx * 1024 + (wv * 4 + i) * 64 + l4 * 16));
    f32x4 ag = {0.f, 0.f, 0.f, 0.f}, au = {0.f, 0.f, 0.f, 0.f};
#pragma unroll
    for (int i = 0; i < 4; ++i) {
      ag = __builtin_amdgcn_mfma_f32_16x16x32_bf16(as_frag(aw[0][i]), as_frag(bx[i]), ag, 0, 0, 0);
      au = __builtin_amdgcn_mfma_f32_16x16x32_bf16(as_frag(aw[1][i]), as_frag(bx[i]), au, 0, 0, 0);
    }
    if (l15 < 2) {
      *(f32x4*)(part + wv * 64 + l15 * 16 + l4 * 4) = ag;
      *(f32x4*)(part + wv * 64 + 32 + l15 * 16 + l4 * 4) = au;
    }
    LDS_BARRIER();
    MS(2);
    if (tid < 16) {
      float a[2];
#pragma unroll
      for (int e = 0; e < 2; ++e) {
        const float* q = part + tid * 2 + e;
        const float g = rbf((q[0] + q[64]) + (q[128] + q[192])), u = rbf((q[32] + q[96]) + (q[160] + q[224]));
        a[e] = rbf(g / (1.0f + __expf(-g))) * u;
      }
      unsigned av = pk2(a[0], a[1]);
      av = av == ~0u ? 0x7fc07fc0u : av;
      __hip_atomic_store((gu32*)(P->ws + O_ACT) + (round * M1_WGS + b) * 32 + tid, av, RLX_AGENT);
    }
    MS(4);
    if (round == 2) break;
    if (SW2 && !round) {
      const u32x2 s2 = wait_sel((gu32*)(P->ws + O_SEL2) + (b & 31) * 32);
      if (s2[0] != sel[0] || s2[1] != sel[1]) { sel = s2; round = 1; __syncthreads(); continue; }
    }
    unsigned tmp;
    const bool again = other_top2b((const float*)(P->ws + O_RBUF), tid, sel[0], sel[1], other_top2a((const float*)(P->ws + O_RBUF), tid, sel[0], sel[1]), si, tmp, __builtin_amdgcn_readfirstlane(fl));
    MS(5);
    asm volatile("s_waitcnt vmcnt(0)" :: "v"(tmp) : "memory");
    MS(6);
    if (!again) break;
    sel = top2<true>((const float*)(P->ws + O_RBUF), tid, S_, si + 4);
    round = 2;
  }
}
__device__ __forceinline__ void mlp2_role(KArgs P, char* lds, int b, int tid) {
  float* part = (float*)lds;
  unsigned* si = (unsigned*)(lds + 4096);
  const int lane = tid & 63, wv = tid >> 6, l15 = lane & 15, l4 = lane >> 4;
  const __amdgpu_buffer_rsrc_t hrs = __builtin_amdgcn_make_buffer_rsrc((void*)(P->ws + O_HBUF), 0, S_ * DM * 2, 0x00020000);
  const __amdgpu_buffer_rsrc_t lrs = __builtin_amdgcn_make_buffer_rsrc((void*)(P->ws + O_LNH), 0, S_ * DM * 2, 0x00020000);
  const __amdgpu_buffer_rsrc_t ors = __builtin_amdgcn_make_buffer_rsrc((void*)P->out, 0, S_ * DM * 2, 0x00020000);
  const __amdgpu_buffer_rsrc_t ars = __builtin_amdgcn_make_buffer_rsrc((void*)(P->ws + O_ACT), 0, 3 * M1_WGS * 128, 0x00020000);
  u32x4 aw[7];
  const int c0 = b * 8;
  MS(0);
  {
    char* p = lds + L_WD + (((l15 >> 1) & 1) * 896 + wv * 224 + l4 * 8 + (l15 >> 2)) * 16 + (l15 & 1) * 8;
#pragma unroll
    for (int i = 0; i < 7; ++i) aw[i] = tr_frag(p + i * 512, p + i * 512 + 64);
  }
  unsigned tsel;
  u32x2 sel = wait_selt(P->ws, b, tsel);
  const unsigned fl = __hip_atomic_load((gu32*)(P->ws + O_KVSTEP + (c0 >> 4) * 64), RLX_AGENT);
  MS(1);
  int round = 0;
#pragma unroll 1
  for (;;) {
    const int idx = 0xffff - (((tid & 8) ? sel[1] : sel[0]) & 0xffff), col = c0 + (tid & 7);
    if (round == 2) wait_set((gu32*)(P->ws + O_RBUF) + S_ + idx);
    const float rr = __hip_atomic_load((const GAS float*)(P->ws + O_RBUF) + S_ + idx, RLX_AGENT);
    const float lv = bf2f(__builtin_amdgcn_raw_buffer_load_b16(lrs, (unsigned)(idx * DM + col) * 2, 0, 16));
    const float hv = bf2f(__builtin_amdgcn_raw_buffer_load_b16(hrs, (unsigned)(idx * DM + col) * 2, 0, 16));
    const float sg = rbf(1.0f / (1.0f + __expf(-rr)));
    const bool bad = other_top2a((const float*)(P->ws + O_RBUF), tid, sel[0], sel[1]);
    if (ADLY && !round) {
#pragma unroll 1
      while (tsel + ADLY - (unsigned)__builtin_amdgcn_s_memrealtime() < 400u) __builtin_amdgcn_s_sleep(1);
    } else if (!round && (ASALL || c0 < 256)) __builtin_amdgcn_s_sleep(ASLEEP);
    u32x4 av[7];
    u64 v2 = 0;
#pragma unroll 1
    for (unsigned spins = 0; spins < (1u << 20); ++spins) {
      asm volatile("" ::: "memory");
      unsigned mx = 0;
      if (SW2) v2 = __hip_atomic_load((gu64*)(P->ws + O_SEL2 + (b & 31) * 128), RLX_AGENT);
#pragma unroll
      for (int i = 0; i < 7; ++i) {
        av[i] = ld_sc1(ars, (unsigned)((round * M1_WGS + ((l15 >> 1) & 1) * 56 + (wv * 7 + i) * 2 + (l4 >> 1)) * 128 + (l15 & 1) * 32 + (l4 & 1) * 16));
        mx = max(max(mx, max(av[i][0], av[i][1])), max(av[i][2], av[i][3]));
      }
      if (__builtin_amdgcn_ballot_w64(mx == ~0u) == 0) break;
      __builtin_amdgcn_s_sleep(4);
    }
    MS(2);
    if (SW2 && !round) {
      const u32x2 s2 = wait_sel((gu32*)(P->ws + O_SEL2) + (b & 31) * 32, v2);
      if (s2[0] != sel[0] || s2[1] != sel[1]) { sel = s2; round = 1; continue; }
    }
    f32x4 a0 = {0.f, 0.f, 0.f, 0.f}, a1 = {0.f, 0.f, 0.f, 0.f};
#pragma unroll
    for (int i = 0; i < 7; ++i) {
      if (i & 1) a1 = __builtin_amdgcn_mfma_f32_16x16x32_bf16(as_frag(aw[i]), as_frag(av[i]), a1, 0, 0, 0);
      else a0 = __builtin_amdgcn_mfma_f32_16x16x32_bf16(as_frag(aw[i]), as_frag(av[i]), a0, 0, 0, 0);
    }
    if ((l15 >> 1) == (l4 >> 1)) *(f32x4*)(part + wv * 32 + l15 * 8 + (l4 & 1) * 4) = a0 + a1;
    LDS_BARRIER();
    const float* q = part + (tid & 15);
    const float y = rbf(((q[0] + q[16]) + (q[32] + q[48])) + ((q[64] + q[80]) + (q[96] + q[112])));
    const float upd = rbf(y * sg);
    const float bb = rbf(lv + upd);
    const u16 res = f2bf(hv + bb);
    MS(4);
    if (tid < 16) __builtin_amdgcn_raw_buffer_store_b16(res, ors, (unsigned)(idx * DM + col) * 2, 0, 16);
    if (round == 2) break;
    unsigned tmp;
    const bool again = other_top2b((const float*)(P->ws + O_RBUF), tid, sel[0], sel[1], bad, si, tmp, __builtin_amdgcn_readfirstlane(fl));
    MS(5);
    asm volatile("s_waitcnt vmcnt(0)" :: "v"(tmp) : "memory");
    MS(6);
    if (!again) break;
    if (tid < 16) __builtin_amdgcn_raw_buffer_store_b16(f2bf(hv + lv), ors, (unsigned)(idx * DM + col) * 2, 0, 16);
    VM_DRAIN();
    sel = top2<true>((const float*)(P->ws + O_RBUF), tid, S_, si + 4);
    round = 2;
  }
}

struct WoRegs { u32x32 a, b, c, d; };
__device__ __forceinline__ unsigned worker_role(int& w, int& wv, KArgs& A, WoRegs& wo) {
  unsigned tag;
  asm volatile(
      "GSTART:\n\t"
      "v_mbcnt_lo_u32_b32 v96, -1, 0\n\t"
      "v_mbcnt_hi_u32_b32 v96, -1, v96\n\t"
      "s_and_b32 s82, %0, 31\n\t"
      "s_mov_b32 s66, 4194304\n\t"
      "s_mov_b32 s67, 0x20000\n\t"
      "s_mov_b32 s62, 111181824\n\t"
      "s_mov_b32 s63, 0x20000\n\t"
      "v_and_b32 v104, 3, v96\n\t"
      "v_lshrrev_b32 v105, 2, v96\n\t"
      "s_lshl_b32 s78, %0, 14\n\t"
      "s_lshl_b32 s79, %1, 12\n\t"
      "s_add_u32 s79, s79, s78\n\t"
      "v_lshlrev_b32 v106, 10, v104\n\t"
      "v_lshl_add_u32 v106, v105, 4, v106\n\t"
      "v_lshlrev_b32 v99, 13, v105\n\t"
      "v_lshl_add_u32 v99, v104, 4, v99\n\t"
      "s_lshl_b32 s80, s82, 8\n\t"
      "s_lshl_b32 s72, %1, 6\n\t"
      "s_add_u32 s80, s80, s72\n\t"
      "v_add_u32 v99, s80, v99\n\t"
      "s_bitcmp1_b32 %0, 5\n\t"
      "s_waitcnt lgkmcnt(0)\n\t"
      "s_mov_b64 s[64:65], s[36:37]\n\t"
      "s_mov_b64 s[60:61], s[58:59]\n\t"
      "buffer_load_dwordx4 v[64:67], v106, s[64:67], s79 offen \n\t"
      "buffer_load_dwordx4 v[68:71], v106, s[64:67], s79 offen offset:256 \n\t"
      "buffer_load_dwordx4 v[72:75], v106, s[64:67], s79 offen offset:512 \n\t"
      "buffer_load_dwordx4 v[76:79], v106, s[64:67], s79 offen offset:768 \n\t"
      "s_cselect_b64 s[38:39], s[44:45], s[42:43]\n\t"
      "s_mov_b32 s78, 0\n\t"
      "s_load_dword s87, s[38:39], s78\n\t"
      "s_mov_b32 s78, 4194300\n\t"
      "s_load_dword s87, s[38:39], s78\n\t"
      "s_mov_b32 s78, 128\n\t"
      "s_load_dword s87, s[60:61], s78\n\t"
      "s_mov_b32 s78, 6520704\n\t"
      "s_load_dword s87, s[60:61], s78\n\t"
      "v_mov_b32 v118, v106\n\t"
      "s_mov_b64 s[64:65], s[36:37]\n\t"
      "s_lshr_b32 s78, %0, 6\n\t"
      "s_lshl_b32 s78, s78, 15\n\t"
      "s_lshl_b32 s79, %1, 12\n\t"
      "s_add_u32 s79, s79, s78\n\t"
      "buffer_load_dwordx4 v[128:131], v118, s[64:67], s79 offen \n\t"
      "buffer_load_dwordx4 v[132:135], v118, s[64:67], s79 offen offset:256 \n\t"
      "buffer_load_dwordx4 v[136:139], v118, s[64:67], s79 offen offset:512 \n\t"
      "buffer_load_dwordx4 v[140:143], v118, s[64:67], s79 offen offset:768 \n\t"
      "s_add_u32 s79, s79, 16384\n\t"
      "buffer_load_dwordx4 v[144:147], v118, s[64:67], s79 offen \n\t"
      "buffer_load_dwordx4 v[148:151], v118, s[64:67], s79 offen offset:256 \n\t"
      "buffer_load_dwordx4 v[152:155], v118, s[64:67], s79 offen offset:512 \n\t"
      "buffer_load_dwordx4 v[156:159], v118, s[64:67], s79 offen offset:768 \n\t"
      "s_mov_b64 s[64:65], s[38:39]\n\t"
      "s_mov_b64 s[64:65], s[38:39]\n\t"
      "buffer_load_dword v107, off, s[60:63], 0 sc1\n\t"
      "v_lshrrev_b32 v101, 4, v96\n\t"
      "v_and_b32 v102, 15, v96\n\t"
      "v_lshlrev_b32 v103, 2, v101\n\t"
      "v_xor_b32 v102, v102, v103\n\t"
      "v_lshlrev_b32 v108, 13, v101\n\t"
      "v_lshl_add_u32 v108, v102, 4, v108\n\t"
      "s_lshl_b32 s78, s82, 8\n\t"
      "s_lshl_b32 s79, %1, 16\n\t"
      "s_add_u32 s78, s78, s79\n\t"
      "v_add_u32 v108, s78, v108\n\t"
      "s_lshl_b32 s76, %1, 11\n\t"
      "s_mov_b32 s77, 0\n\t"
      "s_add_u32 m0, s76, 0\n\t"
      "s_add_u32 s68, s77, 0\n\t"
      "buffer_load_dwordx4 v108, s[64:67], s68 offen lds\n\t"
      "s_add_u32 m0, s76, 1024\n\t"
      "s_add_u32 s68, s77, 32768\n\t"
      "s_add_u32 s76, s76, 8192\n\t"
      "s_add_u32 s77, s77, 0x40000\n\t"
      "buffer_load_dwordx4 v108, s[64:67], s68 offen lds\n\t"
      "s_mov_b32 s84, 0xffff0000\n\t"
      "s_mov_b32 s86, 0\n\t"
      "s_lshl_b32 s85, %1, 13\n\t"
      "v_lshlrev_b32 v97, 4, v96\n\t"
      "v_lshrrev_b32 v101, 4, v96\n\t"
      "v_and_b32 v102, 3, v105\n\t"
      "v_lshlrev_b32 v100, 9, v101\n\t"
      "v_lshl_add_u32 v100, v102, 6, v100\n\t"
      "v_lshl_add_u32 v100, v104, 4, v100\n\t"
      "v_lshlrev_b32 v106, 10, v101\n\t"
      "v_lshl_add_u32 v106, v102, 8, v106\n\t"
      "v_lshl_add_u32 v106, v104, 4, v106\n\t"
      "s_lshl_b32 s74, %0, 14\n\t"
      "s_add_u32 s74, s74, s72\n\t"
      "s_add_u32 s74, s74, 6520832\n\t"
      "s_lshr_b32 s78, %0, 6\n\t"
      "v_min_u32 v104, 31, v96\n\t"
      "v_lshl_add_u32 v104, v104, 1, s78\n\t"
      "s_lshr_b32 s79, %0, 5\n\t"
      "s_and_b32 s83, s79, 1\n\t"
      "s_add_u32 s80, s85, 6520832\n\t"
      "v_lshl_add_u32 v101, v104, 15, s80\n\t"
      "s_mul_i32 s80, s82, 24576\n\t"
      "s_lshl_b32 s72, %1, 11\n\t"
      "s_add_u32 s80, s80, s72\n\t"
      "s_add_u32 s80, s80, 8617984\n\t"
      "s_lshl_b32 s72, s83, 13\n\t"
      "s_add_u32 s80, s80, s72\n\t"
      "v_mov_b32 v102, s80\n\t"
      "s_mov_b32 s72, 786432\n\t"
      "v_mad_u32_u24 v102, v104, s72, v102\n\t"
      "s_lshl_b32 s80, s82, 6\n\t"
      "s_lshl_b32 s72, s83, 2\n\t"
      "s_add_u32 s80, s80, s72\n\t"
      "s_add_u32 s80, s80, 256\n\t"
      "v_lshrrev_b32 v105, 2, v104\n\t"
      "v_lshl_add_u32 v103, v105, 11, s80\n\t"
      "v_and_b32 v104, 3, v104\n\t"
      "v_lshl_add_u32 v103, v104, 4, v103\n\t"
      "s_lshl_b32 s80, s82, 7\n\t"
      "s_lshl_b32 s79, s79, 2\n\t"
      "s_add_u32 s80, s80, s79\n\t"
      "s_add_u32 s80, s80, 33024\n\t"
      "v_writelane_b32 v103, s80, 0\n\t"
      "v_writelane_b32 v103, 0, 63\n\t"
      "s_cmp_eq_u32 %1, 0\n\t"
      "s_cselect_b64 s[90:91], 1, 0\n\t"
      "v_lshrrev_b32 v104, 4, v96\n\t"
      "v_bfe_u32 v105, v96, 2, 2\n\t"
      "v_and_b32 v110, 3, v96\n\t"
      "s_lshl_b32 s78, %1, 2\n\t"
      "v_lshl_add_u32 v109, v104, 3, v105\n\t"
      "v_lshlrev_b32 v109, 8, v109\n\t"
      "v_or_b32 v110, s78, v110\n\t"
      "v_lshlrev_b32 v105, 2, v105\n\t"
      "v_xor_b32 v110, v110, v105\n\t"
      "v_lshl_add_u32 v109, v110, 4, v109\n\t"
      "s_lshl_b32 s78, %1, 6\n\t"
      "s_add_u32 s78, s78, 131072\n\t"
      "v_add_u32 v110, s78, v106\n\t"
      "s_bfe_u32 s80, %0, 0x10005\n\t"
      "s_and_b32 s79, %1, 1\n\t"
      "v_bfe_u32 v105, v96, 4, 1\n\t"
      "v_lshlrev_b32 v105, 1, v105\n\t"
      "v_and_b32 v116, 7, v96\n\t"
      "v_xor_b32 v116, v116, v105\n\t"
      "s_lshl_b32 s78, s79, 2\n\t"
      "v_xor_b32 v116, s78, v116\n\t"
      "v_lshlrev_b32 v116, 4, v116\n\t"
      "v_lshrrev_b32 v105, 3, v96\n\t"
      "v_lshl_add_u32 v116, v105, 13, v116\n\t"
      "s_lshl_b32 s78, s82, 8\n\t"
      "s_lshl_b32 s72, s80, 7\n\t"
      "s_add_u32 s78, s78, s72\n\t"
      "s_lshl_b32 s72, %1, 16\n\t"
      "s_add_u32 s78, s78, s72\n\t"
      "v_add_u32 v116, s78, v116\n\t"
      "v_bfe_u32 v105, v96, 2, 2\n\t"
      "v_lshl_add_u32 v117, v104, 3, v105\n\t"
      "v_lshlrev_b32 v117, 7, v117\n\t"
      "v_bfe_u32 v105, v96, 3, 2\n\t"
      "v_xor_b32 v105, %1, v105\n\t"
      "v_lshl_add_u32 v117, v105, 5, v117\n\t"
      "v_and_b32 v105, 3, v96\n\t"
      "v_lshl_add_u32 v117, v105, 3, v117\n\t"
      "v_and_b32 v111, 15, v96\n\t"
      "v_lshlrev_b32 v111, 4, v111\n\t"
      "v_and_b32 v105, 1, v104\n\t"
      "v_lshl_add_u32 v111, v105, 10, v111\n\t"
      "v_lshrrev_b32 v105, 1, v104\n\t"
      "v_lshl_add_u32 v111, v105, 8, v111\n\t"
      "s_lshl_b32 s78, s79, 9\n\t"
      "s_add_u32 s78, s78, 16384\n\t"
      "s_lshl_b32 s80, s80, 12\n\t"
      "s_sub_u32 s78, s78, s80\n\t"
      "s_add_u32 s80, %1, 1\n\t"
      "s_lshr_b32 s80, s80, 1\n\t"
      "s_lshl_b32 s80, s80, 11\n\t"
      "s_sub_u32 s78, s78, s80\n\t"
      "v_add_u32 v111, s78, v111\n\t"
      "s_mov_b32 s69, 0\n\t"
      "s_add_u32 m0, s76, 0\n\t"
      "s_add_u32 s68, s77, 0\n\t"
      "buffer_load_dwordx4 v108, s[64:67], s68 offen lds\n\t"
      "s_add_u32 m0, s76, 1024\n\t"
      "s_add_u32 s68, s77, 32768\n\t"
      "s_add_u32 s76, s76, 8192\n\t"
      "s_add_u32 s77, s77, 0x40000\n\t"
      "buffer_load_dwordx4 v108, s[64:67], s68 offen lds\n\t"
      "s_add_u32 m0, s76, 0\n\t"
      "s_add_u32 s68, s77, 0\n\t"
      "buffer_load_dwordx4 v108, s[64:67], s68 offen lds\n\t"
      "s_add_u32 m0, s76, 1024\n\t"
      "s_add_u32 s68, s77, 32768\n\t"
      "s_add_u32 s76, s76, 8192\n\t"
      "s_add_u32 s77, s77, 0x40000\n\t"
      "buffer_load_dwordx4 v108, s[64:67], s68 offen lds\n\t"
      "s_add_u32 m0, s76, 0\n\t"
      "s_add_u32 s68, s77, 0\n\t"
      "buffer_load_dwordx4 v108, s[64:67], s68 offen lds\n\t"
      "s_add_u32 m0, s76, 1024\n\t"
      "s_add_u32 s68, s77, 32768\n\t"
      "s_add_u32 s76, s76, 8192\n\t"
      "s_add_u32 s77, s77, 0x40000\n\t"
      "buffer_load_dwordx4 v108, s[64:67], s68 offen lds\n\t"
      "s_mov_b64 s[88:89], 0\n\t"
      "s_waitcnt vmcnt(8)\n\t"
      "GLN%=:\n\t"
      "v_and_b32 v1, s84, v64\n\t"
      "v_lshlrev_b32 v0, 16, v64\n\t"
      "v_and_b32 v3, s84, v65\n\t"
      "v_lshlrev_b32 v2, 16, v65\n\t"
      "v_and_b32 v5, s84, v66\n\t"
      "v_lshlrev_b32 v4, 16, v66\n\t"
      "v_and_b32 v7, s84, v67\n\t"
      "v_lshlrev_b32 v6, 16, v67\n\t"
      "v_and_b32 v9, s84, v68\n\t"
      "v_lshlrev_b32 v8, 16, v68\n\t"
      "v_and_b32 v11, s84, v69\n\t"
      "v_lshlrev_b32 v10, 16, v69\n\t"
      "v_and_b32 v13, s84, v70\n\t"
      "v_lshlrev_b32 v12, 16, v70\n\t"
      "v_and_b32 v15, s84, v71\n\t"
      "v_lshlrev_b32 v14, 16, v71\n\t"
      "s_mov_b64 exec, s[88:89]\n\t"
      "s_add_u32 m0, s76, 0\n\t"
      "s_add_u32 s68, s77, 0\n\t"
      "buffer_load_dwordx4 v108, s[64:67], s68 offen lds\n\t"
      "s_mov_b64 exec, -1\n\t"
      "v_and_b32 v17, s84, v72\n\t"
      "v_lshlrev_b32 v16, 16, v72\n\t"
      "v_and_b32 v19, s84, v73\n\t"
      "v_lshlrev_b32 v18, 16, v73\n\t"
      "v_and_b32 v21, s84, v74\n\t"
      "v_lshlrev_b32 v20, 16, v74\n\t"
      "v_and_b32 v23, s84, v75\n\t"
      "v_lshlrev_b32 v22, 16, v75\n\t"
      "v_and_b32 v25, s84, v76\n\t"
      "v_lshlrev_b32 v24, 16, v76\n\t"
      "v_and_b32 v27, s84, v77\n\t"
      "v_lshlrev_b32 v26, 16, v77\n\t"
      "v_and_b32 v29, s84, v78\n\t"
      "v_lshlrev_b32 v28, 16, v78\n\t"
      "v_and_b32 v31, s84, v79\n\t"
      "v_lshlrev_b32 v30, 16, v79\n\t"
      "s_mov_b64 exec, s[88:89]\n\t"
      "s_add_u32 m0, s76, 1024\n\t"
      "s_add_u32 s68, s77, 32768\n\t"
      "s_add_u32 s76, s76, 8192\n\t"
      "s_add_u32 s77, s77, 0x40000\n\t"
      "buffer_load_dwordx4 v108, s[64:67], s68 offen lds\n\t"
      "s_mov_b64 exec, -1\n\t"
      "v_pk_add_f32 v[32:33], v[0:1], v[2:3]\n\t"
      "v_pk_add_f32 v[34:35], v[4:5], v[6:7]\n\t"
      "v_pk_add_f32 v[36:37], v[8:9], v[10:11]\n\t"
      "v_pk_add_f32 v[38:39], v[12:13], v[14:15]\n\t"
      "v_pk_add_f32 v[40:41], v[16:17], v[18:19]\n\t"
      "v_pk_add_f32 v[42:43], v[20:21], v[22:23]\n\t"
      "v_pk_add_f32 v[44:45], v[24:25], v[26:27]\n\t"
      "v_pk_add_f32 v[46:47], v[28:29], v[30:31]\n\t"
      "v_pk_add_f32 v[32:33], v[32:33], v[40:41]\n\t"
      "v_pk_add_f32 v[34:35], v[34:35], v[42:43]\n\t"
      "v_pk_add_f32 v[36:37], v[36:37], v[44:45]\n\t"
      "v_pk_add_f32 v[38:39], v[38:39], v[46:47]\n\t"
      "v_pk_add_f32 v[32:33], v[32:33], v[36:37]\n\t"
      "v_pk_add_f32 v[34:35], v[34:35], v[38:39]\n\t"
      "v_pk_add_f32 v[32:33], v[32:33], v[34:35]\n\t"
      "v_add_f32 v32, v32, v33\n\t"
      "s_mov_b64 exec, s[88:89]\n\t"
      "s_add_u32 m0, s76, 0\n\t"
      "s_add_u32 s68, s77, 0\n\t"
      "buffer_load_dwordx4 v108, s[64:67], s68 offen lds\n\t"
      "s_mov_b64 exec, -1\n\t"
      "v_add_f32_dpp v32, v32, v32 row_ror:4 row_mask:0xf bank_mask:0xf\n\t"
      "s_nop 1\n\t"
      "v_add_f32_dpp v32, v32, v32 row_ror:8 row_mask:0xf bank_mask:0xf\n\t"
      "v_mov_b32 v33, v32\n\t"
      "s_nop 1\n\t"
      "v_permlane16_swap_b32 v32, v33\n\t"
      "v_add_f32 v32, v32, v33\n\t"
      "v_mov_b32 v33, v32\n\t"
      "s_nop 1\n\t"
      "v_permlane32_swap_b32 v32, v33\n\t"
      "v_add_f32 v32, v32, v33\n\t"
      "v_mul_f32 v80, 0x3b000000, v32\n\t"
      "v_pk_add_f32 v[32:33], v[0:1], v[80:81] op_sel_hi:[1,0] neg_lo:[0,1] neg_hi:[0,1]\n\t"
      "v_pk_add_f32 v[34:35], v[2:3], v[80:81] op_sel_hi:[1,0] neg_lo:[0,1] neg_hi:[0,1]\n\t"
      "v_pk_add_f32 v[36:37], v[4:5], v[80:81] op_sel_hi:[1,0] neg_lo:[0,1] neg_hi:[0,1]\n\t"
      "v_pk_add_f32 v[38:39], v[6:7], v[80:81] op_sel_hi:[1,0] neg_lo:[0,1] neg_hi:[0,1]\n\t"
      "v_pk_add_f32 v[40:41], v[8:9], v[80:81] op_sel_hi:[1,0] neg_lo:[0,1] neg_hi:[0,1]\n\t"
      "v_pk_add_f32 v[42:43], v[10:11], v[80:81] op_sel_hi:[1,0] neg_lo:[0,1] neg_hi:[0,1]\n\t"
      "v_pk_add_f32 v[44:45], v[12:13], v[80:81] op_sel_hi:[1,0] neg_lo:[0,1] neg_hi:[0,1]\n\t"
      "s_mov_b64 exec, s[88:89]\n\t"
      "s_add_u32 m0, s76, 1024\n\t"
      "s_add_u32 s68, s77, 32768\n\t"
      "s_add_u32 s76, s76, 8192\n\t"
      "s_add_u32 s77, s77, 0x40000\n\t"
      "buffer_load_dwordx4 v108, s[64:67], s68 offen lds\n\t"
      "s_mov_b64 exec, -1\n\t"
      "v_pk_add_f32 v[46:47], v[14:15], v[80:81] op_sel_hi:[1,0] neg_lo:[0,1] neg_hi:[0,1]\n\t"
      "v_pk_add_f32 v[48:49], v[16:17], v[80:81] op_sel_hi:[1,0] neg_lo:[0,1] neg_hi:[0,1]\n\t"
      "v_pk_add_f32 v[50:51], v[18:19], v[80:81] op_sel_hi:[1,0] neg_lo:[0,1] neg_hi:[0,1]\n\t"
      "v_pk_add_f32 v[52:53], v[20:21], v[80:81] op_sel_hi:[1,0] neg_lo:[0,1] neg_hi:[0,1]\n\t"
      "v_pk_add_f32 v[54:55], v[22:23], v[80:81] op_sel_hi:[1,0] neg_lo:[0,1] neg_hi:[0,1]\n\t"
      "v_pk_add_f32 v[56:57], v[24:25], v[80:81] op_sel_hi:[1,0] neg_lo:[0,1] neg_hi:[0,1]\n\t"
      "v_pk_add_f32 v[58:59], v[26:27], v[80:81] op_sel_hi:[1,0] neg_lo:[0,1] neg_hi:[0,1]\n\t"
      "v_pk_add_f32 v[60:61], v[28:29], v[80:81] op_sel_hi:[1,0] neg_lo:[0,1] neg_hi:[0,1]\n\t"
      "v_pk_add_f32 v[62:63], v[30:31], v[80:81] op_sel_hi:[1,0] neg_lo:[0,1] neg_hi:[0,1]\n\t"
      "v_pk_mul_f32 v[64:65], v[32:33], v[32:33]\n\t"
      "v_pk_mul_f32 v[66:67], v[34:35], v[34:35]\n\t"
      "v_pk_mul_f32 v[68:69], v[36:37], v[36:37]\n\t"
      "v_pk_mul_f32 v[70:71], v[38:39], v[38:39]\n\t"
      "v_pk_fma_f32 v[64:65], v[40:41], v[40:41], v[64:65]\n\t"
      "v_pk_fma_f32 v[66:67], v[42:43], v[42:43], v[66:67]\n\t"
      "v_pk_fma_f32 v[68:69], v[44:45], v[44:45], v[68:69]\n\t"
      "s_mov_b64 exec, s[88:89]\n\t"
      "s_add_u32 m0, s76, 0\n\t"
      "s_add_u32 s68, s77, 0\n\t"
      "buffer_load_dwordx4 v108, s[64:67], s68 offen lds\n\t"
      "s_mov_b64 exec, -1\n\t"
      "v_pk_fma_f32 v[70:71], v[46:47], v[46:47], v[70:71]\n\t"
      "v_pk_fma_f32 v[64:65], v[48:49], v[48:49], v[64:65]\n\t"
      "v_pk_fma_f32 v[66:67], v[50:51], v[50:51], v[66:67]\n\t"
      "v_pk_fma_f32 v[68:69], v[52:53], v[52:53], v[68:69]\n\t"
      "v_pk_fma_f32 v[70:71], v[54:55], v[54:55], v[70:71]\n\t"
      "v_pk_fma_f32 v[64:65], v[56:57], v[56:57], v[64:65]\n\t"
      "v_pk_fma_f32 v[66:67], v[58:59], v[58:59], v[66:67]\n\t"
      "v_pk_fma_f32 v[68:69], v[60:61], v[60:61], v[68:69]\n\t"
      "v_pk_fma_f32 v[70:71], v[62:63], v[62:63], v[70:71]\n\t"
      "v_pk_add_f32 v[64:65], v[64:65], v[66:67]\n\t"
      "v_pk_add_f32 v[68:69], v[68:69], v[70:71]\n\t"
      "v_pk_add_f32 v[64:65], v[64:65], v[68:69]\n\t"
      "v_add_f32 v64, v64, v65\n\t"
      "s_nop 1\n\t"
      "v_add_f32_dpp v64, v64, v64 row_ror:4 row_mask:0xf bank_mask:0xf\n\t"
      "s_nop 1\n\t"
      "v_add_f32_dpp v64, v64, v64 row_ror:8 row_mask:0xf bank_mask:0xf\n\t"
      "v_mov_b32 v65, v64\n\t"
      "s_mov_b64 exec, s[88:89]\n\t"
      "s_add_u32 m0, s76, 1024\n\t"
      "s_add_u32 s68, s77, 32768\n\t"
      "s_add_u32 s76, s76, 8192\n\t"
      "s_add_u32 s77, s77, 0x40000\n\t"
      "buffer_load_dwordx4 v108, s[64:67], s68 offen lds\n\t"
      "s_mov_b64 exec, -1\n\t"
      "v_permlane16_swap_b32 v64, v65\n\t"
      "v_add_f32 v64, v64, v65\n\t"
      "v_mov_b32 v65, v64\n\t"
      "s_nop 1\n\t"
      "v_permlane32_swap_b32 v64, v65\n\t"
      "v_add_f32 v64, v64, v65\n\t"
      "v_mul_f32 v81, 0x3b000000, v64\n\t"
      "v_cvt_pk_bf16_f32 v80, v80, v81\n\t"
      "v_and_b32 v81, s84, v80\n\t"
      "v_lshlrev_b32 v80, 16, v80\n\t"
      "v_add_f32 v81, 0x3727c5ac, v81\n\t"
      "v_pk_add_f32 v[32:33], v[0:1], v[80:81] op_sel_hi:[1,0] neg_lo:[0,1] neg_hi:[0,1]\n\t"
      "v_pk_add_f32 v[34:35], v[2:3], v[80:81] op_sel_hi:[1,0] neg_lo:[0,1] neg_hi:[0,1]\n\t"
      "v_pk_add_f32 v[36:37], v[4:5], v[80:81] op_sel_hi:[1,0] neg_lo:[0,1] neg_hi:[0,1]\n\t"
      "v_pk_add_f32 v[38:39], v[6:7], v[80:81] op_sel_hi:[1,0] neg_lo:[0,1] neg_hi:[0,1]\n\t"
      "v_pk_add_f32 v[40:41], v[8:9], v[80:81] op_sel_hi:[1,0] neg_lo:[0,1] neg_hi:[0,1]\n\t"
      "v_pk_add_f32 v[42:43], v[10:11], v[80:81] op_sel_hi:[1,0] neg_lo:[0,1] neg_hi:[0,1]\n\t"
      "s_mov_b64 exec, s[88:89]\n\t"
      "s_add_u32 m0, s76, 0\n\t"
      "s_add_u32 s68, s77, 0\n\t"
      "buffer_load_dwordx4 v108, s[64:67], s68 offen lds\n\t"
      "s_mov_b64 exec, -1\n\t"
      "v_pk_add_f32 v[44:45], v[12:13], v[80:81] op_sel_hi:[1,0] neg_lo:[0,1] neg_hi:[0,1]\n\t"
      "v_pk_add_f32 v[46:47], v[14:15], v[80:81] op_sel_hi:[1,0] neg_lo:[0,1] neg_hi:[0,1]\n\t"
      "v_pk_add_f32 v[48:49], v[16:17], v[80:81] op_sel_hi:[1,0] neg_lo:[0,1] neg_hi:[0,1]\n\t"
      "v_pk_add_f32 v[50:51], v[18:19], v[80:81] op_sel_hi:[1,0] neg_lo:[0,1] neg_hi:[0,1]\n\t"
      "v_pk_add_f32 v[52:53], v[20:21], v[80:81] op_sel_hi:[1,0] neg_lo:[0,1] neg_hi:[0,1]\n\t"
      "v_pk_add_f32 v[54:55], v[22:23], v[80:81] op_sel_hi:[1,0] neg_lo:[0,1] neg_hi:[0,1]\n\t"
      "v_pk_add_f32 v[56:57], v[24:25], v[80:81] op_sel_hi:[1,0] neg_lo:[0,1] neg_hi:[0,1]\n\t"
      "v_pk_add_f32 v[58:59], v[26:27], v[80:81] op_sel_hi:[1,0] neg_lo:[0,1] neg_hi:[0,1]\n\t"
      "v_pk_add_f32 v[60:61], v[28:29], v[80:81] op_sel_hi:[1,0] neg_lo:[0,1] neg_hi:[0,1]\n\t"
      "v_pk_add_f32 v[62:63], v[30:31], v[80:81] op_sel_hi:[1,0] neg_lo:[0,1] neg_hi:[0,1]\n\t"
      "v_cvt_pk_bf16_f32 v81, v81, v81\n\t"
      "v_and_b32 v81, s84, v81\n\t"
      "v_sqrt_f32 v81, v81\n\t"
      "v_cvt_pk_bf16_f32 v32, v32, v33\n\t"
      "v_cvt_pk_bf16_f32 v34, v34, v35\n\t"
      "v_cvt_pk_bf16_f32 v36, v36, v37\n\t"
      "s_mov_b64 exec, s[88:89]\n\t"
      "s_add_u32 m0, s76, 1024\n\t"
      "s_add_u32 s68, s77, 32768\n\t"
      "s_add_u32 s76, s76, 8192\n\t"
      "s_add_u32 s77, s77, 0x40000\n\t"
      "buffer_load_dwordx4 v108, s[64:67], s68 offen lds\n\t"
      "s_mov_b64 exec, -1\n\t"
      "v_cvt_pk_bf16_f32 v38, v38, v39\n\t"
      "v_cvt_pk_bf16_f32 v40, v40, v41\n\t"
      "v_cvt_pk_bf16_f32 v42, v42, v43\n\t"
      "v_cvt_pk_bf16_f32 v44, v44, v45\n\t"
      "v_cvt_pk_bf16_f32 v46, v46, v47\n\t"
      "v_cvt_pk_bf16_f32 v48, v48, v49\n\t"
      "v_cvt_pk_bf16_f32 v50, v50, v51\n\t"
      "v_cvt_pk_bf16_f32 v52, v52, v53\n\t"
      "v_cvt_pk_bf16_f32 v54, v54, v55\n\t"
      "v_cvt_pk_bf16_f32 v56, v56, v57\n\t"
      "v_cvt_pk_bf16_f32 v58, v58, v59\n\t"
      "v_cvt_pk_bf16_f32 v60, v60, v61\n\t"
      "v_cvt_pk_bf16_f32 v62, v62, v63\n\t"
      "v_cvt_pk_bf16_f32 v81, v81, v81\n\t"
      "v_and_b32 v81, s84, v81\n\t"
      "v_rcp_f32 v80, v81\n\t"
      "s_mov_b64 exec, s[88:89]\n\t"
      "s_add_u32 m0, s76, 0\n\t"
      "s_add_u32 s68, s77, 0\n\t"
      "buffer_load_dwordx4 v108, s[64:67], s68 offen lds\n\t"
      "s_mov_b64 exec, -1\n\t"
      "v_and_b32 v33, s84, v32\n\t"
      "v_lshlrev_b32 v32, 16, v32\n\t"
      "v_and_b32 v35, s84, v34\n\t"
      "v_lshlrev_b32 v34, 16, v34\n\t"
      "v_and_b32 v37, s84, v36\n\t"
      "v_lshlrev_b32 v36, 16, v36\n\t"
      "v_and_b32 v39, s84, v38\n\t"
      "v_lshlrev_b32 v38, 16, v38\n\t"
      "v_pk_mul_f32 v[32:33], v[32:33], v[80:81] op_sel_hi:[1,0]\n\t"
      "v_pk_mul_f32 v[34:35], v[34:35], v[80:81] op_sel_hi:[1,0]\n\t"
      "v_pk_mul_f32 v[36:37], v[36:37], v[80:81] op_sel_hi:[1,0]\n\t"
      "v_pk_mul_f32 v[38:39], v[38:39], v[80:81] op_sel_hi:[1,0]\n\t"
      "v_cvt_pk_bf16_f32 v64, v32, v33\n\t"
      "v_cvt_pk_bf16_f32 v65, v34, v35\n\t"
      "v_cvt_pk_bf16_f32 v66, v36, v37\n\t"
      "v_cvt_pk_bf16_f32 v67, v38, v39\n\t"
      "s_mov_b64 exec, s[88:89]\n\t"
      "s_add_u32 m0, s76, 1024\n\t"
      "s_add_u32 s68, s77, 32768\n\t"
      "s_add_u32 s76, s76, 8192\n\t"
      "s_add_u32 s77, s77, 0x40000\n\t"
      "buffer_load_dwordx4 v108, s[64:67], s68 offen lds\n\t"
      "s_mov_b64 exec, -1\n\t"
      "v_and_b32 v41, s84, v40\n\t"
      "v_lshlrev_b32 v40, 16, v40\n\t"
      "v_and_b32 v43, s84, v42\n\t"
      "v_lshlrev_b32 v42, 16, v42\n\t"
      "v_and_b32 v45, s84, v44\n\t"
      "v_lshlrev_b32 v44, 16, v44\n\t"
      "v_and_b32 v47, s84, v46\n\t"
      "v_lshlrev_b32 v46, 16, v46\n\t"
      "v_pk_mul_f32 v[40:41], v[40:41], v[80:81] op_sel_hi:[1,0]\n\t"
      "v_pk_mul_f32 v[42:43], v[42:43], v[80:81] op_sel_hi:[1,0]\n\t"
      "v_pk_mul_f32 v[44:45], v[44:45], v[80:81] op_sel_hi:[1,0]\n\t"
      "v_pk_mul_f32 v[46:47], v[46:47], v[80:81] op_sel_hi:[1,0]\n\t"
      "v_cvt_pk_bf16_f32 v68, v40, v41\n\t"
      "v_cvt_pk_bf16_f32 v69, v42, v43\n\t"
      "v_cvt_pk_bf16_f32 v70, v44, v45\n\t"
      "v_cvt_pk_bf16_f32 v71, v46, v47\n\t"
      "s_mov_b64 exec, s[88:89]\n\t"
      "s_add_u32 m0, s76, 0\n\t"
      "s_add_u32 s68, s77, 0\n\t"
      "buffer_load_dwordx4 v108, s[64:67], s68 offen lds\n\t"
      "s_mov_b64 exec, -1\n\t"
      "v_and_b32 v49, s84, v48\n\t"
      "v_lshlrev_b32 v48, 16, v48\n\t"
      "v_and_b32 v51, s84, v50\n\t"
      "v_lshlrev_b32 v50, 16, v50\n\t"
      "v_and_b32 v53, s84, v52\n\t"
      "v_lshlrev_b32 v52, 16, v52\n\t"
      "v_and_b32 v55, s84, v54\n\t"
      "v_lshlrev_b32 v54, 16, v54\n\t"
      "v_pk_mul_f32 v[48:49], v[48:49], v[80:81] op_sel_hi:[1,0]\n\t"
      "v_pk_mul_f32 v[50:51], v[50:51], v[80:81] op_sel_hi:[1,0]\n\t"
      "v_pk_mul_f32 v[52:53], v[52:53], v[80:81] op_sel_hi:[1,0]\n\t"
      "v_pk_mul_f32 v[54:55], v[54:55], v[80:81] op_sel_hi:[1,0]\n\t"
      "v_cvt_pk_bf16_f32 v72, v48, v49\n\t"
      "v_cvt_pk_bf16_f32 v73, v50, v51\n\t"
      "v_cvt_pk_bf16_f32 v74, v52, v53\n\t"
      "v_cvt_pk_bf16_f32 v75, v54, v55\n\t"
      "s_mov_b64 exec, s[88:89]\n\t"
      "s_add_u32 m0, s76, 1024\n\t"
      "s_add_u32 s68, s77, 32768\n\t"
      "s_add_u32 s76, s76, 8192\n\t"
      "s_add_u32 s77, s77, 0x40000\n\t"
      "buffer_load_dwordx4 v108, s[64:67], s68 offen lds\n\t"
      "s_mov_b64 exec, -1\n\t"
      "v_and_b32 v57, s84, v56\n\t"
      "v_lshlrev_b32 v56, 16, v56\n\t"
      "v_and_b32 v59, s84, v58\n\t"
      "v_lshlrev_b32 v58, 16, v58\n\t"
      "v_and_b32 v61, s84, v60\n\t"
      "v_lshlrev_b32 v60, 16, v60\n\t"
      "v_and_b32 v63, s84, v62\n\t"
      "v_lshlrev_b32 v62, 16, v62\n\t"
      "v_pk_mul_f32 v[56:57], v[56:57], v[80:81] op_sel_hi:[1,0]\n\t"
      "v_pk_mul_f32 v[58:59], v[58:59], v[80:81] op_sel_hi:[1,0]\n\t"
      "v_pk_mul_f32 v[60:61], v[60:61], v[80:81] op_sel_hi:[1,0]\n\t"
      "v_pk_mul_f32 v[62:63], v[62:63], v[80:81] op_sel_hi:[1,0]\n\t"
      "v_cvt_pk_bf16_f32 v76, v56, v57\n\t"
      "v_cvt_pk_bf16_f32 v77, v58, v59\n\t"
      "v_cvt_pk_bf16_f32 v78, v60, v61\n\t"
      "v_cvt_pk_bf16_f32 v79, v62, v63\n\t"
      "s_cmp_lg_u32 s69, 0\n\t"
      "s_cbranch_scc1 GLN1%=\n\t"
      "buffer_store_dwordx4 v[64:67], v106, s[60:63], s74 offen sc1\n\t"
      "s_add_u32 s74, s74, 4096\n\t"
      "buffer_store_dwordx4 v[68:71], v106, s[60:63], s74 offen sc1\n\t"
      "s_add_u32 s74, s74, 4096\n\t"
      "buffer_store_dwordx4 v[72:75], v106, s[60:63], s74 offen sc1\n\t"
      "s_add_u32 s74, s74, 4096\n\t"
      "buffer_store_dwordx4 v[76:79], v106, s[60:63], s74 offen sc1\n\t"
      "s_add_u32 s74, s74, 4096\n\t"
      "s_sub_u32 s76, s76, 49152\n\t"
      "s_sub_u32 s77, s77, 1572864\n\t"
      "s_mov_b64 s[88:89], -1\n\t"
      "v_readfirstlane_b32 %3, v107\n\t"
      "v_mov_b64 v[64:65], v[128:129]\n\t"
      "v_mov_b64 v[66:67], v[130:131]\n\t"
      "v_mov_b64 v[68:69], v[132:133]\n\t"
      "v_mov_b64 v[70:71], v[134:135]\n\t"
      "v_mov_b64 v[72:73], v[136:137]\n\t"
      "v_mov_b64 v[74:75], v[138:139]\n\t"
      "v_mov_b64 v[76:77], v[140:141]\n\t"
      "v_mov_b64 v[78:79], v[142:143]\n\t"
      "s_mul_i32 %3, %3, 0x9e3779b1\n\t"
      "s_add_u32 %3, %3, 0x7f4a7c15\n\t"
      "s_mov_b32 s69, 1\n\t"
      "s_branch GLN%=\n\t"
      "GLN1%=:\n\t"
      "ds_write_b128 v110, v[64:67]\n\t"
      "ds_write_b128 v110, v[68:71] offset:4096\n\t"
      "ds_write_b128 v110, v[72:75] offset:8192\n\t"
      "ds_write_b128 v110, v[76:79] offset:12288\n\t"
      "s_cmp_eq_u32 s69, 2\n\t"
      "s_cbranch_scc1 GLN2%=\n\t"
      "v_mov_b32 v107, %3\n\t"
      "v_add_u32 v110, 0x4000, v110\n\t"
      "s_lshl_b32 s79, %0, 7\n\t"
      "s_add_u32 s79, s79, 37120\n\t"
      "s_waitcnt vmcnt(12) lgkmcnt(0)\n\t"
      "s_barrier\n\t"
      "s_mov_b64 exec, s[90:91]\n\t"
      "buffer_store_dword v107, off, s[60:63], s79 sc1\n\t"
      "s_mov_b64 exec, -1\n\t"
      "v_mov_b64 v[64:65], v[144:145]\n\t"
      "v_mov_b64 v[66:67], v[146:147]\n\t"
      "v_mov_b64 v[68:69], v[148:149]\n\t"
      "v_mov_b64 v[70:71], v[150:151]\n\t"
      "v_mov_b64 v[72:73], v[152:153]\n\t"
      "v_mov_b64 v[74:75], v[154:155]\n\t"
      "v_mov_b64 v[76:77], v[156:157]\n\t"
      "v_mov_b64 v[78:79], v[158:159]\n\t"
      "s_mov_b32 s69, 2\n\t"
      "s_branch GLN%=\n\t"
      "GLN2%=:\n\t"
      "s_waitcnt lgkmcnt(10)\n\t"
      "ds_read_b64_tr_b16 a[0:1], v109\n\t"
      "ds_read_b64_tr_b16 a[2:3], v109 offset:1024\n\t"
      "ds_read_b64_tr_b16 a[4:5], v109 offset:8\n\t"
      "ds_read_b64_tr_b16 a[6:7], v109 offset:1032\n\t"
      "s_waitcnt lgkmcnt(10)\n\t"
      "ds_read_b64_tr_b16 a[8:9], v109 offset:8192\n\t"
      "ds_read_b64_tr_b16 a[10:11], v109 offset:9216\n\t"
      "ds_read_b64_tr_b16 a[12:13], v109 offset:8200\n\t"
      "ds_read_b64_tr_b16 a[14:15], v109 offset:9224\n\t"
      "s_waitcnt lgkmcnt(10)\n\t"
      "ds_read_b64_tr_b16 a[16:17], v109 offset:16384\n\t"
      "ds_read_b64_tr_b16 a[18:19], v109 offset:17408\n\t"
      "ds_read_b64_tr_b16 a[20:21], v109 offset:16392\n\t"
      "ds_read_b64_tr_b16 a[22:23], v109 offset:17416\n\t"
      "s_waitcnt lgkmcnt(10)\n\t"
      "ds_read_b64_tr_b16 a[24:25], v109 offset:24576\n\t"
      "ds_read_b64_tr_b16 a[26:27], v109 offset:25600\n\t"
      "ds_read_b64_tr_b16 a[28:29], v109 offset:24584\n\t"
      "ds_read_b64_tr_b16 a[30:31], v109 offset:25608\n\t"
      "v_lshlrev_b32 v106, 8, v96\n\t"
      "s_mov_b32 s79, 37120\n\t"
      "s_mov_b32 s81, 0x40000\n\t"
      "v_add_u32 v98, 131072, v97\n\t"
      "v_readlane_b32 s73, v102, 0\n\t"
      "v_readlane_b32 s75, v103, 0\n\t"
      "s_mov_b64 s[64:65], s[40:41]\n\t"
      "s_lshl_b32 s74, %1, 10\n\t"
      "v_readlane_b32 s72, v101, 1\n\t"
      "s_add_u32 s76, s85, 0\n\t"
      "s_waitcnt vmcnt(12)\n\t"
      "s_barrier\n\t"
      "s_waitcnt lgkmcnt(10)\n\t"
      "ds_read_b64_tr_b16 a[32:33], v109 offset:32768\n\t"
      "ds_read_b64_tr_b16 a[34:35], v109 offset:33792\n\t"
      "ds_read_b64_tr_b16 a[36:37], v109 offset:32776\n\t"
      "ds_read_b64_tr_b16 a[38:39], v109 offset:33800\n\t"
      "s_waitcnt lgkmcnt(10)\n\t"
      "ds_read_b64_tr_b16 a[40:41], v109 offset:40960\n\t"
      "ds_read_b64_tr_b16 a[42:43], v109 offset:41984\n\t"
      "ds_read_b64_tr_b16 a[44:45], v109 offset:40968\n\t"
      "ds_read_b64_tr_b16 a[46:47], v109 offset:41992\n\t"
      "s_waitcnt lgkmcnt(10)\n\t"
      "ds_read_b64_tr_b16 a[48:49], v109 offset:49152\n\t"
      "ds_read_b64_tr_b16 a[50:51], v109 offset:50176\n\t"
      "ds_read_b64_tr_b16 a[52:53], v109 offset:49160\n\t"
      "ds_read_b64_tr_b16 a[54:55], v109 offset:50184\n\t"
      "s_waitcnt lgkmcnt(10)\n\t"
      "ds_read_b64_tr_b16 a[56:57], v109 offset:57344\n\t"
      "ds_read_b64_tr_b16 a[58:59], v109 offset:58368\n\t"
      "ds_read_b64_tr_b16 a[60:61], v109 offset:57352\n\t"
      "ds_read_b64_tr_b16 a[62:63], v109 offset:58376\n\t"
      "v_add_u32 v109, 0x10000, v109\n\t"
      "s_waitcnt lgkmcnt(10)\n\t"
      "ds_read_b64_tr_b16 a[64:65], v109\n\t"
      "ds_read_b64_tr_b16 a[66:67], v109 offset:1024\n\t"
      "ds_read_b64_tr_b16 a[68:69], v109 offset:8\n\t"
      "ds_read_b64_tr_b16 a[70:71], v109 offset:1032\n\t"
      "s_waitcnt lgkmcnt(10)\n\t"
      "ds_read_b64_tr_b16 a[72:73], v109 offset:8192\n\t"
      "ds_read_b64_tr_b16 a[74:75], v109 offset:9216\n\t"
      "ds_read_b64_tr_b16 a[76:77], v109 offset:8200\n\t"
      "ds_read_b64_tr_b16 a[78:79], v109 offset:9224\n\t"
      "s_waitcnt vmcnt(0)\n\t"
      "s_barrier\n\t"
      "buffer_load_dword v128, v106, s[60:63], s79 offen sc1\n\t"
      "buffer_load_dword v129, v106, s[60:63], s79 offen offset:128 sc1\n\t"
      "s_waitcnt lgkmcnt(10)\n\t"
      "ds_read_b64_tr_b16 a[80:81], v109 offset:16384\n\t"
      "ds_read_b64_tr_b16 a[82:83], v109 offset:17408\n\t"
      "ds_read_b64_tr_b16 a[84:85], v109 offset:16392\n\t"
      "ds_read_b64_tr_b16 a[86:87], v109 offset:17416\n\t"
      "s_waitcnt lgkmcnt(10)\n\t"
      "ds_read_b64_tr_b16 a[88:89], v109 offset:24576\n\t"
      "ds_read_b64_tr_b16 a[90:91], v109 offset:25600\n\t"
      "ds_read_b64_tr_b16 a[92:93], v109 offset:24584\n\t"
      "ds_read_b64_tr_b16 a[94:95], v109 offset:25608\n\t"
      "s_waitcnt lgkmcnt(10)\n\t"
      "ds_read_b64_tr_b16 a[96:97], v109 offset:32768\n\t"
      "ds_read_b64_tr_b16 a[98:99], v109 offset:33792\n\t"
      "ds_read_b64_tr_b16 a[100:101], v109 offset:32776\n\t"
      "ds_read_b64_tr_b16 a[102:103], v109 offset:33800\n\t"
      "s_waitcnt lgkmcnt(10)\n\t"
      "ds_read_b64_tr_b16 a[104:105], v109 offset:40960\n\t"
      "ds_read_b64_tr_b16 a[106:107], v109 offset:41984\n\t"
      "ds_read_b64_tr_b16 a[108:109], v109 offset:40968\n\t"
      "ds_read_b64_tr_b16 a[110:111], v109 offset:41992\n\t"
      "s_waitcnt lgkmcnt(10)\n\t"
      "ds_read_b64_tr_b16 a[112:113], v109 offset:49152\n\t"
      "ds_read_b64_tr_b16 a[114:115], v109 offset:50176\n\t"
      "ds_read_b64_tr_b16 a[116:117], v109 offset:49160\n\t"
      "ds_read_b64_tr_b16 a[118:119], v109 offset:50184\n\t"
      "s_waitcnt lgkmcnt(10)\n\t"
      "ds_read_b64_tr_b16 a[120:121], v109 offset:57344\n\t"
      "ds_read_b64_tr_b16 a[122:123], v109 offset:58368\n\t"
      "ds_read_b64_tr_b16 a[124:125], v109 offset:57352\n\t"
      "ds_read_b64_tr_b16 a[126:127], v109 offset:58376\n\t"
      "s_mov_b64 s[88:89], 0\n\t"
      "s_mov_b64 s[38:39], 0\n\t"
      "s_waitcnt lgkmcnt(4)\n\t"
      "GKV%=:\n\t"
      "ds_read_b128 v[0:3], v98\n\t"
      "ds_read_b128 v[4:7], v98 offset:1024\n\t"
      "ds_read_b128 v[8:11], v98 offset:2048\n\t"
      "ds_read_b128 v[12:15], v98 offset:3072\n\t"
      "ds_read_b128 v[16:19], v98 offset:4096\n\t"
      "ds_read_b128 v[20:23], v98 offset:5120\n\t"
      "ds_read_b128 v[24:27], v98 offset:6144\n\t"
      "ds_read_b128 v[28:31], v98 offset:7168\n\t"
      "ds_read_b128 v[32:35], v98 offset:8192\n\t"
      "ds_read_b128 v[36:39], v98 offset:9216\n\t"
      "s_waitcnt lgkmcnt(9)\n\t"
      "v_mfma_f32_16x16x32_bf16 v[64:67], a[0:3], v[0:3], 0\n\t"
      "ds_read_b128 v[40:43], v98 offset:10240\n\t"
      "v_mfma_f32_16x16x32_bf16 v[68:71], a[4:7], v[0:3], 0\n\t"
      "s_waitcnt lgkmcnt(9)\n\t"
      "v_mfma_f32_16x16x32_bf16 v[64:67], a[8:11], v[4:7], v[64:67]\n\t"
      "ds_read_b128 v[44:47], v98 offset:11264\n\t"
      "v_mfma_f32_16x16x32_bf16 v[68:71], a[12:15], v[4:7], v[68:71]\n\t"
      "s_waitcnt lgkmcnt(9)\n\t"
      "v_mfma_f32_16x16x32_bf16 v[64:67], a[16:19], v[8:11], v[64:67]\n\t"
      "ds_read_b128 v[48:51], v98 offset:12288\n\t"
      "v_mfma_f32_16x16x32_bf16 v[68:71], a[20:23], v[8:11], v[68:71]\n\t"
      "s_waitcnt lgkmcnt(9)\n\t"
      "v_mfma_f32_16x16x32_bf16 v[64:67], a[24:27], v[12:15], v[64:67]\n\t"
      "ds_read_b128 v[52:55], v98 offset:13312\n\t"
      "v_mfma_f32_16x16x32_bf16 v[68:71], a[28:31], v[12:15], v[68:71]\n\t"
      "s_waitcnt lgkmcnt(9)\n\t"
      "v_mfma_f32_16x16x32_bf16 v[64:67], a[32:35], v[16:19], v[64:67]\n\t"
      "ds_read_b128 v[56:59], v98 offset:14336\n\t"
      "v_mfma_f32_16x16x32_bf16 v[68:71], a[36:39], v[16:19], v[68:71]\n\t"
      "s_waitcnt lgkmcnt(9)\n\t"
      "v_mfma_f32_16x16x32_bf16 v[64:67], a[40:43], v[20:23], v[64:67]\n\t"
      "ds_read_b128 v[60:63], v98 offset:15360\n\t"
      "v_mfma_f32_16x16x32_bf16 v[68:71], a[44:47], v[20:23], v[68:71]\n\t"
      "s_waitcnt lgkmcnt(9)\n\t"
      "v_mfma_f32_16x16x32_bf16 v[64:67], a[48:51], v[24:27], v[64:67]\n\t"
      "ds_read_b128 v[0:3], v98 offset:16384\n\t"
      "v_mfma_f32_16x16x32_bf16 v[68:71], a[52:55], v[24:27], v[68:71]\n\t"
      "s_waitcnt lgkmcnt(9)\n\t"
      "v_mfma_f32_16x16x32_bf16 v[64:67], a[56:59], v[28:31], v[64:67]\n\t"
      "ds_read_b128 v[4:7], v98 offset:17408\n\t"
      "v_mfma_f32_16x16x32_bf16 v[68:71], a[60:63], v[28:31], v[68:71]\n\t"
      "s_waitcnt lgkmcnt(9)\n\t"
      "v_mfma_f32_16x16x32_bf16 v[64:67], a[64:67], v[32:35], v[64:67]\n\t"
      "ds_read_b128 v[8:11], v98 offset:18432\n\t"
      "v_mfma_f32_16x16x32_bf16 v[68:71], a[68:71], v[32:35], v[68:71]\n\t"
      "s_waitcnt lgkmcnt(9)\n\t"
      "v_mfma_f32_16x16x32_bf16 v[64:67], a[72:75], v[36:39], v[64:67]\n\t"
      "ds_read_b128 v[12:15], v98 offset:19456\n\t"
      "v_mfma_f32_16x16x32_bf16 v[68:71], a[76:79], v[36:39], v[68:71]\n\t"
      "s_waitcnt lgkmcnt(9)\n\t"
      "v_mfma_f32_16x16x32_bf16 v[64:67], a[80:83], v[40:43], v[64:67]\n\t"
      "ds_read_b128 v[16:19], v98 offset:20480\n\t"
      "v_mfma_f32_16x16x32_bf16 v[68:71], a[84:87], v[40:43], v[68:71]\n\t"
      "s_waitcnt lgkmcnt(9)\n\t"
      "v_mfma_f32_16x16x32_bf16 v[64:67], a[88:91], v[44:47], v[64:67]\n\t"
      "ds_read_b128 v[20:23], v98 offset:21504\n\t"
      "v_mfma_f32_16x16x32_bf16 v[68:71], a[92:95], v[44:47], v[68:71]\n\t"
      "s_waitcnt lgkmcnt(9)\n\t"
      "v_mfma_f32_16x16x32_bf16 v[64:67], a[96:99], v[48:51], v[64:67]\n\t"
      "ds_read_b128 v[24:27], v98 offset:22528\n\t"
      "v_mfma_f32_16x16x32_bf16 v[68:71], a[100:103], v[48:51], v[68:71]\n\t"
      "s_cmp_lg_u64 s[88:89], 0\n\t"
      "s_barrier\n\t"
      "s_cbranch_scc1 GKV0%=\n\t"
      "s_waitcnt vmcnt(0)\n\t"
      "v_bitop3_b32 v128, v128, v129, %3 bitop3:0x7e\n\t"
      "v_cmp_ne_u32 vcc, 0, v128\n\t"
      "s_nop 0\n\t"
      "s_cmp_eq_u64 vcc, 0\n\t"
      "s_cselect_b64 s[36:37], -1, 0\n\t"
      "GKV0%=:\n\t"
      "s_waitcnt lgkmcnt(9)\n\t"
      "v_mfma_f32_16x16x32_bf16 v[64:67], a[104:107], v[52:55], v[64:67]\n\t"
      "ds_read_b128 v[28:31], v98 offset:23552\n\t"
      "v_mfma_f32_16x16x32_bf16 v[68:71], a[108:111], v[52:55], v[68:71]\n\t"
      "s_add_u32 m0, s76, 0\n\t"
      "s_mov_b64 exec, s[36:37]\n\t"
      "buffer_load_dwordx4 v97, s[60:63], s72 offen sc1 lds\n\t"
      "s_mov_b64 exec, -1\n\t"
      "s_waitcnt lgkmcnt(9)\n\t"
      "v_mfma_f32_16x16x32_bf16 v[64:67], a[112:115], v[56:59], v[64:67]\n\t"
      "ds_read_b128 v[32:35], v98 offset:24576\n\t"
      "v_mfma_f32_16x16x32_bf16 v[68:71], a[116:119], v[56:59], v[68:71]\n\t"
      "s_waitcnt lgkmcnt(9)\n\t"
      "v_mfma_f32_16x16x32_bf16 v[64:67], a[120:123], v[60:63], v[64:67]\n\t"
      "ds_read_b128 v[36:39], v98 offset:25600\n\t"
      "v_mfma_f32_16x16x32_bf16 v[68:71], a[124:127], v[60:63], v[68:71]\n\t"
      "s_add_u32 m0, s76, 0\n\t"
      "s_mov_b64 exec, s[36:37]\n\t"
      "buffer_load_dwordx4 v97, s[60:63], s72 offen offset:1024 sc1 lds\n\t"
      "s_mov_b64 exec, -1\n\t"
      "s_waitcnt lgkmcnt(9)\n\t"
      "v_mfma_f32_16x16x32_bf16 v[72:75], a[0:3], v[0:3], 0\n\t"
      "ds_read_b128 v[40:43], v98 offset:26624\n\t"
      "v_mfma_f32_16x16x32_bf16 v[76:79], a[4:7], v[0:3], 0\n\t"
      "s_waitcnt lgkmcnt(9)\n\t"
      "v_mfma_f32_16x16x32_bf16 v[72:75], a[8:11], v[4:7], v[72:75]\n\t"
      "ds_read_b128 v[44:47], v98 offset:27648\n\t"
      "v_mfma_f32_16x16x32_bf16 v[76:79], a[12:15], v[4:7], v[76:79]\n\t"
      "s_add_u32 m0, s76, 0\n\t"
      "s_mov_b64 exec, s[36:37]\n\t"
      "buffer_load_dwordx4 v97, s[60:63], s72 offen offset:2048 sc1 lds\n\t"
      "s_mov_b64 exec, -1\n\t"
      "s_waitcnt vmcnt(3)\n\t"
      "s_barrier\n\t"
      "s_mov_b64 exec, s[38:39]\n\t"
      "buffer_store_dword v107, off, s[60:63], s75 sc1\n\t"
      "s_mov_b64 exec, -1\n\t"
      "s_waitcnt lgkmcnt(9)\n\t"
      "v_mfma_f32_16x16x32_bf16 v[72:75], a[16:19], v[8:11], v[72:75]\n\t"
      "ds_read_b128 v[48:51], v98 offset:28672\n\t"
      "v_mfma_f32_16x16x32_bf16 v[76:79], a[20:23], v[8:11], v[76:79]\n\t"
      "s_add_u32 m0, s74, 65536\n\t"
      "s_mov_b32 s78, 0\n\t"
      "s_mov_b64 exec, s[88:89]\n\t"
      "buffer_load_dwordx4 v116, s[64:67], s78 offen lds\n\t"
      "s_mov_b64 exec, -1\n\t"
      "s_waitcnt lgkmcnt(9)\n\t"
      "v_mfma_f32_16x16x32_bf16 v[72:75], a[24:27], v[12:15], v[72:75]\n\t"
      "ds_read_b128 v[52:55], v98 offset:29696\n\t"
      "v_mfma_f32_16x16x32_bf16 v[76:79], a[28:31], v[12:15], v[76:79]\n\t"
      "s_add_u32 m0, s76, 0\n\t"
      "s_mov_b64 exec, s[36:37]\n\t"
      "buffer_load_dwordx4 v97, s[60:63], s72 offen offset:3072 sc1 lds\n\t"
      "s_mov_b64 exec, -1\n\t"
      "s_waitcnt lgkmcnt(9)\n\t"
      "v_mfma_f32_16x16x32_bf16 v[72:75], a[32:35], v[16:19], v[72:75]\n\t"
      "ds_read_b128 v[56:59], v98 offset:30720\n\t"
      "v_mfma_f32_16x16x32_bf16 v[76:79], a[36:39], v[16:19], v[76:79]\n\t"
      "s_add_u32 m0, s74, 69632\n\t"
      "s_mov_b32 s78, 262144\n\t"
      "s_mov_b64 exec, s[88:89]\n\t"
      "buffer_load_dwordx4 v116, s[64:67], s78 offen lds\n\t"
      "s_mov_b64 exec, -1\n\t"
      "s_waitcnt lgkmcnt(9)\n\t"
      "v_mfma_f32_16x16x32_bf16 v[72:75], a[40:43], v[20:23], v[72:75]\n\t"
      "ds_read_b128 v[60:63], v98 offset:31744\n\t"
      "v_mfma_f32_16x16x32_bf16 v[76:79], a[44:47], v[20:23], v[76:79]\n\t"
      "s_add_u32 m0, s76, 4096\n\t"
      "s_add_u32 s72, s72, 4096\n\t"
      "s_mov_b64 exec, s[36:37]\n\t"
      "buffer_load_dwordx4 v97, s[60:63], s72 offen sc1 lds\n\t"
      "s_mov_b64 exec, -1\n\t"
      "v_cvt_pk_bf16_f32 v80, v64, v65\n\t"
      "v_cvt_pk_bf16_f32 v81, v66, v67\n\t"
      "v_cvt_pk_bf16_f32 v82, v68, v69\n\t"
      "v_cvt_pk_bf16_f32 v83, v70, v71\n\t"
      "buffer_store_dwordx4 v[80:83], v97, s[60:63], s73 offen sc1\n\t"
      "s_waitcnt lgkmcnt(9)\n\t"
      "v_mfma_f32_16x16x32_bf16 v[72:75], a[48:51], v[24:27], v[72:75]\n\t"
      "v_mfma_f32_16x16x32_bf16 v[76:79], a[52:55], v[24:27], v[76:79]\n\t"
      "s_add_u32 m0, s74, 73728\n\t"
      "s_mov_b32 s78, 524288\n\t"
      "s_mov_b64 exec, s[88:89]\n\t"
      "buffer_load_dwordx4 v116, s[64:67], s78 offen lds\n\t"
      "s_mov_b64 exec, -1\n\t"
      "s_waitcnt lgkmcnt(8)\n\t"
      "v_mfma_f32_16x16x32_bf16 v[72:75], a[56:59], v[28:31], v[72:75]\n\t"
      "v_mfma_f32_16x16x32_bf16 v[76:79], a[60:63], v[28:31], v[76:79]\n\t"
      "s_add_u32 m0, s76, 4096\n\t"
      "s_mov_b64 exec, s[36:37]\n\t"
      "buffer_load_dwordx4 v97, s[60:63], s72 offen offset:1024 sc1 lds\n\t"
      "s_mov_b64 exec, -1\n\t"
      "s_waitcnt lgkmcnt(7)\n\t"
      "v_mfma_f32_16x16x32_bf16 v[72:75], a[64:67], v[32:35], v[72:75]\n\t"
      "v_mfma_f32_16x16x32_bf16 v[76:79], a[68:71], v[32:35], v[76:79]\n\t"
      "s_add_u32 m0, s74, 77824\n\t"
      "s_mov_b32 s78, 786432\n\t"
      "s_mov_b64 exec, s[88:89]\n\t"
      "buffer_load_dwordx4 v116, s[64:67], s78 offen lds\n\t"
      "s_mov_b64 exec, -1\n\t"
      "s_waitcnt lgkmcnt(6)\n\t"
      "v_mfma_f32_16x16x32_bf16 v[72:75], a[72:75], v[36:39], v[72:75]\n\t"
      "v_mfma_f32_16x16x32_bf16 v[76:79], a[76:79], v[36:39], v[76:79]\n\t"
      "s_add_u32 m0, s76, 4096\n\t"
      "s_mov_b64 exec, s[36:37]\n\t"
      "buffer_load_dwordx4 v97, s[60:63], s72 offen offset:2048 sc1 lds\n\t"
      "s_mov_b64 exec, -1\n\t"
      "s_waitcnt lgkmcnt(5)\n\t"
      "v_mfma_f32_16x16x32_bf16 v[72:75], a[80:83], v[40:43], v[72:75]\n\t"
      "v_mfma_f32_16x16x32_bf16 v[76:79], a[84:87], v[40:43], v[76:79]\n\t"
      "s_add_u32 m0, s74, 81920\n\t"
      "s_mov_b32 s78, 1048576\n\t"
      "s_mov_b64 exec, s[88:89]\n\t"
      "buffer_load_dwordx4 v116, s[64:67], s78 offen lds\n\t"
      "s_mov_b64 exec, -1\n\t"
      "s_waitcnt lgkmcnt(4)\n\t"
      "v_mfma_f32_16x16x32_bf16 v[72:75], a[88:91], v[44:47], v[72:75]\n\t"
      "v_mfma_f32_16x16x32_bf16 v[76:79], a[92:95], v[44:47], v[76:79]\n\t"
      "s_add_u32 m0, s76, 4096\n\t"
      "s_mov_b64 exec, s[36:37]\n\t"
      "buffer_load_dwordx4 v97, s[60:63], s72 offen offset:3072 sc1 lds\n\t"
      "s_mov_b64 exec, -1\n\t"
      "s_waitcnt lgkmcnt(3)\n\t"
      "v_mfma_f32_16x16x32_bf16 v[72:75], a[96:99], v[48:51], v[72:75]\n\t"
      "v_mfma_f32_16x16x32_bf16 v[76:79], a[100:103], v[48:51], v[76:79]\n\t"
      "s_add_u32 m0, s74, 86016\n\t"
      "s_mov_b32 s78, 1310720\n\t"
      "s_mov_b64 exec, s[88:89]\n\t"
      "buffer_load_dwordx4 v116, s[64:67], s78 offen lds\n\t"
      "s_mov_b64 exec, -1\n\t"
      "s_waitcnt lgkmcnt(2)\n\t"
      "v_mfma_f32_16x16x32_bf16 v[72:75], a[104:107], v[52:55], v[72:75]\n\t"
      "v_mfma_f32_16x16x32_bf16 v[76:79], a[108:111], v[52:55], v[76:79]\n\t"
      "s_waitcnt lgkmcnt(1)\n\t"
      "v_mfma_f32_16x16x32_bf16 v[72:75], a[112:115], v[56:59], v[72:75]\n\t"
      "v_mfma_f32_16x16x32_bf16 v[76:79], a[116:119], v[56:59], v[76:79]\n\t"
      "s_add_u32 m0, s74, 90112\n\t"
      "s_mov_b32 s78, 1572864\n\t"
      "s_mov_b64 exec, s[88:89]\n\t"
      "buffer_load_dwordx4 v116, s[64:67], s78 offen lds\n\t"
      "s_mov_b64 exec, -1\n\t"
      "s_waitcnt lgkmcnt(0)\n\t"
      "v_mfma_f32_16x16x32_bf16 v[72:75], a[120:123], v[60:63], v[72:75]\n\t"
      "v_mfma_f32_16x16x32_bf16 v[76:79], a[124:127], v[60:63], v[76:79]\n\t"
      "s_nop 6\n\t"
      "v_cvt_pk_bf16_f32 v84, v72, v73\n\t"
      "v_cvt_pk_bf16_f32 v85, v74, v75\n\t"
      "v_cvt_pk_bf16_f32 v86, v76, v77\n\t"
      "v_cvt_pk_bf16_f32 v87, v78, v79\n\t"
      "buffer_store_dwordx4 v[84:87], v97, s[60:63], s73 offen offset:1024 sc1\n\t"
      "s_cmp_lg_u64 s[88:89], 0\n\t"
      "s_cbranch_scc1 GKV1%=\n\t"
      "s_cmp_lg_u64 s[36:37], 0\n\t"
      "s_cbranch_scc1 GL2%=\n\t"
      "GL0%=:\n\t"
      "buffer_load_dword v128, v106, s[60:63], s79 offen sc1\n\t"
      "buffer_load_dword v129, v106, s[60:63], s79 offen offset:128 sc1\n\t"
      "s_sub_u32 s81, s81, 1\n\t"
      "s_waitcnt vmcnt(0)\n\t"
      "v_bitop3_b32 v128, v128, v129, %3 bitop3:0x7e\n\t"
      "v_cmp_ne_u32 vcc, 0, v128\n\t"
      "s_cmp_eq_u32 s81, 0\n\t"
      "s_cbranch_scc1 GL1%=\n\t"
      "s_cbranch_vccnz GL0%=\n\t"
      "GL1%=:\n\t"
      "v_readlane_b32 s72, v101, 1\n\t"
      "s_add_u32 s76, s85, 0\n\t"
      "s_add_u32 m0, s76, 0\n\t"
      "s_nop 2\n\t"
      "buffer_load_dwordx4 v97, s[60:63], s72 offen sc1 lds\n\t"
      "buffer_load_dwordx4 v97, s[60:63], s72 offen offset:1024 sc1 lds\n\t"
      "buffer_load_dwordx4 v97, s[60:63], s72 offen offset:2048 sc1 lds\n\t"
      "buffer_load_dwordx4 v97, s[60:63], s72 offen offset:3072 sc1 lds\n\t"
      "s_add_u32 m0, s76, 4096\n\t"
      "s_add_u32 s72, s72, 4096\n\t"
      "buffer_load_dwordx4 v97, s[60:63], s72 offen sc1 lds\n\t"
      "buffer_load_dwordx4 v97, s[60:63], s72 offen offset:1024 sc1 lds\n\t"
      "buffer_load_dwordx4 v97, s[60:63], s72 offen offset:2048 sc1 lds\n\t"
      "buffer_load_dwordx4 v97, s[60:63], s72 offen offset:3072 sc1 lds\n\t"
      "s_waitcnt vmcnt(0)\n\t"
      "GL2%=:\n\t"
      "s_mov_b32 s77, s73\n\t"
      "v_readlane_b32 s73, v102, 1\n\t"
      "v_mov_b32 v98, v97\n\t"
      "s_mov_b64 s[88:89], -1\n\t"
      "s_mov_b64 s[38:39], s[90:91]\n\t"
      "s_mov_b64 s[36:37], -1\n\t"
      "v_readlane_b32 s72, v101, 2\n\t"
      "s_add_u32 s76, s85, 32768\n\t"
      "s_waitcnt vmcnt(1)\n\t"
      "s_barrier\n\t"
      "s_branch GKV%=\n\t"
      "GKV1%=:\n\t"
      "v_readlane_b32 s75, v103, 1\n\t"
      "v_add_u32 v117, 0x10000, v117\n\t"
      "v_add_u32 v98, 131072, v97\n\t"
      "s_mov_b32 s68, 0\n\t"
      "s_add_u32 m0, s74, 94208\n\t"
      "s_mov_b32 s78, 1835008\n\t"
      "buffer_load_dwordx4 v116, s[64:67], s78 offen lds\n\t"
      "s_add_u32 m0, s74, 98304\n\t"
      "s_mov_b32 s78, 2097152\n\t"
      "buffer_load_dwordx4 v116, s[64:67], s78 offen lds\n\t"
      "s_add_u32 m0, s74, 102400\n\t"
      "s_mov_b32 s78, 2359296\n\t"
      "buffer_load_dwordx4 v116, s[64:67], s78 offen lds\n\t"
      "s_add_u32 m0, s74, 106496\n\t"
      "s_mov_b32 s78, 2621440\n\t"
      "buffer_load_dwordx4 v116, s[64:67], s78 offen lds\n\t"
      "s_add_u32 m0, s74, 110592\n\t"
      "s_mov_b32 s78, 2883584\n\t"
      "buffer_load_dwordx4 v116, s[64:67], s78 offen lds\n\t"
      "s_add_u32 m0, s74, 114688\n\t"
      "s_mov_b32 s78, 3145728\n\t"
      "buffer_load_dwordx4 v116, s[64:67], s78 offen lds\n\t"
      "s_add_u32 m0, s74, 118784\n\t"
      "s_mov_b32 s78, 3407872\n\t"
      "buffer_load_dwordx4 v116, s[64:67], s78 offen lds\n\t"
      "s_add_u32 m0, s74, 122880\n\t"
      "s_mov_b32 s78, 3670016\n\t"
      "buffer_load_dwordx4 v116, s[64:67], s78 offen lds\n\t"
      "s_add_u32 m0, s74, 126976\n\t"
      "s_mov_b32 s78, 3932160\n\t"
      "buffer_load_dwordx4 v116, s[64:67], s78 offen lds\n\t"
      "s_waitcnt vmcnt(9)\n\t"
      "s_barrier\n\t"
      "s_mov_b64 exec, s[90:91]\n\t"
      "buffer_store_dword v107, off, s[60:63], s75 sc1\n\t"
      "s_mov_b64 exec, -1\n\t"
      "s_waitcnt vmcnt(0)\n\t"
      "s_barrier\n\t"
      "s_waitcnt lgkmcnt(12)\n\t"
      "ds_read_b64_tr_b16 a[128:129], v117\n\t"
      "ds_read_b64_tr_b16 a[130:131], v117 offset:512\n\t"
      "s_waitcnt lgkmcnt(12)\n\t"
      "ds_read_b64_tr_b16 a[132:133], v117 offset:4096\n\t"
      "ds_read_b64_tr_b16 a[134:135], v117 offset:4608\n\t"
      "s_waitcnt lgkmcnt(12)\n\t"
      "ds_read_b64_tr_b16 a[136:137], v117 offset:8192\n\t"
      "ds_read_b64_tr_b16 a[138:139], v117 offset:8704\n\t"
      "s_waitcnt lgkmcnt(12)\n\t"
      "ds_read_b64_tr_b16 a[140:141], v117 offset:12288\n\t"
      "ds_read_b64_tr_b16 a[142:143], v117 offset:12800\n\t"
      "s_waitcnt lgkmcnt(12)\n\t"
      "ds_read_b64_tr_b16 a[144:145], v117 offset:16384\n\t"
      "ds_read_b64_tr_b16 a[146:147], v117 offset:16896\n\t"
      "s_waitcnt lgkmcnt(12)\n\t"
      "ds_read_b64_tr_b16 a[148:149], v117 offset:20480\n\t"
      "ds_read_b64_tr_b16 a[150:151], v117 offset:20992\n\t"
      "s_waitcnt lgkmcnt(12)\n\t"
      "ds_read_b64_tr_b16 a[152:153], v117 offset:24576\n\t"
      "ds_read_b64_tr_b16 a[154:155], v117 offset:25088\n\t"
      "s_waitcnt lgkmcnt(12)\n\t"
      "ds_read_b64_tr_b16 a[156:157], v117 offset:28672\n\t"
      "ds_read_b64_tr_b16 a[158:159], v117 offset:29184\n\t"
      "s_waitcnt lgkmcnt(12)\n\t"
      "ds_read_b64_tr_b16 a[160:161], v117 offset:32768\n\t"
      "ds_read_b64_tr_b16 a[162:163], v117 offset:33280\n\t"
      "s_waitcnt lgkmcnt(12)\n\t"
      "ds_read_b64_tr_b16 a[164:165], v117 offset:36864\n\t"
      "ds_read_b64_tr_b16 a[166:167], v117 offset:37376\n\t"
      "s_waitcnt lgkmcnt(12)\n\t"
      "ds_read_b64_tr_b16 a[168:169], v117 offset:40960\n\t"
      "ds_read_b64_tr_b16 a[170:171], v117 offset:41472\n\t"
      "s_waitcnt lgkmcnt(12)\n\t"
      "ds_read_b64_tr_b16 a[172:173], v117 offset:45056\n\t"
      "ds_read_b64_tr_b16 a[174:175], v117 offset:45568\n\t"
      "s_waitcnt lgkmcnt(12)\n\t"
      "ds_read_b64_tr_b16 a[176:177], v117 offset:49152\n\t"
      "ds_read_b64_tr_b16 a[178:179], v117 offset:49664\n\t"
      "s_waitcnt lgkmcnt(12)\n\t"
      "ds_read_b64_tr_b16 a[180:181], v117 offset:53248\n\t"
      "ds_read_b64_tr_b16 a[182:183], v117 offset:53760\n\t"
      "s_waitcnt lgkmcnt(12)\n\t"
      "ds_read_b64_tr_b16 a[184:185], v117 offset:57344\n\t"
      "ds_read_b64_tr_b16 a[186:187], v117 offset:57856\n\t"
      "s_waitcnt lgkmcnt(12)\n\t"
      "ds_read_b64_tr_b16 a[188:189], v117 offset:61440\n\t"
      "ds_read_b64_tr_b16 a[190:191], v117 offset:61952\n\t"
      "s_waitcnt lgkmcnt(4)\n\t"
      "GQ%=:\n\t"
      "ds_read_b128 v[0:3], v98\n\t"
      "ds_read_b128 v[4:7], v98 offset:1024\n\t"
      "ds_read_b128 v[8:11], v98 offset:2048\n\t"
      "ds_read_b128 v[12:15], v98 offset:3072\n\t"
      "ds_read_b128 v[16:19], v98 offset:4096\n\t"
      "ds_read_b128 v[20:23], v98 offset:5120\n\t"
      "ds_read_b128 v[24:27], v98 offset:6144\n\t"
      "ds_read_b128 v[28:31], v98 offset:7168\n\t"
      "ds_read_b128 v[32:35], v98 offset:8192\n\t"
      "ds_read_b128 v[36:39], v98 offset:9216\n\t"
      "s_waitcnt lgkmcnt(9)\n\t"
      "v_mfma_f32_16x16x32_bf16 v[88:91], a[128:131], v[0:3], 0\n\t"
      "ds_read_b128 v[40:43], v98 offset:10240\n\t"
      "s_waitcnt lgkmcnt(9)\n\t"
      "v_mfma_f32_16x16x32_bf16 v[88:91], a[132:135], v[4:7], v[88:91]\n\t"
      "ds_read_b128 v[44:47], v98 offset:11264\n\t"
      "s_waitcnt lgkmcnt(9)\n\t"
      "v_mfma_f32_16x16x32_bf16 v[88:91], a[136:139], v[8:11], v[88:91]\n\t"
      "ds_read_b128 v[48:51], v98 offset:12288\n\t"
      "s_waitcnt lgkmcnt(9)\n\t"
      "v_mfma_f32_16x16x32_bf16 v[88:91], a[140:143], v[12:15], v[88:91]\n\t"
      "ds_read_b128 v[52:55], v98 offset:13312\n\t"
      "s_waitcnt lgkmcnt(9)\n\t"
      "v_mfma_f32_16x16x32_bf16 v[88:91], a[144:147], v[16:19], v[88:91]\n\t"
      "ds_read_b128 v[56:59], v98 offset:14336\n\t"
      "s_waitcnt lgkmcnt(9)\n\t"
      "v_mfma_f32_16x16x32_bf16 v[88:91], a[148:151], v[20:23], v[88:91]\n\t"
      "ds_read_b128 v[60:63], v98 offset:15360\n\t"
      "s_waitcnt lgkmcnt(9)\n\t"
      "v_mfma_f32_16x16x32_bf16 v[88:91], a[152:155], v[24:27], v[88:91]\n\t"
      "ds_read_b128 v[0:3], v98 offset:16384\n\t"
      "s_waitcnt lgkmcnt(9)\n\t"
      "v_mfma_f32_16x16x32_bf16 v[88:91], a[156:159], v[28:31], v[88:91]\n\t"
      "ds_read_b128 v[4:7], v98 offset:17408\n\t"
      "s_waitcnt lgkmcnt(9)\n\t"
      "v_mfma_f32_16x16x32_bf16 v[88:91], a[160:163], v[32:35], v[88:91]\n\t"
      "ds_read_b128 v[8:11], v98 offset:18432\n\t"
      "s_waitcnt lgkmcnt(9)\n\t"
      "v_mfma_f32_16x16x32_bf16 v[88:91], a[164:167], v[36:39], v[88:91]\n\t"
      "ds_read_b128 v[12:15], v98 offset:19456\n\t"
      "s_waitcnt lgkmcnt(9)\n\t"
      "v_mfma_f32_16x16x32_bf16 v[88:91], a[168:171], v[40:43], v[88:91]\n\t"
      "ds_read_b128 v[16:19], v98 offset:20480\n\t"
      "s_waitcnt lgkmcnt(9)\n\t"
      "v_mfma_f32_16x16x32_bf16 v[88:91], a[172:175], v[44:47], v[88:91]\n\t"
      "ds_read_b128 v[20:23], v98 offset:21504\n\t"
      "s_waitcnt lgkmcnt(9)\n\t"
      "v_mfma_f32_16x16x32_bf16 v[88:91], a[176:179], v[48:51], v[88:91]\n\t"
      "ds_read_b128 v[24:27], v98 offset:22528\n\t"
      "s_waitcnt lgkmcnt(9)\n\t"
      "v_mfma_f32_16x16x32_bf16 v[88:91], a[180:183], v[52:55], v[88:91]\n\t"
      "ds_read_b128 v[28:31], v98 offset:23552\n\t"
      "s_waitcnt lgkmcnt(9)\n\t"
      "v_mfma_f32_16x16x32_bf16 v[88:91], a[184:187], v[56:59], v[88:91]\n\t"
      "ds_read_b128 v[32:35], v98 offset:24576\n\t"
      "s_waitcnt lgkmcnt(9)\n\t"
      "v_mfma_f32_16x16x32_bf16 v[88:91], a[188:191], v[60:63], v[88:91]\n\t"
      "ds_read_b128 v[36:39], v98 offset:25600\n\t"
      "s_waitcnt lgkmcnt(9)\n\t"
      "v_mfma_f32_16x16x32_bf16 v[92:95], a[128:131], v[0:3], 0\n\t"
      "ds_read_b128 v[40:43], v98 offset:26624\n\t"
      "s_waitcnt lgkmcnt(9)\n\t"
      "v_mfma_f32_16x16x32_bf16 v[92:95], a[132:135], v[4:7], v[92:95]\n\t"
      "ds_read_b128 v[44:47], v98 offset:27648\n\t"
      "s_waitcnt lgkmcnt(9)\n\t"
      "v_mfma_f32_16x16x32_bf16 v[92:95], a[136:139], v[8:11], v[92:95]\n\t"
      "ds_read_b128 v[48:51], v98 offset:28672\n\t"
      "s_waitcnt lgkmcnt(9)\n\t"
      "v_mfma_f32_16x16x32_bf16 v[92:95], a[140:143], v[12:15], v[92:95]\n\t"
      "ds_read_b128 v[52:55], v98 offset:29696\n\t"
      "s_waitcnt lgkmcnt(9)\n\t"
      "v_mfma_f32_16x16x32_bf16 v[92:95], a[144:147], v[16:19], v[92:95]\n\t"
      "ds_read_b128 v[56:59], v98 offset:30720\n\t"
      "s_waitcnt lgkmcnt(9)\n\t"
      "v_mfma_f32_16x16x32_bf16 v[92:95], a[148:151], v[20:23], v[92:95]\n\t"
      "ds_read_b128 v[60:63], v98 offset:31744\n\t"
      "s_waitcnt lgkmcnt(9)\n\t"
      "v_mfma_f32_16x16x32_bf16 v[92:95], a[152:155], v[24:27], v[92:95]\n\t"
      "s_waitcnt lgkmcnt(8)\n\t"
      "v_mfma_f32_16x16x32_bf16 v[92:95], a[156:159], v[28:31], v[92:95]\n\t"
      "s_waitcnt lgkmcnt(7)\n\t"
      "v_mfma_f32_16x16x32_bf16 v[92:95], a[160:163], v[32:35], v[92:95]\n\t"
      "s_waitcnt lgkmcnt(6)\n\t"
      "v_mfma_f32_16x16x32_bf16 v[92:95], a[164:167], v[36:39], v[92:95]\n\t"
      "s_waitcnt lgkmcnt(5)\n\t"
      "v_mfma_f32_16x16x32_bf16 v[92:95], a[168:171], v[40:43], v[92:95]\n\t"
      "s_waitcnt lgkmcnt(4)\n\t"
      "v_mfma_f32_16x16x32_bf16 v[92:95], a[172:175], v[44:47], v[92:95]\n\t"
      "s_waitcnt lgkmcnt(3)\n\t"
      "v_mfma_f32_16x16x32_bf16 v[92:95], a[176:179], v[48:51], v[92:95]\n\t"
      "s_waitcnt lgkmcnt(2)\n\t"
      "v_mfma_f32_16x16x32_bf16 v[92:95], a[180:183], v[52:55], v[92:95]\n\t"
      "s_waitcnt lgkmcnt(1)\n\t"
      "v_mfma_f32_16x16x32_bf16 v[92:95], a[184:187], v[56:59], v[92:95]\n\t"
      "s_waitcnt lgkmcnt(0)\n\t"
      "v_mfma_f32_16x16x32_bf16 v[92:95], a[188:191], v[60:63], v[92:95]\n\t"
      "v_cvt_pk_bf16_f32 v112, v88, v89\n\t"
      "v_cvt_pk_bf16_f32 v113, v90, v91\n\t"
      "s_cmp_lg_u32 s68, 0\n\t"
      "s_barrier\n\t"
      "s_cbranch_scc1 GQ1%=\n\t"
      "s_nop 3\n\t"
      "v_cvt_pk_bf16_f32 v114, v92, v93\n\t"
      "v_cvt_pk_bf16_f32 v115, v94, v95\n\t"
      "s_nop 0\n\t"
      "v_permlane16_swap_b32 v112, v114\n\t"
      "v_permlane16_swap_b32 v113, v115\n\t"
      "buffer_store_dwordx4 v[112:115], v111, s[60:63], s77 offen sc1\n\t"
      "v_readlane_b32 s72, v101, 3\n\t"
      "s_add_u32 s76, s85, 65536\n\t"
      "s_add_u32 m0, s76, 0\n\t"
      "s_nop 2\n\t"
      "buffer_load_dwordx4 v97, s[60:63], s72 offen sc1 lds\n\t"
      "buffer_load_dwordx4 v97, s[60:63], s72 offen offset:1024 sc1 lds\n\t"
      "buffer_load_dwordx4 v97, s[60:63], s72 offen offset:2048 sc1 lds\n\t"
      "buffer_load_dwordx4 v97, s[60:63], s72 offen offset:3072 sc1 lds\n\t"
      "s_add_u32 m0, s76, 4096\n\t"
      "s_add_u32 s72, s72, 4096\n\t"
      "buffer_load_dwordx4 v97, s[60:63], s72 offen sc1 lds\n\t"
      "buffer_load_dwordx4 v97, s[60:63], s72 offen offset:1024 sc1 lds\n\t"
      "buffer_load_dwordx4 v97, s[60:63], s72 offen offset:2048 sc1 lds\n\t"
      "buffer_load_dwordx4 v97, s[60:63], s72 offen offset:3072 sc1 lds\n\t"
      "v_readlane_b32 s72, v101, 4\n\t"
      "s_add_u32 s76, s85, 98304\n\t"
      "s_add_u32 m0, s76, 0\n\t"
      "s_nop 2\n\t"
      "buffer_load_dwordx4 v97, s[60:63], s72 offen sc1 lds\n\t"
      "buffer_load_dwordx4 v97, s[60:63], s72 offen offset:1024 sc1 lds\n\t"
      "buffer_load_dwordx4 v97, s[60:63], s72 offen offset:2048 sc1 lds\n\t"
      "buffer_load_dwordx4 v97, s[60:63], s72 offen offset:3072 sc1 lds\n\t"
      "s_add_u32 m0, s76, 4096\n\t"
      "s_add_u32 s72, s72, 4096\n\t"
      "buffer_load_dwordx4 v97, s[60:63], s72 offen sc1 lds\n\t"
      "buffer_load_dwordx4 v97, s[60:63], s72 offen offset:1024 sc1 lds\n\t"
      "buffer_load_dwordx4 v97, s[60:63], s72 offen offset:2048 sc1 lds\n\t"
      "buffer_load_dwordx4 v97, s[60:63], s72 offen offset:3072 sc1 lds\n\t"
      "v_mov_b32 v98, v97\n\t"
      "s_mov_b32 s68, 2\n\t"
      "s_branch GQ%=\n\t"
      "GQ1%=:\n\t"
      "s_mov_b32 s69, 5\n\t"
      "s_mov_b32 s70, 63\n\t"
      "s_waitcnt vmcnt(8)\n\t"
      "s_mov_b32 s77, 0x8000\n\t"
      "s_mov_b32 s71, 3\n\t"
      "s_getpc_b64 s[88:89]\n\t"
      "GPC%=:\n\t"
      "s_add_u32 s88, s88, GJOBS%=-GPC%=\n\t"
      "s_addc_u32 s89, s89, 0\n\t"
      "v_add_u32 v98, s77, v97\n\t"
      "s_mov_b32 s76, s85\n\t"
      "ds_read_b128 v[0:3], v98\n\t"
      "ds_read_b128 v[4:7], v98 offset:1024\n\t"
      "ds_read_b128 v[8:11], v98 offset:2048\n\t"
      "ds_read_b128 v[12:15], v98 offset:3072\n\t"
      "ds_read_b128 v[16:19], v98 offset:4096\n\t"
      "ds_read_b128 v[20:23], v98 offset:5120\n\t"
      "ds_read_b128 v[24:27], v98 offset:6144\n\t"
      "ds_read_b128 v[28:31], v98 offset:7168\n\t"
      "ds_read_b128 v[32:35], v98 offset:8192\n\t"
      "ds_read_b128 v[36:39], v98 offset:9216\n\t"
      "GU%=:\n\t"
      "s_cmp_eq_u32 s68, s71\n\t"
      "s_cbranch_scc1 GCALL%=\n\t"
      "GBACK%=:\n\t"
      "s_waitcnt lgkmcnt(9)\n\t"
      "v_mfma_f32_16x16x32_bf16 v[64:67], a[0:3], v[0:3], 0\n\t"
      "ds_read_b128 v[40:43], v98 offset:10240\n\t"
      "v_mfma_f32_16x16x32_bf16 v[68:71], a[4:7], v[0:3], 0\n\t"
      "v_readlane_b32 s72, v101, s69\n\t"
      "v_mfma_f32_16x16x32_bf16 v[88:91], a[128:131], v[0:3], 0\n\t"
      "s_waitcnt lgkmcnt(9)\n\t"
      "v_mfma_f32_16x16x32_bf16 v[64:67], a[8:11], v[4:7], v[64:67]\n\t"
      "ds_read_b128 v[44:47], v98 offset:11264\n\t"
      "v_mfma_f32_16x16x32_bf16 v[68:71], a[12:15], v[4:7], v[68:71]\n\t"
      "v_readlane_b32 s75, v103, s70\n\t"
      "v_mfma_f32_16x16x32_bf16 v[88:91], a[132:135], v[4:7], v[88:91]\n\t"
      "s_waitcnt lgkmcnt(9)\n\t"
      "v_mfma_f32_16x16x32_bf16 v[64:67], a[16:19], v[8:11], v[64:67]\n\t"
      "ds_read_b128 v[48:51], v98 offset:12288\n\t"
      "v_mfma_f32_16x16x32_bf16 v[68:71], a[20:23], v[8:11], v[68:71]\n\t"
      "v_cvt_pk_bf16_f32 v84, v72, v73\n\t"
      "v_cvt_pk_bf16_f32 v85, v74, v75\n\t"
      "v_mfma_f32_16x16x32_bf16 v[88:91], a[136:139], v[8:11], v[88:91]\n\t"
      "v_cvt_pk_bf16_f32 v114, v92, v93\n\t"
      "v_cvt_pk_bf16_f32 v115, v94, v95\n\t"
      "s_waitcnt lgkmcnt(9)\n\t"
      "v_mfma_f32_16x16x32_bf16 v[64:67], a[24:27], v[12:15], v[64:67]\n\t"
      "ds_read_b128 v[52:55], v98 offset:13312\n\t"
      "v_mfma_f32_16x16x32_bf16 v[68:71], a[28:31], v[12:15], v[68:71]\n\t"
      "v_cvt_pk_bf16_f32 v86, v76, v77\n\t"
      "v_cvt_pk_bf16_f32 v87, v78, v79\n\t"
      "v_mfma_f32_16x16x32_bf16 v[88:91], a[140:143], v[12:15], v[88:91]\n\t"
      "v_permlane16_swap_b32 v112, v114\n\t"
      "v_permlane16_swap_b32 v113, v115\n\t"
      "s_waitcnt lgkmcnt(9)\n\t"
      "v_mfma_f32_16x16x32_bf16 v[64:67], a[32:35], v[16:19], v[64:67]\n\t"
      "ds_read_b128 v[56:59], v98 offset:14336\n\t"
      "v_mfma_f32_16x16x32_bf16 v[68:71], a[36:39], v[16:19], v[68:71]\n\t"
      "buffer_store_dwordx4 v[84:87], v97, s[60:63], s73 offen offset:1024 sc1\n\t"
      "buffer_store_dwordx4 v[112:115], v111, s[60:63], s73 offen sc1\n\t"
      "v_mfma_f32_16x16x32_bf16 v[88:91], a[144:147], v[16:19], v[88:91]\n\t"
      "s_add_u32 m0, s76, 0\n\t"
      "buffer_load_dwordx4 v97, s[60:63], s72 offen sc1 lds\n\t"
      "s_waitcnt lgkmcnt(9)\n\t"
      "v_mfma_f32_16x16x32_bf16 v[64:67], a[40:43], v[20:23], v[64:67]\n\t"
      "ds_read_b128 v[60:63], v98 offset:15360\n\t"
      "v_mfma_f32_16x16x32_bf16 v[68:71], a[44:47], v[20:23], v[68:71]\n\t"
      "v_mfma_f32_16x16x32_bf16 v[88:91], a[148:151], v[20:23], v[88:91]\n\t"
      "s_waitcnt lgkmcnt(9)\n\t"
      "v_mfma_f32_16x16x32_bf16 v[64:67], a[48:51], v[24:27], v[64:67]\n\t"
      "ds_read_b128 v[0:3], v98 offset:16384\n\t"
      "v_mfma_f32_16x16x32_bf16 v[68:71], a[52:55], v[24:27], v[68:71]\n\t"
      "v_mfma_f32_16x16x32_bf16 v[88:91], a[152:155], v[24:27], v[88:91]\n\t"
      "s_waitcnt lgkmcnt(9)\n\t"
      "v_mfma_f32_16x16x32_bf16 v[64:67], a[56:59], v[28:31], v[64:67]\n\t"
      "ds_read_b128 v[4:7], v98 offset:17408\n\t"
      "v_mfma_f32_16x16x32_bf16 v[68:71], a[60:63], v[28:31], v[68:71]\n\t"
      "buffer_load_dwordx4 v97, s[60:63], s72 offen offset:1024 sc1 lds\n\t"
      "v_mfma_f32_16x16x32_bf16 v[88:91], a[156:159], v[28:31], v[88:91]\n\t"
      "s_waitcnt lgkmcnt(9)\n\t"
      "v_mfma_f32_16x16x32_bf16 v[64:67], a[64:67], v[32:35], v[64:67]\n\t"
      "ds_read_b128 v[8:11], v98 offset:18432\n\t"
      "v_mfma_f32_16x16x32_bf16 v[68:71], a[68:71], v[32:35], v[68:71]\n\t"
      "v_mfma_f32_16x16x32_bf16 v[88:91], a[160:163], v[32:35], v[88:91]\n\t"
      "s_waitcnt lgkmcnt(9)\n\t"
      "v_mfma_f32_16x16x32_bf16 v[64:67], a[72:75], v[36:39], v[64:67]\n\t"
      "ds_read_b128 v[12:15], v98 offset:19456\n\t"
      "v_mfma_f32_16x16x32_bf16 v[68:71], a[76:79], v[36:39], v[68:71]\n\t"
      "v_readlane_b32 s73, v102, s68\n\t"
      "v_mfma_f32_16x16x32_bf16 v[88:91], a[164:167], v[36:39], v[88:91]\n\t"
      "s_waitcnt lgkmcnt(9)\n\t"
      "v_mfma_f32_16x16x32_bf16 v[64:67], a[80:83], v[40:43], v[64:67]\n\t"
      "ds_read_b128 v[16:19], v98 offset:20480\n\t"
      "v_mfma_f32_16x16x32_bf16 v[68:71], a[84:87], v[40:43], v[68:71]\n\t"
      "buffer_load_dwordx4 v97, s[60:63], s72 offen offset:2048 sc1 lds\n\t"
      "v_mfma_f32_16x16x32_bf16 v[88:91], a[168:171], v[40:43], v[88:91]\n\t"
      "s_waitcnt lgkmcnt(9)\n\t"
      "v_mfma_f32_16x16x32_bf16 v[64:67], a[88:91], v[44:47], v[64:67]\n\t"
      "ds_read_b128 v[20:23], v98 offset:21504\n\t"
      "v_mfma_f32_16x16x32_bf16 v[68:71], a[92:95], v[44:47], v[68:71]\n\t"
      "v_mfma_f32_16x16x32_bf16 v[88:91], a[172:175], v[44:47], v[88:91]\n\t"
      "s_waitcnt lgkmcnt(9)\n\t"
      "v_mfma_f32_16x16x32_bf16 v[64:67], a[96:99], v[48:51], v[64:67]\n\t"
      "ds_read_b128 v[24:27], v98 offset:22528\n\t"
      "v_mfma_f32_16x16x32_bf16 v[68:71], a[100:103], v[48:51], v[68:71]\n\t"
      "v_mfma_f32_16x16x32_bf16 v[88:91], a[176:179], v[48:51], v[88:91]\n\t"
      "s_waitcnt lgkmcnt(9)\n\t"
      "v_mfma_f32_16x16x32_bf16 v[64:67], a[104:107], v[52:55], v[64:67]\n\t"
      "ds_read_b128 v[28:31], v98 offset:23552\n\t"
      "v_mfma_f32_16x16x32_bf16 v[68:71], a[108:111], v[52:55], v[68:71]\n\t"
      "buffer_load_dwordx4 v97, s[60:63], s72 offen offset:3072 sc1 lds\n\t"
      "v_mfma_f32_16x16x32_bf16 v[88:91], a[180:183], v[52:55], v[88:91]\n\t"
      "s_waitcnt lgkmcnt(9)\n\t"
      "v_mfma_f32_16x16x32_bf16 v[64:67], a[112:115], v[56:59], v[64:67]\n\t"
      "ds_read_b128 v[32:35], v98 offset:24576\n\t"
      "v_mfma_f32_16x16x32_bf16 v[68:71], a[116:119], v[56:59], v[68:71]\n\t"
      "v_mfma_f32_16x16x32_bf16 v[88:91], a[184:187], v[56:59], v[88:91]\n\t"
      "s_waitcnt lgkmcnt(9)\n\t"
      "v_mfma_f32_16x16x32_bf16 v[64:67], a[120:123], v[60:63], v[64:67]\n\t"
      "ds_read_b128 v[36:39], v98 offset:25600\n\t"
      "v_mfma_f32_16x16x32_bf16 v[68:71], a[124:127], v[60:63], v[68:71]\n\t"
      "s_add_u32 s68, s68, 1\n\t"
      "v_mfma_f32_16x16x32_bf16 v[88:91], a[188:191], v[60:63], v[88:91]\n\t"
      "s_waitcnt lgkmcnt(9)\n\t"
      "v_mfma_f32_16x16x32_bf16 v[72:75], a[0:3], v[0:3], 0\n\t"
      "ds_read_b128 v[40:43], v98 offset:26624\n\t"
      "v_mfma_f32_16x16x32_bf16 v[76:79], a[4:7], v[0:3], 0\n\t"
      "s_add_u32 m0, s76, 4096\n\t"
      "s_add_u32 s72, s72, 4096\n\t"
      "buffer_load_dwordx4 v97, s[60:63], s72 offen sc1 lds\n\t"
      "v_mfma_f32_16x16x32_bf16 v[92:95], a[128:131], v[0:3], 0\n\t"
      "s_add_u32 s69, s69, 1\n\t"
      "s_add_u32 s70, s70, 1\n\t"
      "s_waitcnt lgkmcnt(9)\n\t"
      "v_mfma_f32_16x16x32_bf16 v[72:75], a[8:11], v[4:7], v[72:75]\n\t"
      "ds_read_b128 v[44:47], v98 offset:27648\n\t"
      "v_mfma_f32_16x16x32_bf16 v[76:79], a[12:15], v[4:7], v[76:79]\n\t"
      "s_and_b32 s70, s70, 63\n\t"
      "s_add_u32 s76, s77, s85\n\t"
      "v_mfma_f32_16x16x32_bf16 v[92:95], a[132:135], v[4:7], v[92:95]\n\t"
      "s_add_u32 s77, s77, 0x8000\n\t"
      "s_and_b32 s77, s77, 0x1ffff\n\t"
      "s_waitcnt lgkmcnt(9)\n\t"
      "v_mfma_f32_16x16x32_bf16 v[72:75], a[16:19], v[8:11], v[72:75]\n\t"
      "ds_read_b128 v[48:51], v98 offset:28672\n\t"
      "v_mfma_f32_16x16x32_bf16 v[76:79], a[20:23], v[8:11], v[76:79]\n\t"
      "v_cvt_pk_bf16_f32 v80, v64, v65\n\t"
      "v_cvt_pk_bf16_f32 v81, v66, v67\n\t"
      "v_mfma_f32_16x16x32_bf16 v[92:95], a[136:139], v[8:11], v[92:95]\n\t"
      "v_cvt_pk_bf16_f32 v112, v88, v89\n\t"
      "s_waitcnt lgkmcnt(9)\n\t"
      "v_mfma_f32_16x16x32_bf16 v[72:75], a[24:27], v[12:15], v[72:75]\n\t"
      "ds_read_b128 v[52:55], v98 offset:29696\n\t"
      "v_mfma_f32_16x16x32_bf16 v[76:79], a[28:31], v[12:15], v[76:79]\n\t"
      "buffer_load_dwordx4 v97, s[60:63], s72 offen offset:1024 sc1 lds\n\t"
      "v_cvt_pk_bf16_f32 v82, v68, v69\n\t"
      "v_mfma_f32_16x16x32_bf16 v[92:95], a[140:143], v[12:15], v[92:95]\n\t"
      "v_cvt_pk_bf16_f32 v83, v70, v71\n\t"
      "v_cvt_pk_bf16_f32 v113, v90, v91\n\t"
      "s_waitcnt lgkmcnt(9)\n\t"
      "v_mfma_f32_16x16x32_bf16 v[72:75], a[32:35], v[16:19], v[72:75]\n\t"
      "ds_read_b128 v[56:59], v98 offset:30720\n\t"
      "v_mfma_f32_16x16x32_bf16 v[76:79], a[36:39], v[16:19], v[76:79]\n\t"
      "buffer_store_dwordx4 v[80:83], v97, s[60:63], s73 offen sc1\n\t"
      "v_mfma_f32_16x16x32_bf16 v[92:95], a[144:147], v[16:19], v[92:95]\n\t"
      "s_waitcnt lgkmcnt(9)\n\t"
      "v_mfma_f32_16x16x32_bf16 v[72:75], a[40:43], v[20:23], v[72:75]\n\t"
      "ds_read_b128 v[60:63], v98 offset:31744\n\t"
      "v_mfma_f32_16x16x32_bf16 v[76:79], a[44:47], v[20:23], v[76:79]\n\t"
      "v_mfma_f32_16x16x32_bf16 v[92:95], a[148:151], v[20:23], v[92:95]\n\t"
      "s_waitcnt lgkmcnt(9)\n\t"
      "v_mfma_f32_16x16x32_bf16 v[72:75], a[48:51], v[24:27], v[72:75]\n\t"
      "v_mfma_f32_16x16x32_bf16 v[76:79], a[52:55], v[24:27], v[76:79]\n\t"
      "buffer_load_dwordx4 v97, s[60:63], s72 offen offset:2048 sc1 lds\n\t"
      "v_add_u32 v98, s77, v97\n\t"
      "v_mfma_f32_16x16x32_bf16 v[92:95], a[152:155], v[24:27], v[92:95]\n\t"
      "s_cmp_lg_u32 s75, 0\n\t"
      "s_waitcnt lgkmcnt(8)\n\t"
      "v_mfma_f32_16x16x32_bf16 v[72:75], a[56:59], v[28:31], v[72:75]\n\t"
      "v_mfma_f32_16x16x32_bf16 v[76:79], a[60:63], v[28:31], v[76:79]\n\t"
      "s_cselect_b64 vcc, s[90:91], 0\n\t"
      "v_mfma_f32_16x16x32_bf16 v[92:95], a[156:159], v[28:31], v[92:95]\n\t"
      "s_waitcnt lgkmcnt(7)\n\t"
      "v_mfma_f32_16x16x32_bf16 v[72:75], a[64:67], v[32:35], v[72:75]\n\t"
      "v_mfma_f32_16x16x32_bf16 v[76:79], a[68:71], v[32:35], v[76:79]\n\t"
      "buffer_load_dwordx4 v97, s[60:63], s72 offen offset:3072 sc1 lds\n\t"
      "v_mfma_f32_16x16x32_bf16 v[92:95], a[160:163], v[32:35], v[92:95]\n\t"
      "s_waitcnt lgkmcnt(6)\n\t"
      "v_mfma_f32_16x16x32_bf16 v[72:75], a[72:75], v[36:39], v[72:75]\n\t"
      "v_mfma_f32_16x16x32_bf16 v[76:79], a[76:79], v[36:39], v[76:79]\n\t"
      "v_mfma_f32_16x16x32_bf16 v[92:95], a[164:167], v[36:39], v[92:95]\n\t"
      "s_waitcnt vmcnt(22) lgkmcnt(0)\n\t"
      "s_barrier\n\t"
      "v_mfma_f32_16x16x32_bf16 v[72:75], a[80:83], v[40:43], v[72:75]\n\t"
      "ds_read_b128 v[0:3], v98\n\t"
      "v_mfma_f32_16x16x32_bf16 v[76:79], a[84:87], v[40:43], v[76:79]\n\t"
      "ds_read_b128 v[4:7], v98 offset:1024\n\t"
      "v_mfma_f32_16x16x32_bf16 v[92:95], a[168:171], v[40:43], v[92:95]\n\t"
      "v_mfma_f32_16x16x32_bf16 v[72:75], a[88:91], v[44:47], v[72:75]\n\t"
      "ds_read_b128 v[8:11], v98 offset:2048\n\t"
      "v_mfma_f32_16x16x32_bf16 v[76:79], a[92:95], v[44:47], v[76:79]\n\t"
      "ds_read_b128 v[12:15], v98 offset:3072\n\t"
      "v_mfma_f32_16x16x32_bf16 v[92:95], a[172:175], v[44:47], v[92:95]\n\t"
      "v_mfma_f32_16x16x32_bf16 v[72:75], a[96:99], v[48:51], v[72:75]\n\t"
      "ds_read_b128 v[16:19], v98 offset:4096\n\t"
      "v_mfma_f32_16x16x32_bf16 v[76:79], a[100:103], v[48:51], v[76:79]\n\t"
      "ds_read_b128 v[20:23], v98 offset:5120\n\t"
      "v_mfma_f32_16x16x32_bf16 v[92:95], a[176:179], v[48:51], v[92:95]\n\t"
      "v_mfma_f32_16x16x32_bf16 v[72:75], a[104:107], v[52:55], v[72:75]\n\t"
      "ds_read_b128 v[24:27], v98 offset:6144\n\t"
      "v_mfma_f32_16x16x32_bf16 v[76:79], a[108:111], v[52:55], v[76:79]\n\t"
      "ds_read_b128 v[28:31], v98 offset:7168\n\t"
      "v_mfma_f32_16x16x32_bf16 v[92:95], a[180:183], v[52:55], v[92:95]\n\t"
      "v_mfma_f32_16x16x32_bf16 v[72:75], a[112:115], v[56:59], v[72:75]\n\t"
      "ds_read_b128 v[32:35], v98 offset:8192\n\t"
      "v_mfma_f32_16x16x32_bf16 v[76:79], a[116:119], v[56:59], v[76:79]\n\t"
      "ds_read_b128 v[36:39], v98 offset:9216\n\t"
      "v_mfma_f32_16x16x32_bf16 v[92:95], a[184:187], v[56:59], v[92:95]\n\t"
      "s_cbranch_vccnz GFLAG%=\n\t"
      "GFB%=:\n\t"
      "v_mfma_f32_16x16x32_bf16 v[72:75], a[120:123], v[60:63], v[72:75]\n\t"
      "v_mfma_f32_16x16x32_bf16 v[76:79], a[124:127], v[60:63], v[76:79]\n\t"
      "v_mfma_f32_16x16x32_bf16 v[92:95], a[188:191], v[60:63], v[92:95]\n\t"
      "s_branch GU%=\n\t"
      "GFLAG%=:\n\t"
      "s_mov_b64 exec, 1\n\t"
      "buffer_store_dword v107, off, s[60:63], s75 sc1\n\t"
      "s_mov_b64 exec, -1\n\t"
      "s_branch GFB%=\n\t"
      "GCALL%=:\n\t"
      "s_setpc_b64 s[88:89]\n\t"
      "GJOBS%=:\n\t"
      "s_nop 7\n\t"
      "v_cvt_pk_bf16_f32 v84, v72, v73\n\t"
      "v_cvt_pk_bf16_f32 v85, v74, v75\n\t"
      "v_cvt_pk_bf16_f32 v86, v76, v77\n\t"
      "v_cvt_pk_bf16_f32 v87, v78, v79\n\t"
      "buffer_store_dwordx4 v[84:87], v97, s[60:63], s73 offen offset:1024 sc1\n\t"
      "v_readlane_b32 s75, v103, 2\n\t"
      "s_waitcnt vmcnt(0)\n\t"
      "s_barrier\n\t"
      "s_mov_b64 exec, s[90:91]\n\t"
      "s_nop 1\n\t"
      "buffer_store_dword v107, off, s[60:63], s75 sc1\n\t"
      "s_mov_b64 exec, -1\n\t"
      "s_mov_b32 s71, 5\n\t"
      "s_getpc_b64 s[88:89]\n\t"
      "s_add_u32 s88, s88, 12\n\t"
      "s_addc_u32 s89, s89, 0\n\t"
      "s_branch GBACK%=\n\t"
      "s_nop 7\n\t"
      "v_readlane_b32 s75, v103, 3\n\t"
      "s_waitcnt vmcnt(9)\n\t"
      "s_barrier\n\t"
      "s_mov_b64 exec, s[90:91]\n\t"
      "s_nop 1\n\t"
      "buffer_store_dword v107, off, s[60:63], s75 sc1\n\t"
      "s_mov_b64 exec, -1\n\t"
      "s_mov_b32 s71, 6\n\t"
      "s_getpc_b64 s[88:89]\n\t"
      "s_add_u32 s88, s88, 12\n\t"
      "s_addc_u32 s89, s89, 0\n\t"
      "s_branch GBACK%=\n\t"
      "s_bitcmp0_b32 %0, 1\n\t"
      "s_cselect_b64 s[36:37], -1, 0\n\t"
      "s_cmp_lt_u32 s82, 28\n\t"
      "s_cselect_b64 s[38:39], -1, 0\n\t"
      "s_min_u32 s74, s82, 27\n\t"
      "v_lshrrev_b32 v104, 3, v96\n\t"
      "v_and_b32 v105, 7, v96\n\t"
      "s_mov_b32 s78, 3584\n\t"
      "v_mul_u32_u24 v104, s78, v104\n\t"
      "v_lshl_add_u32 v104, v105, 4, v104\n\t"
      "s_lshr_b32 s78, %0, 5\n\t"
      "s_lshl_b32 s78, s78, 2\n\t"
      "s_add_u32 s78, s78, %1\n\t"
      "s_mul_i32 s78, s78, 114688\n\t"
      "s_lshl_b32 s80, s74, 7\n\t"
      "s_add_u32 s80, s80, s78\n\t"
      "s_mov_b64 s[64:65], s[50:51]\n\t"
      "s_add_u32 s78, s80, 0\n\t"
      "buffer_load_dwordx4 v[128:131], v104, s[64:67], s78 offen \n\t"
      "s_add_u32 s78, s80, 28672\n\t"
      "buffer_load_dwordx4 v[132:135], v104, s[64:67], s78 offen \n\t"
      "s_add_u32 s78, s80, 57344\n\t"
      "buffer_load_dwordx4 v[136:139], v104, s[64:67], s78 offen \n\t"
      "s_add_u32 s71, s71, 1\n\t"
      "s_getpc_b64 s[88:89]\n\t"
      "s_add_u32 s88, s88, 12\n\t"
      "s_addc_u32 s89, s89, 0\n\t"
      "s_branch GBACK%=\n\t"
      "s_add_u32 s78, s80, 86016\n\t"
      "buffer_load_dwordx4 v[140:143], v104, s[64:67], s78 offen \n\t"
      "s_mov_b64 s[64:65], s[52:53]\n\t"
      "s_add_u32 s78, s80, 0\n\t"
      "buffer_load_dwordx4 v[144:147], v104, s[64:67], s78 offen \n\t"
      "s_add_u32 s78, s80, 28672\n\t"
      "buffer_load_dwordx4 v[148:151], v104, s[64:67], s78 offen \n\t"
      "s_add_u32 s71, s71, 1\n\t"
      "s_getpc_b64 s[88:89]\n\t"
      "s_add_u32 s88, s88, 12\n\t"
      "s_addc_u32 s89, s89, 0\n\t"
      "s_branch GBACK%=\n\t"
      "s_add_u32 s78, s80, 57344\n\t"
      "buffer_load_dwordx4 v[152:155], v104, s[64:67], s78 offen \n\t"
      "s_add_u32 s78, s80, 86016\n\t"
      "buffer_load_dwordx4 v[156:159], v104, s[64:67], s78 offen \n\t"
      "v_mov_b32 v120, -1\n\t"
      "v_mov_b32 v121, -1\n\t"
      "v_mov_b32 v122, -1\n\t"
      "v_mov_b32 v123, -1\n\t"
      "s_lshr_b32 s78, %0, 5\n\t"
      "s_lshl_b32 s78, s78, 2\n\t"
      "s_add_u32 s78, s78, %1\n\t"
      "s_lshr_b32 s79, s78, 1\n\t"
      "s_lshl_b32 s79, s79, 16\n\t"
      "s_lshl_b32 s80, s82, 11\n\t"
      "s_add_u32 s79, s79, s80\n\t"
      "s_and_b32 s78, s78, 1\n\t"
      "s_lshl_b32 s78, s78, 10\n\t"
      "s_add_u32 s79, s79, s78\n\t"
      "s_add_u32 s79, s79, 66813952\n\t"
      "buffer_store_dwordx4 v[120:123], v97, s[60:63], s79 offen sc1\n\t"
      "s_add_u32 s71, s71, 1\n\t"
      "s_getpc_b64 s[88:89]\n\t"
      "s_add_u32 s88, s88, 12\n\t"
      "s_addc_u32 s89, s89, 0\n\t"
      "s_branch GBACK%=\n\t"
      "s_sub_u32 s79, s79, 4194304\n\t"
      "buffer_store_dwordx4 v[120:123], v97, s[60:63], s79 offen sc1\n\t"
      "s_add_u32 s79, s79, 3670016\n\t"
      "buffer_store_dwordx4 v[120:123], v97, s[60:63], s79 offen sc1\n\t"
      "s_sub_u32 s79, s79, 4194304\n\t"
      "buffer_store_dwordx4 v[120:123], v97, s[60:63], s79 offen sc1\n\t"
      "s_add_u32 s71, s71, 1\n\t"
      "s_getpc_b64 s[88:89]\n\t"
      "s_add_u32 s88, s88, 12\n\t"
      "s_addc_u32 s89, s89, 0\n\t"
      "s_branch GBACK%=\n\t"
      "s_add_u32 s79, s79, 3670016\n\t"
      "buffer_store_dwordx4 v[120:123], v97, s[60:63], s79 offen sc1\n\t"
      "s_sub_u32 s79, s79, 4194304\n\t"
      "buffer_store_dwordx4 v[120:123], v97, s[60:63], s79 offen sc1\n\t"
      "s_mov_b64 s[64:65], s[54:55]\n\t"
      "v_lshrrev_b32 v104, 5, v96\n\t"
      "v_and_b32 v105, 31, v96\n\t"
      "v_lshlrev_b32 v104, 10, v104\n\t"
      "v_lshl_add_u32 v104, v105, 4, v104\n\t"
      "s_andn2_b32 s78, %0, 3\n\t"
      "s_add_u32 s78, s78, %1\n\t"
      "s_mul_i32 s78, s78, 14336\n\t"
      "s_and_b32 s79, %0, 1\n\t"
      "s_lshl_b32 s79, s79, 9\n\t"
      "s_add_u32 s78, s78, s79\n\t"
      "s_mov_b64 exec, s[36:37]\n\t"
      "buffer_load_dwordx4 v[160:163], v104, s[64:67], s78 offen \n\t"
      "s_mov_b64 exec, -1\n\t"
      "s_add_u32 s71, s71, 1\n\t"
      "s_getpc_b64 s[88:89]\n\t"
      "s_add_u32 s88, s88, 12\n\t"
      "s_addc_u32 s89, s89, 0\n\t"
      "s_branch GBACK%=\n\t"
      "s_mov_b64 exec, s[36:37]\n\t"
      "buffer_load_dwordx4 v[164:167], v104, s[64:67], s78 offen offset:2048 \n\t"
      "s_add_u32 s78, s78, 4096\n\t"
      "buffer_load_dwordx4 v[168:171], v104, s[64:67], s78 offen \n\t"
      "buffer_load_dwordx4 v[172:175], v104, s[64:67], s78 offen offset:2048 \n\t"
      "s_mov_b64 exec, -1\n\t"
      "s_add_u32 s71, s71, 1\n\t"
      "s_getpc_b64 s[88:89]\n\t"
      "s_add_u32 s88, s88, 12\n\t"
      "s_addc_u32 s89, s89, 0\n\t"
      "s_branch GBACK%=\n\t"
      "s_mov_b64 exec, s[36:37]\n\t"
      "s_add_u32 s78, s78, 4096\n\t"
      "buffer_load_dwordx4 v[176:179], v104, s[64:67], s78 offen \n\t"
      "buffer_load_dwordx4 v[180:183], v104, s[64:67], s78 offen offset:2048 \n\t"
      "s_add_u32 s78, s78, 4096\n\t"
      "buffer_load_dwordx4 v[184:187], v104, s[64:67], s78 offen \n\t"
      "s_mov_b64 exec, -1\n\t"
      "s_add_u32 s71, s71, 1\n\t"
      "s_getpc_b64 s[88:89]\n\t"
      "s_add_u32 s88, s88, 12\n\t"
      "s_addc_u32 s89, s89, 0\n\t"
      "s_branch GBACK%=\n\t"
      "s_mov_b64 exec, s[36:37]\n\t"
      "s_mov_b64 exec, -1\n\t"
      "v_and_b32 v106, 3, v96\n\t"
      "v_mul_u32_u24 v106, 24, v106\n\t"
      "v_lshlrev_b32 v104, 1, v100\n\t"
      "v_sub_u32 v106, v104, v106\n\t"
      "v_add_u32 v104, s76, v97\n\t"
      "v_add_u32 v105, s76, v106\n\t"
      "s_waitcnt vmcnt(30) lgkmcnt(0)\n\t"
      "ds_write_b128 v104, v[128:131]\n\t"
      "ds_write_b128 v104, v[132:135] offset:1024\n\t"
      "ds_write_b128 v104, v[136:139] offset:2048\n\t"
      "ds_write_b128 v104, v[140:143] offset:3072\n\t"
      "ds_write_b128 v104, v[144:147] offset:4096\n\t"
      "ds_write_b128 v104, v[148:151] offset:5120\n\t"
      "ds_write_b128 v104, v[152:155] offset:6144\n\t"
      "ds_write_b128 v104, v[156:159] offset:7168\n\t"
      "s_lshr_b32 s78, %0, 5\n\t"
      "s_lshl_b32 s78, s78, 2\n\t"
      "s_add_u32 s78, s78, %1\n\t"
      "s_lshl_b32 s78, s78, 10\n\t"
      "s_lshl_b32 s79, s74, 17\n\t"
      "s_add_u32 s78, s78, s79\n\t"
      "s_add_u32 s78, s78, 105086976\n\t"
      "s_waitcnt lgkmcnt(6)\n\t"
      "ds_read_b64_tr_b16 v[128:129], v105\n\t"
      "ds_read_b64_tr_b16 v[130:131], v105 offset:512\n\t"
      "ds_read_b64_tr_b16 v[132:133], v105 offset:4096\n\t"
      "ds_read_b64_tr_b16 v[134:135], v105 offset:4608\n\t"
      "ds_read_b64_tr_b16 v[136:137], v105 offset:32\n\t"
      "ds_read_b64_tr_b16 v[138:139], v105 offset:544\n\t"
      "ds_read_b64_tr_b16 v[140:141], v105 offset:4128\n\t"
      "ds_read_b64_tr_b16 v[142:143], v105 offset:4640\n\t"
      "s_waitcnt lgkmcnt(6)\n\t"
      "ds_read_b64_tr_b16 v[144:145], v105 offset:64\n\t"
      "ds_read_b64_tr_b16 v[146:147], v105 offset:576\n\t"
      "ds_read_b64_tr_b16 v[148:149], v105 offset:4160\n\t"
      "ds_read_b64_tr_b16 v[150:151], v105 offset:4672\n\t"
      "ds_read_b64_tr_b16 v[152:153], v105 offset:96\n\t"
      "ds_read_b64_tr_b16 v[154:155], v105 offset:608\n\t"
      "ds_read_b64_tr_b16 v[156:157], v105 offset:4192\n\t"
      "ds_read_b64_tr_b16 v[158:159], v105 offset:4704\n\t"
      "s_waitcnt lgkmcnt(0)\n\t"
      "s_mov_b64 exec, s[38:39]\n\t"
      "buffer_store_dwordx4 v[128:131], v97, s[60:63], s78 offen sc1\n\t"
      "s_add_u32 s78, s78, 16384\n\t"
      "buffer_store_dwordx4 v[132:135], v97, s[60:63], s78 offen sc1\n\t"
      "s_add_u32 s78, s78, 16384\n\t"
      "buffer_store_dwordx4 v[136:139], v97, s[60:63], s78 offen sc1\n\t"
      "s_mov_b64 exec, -1\n\t"
      "s_add_u32 s71, s71, 1\n\t"
      "s_getpc_b64 s[88:89]\n\t"
      "s_add_u32 s88, s88, 12\n\t"
      "s_addc_u32 s89, s89, 0\n\t"
      "s_branch GBACK%=\n\t"
      "s_mov_b64 exec, s[38:39]\n\t"
      "s_add_u32 s78, s78, 16384\n\t"
      "buffer_store_dwordx4 v[140:143], v97, s[60:63], s78 offen sc1\n\t"
      "s_add_u32 s78, s78, 16384\n\t"
      "buffer_store_dwordx4 v[144:147], v97, s[60:63], s78 offen sc1\n\t"
      "s_add_u32 s78, s78, 16384\n\t"
      "buffer_store_dwordx4 v[148:151], v97, s[60:63], s78 offen sc1\n\t"
      "s_mov_b64 exec, -1\n\t"
      "s_add_u32 s71, s71, 1\n\t"
      "s_getpc_b64 s[88:89]\n\t"
      "s_add_u32 s88, s88, 12\n\t"
      "s_addc_u32 s89, s89, 0\n\t"
      "s_branch GBACK%=\n\t"
      "s_mov_b64 exec, s[38:39]\n\t"
      "s_add_u32 s78, s78, 16384\n\t"
      "buffer_store_dwordx4 v[152:155], v97, s[60:63], s78 offen sc1\n\t"
      "s_add_u32 s78, s78, 16384\n\t"
      "buffer_store_dwordx4 v[156:159], v97, s[60:63], s78 offen sc1\n\t"
      "s_add_u32 s78, s78, 16384\n\t"
      "s_mov_b64 exec, -1\n\t"
      "v_lshlrev_b32 v104, 10, v96\n\t"
      "s_and_b32 s78, %0, 1\n\t"
      "s_lshl_b32 s78, s78, 5\n\t"
      "s_lshr_b32 s79, %0, 2\n\t"
      "s_add_u32 s78, s78, s79\n\t"
      "s_lshl_b32 s78, s78, 4\n\t"
      "s_lshl_b32 s79, %1, 16\n\t"
      "s_add_u32 s78, s78, s79\n\t"
      "s_lshl_b32 s79, %1, 10\n\t"
      "s_mov_b64 exec, s[36:37]\n\t"
      "s_add_u32 m0, s79, 131072\n\t"
      "s_nop 0\n\t"
      "buffer_load_dwordx4 v104, s[64:67], s78 offen lds\n\t"
      "s_mov_b64 exec, -1\n\t"
      "s_add_u32 s71, s71, 1\n\t"
      "s_getpc_b64 s[88:89]\n\t"
      "s_add_u32 s88, s88, 12\n\t"
      "s_addc_u32 s89, s89, 0\n\t"
      "s_branch GBACK%=\n\t"
      "s_mov_b64 exec, s[36:37]\n\t"
      "s_add_u32 s78, s78, 0x40000\n\t"
      "s_add_u32 m0, s79, 135168\n\t"
      "s_nop 0\n\t"
      "buffer_load_dwordx4 v104, s[64:67], s78 offen lds\n\t"
      "s_add_u32 s78, s78, 0x40000\n\t"
      "s_add_u32 m0, s79, 139264\n\t"
      "s_nop 0\n\t"
      "buffer_load_dwordx4 v104, s[64:67], s78 offen lds\n\t"
      "s_add_u32 s78, s78, 0x40000\n\t"
      "s_add_u32 m0, s79, 143360\n\t"
      "s_nop 0\n\t"
      "buffer_load_dwordx4 v104, s[64:67], s78 offen lds\n\t"
      "s_mov_b64 exec, -1\n\t"
      "s_add_u32 s71, s71, 1\n\t"
      "s_getpc_b64 s[88:89]\n\t"
      "s_add_u32 s88, s88, 12\n\t"
      "s_addc_u32 s89, s89, 0\n\t"
      "s_branch GBACK%=\n\t"
      "s_mov_b64 exec, s[36:37]\n\t"
      "s_add_u32 s78, s78, 0x40000\n\t"
      "s_add_u32 m0, s79, 147456\n\t"
      "s_nop 0\n\t"
      "buffer_load_dwordx4 v104, s[64:67], s78 offen lds\n\t"
      "s_add_u32 s78, s78, 0x40000\n\t"
      "s_add_u32 m0, s79, 151552\n\t"
      "s_nop 0\n\t"
      "buffer_load_dwordx4 v104, s[64:67], s78 offen lds\n\t"
      "s_add_u32 s78, s78, 0x40000\n\t"
      "s_add_u32 m0, s79, 155648\n\t"
      "s_nop 0\n\t"
      "buffer_load_dwordx4 v104, s[64:67], s78 offen lds\n\t"
      "s_mov_b64 exec, -1\n\t"
      "s_add_u32 s71, s71, 1\n\t"
      "s_getpc_b64 s[88:89]\n\t"
      "s_add_u32 s88, s88, 12\n\t"
      "s_addc_u32 s89, s89, 0\n\t"
      "s_branch GBACK%=\n\t"
      "s_mov_b64 exec, s[36:37]\n\t"
      "s_add_u32 s78, s78, 0x40000\n\t"
      "s_mov_b64 exec, -1\n\t"
      "s_mov_b64 s[64:65], s[46:47]\n\t"
      "v_lshrrev_b32 v99, 2, v96\n\t"
      "v_and_b32 v106, 3, v96\n\t"
      "v_lshlrev_b32 v99, 10, v99\n\t"
      "v_lshl_add_u32 v99, v106, 4, v99\n\t"
      "s_lshr_b32 s78, %0, 4\n\t"
      "s_lshl_b32 s78, s78, 19\n\t"
      "s_bfe_u32 s79, %0, 0x20002\n\t"
      "s_lshl_b32 s79, s79, 8\n\t"
      "s_add_u32 s78, s78, s79\n\t"
      "s_lshl_b32 s79, %1, 6\n\t"
      "s_add_u32 s78, s78, s79\n\t"
      "v_add_u32 v99, s78, v99\n\t"
      "s_mov_b32 s78, 262144\n\t"
      "buffer_load_dwordx4 v[192:195], v99, s[64:67], s78 offen \n\t"
      "s_mov_b32 s78, 278528\n\t"
      "buffer_load_dwordx4 v[196:199], v99, s[64:67], s78 offen \n\t"
      "s_mov_b32 s78, 294912\n\t"
      "buffer_load_dwordx4 v[200:203], v99, s[64:67], s78 offen \n\t"
      "s_add_u32 s71, s71, 1\n\t"
      "s_getpc_b64 s[88:89]\n\t"
      "s_add_u32 s88, s88, 12\n\t"
      "s_addc_u32 s89, s89, 0\n\t"
      "s_branch GBACK%=\n\t"
      "s_mov_b32 s78, 311296\n\t"
      "buffer_load_dwordx4 v[204:207], v99, s[64:67], s78 offen \n\t"
      "s_mov_b32 s78, 327680\n\t"
      "buffer_load_dwordx4 v[208:211], v99, s[64:67], s78 offen \n\t"
      "s_mov_b32 s78, 344064\n\t"
      "buffer_load_dwordx4 v[212:215], v99, s[64:67], s78 offen \n\t"
      "s_add_u32 s71, s71, 1\n\t"
      "s_getpc_b64 s[88:89]\n\t"
      "s_add_u32 s88, s88, 12\n\t"
      "s_addc_u32 s89, s89, 0\n\t"
      "s_branch GBACK%=\n\t"
      "s_mov_b32 s78, 360448\n\t"
      "buffer_load_dwordx4 v[216:219], v99, s[64:67], s78 offen \n\t"
      "s_mov_b32 s78, 376832\n\t"
      "buffer_load_dwordx4 v[220:223], v99, s[64:67], s78 offen \n\t"
      "s_mov_b32 s78, 393216\n\t"
      "buffer_load_dwordx4 v[224:227], v99, s[64:67], s78 offen \n\t"
      "s_add_u32 s71, s71, 1\n\t"
      "s_getpc_b64 s[88:89]\n\t"
      "s_add_u32 s88, s88, 12\n\t"
      "s_addc_u32 s89, s89, 0\n\t"
      "s_branch GBACK%=\n\t"
      "s_mov_b32 s78, 409600\n\t"
      "buffer_load_dwordx4 v[228:231], v99, s[64:67], s78 offen \n\t"
      "s_mov_b32 s78, 425984\n\t"
      "buffer_load_dwordx4 v[232:235], v99, s[64:67], s78 offen \n\t"
      "s_mov_b32 s78, 442368\n\t"
      "buffer_load_dwordx4 v[236:239], v99, s[64:67], s78 offen \n\t"
      "s_add_u32 s71, s71, 1\n\t"
      "s_getpc_b64 s[88:89]\n\t"
      "s_add_u32 s88, s88, 12\n\t"
      "s_addc_u32 s89, s89, 0\n\t"
      "s_branch GBACK%=\n\t"
      "s_mov_b32 s78, 458752\n\t"
      "buffer_load_dwordx4 v[240:243], v99, s[64:67], s78 offen \n\t"
      "s_mov_b32 s78, 475136\n\t"
      "buffer_load_dwordx4 v[244:247], v99, s[64:67], s78 offen \n\t"
      "s_mov_b32 s78, 491520\n\t"
      "buffer_load_dwordx4 v[248:251], v99, s[64:67], s78 offen \n\t"
      "s_add_u32 s71, s71, 1\n\t"
      "s_getpc_b64 s[88:89]\n\t"
      "s_add_u32 s88, s88, 12\n\t"
      "s_addc_u32 s89, s89, 0\n\t"
      "s_branch GBACK%=\n\t"
      "s_mov_b32 s78, 507904\n\t"
      "buffer_load_dwordx4 v[252:255], v99, s[64:67], s78 offen \n\t"
      "s_mov_b32 s78, 0\n\t"
      "buffer_load_dwordx4 v[128:131], v99, s[64:67], s78 offen \n\t"
      "s_mov_b32 s78, 16384\n\t"
      "buffer_load_dwordx4 v[132:135], v99, s[64:67], s78 offen \n\t"
      "s_add_u32 s71, s71, 1\n\t"
      "s_getpc_b64 s[88:89]\n\t"
      "s_add_u32 s88, s88, 12\n\t"
      "s_addc_u32 s89, s89, 0\n\t"
      "s_branch GBACK%=\n\t"
      "s_mov_b32 s78, 32768\n\t"
      "buffer_load_dwordx4 v[136:139], v99, s[64:67], s78 offen \n\t"
      "s_mov_b32 s78, 49152\n\t"
      "buffer_load_dwordx4 v[140:143], v99, s[64:67], s78 offen \n\t"
      "s_mov_b32 s78, 65536\n\t"
      "buffer_load_dwordx4 v[144:147], v99, s[64:67], s78 offen \n\t"
      "s_add_u32 s71, s71, 1\n\t"
      "s_getpc_b64 s[88:89]\n\t"
      "s_add_u32 s88, s88, 12\n\t"
      "s_addc_u32 s89, s89, 0\n\t"
      "s_branch GBACK%=\n\t"
      "s_mov_b32 s78, 81920\n\t"
      "buffer_load_dwordx4 v[148:151], v99, s[64:67], s78 offen \n\t"
      "s_mov_b32 s78, 98304\n\t"
      "buffer_load_dwordx4 v[152:155], v99, s[64:67], s78 offen \n\t"
      "s_mov_b32 s78, 114688\n\t"
      "buffer_load_dwordx4 v[156:159], v99, s[64:67], s78 offen \n\t"
      "s_add_u32 s71, s71, 1\n\t"
      "s_getpc_b64 s[88:89]\n\t"
      "s_add_u32 s88, s88, 12\n\t"
      "s_addc_u32 s89, s89, 0\n\t"
      "s_branch GBACK%=\n\t"
      "s_mov_b32 s78, 131072\n\t"
      "buffer_load_dwordx4 v[160:163], v99, s[64:67], s78 offen \n\t"
      "s_mov_b32 s78, 147456\n\t"
      "buffer_load_dwordx4 v[164:167], v99, s[64:67], s78 offen \n\t"
      "s_mov_b32 s78, 163840\n\t"
      "buffer_load_dwordx4 v[168:171], v99, s[64:67], s78 offen \n\t"
      "s_add_u32 s71, s71, 1\n\t"
      "s_getpc_b64 s[88:89]\n\t"
      "s_add_u32 s88, s88, 12\n\t"
      "s_addc_u32 s89, s89, 0\n\t"
      "s_branch GBACK%=\n\t"
      "s_mov_b32 s78, 180224\n\t"
      "buffer_load_dwordx4 v[172:175], v99, s[64:67], s78 offen \n\t"
      "s_mov_b32 s78, 196608\n\t"
      "buffer_load_dwordx4 v[176:179], v99, s[64:67], s78 offen \n\t"
      "s_mov_b32 s78, 212992\n\t"
      "buffer_load_dwordx4 v[180:183], v99, s[64:67], s78 offen \n\t"
      "s_add_u32 s71, s71, 1\n\t"
      "s_getpc_b64 s[88:89]\n\t"
      "s_add_u32 s88, s88, 12\n\t"
      "s_addc_u32 s89, s89, 0\n\t"
      "s_branch GBACK%=\n\t"
      "s_mov_b32 s78, 229376\n\t"
      "buffer_load_dwordx4 v[184:187], v99, s[64:67], s78 offen \n\t"
      "s_mov_b32 s78, 245760\n\t"
      "buffer_load_dwordx4 v[188:191], v99, s[64:67], s78 offen \n\t"
      "s_mov_b32 s71, 32\n\t"
      "s_getpc_b64 s[88:89]\n\t"
      "s_add_u32 s88, s88, 12\n\t"
      "s_addc_u32 s89, s89, 0\n\t"
      "s_branch GBACK%=\n\t"
      "s_nop 7\n\t"
      "v_add_u32 v104, s76, v97\n\t"
      "v_add_u32 v105, s76, v100\n\t"
      "s_sub_u32 s68, s68, 1\n\t"
      "s_waitcnt vmcnt(11) lgkmcnt(0)\n\t"
      "v_cvt_pk_bf16_f32 v84, v72, v73\n\t"
      "v_cvt_pk_bf16_f32 v85, v74, v75\n\t"
      "v_cvt_pk_bf16_f32 v86, v76, v77\n\t"
      "v_cvt_pk_bf16_f32 v87, v78, v79\n\t"
      "buffer_store_dwordx4 v[84:87], v97, s[60:63], s73 offen offset:1024 sc1\n\t"
      "v_cvt_pk_bf16_f32 v114, v92, v93\n\t"
      "v_cvt_pk_bf16_f32 v115, v94, v95\n\t"
      "s_nop 0\n\t"
      "v_permlane16_swap_b32 v112, v114\n\t"
      "v_permlane16_swap_b32 v113, v115\n\t"
      "buffer_store_dwordx4 v[112:115], v111, s[60:63], s73 offen sc1\n\t"
      "v_readlane_b32 s75, v103, s68\n\t"
      "s_waitcnt lgkmcnt(6)\n\t"
      "ds_write_b128 v104, v[128:131]\n\t"
      "ds_write_b128 v104, v[132:135] offset:1024\n\t"
      "ds_read_b64_tr_b16 a[0:1], v105\n\t"
      "ds_read_b64_tr_b16 a[2:3], v105 offset:256\n\t"
      "ds_read_b64_tr_b16 a[64:65], v105 offset:8\n\t"
      "ds_read_b64_tr_b16 a[66:67], v105 offset:264\n\t"
      "s_waitcnt lgkmcnt(6)\n\t"
      "ds_write_b128 v104, v[136:139] offset:2048\n\t"
      "ds_write_b128 v104, v[140:143] offset:3072\n\t"
      "ds_read_b64_tr_b16 a[4:5], v105 offset:2048\n\t"
      "ds_read_b64_tr_b16 a[6:7], v105 offset:2304\n\t"
      "ds_read_b64_tr_b16 a[68:69], v105 offset:2056\n\t"
      "ds_read_b64_tr_b16 a[70:71], v105 offset:2312\n\t"
      "s_waitcnt lgkmcnt(6)\n\t"
      "ds_write_b128 v104, v[144:147] offset:4096\n\t"
      "ds_write_b128 v104, v[148:151] offset:5120\n\t"
      "ds_read_b64_tr_b16 a[8:9], v105 offset:4096\n\t"
      "ds_read_b64_tr_b16 a[10:11], v105 offset:4352\n\t"
      "ds_read_b64_tr_b16 a[72:73], v105 offset:4104\n\t"
      "ds_read_b64_tr_b16 a[74:75], v105 offset:4360\n\t"
      "s_waitcnt lgkmcnt(6)\n\t"
      "ds_write_b128 v104, v[152:155] offset:6144\n\t"
      "ds_write_b128 v104, v[156:159] offset:7168\n\t"
      "ds_read_b64_tr_b16 a[12:13], v105 offset:6144\n\t"
      "ds_read_b64_tr_b16 a[14:15], v105 offset:6400\n\t"
      "ds_read_b64_tr_b16 a[76:77], v105 offset:6152\n\t"
      "ds_read_b64_tr_b16 a[78:79], v105 offset:6408\n\t"
      "s_waitcnt lgkmcnt(6)\n\t"
      "ds_write_b128 v104, v[160:163]\n\t"
      "ds_write_b128 v104, v[164:167] offset:1024\n\t"
      "ds_read_b64_tr_b16 a[16:17], v105\n\t"
      "ds_read_b64_tr_b16 a[18:19], v105 offset:256\n\t"
      "ds_read_b64_tr_b16 a[80:81], v105 offset:8\n\t"
      "ds_read_b64_tr_b16 a[82:83], v105 offset:264\n\t"
      "s_waitcnt lgkmcnt(6)\n\t"
      "ds_write_b128 v104, v[168:171] offset:2048\n\t"
      "ds_write_b128 v104, v[172:175] offset:3072\n\t"
      "ds_read_b64_tr_b16 a[20:21], v105 offset:2048\n\t"
      "ds_read_b64_tr_b16 a[22:23], v105 offset:2304\n\t"
      "ds_read_b64_tr_b16 a[84:85], v105 offset:2056\n\t"
      "ds_read_b64_tr_b16 a[86:87], v105 offset:2312\n\t"
      "s_waitcnt lgkmcnt(6)\n\t"
      "ds_write_b128 v104, v[176:179] offset:4096\n\t"
      "ds_write_b128 v104, v[180:183] offset:5120\n\t"
      "ds_read_b64_tr_b16 a[24:25], v105 offset:4096\n\t"
      "ds_read_b64_tr_b16 a[26:27], v105 offset:4352\n\t"
      "ds_read_b64_tr_b16 a[88:89], v105 offset:4104\n\t"
      "ds_read_b64_tr_b16 a[90:91], v105 offset:4360\n\t"
      "s_waitcnt lgkmcnt(6)\n\t"
      "ds_write_b128 v104, v[184:187] offset:6144\n\t"
      "ds_write_b128 v104, v[188:191] offset:7168\n\t"
      "ds_read_b64_tr_b16 a[28:29], v105 offset:6144\n\t"
      "ds_read_b64_tr_b16 a[30:31], v105 offset:6400\n\t"
      "ds_read_b64_tr_b16 a[92:93], v105 offset:6152\n\t"
      "ds_read_b64_tr_b16 a[94:95], v105 offset:6408\n\t"
      "s_waitcnt lgkmcnt(6)\n\t"
      "ds_write_b128 v104, v[192:195]\n\t"
      "ds_write_b128 v104, v[196:199] offset:1024\n\t"
      "ds_read_b64_tr_b16 a[32:33], v105\n\t"
      "ds_read_b64_tr_b16 a[34:35], v105 offset:256\n\t"
      "ds_read_b64_tr_b16 a[96:97], v105 offset:8\n\t"
      "ds_read_b64_tr_b16 a[98:99], v105 offset:264\n\t"
      "s_waitcnt lgkmcnt(6)\n\t"
      "ds_write_b128 v104, v[200:203] offset:2048\n\t"
      "ds_write_b128 v104, v[204:207] offset:3072\n\t"
      "ds_read_b64_tr_b16 a[36:37], v105 offset:2048\n\t"
      "ds_read_b64_tr_b16 a[38:39], v105 offset:2304\n\t"
      "ds_read_b64_tr_b16 a[100:101], v105 offset:2056\n\t"
      "ds_read_b64_tr_b16 a[102:103], v105 offset:2312\n\t"
      "s_waitcnt lgkmcnt(6)\n\t"
      "ds_write_b128 v104, v[208:211] offset:4096\n\t"
      "ds_write_b128 v104, v[212:215] offset:5120\n\t"
      "ds_read_b64_tr_b16 a[40:41], v105 offset:4096\n\t"
      "ds_read_b64_tr_b16 a[42:43], v105 offset:4352\n\t"
      "ds_read_b64_tr_b16 a[104:105], v105 offset:4104\n\t"
      "ds_read_b64_tr_b16 a[106:107], v105 offset:4360\n\t"
      "s_waitcnt lgkmcnt(6)\n\t"
      "ds_write_b128 v104, v[216:219] offset:6144\n\t"
      "ds_write_b128 v104, v[220:223] offset:7168\n\t"
      "ds_read_b64_tr_b16 a[44:45], v105 offset:6144\n\t"
      "ds_read_b64_tr_b16 a[46:47], v105 offset:6400\n\t"
      "ds_read_b64_tr_b16 a[108:109], v105 offset:6152\n\t"
      "ds_read_b64_tr_b16 a[110:111], v105 offset:6408\n\t"
      "s_waitcnt lgkmcnt(6)\n\t"
      "ds_write_b128 v104, v[224:227]\n\t"
      "ds_write_b128 v104, v[228:231] offset:1024\n\t"
      "ds_read_b64_tr_b16 a[48:49], v105\n\t"
      "ds_read_b64_tr_b16 a[50:51], v105 offset:256\n\t"
      "ds_read_b64_tr_b16 a[112:113], v105 offset:8\n\t"
      "ds_read_b64_tr_b16 a[114:115], v105 offset:264\n\t"
      "s_waitcnt lgkmcnt(6)\n\t"
      "ds_write_b128 v104, v[232:235] offset:2048\n\t"
      "ds_write_b128 v104, v[236:239] offset:3072\n\t"
      "ds_read_b64_tr_b16 a[52:53], v105 offset:2048\n\t"
      "ds_read_b64_tr_b16 a[54:55], v105 offset:2304\n\t"
      "ds_read_b64_tr_b16 a[116:117], v105 offset:2056\n\t"
      "ds_read_b64_tr_b16 a[118:119], v105 offset:2312\n\t"
      "s_waitcnt lgkmcnt(6)\n\t"
      "ds_write_b128 v104, v[240:243] offset:4096\n\t"
      "ds_write_b128 v104, v[244:247] offset:5120\n\t"
      "ds_read_b64_tr_b16 a[56:57], v105 offset:4096\n\t"
      "ds_read_b64_tr_b16 a[58:59], v105 offset:4352\n\t"
      "ds_read_b64_tr_b16 a[120:121], v105 offset:4104\n\t"
      "ds_read_b64_tr_b16 a[122:123], v105 offset:4360\n\t"
      "s_waitcnt lgkmcnt(6)\n\t"
      "ds_write_b128 v104, v[248:251] offset:6144\n\t"
      "ds_write_b128 v104, v[252:255] offset:7168\n\t"
      "ds_read_b64_tr_b16 a[60:61], v105 offset:6144\n\t"
      "ds_read_b64_tr_b16 a[62:63], v105 offset:6400\n\t"
      "ds_read_b64_tr_b16 a[124:125], v105 offset:6152\n\t"
      "ds_read_b64_tr_b16 a[126:127], v105 offset:6408\n\t"
      "s_waitcnt vmcnt(0) lgkmcnt(0)\n\t"
      "s_barrier\n\t"
      "s_mov_b64 exec, s[90:91]\n\t"
      "buffer_store_dword v107, off, s[60:63], s75 sc1\n\t"
      "s_sub_u32 s75, s75, 32\n\t"
      "buffer_store_dword v107, off, s[60:63], s75 sc1\n\t"
      "s_sub_u32 s75, s75, 2016\n\t"
      "buffer_store_dword v107, off, s[60:63], s75 sc1\n\t"
      "s_mov_b64 exec, -1\n\t"
      : "+{s34}"(w), "+{s33}"(wv), "+{s[0:1]}"(A), "=&s"(tag), "={a[0:31]}"(wo.a), "={a[32:63]}"(wo.b), "={a[64:95]}"(wo.c), "={a[96:127]}"(wo.d)
      :
      : "memory", "vcc", "scc", "v0", "v1", "v2", "v3", "v4", "v5", "v6", "v7", "v8", "v9", "v10", "v11", "v12", "v13", "v14", "v15", "v16", "v17", "v18", "v19", "v20", "v21", "v22", "v23", "v24", "v25", "v26", "v27", "v28", "v29", "v30", "v31", "v32", "v33", "v34", "v35", "v36", "v37", "v38", "v39", "v40", "v41", "v42", "v43", "v44", "v45", "v46", "v47", "v48", "v49", "v50", "v51", "v52", "v53", "v54", "v55", "v56", "v57", "v58", "v59", "v60", "v61", "v62", "v63", "v64", "v65", "v66", "v67", "v68", "v69", "v70", "v71", "v72", "v73", "v74", "v75", "v76", "v77", "v78", "v79", "v80", "v81", "v82", "v83", "v84", "v85", "v86", "v87", "v88", "v89", "v90", "v91", "v92", "v93", "v94", "v95", "v96", "v97", "v98", "v99", "v100", "v101", "v102", "v103", "v104", "v105", "v106", "v107", "v108", "v109", "v110", "v111", "v112", "v113", "v114", "v115", "v116", "v117", "v118", "v119", "v120", "v121", "v122", "v123", "v124", "v125", "v126", "v127", "v128", "v129", "v130", "v131", "v132", "v133", "v134", "v135", "v136", "v137", "v138", "v139", "v140", "v141", "v142", "v143", "v144", "v145", "v146", "v147", "v148", "v149", "v150", "v151", "v152", "v153", "v154", "v155", "v156", "v157", "v158", "v159", "v160", "v161", "v162", "v163", "v164", "v165", "v166", "v167", "v168", "v169", "v170", "v171", "v172", "v173", "v174", "v175", "v176", "v177", "v178", "v179", "v180", "v181", "v182", "v183", "v184", "v185", "v186", "v187", "v188", "v189", "v190", "v191", "v192", "v193", "v194", "v195", "v196", "v197", "v198", "v199", "v200", "v201", "v202", "v203", "v204", "v205", "v206", "v207", "v208", "v209", "v210", "v211", "v212", "v213", "v214", "v215", "v216", "v217", "v218", "v219", "v220", "v221", "v222", "v223", "v224", "v225", "v226", "v227", "v228", "v229", "v230", "v231", "v232", "v233", "v234", "v235", "v236", "v237", "v238", "v239", "v240", "v241", "v242", "v243", "v244", "v245", "v246", "v247", "v248", "v249", "v250", "v251", "v252", "v253", "v254", "v255", "a128", "a129", "a130", "a131", "a132", "a133", "a134", "a135", "a136", "a137", "a138", "a139", "a140", "a141", "a142", "a143", "a144", "a145", "a146", "a147", "a148", "a149", "a150", "a151", "a152", "a153", "a154", "a155", "a156", "a157", "a158", "a159", "a160", "a161", "a162", "a163", "a164", "a165", "a166", "a167", "a168", "a169", "a170", "a171", "a172", "a173", "a174", "a175", "a176", "a177", "a178", "a179", "a180", "a181", "a182", "a183", "a184", "a185", "a186", "a187", "a188", "a189", "a190", "a191", "a192", "a193", "a194", "a195", "a196", "a197", "a198", "a199", "a200", "a201", "a202", "a203", "a204", "a205", "a206", "a207", "a208", "a209", "a210", "a211", "a212", "a213", "a214", "a215", "a216", "a217", "a218", "a219", "a220", "a221", "a222", "a223", "a224", "a225", "a226", "a227", "a228", "a229", "a230", "a231", "a232", "a233", "a234", "a235", "a236", "a237", "a238", "a239", "a240", "a241", "a242", "a243", "a244", "a245", "a246", "a247", "a248", "a249", "a250", "a251", "a252", "a253", "a254", "a255", "s36", "s37", "s38", "s39", "s40", "s41", "s42", "s43", "s44", "s45", "s46", "s47", "s48", "s49", "s50", "s51", "s52", "s53", "s54", "s55", "s56", "s57", "s58", "s59", "s60", "s61", "s62", "s63", "s64", "s65", "s66", "s67", "s68", "s69", "s70", "s71", "s72", "s73", "s74", "s75", "s76", "s77", "s78", "s79", "s80", "s81", "s82", "s83", "s84", "s85", "s86", "s87", "s88", "s89", "s90", "s91", "s92", "s93", "s94", "s95");
  return tag;
}

__device__ __forceinline__ void worker_tasks(char* lds, int& w, int& wv, KArgs& A, unsigned tag, WoRegs& wo) {
  asm volatile("" : "+s"(w), "+s"(wv), "+s"(A));
  const int lane = fresh_lane(), l15 = lane & 15, l4 = lane >> 4;
  const int team = w & 3, j = w >> 2, g = j >> 2, c = j & 3, h = g * 4 + wv;
  const u64 wsa = (u64)A->ws, xa = (u64)A->x, oa = (u64)A->out;
  u32x16 c0 = {(unsigned)wsa, (unsigned)(wsa >> 32) & 0xffffu, WS_BYTES, 0x00020000u, (unsigned)xa, (unsigned)(xa >> 32) & 0xffffu, S_ * DM * 2, 0x00020000u,
               (unsigned)oa, (unsigned)(oa >> 32) & 0xffffu, S_ * DM * 2, 0x00020000u, O_STAMPS + (310000 + j * 4 + wv) * 8, (unsigned)(l15 - l4 * 4),
               (unsigned)(team + 4 * wv + 48), (unsigned)j};
  const int r0 = 0 ? h : j;
  u32x16 c1 = {(unsigned)team, (unsigned)(team + 4 * (0 ? c : wv)), O_QKV + (team * 32 + h) * 24576, O_KVM + (team * 32 + h) * 2048, O_VTM + (team * 32 + h) * 2048,
               O_PART + (team * 8 + g) * 32768, O_PART + r0 * 1024, O_PFLAG + team * 16384, O_KVSTEP + h * 64, O_RBUF, O_LNH, O_HBUF, (unsigned)r0,
               (unsigned)wv * 8192, tag, O_PFLAG + team * 16384 + (j * 4 + wv) * 128};
  u32x8 c4 = {(unsigned)lane, (unsigned)lane, (unsigned)lane, (unsigned)lane, O_TFLAG + (6 * 32 + h) * 64 + 32 + (l15 & 1) * 16, O_TFLAG + (15 * 32 + h) * 64 + 32, O_WM + lane * 256 + 4, (unsigned)lane};
  const int rc = 8 * (l15 + 16 * (l4 >> 1)) + 4 * (l4 & 1);
  const u32x2 w0 = *(const u32x2*)(A->w_r + rc), w1 = *(const u32x2*)(A->w_r + 256 + rc);
  u32x16 c2 = {w0[0] << 16, w0[0] & 0xffff0000u, w0[1] << 16, w0[1] & 0xffff0000u, w1[0] << 16, w1[0] & 0xffff0000u, w1[1] << 16, w1[1] & 0xffff0000u,
               (unsigned)lane * 16, (unsigned)rc * 2, (unsigned)(lane ^ ((l4 & 1) * 8)) * 16,
               (unsigned)(wv * 8192 + (l15 & 3) * 256 + (((l4 * 4 + (l15 >> 2)) ^ ((l15 & 1) * 8)) << 4)),
               (unsigned)(l4 * 128 + (l15 & 7) * 16), (unsigned)(lane & 31) * 16,
               (unsigned)(65536 + wv * 8192 + lane * 16), (unsigned)(65536 + lane * 16)};
  unsigned c3 = (unsigned)(l15 * DM + c * 128 + wv * 32 + l4 * 8) * 2;
  asm volatile(
      "s_nop 1\n\t"
      "v_readfirstlane_b32 s36, v0\n\t"
      "v_readfirstlane_b32 s37, v1\n\t"
      "v_readfirstlane_b32 s38, v2\n\t"
      "v_readfirstlane_b32 s39, v3\n\t"
      "v_readfirstlane_b32 s40, v4\n\t"
      "v_readfirstlane_b32 s41, v5\n\t"
      "v_readfirstlane_b32 s42, v6\n\t"
      "v_readfirstlane_b32 s43, v7\n\t"
      "v_readfirstlane_b32 s44, v8\n\t"
      "v_readfirstlane_b32 s45, v9\n\t"
      "v_readfirstlane_b32 s46, v10\n\t"
      "v_readfirstlane_b32 s47, v11\n\t"
      "v_readfirstlane_b32 s48, v16\n\t"
      "v_readfirstlane_b32 s49, v17\n\t"
      "v_readfirstlane_b32 s50, v18\n\t"
      "v_readfirstlane_b32 s51, v19\n\t"
      "v_readfirstlane_b32 s52, v20\n\t"
      "v_readfirstlane_b32 s53, v21\n\t"
      "v_readfirstlane_b32 s54, v22\n\t"
      "v_readfirstlane_b32 s55, v23\n\t"
      "v_readfirstlane_b32 s56, v24\n\t"
      "v_readfirstlane_b32 s57, v25\n\t"
      "v_readfirstlane_b32 s58, v26\n\t"
      "v_readfirstlane_b32 s59, v27\n\t"
      "v_readfirstlane_b32 s60, v28\n\t"
      "v_readfirstlane_b32 s61, v29\n\t"
      "v_readfirstlane_b32 s62, v30\n\t"
      "v_readfirstlane_b32 s63, v31\n\t"
      "v_readfirstlane_b32 s75, v12\n\t"
      "v_readfirstlane_b32 s93, v14\n\t"
      "v_readfirstlane_b32 s94, v15\n\t"
      "v_cmp_ge_i32 s[76:77], v13, 0\n\t"
      "v_cmp_ge_i32 s[78:79], v13, 1\n\t"
      "v_cmp_ge_i32 s[80:81], v13, 2\n\t"
      "v_cmp_ge_i32 s[82:83], v13, 3\n\t"
      "s_nop 4\n\t"
      "v_readfirstlane_b32 s99, v37\n\t"
      "s_add_u32 s98, s53, 12582912\n\t"
      "v_lshrrev_b32 v40, 4, v248\n\t"
      "v_and_b32 v41, 15, v40\n\t"
      "v_lshrrev_b32 v40, 1, v40\n\t"
      "v_and_b32 v40, 24, v40\n\t"
      "v_sub_u32 v41, v41, v40\n\t"
      "s_mov_b32 s69, 0x3f80\n\t"
      "s_mov_b32 s70, 0x3f800000\n\t"
      "v_mov_b32 v42, 0\n\t"
      "v_cmp_eq_i32 vcc, 0, v41\n\t"
      "v_mov_b32 v43, s69\n\t"
      "s_nop 0\n\t"
      "v_cndmask_b32 v42, v42, v43\n\t"
      "v_cmp_eq_i32 vcc, 1, v41\n\t"
      "v_mov_b32 v43, s70\n\t"
      "s_nop 0\n\t"
      "v_cndmask_b32 v42, v42, v43\n\t"
      "v_accvgpr_write_b32 a224, v42\n\t"
      "v_mov_b32 v42, 0\n\t"
      "v_cmp_eq_i32 vcc, 2, v41\n\t"
      "v_mov_b32 v43, s69\n\t"
      "s_nop 0\n\t"
      "v_cndmask_b32 v42, v42, v43\n\t"
      "v_cmp_eq_i32 vcc, 3, v41\n\t"
      "v_mov_b32 v43, s70\n\t"
      "s_nop 0\n\t"
      "v_cndmask_b32 v42, v42, v43\n\t"
      "v_accvgpr_write_b32 a225, v42\n\t"
      "v_mov_b32 v42, 0\n\t"
      "v_cmp_eq_i32 vcc, 4, v41\n\t"
      "v_mov_b32 v43, s69\n\t"
      "s_nop 0\n\t"
      "v_cndmask_b32 v42, v42, v43\n\t"
      "v_cmp_eq_i32 vcc, 5, v41\n\t"
      "v_mov_b32 v43, s70\n\t"
      "s_nop 0\n\t"
      "v_cndmask_b32 v42, v42, v43\n\t"
      "v_accvgpr_write_b32 a226, v42\n\t"
      "v_mov_b32 v42, 0\n\t"
      "v_cmp_eq_i32 vcc, 6, v41\n\t"
      "v_mov_b32 v43, s69\n\t"
      "s_nop 0\n\t"
      "v_cndmask_b32 v42, v42, v43\n\t"
      "v_cmp_eq_i32 vcc, 7, v41\n\t"
      "v_mov_b32 v43, s70\n\t"
      "s_nop 0\n\t"
      "v_cndmask_b32 v42, v42, v43\n\t"
      "v_accvgpr_write_b32 a227, v42\n\t"
      "v_mov_b32 v42, 0\n\t"
      "v_cmp_eq_i32 vcc, -16, v41\n\t"
      "v_mov_b32 v43, s69\n\t"
      "s_nop 0\n\t"
      "v_cndmask_b32 v42, v42, v43\n\t"
      "v_cmp_eq_i32 vcc, -15, v41\n\t"
      "v_mov_b32 v43, s70\n\t"
      "s_nop 0\n\t"
      "v_cndmask_b32 v42, v42, v43\n\t"
      "v_accvgpr_write_b32 a228, v42\n\t"
      "v_mov_b32 v42, 0\n\t"
      "v_cmp_eq_i32 vcc, -14, v41\n\t"
      "v_mov_b32 v43, s69\n\t"
      "s_nop 0\n\t"
      "v_cndmask_b32 v42, v42, v43\n\t"
      "v_cmp_eq_i32 vcc, -13, v41\n\t"
      "v_mov_b32 v43, s70\n\t"
      "s_nop 0\n\t"
      "v_cndmask_b32 v42, v42, v43\n\t"
      "v_accvgpr_write_b32 a229, v42\n\t"
      "v_mov_b32 v42, 0\n\t"
      "v_cmp_eq_i32 vcc, -12, v41\n\t"
      "v_mov_b32 v43, s69\n\t"
      "s_nop 0\n\t"
      "v_cndmask_b32 v42, v42, v43\n\t"
      "v_cmp_eq_i32 vcc, -11, v41\n\t"
      "v_mov_b32 v43, s70\n\t"
      "s_nop 0\n\t"
      "v_cndmask_b32 v42, v42, v43\n\t"
      "v_accvgpr_write_b32 a230, v42\n\t"
      "v_mov_b32 v42, 0\n\t"
      "v_cmp_eq_i32 vcc, -10, v41\n\t"
      "v_mov_b32 v43, s69\n\t"
      "s_nop 0\n\t"
      "v_cndmask_b32 v42, v42, v43\n\t"
      "v_cmp_eq_i32 vcc, -9, v41\n\t"
      "v_mov_b32 v43, s70\n\t"
      "s_nop 0\n\t"
      "v_cndmask_b32 v42, v42, v43\n\t"
      "v_accvgpr_write_b32 a231, v42\n\t"
      "s_mov_b32 s72, 0x40000\n\t"
      "WE0%=:\n\t"
      "buffer_load_dwordx4 v[40:43], v36, s[36:39], 0 offen sc1\n\t"
      "buffer_load_dword v48, v38, s[36:39], 0 offen sc1\n\t"
      "buffer_load_dword v49, v38, s[36:39], 0 offen offset:128 sc1\n\t"
      "s_waitcnt vmcnt(0)\n\t"
      "v_bitop3_b32 v40, v40, v41, s62 bitop3:0x7e\n\t"
      "v_bitop3_b32 v48, v48, v49, s62 bitop3:0x7e\n\t"
      "v_or_b32 v40, v40, v48\n\t"
      "v_cmp_ne_u32 vcc, 0, v40\n\t"
      "s_nop 0\n\t"
      "s_cbranch_vccz WE1%=\n\t"
      "s_sleep 8\n\t"
      "s_sub_u32 s72, s72, 1\n\t"
      "s_cmp_lg_u32 s72, 0\n\t"
      "s_cbranch_scc1 WE0%=\n\t"
      "WE1%=:\n\t"
      "buffer_load_dwordx4 a[128:131], v248, s[36:39], s50 offen  sc1\n\t"
      "buffer_load_dwordx4 a[132:135], v248, s[36:39], s50 offen offset:1024  sc1\n\t"
      "buffer_load_dwordx4 a[136:139], v248, s[36:39], s50 offen offset:2048  sc1\n\t"
      "buffer_load_dwordx4 a[140:143], v248, s[36:39], s50 offen offset:3072  sc1\n\t"
      "s_add_u32 s69, s50, 4096\n\t"
      "buffer_load_dwordx4 a[144:147], v248, s[36:39], s69 offen  sc1\n\t"
      "buffer_load_dwordx4 a[148:151], v248, s[36:39], s69 offen offset:1024  sc1\n\t"
      "buffer_load_dwordx4 a[152:155], v248, s[36:39], s69 offen offset:2048  sc1\n\t"
      "buffer_load_dwordx4 a[156:159], v248, s[36:39], s69 offen offset:3072  sc1\n\t"
      "s_add_u32 s69, s50, 8192\n\t"
      "s_add_u32 m0, s61, 0\n\t"
      "s_nop 0\n\t"
      "buffer_load_dwordx4 v250, s[36:39], s69 offen sc1 lds\n\t"
      "buffer_load_dwordx4 v250, s[36:39], s69 offen offset:1024 sc1 lds\n\t"
      "buffer_load_dwordx4 v250, s[36:39], s69 offen offset:2048 sc1 lds\n\t"
      "buffer_load_dwordx4 v250, s[36:39], s69 offen offset:3072 sc1 lds\n\t"
      "s_add_u32 s69, s50, 12288\n\t"
      "s_add_u32 m0, s61, 4096\n\t"
      "s_nop 0\n\t"
      "buffer_load_dwordx4 v250, s[36:39], s69 offen sc1 lds\n\t"
      "buffer_load_dwordx4 v250, s[36:39], s69 offen offset:1024 sc1 lds\n\t"
      "buffer_load_dwordx4 v250, s[36:39], s69 offen offset:2048 sc1 lds\n\t"
      "buffer_load_dwordx4 v250, s[36:39], s69 offen offset:3072 sc1 lds\n\t"
      "s_add_u32 s69, s50, 16384\n\t"
      "buffer_load_dwordx4 a[160:163], v248, s[36:39], s69 offen  sc1\n\t"
      "buffer_load_dwordx4 a[164:167], v248, s[36:39], s69 offen offset:1024  sc1\n\t"
      "buffer_load_dwordx4 a[168:171], v248, s[36:39], s69 offen offset:2048  sc1\n\t"
      "buffer_load_dwordx4 a[172:175], v248, s[36:39], s69 offen offset:3072  sc1\n\t"
      "s_add_u32 s69, s50, 20480\n\t"
      "buffer_load_dwordx4 a[176:179], v248, s[36:39], s69 offen  sc1\n\t"
      "buffer_load_dwordx4 a[180:183], v248, s[36:39], s69 offen offset:1024  sc1\n\t"
      "buffer_load_dwordx4 a[184:187], v248, s[36:39], s69 offen offset:2048  sc1\n\t"
      "buffer_load_dwordx4 a[188:191], v248, s[36:39], s69 offen offset:3072  sc1\n\t"
      "s_add_u32 s50, s50, 3145728\n\t"
      "s_mov_b32 s64, 0\n\t"
      "s_mov_b32 s65, 0\n\t"
      "s_mov_b32 s66, 0\n\t"
      "s_mov_b32 s96, 0\n\t"
      "s_mov_b32 s97, 0\n\t"
      "s_add_u32 s89, s49, 16\n\t"
      "s_sub_u32 s95, s94, s60\n\t"
      "s_lshl_b32 s95, s95, 10\n\t"
      "s_add_u32 s95, s95, s54\n\t"
      "s_mov_b32 s67, -1\n\t"
      "s_mov_b32 s74, 0xffff0000\n\t"
      "s_mov_b32 s84, 0x3db504f3\n\t"
      "s_mov_b32 s86, 0x3fb8aa3b\n\t"
      "s_mov_b32 s85, 0x3db504f3\n\t"
      "s_mov_b32 s87, 0x3fb8aa3b\n\t"
      "v_mov_b32 v239, 0xff61b1e6\n\t"
      "s_waitcnt vmcnt(0) lgkmcnt(0)\n\t"
      "s_branch WA%=\n\t"
      "WTOP%=:\n\t"
      "s_cmp_ge_u32 s48, 64\n\t"
      "s_cbranch_scc1 WFIN%=\n\t"
      "s_lshr_b32 s69, s48, 2\n\t"
      "s_cmp_eq_u32 s69, 5\n\t"
      "s_cbranch_scc1 WH2%=\n\t"
      "WT0%=:\n\t"
      "s_waitcnt vmcnt(17)\n\t"
      "s_cmp_eq_u32 s24, 9\n\t"
      "s_cbranch_scc1 WT1%=\n\t"
      "s_waitcnt vmcnt(10)\n\t"
      "s_cmp_lg_u32 s97, 0\n\t"
      "s_cbranch_scc1 WT1%=\n\t"
      "s_waitcnt vmcnt(9)\n\t"
      "s_cmp_eq_u32 s24, 8\n\t"
      "s_cbranch_scc1 WT1%=\n\t"
      "s_waitcnt vmcnt(2)\n\t"
      "WT1%=:\n\t"
      "v_min_u32 v228, v228, v229\n\t"
      "v_min3_u32 v228, v228, v230, v231\n\t"
      "v_min_u32 v232, v232, v233\n\t"
      "s_cmp_lg_u32 s65, 0\n\t"
      "s_cselect_b32 s70, s89, s49\n\t"
      "v_readfirstlane_b32 s88, v228\n\t"
      "s_memrealtime s[90:91]\n\t"
      "v_cmp_ge_u32 vcc, s70, v232\n\t"
      "s_lshr_b32 s69, s88, 16\n\t"
      "s_cmp_gt_u32 s69, s48\n\t"
      "s_cselect_b32 s64, 1, 0\n\t"
      "s_cmp_eq_u64 vcc, 0\n\t"
      "s_cselect_b32 s66, 1, 0\n\t"
      "s_cmp_eq_u32 s65, 2\n\t"
      "s_cselect_b32 s66, 0, s66\n\t"
      "s_add_u32 s70, s48, 4\n\t"
      "s_cmp_gt_u32 s69, s70\n\t"
      "s_cselect_b32 s96, 1, 0\n\t"
      "s_or_b32 s64, s64, s97\n\t"
      "s_cmp_lg_u32 s97, 0\n\t"
      "s_cbranch_scc1 WA%=\n\t"
      "s_cmp_lg_u32 s64, 0\n\t"
      "s_cbranch_scc1 WT2%=\n\t"
      "s_sub_u32 s69, s48, s69\n\t"
      "s_mul_i32 s69, s69, 170\n\t"
      "s_waitcnt lgkmcnt(0)\n\t"
      "s_sub_u32 s70, s90, s88\n\t"
      "s_add_u32 s70, s70, 200\n\t"
      "s_and_b32 s70, s70, 0xffff\n\t"
      "s_add_u32 s69, s69, 60\n\t"
      "s_cmp_ge_u32 s70, s69\n\t"
      "s_cselect_b32 s69, s48, 0\n\t"
      "s_cmp_ge_u32 s69, 40\n\t"
      "s_cselect_b32 s64, 2, 0\n\t"
      "s_cmp_eq_u32 s64, 0\n\t"
      "s_cbranch_scc1 WA%=\n\t"
      "WT2%=:\n\t"
      "buffer_load_dwordx4 v[128:131], v252, s[36:39], s51 offen sc1\n\t"
      "buffer_load_dwordx4 v[132:135], v252, s[36:39], s51 offen offset:512 sc1\n\t"
      "buffer_load_dwordx4 v[136:139], v252, s[36:39], s51 offen offset:1024 sc1\n\t"
      "buffer_load_dwordx4 v[140:143], v252, s[36:39], s51 offen offset:1536 sc1\n\t"
      "buffer_load_dwordx4 v[144:147], v253, s[36:39], s52 offen sc1\n\t"
      "buffer_load_dwordx4 v[148:151], v253, s[36:39], s52 offen offset:512 sc1\n\t"
      "buffer_load_dwordx4 v[152:155], v253, s[36:39], s52 offen offset:1024 sc1\n\t"
      "buffer_load_dwordx4 v[156:159], v253, s[36:39], s52 offen offset:1536 sc1\n\t"
      "WA%=:\n\t"
      "v_mfma_f32_16x16x32_bf16 v[0:3], a[128:131], a[160:163], 0\n\t"
      "ds_read_b64_tr_b16 a[192:193], v251\n\t"
      "ds_read_b64_tr_b16 a[194:195], v251 offset:1024\n\t"
      "v_mfma_f32_16x16x32_bf16 v[4:7], a[128:131], a[164:167], 0\n\t"
      "ds_read_b64_tr_b16 a[196:197], v251 offset:8\n\t"
      "ds_read_b64_tr_b16 a[198:199], v251 offset:1032\n\t"
      "v_mfma_f32_16x16x32_bf16 v[8:11], a[132:135], a[164:167], 0\n\t"
      "ds_read_b64_tr_b16 a[200:201], v251 offset:2048\n\t"
      "ds_read_b64_tr_b16 a[202:203], v251 offset:3072\n\t"
      "v_mfma_f32_16x16x32_bf16 v[0:3], a[136:139], a[168:171], v[0:3]\n\t"
      "ds_read_b64_tr_b16 a[204:205], v251 offset:2056\n\t"
      "ds_read_b64_tr_b16 a[206:207], v251 offset:3080\n\t"
      "v_mfma_f32_16x16x32_bf16 v[4:7], a[136:139], a[172:175], v[4:7]\n\t"
      "v_mfma_f32_16x16x32_bf16 v[8:11], a[140:143], a[172:175], v[8:11]\n\t"
      "s_waitcnt lgkmcnt(0)\n\t"
      "ds_read_b64_tr_b16 a[208:209], v251 offset:4096\n\t"
      "ds_read_b64_tr_b16 a[210:211], v251 offset:5120\n\t"
      "ds_read_b64_tr_b16 a[212:213], v251 offset:4104\n\t"
      "ds_read_b64_tr_b16 a[214:215], v251 offset:5128\n\t"
      "ds_read_b64_tr_b16 a[216:217], v251 offset:6144\n\t"
      "ds_read_b64_tr_b16 a[218:219], v251 offset:7168\n\t"
      "ds_read_b64_tr_b16 a[220:221], v251 offset:6152\n\t"
      "ds_read_b64_tr_b16 a[222:223], v251 offset:7176\n\t"
      "v_mfma_f32_16x16x32_bf16 v[0:3], a[144:147], a[176:179], v[0:3]\n\t"
      "v_mfma_f32_16x16x32_bf16 v[4:7], a[144:147], a[180:183], v[4:7]\n\t"
      "v_mfma_f32_16x16x32_bf16 v[8:11], a[148:151], a[180:183], v[8:11]\n\t"
      "v_mfma_f32_16x16x32_bf16 v[0:3], a[152:155], a[184:187], v[0:3]\n\t"
      "v_mfma_f32_16x16x32_bf16 v[4:7], a[152:155], a[188:191], v[4:7]\n\t"
      "v_mfma_f32_16x16x32_bf16 v[8:11], a[156:159], a[188:191], v[8:11]\n\t"
      "s_waitcnt lgkmcnt(0)\n\t"
      "buffer_load_dwordx4 a[128:131], v248, s[36:39], s50 offen  sc1\n\t"
      "s_nop 3\n\t"
      "v_cvt_pk_bf16_f32 v0, v0, v1\n\t"
      "v_cvt_pk_bf16_f32 v2, v2, v3\n\t"
      "v_cvt_pk_bf16_f32 v4, v4, v5\n\t"
      "v_cvt_pk_bf16_f32 v6, v6, v7\n\t"
      "v_cvt_pk_bf16_f32 v8, v8, v9\n\t"
      "v_cvt_pk_bf16_f32 v10, v10, v11\n\t"
      "v_and_b32 v1, s74, v0\n\t"
      "v_lshlrev_b32 v0, 16, v0\n\t"
      "v_and_b32 v3, s74, v2\n\t"
      "v_lshlrev_b32 v2, 16, v2\n\t"
      "v_and_b32 v5, s74, v4\n\t"
      "v_lshlrev_b32 v4, 16, v4\n\t"
      "v_and_b32 v7, s74, v6\n\t"
      "v_lshlrev_b32 v6, 16, v6\n\t"
      "v_and_b32 v9, s74, v8\n\t"
      "v_lshlrev_b32 v8, 16, v8\n\t"
      "v_and_b32 v11, s74, v10\n\t"
      "v_lshlrev_b32 v10, 16, v10\n\t"
      "v_pk_mul_f32 v[0:1], v[0:1], s[84:85]\n\t"
      "v_pk_mul_f32 v[2:3], v[2:3], s[84:85]\n\t"
      "v_pk_mul_f32 v[4:5], v[4:5], s[84:85]\n\t"
      "buffer_load_dwordx4 a[132:135], v248, s[36:39], s50 offen offset:1024  sc1\n\t"
      "v_pk_mul_f32 v[6:7], v[6:7], s[84:85]\n\t"
      "v_pk_mul_f32 v[8:9], v[8:9], s[84:85]\n\t"
      "v_pk_mul_f32 v[10:11], v[10:11], s[84:85]\n\t"
      "v_cvt_pk_bf16_f32 v0, v0, v1\n\t"
      "v_cvt_pk_bf16_f32 v2, v2, v3\n\t"
      "v_cvt_pk_bf16_f32 v4, v4, v5\n\t"
      "v_cvt_pk_bf16_f32 v6, v6, v7\n\t"
      "v_cvt_pk_bf16_f32 v8, v8, v9\n\t"
      "v_cvt_pk_bf16_f32 v10, v10, v11\n\t"
      "v_and_b32 v1, s74, v0\n\t"
      "v_lshlrev_b32 v0, 16, v0\n\t"
      "v_and_b32 v3, s74, v2\n\t"
      "v_lshlrev_b32 v2, 16, v2\n\t"
      "v_and_b32 v5, s74, v4\n\t"
      "v_lshlrev_b32 v4, 16, v4\n\t"
      "v_and_b32 v7, s74, v6\n\t"
      "v_lshlrev_b32 v6, 16, v6\n\t"
      "v_and_b32 v9, s74, v8\n\t"
      "v_lshlrev_b32 v8, 16, v8\n\t"
      "v_and_b32 v11, s74, v10\n\t"
      "buffer_load_dwordx4 a[136:139], v248, s[36:39], s50 offen offset:2048  sc1\n\t"
      "v_lshlrev_b32 v10, 16, v10\n\t"
      "v_cndmask_b32_e64 v0, v239, v0, s[76:77]\n\t"
      "v_cndmask_b32_e64 v8, v239, v8, s[76:77]\n\t"
      "v_cndmask_b32_e64 v1, v239, v1, s[78:79]\n\t"
      "v_cndmask_b32_e64 v9, v239, v9, s[78:79]\n\t"
      "v_cndmask_b32_e64 v2, v239, v2, s[80:81]\n\t"
      "v_cndmask_b32_e64 v10, v239, v10, s[80:81]\n\t"
      "v_cndmask_b32_e64 v3, v239, v3, s[82:83]\n\t"
      "v_cndmask_b32_e64 v11, v239, v11, s[82:83]\n\t"
      "v_max3_f32 v234, v0, v1, v2\n\t"
      "v_max3_f32 v235, v4, v5, v6\n\t"
      "v_max_f32 v234, v234, v3\n\t"
      "v_max3_f32 v235, v235, v7, v8\n\t"
      "v_max3_f32 v235, v235, v9, v10\n\t"
      "v_max_f32 v235, v235, v11\n\t"
      "s_nop 1\n\t"
      "v_permlane16_swap_b32 v234, v235\n\t"
      "v_max_f32 v234, v234, v235\n\t"
      "v_mov_b32 v235, v234\n\t"
      "s_nop 1\n\t"
      "v_permlane32_swap_b32 v234, v235\n\t"
      "v_max_f32 v234, v234, v235\n\t"
      "v_mov_b32 v235, v234\n\t"
      "buffer_load_dwordx4 a[140:143], v248, s[36:39], s50 offen offset:3072  sc1\n\t"
      "s_nop 0\n\t"
      "v_permlane16_swap_b32 v234, v235\n\t"
      "v_pk_mul_f32 v[12:13], v[234:235], s[86:87]\n\t"
      "v_pk_fma_f32 v[0:1], v[0:1], s[86:87], v[12:13] op_sel:[0,0,0] op_sel_hi:[1,1,0] neg_lo:[0,0,1] neg_hi:[0,0,1]\n\t"
      "v_pk_fma_f32 v[2:3], v[2:3], s[86:87], v[12:13] op_sel:[0,0,0] op_sel_hi:[1,1,0] neg_lo:[0,0,1] neg_hi:[0,0,1]\n\t"
      "v_pk_fma_f32 v[4:5], v[4:5], s[86:87], v[12:13] op_sel:[0,0,1] op_sel_hi:[1,1,1] neg_lo:[0,0,1] neg_hi:[0,0,1]\n\t"
      "v_pk_fma_f32 v[6:7], v[6:7], s[86:87], v[12:13] op_sel:[0,0,1] op_sel_hi:[1,1,1] neg_lo:[0,0,1] neg_hi:[0,0,1]\n\t"
      "v_pk_fma_f32 v[8:9], v[8:9], s[86:87], v[12:13] op_sel:[0,0,1] op_sel_hi:[1,1,1] neg_lo:[0,0,1] neg_hi:[0,0,1]\n\t"
      "v_pk_fma_f32 v[10:11], v[10:11], s[86:87], v[12:13] op_sel:[0,0,1] op_sel_hi:[1,1,1] neg_lo:[0,0,1] neg_hi:[0,0,1]\n\t"
      "v_exp_f32 v0, v0\n\t"
      "v_exp_f32 v1, v1\n\t"
      "v_exp_f32 v2, v2\n\t"
      "v_exp_f32 v3, v3\n\t"
      "v_exp_f32 v4, v4\n\t"
      "v_exp_f32 v5, v5\n\t"
      "v_exp_f32 v6, v6\n\t"
      "s_add_u32 s69, s50, 4096\n\t"
      "buffer_load_dwordx4 a[144:147], v248, s[36:39], s69 offen  sc1\n\t"
      "v_exp_f32 v7, v7\n\t"
      "v_exp_f32 v8, v8\n\t"
      "v_exp_f32 v9, v9\n\t"
      "v_exp_f32 v10, v10\n\t"
      "v_exp_f32 v11, v11\n\t"
      "v_mov_b64 v[18:19], 0\n\t"
      "v_cvt_pk_bf16_f32 v16, v0, v1\n\t"
      "v_cvt_pk_bf16_f32 v17, v2, v3\n\t"
      "v_cvt_pk_bf16_f32 v20, v4, v5\n\t"
      "v_cvt_pk_bf16_f32 v21, v6, v7\n\t"
      "v_cvt_pk_bf16_f32 v22, v8, v9\n\t"
      "v_cvt_pk_bf16_f32 v23, v10, v11\n\t"
      "v_mfma_f32_16x16x32_bf16 v[64:67], a[192:195], v[16:19], 0\n\t"
      "buffer_load_dwordx4 a[148:151], v248, s[36:39], s69 offen offset:1024  sc1\n\t"
      "v_pk_add_f32 v[0:1], v[0:1], v[2:3]\n\t"
      "v_mfma_f32_16x16x32_bf16 v[96:99], a[192:195], v[20:23], 0\n\t"
      "v_pk_add_f32 v[4:5], v[4:5], v[6:7]\n\t"
      "v_mfma_f32_16x16x32_bf16 v[68:71], a[196:199], v[16:19], 0\n\t"
      "v_pk_add_f32 v[8:9], v[8:9], v[10:11]\n\t"
      "v_mfma_f32_16x16x32_bf16 v[100:103], a[196:199], v[20:23], 0\n\t"
      "v_add_f32 v236, v0, v1\n\t"
      "v_mfma_f32_16x16x32_bf16 v[72:75], a[200:203], v[16:19], 0\n\t"
      "v_pk_add_f32 v[4:5], v[4:5], v[8:9]\n\t"
      "v_mfma_f32_16x16x32_bf16 v[104:107], a[200:203], v[20:23], 0\n\t"
      "buffer_load_dwordx4 a[152:155], v248, s[36:39], s69 offen offset:2048  sc1\n\t"
      "v_add_f32 v237, v4, v5\n\t"
      "v_mfma_f32_16x16x32_bf16 v[76:79], a[204:207], v[16:19], 0\n\t"
      "v_mfma_f32_16x16x32_bf16 v[108:111], a[204:207], v[20:23], 0\n\t"
      "v_mfma_f32_16x16x32_bf16 v[80:83], a[208:211], v[16:19], 0\n\t"
      "v_mfma_f32_16x16x32_bf16 v[112:115], a[208:211], v[20:23], 0\n\t"
      "v_mfma_f32_16x16x32_bf16 v[84:87], a[212:215], v[16:19], 0\n\t"
      "buffer_load_dwordx4 a[156:159], v248, s[36:39], s69 offen offset:3072  sc1\n\t"
      "v_mfma_f32_16x16x32_bf16 v[116:119], a[212:215], v[20:23], 0\n\t"
      "v_mfma_f32_16x16x32_bf16 v[88:91], a[216:219], v[16:19], 0\n\t"
      "v_mfma_f32_16x16x32_bf16 v[120:123], a[216:219], v[20:23], 0\n\t"
      "v_mfma_f32_16x16x32_bf16 v[92:95], a[220:223], v[16:19], 0\n\t"
      "v_mfma_f32_16x16x32_bf16 v[124:127], a[220:223], v[20:23], 0\n\t"
      "s_waitcnt vmcnt(8)\n\t"
      "s_cmp_lt_u32 s48, 4\n\t"
      "s_cbranch_scc1 WGO%=\n\t"
      "v_mov_b32 v0, s48\n\t"
      "s_mov_b64 exec, 1\n\t"
      "buffer_store_dword v0, off, s[36:39], s63 sc1\n\t"
      "s_mov_b64 exec, -1\n\t"
      "s_cmp_lt_i32 s67, 0\n\t"
      "s_cbranch_scc1 WPB1%=\n\t"
      "v_mov_b32 v8, s68\n\t"
      "s_mov_b64 exec, 1\n\t"
      "buffer_store_dword v8, off, s[36:39], s67 sc1\n\t"
      "s_mov_b64 exec, -1\n\t"
      "s_mov_b32 s67, -1\n\t"
      "WPB1%=:\n\t"
      "WGO%=:\n\t"
      "s_cmp_eq_u32 s64, 1\n\t"
      "s_cbranch_scc1 WKV%=\n\t"
      "s_cmp_lg_u32 s65, 0\n\t"
      "s_cbranch_scc1 WR2%=\n\t"
      "WGO2%=:\n\t"
      "s_cmp_lt_u32 s48, 40\n\t"
      "s_cselect_b32 s71, 1, -1\n\t"
      "s_add_u32 s71, s71, s48\n\t"
      "s_mov_b32 s72, 0x40000\n\t"
      "WLOOP%=:\n\t"
      "s_cmp_ge_u32 s49, 48\n\t"
      "s_cselect_b32 s69, s49, 99\n\t"
      "s_cmp_lt_u32 s69, s48\n\t"
      "s_cbranch_scc1 WRW%=\n\t"
      "s_cmp_eq_u32 s64, 2\n\t"
      "s_cbranch_scc1 WKC%=\n\t"
      "v_lshlrev_b32 v6, 4, v248\n\t"
      "v_mov_b64 v[4:5], 0\n\t"
      "buffer_load_dwordx4 v[0:3], off, s[36:39], s56 sc1\n\t"
      "s_cmp_ge_u32 s49, s48\n\t"
      "s_cbranch_scc1 WL1%=\n\t"
      "buffer_load_dword v4, v6, s[36:39], s55 offen sc1\n\t"
      "buffer_load_dword v5, v6, s[36:39], s55 offen offset:128 sc1\n\t"
      "WL1%=:\n\t"
      "s_waitcnt vmcnt(0)\n\t"
      "s_cmp_lt_i32 s67, 0\n\t"
      "s_cbranch_scc1 WL2%=\n\t"
      "v_mov_b32 v8, s68\n\t"
      "s_mov_b64 exec, 1\n\t"
      "buffer_store_dword v8, off, s[36:39], s67 sc1\n\t"
      "s_mov_b64 exec, -1\n\t"
      "s_mov_b32 s67, -1\n\t"
      "WL2%=:\n\t"
      "v_min_u32 v0, v0, v1\n\t"
      "v_min3_u32 v0, v0, v2, v3\n\t"
      "v_min_u32 v4, v4, v5\n\t"
      "v_readfirstlane_b32 s88, v0\n\t"
      "v_cmp_ge_u32 vcc, s49, v4\n\t"
      "s_lshr_b32 s69, s88, 16\n\t"
      "s_max_u32 s70, s71, s48\n\t"
      "s_cmp_ge_u32 s69, s70\n\t"
      "s_cbranch_scc1 WKL%=\n\t"
      "s_cmp_ge_u32 s69, s71\n\t"
      "s_cbranch_scc1 WKF%=\n\t"
      "s_cbranch_vccz WRW%=\n\t"
      "s_sleep 6\n\t"
      "s_cmp_lt_u32 s49, s48\n\t"
      "s_cbranch_scc1 WKM%=\n\t"
      "s_sub_u32 s69, s71, s69\n\t"
      "s_min_u32 s69, s69, 9\n\t"
      "WKN%=:\n\t"
      "s_sub_u32 s69, s69, 1\n\t"
      "s_cmp_eq_u32 s69, 0\n\t"
      "s_cbranch_scc1 WKM%=\n\t"
      "s_sleep 48\n\t"
      "s_branch WKN%=\n\t"
      "WKM%=:\n\t"
      "s_sub_u32 s72, s72, 1\n\t"
      "s_cmp_gt_i32 s72, 0\n\t"
      "s_cbranch_scc1 WLOOP%=\n\t"
      "s_branch WKL%=\n\t"
      "WFIN%=:\n\t"
      "s_mov_b32 s64, 0\n\t"
      "s_mov_b32 s72, 0x40000\n\t"
      "s_waitcnt vmcnt(2)\n\t"
      "WFIN1%=:\n\t"
      "s_cmp_lt_i32 s67, 0\n\t"
      "s_cbranch_scc1 WF1%=\n\t"
      "v_mov_b32 v8, s68\n\t"
      "s_mov_b64 exec, 1\n\t"
      "buffer_store_dword v8, off, s[36:39], s67 sc1\n\t"
      "s_mov_b64 exec, -1\n\t"
      "s_mov_b32 s67, -1\n\t"
      "WF1%=:\n\t"
      "s_cmp_lg_u32 s65, 0\n\t"
      "s_cbranch_scc1 WRC%=\n\t"
      "s_cmp_ge_u32 s49, 64\n\t"
      "s_cbranch_scc1 WEND%=\n\t"
      "s_cmp_ge_u32 s49, 48\n\t"
      "s_cbranch_scc1 WRS%=\n\t"
      "v_lshlrev_b32 v6, 4, v248\n\t"
      "WRF%=:\n\t"
      "buffer_load_dword v4, v6, s[36:39], s55 offen sc1\n\t"
      "buffer_load_dword v5, v6, s[36:39], s55 offen offset:128 sc1\n\t"
      "s_waitcnt vmcnt(0)\n\t"
      "v_min_u32 v4, v4, v5\n\t"
      "v_cmp_ge_u32 vcc, s49, v4\n\t"
      "s_nop 0\n\t"
      "s_cbranch_vccz WRW%=\n\t"
      "s_sleep 8\n\t"
      "s_sub_u32 s72, s72, 1\n\t"
      "s_cmp_gt_i32 s72, 0\n\t"
      "s_cbranch_scc1 WRF%=\n\t"
      "WRS%=:\n\t"
      "s_waitcnt vmcnt(0)\n\t"
      "WRW%=:\n\t"
      "s_lshl_b32 s70, s49, 5\n\t"
      "s_add_u32 s70, s70, s60\n\t"
      "s_lshl_b32 s70, s70, 10\n\t"
      "buffer_load_dwordx2 v[224:225], v249, s[40:43], s70 offen \n\t"
      "buffer_load_dwordx2 v[226:227], v249, s[40:43], s70 offen offset:512 \n\t"
      "WRL%=:\n\t"
      "s_lshl_b32 s69, s49, 18\n\t"
      "s_add_u32 s69, s69, s54\n\t"
      "buffer_load_dwordx4 v[160:163], v248, s[36:39], s69 offen sc1\n\t"
      "s_add_u32 s69, s69, 0x8000\n\t"
      "buffer_load_dwordx4 v[164:167], v248, s[36:39], s69 offen sc1\n\t"
      "s_add_u32 s69, s69, 0x8000\n\t"
      "buffer_load_dwordx4 v[168:171], v248, s[36:39], s69 offen sc1\n\t"
      "s_add_u32 s69, s69, 0x8000\n\t"
      "buffer_load_dwordx4 v[172:175], v248, s[36:39], s69 offen sc1\n\t"
      "s_add_u32 s69, s69, 0x8000\n\t"
      "buffer_load_dwordx4 v[176:179], v248, s[36:39], s69 offen sc1\n\t"
      "s_add_u32 s69, s69, 0x8000\n\t"
      "buffer_load_dwordx4 v[180:183], v248, s[36:39], s69 offen sc1\n\t"
      "s_add_u32 s69, s69, 0x8000\n\t"
      "buffer_load_dwordx4 v[184:187], v248, s[36:39], s69 offen sc1\n\t"
      "s_add_u32 s69, s69, 0x8000\n\t"
      "buffer_load_dwordx4 v[188:191], v248, s[36:39], s69 offen sc1\n\t"
      "s_waitcnt vmcnt(0)\n\t"
      "s_cmp_lt_i32 s67, 0\n\t"
      "s_cbranch_scc1 WF2%=\n\t"
      "v_mov_b32 v8, s68\n\t"
      "s_mov_b64 exec, 1\n\t"
      "buffer_store_dword v8, off, s[36:39], s67 sc1\n\t"
      "s_mov_b64 exec, -1\n\t"
      "s_mov_b32 s67, -1\n\t"
      "WF2%=:\n\t"
      "WRC%=:\n\t"
      "v_mfma_f32_16x16x32_bf16 v[16:19], a[224:227], v[160:163], 0\n\t"
      "v_mfma_f32_16x16x32_bf16 v[20:23], a[228:231], v[160:163], 0\n\t"
      "v_and_b32 v193, s74, v224\n\t"
      "v_lshlrev_b32 v192, 16, v224\n\t"
      "v_mfma_f32_16x16x32_bf16 v[16:19], a[224:227], v[164:167], v[16:19]\n\t"
      "v_mfma_f32_16x16x32_bf16 v[20:23], a[228:231], v[164:167], v[20:23]\n\t"
      "v_and_b32 v195, s74, v225\n\t"
      "v_lshlrev_b32 v194, 16, v225\n\t"
      "v_mfma_f32_16x16x32_bf16 v[16:19], a[224:227], v[168:171], v[16:19]\n\t"
      "v_mfma_f32_16x16x32_bf16 v[20:23], a[228:231], v[168:171], v[20:23]\n\t"
      "v_and_b32 v197, s74, v226\n\t"
      "v_lshlrev_b32 v196, 16, v226\n\t"
      "v_mfma_f32_16x16x32_bf16 v[16:19], a[224:227], v[172:175], v[16:19]\n\t"
      "v_mfma_f32_16x16x32_bf16 v[20:23], a[228:231], v[172:175], v[20:23]\n\t"
      "v_and_b32 v199, s74, v227\n\t"
      "v_lshlrev_b32 v198, 16, v227\n\t"
      "v_mfma_f32_16x16x32_bf16 v[16:19], a[224:227], v[176:179], v[16:19]\n\t"
      "v_mfma_f32_16x16x32_bf16 v[20:23], a[228:231], v[176:179], v[20:23]\n\t"
      "v_mfma_f32_16x16x32_bf16 v[16:19], a[224:227], v[180:183], v[16:19]\n\t"
      "v_mfma_f32_16x16x32_bf16 v[20:23], a[228:231], v[180:183], v[20:23]\n\t"
      "v_mfma_f32_16x16x32_bf16 v[16:19], a[224:227], v[184:187], v[16:19]\n\t"
      "v_mfma_f32_16x16x32_bf16 v[20:23], a[228:231], v[184:187], v[20:23]\n\t"
      "v_mfma_f32_16x16x32_bf16 v[16:19], a[224:227], v[188:191], v[16:19]\n\t"
      "v_mfma_f32_16x16x32_bf16 v[20:23], a[228:231], v[188:191], v[20:23]\n\t"
      "s_nop 6\n\t"
      "v_cvt_pk_bf16_f32 v32, v16, v17\n\t"
      "v_cvt_pk_bf16_f32 v34, v18, v19\n\t"
      "v_cvt_pk_bf16_f32 v36, v20, v21\n\t"
      "v_cvt_pk_bf16_f32 v38, v22, v23\n\t"
      "v_and_b32 v33, s74, v32\n\t"
      "v_lshlrev_b32 v32, 16, v32\n\t"
      "v_and_b32 v35, s74, v34\n\t"
      "v_lshlrev_b32 v34, 16, v34\n\t"
      "v_and_b32 v37, s74, v36\n\t"
      "v_lshlrev_b32 v36, 16, v36\n\t"
      "v_and_b32 v39, s74, v38\n\t"
      "v_lshlrev_b32 v38, 16, v38\n\t"
      "v_pk_add_f32 v[192:193], v[192:193], v[32:33]\n\t"
      "v_pk_add_f32 v[194:195], v[194:195], v[34:35]\n\t"
      "v_pk_add_f32 v[196:197], v[196:197], v[36:37]\n\t"
      "v_pk_add_f32 v[198:199], v[198:199], v[38:39]\n\t"
      "v_cvt_pk_bf16_f32 v200, v192, v193\n\t"
      "v_cvt_pk_bf16_f32 v201, v194, v195\n\t"
      "v_cvt_pk_bf16_f32 v202, v196, v197\n\t"
      "v_cvt_pk_bf16_f32 v203, v198, v199\n\t"
      "v_and_b32 v193, s74, v200\n\t"
      "v_lshlrev_b32 v192, 16, v200\n\t"
      "v_and_b32 v195, s74, v201\n\t"
      "v_lshlrev_b32 v194, 16, v201\n\t"
      "v_and_b32 v197, s74, v202\n\t"
      "v_lshlrev_b32 v196, 16, v202\n\t"
      "v_and_b32 v199, s74, v203\n\t"
      "v_lshlrev_b32 v198, 16, v203\n\t"
      "v_pk_add_f32 v[32:33], v[192:193], v[196:197]\n\t"
      "v_pk_add_f32 v[34:35], v[194:195], v[198:199]\n\t"
      "v_pk_add_f32 v[32:33], v[32:33], v[34:35]\n\t"
      "v_add_f32 v32, v32, v33\n\t"
      "s_nop 1\n\t"
      "v_add_f32_dpp v32, v32, v32 quad_perm:[1,0,3,2] row_mask:0xf bank_mask:0xf\n\t"
      "s_nop 1\n\t"
      "v_add_f32_dpp v32, v32, v32 quad_perm:[2,3,0,1] row_mask:0xf bank_mask:0xf\n\t"
      "s_nop 1\n\t"
      "v_add_f32_dpp v32, v32, v32 row_ror:4 row_mask:0xf bank_mask:0xf\n\t"
      "s_nop 1\n\t"
      "v_add_f32_dpp v32, v32, v32 row_ror:8 row_mask:0xf bank_mask:0xf\n\t"
      "v_mov_b32 v33, v32\n\t"
      "s_nop 1\n\t"
      "v_permlane16_swap_b32 v32, v33\n\t"
      "v_add_f32 v32, v32, v33\n\t"
      "v_mov_b32 v33, v32\n\t"
      "s_nop 1\n\t"
      "v_permlane32_swap_b32 v32, v33\n\t"
      "v_add_f32 v32, v32, v33\n\t"
      "v_cmp_u_f32 vcc, v32, v32\n\t"
      "v_mul_f32 v204, 0x3b000000, v32\n\t"
      "s_cbranch_vccnz WRN%=\n\t"
      "WRD%=:\n\t"
      "v_pk_add_f32 v[40:41], v[192:193], v[204:205] op_sel_hi:[1,0] neg_lo:[0,1] neg_hi:[0,1]\n\t"
      "v_pk_add_f32 v[42:43], v[194:195], v[204:205] op_sel_hi:[1,0] neg_lo:[0,1] neg_hi:[0,1]\n\t"
      "v_pk_add_f32 v[44:45], v[196:197], v[204:205] op_sel_hi:[1,0] neg_lo:[0,1] neg_hi:[0,1]\n\t"
      "v_pk_add_f32 v[46:47], v[198:199], v[204:205] op_sel_hi:[1,0] neg_lo:[0,1] neg_hi:[0,1]\n\t"
      "v_pk_mul_f32 v[32:33], v[40:41], v[40:41]\n\t"
      "v_pk_fma_f32 v[32:33], v[42:43], v[42:43], v[32:33]\n\t"
      "v_pk_fma_f32 v[32:33], v[44:45], v[44:45], v[32:33]\n\t"
      "v_pk_fma_f32 v[32:33], v[46:47], v[46:47], v[32:33]\n\t"
      "v_add_f32 v32, v32, v33\n\t"
      "s_nop 1\n\t"
      "v_add_f32_dpp v32, v32, v32 quad_perm:[1,0,3,2] row_mask:0xf bank_mask:0xf\n\t"
      "s_nop 1\n\t"
      "v_add_f32_dpp v32, v32, v32 quad_perm:[2,3,0,1] row_mask:0xf bank_mask:0xf\n\t"
      "s_nop 1\n\t"
      "v_add_f32_dpp v32, v32, v32 row_ror:4 row_mask:0xf bank_mask:0xf\n\t"
      "s_nop 1\n\t"
      "v_add_f32_dpp v32, v32, v32 row_ror:8 row_mask:0xf bank_mask:0xf\n\t"
      "v_mov_b32 v33, v32\n\t"
      "s_nop 1\n\t"
      "v_permlane16_swap_b32 v32, v33\n\t"
      "v_add_f32 v32, v32, v33\n\t"
      "v_mov_b32 v33, v32\n\t"
      "s_nop 1\n\t"
      "v_permlane32_swap_b32 v32, v33\n\t"
      "v_add_f32 v32, v32, v33\n\t"
      "v_mul_f32 v205, 0x3b000000, v32\n\t"
      "v_cvt_pk_bf16_f32 v204, v204, v205\n\t"
      "v_and_b32 v205, s74, v204\n\t"
      "v_lshlrev_b32 v204, 16, v204\n\t"
      "v_add_f32 v205, 0x3727c5ac, v205\n\t"
      "v_pk_add_f32 v[40:41], v[192:193], v[204:205] op_sel_hi:[1,0] neg_lo:[0,1] neg_hi:[0,1]\n\t"
      "v_pk_add_f32 v[42:43], v[194:195], v[204:205] op_sel_hi:[1,0] neg_lo:[0,1] neg_hi:[0,1]\n\t"
      "v_pk_add_f32 v[44:45], v[196:197], v[204:205] op_sel_hi:[1,0] neg_lo:[0,1] neg_hi:[0,1]\n\t"
      "v_pk_add_f32 v[46:47], v[198:199], v[204:205] op_sel_hi:[1,0] neg_lo:[0,1] neg_hi:[0,1]\n\t"
      "v_cvt_pk_bf16_f32 v205, v205, v205\n\t"
      "v_and_b32 v205, s74, v205\n\t"
      "v_sqrt_f32 v205, v205\n\t"
      "v_cvt_pk_bf16_f32 v40, v40, v41\n\t"
      "v_cvt_pk_bf16_f32 v42, v42, v43\n\t"
      "v_cvt_pk_bf16_f32 v44, v44, v45\n\t"
      "v_cvt_pk_bf16_f32 v46, v46, v47\n\t"
      "v_cvt_pk_bf16_f32 v205, v205, v205\n\t"
      "v_and_b32 v205, s74, v205\n\t"
      "v_rcp_f32 v204, v205\n\t"
      "v_and_b32 v41, s74, v40\n\t"
      "v_lshlrev_b32 v40, 16, v40\n\t"
      "v_and_b32 v43, s74, v42\n\t"
      "v_lshlrev_b32 v42, 16, v42\n\t"
      "v_and_b32 v45, s74, v44\n\t"
      "v_lshlrev_b32 v44, 16, v44\n\t"
      "v_and_b32 v47, s74, v46\n\t"
      "v_lshlrev_b32 v46, 16, v46\n\t"
      "v_pk_mul_f32 v[40:41], v[40:41], v[204:205] op_sel_hi:[1,0]\n\t"
      "v_pk_mul_f32 v[42:43], v[42:43], v[204:205] op_sel_hi:[1,0]\n\t"
      "v_pk_mul_f32 v[44:45], v[44:45], v[204:205] op_sel_hi:[1,0]\n\t"
      "v_pk_mul_f32 v[46:47], v[46:47], v[204:205] op_sel_hi:[1,0]\n\t"
      "v_cvt_pk_bf16_f32 v52, v40, v41\n\t"
      "v_cvt_pk_bf16_f32 v53, v42, v43\n\t"
      "v_cvt_pk_bf16_f32 v54, v44, v45\n\t"
      "v_cvt_pk_bf16_f32 v55, v46, v47\n\t"
      "v_and_b32 v33, s74, v52\n\t"
      "v_lshlrev_b32 v32, 16, v52\n\t"
      "v_and_b32 v35, s74, v53\n\t"
      "v_lshlrev_b32 v34, 16, v53\n\t"
      "v_and_b32 v37, s74, v54\n\t"
      "v_lshlrev_b32 v36, 16, v54\n\t"
      "v_and_b32 v39, s74, v55\n\t"
      "v_lshlrev_b32 v38, 16, v55\n\t"
      "v_pk_mul_f32 v[40:41], v[32:33], v[240:241]\n\t"
      "v_pk_fma_f32 v[40:41], v[34:35], v[242:243], v[40:41]\n\t"
      "v_pk_fma_f32 v[40:41], v[36:37], v[244:245], v[40:41]\n\t"
      "v_pk_fma_f32 v[40:41], v[38:39], v[246:247], v[40:41]\n\t"
      "v_add_f32 v40, v40, v41\n\t"
      "s_lshl_b32 s73, s49, 5\n\t"
      "s_add_u32 s73, s73, s60\n\t"
      "s_lshl_b32 s69, s73, 2\n\t"
      "s_add_u32 s69, s69, s57\n\t"
      "s_lshl_b32 s70, s73, 7\n\t"
      "s_add_u32 s70, s70, s57\n\t"
      "s_sub_u32 s70, s70, 212992\n\t"
      "s_cmp_ge_u32 s49, 56\n\t"
      "s_cselect_b32 s70, s70, s69\n\t"
      "v_add_f32_dpp v40, v40, v40 quad_perm:[1,0,3,2] row_mask:0xf bank_mask:0xf\n\t"
      "s_nop 1\n\t"
      "v_add_f32_dpp v40, v40, v40 quad_perm:[2,3,0,1] row_mask:0xf bank_mask:0xf\n\t"
      "s_nop 1\n\t"
      "v_add_f32_dpp v40, v40, v40 row_ror:4 row_mask:0xf bank_mask:0xf\n\t"
      "s_nop 1\n\t"
      "v_add_f32_dpp v40, v40, v40 row_ror:8 row_mask:0xf bank_mask:0xf\n\t"
      "v_mov_b32 v41, v40\n\t"
      "s_nop 1\n\t"
      "v_permlane16_swap_b32 v40, v41\n\t"
      "v_add_f32 v40, v40, v41\n\t"
      "v_mov_b32 v41, v40\n\t"
      "s_nop 1\n\t"
      "v_permlane32_swap_b32 v40, v41\n\t"
      "v_add_f32 v40, v40, v41\n\t"
      "v_cvt_pk_bf16_f32 v40, v40, v40\n\t"
      "v_and_b32 v40, s74, v40\n\t"
      "s_mov_b64 exec, 1\n\t"
      "buffer_store_dword v40, off, s[36:39], s70 sc1\n\t"
      "s_mov_b64 exec, -1\n\t"
      "v_pk_add_f32 v[192:193], v[192:193], v[32:33]\n\t"
      "v_pk_add_f32 v[194:195], v[194:195], v[34:35]\n\t"
      "v_pk_add_f32 v[196:197], v[196:197], v[36:37]\n\t"
      "v_pk_add_f32 v[198:199], v[198:199], v[38:39]\n\t"
      "v_readfirstlane_b32 s68, v40\n\t"
      "v_cvt_pk_bf16_f32 v56, v192, v193\n\t"
      "v_cvt_pk_bf16_f32 v57, v194, v195\n\t"
      "v_cvt_pk_bf16_f32 v58, v196, v197\n\t"
      "v_cvt_pk_bf16_f32 v59, v198, v199\n\t"
      "s_add_u32 s67, s69, 8192\n\t"
      "s_lshl_b32 s73, s73, 10\n\t"
      "s_add_u32 s69, s73, s58\n\t"
      "s_add_u32 s70, s73, s59\n\t"
      "buffer_store_dwordx2 v[56:57], v249, s[44:47], s73 offen sc1\n\t"
      "buffer_store_dwordx2 v[52:53], v249, s[36:39], s69 offen sc1\n\t"
      "buffer_store_dwordx2 v[200:201], v249, s[36:39], s70 offen sc1\n\t"
      "buffer_store_dwordx2 v[58:59], v249, s[44:47], s73 offen offset:512 sc1\n\t"
      "buffer_store_dwordx2 v[54:55], v249, s[36:39], s69 offen offset:512 sc1\n\t"
      "buffer_store_dwordx2 v[202:203], v249, s[36:39], s70 offen offset:512 sc1\n\t"
      "s_mov_b32 s49, s89\n\t"
      "s_add_u32 s89, s89, 16\n\t"
      "s_lshr_b32 s69, s89, 4\n\t"
      "s_cmp_eq_u32 s69, 3\n\t"
      "s_cselect_b32 s89, s93, s89\n\t"
      "s_cmp_ge_u32 s49, 48\n\t"
      "s_cselect_b32 s60, s94, s60\n\t"
      "s_cselect_b32 s54, s95, s54\n\t"
      "s_cmp_ge_u32 s48, 64\n\t"
      "s_cbranch_scc1 WRX%=\n\t"
      "s_cmp_lg_u32 s65, 0\n\t"
      "s_mov_b32 s65, 0\n\t"
      "s_cbranch_scc1 WGO%=\n\t"
      "s_mov_b32 s66, 0\n\t"
      "s_branch WLOOP%=\n\t"
      "WRX%=:\n\t"
      "s_mov_b32 s65, 0\n\t"
      "s_waitcnt vmcnt(0)\n\t"
      "s_branch WFIN1%=\n\t"
      "WRN%=:\n\t"
      "v_max3_u32 v40, v160, v161, v162\n\t"
      "v_max3_u32 v40, v40, v163, v164\n\t"
      "v_max3_u32 v40, v40, v165, v166\n\t"
      "v_max3_u32 v40, v40, v167, v168\n\t"
      "v_max3_u32 v40, v40, v169, v170\n\t"
      "v_max3_u32 v40, v40, v171, v172\n\t"
      "v_max3_u32 v40, v40, v173, v174\n\t"
      "v_max3_u32 v40, v40, v175, v176\n\t"
      "v_max3_u32 v40, v40, v177, v178\n\t"
      "v_max3_u32 v40, v40, v179, v180\n\t"
      "v_max3_u32 v40, v40, v181, v182\n\t"
      "v_max3_u32 v40, v40, v183, v184\n\t"
      "v_max3_u32 v40, v40, v185, v186\n\t"
      "v_max3_u32 v40, v40, v187, v188\n\t"
      "v_max3_u32 v40, v40, v189, v190\n\t"
      "v_max_u32 v40, v40, v191\n\t"
      "v_cmp_eq_u32 vcc, -1, v40\n\t"
      "s_nop 0\n\t"
      "s_cbranch_vccz WRD%=\n\t"
      "s_cmp_lg_u32 s65, 0\n\t"
      "s_cbranch_scc1 WRD%=\n\t"
      "s_sub_u32 s72, s72, 1\n\t"
      "s_cmp_gt_i32 s72, 0\n\t"
      "s_cbranch_scc1 WRL%=\n\t"
      "s_branch WRD%=\n\t"
      "WH2%=:\n\t"
      "s_mov_b32 s72, 0x40000\n\t"
      "WH3%=:\n\t"
      "buffer_load_dwordx2 v[0:1], off, s[36:39], s99 sc1\n\t"
      "buffer_load_dwordx2 v[2:3], off, s[36:39], s99 offset:16 sc1\n\t"
      "s_sub_u32 s72, s72, 1\n\t"
      "s_waitcnt vmcnt(0)\n\t"
      "v_bitop3_b32 v0, v0, v1, s62 bitop3:0x7e\n\t"
      "v_bitop3_b32 v2, v2, v3, s62 bitop3:0x7e\n\t"
      "v_or_b32 v0, v0, v2\n\t"
      "v_cmp_ne_u32 vcc, 0, v0\n\t"
      "s_cmp_eq_u32 s72, 0\n\t"
      "s_cbranch_scc1 WT0%=\n\t"
      "s_cbranch_vccnz WH3%=\n\t"
      "s_branch WT0%=\n\t"
      "WKV1%=:\n\t"
      "s_cmp_eq_u32 s66, 0\n\t"
      "s_cbranch_scc1 WB1%=\n\t"
      "WB2%=:\n\t"
      "s_mov_b32 s65, 1\n\t"
      "v_mfma_f32_16x16x32_bf16 v[0:3], v[128:131], a[160:163], 0\n\t"
      "v_mfma_f32_16x16x32_bf16 v[4:7], v[128:131], a[164:167], 0\n\t"
      "v_mfma_f32_16x16x32_bf16 v[0:3], v[132:135], a[168:171], v[0:3]\n\t"
      "v_mfma_f32_16x16x32_bf16 v[4:7], v[132:135], a[172:175], v[4:7]\n\t"
      "v_mfma_f32_16x16x32_bf16 v[0:3], v[136:139], a[176:179], v[0:3]\n\t"
      "v_mfma_f32_16x16x32_bf16 v[4:7], v[136:139], a[180:183], v[4:7]\n\t"
      "v_mfma_f32_16x16x32_bf16 v[0:3], v[140:143], a[184:187], v[0:3]\n\t"
      "v_mfma_f32_16x16x32_bf16 v[4:7], v[140:143], a[188:191], v[4:7]\n\t"
      "s_add_u32 s51, s51, 262144\n\t"
      "s_add_u32 s52, s52, 262144\n\t"
      "s_lshl_b32 s70, s49, 5\n\t"
      "s_add_u32 s70, s70, s60\n\t"
      "s_lshl_b32 s70, s70, 10\n\t"
      "buffer_load_dwordx2 v[224:225], v249, s[40:43], s70 offen \n\t"
      "s_nop 0\n\t"
      "v_cvt_pk_bf16_f32 v0, v0, v1\n\t"
      "v_cvt_pk_bf16_f32 v2, v2, v3\n\t"
      "v_cvt_pk_bf16_f32 v4, v4, v5\n\t"
      "v_cvt_pk_bf16_f32 v6, v6, v7\n\t"
      "v_and_b32 v1, s74, v0\n\t"
      "v_lshlrev_b32 v0, 16, v0\n\t"
      "v_and_b32 v3, s74, v2\n\t"
      "buffer_load_dwordx2 v[226:227], v249, s[40:43], s70 offen offset:512 \n\t"
      "v_lshlrev_b32 v2, 16, v2\n\t"
      "v_and_b32 v5, s74, v4\n\t"
      "v_lshlrev_b32 v4, 16, v4\n\t"
      "v_and_b32 v7, s74, v6\n\t"
      "v_lshlrev_b32 v6, 16, v6\n\t"
      "v_pk_mul_f32 v[0:1], v[0:1], s[84:85]\n\t"
      "v_pk_mul_f32 v[2:3], v[2:3], s[84:85]\n\t"
      "s_lshl_b32 s69, s49, 18\n\t"
      "s_add_u32 s69, s69, s54\n\t"
      "buffer_load_dwordx4 v[160:163], v248, s[36:39], s69 offen sc1\n\t"
      "v_pk_mul_f32 v[4:5], v[4:5], s[84:85]\n\t"
      "v_pk_mul_f32 v[6:7], v[6:7], s[84:85]\n\t"
      "v_cvt_pk_bf16_f32 v0, v0, v1\n\t"
      "v_cvt_pk_bf16_f32 v2, v2, v3\n\t"
      "v_cvt_pk_bf16_f32 v4, v4, v5\n\t"
      "v_cvt_pk_bf16_f32 v6, v6, v7\n\t"
      "v_and_b32 v1, s74, v0\n\t"
      "s_add_u32 s69, s69, 0x8000\n\t"
      "buffer_load_dwordx4 v[164:167], v248, s[36:39], s69 offen sc1\n\t"
      "v_lshlrev_b32 v0, 16, v0\n\t"
      "v_and_b32 v3, s74, v2\n\t"
      "v_lshlrev_b32 v2, 16, v2\n\t"
      "v_and_b32 v5, s74, v4\n\t"
      "v_lshlrev_b32 v4, 16, v4\n\t"
      "v_and_b32 v7, s74, v6\n\t"
      "v_lshlrev_b32 v6, 16, v6\n\t"
      "s_add_u32 s69, s69, 0x8000\n\t"
      "buffer_load_dwordx4 v[168:171], v248, s[36:39], s69 offen sc1\n\t"
      "v_max3_f32 v8, v0, v1, v2\n\t"
      "v_max3_f32 v8, v8, v3, v234\n\t"
      "v_max3_f32 v9, v4, v5, v6\n\t"
      "v_max3_f32 v9, v9, v7, v235\n\t"
      "s_nop 1\n\t"
      "v_permlane16_swap_b32 v8, v9\n\t"
      "v_max_f32 v8, v8, v9\n\t"
      "v_mov_b32 v9, v8\n\t"
      "s_add_u32 s69, s69, 0x8000\n\t"
      "buffer_load_dwordx4 v[172:175], v248, s[36:39], s69 offen sc1\n\t"
      "v_permlane16_swap_b32 v8, v9\n\t"
      "v_pk_mul_f32 v[8:9], v[8:9], s[86:87]\n\t"
      "v_pk_fma_f32 v[10:11], v[234:235], s[86:87], v[8:9] neg_lo:[0,0,1] neg_hi:[0,0,1]\n\t"
      "v_pk_fma_f32 v[0:1], v[0:1], s[86:87], v[8:9] op_sel:[0,0,0] op_sel_hi:[1,1,0] neg_lo:[0,0,1] neg_hi:[0,0,1]\n\t"
      "v_pk_fma_f32 v[2:3], v[2:3], s[86:87], v[8:9] op_sel:[0,0,0] op_sel_hi:[1,1,0] neg_lo:[0,0,1] neg_hi:[0,0,1]\n\t"
      "v_pk_fma_f32 v[4:5], v[4:5], s[86:87], v[8:9] op_sel:[0,0,1] op_sel_hi:[1,1,1] neg_lo:[0,0,1] neg_hi:[0,0,1]\n\t"
      "v_pk_fma_f32 v[6:7], v[6:7], s[86:87], v[8:9] op_sel:[0,0,1] op_sel_hi:[1,1,1] neg_lo:[0,0,1] neg_hi:[0,0,1]\n\t"
      "s_add_u32 s69, s69, 0x8000\n\t"
      "buffer_load_dwordx4 v[176:179], v248, s[36:39], s69 offen sc1\n\t"
      "v_exp_f32 v10, v10\n\t"
      "v_exp_f32 v11, v11\n\t"
      "v_exp_f32 v0, v0\n\t"
      "v_exp_f32 v1, v1\n\t"
      "s_add_u32 s69, s69, 0x8000\n\t"
      "buffer_load_dwordx4 v[180:183], v248, s[36:39], s69 offen sc1\n\t"
      "v_exp_f32 v2, v2\n\t"
      "v_exp_f32 v3, v3\n\t"
      "v_exp_f32 v4, v4\n\t"
      "v_exp_f32 v5, v5\n\t"
      "s_add_u32 s69, s69, 0x8000\n\t"
      "buffer_load_dwordx4 v[184:187], v248, s[36:39], s69 offen sc1\n\t"
      "v_exp_f32 v6, v6\n\t"
      "v_exp_f32 v7, v7\n\t"
      "v_pk_mul_f32 v[12:13], v[236:237], v[10:11]\n\t"
      "v_pk_add_f32 v[14:15], v[0:1], v[2:3]\n\t"
      "s_add_u32 s69, s69, 0x8000\n\t"
      "buffer_load_dwordx4 v[188:191], v248, s[36:39], s69 offen sc1\n\t"
      "v_pk_add_f32 v[16:17], v[4:5], v[6:7]\n\t"
      "v_add_f32 v14, v14, v15\n\t"
      "v_add_f32 v15, v16, v17\n\t"
      "v_fma_f32 v12, v14, 0.5, v12\n\t"
      "v_fma_f32 v13, v15, 0.5, v13\n\t"
      "s_nop 1\n\t"
      "v_permlane16_swap_b32 v12, v13\n\t"
      "v_add_f32 v12, v12, v13\n\t"
      "s_add_u32 s69, s50, 8192\n\t"
      "s_add_u32 m0, s61, 0\n\t"
      "s_nop 0\n\t"
      "buffer_load_dwordx4 v250, s[36:39], s69 offen sc1 lds\n\t"
      "v_mov_b32 v13, v12\n\t"
      "s_nop 1\n\t"
      "v_permlane32_swap_b32 v12, v13\n\t"
      "v_add_f32 v12, v12, v13\n\t"
      "v_mov_b32 v13, v12\n\t"
      "s_nop 1\n\t"
      "v_permlane16_swap_b32 v12, v13\n\t"
      "v_rcp_f32 v12, v12\n\t"
      "buffer_load_dwordx4 v250, s[36:39], s69 offen offset:1024 sc1 lds\n\t"
      "v_rcp_f32 v13, v13\n\t"
      "s_nop 0\n\t"
      "v_pk_mul_f32 v[10:11], v[10:11], v[12:13]\n\t"
      "v_pk_mul_f32 v[12:13], v[12:13], 0.5 op_sel_hi:[1,0]\n\t"
      "v_pk_mul_f32 v[0:1], v[0:1], v[12:13] op_sel:[0,0] op_sel_hi:[1,0]\n\t"
      "v_pk_mul_f32 v[2:3], v[2:3], v[12:13] op_sel:[0,0] op_sel_hi:[1,0]\n\t"
      "v_pk_mul_f32 v[4:5], v[4:5], v[12:13] op_sel:[0,1] op_sel_hi:[1,1]\n\t"
      "buffer_load_dwordx4 v250, s[36:39], s69 offen offset:2048 sc1 lds\n\t"
      "v_pk_mul_f32 v[6:7], v[6:7], v[12:13] op_sel:[0,1] op_sel_hi:[1,1]\n\t"
      "v_cvt_pk_bf16_f32 v16, v0, v1\n\t"
      "v_cvt_pk_bf16_f32 v17, v2, v3\n\t"
      "v_cvt_pk_bf16_f32 v18, v4, v5\n\t"
      "v_cvt_pk_bf16_f32 v19, v6, v7\n\t"
      "v_pk_mul_f32 v[64:65], v[64:65], v[10:11] op_sel:[0,0] op_sel_hi:[1,0]\n\t"
      "v_pk_mul_f32 v[66:67], v[66:67], v[10:11] op_sel:[0,0] op_sel_hi:[1,0]\n\t"
      "buffer_load_dwordx4 v250, s[36:39], s69 offen offset:3072 sc1 lds\n\t"
      "v_pk_mul_f32 v[68:69], v[68:69], v[10:11] op_sel:[0,0] op_sel_hi:[1,0]\n\t"
      "v_pk_mul_f32 v[70:71], v[70:71], v[10:11] op_sel:[0,0] op_sel_hi:[1,0]\n\t"
      "v_mfma_f32_16x16x16_bf16 v[64:67], v[144:145], v[16:17], v[64:67]\n\t"
      "v_pk_mul_f32 v[72:73], v[72:73], v[10:11] op_sel:[0,0] op_sel_hi:[1,0]\n\t"
      "v_pk_mul_f32 v[74:75], v[74:75], v[10:11] op_sel:[0,0] op_sel_hi:[1,0]\n\t"
      "v_mfma_f32_16x16x16_bf16 v[68:71], v[146:147], v[16:17], v[68:71]\n\t"
      "s_add_u32 s69, s50, 12288\n\t"
      "s_add_u32 m0, s61, 4096\n\t"
      "s_nop 0\n\t"
      "buffer_load_dwordx4 v250, s[36:39], s69 offen sc1 lds\n\t"
      "v_pk_mul_f32 v[76:77], v[76:77], v[10:11] op_sel:[0,0] op_sel_hi:[1,0]\n\t"
      "v_pk_mul_f32 v[78:79], v[78:79], v[10:11] op_sel:[0,0] op_sel_hi:[1,0]\n\t"
      "v_mfma_f32_16x16x16_bf16 v[72:75], v[148:149], v[16:17], v[72:75]\n\t"
      "v_pk_mul_f32 v[80:81], v[80:81], v[10:11] op_sel:[0,0] op_sel_hi:[1,0]\n\t"
      "v_pk_mul_f32 v[82:83], v[82:83], v[10:11] op_sel:[0,0] op_sel_hi:[1,0]\n\t"
      "buffer_load_dwordx4 v250, s[36:39], s69 offen offset:1024 sc1 lds\n\t"
      "v_mfma_f32_16x16x16_bf16 v[76:79], v[150:151], v[16:17], v[76:79]\n\t"
      "v_pk_mul_f32 v[84:85], v[84:85], v[10:11] op_sel:[0,0] op_sel_hi:[1,0]\n\t"
      "v_pk_mul_f32 v[86:87], v[86:87], v[10:11] op_sel:[0,0] op_sel_hi:[1,0]\n\t"
      "v_mfma_f32_16x16x16_bf16 v[80:83], v[152:153], v[16:17], v[80:83]\n\t"
      "v_pk_mul_f32 v[88:89], v[88:89], v[10:11] op_sel:[0,0] op_sel_hi:[1,0]\n\t"
      "v_pk_mul_f32 v[90:91], v[90:91], v[10:11] op_sel:[0,0] op_sel_hi:[1,0]\n\t"
      "buffer_load_dwordx4 v250, s[36:39], s69 offen offset:2048 sc1 lds\n\t"
      "v_mfma_f32_16x16x16_bf16 v[84:87], v[154:155], v[16:17], v[84:87]\n\t"
      "v_pk_mul_f32 v[92:93], v[92:93], v[10:11] op_sel:[0,0] op_sel_hi:[1,0]\n\t"
      "v_pk_mul_f32 v[94:95], v[94:95], v[10:11] op_sel:[0,0] op_sel_hi:[1,0]\n\t"
      "v_cvt_pk_bf16_f32 v24, v64, v65\n\t"
      "v_cvt_pk_bf16_f32 v25, v66, v67\n\t"
      "v_cvt_pk_bf16_f32 v26, v68, v69\n\t"
      "buffer_load_dwordx4 v250, s[36:39], s69 offen offset:3072 sc1 lds\n\t"
      "v_cvt_pk_bf16_f32 v27, v70, v71\n\t"
      "ds_write_b128 v254, v[24:27]\n\t"
      "v_mfma_f32_16x16x16_bf16 v[88:91], v[156:157], v[16:17], v[88:91]\n\t"
      "v_pk_mul_f32 v[96:97], v[96:97], v[10:11] op_sel:[0,1] op_sel_hi:[1,1]\n\t"
      "v_pk_mul_f32 v[98:99], v[98:99], v[10:11] op_sel:[0,1] op_sel_hi:[1,1]\n\t"
      "v_mfma_f32_16x16x16_bf16 v[92:95], v[158:159], v[16:17], v[92:95]\n\t"
      "s_add_u32 s69, s50, 16384\n\t"
      "buffer_load_dwordx4 a[160:163], v248, s[36:39], s69 offen  sc1\n\t"
      "v_pk_mul_f32 v[100:101], v[100:101], v[10:11] op_sel:[0,1] op_sel_hi:[1,1]\n\t"
      "v_pk_mul_f32 v[102:103], v[102:103], v[10:11] op_sel:[0,1] op_sel_hi:[1,1]\n\t"
      "v_cvt_pk_bf16_f32 v28, v72, v73\n\t"
      "v_cvt_pk_bf16_f32 v29, v74, v75\n\t"
      "v_cvt_pk_bf16_f32 v30, v76, v77\n\t"
      "v_cvt_pk_bf16_f32 v31, v78, v79\n\t"
      "buffer_load_dwordx4 a[164:167], v248, s[36:39], s69 offen offset:1024  sc1\n\t"
      "ds_write_b128 v254, v[28:31] offset:2048\n\t"
      "v_mfma_f32_16x16x16_bf16 v[96:99], v[144:145], v[18:19], v[96:99]\n\t"
      "v_pk_mul_f32 v[104:105], v[104:105], v[10:11] op_sel:[0,1] op_sel_hi:[1,1]\n\t"
      "v_pk_mul_f32 v[106:107], v[106:107], v[10:11] op_sel:[0,1] op_sel_hi:[1,1]\n\t"
      "v_mfma_f32_16x16x16_bf16 v[100:103], v[146:147], v[18:19], v[100:103]\n\t"
      "buffer_load_dwordx4 a[168:171], v248, s[36:39], s69 offen offset:2048  sc1\n\t"
      "v_pk_mul_f32 v[108:109], v[108:109], v[10:11] op_sel:[0,1] op_sel_hi:[1,1]\n\t"
      "v_pk_mul_f32 v[110:111], v[110:111], v[10:11] op_sel:[0,1] op_sel_hi:[1,1]\n\t"
      "v_cvt_pk_bf16_f32 v24, v80, v81\n\t"
      "v_cvt_pk_bf16_f32 v25, v82, v83\n\t"
      "v_cvt_pk_bf16_f32 v26, v84, v85\n\t"
      "v_cvt_pk_bf16_f32 v27, v86, v87\n\t"
      "ds_write_b128 v254, v[24:27] offset:4096\n\t"
      "buffer_load_dwordx4 a[172:175], v248, s[36:39], s69 offen offset:3072  sc1\n\t"
      "v_mfma_f32_16x16x16_bf16 v[104:107], v[148:149], v[18:19], v[104:107]\n\t"
      "v_pk_mul_f32 v[112:113], v[112:113], v[10:11] op_sel:[0,1] op_sel_hi:[1,1]\n\t"
      "v_pk_mul_f32 v[114:115], v[114:115], v[10:11] op_sel:[0,1] op_sel_hi:[1,1]\n\t"
      "v_mfma_f32_16x16x16_bf16 v[108:111], v[150:151], v[18:19], v[108:111]\n\t"
      "v_pk_mul_f32 v[116:117], v[116:117], v[10:11] op_sel:[0,1] op_sel_hi:[1,1]\n\t"
      "s_add_u32 s69, s50, 20480\n\t"
      "buffer_load_dwordx4 a[176:179], v248, s[36:39], s69 offen  sc1\n\t"
      "v_pk_mul_f32 v[118:119], v[118:119], v[10:11] op_sel:[0,1] op_sel_hi:[1,1]\n\t"
      "v_cvt_pk_bf16_f32 v28, v88, v89\n\t"
      "v_cvt_pk_bf16_f32 v29, v90, v91\n\t"
      "v_cvt_pk_bf16_f32 v30, v92, v93\n\t"
      "v_cvt_pk_bf16_f32 v31, v94, v95\n\t"
      "ds_write_b128 v254, v[28:31] offset:6144\n\t"
      "v_mfma_f32_16x16x16_bf16 v[112:115], v[152:153], v[18:19], v[112:115]\n\t"
      "buffer_load_dwordx4 a[180:183], v248, s[36:39], s69 offen offset:1024  sc1\n\t"
      "v_pk_mul_f32 v[120:121], v[120:121], v[10:11] op_sel:[0,1] op_sel_hi:[1,1]\n\t"
      "v_pk_mul_f32 v[122:123], v[122:123], v[10:11] op_sel:[0,1] op_sel_hi:[1,1]\n\t"
      "v_mfma_f32_16x16x16_bf16 v[116:119], v[154:155], v[18:19], v[116:119]\n\t"
      "v_pk_mul_f32 v[124:125], v[124:125], v[10:11] op_sel:[0,1] op_sel_hi:[1,1]\n\t"
      "v_pk_mul_f32 v[126:127], v[126:127], v[10:11] op_sel:[0,1] op_sel_hi:[1,1]\n\t"
      "buffer_load_dwordx4 a[184:187], v248, s[36:39], s69 offen offset:2048  sc1\n\t"
      "v_cvt_pk_bf16_f32 v24, v96, v97\n\t"
      "v_cvt_pk_bf16_f32 v25, v98, v99\n\t"
      "v_cvt_pk_bf16_f32 v26, v100, v101\n\t"
      "v_cvt_pk_bf16_f32 v27, v102, v103\n\t"
      "ds_write_b128 v254, v[24:27] offset:1024\n\t"
      "v_mfma_f32_16x16x16_bf16 v[120:123], v[156:157], v[18:19], v[120:123]\n\t"
      "buffer_load_dwordx4 a[188:191], v248, s[36:39], s69 offen offset:3072  sc1\n\t"
      "v_mfma_f32_16x16x16_bf16 v[124:127], v[158:159], v[18:19], v[124:127]\n\t"
      "v_cvt_pk_bf16_f32 v28, v104, v105\n\t"
      "v_cvt_pk_bf16_f32 v29, v106, v107\n\t"
      "v_cvt_pk_bf16_f32 v30, v108, v109\n\t"
      "v_cvt_pk_bf16_f32 v31, v110, v111\n\t"
      "ds_write_b128 v254, v[28:31] offset:3072\n\t"
      "v_cvt_pk_bf16_f32 v24, v112, v113\n\t"
      "v_cvt_pk_bf16_f32 v25, v114, v115\n\t"
      "v_cvt_pk_bf16_f32 v26, v116, v117\n\t"
      "v_cvt_pk_bf16_f32 v27, v118, v119\n\t"
      "ds_write_b128 v254, v[24:27] offset:5120\n\t"
      "v_cvt_pk_bf16_f32 v28, v120, v121\n\t"
      "v_cvt_pk_bf16_f32 v29, v122, v123\n\t"
      "v_cvt_pk_bf16_f32 v30, v124, v125\n\t"
      "v_cvt_pk_bf16_f32 v31, v126, v127\n\t"
      "ds_write_b128 v254, v[28:31] offset:7168\n\t"
      "s_waitcnt vmcnt(40) lgkmcnt(0)\n\t"
      "s_barrier\n\t"
      "s_branch WO%=\n\t"
      "WB1%=:\n\t"
      "v_mfma_f32_16x16x32_bf16 v[0:3], v[128:131], a[160:163], 0\n\t"
      "v_mfma_f32_16x16x32_bf16 v[4:7], v[128:131], a[164:167], 0\n\t"
      "v_mfma_f32_16x16x32_bf16 v[0:3], v[132:135], a[168:171], v[0:3]\n\t"
      "v_mfma_f32_16x16x32_bf16 v[4:7], v[132:135], a[172:175], v[4:7]\n\t"
      "v_mfma_f32_16x16x32_bf16 v[0:3], v[136:139], a[176:179], v[0:3]\n\t"
      "v_mfma_f32_16x16x32_bf16 v[4:7], v[136:139], a[180:183], v[4:7]\n\t"
      "v_mfma_f32_16x16x32_bf16 v[0:3], v[140:143], a[184:187], v[0:3]\n\t"
      "v_mfma_f32_16x16x32_bf16 v[4:7], v[140:143], a[188:191], v[4:7]\n\t"
      "s_add_u32 s51, s51, 262144\n\t"
      "s_add_u32 s52, s52, 262144\n\t"
      "s_add_u32 s69, s50, 8192\n\t"
      "s_add_u32 m0, s61, 0\n\t"
      "s_nop 0\n\t"
      "buffer_load_dwordx4 v250, s[36:39], s69 offen sc1 lds\n\t"
      "s_nop 0\n\t"
      "v_cvt_pk_bf16_f32 v0, v0, v1\n\t"
      "v_cvt_pk_bf16_f32 v2, v2, v3\n\t"
      "v_cvt_pk_bf16_f32 v4, v4, v5\n\t"
      "v_cvt_pk_bf16_f32 v6, v6, v7\n\t"
      "v_and_b32 v1, s74, v0\n\t"
      "v_lshlrev_b32 v0, 16, v0\n\t"
      "v_and_b32 v3, s74, v2\n\t"
      "v_lshlrev_b32 v2, 16, v2\n\t"
      "v_and_b32 v5, s74, v4\n\t"
      "v_lshlrev_b32 v4, 16, v4\n\t"
      "v_and_b32 v7, s74, v6\n\t"
      "v_lshlrev_b32 v6, 16, v6\n\t"
      "buffer_load_dwordx4 v250, s[36:39], s69 offen offset:1024 sc1 lds\n\t"
      "v_pk_mul_f32 v[0:1], v[0:1], s[84:85]\n\t"
      "v_pk_mul_f32 v[2:3], v[2:3], s[84:85]\n\t"
      "v_pk_mul_f32 v[4:5], v[4:5], s[84:85]\n\t"
      "v_pk_mul_f32 v[6:7], v[6:7], s[84:85]\n\t"
      "v_cvt_pk_bf16_f32 v0, v0, v1\n\t"
      "v_cvt_pk_bf16_f32 v2, v2, v3\n\t"
      "v_cvt_pk_bf16_f32 v4, v4, v5\n\t"
      "v_cvt_pk_bf16_f32 v6, v6, v7\n\t"
      "v_and_b32 v1, s74, v0\n\t"
      "v_lshlrev_b32 v0, 16, v0\n\t"
      "v_and_b32 v3, s74, v2\n\t"
      "buffer_load_dwordx4 v250, s[36:39], s69 offen offset:2048 sc1 lds\n\t"
      "v_lshlrev_b32 v2, 16, v2\n\t"
      "v_and_b32 v5, s74, v4\n\t"
      "v_lshlrev_b32 v4, 16, v4\n\t"
      "v_and_b32 v7, s74, v6\n\t"
      "v_lshlrev_b32 v6, 16, v6\n\t"
      "v_max3_f32 v8, v0, v1, v2\n\t"
      "v_max3_f32 v8, v8, v3, v234\n\t"
      "v_max3_f32 v9, v4, v5, v6\n\t"
      "v_max3_f32 v9, v9, v7, v235\n\t"
      "s_nop 1\n\t"
      "v_permlane16_swap_b32 v8, v9\n\t"
      "v_max_f32 v8, v8, v9\n\t"
      "buffer_load_dwordx4 v250, s[36:39], s69 offen offset:3072 sc1 lds\n\t"
      "v_mov_b32 v9, v8\n\t"
      "s_nop 1\n\t"
      "v_permlane16_swap_b32 v8, v9\n\t"
      "v_pk_mul_f32 v[8:9], v[8:9], s[86:87]\n\t"
      "v_pk_fma_f32 v[10:11], v[234:235], s[86:87], v[8:9] neg_lo:[0,0,1] neg_hi:[0,0,1]\n\t"
      "v_pk_fma_f32 v[0:1], v[0:1], s[86:87], v[8:9] op_sel:[0,0,0] op_sel_hi:[1,1,0] neg_lo:[0,0,1] neg_hi:[0,0,1]\n\t"
      "v_pk_fma_f32 v[2:3], v[2:3], s[86:87], v[8:9] op_sel:[0,0,0] op_sel_hi:[1,1,0] neg_lo:[0,0,1] neg_hi:[0,0,1]\n\t"
      "v_pk_fma_f32 v[4:5], v[4:5], s[86:87], v[8:9] op_sel:[0,0,1] op_sel_hi:[1,1,1] neg_lo:[0,0,1] neg_hi:[0,0,1]\n\t"
      "v_pk_fma_f32 v[6:7], v[6:7], s[86:87], v[8:9] op_sel:[0,0,1] op_sel_hi:[1,1,1] neg_lo:[0,0,1] neg_hi:[0,0,1]\n\t"
      "v_exp_f32 v10, v10\n\t"
      "v_exp_f32 v11, v11\n\t"
      "s_add_u32 s69, s50, 12288\n\t"
      "s_add_u32 m0, s61, 4096\n\t"
      "s_nop 0\n\t"
      "buffer_load_dwordx4 v250, s[36:39], s69 offen sc1 lds\n\t"
      "v_exp_f32 v0, v0\n\t"
      "v_exp_f32 v1, v1\n\t"
      "v_exp_f32 v2, v2\n\t"
      "v_exp_f32 v3, v3\n\t"
      "v_exp_f32 v4, v4\n\t"
      "v_exp_f32 v5, v5\n\t"
      "buffer_load_dwordx4 v250, s[36:39], s69 offen offset:1024 sc1 lds\n\t"
      "v_exp_f32 v6, v6\n\t"
      "v_exp_f32 v7, v7\n\t"
      "v_pk_mul_f32 v[12:13], v[236:237], v[10:11]\n\t"
      "v_pk_add_f32 v[14:15], v[0:1], v[2:3]\n\t"
      "v_pk_add_f32 v[16:17], v[4:5], v[6:7]\n\t"
      "v_add_f32 v14, v14, v15\n\t"
      "v_add_f32 v15, v16, v17\n\t"
      "v_fma_f32 v12, v14, 0.5, v12\n\t"
      "v_fma_f32 v13, v15, 0.5, v13\n\t"
      "buffer_load_dwordx4 v250, s[36:39], s69 offen offset:2048 sc1 lds\n\t"
      "s_nop 0\n\t"
      "v_permlane16_swap_b32 v12, v13\n\t"
      "v_add_f32 v12, v12, v13\n\t"
      "v_mov_b32 v13, v12\n\t"
      "s_nop 1\n\t"
      "v_permlane32_swap_b32 v12, v13\n\t"
      "v_add_f32 v12, v12, v13\n\t"
      "v_mov_b32 v13, v12\n\t"
      "s_nop 1\n\t"
      "v_permlane16_swap_b32 v12, v13\n\t"
      "v_rcp_f32 v12, v12\n\t"
      "v_rcp_f32 v13, v13\n\t"
      "s_nop 0\n\t"
      "v_pk_mul_f32 v[10:11], v[10:11], v[12:13]\n\t"
      "buffer_load_dwordx4 v250, s[36:39], s69 offen offset:3072 sc1 lds\n\t"
      "v_pk_mul_f32 v[12:13], v[12:13], 0.5 op_sel_hi:[1,0]\n\t"
      "v_pk_mul_f32 v[0:1], v[0:1], v[12:13] op_sel:[0,0] op_sel_hi:[1,0]\n\t"
      "v_pk_mul_f32 v[2:3], v[2:3], v[12:13] op_sel:[0,0] op_sel_hi:[1,0]\n\t"
      "v_pk_mul_f32 v[4:5], v[4:5], v[12:13] op_sel:[0,1] op_sel_hi:[1,1]\n\t"
      "v_pk_mul_f32 v[6:7], v[6:7], v[12:13] op_sel:[0,1] op_sel_hi:[1,1]\n\t"
      "v_cvt_pk_bf16_f32 v16, v0, v1\n\t"
      "v_cvt_pk_bf16_f32 v17, v2, v3\n\t"
      "v_cvt_pk_bf16_f32 v18, v4, v5\n\t"
      "v_cvt_pk_bf16_f32 v19, v6, v7\n\t"
      "v_pk_mul_f32 v[64:65], v[64:65], v[10:11] op_sel:[0,0] op_sel_hi:[1,0]\n\t"
      "v_pk_mul_f32 v[66:67], v[66:67], v[10:11] op_sel:[0,0] op_sel_hi:[1,0]\n\t"
      "s_add_u32 s69, s50, 16384\n\t"
      "buffer_load_dwordx4 a[160:163], v248, s[36:39], s69 offen  sc1\n\t"
      "v_pk_mul_f32 v[68:69], v[68:69], v[10:11] op_sel:[0,0] op_sel_hi:[1,0]\n\t"
      "v_pk_mul_f32 v[70:71], v[70:71], v[10:11] op_sel:[0,0] op_sel_hi:[1,0]\n\t"
      "v_mfma_f32_16x16x16_bf16 v[64:67], v[144:145], v[16:17], v[64:67]\n\t"
      "v_pk_mul_f32 v[72:73], v[72:73], v[10:11] op_sel:[0,0] op_sel_hi:[1,0]\n\t"
      "v_pk_mul_f32 v[74:75], v[74:75], v[10:11] op_sel:[0,0] op_sel_hi:[1,0]\n\t"
      "v_mfma_f32_16x16x16_bf16 v[68:71], v[146:147], v[16:17], v[68:71]\n\t"
      "v_pk_mul_f32 v[76:77], v[76:77], v[10:11] op_sel:[0,0] op_sel_hi:[1,0]\n\t"
      "v_pk_mul_f32 v[78:79], v[78:79], v[10:11] op_sel:[0,0] op_sel_hi:[1,0]\n\t"
      "v_mfma_f32_16x16x16_bf16 v[72:75], v[148:149], v[16:17], v[72:75]\n\t"
      "buffer_load_dwordx4 a[164:167], v248, s[36:39], s69 offen offset:1024  sc1\n\t"
      "v_pk_mul_f32 v[80:81], v[80:81], v[10:11] op_sel:[0,0] op_sel_hi:[1,0]\n\t"
      "v_pk_mul_f32 v[82:83], v[82:83], v[10:11] op_sel:[0,0] op_sel_hi:[1,0]\n\t"
      "v_mfma_f32_16x16x16_bf16 v[76:79], v[150:151], v[16:17], v[76:79]\n\t"
      "v_pk_mul_f32 v[84:85], v[84:85], v[10:11] op_sel:[0,0] op_sel_hi:[1,0]\n\t"
      "v_pk_mul_f32 v[86:87], v[86:87], v[10:11] op_sel:[0,0] op_sel_hi:[1,0]\n\t"
      "v_mfma_f32_16x16x16_bf16 v[80:83], v[152:153], v[16:17], v[80:83]\n\t"
      "v_pk_mul_f32 v[88:89], v[88:89], v[10:11] op_sel:[0,0] op_sel_hi:[1,0]\n\t"
      "v_pk_mul_f32 v[90:91], v[90:91], v[10:11] op_sel:[0,0] op_sel_hi:[1,0]\n\t"
      "v_mfma_f32_16x16x16_bf16 v[84:87], v[154:155], v[16:17], v[84:87]\n\t"
      "buffer_load_dwordx4 a[168:171], v248, s[36:39], s69 offen offset:2048  sc1\n\t"
      "v_pk_mul_f32 v[92:93], v[92:93], v[10:11] op_sel:[0,0] op_sel_hi:[1,0]\n\t"
      "v_pk_mul_f32 v[94:95], v[94:95], v[10:11] op_sel:[0,0] op_sel_hi:[1,0]\n\t"
      "v_cvt_pk_bf16_f32 v24, v64, v65\n\t"
      "v_cvt_pk_bf16_f32 v25, v66, v67\n\t"
      "v_cvt_pk_bf16_f32 v26, v68, v69\n\t"
      "v_cvt_pk_bf16_f32 v27, v70, v71\n\t"
      "ds_write_b128 v254, v[24:27]\n\t"
      "v_mfma_f32_16x16x16_bf16 v[88:91], v[156:157], v[16:17], v[88:91]\n\t"
      "v_pk_mul_f32 v[96:97], v[96:97], v[10:11] op_sel:[0,1] op_sel_hi:[1,1]\n\t"
      "v_pk_mul_f32 v[98:99], v[98:99], v[10:11] op_sel:[0,1] op_sel_hi:[1,1]\n\t"
      "buffer_load_dwordx4 a[172:175], v248, s[36:39], s69 offen offset:3072  sc1\n\t"
      "v_mfma_f32_16x16x16_bf16 v[92:95], v[158:159], v[16:17], v[92:95]\n\t"
      "v_pk_mul_f32 v[100:101], v[100:101], v[10:11] op_sel:[0,1] op_sel_hi:[1,1]\n\t"
      "v_pk_mul_f32 v[102:103], v[102:103], v[10:11] op_sel:[0,1] op_sel_hi:[1,1]\n\t"
      "v_cvt_pk_bf16_f32 v28, v72, v73\n\t"
      "v_cvt_pk_bf16_f32 v29, v74, v75\n\t"
      "v_cvt_pk_bf16_f32 v30, v76, v77\n\t"
      "v_cvt_pk_bf16_f32 v31, v78, v79\n\t"
      "ds_write_b128 v254, v[28:31] offset:2048\n\t"
      "v_mfma_f32_16x16x16_bf16 v[96:99], v[144:145], v[18:19], v[96:99]\n\t"
      "s_add_u32 s69, s50, 20480\n\t"
      "buffer_load_dwordx4 a[176:179], v248, s[36:39], s69 offen  sc1\n\t"
      "v_pk_mul_f32 v[104:105], v[104:105], v[10:11] op_sel:[0,1] op_sel_hi:[1,1]\n\t"
      "v_pk_mul_f32 v[106:107], v[106:107], v[10:11] op_sel:[0,1] op_sel_hi:[1,1]\n\t"
      "v_mfma_f32_16x16x16_bf16 v[100:103], v[146:147], v[18:19], v[100:103]\n\t"
      "v_pk_mul_f32 v[108:109], v[108:109], v[10:11] op_sel:[0,1] op_sel_hi:[1,1]\n\t"
      "v_pk_mul_f32 v[110:111], v[110:111], v[10:11] op_sel:[0,1] op_sel_hi:[1,1]\n\t"
      "v_cvt_pk_bf16_f32 v24, v80, v81\n\t"
      "v_cvt_pk_bf16_f32 v25, v82, v83\n\t"
      "v_cvt_pk_bf16_f32 v26, v84, v85\n\t"
      "v_cvt_pk_bf16_f32 v27, v86, v87\n\t"
      "ds_write_b128 v254, v[24:27] offset:4096\n\t"
      "v_mfma_f32_16x16x16_bf16 v[104:107], v[148:149], v[18:19], v[104:107]\n\t"
      "buffer_load_dwordx4 a[180:183], v248, s[36:39], s69 offen offset:1024  sc1\n\t"
      "v_pk_mul_f32 v[112:113], v[112:113], v[10:11] op_sel:[0,1] op_sel_hi:[1,1]\n\t"
      "v_pk_mul_f32 v[114:115], v[114:115], v[10:11] op_sel:[0,1] op_sel_hi:[1,1]\n\t"
      "v_mfma_f32_16x16x16_bf16 v[108:111], v[150:151], v[18:19], v[108:111]\n\t"
      "v_pk_mul_f32 v[116:117], v[116:117], v[10:11] op_sel:[0,1] op_sel_hi:[1,1]\n\t"
      "v_pk_mul_f32 v[118:119], v[118:119], v[10:11] op_sel:[0,1] op_sel_hi:[1,1]\n\t"
      "v_cvt_pk_bf16_f32 v28, v88, v89\n\t"
      "v_cvt_pk_bf16_f32 v29, v90, v91\n\t"
      "v_cvt_pk_bf16_f32 v30, v92, v93\n\t"
      "v_cvt_pk_bf16_f32 v31, v94, v95\n\t"
      "buffer_load_dwordx4 a[184:187], v248, s[36:39], s69 offen offset:2048  sc1\n\t"
      "ds_write_b128 v254, v[28:31] offset:6144\n\t"
      "v_mfma_f32_16x16x16_bf16 v[112:115], v[152:153], v[18:19], v[112:115]\n\t"
      "v_pk_mul_f32 v[120:121], v[120:121], v[10:11] op_sel:[0,1] op_sel_hi:[1,1]\n\t"
      "v_pk_mul_f32 v[122:123], v[122:123], v[10:11] op_sel:[0,1] op_sel_hi:[1,1]\n\t"
      "v_mfma_f32_16x16x16_bf16 v[116:119], v[154:155], v[18:19], v[116:119]\n\t"
      "v_pk_mul_f32 v[124:125], v[124:125], v[10:11] op_sel:[0,1] op_sel_hi:[1,1]\n\t"
      "v_pk_mul_f32 v[126:127], v[126:127], v[10:11] op_sel:[0,1] op_sel_hi:[1,1]\n\t"
      "v_cvt_pk_bf16_f32 v24, v96, v97\n\t"
      "v_cvt_pk_bf16_f32 v25, v98, v99\n\t"
      "v_cvt_pk_bf16_f32 v26, v100, v101\n\t"
      "buffer_load_dwordx4 a[188:191], v248, s[36:39], s69 offen offset:3072  sc1\n\t"
      "v_cvt_pk_bf16_f32 v27, v102, v103\n\t"
      "ds_write_b128 v254, v[24:27] offset:1024\n\t"
      "v_mfma_f32_16x16x16_bf16 v[120:123], v[156:157], v[18:19], v[120:123]\n\t"
      "v_mfma_f32_16x16x16_bf16 v[124:127], v[158:159], v[18:19], v[124:127]\n\t"
      "v_cvt_pk_bf16_f32 v28, v104, v105\n\t"
      "v_cvt_pk_bf16_f32 v29, v106, v107\n\t"
      "v_cvt_pk_bf16_f32 v30, v108, v109\n\t"
      "v_cvt_pk_bf16_f32 v31, v110, v111\n\t"
      "ds_write_b128 v254, v[28:31] offset:3072\n\t"
      "v_cvt_pk_bf16_f32 v24, v112, v113\n\t"
      "v_cvt_pk_bf16_f32 v25, v114, v115\n\t"
      "v_cvt_pk_bf16_f32 v26, v116, v117\n\t"
      "v_cvt_pk_bf16_f32 v27, v118, v119\n\t"
      "ds_write_b128 v254, v[24:27] offset:5120\n\t"
      "v_cvt_pk_bf16_f32 v28, v120, v121\n\t"
      "v_cvt_pk_bf16_f32 v29, v122, v123\n\t"
      "v_cvt_pk_bf16_f32 v30, v124, v125\n\t"
      "v_cvt_pk_bf16_f32 v31, v126, v127\n\t"
      "ds_write_b128 v254, v[28:31] offset:7168\n\t"
      "s_waitcnt vmcnt(40) lgkmcnt(0)\n\t"
      "s_barrier\n\t"
      "s_branch WO%=\n\t"
      "WOL1%=:\n\t"
      "buffer_load_dwordx4 v[128:131], v252, s[36:39], s51 offen sc1\n\t"
      "s_waitcnt lgkmcnt(11)\n\t"
      "v_mfma_f32_16x16x32_bf16 v[0:3], a[0:3], v[64:67], 0\n\t"
      "v_mfma_f32_16x16x32_bf16 v[4:7], a[64:67], v[64:67], 0\n\t"
      "ds_read_b128 v[112:115], v255 offset:12288\n\t"
      "s_waitcnt lgkmcnt(11)\n\t"
      "v_mfma_f32_16x16x32_bf16 v[8:11], a[0:3], v[68:71], 0\n\t"
      "v_mfma_f32_16x16x32_bf16 v[12:15], a[64:67], v[68:71], 0\n\t"
      "ds_read_b128 v[116:119], v255 offset:13312\n\t"
      "s_waitcnt lgkmcnt(11)\n\t"
      "v_mfma_f32_16x16x32_bf16 v[0:3], a[4:7], v[72:75], v[0:3]\n\t"
      "v_mfma_f32_16x16x32_bf16 v[4:7], a[68:71], v[72:75], v[4:7]\n\t"
      "ds_read_b128 v[120:123], v255 offset:14336\n\t"
      "s_waitcnt lgkmcnt(11)\n\t"
      "v_mfma_f32_16x16x32_bf16 v[8:11], a[4:7], v[76:79], v[8:11]\n\t"
      "v_mfma_f32_16x16x32_bf16 v[12:15], a[68:71], v[76:79], v[12:15]\n\t"
      "buffer_load_dwordx4 v[132:135], v252, s[36:39], s51 offen offset:512 sc1\n\t"
      "ds_read_b128 v[124:127], v255 offset:15360\n\t"
      "s_waitcnt lgkmcnt(11)\n\t"
      "v_mfma_f32_16x16x32_bf16 v[0:3], a[8:11], v[80:83], v[0:3]\n\t"
      "v_mfma_f32_16x16x32_bf16 v[4:7], a[72:75], v[80:83], v[4:7]\n\t"
      "ds_read_b128 v[64:67], v255 offset:16384\n\t"
      "s_waitcnt lgkmcnt(11)\n\t"
      "v_mfma_f32_16x16x32_bf16 v[8:11], a[8:11], v[84:87], v[8:11]\n\t"
      "v_mfma_f32_16x16x32_bf16 v[12:15], a[72:75], v[84:87], v[12:15]\n\t"
      "ds_read_b128 v[68:71], v255 offset:17408\n\t"
      "s_waitcnt lgkmcnt(11)\n\t"
      "v_mfma_f32_16x16x32_bf16 v[0:3], a[12:15], v[88:91], v[0:3]\n\t"
      "v_mfma_f32_16x16x32_bf16 v[4:7], a[76:79], v[88:91], v[4:7]\n\t"
      "ds_read_b128 v[72:75], v255 offset:18432\n\t"
      "s_waitcnt lgkmcnt(11)\n\t"
      "v_mfma_f32_16x16x32_bf16 v[8:11], a[12:15], v[92:95], v[8:11]\n\t"
      "v_mfma_f32_16x16x32_bf16 v[12:15], a[76:79], v[92:95], v[12:15]\n\t"
      "buffer_load_dwordx4 v[136:139], v252, s[36:39], s51 offen offset:1024 sc1\n\t"
      "ds_read_b128 v[76:79], v255 offset:19456\n\t"
      "s_waitcnt lgkmcnt(11)\n\t"
      "v_mfma_f32_16x16x32_bf16 v[0:3], a[16:19], v[96:99], v[0:3]\n\t"
      "v_mfma_f32_16x16x32_bf16 v[4:7], a[80:83], v[96:99], v[4:7]\n\t"
      "ds_read_b128 v[80:83], v255 offset:20480\n\t"
      "s_waitcnt lgkmcnt(11)\n\t"
      "v_mfma_f32_16x16x32_bf16 v[8:11], a[16:19], v[100:103], v[8:11]\n\t"
      "v_mfma_f32_16x16x32_bf16 v[12:15], a[80:83], v[100:103], v[12:15]\n\t"
      "ds_read_b128 v[84:87], v255 offset:21504\n\t"
      "s_waitcnt lgkmcnt(11)\n\t"
      "v_mfma_f32_16x16x32_bf16 v[0:3], a[20:23], v[104:107], v[0:3]\n\t"
      "v_mfma_f32_16x16x32_bf16 v[4:7], a[84:87], v[104:107], v[4:7]\n\t"
      "ds_read_b128 v[88:91], v255 offset:22528\n\t"
      "s_waitcnt lgkmcnt(11)\n\t"
      "v_mfma_f32_16x16x32_bf16 v[8:11], a[20:23], v[108:111], v[8:11]\n\t"
      "v_mfma_f32_16x16x32_bf16 v[12:15], a[84:87], v[108:111], v[12:15]\n\t"
      "buffer_load_dwordx4 v[140:143], v252, s[36:39], s51 offen offset:1536 sc1\n\t"
      "ds_read_b128 v[92:95], v255 offset:23552\n\t"
      "s_waitcnt lgkmcnt(11)\n\t"
      "v_mfma_f32_16x16x32_bf16 v[0:3], a[24:27], v[112:115], v[0:3]\n\t"
      "v_mfma_f32_16x16x32_bf16 v[4:7], a[88:91], v[112:115], v[4:7]\n\t"
      "ds_read_b128 v[96:99], v255 offset:24576\n\t"
      "s_waitcnt lgkmcnt(11)\n\t"
      "v_mfma_f32_16x16x32_bf16 v[8:11], a[24:27], v[116:119], v[8:11]\n\t"
      "v_mfma_f32_16x16x32_bf16 v[12:15], a[88:91], v[116:119], v[12:15]\n\t"
      "ds_read_b128 v[100:103], v255 offset:25600\n\t"
      "s_waitcnt lgkmcnt(11)\n\t"
      "v_mfma_f32_16x16x32_bf16 v[0:3], a[28:31], v[120:123], v[0:3]\n\t"
      "v_mfma_f32_16x16x32_bf16 v[4:7], a[92:95], v[120:123], v[4:7]\n\t"
      "ds_read_b128 v[104:107], v255 offset:26624\n\t"
      "s_waitcnt lgkmcnt(11)\n\t"
      "v_mfma_f32_16x16x32_bf16 v[8:11], a[28:31], v[124:127], v[8:11]\n\t"
      "buffer_load_dwordx4 v[144:147], v253, s[36:39], s52 offen sc1\n\t"
      "v_mfma_f32_16x16x32_bf16 v[12:15], a[92:95], v[124:127], v[12:15]\n\t"
      "ds_read_b128 v[108:111], v255 offset:27648\n\t"
      "s_waitcnt lgkmcnt(11)\n\t"
      "v_mfma_f32_16x16x32_bf16 v[0:3], a[32:35], v[64:67], v[0:3]\n\t"
      "v_mfma_f32_16x16x32_bf16 v[4:7], a[96:99], v[64:67], v[4:7]\n\t"
      "ds_read_b128 v[112:115], v255 offset:28672\n\t"
      "s_waitcnt lgkmcnt(11)\n\t"
      "v_mfma_f32_16x16x32_bf16 v[8:11], a[32:35], v[68:71], v[8:11]\n\t"
      "v_mfma_f32_16x16x32_bf16 v[12:15], a[96:99], v[68:71], v[12:15]\n\t"
      "ds_read_b128 v[116:119], v255 offset:29696\n\t"
      "s_waitcnt lgkmcnt(11)\n\t"
      "v_mfma_f32_16x16x32_bf16 v[0:3], a[36:39], v[72:75], v[0:3]\n\t"
      "v_mfma_f32_16x16x32_bf16 v[4:7], a[100:103], v[72:75], v[4:7]\n\t"
      "ds_read_b128 v[120:123], v255 offset:30720\n\t"
      "s_waitcnt lgkmcnt(11)\n\t"
      "v_mfma_f32_16x16x32_bf16 v[8:11], a[36:39], v[76:79], v[8:11]\n\t"
      "buffer_load_dwordx4 v[148:151], v253, s[36:39], s52 offen offset:512 sc1\n\t"
      "v_mfma_f32_16x16x32_bf16 v[12:15], a[100:103], v[76:79], v[12:15]\n\t"
      "ds_read_b128 v[124:127], v255 offset:31744\n\t"
      "s_waitcnt lgkmcnt(11)\n\t"
      "v_mfma_f32_16x16x32_bf16 v[0:3], a[40:43], v[80:83], v[0:3]\n\t"
      "v_mfma_f32_16x16x32_bf16 v[4:7], a[104:107], v[80:83], v[4:7]\n\t"
      "s_waitcnt lgkmcnt(10)\n\t"
      "v_mfma_f32_16x16x32_bf16 v[8:11], a[40:43], v[84:87], v[8:11]\n\t"
      "v_mfma_f32_16x16x32_bf16 v[12:15], a[104:107], v[84:87], v[12:15]\n\t"
      "s_waitcnt lgkmcnt(9)\n\t"
      "v_mfma_f32_16x16x32_bf16 v[0:3], a[44:47], v[88:91], v[0:3]\n\t"
      "v_mfma_f32_16x16x32_bf16 v[4:7], a[108:111], v[88:91], v[4:7]\n\t"
      "s_waitcnt lgkmcnt(8)\n\t"
      "v_mfma_f32_16x16x32_bf16 v[8:11], a[44:47], v[92:95], v[8:11]\n\t"
      "buffer_load_dwordx4 v[152:155], v253, s[36:39], s52 offen offset:1024 sc1\n\t"
      "v_mfma_f32_16x16x32_bf16 v[12:15], a[108:111], v[92:95], v[12:15]\n\t"
      "s_waitcnt lgkmcnt(7)\n\t"
      "v_mfma_f32_16x16x32_bf16 v[0:3], a[48:51], v[96:99], v[0:3]\n\t"
      "v_mfma_f32_16x16x32_bf16 v[4:7], a[112:115], v[96:99], v[4:7]\n\t"
      "s_waitcnt lgkmcnt(6)\n\t"
      "v_mfma_f32_16x16x32_bf16 v[8:11], a[48:51], v[100:103], v[8:11]\n\t"
      "v_mfma_f32_16x16x32_bf16 v[12:15], a[112:115], v[100:103], v[12:15]\n\t"
      "s_waitcnt lgkmcnt(5)\n\t"
      "v_mfma_f32_16x16x32_bf16 v[0:3], a[52:55], v[104:107], v[0:3]\n\t"
      "v_mfma_f32_16x16x32_bf16 v[4:7], a[116:119], v[104:107], v[4:7]\n\t"
      "s_waitcnt lgkmcnt(4)\n\t"
      "v_mfma_f32_16x16x32_bf16 v[8:11], a[52:55], v[108:111], v[8:11]\n\t"
      "v_mfma_f32_16x16x32_bf16 v[12:15], a[116:119], v[108:111], v[12:15]\n\t"
      "buffer_load_dwordx4 v[156:159], v253, s[36:39], s52 offen offset:1536 sc1\n\t"
      "s_waitcnt lgkmcnt(3)\n\t"
      "v_mfma_f32_16x16x32_bf16 v[0:3], a[56:59], v[112:115], v[0:3]\n\t"
      "v_mfma_f32_16x16x32_bf16 v[4:7], a[120:123], v[112:115], v[4:7]\n\t"
      "s_waitcnt lgkmcnt(2)\n\t"
      "v_mfma_f32_16x16x32_bf16 v[8:11], a[56:59], v[116:119], v[8:11]\n\t"
      "v_mfma_f32_16x16x32_bf16 v[12:15], a[120:123], v[116:119], v[12:15]\n\t"
      "s_waitcnt lgkmcnt(1)\n\t"
      "v_mfma_f32_16x16x32_bf16 v[0:3], a[60:63], v[120:123], v[0:3]\n\t"
      "v_mfma_f32_16x16x32_bf16 v[4:7], a[124:127], v[120:123], v[4:7]\n\t"
      "s_waitcnt lgkmcnt(0)\n\t"
      "v_mfma_f32_16x16x32_bf16 v[8:11], a[60:63], v[124:127], v[8:11]\n\t"
      "v_mfma_f32_16x16x32_bf16 v[12:15], a[124:127], v[124:127], v[12:15]\n\t"
      "v_xor_b32 v254, 0x8000, v254\n\t"
      "v_xor_b32 v255, 0x8000, v255\n\t"
      "s_add_u32 s69, s53, 0x4000\n\t"
      "s_add_u32 s48, s48, 4\n\t"
      "s_waitcnt vmcnt(40)\n\t"
      "v_cvt_pk_bf16_f32 v0, v0, v1\n\t"
      "v_cvt_pk_bf16_f32 v1, v2, v3\n\t"
      "v_cvt_pk_bf16_f32 v2, v4, v5\n\t"
      "v_cvt_pk_bf16_f32 v3, v6, v7\n\t"
      "v_min_u32 v0, -2, v0\n\t"
      "v_min_u32 v1, -2, v1\n\t"
      "v_min_u32 v2, -2, v2\n\t"
      "v_min_u32 v3, -2, v3\n\t"
      "buffer_store_dwordx4 v[0:3], v238, s[36:39], s53 offen sc1\n\t"
      "v_cvt_pk_bf16_f32 v8, v8, v9\n\t"
      "v_cvt_pk_bf16_f32 v9, v10, v11\n\t"
      "v_cvt_pk_bf16_f32 v10, v12, v13\n\t"
      "v_cvt_pk_bf16_f32 v11, v14, v15\n\t"
      "v_min_u32 v8, -2, v8\n\t"
      "v_min_u32 v9, -2, v9\n\t"
      "v_min_u32 v10, -2, v10\n\t"
      "v_min_u32 v11, -2, v11\n\t"
      "buffer_store_dwordx4 v[8:11], v238, s[36:39], s69 offen sc1\n\t"
      "s_add_u32 s53, s53, 1048576\n\t"
      "s_nop 1\n\t"
      "s_branch WTOP%=\n\t"
      "WR2%=:\n\t"
      "v_cvt_pk_bf16_f32 v204, v204, v205\n\t"
      "v_and_b32 v205, s74, v204\n\t"
      "v_lshlrev_b32 v204, 16, v204\n\t"
      "v_add_f32 v205, 0x3727c5ac, v205\n\t"
      "v_pk_add_f32 v[40:41], v[192:193], v[204:205] op_sel_hi:[1,0] neg_lo:[0,1] neg_hi:[0,1]\n\t"
      "v_pk_add_f32 v[42:43], v[194:195], v[204:205] op_sel_hi:[1,0] neg_lo:[0,1] neg_hi:[0,1]\n\t"
      "v_pk_add_f32 v[44:45], v[196:197], v[204:205] op_sel_hi:[1,0] neg_lo:[0,1] neg_hi:[0,1]\n\t"
      "v_pk_add_f32 v[46:47], v[198:199], v[204:205] op_sel_hi:[1,0] neg_lo:[0,1] neg_hi:[0,1]\n\t"
      "v_cvt_pk_bf16_f32 v205, v205, v205\n\t"
      "v_and_b32 v205, s74, v205\n\t"
      "v_sqrt_f32 v205, v205\n\t"
      "v_cvt_pk_bf16_f32 v40, v40, v41\n\t"
      "v_cvt_pk_bf16_f32 v42, v42, v43\n\t"
      "v_cvt_pk_bf16_f32 v44, v44, v45\n\t"
      "v_cvt_pk_bf16_f32 v46, v46, v47\n\t"
      "v_cvt_pk_bf16_f32 v205, v205, v205\n\t"
      "v_and_b32 v205, s74, v205\n\t"
      "v_rcp_f32 v204, v205\n\t"
      "v_and_b32 v41, s74, v40\n\t"
      "v_lshlrev_b32 v40, 16, v40\n\t"
      "v_and_b32 v43, s74, v42\n\t"
      "v_lshlrev_b32 v42, 16, v42\n\t"
      "v_and_b32 v45, s74, v44\n\t"
      "v_lshlrev_b32 v44, 16, v44\n\t"
      "v_and_b32 v47, s74, v46\n\t"
      "v_lshlrev_b32 v46, 16, v46\n\t"
      "v_pk_mul_f32 v[40:41], v[40:41], v[204:205] op_sel_hi:[1,0]\n\t"
      "v_pk_mul_f32 v[42:43], v[42:43], v[204:205] op_sel_hi:[1,0]\n\t"
      "v_pk_mul_f32 v[44:45], v[44:45], v[204:205] op_sel_hi:[1,0]\n\t"
      "v_pk_mul_f32 v[46:47], v[46:47], v[204:205] op_sel_hi:[1,0]\n\t"
      "v_cvt_pk_bf16_f32 v52, v40, v41\n\t"
      "v_cvt_pk_bf16_f32 v53, v42, v43\n\t"
      "v_cvt_pk_bf16_f32 v54, v44, v45\n\t"
      "v_cvt_pk_bf16_f32 v55, v46, v47\n\t"
      "v_and_b32 v33, s74, v52\n\t"
      "v_lshlrev_b32 v32, 16, v52\n\t"
      "v_and_b32 v35, s74, v53\n\t"
      "v_lshlrev_b32 v34, 16, v53\n\t"
      "v_and_b32 v37, s74, v54\n\t"
      "v_lshlrev_b32 v36, 16, v54\n\t"
      "v_and_b32 v39, s74, v55\n\t"
      "v_lshlrev_b32 v38, 16, v55\n\t"
      "v_pk_mul_f32 v[40:41], v[32:33], v[240:241]\n\t"
      "v_pk_fma_f32 v[40:41], v[34:35], v[242:243], v[40:41]\n\t"
      "v_pk_fma_f32 v[40:41], v[36:37], v[244:245], v[40:41]\n\t"
      "v_pk_fma_f32 v[40:41], v[38:39], v[246:247], v[40:41]\n\t"
      "v_add_f32 v40, v40, v41\n\t"
      "s_lshl_b32 s73, s49, 5\n\t"
      "s_add_u32 s73, s73, s60\n\t"
      "s_lshl_b32 s69, s73, 2\n\t"
      "s_add_u32 s69, s69, s57\n\t"
      "s_lshl_b32 s70, s73, 7\n\t"
      "s_add_u32 s70, s70, s57\n\t"
      "s_sub_u32 s70, s70, 212992\n\t"
      "s_cmp_ge_u32 s49, 56\n\t"
      "s_cselect_b32 s70, s70, s69\n\t"
      "v_add_f32_dpp v40, v40, v40 quad_perm:[1,0,3,2] row_mask:0xf bank_mask:0xf\n\t"
      "s_nop 1\n\t"
      "v_add_f32_dpp v40, v40, v40 quad_perm:[2,3,0,1] row_mask:0xf bank_mask:0xf\n\t"
      "s_nop 1\n\t"
      "v_add_f32_dpp v40, v40, v40 row_ror:4 row_mask:0xf bank_mask:0xf\n\t"
      "s_nop 1\n\t"
      "v_add_f32_dpp v40, v40, v40 row_ror:8 row_mask:0xf bank_mask:0xf\n\t"
      "v_mov_b32 v41, v40\n\t"
      "s_nop 1\n\t"
      "v_permlane16_swap_b32 v40, v41\n\t"
      "v_add_f32 v40, v40, v41\n\t"
      "v_mov_b32 v41, v40\n\t"
      "s_nop 1\n\t"
      "v_permlane32_swap_b32 v40, v41\n\t"
      "v_add_f32 v40, v40, v41\n\t"
      "v_cvt_pk_bf16_f32 v40, v40, v40\n\t"
      "v_and_b32 v40, s74, v40\n\t"
      "s_mov_b64 exec, 1\n\t"
      "buffer_store_dword v40, off, s[36:39], s70 sc1\n\t"
      "s_mov_b64 exec, -1\n\t"
      "v_pk_add_f32 v[192:193], v[192:193], v[32:33]\n\t"
      "v_pk_add_f32 v[194:195], v[194:195], v[34:35]\n\t"
      "v_pk_add_f32 v[196:197], v[196:197], v[36:37]\n\t"
      "v_pk_add_f32 v[198:199], v[198:199], v[38:39]\n\t"
      "v_readfirstlane_b32 s68, v40\n\t"
      "v_cvt_pk_bf16_f32 v56, v192, v193\n\t"
      "v_cvt_pk_bf16_f32 v57, v194, v195\n\t"
      "v_cvt_pk_bf16_f32 v58, v196, v197\n\t"
      "v_cvt_pk_bf16_f32 v59, v198, v199\n\t"
      "s_add_u32 s67, s69, 8192\n\t"
      "s_lshl_b32 s73, s73, 10\n\t"
      "s_add_u32 s69, s73, s58\n\t"
      "s_add_u32 s70, s73, s59\n\t"
      "buffer_store_dwordx2 v[56:57], v249, s[44:47], s73 offen sc1\n\t"
      "buffer_store_dwordx2 v[52:53], v249, s[36:39], s69 offen sc1\n\t"
      "buffer_store_dwordx2 v[200:201], v249, s[36:39], s70 offen sc1\n\t"
      "buffer_store_dwordx2 v[58:59], v249, s[44:47], s73 offen offset:512 sc1\n\t"
      "buffer_store_dwordx2 v[54:55], v249, s[36:39], s69 offen offset:512 sc1\n\t"
      "buffer_store_dwordx2 v[202:203], v249, s[36:39], s70 offen offset:512 sc1\n\t"
      "s_mov_b32 s49, s89\n\t"
      "s_add_u32 s89, s89, 16\n\t"
      "s_lshr_b32 s69, s89, 4\n\t"
      "s_cmp_eq_u32 s69, 3\n\t"
      "s_cselect_b32 s89, s93, s89\n\t"
      "s_cmp_ge_u32 s49, 48\n\t"
      "s_cselect_b32 s60, s94, s60\n\t"
      "s_cselect_b32 s54, s95, s54\n\t"
      "s_mov_b32 s65, 0\n\t"
      "s_branch WGO2%=\n\t"
      "WOM1%=:\n\t"
      "s_mov_b32 s65, 2\n\t"
      "s_cmp_lg_u32 s96, 0\n\t"
      "s_cbranch_scc1 WOL3%=\n\t"
      "WOL2%=:\n\t"
      "s_waitcnt lgkmcnt(11)\n\t"
      "v_mfma_f32_16x16x32_bf16 v[0:3], a[0:3], v[64:67], 0\n\t"
      "v_mfma_f32_16x16x32_bf16 v[16:19], a[224:227], v[160:163], 0\n\t"
      "v_mfma_f32_16x16x32_bf16 v[4:7], a[64:67], v[64:67], 0\n\t"
      "v_mfma_f32_16x16x32_bf16 v[20:23], a[228:231], v[160:163], 0\n\t"
      "v_and_b32 v193, s74, v224\n\t"
      "v_lshlrev_b32 v192, 16, v224\n\t"
      "ds_read_b128 v[112:115], v255 offset:12288\n\t"
      "s_waitcnt lgkmcnt(11)\n\t"
      "v_mfma_f32_16x16x32_bf16 v[8:11], a[0:3], v[68:71], 0\n\t"
      "v_mfma_f32_16x16x32_bf16 v[16:19], a[224:227], v[164:167], v[16:19]\n\t"
      "v_mfma_f32_16x16x32_bf16 v[12:15], a[64:67], v[68:71], 0\n\t"
      "v_mfma_f32_16x16x32_bf16 v[20:23], a[228:231], v[164:167], v[20:23]\n\t"
      "v_and_b32 v195, s74, v225\n\t"
      "v_lshlrev_b32 v194, 16, v225\n\t"
      "ds_read_b128 v[116:119], v255 offset:13312\n\t"
      "s_waitcnt lgkmcnt(11)\n\t"
      "v_mfma_f32_16x16x32_bf16 v[0:3], a[4:7], v[72:75], v[0:3]\n\t"
      "v_mfma_f32_16x16x32_bf16 v[16:19], a[224:227], v[168:171], v[16:19]\n\t"
      "v_mfma_f32_16x16x32_bf16 v[4:7], a[68:71], v[72:75], v[4:7]\n\t"
      "v_mfma_f32_16x16x32_bf16 v[20:23], a[228:231], v[168:171], v[20:23]\n\t"
      "v_and_b32 v197, s74, v226\n\t"
      "v_lshlrev_b32 v196, 16, v226\n\t"
      "ds_read_b128 v[120:123], v255 offset:14336\n\t"
      "s_waitcnt lgkmcnt(11)\n\t"
      "v_mfma_f32_16x16x32_bf16 v[8:11], a[4:7], v[76:79], v[8:11]\n\t"
      "v_mfma_f32_16x16x32_bf16 v[16:19], a[224:227], v[172:175], v[16:19]\n\t"
      "v_mfma_f32_16x16x32_bf16 v[12:15], a[68:71], v[76:79], v[12:15]\n\t"
      "v_mfma_f32_16x16x32_bf16 v[20:23], a[228:231], v[172:175], v[20:23]\n\t"
      "v_and_b32 v199, s74, v227\n\t"
      "v_lshlrev_b32 v198, 16, v227\n\t"
      "ds_read_b128 v[124:127], v255 offset:15360\n\t"
      "s_waitcnt lgkmcnt(11)\n\t"
      "v_mfma_f32_16x16x32_bf16 v[0:3], a[8:11], v[80:83], v[0:3]\n\t"
      "v_mfma_f32_16x16x32_bf16 v[16:19], a[224:227], v[176:179], v[16:19]\n\t"
      "v_mfma_f32_16x16x32_bf16 v[4:7], a[72:75], v[80:83], v[4:7]\n\t"
      "v_mfma_f32_16x16x32_bf16 v[20:23], a[228:231], v[176:179], v[20:23]\n\t"
      "ds_read_b128 v[64:67], v255 offset:16384\n\t"
      "s_waitcnt lgkmcnt(11)\n\t"
      "v_mfma_f32_16x16x32_bf16 v[8:11], a[8:11], v[84:87], v[8:11]\n\t"
      "v_mfma_f32_16x16x32_bf16 v[16:19], a[224:227], v[180:183], v[16:19]\n\t"
      "v_mfma_f32_16x16x32_bf16 v[12:15], a[72:75], v[84:87], v[12:15]\n\t"
      "v_mfma_f32_16x16x32_bf16 v[20:23], a[228:231], v[180:183], v[20:23]\n\t"
      "ds_read_b128 v[68:71], v255 offset:17408\n\t"
      "s_waitcnt lgkmcnt(11)\n\t"
      "v_mfma_f32_16x16x32_bf16 v[0:3], a[12:15], v[88:91], v[0:3]\n\t"
      "v_mfma_f32_16x16x32_bf16 v[16:19], a[224:227], v[184:187], v[16:19]\n\t"
      "v_mfma_f32_16x16x32_bf16 v[4:7], a[76:79], v[88:91], v[4:7]\n\t"
      "v_mfma_f32_16x16x32_bf16 v[20:23], a[228:231], v[184:187], v[20:23]\n\t"
      "ds_read_b128 v[72:75], v255 offset:18432\n\t"
      "s_waitcnt lgkmcnt(11)\n\t"
      "v_mfma_f32_16x16x32_bf16 v[8:11], a[12:15], v[92:95], v[8:11]\n\t"
      "v_mfma_f32_16x16x32_bf16 v[16:19], a[224:227], v[188:191], v[16:19]\n\t"
      "v_mfma_f32_16x16x32_bf16 v[12:15], a[76:79], v[92:95], v[12:15]\n\t"
      "v_mfma_f32_16x16x32_bf16 v[20:23], a[228:231], v[188:191], v[20:23]\n\t"
      "ds_read_b128 v[76:79], v255 offset:19456\n\t"
      "s_waitcnt lgkmcnt(11)\n\t"
      "v_mfma_f32_16x16x32_bf16 v[0:3], a[16:19], v[96:99], v[0:3]\n\t"
      "v_cvt_pk_bf16_f32 v32, v16, v17\n\t"
      "v_cvt_pk_bf16_f32 v34, v18, v19\n\t"
      "v_mfma_f32_16x16x32_bf16 v[4:7], a[80:83], v[96:99], v[4:7]\n\t"
      "ds_read_b128 v[80:83], v255 offset:20480\n\t"
      "s_waitcnt lgkmcnt(11)\n\t"
      "v_mfma_f32_16x16x32_bf16 v[8:11], a[16:19], v[100:103], v[8:11]\n\t"
      "v_cvt_pk_bf16_f32 v36, v20, v21\n\t"
      "v_cvt_pk_bf16_f32 v38, v22, v23\n\t"
      "v_mfma_f32_16x16x32_bf16 v[12:15], a[80:83], v[100:103], v[12:15]\n\t"
      "v_and_b32 v33, s74, v32\n\t"
      "v_lshlrev_b32 v32, 16, v32\n\t"
      "ds_read_b128 v[84:87], v255 offset:21504\n\t"
      "s_waitcnt lgkmcnt(11)\n\t"
      "v_mfma_f32_16x16x32_bf16 v[0:3], a[20:23], v[104:107], v[0:3]\n\t"
      "v_and_b32 v35, s74, v34\n\t"
      "v_lshlrev_b32 v34, 16, v34\n\t"
      "v_mfma_f32_16x16x32_bf16 v[4:7], a[84:87], v[104:107], v[4:7]\n\t"
      "v_and_b32 v37, s74, v36\n\t"
      "v_lshlrev_b32 v36, 16, v36\n\t"
      "ds_read_b128 v[88:91], v255 offset:22528\n\t"
      "s_waitcnt lgkmcnt(11)\n\t"
      "v_mfma_f32_16x16x32_bf16 v[8:11], a[20:23], v[108:111], v[8:11]\n\t"
      "v_and_b32 v39, s74, v38\n\t"
      "v_lshlrev_b32 v38, 16, v38\n\t"
      "v_mfma_f32_16x16x32_bf16 v[12:15], a[84:87], v[108:111], v[12:15]\n\t"
      "v_add_f32 v192, v192, v32\n\t"
      "v_add_f32 v193, v193, v33\n\t"
      "ds_read_b128 v[92:95], v255 offset:23552\n\t"
      "s_waitcnt lgkmcnt(11)\n\t"
      "v_mfma_f32_16x16x32_bf16 v[0:3], a[24:27], v[112:115], v[0:3]\n\t"
      "v_add_f32 v194, v194, v34\n\t"
      "v_add_f32 v195, v195, v35\n\t"
      "v_mfma_f32_16x16x32_bf16 v[4:7], a[88:91], v[112:115], v[4:7]\n\t"
      "v_add_f32 v196, v196, v36\n\t"
      "v_add_f32 v197, v197, v37\n\t"
      "ds_read_b128 v[96:99], v255 offset:24576\n\t"
      "s_waitcnt lgkmcnt(11)\n\t"
      "v_mfma_f32_16x16x32_bf16 v[8:11], a[24:27], v[116:119], v[8:11]\n\t"
      "v_add_f32 v198, v198, v38\n\t"
      "v_add_f32 v199, v199, v39\n\t"
      "v_mfma_f32_16x16x32_bf16 v[12:15], a[88:91], v[116:119], v[12:15]\n\t"
      "v_cvt_pk_bf16_f32 v200, v192, v193\n\t"
      "v_cvt_pk_bf16_f32 v201, v194, v195\n\t"
      "ds_read_b128 v[100:103], v255 offset:25600\n\t"
      "s_waitcnt lgkmcnt(11)\n\t"
      "v_mfma_f32_16x16x32_bf16 v[0:3], a[28:31], v[120:123], v[0:3]\n\t"
      "v_cvt_pk_bf16_f32 v202, v196, v197\n\t"
      "v_cvt_pk_bf16_f32 v203, v198, v199\n\t"
      "v_mfma_f32_16x16x32_bf16 v[4:7], a[92:95], v[120:123], v[4:7]\n\t"
      "v_and_b32 v193, s74, v200\n\t"
      "v_lshlrev_b32 v192, 16, v200\n\t"
      "ds_read_b128 v[104:107], v255 offset:26624\n\t"
      "s_waitcnt lgkmcnt(11)\n\t"
      "v_mfma_f32_16x16x32_bf16 v[8:11], a[28:31], v[124:127], v[8:11]\n\t"
      "v_and_b32 v195, s74, v201\n\t"
      "v_lshlrev_b32 v194, 16, v201\n\t"
      "v_mfma_f32_16x16x32_bf16 v[12:15], a[92:95], v[124:127], v[12:15]\n\t"
      "v_and_b32 v197, s74, v202\n\t"
      "v_lshlrev_b32 v196, 16, v202\n\t"
      "ds_read_b128 v[108:111], v255 offset:27648\n\t"
      "s_waitcnt lgkmcnt(11)\n\t"
      "v_mfma_f32_16x16x32_bf16 v[0:3], a[32:35], v[64:67], v[0:3]\n\t"
      "v_and_b32 v199, s74, v203\n\t"
      "v_lshlrev_b32 v198, 16, v203\n\t"
      "v_mfma_f32_16x16x32_bf16 v[4:7], a[96:99], v[64:67], v[4:7]\n\t"
      "v_add_f32 v32, v192, v196\n\t"
      "v_add_f32 v33, v193, v197\n\t"
      "ds_read_b128 v[112:115], v255 offset:28672\n\t"
      "s_waitcnt lgkmcnt(11)\n\t"
      "v_mfma_f32_16x16x32_bf16 v[8:11], a[32:35], v[68:71], v[8:11]\n\t"
      "v_add_f32 v34, v194, v198\n\t"
      "v_add_f32 v35, v195, v199\n\t"
      "v_mfma_f32_16x16x32_bf16 v[12:15], a[96:99], v[68:71], v[12:15]\n\t"
      "v_add_f32 v32, v32, v34\n\t"
      "v_add_f32 v33, v33, v35\n\t"
      "ds_read_b128 v[116:119], v255 offset:29696\n\t"
      "s_waitcnt lgkmcnt(11)\n\t"
      "v_mfma_f32_16x16x32_bf16 v[0:3], a[36:39], v[72:75], v[0:3]\n\t"
      "v_add_f32 v32, v32, v33\n\t"
      "v_mfma_f32_16x16x32_bf16 v[4:7], a[100:103], v[72:75], v[4:7]\n\t"
      "ds_read_b128 v[120:123], v255 offset:30720\n\t"
      "s_waitcnt lgkmcnt(11)\n\t"
      "v_mfma_f32_16x16x32_bf16 v[8:11], a[36:39], v[76:79], v[8:11]\n\t"
      "v_add_f32_dpp v32, v32, v32 quad_perm:[1,0,3,2] row_mask:0xf bank_mask:0xf\n\t"
      "v_mfma_f32_16x16x32_bf16 v[12:15], a[100:103], v[76:79], v[12:15]\n\t"
      "ds_read_b128 v[124:127], v255 offset:31744\n\t"
      "s_waitcnt lgkmcnt(11)\n\t"
      "v_mfma_f32_16x16x32_bf16 v[0:3], a[40:43], v[80:83], v[0:3]\n\t"
      "v_add_f32_dpp v32, v32, v32 quad_perm:[2,3,0,1] row_mask:0xf bank_mask:0xf\n\t"
      "v_mfma_f32_16x16x32_bf16 v[4:7], a[104:107], v[80:83], v[4:7]\n\t"
      "s_waitcnt lgkmcnt(10)\n\t"
      "v_mfma_f32_16x16x32_bf16 v[8:11], a[40:43], v[84:87], v[8:11]\n\t"
      "v_add_f32_dpp v32, v32, v32 row_ror:4 row_mask:0xf bank_mask:0xf\n\t"
      "v_mfma_f32_16x16x32_bf16 v[12:15], a[104:107], v[84:87], v[12:15]\n\t"
      "s_waitcnt lgkmcnt(9)\n\t"
      "v_mfma_f32_16x16x32_bf16 v[0:3], a[44:47], v[88:91], v[0:3]\n\t"
      "v_add_f32_dpp v32, v32, v32 row_ror:8 row_mask:0xf bank_mask:0xf\n\t"
      "v_mov_b32 v33, v32\n\t"
      "v_mfma_f32_16x16x32_bf16 v[4:7], a[108:111], v[88:91], v[4:7]\n\t"
      "s_waitcnt lgkmcnt(8)\n\t"
      "v_mfma_f32_16x16x32_bf16 v[8:11], a[44:47], v[92:95], v[8:11]\n\t"
      "v_permlane16_swap_b32 v32, v33\n\t"
      "v_add_f32 v32, v32, v33\n\t"
      "v_mfma_f32_16x16x32_bf16 v[12:15], a[108:111], v[92:95], v[12:15]\n\t"
      "v_mov_b32 v33, v32\n\t"
      "s_waitcnt lgkmcnt(7)\n\t"
      "v_mfma_f32_16x16x32_bf16 v[0:3], a[48:51], v[96:99], v[0:3]\n\t"
      "v_permlane32_swap_b32 v32, v33\n\t"
      "v_add_f32 v32, v32, v33\n\t"
      "v_mfma_f32_16x16x32_bf16 v[4:7], a[112:115], v[96:99], v[4:7]\n\t"
      "v_mul_f32 v204, 0x3b000000, v32\n\t"
      "v_sub_f32 v40, v192, v204\n\t"
      "s_waitcnt lgkmcnt(6)\n\t"
      "v_mfma_f32_16x16x32_bf16 v[8:11], a[48:51], v[100:103], v[8:11]\n\t"
      "v_sub_f32 v41, v193, v204\n\t"
      "v_sub_f32 v42, v194, v204\n\t"
      "v_mfma_f32_16x16x32_bf16 v[12:15], a[112:115], v[100:103], v[12:15]\n\t"
      "v_sub_f32 v43, v195, v204\n\t"
      "v_sub_f32 v44, v196, v204\n\t"
      "s_waitcnt lgkmcnt(5)\n\t"
      "v_mfma_f32_16x16x32_bf16 v[0:3], a[52:55], v[104:107], v[0:3]\n\t"
      "v_sub_f32 v45, v197, v204\n\t"
      "v_sub_f32 v46, v198, v204\n\t"
      "v_mfma_f32_16x16x32_bf16 v[4:7], a[116:119], v[104:107], v[4:7]\n\t"
      "v_sub_f32 v47, v199, v204\n\t"
      "v_mul_f32 v32, v40, v40\n\t"
      "s_waitcnt lgkmcnt(4)\n\t"
      "v_mfma_f32_16x16x32_bf16 v[8:11], a[52:55], v[108:111], v[8:11]\n\t"
      "v_mul_f32 v33, v41, v41\n\t"
      "v_fma_f32 v32, v42, v42, v32\n\t"
      "v_mfma_f32_16x16x32_bf16 v[12:15], a[116:119], v[108:111], v[12:15]\n\t"
      "v_fma_f32 v33, v43, v43, v33\n\t"
      "v_fma_f32 v32, v44, v44, v32\n\t"
      "s_waitcnt lgkmcnt(3)\n\t"
      "v_mfma_f32_16x16x32_bf16 v[0:3], a[56:59], v[112:115], v[0:3]\n\t"
      "v_fma_f32 v33, v45, v45, v33\n\t"
      "v_fma_f32 v32, v46, v46, v32\n\t"
      "v_mfma_f32_16x16x32_bf16 v[4:7], a[120:123], v[112:115], v[4:7]\n\t"
      "v_fma_f32 v33, v47, v47, v33\n\t"
      "v_add_f32 v32, v32, v33\n\t"
      "s_waitcnt lgkmcnt(2)\n\t"
      "v_mfma_f32_16x16x32_bf16 v[8:11], a[56:59], v[116:119], v[8:11]\n\t"
      "v_add_f32_dpp v32, v32, v32 quad_perm:[1,0,3,2] row_mask:0xf bank_mask:0xf\n\t"
      "v_mfma_f32_16x16x32_bf16 v[12:15], a[120:123], v[116:119], v[12:15]\n\t"
      "s_waitcnt lgkmcnt(1)\n\t"
      "v_mfma_f32_16x16x32_bf16 v[0:3], a[60:63], v[120:123], v[0:3]\n\t"
      "v_add_f32_dpp v32, v32, v32 quad_perm:[2,3,0,1] row_mask:0xf bank_mask:0xf\n\t"
      "v_mfma_f32_16x16x32_bf16 v[4:7], a[124:127], v[120:123], v[4:7]\n\t"
      "s_waitcnt lgkmcnt(0)\n\t"
      "v_mfma_f32_16x16x32_bf16 v[8:11], a[60:63], v[124:127], v[8:11]\n\t"
      "v_add_f32_dpp v32, v32, v32 row_ror:4 row_mask:0xf bank_mask:0xf\n\t"
      "v_mfma_f32_16x16x32_bf16 v[12:15], a[124:127], v[124:127], v[12:15]\n\t"
      "s_nop 0\n\t"
      "v_add_f32_dpp v32, v32, v32 row_ror:8 row_mask:0xf bank_mask:0xf\n\t"
      "v_mov_b32 v33, v32\n\t"
      "s_nop 1\n\t"
      "v_permlane16_swap_b32 v32, v33\n\t"
      "v_add_f32 v32, v32, v33\n\t"
      "v_mov_b32 v33, v32\n\t"
      "s_nop 1\n\t"
      "v_permlane32_swap_b32 v32, v33\n\t"
      "v_add_f32 v32, v32, v33\n\t"
      "v_mul_f32 v205, 0x3b000000, v32\n\t"
      "v_xor_b32 v254, 0x8000, v254\n\t"
      "v_xor_b32 v255, 0x8000, v255\n\t"
      "s_add_u32 s69, s53, 0x4000\n\t"
      "s_add_u32 s48, s48, 4\n\t"
      "s_waitcnt vmcnt(40)\n\t"
      "v_cvt_pk_bf16_f32 v0, v0, v1\n\t"
      "v_cvt_pk_bf16_f32 v1, v2, v3\n\t"
      "v_cvt_pk_bf16_f32 v2, v4, v5\n\t"
      "v_cvt_pk_bf16_f32 v3, v6, v7\n\t"
      "v_min_u32 v0, -2, v0\n\t"
      "v_min_u32 v1, -2, v1\n\t"
      "v_min_u32 v2, -2, v2\n\t"
      "v_min_u32 v3, -2, v3\n\t"
      "buffer_store_dwordx4 v[0:3], v238, s[36:39], s53 offen sc1\n\t"
      "v_cvt_pk_bf16_f32 v8, v8, v9\n\t"
      "v_cvt_pk_bf16_f32 v9, v10, v11\n\t"
      "v_cvt_pk_bf16_f32 v10, v12, v13\n\t"
      "v_cvt_pk_bf16_f32 v11, v14, v15\n\t"
      "v_min_u32 v8, -2, v8\n\t"
      "v_min_u32 v9, -2, v9\n\t"
      "v_min_u32 v10, -2, v10\n\t"
      "v_min_u32 v11, -2, v11\n\t"
      "buffer_store_dwordx4 v[8:11], v238, s[36:39], s69 offen sc1\n\t"
      "s_add_u32 s53, s53, 1048576\n\t"
      "s_nop 1\n\t"
      "s_branch WTOP%=\n\t"
      "WOL3%=:\n\t"
      "buffer_load_dwordx4 v[128:131], v252, s[36:39], s51 offen sc1\n\t"
      "s_waitcnt lgkmcnt(11)\n\t"
      "v_mfma_f32_16x16x32_bf16 v[0:3], a[0:3], v[64:67], 0\n\t"
      "v_mfma_f32_16x16x32_bf16 v[16:19], a[224:227], v[160:163], 0\n\t"
      "v_mfma_f32_16x16x32_bf16 v[4:7], a[64:67], v[64:67], 0\n\t"
      "v_mfma_f32_16x16x32_bf16 v[20:23], a[228:231], v[160:163], 0\n\t"
      "v_and_b32 v193, s74, v224\n\t"
      "v_lshlrev_b32 v192, 16, v224\n\t"
      "ds_read_b128 v[112:115], v255 offset:12288\n\t"
      "s_waitcnt lgkmcnt(11)\n\t"
      "v_mfma_f32_16x16x32_bf16 v[8:11], a[0:3], v[68:71], 0\n\t"
      "v_mfma_f32_16x16x32_bf16 v[16:19], a[224:227], v[164:167], v[16:19]\n\t"
      "v_mfma_f32_16x16x32_bf16 v[12:15], a[64:67], v[68:71], 0\n\t"
      "v_mfma_f32_16x16x32_bf16 v[20:23], a[228:231], v[164:167], v[20:23]\n\t"
      "v_and_b32 v195, s74, v225\n\t"
      "v_lshlrev_b32 v194, 16, v225\n\t"
      "ds_read_b128 v[116:119], v255 offset:13312\n\t"
      "s_waitcnt lgkmcnt(11)\n\t"
      "v_mfma_f32_16x16x32_bf16 v[0:3], a[4:7], v[72:75], v[0:3]\n\t"
      "v_mfma_f32_16x16x32_bf16 v[16:19], a[224:227], v[168:171], v[16:19]\n\t"
      "v_mfma_f32_16x16x32_bf16 v[4:7], a[68:71], v[72:75], v[4:7]\n\t"
      "v_mfma_f32_16x16x32_bf16 v[20:23], a[228:231], v[168:171], v[20:23]\n\t"
      "v_and_b32 v197, s74, v226\n\t"
      "v_lshlrev_b32 v196, 16, v226\n\t"
      "ds_read_b128 v[120:123], v255 offset:14336\n\t"
      "s_waitcnt lgkmcnt(11)\n\t"
      "v_mfma_f32_16x16x32_bf16 v[8:11], a[4:7], v[76:79], v[8:11]\n\t"
      "v_mfma_f32_16x16x32_bf16 v[16:19], a[224:227], v[172:175], v[16:19]\n\t"
      "v_mfma_f32_16x16x32_bf16 v[12:15], a[68:71], v[76:79], v[12:15]\n\t"
      "buffer_load_dwordx4 v[132:135], v252, s[36:39], s51 offen offset:512 sc1\n\t"
      "ds_read_b128 v[124:127], v255 offset:15360\n\t"
      "s_waitcnt lgkmcnt(11)\n\t"
      "v_mfma_f32_16x16x32_bf16 v[0:3], a[8:11], v[80:83], v[0:3]\n\t"
      "v_mfma_f32_16x16x32_bf16 v[20:23], a[228:231], v[172:175], v[20:23]\n\t"
      "v_and_b32 v199, s74, v227\n\t"
      "v_lshlrev_b32 v198, 16, v227\n\t"
      "v_mfma_f32_16x16x32_bf16 v[4:7], a[72:75], v[80:83], v[4:7]\n\t"
      "v_mfma_f32_16x16x32_bf16 v[16:19], a[224:227], v[176:179], v[16:19]\n\t"
      "ds_read_b128 v[64:67], v255 offset:16384\n\t"
      "s_waitcnt lgkmcnt(11)\n\t"
      "v_mfma_f32_16x16x32_bf16 v[8:11], a[8:11], v[84:87], v[8:11]\n\t"
      "v_mfma_f32_16x16x32_bf16 v[20:23], a[228:231], v[176:179], v[20:23]\n\t"
      "v_mfma_f32_16x16x32_bf16 v[12:15], a[72:75], v[84:87], v[12:15]\n\t"
      "v_mfma_f32_16x16x32_bf16 v[16:19], a[224:227], v[180:183], v[16:19]\n\t"
      "ds_read_b128 v[68:71], v255 offset:17408\n\t"
      "s_waitcnt lgkmcnt(11)\n\t"
      "v_mfma_f32_16x16x32_bf16 v[0:3], a[12:15], v[88:91], v[0:3]\n\t"
      "v_mfma_f32_16x16x32_bf16 v[20:23], a[228:231], v[180:183], v[20:23]\n\t"
      "v_mfma_f32_16x16x32_bf16 v[4:7], a[76:79], v[88:91], v[4:7]\n\t"
      "v_mfma_f32_16x16x32_bf16 v[16:19], a[224:227], v[184:187], v[16:19]\n\t"
      "ds_read_b128 v[72:75], v255 offset:18432\n\t"
      "s_waitcnt lgkmcnt(11)\n\t"
      "v_mfma_f32_16x16x32_bf16 v[8:11], a[12:15], v[92:95], v[8:11]\n\t"
      "v_mfma_f32_16x16x32_bf16 v[20:23], a[228:231], v[184:187], v[20:23]\n\t"
      "v_mfma_f32_16x16x32_bf16 v[12:15], a[76:79], v[92:95], v[12:15]\n\t"
      "buffer_load_dwordx4 v[136:139], v252, s[36:39], s51 offen offset:1024 sc1\n\t"
      "ds_read_b128 v[76:79], v255 offset:19456\n\t"
      "s_waitcnt lgkmcnt(11)\n\t"
      "v_mfma_f32_16x16x32_bf16 v[0:3], a[16:19], v[96:99], v[0:3]\n\t"
      "v_mfma_f32_16x16x32_bf16 v[16:19], a[224:227], v[188:191], v[16:19]\n\t"
      "v_mfma_f32_16x16x32_bf16 v[4:7], a[80:83], v[96:99], v[4:7]\n\t"
      "v_mfma_f32_16x16x32_bf16 v[20:23], a[228:231], v[188:191], v[20:23]\n\t"
      "ds_read_b128 v[80:83], v255 offset:20480\n\t"
      "s_waitcnt lgkmcnt(11)\n\t"
      "v_mfma_f32_16x16x32_bf16 v[8:11], a[16:19], v[100:103], v[8:11]\n\t"
      "v_cvt_pk_bf16_f32 v32, v16, v17\n\t"
      "v_cvt_pk_bf16_f32 v34, v18, v19\n\t"
      "v_mfma_f32_16x16x32_bf16 v[12:15], a[80:83], v[100:103], v[12:15]\n\t"
      "ds_read_b128 v[84:87], v255 offset:21504\n\t"
      "s_waitcnt lgkmcnt(11)\n\t"
      "v_mfma_f32_16x16x32_bf16 v[0:3], a[20:23], v[104:107], v[0:3]\n\t"
      "v_cvt_pk_bf16_f32 v36, v20, v21\n\t"
      "v_cvt_pk_bf16_f32 v38, v22, v23\n\t"
      "v_mfma_f32_16x16x32_bf16 v[4:7], a[84:87], v[104:107], v[4:7]\n\t"
      "v_and_b32 v33, s74, v32\n\t"
      "v_lshlrev_b32 v32, 16, v32\n\t"
      "ds_read_b128 v[88:91], v255 offset:22528\n\t"
      "s_waitcnt lgkmcnt(11)\n\t"
      "v_mfma_f32_16x16x32_bf16 v[8:11], a[20:23], v[108:111], v[8:11]\n\t"
      "v_and_b32 v35, s74, v34\n\t"
      "v_lshlrev_b32 v34, 16, v34\n\t"
      "v_mfma_f32_16x16x32_bf16 v[12:15], a[84:87], v[108:111], v[12:15]\n\t"
      "buffer_load_dwordx4 v[140:143], v252, s[36:39], s51 offen offset:1536 sc1\n\t"
      "ds_read_b128 v[92:95], v255 offset:23552\n\t"
      "s_waitcnt lgkmcnt(11)\n\t"
      "v_mfma_f32_16x16x32_bf16 v[0:3], a[24:27], v[112:115], v[0:3]\n\t"
      "v_and_b32 v37, s74, v36\n\t"
      "v_lshlrev_b32 v36, 16, v36\n\t"
      "v_mfma_f32_16x16x32_bf16 v[4:7], a[88:91], v[112:115], v[4:7]\n\t"
      "v_and_b32 v39, s74, v38\n\t"
      "v_lshlrev_b32 v38, 16, v38\n\t"
      "ds_read_b128 v[96:99], v255 offset:24576\n\t"
      "s_waitcnt lgkmcnt(11)\n\t"
      "v_mfma_f32_16x16x32_bf16 v[8:11], a[24:27], v[116:119], v[8:11]\n\t"
      "v_add_f32 v192, v192, v32\n\t"
      "v_add_f32 v193, v193, v33\n\t"
      "v_mfma_f32_16x16x32_bf16 v[12:15], a[88:91], v[116:119], v[12:15]\n\t"
      "v_add_f32 v194, v194, v34\n\t"
      "v_add_f32 v195, v195, v35\n\t"
      "ds_read_b128 v[100:103], v255 offset:25600\n\t"
      "s_waitcnt lgkmcnt(11)\n\t"
      "v_mfma_f32_16x16x32_bf16 v[0:3], a[28:31], v[120:123], v[0:3]\n\t"
      "v_add_f32 v196, v196, v36\n\t"
      "v_add_f32 v197, v197, v37\n\t"
      "v_mfma_f32_16x16x32_bf16 v[4:7], a[92:95], v[120:123], v[4:7]\n\t"
      "v_add_f32 v198, v198, v38\n\t"
      "v_add_f32 v199, v199, v39\n\t"
      "ds_read_b128 v[104:107], v255 offset:26624\n\t"
      "s_waitcnt lgkmcnt(11)\n\t"
      "v_mfma_f32_16x16x32_bf16 v[8:11], a[28:31], v[124:127], v[8:11]\n\t"
      "buffer_load_dwordx4 v[144:147], v253, s[36:39], s52 offen sc1\n\t"
      "v_mfma_f32_16x16x32_bf16 v[12:15], a[92:95], v[124:127], v[12:15]\n\t"
      "v_cvt_pk_bf16_f32 v200, v192, v193\n\t"
      "v_cvt_pk_bf16_f32 v201, v194, v195\n\t"
      "ds_read_b128 v[108:111], v255 offset:27648\n\t"
      "s_waitcnt lgkmcnt(11)\n\t"
      "v_mfma_f32_16x16x32_bf16 v[0:3], a[32:35], v[64:67], v[0:3]\n\t"
      "v_cvt_pk_bf16_f32 v202, v196, v197\n\t"
      "v_cvt_pk_bf16_f32 v203, v198, v199\n\t"
      "v_mfma_f32_16x16x32_bf16 v[4:7], a[96:99], v[64:67], v[4:7]\n\t"
      "v_and_b32 v193, s74, v200\n\t"
      "v_lshlrev_b32 v192, 16, v200\n\t"
      "ds_read_b128 v[112:115], v255 offset:28672\n\t"
      "s_waitcnt lgkmcnt(11)\n\t"
      "v_mfma_f32_16x16x32_bf16 v[8:11], a[32:35], v[68:71], v[8:11]\n\t"
      "v_and_b32 v195, s74, v201\n\t"
      "v_lshlrev_b32 v194, 16, v201\n\t"
      "v_mfma_f32_16x16x32_bf16 v[12:15], a[96:99], v[68:71], v[12:15]\n\t"
      "v_and_b32 v197, s74, v202\n\t"
      "v_lshlrev_b32 v196, 16, v202\n\t"
      "ds_read_b128 v[116:119], v255 offset:29696\n\t"
      "s_waitcnt lgkmcnt(11)\n\t"
      "v_mfma_f32_16x16x32_bf16 v[0:3], a[36:39], v[72:75], v[0:3]\n\t"
      "v_and_b32 v199, s74, v203\n\t"
      "v_lshlrev_b32 v198, 16, v203\n\t"
      "v_mfma_f32_16x16x32_bf16 v[4:7], a[100:103], v[72:75], v[4:7]\n\t"
      "v_add_f32 v32, v192, v196\n\t"
      "v_add_f32 v33, v193, v197\n\t"
      "ds_read_b128 v[120:123], v255 offset:30720\n\t"
      "s_waitcnt lgkmcnt(11)\n\t"
      "v_mfma_f32_16x16x32_bf16 v[8:11], a[36:39], v[76:79], v[8:11]\n\t"
      "buffer_load_dwordx4 v[148:151], v253, s[36:39], s52 offen offset:512 sc1\n\t"
      "v_mfma_f32_16x16x32_bf16 v[12:15], a[100:103], v[76:79], v[12:15]\n\t"
      "v_add_f32 v34, v194, v198\n\t"
      "v_add_f32 v35, v195, v199\n\t"
      "ds_read_b128 v[124:127], v255 offset:31744\n\t"
      "s_waitcnt lgkmcnt(11)\n\t"
      "v_mfma_f32_16x16x32_bf16 v[0:3], a[40:43], v[80:83], v[0:3]\n\t"
      "v_add_f32 v32, v32, v34\n\t"
      "v_add_f32 v33, v33, v35\n\t"
      "v_mfma_f32_16x16x32_bf16 v[4:7], a[104:107], v[80:83], v[4:7]\n\t"
      "v_add_f32 v32, v32, v33\n\t"
      "s_waitcnt lgkmcnt(10)\n\t"
      "v_mfma_f32_16x16x32_bf16 v[8:11], a[40:43], v[84:87], v[8:11]\n\t"
      "v_add_f32_dpp v32, v32, v32 quad_perm:[1,0,3,2] row_mask:0xf bank_mask:0xf\n\t"
      "v_mfma_f32_16x16x32_bf16 v[12:15], a[104:107], v[84:87], v[12:15]\n\t"
      "s_waitcnt lgkmcnt(9)\n\t"
      "v_mfma_f32_16x16x32_bf16 v[0:3], a[44:47], v[88:91], v[0:3]\n\t"
      "v_add_f32_dpp v32, v32, v32 quad_perm:[2,3,0,1] row_mask:0xf bank_mask:0xf\n\t"
      "v_mfma_f32_16x16x32_bf16 v[4:7], a[108:111], v[88:91], v[4:7]\n\t"
      "s_waitcnt lgkmcnt(8)\n\t"
      "v_mfma_f32_16x16x32_bf16 v[8:11], a[44:47], v[92:95], v[8:11]\n\t"
      "buffer_load_dwordx4 v[152:155], v253, s[36:39], s52 offen offset:1024 sc1\n\t"
      "v_mfma_f32_16x16x32_bf16 v[12:15], a[108:111], v[92:95], v[12:15]\n\t"
      "v_add_f32_dpp v32, v32, v32 row_ror:4 row_mask:0xf bank_mask:0xf\n\t"
      "s_waitcnt lgkmcnt(7)\n\t"
      "v_mfma_f32_16x16x32_bf16 v[0:3], a[48:51], v[96:99], v[0:3]\n\t"
      "v_add_f32_dpp v32, v32, v32 row_ror:8 row_mask:0xf bank_mask:0xf\n\t"
      "v_mov_b32 v33, v32\n\t"
      "v_mfma_f32_16x16x32_bf16 v[4:7], a[112:115], v[96:99], v[4:7]\n\t"
      "s_waitcnt lgkmcnt(6)\n\t"
      "v_mfma_f32_16x16x32_bf16 v[8:11], a[48:51], v[100:103], v[8:11]\n\t"
      "v_permlane16_swap_b32 v32, v33\n\t"
      "v_add_f32 v32, v32, v33\n\t"
      "v_mfma_f32_16x16x32_bf16 v[12:15], a[112:115], v[100:103], v[12:15]\n\t"
      "v_mov_b32 v33, v32\n\t"
      "s_waitcnt lgkmcnt(5)\n\t"
      "v_mfma_f32_16x16x32_bf16 v[0:3], a[52:55], v[104:107], v[0:3]\n\t"
      "v_permlane32_swap_b32 v32, v33\n\t"
      "v_add_f32 v32, v32, v33\n\t"
      "v_mfma_f32_16x16x32_bf16 v[4:7], a[116:119], v[104:107], v[4:7]\n\t"
      "v_mul_f32 v204, 0x3b000000, v32\n\t"
      "v_sub_f32 v40, v192, v204\n\t"
      "s_waitcnt lgkmcnt(4)\n\t"
      "v_mfma_f32_16x16x32_bf16 v[8:11], a[52:55], v[108:111], v[8:11]\n\t"
      "v_sub_f32 v41, v193, v204\n\t"
      "v_sub_f32 v42, v194, v204\n\t"
      "v_mfma_f32_16x16x32_bf16 v[12:15], a[116:119], v[108:111], v[12:15]\n\t"
      "buffer_load_dwordx4 v[156:159], v253, s[36:39], s52 offen offset:1536 sc1\n\t"
      "s_waitcnt lgkmcnt(3)\n\t"
      "v_mfma_f32_16x16x32_bf16 v[0:3], a[56:59], v[112:115], v[0:3]\n\t"
      "v_sub_f32 v43, v195, v204\n\t"
      "v_sub_f32 v44, v196, v204\n\t"
      "v_mfma_f32_16x16x32_bf16 v[4:7], a[120:123], v[112:115], v[4:7]\n\t"
      "v_sub_f32 v45, v197, v204\n\t"
      "v_sub_f32 v46, v198, v204\n\t"
      "s_waitcnt lgkmcnt(2)\n\t"
      "v_mfma_f32_16x16x32_bf16 v[8:11], a[56:59], v[116:119], v[8:11]\n\t"
      "v_sub_f32 v47, v199, v204\n\t"
      "v_mul_f32 v32, v40, v40\n\t"
      "v_mfma_f32_16x16x32_bf16 v[12:15], a[120:123], v[116:119], v[12:15]\n\t"
      "v_mul_f32 v33, v41, v41\n\t"
      "v_fma_f32 v32, v42, v42, v32\n\t"
      "s_waitcnt lgkmcnt(1)\n\t"
      "v_mfma_f32_16x16x32_bf16 v[0:3], a[60:63], v[120:123], v[0:3]\n\t"
      "v_fma_f32 v33, v43, v43, v33\n\t"
      "v_fma_f32 v32, v44, v44, v32\n\t"
      "v_mfma_f32_16x16x32_bf16 v[4:7], a[124:127], v[120:123], v[4:7]\n\t"
      "v_fma_f32 v33, v45, v45, v33\n\t"
      "v_fma_f32 v32, v46, v46, v32\n\t"
      "s_waitcnt lgkmcnt(0)\n\t"
      "v_mfma_f32_16x16x32_bf16 v[8:11], a[60:63], v[124:127], v[8:11]\n\t"
      "v_fma_f32 v33, v47, v47, v33\n\t"
      "v_add_f32 v32, v32, v33\n\t"
      "v_mfma_f32_16x16x32_bf16 v[12:15], a[124:127], v[124:127], v[12:15]\n\t"
      "s_nop 0\n\t"
      "v_add_f32_dpp v32, v32, v32 quad_perm:[1,0,3,2] row_mask:0xf bank_mask:0xf\n\t"
      "s_nop 1\n\t"
      "v_add_f32_dpp v32, v32, v32 quad_perm:[2,3,0,1] row_mask:0xf bank_mask:0xf\n\t"
      "s_nop 1\n\t"
      "v_add_f32_dpp v32, v32, v32 row_ror:4 row_mask:0xf bank_mask:0xf\n\t"
      "s_nop 1\n\t"
      "v_add_f32_dpp v32, v32, v32 row_ror:8 row_mask:0xf bank_mask:0xf\n\t"
      "v_mov_b32 v33, v32\n\t"
      "s_nop 1\n\t"
      "v_permlane16_swap_b32 v32, v33\n\t"
      "v_add_f32 v32, v32, v33\n\t"
      "v_mov_b32 v33, v32\n\t"
      "s_nop 1\n\t"
      "v_permlane32_swap_b32 v32, v33\n\t"
      "v_add_f32 v32, v32, v33\n\t"
      "v_mul_f32 v205, 0x3b000000, v32\n\t"
      "v_xor_b32 v254, 0x8000, v254\n\t"
      "v_xor_b32 v255, 0x8000, v255\n\t"
      "s_add_u32 s69, s53, 0x4000\n\t"
      "s_add_u32 s48, s48, 4\n\t"
      "s_waitcnt vmcnt(40)\n\t"
      "v_cvt_pk_bf16_f32 v0, v0, v1\n\t"
      "v_cvt_pk_bf16_f32 v1, v2, v3\n\t"
      "v_cvt_pk_bf16_f32 v2, v4, v5\n\t"
      "v_cvt_pk_bf16_f32 v3, v6, v7\n\t"
      "v_min_u32 v0, -2, v0\n\t"
      "v_min_u32 v1, -2, v1\n\t"
      "v_min_u32 v2, -2, v2\n\t"
      "v_min_u32 v3, -2, v3\n\t"
      "buffer_store_dwordx4 v[0:3], v238, s[36:39], s53 offen sc1\n\t"
      "v_cvt_pk_bf16_f32 v8, v8, v9\n\t"
      "v_cvt_pk_bf16_f32 v9, v10, v11\n\t"
      "v_cvt_pk_bf16_f32 v10, v12, v13\n\t"
      "v_cvt_pk_bf16_f32 v11, v14, v15\n\t"
      "v_min_u32 v8, -2, v8\n\t"
      "v_min_u32 v9, -2, v9\n\t"
      "v_min_u32 v10, -2, v10\n\t"
      "v_min_u32 v11, -2, v11\n\t"
      "buffer_store_dwordx4 v[8:11], v238, s[36:39], s69 offen sc1\n\t"
      "s_add_u32 s53, s53, 1048576\n\t"
      "s_nop 1\n\t"
      "s_branch WTOP%=\n\t"
      "WOM2%=:\n\t"
      "s_mov_b32 s65, 0\n\t"
      "s_add_u32 s24, s96, 8\n\t"
      "s_cmp_lg_u32 s96, 0\n\t"
      "s_cbranch_scc1 WOL5%=\n\t"
      "WOL4%=:\n\t"
      "s_waitcnt lgkmcnt(11)\n\t"
      "v_mfma_f32_16x16x32_bf16 v[0:3], a[0:3], v[64:67], 0\n\t"
      "v_cvt_pk_bf16_f32 v204, v204, v205\n\t"
      "v_and_b32 v205, s74, v204\n\t"
      "v_mfma_f32_16x16x32_bf16 v[4:7], a[64:67], v[64:67], 0\n\t"
      "v_lshlrev_b32 v204, 16, v204\n\t"
      "v_add_f32 v205, 0x3727c5ac, v205\n\t"
      "ds_read_b128 v[112:115], v255 offset:12288\n\t"
      "s_waitcnt lgkmcnt(11)\n\t"
      "v_mfma_f32_16x16x32_bf16 v[8:11], a[0:3], v[68:71], 0\n\t"
      "v_sub_f32 v40, v192, v204\n\t"
      "v_sub_f32 v41, v193, v204\n\t"
      "v_mfma_f32_16x16x32_bf16 v[12:15], a[64:67], v[68:71], 0\n\t"
      "v_sub_f32 v42, v194, v204\n\t"
      "v_sub_f32 v43, v195, v204\n\t"
      "ds_read_b128 v[116:119], v255 offset:13312\n\t"
      "s_waitcnt lgkmcnt(11)\n\t"
      "v_mfma_f32_16x16x32_bf16 v[0:3], a[4:7], v[72:75], v[0:3]\n\t"
      "v_sub_f32 v44, v196, v204\n\t"
      "v_sub_f32 v45, v197, v204\n\t"
      "v_mfma_f32_16x16x32_bf16 v[4:7], a[68:71], v[72:75], v[4:7]\n\t"
      "v_sub_f32 v46, v198, v204\n\t"
      "v_sub_f32 v47, v199, v204\n\t"
      "ds_read_b128 v[120:123], v255 offset:14336\n\t"
      "s_waitcnt lgkmcnt(11)\n\t"
      "v_mfma_f32_16x16x32_bf16 v[8:11], a[4:7], v[76:79], v[8:11]\n\t"
      "v_cvt_pk_bf16_f32 v205, v205, v205\n\t"
      "v_and_b32 v205, s74, v205\n\t"
      "v_mfma_f32_16x16x32_bf16 v[12:15], a[68:71], v[76:79], v[12:15]\n\t"
      "v_sqrt_f32 v205, v205\n\t"
      "ds_read_b128 v[124:127], v255 offset:15360\n\t"
      "s_waitcnt lgkmcnt(11)\n\t"
      "v_mfma_f32_16x16x32_bf16 v[0:3], a[8:11], v[80:83], v[0:3]\n\t"
      "v_cvt_pk_bf16_f32 v40, v40, v41\n\t"
      "v_cvt_pk_bf16_f32 v42, v42, v43\n\t"
      "v_mfma_f32_16x16x32_bf16 v[4:7], a[72:75], v[80:83], v[4:7]\n\t"
      "v_cvt_pk_bf16_f32 v44, v44, v45\n\t"
      "v_cvt_pk_bf16_f32 v46, v46, v47\n\t"
      "ds_read_b128 v[64:67], v255 offset:16384\n\t"
      "s_waitcnt lgkmcnt(11)\n\t"
      "v_mfma_f32_16x16x32_bf16 v[8:11], a[8:11], v[84:87], v[8:11]\n\t"
      "v_cvt_pk_bf16_f32 v205, v205, v205\n\t"
      "v_and_b32 v205, s74, v205\n\t"
      "v_mfma_f32_16x16x32_bf16 v[12:15], a[72:75], v[84:87], v[12:15]\n\t"
      "v_rcp_f32 v204, v205\n\t"
      "ds_read_b128 v[68:71], v255 offset:17408\n\t"
      "s_waitcnt lgkmcnt(11)\n\t"
      "v_mfma_f32_16x16x32_bf16 v[0:3], a[12:15], v[88:91], v[0:3]\n\t"
      "v_and_b32 v41, s74, v40\n\t"
      "v_lshlrev_b32 v40, 16, v40\n\t"
      "v_mfma_f32_16x16x32_bf16 v[4:7], a[76:79], v[88:91], v[4:7]\n\t"
      "v_and_b32 v43, s74, v42\n\t"
      "v_lshlrev_b32 v42, 16, v42\n\t"
      "ds_read_b128 v[72:75], v255 offset:18432\n\t"
      "s_waitcnt lgkmcnt(11)\n\t"
      "v_mfma_f32_16x16x32_bf16 v[8:11], a[12:15], v[92:95], v[8:11]\n\t"
      "v_and_b32 v45, s74, v44\n\t"
      "v_lshlrev_b32 v44, 16, v44\n\t"
      "v_mfma_f32_16x16x32_bf16 v[12:15], a[76:79], v[92:95], v[12:15]\n\t"
      "v_and_b32 v47, s74, v46\n\t"
      "v_lshlrev_b32 v46, 16, v46\n\t"
      "ds_read_b128 v[76:79], v255 offset:19456\n\t"
      "s_waitcnt lgkmcnt(11)\n\t"
      "v_mfma_f32_16x16x32_bf16 v[0:3], a[16:19], v[96:99], v[0:3]\n\t"
      "v_mul_f32 v40, v40, v204\n\t"
      "v_mul_f32 v41, v41, v204\n\t"
      "v_mfma_f32_16x16x32_bf16 v[4:7], a[80:83], v[96:99], v[4:7]\n\t"
      "v_mul_f32 v42, v42, v204\n\t"
      "v_mul_f32 v43, v43, v204\n\t"
      "ds_read_b128 v[80:83], v255 offset:20480\n\t"
      "s_waitcnt lgkmcnt(11)\n\t"
      "v_mfma_f32_16x16x32_bf16 v[8:11], a[16:19], v[100:103], v[8:11]\n\t"
      "v_mul_f32 v44, v44, v204\n\t"
      "v_mul_f32 v45, v45, v204\n\t"
      "v_mfma_f32_16x16x32_bf16 v[12:15], a[80:83], v[100:103], v[12:15]\n\t"
      "v_mul_f32 v46, v46, v204\n\t"
      "v_mul_f32 v47, v47, v204\n\t"
      "ds_read_b128 v[84:87], v255 offset:21504\n\t"
      "s_waitcnt lgkmcnt(11)\n\t"
      "v_mfma_f32_16x16x32_bf16 v[0:3], a[20:23], v[104:107], v[0:3]\n\t"
      "v_cvt_pk_bf16_f32 v52, v40, v41\n\t"
      "v_cvt_pk_bf16_f32 v53, v42, v43\n\t"
      "v_mfma_f32_16x16x32_bf16 v[4:7], a[84:87], v[104:107], v[4:7]\n\t"
      "v_cvt_pk_bf16_f32 v54, v44, v45\n\t"
      "v_cvt_pk_bf16_f32 v55, v46, v47\n\t"
      "ds_read_b128 v[88:91], v255 offset:22528\n\t"
      "s_waitcnt lgkmcnt(11)\n\t"
      "v_mfma_f32_16x16x32_bf16 v[8:11], a[20:23], v[108:111], v[8:11]\n\t"
      "v_and_b32 v33, s74, v52\n\t"
      "v_lshlrev_b32 v32, 16, v52\n\t"
      "v_mfma_f32_16x16x32_bf16 v[12:15], a[84:87], v[108:111], v[12:15]\n\t"
      "v_and_b32 v35, s74, v53\n\t"
      "v_lshlrev_b32 v34, 16, v53\n\t"
      "ds_read_b128 v[92:95], v255 offset:23552\n\t"
      "s_waitcnt lgkmcnt(11)\n\t"
      "v_mfma_f32_16x16x32_bf16 v[0:3], a[24:27], v[112:115], v[0:3]\n\t"
      "v_and_b32 v37, s74, v54\n\t"
      "v_lshlrev_b32 v36, 16, v54\n\t"
      "v_mfma_f32_16x16x32_bf16 v[4:7], a[88:91], v[112:115], v[4:7]\n\t"
      "v_and_b32 v39, s74, v55\n\t"
      "v_lshlrev_b32 v38, 16, v55\n\t"
      "ds_read_b128 v[96:99], v255 offset:24576\n\t"
      "s_waitcnt lgkmcnt(11)\n\t"
      "v_mfma_f32_16x16x32_bf16 v[8:11], a[24:27], v[116:119], v[8:11]\n\t"
      "v_mul_f32 v40, v32, v240\n\t"
      "v_mul_f32 v41, v33, v241\n\t"
      "v_mfma_f32_16x16x32_bf16 v[12:15], a[88:91], v[116:119], v[12:15]\n\t"
      "v_fma_f32 v40, v34, v242, v40\n\t"
      "v_fma_f32 v41, v35, v243, v41\n\t"
      "ds_read_b128 v[100:103], v255 offset:25600\n\t"
      "s_waitcnt lgkmcnt(11)\n\t"
      "v_mfma_f32_16x16x32_bf16 v[0:3], a[28:31], v[120:123], v[0:3]\n\t"
      "v_fma_f32 v40, v36, v244, v40\n\t"
      "v_fma_f32 v41, v37, v245, v41\n\t"
      "v_mfma_f32_16x16x32_bf16 v[4:7], a[92:95], v[120:123], v[4:7]\n\t"
      "v_fma_f32 v40, v38, v246, v40\n\t"
      "v_fma_f32 v41, v39, v247, v41\n\t"
      "ds_read_b128 v[104:107], v255 offset:26624\n\t"
      "s_waitcnt lgkmcnt(11)\n\t"
      "v_mfma_f32_16x16x32_bf16 v[8:11], a[28:31], v[124:127], v[8:11]\n\t"
      "v_add_f32 v40, v40, v41\n\t"
      "s_lshl_b32 s73, s49, 5\n\t"
      "s_add_u32 s73, s73, s60\n\t"
      "s_lshl_b32 s69, s73, 2\n\t"
      "s_add_u32 s69, s69, s57\n\t"
      "s_lshl_b32 s70, s73, 7\n\t"
      "s_add_u32 s70, s70, s57\n\t"
      "s_sub_u32 s70, s70, 212992\n\t"
      "s_cmp_ge_u32 s49, 56\n\t"
      "s_cselect_b32 s70, s70, s69\n\t"
      "v_add_f32_dpp v40, v40, v40 quad_perm:[1,0,3,2] row_mask:0xf bank_mask:0xf\n\t"
      "v_mfma_f32_16x16x32_bf16 v[12:15], a[92:95], v[124:127], v[12:15]\n\t"
      "ds_read_b128 v[108:111], v255 offset:27648\n\t"
      "s_waitcnt lgkmcnt(11)\n\t"
      "v_mfma_f32_16x16x32_bf16 v[0:3], a[32:35], v[64:67], v[0:3]\n\t"
      "v_add_f32_dpp v40, v40, v40 quad_perm:[2,3,0,1] row_mask:0xf bank_mask:0xf\n\t"
      "v_mfma_f32_16x16x32_bf16 v[4:7], a[96:99], v[64:67], v[4:7]\n\t"
      "ds_read_b128 v[112:115], v255 offset:28672\n\t"
      "s_waitcnt lgkmcnt(11)\n\t"
      "v_mfma_f32_16x16x32_bf16 v[8:11], a[32:35], v[68:71], v[8:11]\n\t"
      "v_add_f32_dpp v40, v40, v40 row_ror:4 row_mask:0xf bank_mask:0xf\n\t"
      "v_mfma_f32_16x16x32_bf16 v[12:15], a[96:99], v[68:71], v[12:15]\n\t"
      "ds_read_b128 v[116:119], v255 offset:29696\n\t"
      "s_waitcnt lgkmcnt(11)\n\t"
      "v_mfma_f32_16x16x32_bf16 v[0:3], a[36:39], v[72:75], v[0:3]\n\t"
      "v_add_f32_dpp v40, v40, v40 row_ror:8 row_mask:0xf bank_mask:0xf\n\t"
      "v_mov_b32 v41, v40\n\t"
      "v_mfma_f32_16x16x32_bf16 v[4:7], a[100:103], v[72:75], v[4:7]\n\t"
      "ds_read_b128 v[120:123], v255 offset:30720\n\t"
      "s_waitcnt lgkmcnt(11)\n\t"
      "v_mfma_f32_16x16x32_bf16 v[8:11], a[36:39], v[76:79], v[8:11]\n\t"
      "v_permlane16_swap_b32 v40, v41\n\t"
      "v_add_f32 v40, v40, v41\n\t"
      "v_mfma_f32_16x16x32_bf16 v[12:15], a[100:103], v[76:79], v[12:15]\n\t"
      "v_mov_b32 v41, v40\n\t"
      "ds_read_b128 v[124:127], v255 offset:31744\n\t"
      "s_waitcnt lgkmcnt(11)\n\t"
      "v_mfma_f32_16x16x32_bf16 v[0:3], a[40:43], v[80:83], v[0:3]\n\t"
      "v_permlane32_swap_b32 v40, v41\n\t"
      "v_add_f32 v40, v40, v41\n\t"
      "v_mfma_f32_16x16x32_bf16 v[4:7], a[104:107], v[80:83], v[4:7]\n\t"
      "v_cvt_pk_bf16_f32 v40, v40, v40\n\t"
      "v_and_b32 v40, s74, v40\n\t"
      "s_waitcnt lgkmcnt(10)\n\t"
      "v_mfma_f32_16x16x32_bf16 v[8:11], a[40:43], v[84:87], v[8:11]\n\t"
      "s_mov_b64 exec, 1\n\t"
      "buffer_store_dword v40, off, s[36:39], s70 sc1\n\t"
      "s_mov_b64 exec, -1\n\t"
      "v_add_f32 v192, v192, v32\n\t"
      "v_add_f32 v193, v193, v33\n\t"
      "v_mfma_f32_16x16x32_bf16 v[12:15], a[104:107], v[84:87], v[12:15]\n\t"
      "v_add_f32 v194, v194, v34\n\t"
      "v_add_f32 v195, v195, v35\n\t"
      "s_waitcnt lgkmcnt(9)\n\t"
      "v_mfma_f32_16x16x32_bf16 v[0:3], a[44:47], v[88:91], v[0:3]\n\t"
      "v_add_f32 v196, v196, v36\n\t"
      "v_add_f32 v197, v197, v37\n\t"
      "v_mfma_f32_16x16x32_bf16 v[4:7], a[108:111], v[88:91], v[4:7]\n\t"
      "v_add_f32 v198, v198, v38\n\t"
      "v_add_f32 v199, v199, v39\n\t"
      "s_waitcnt lgkmcnt(8)\n\t"
      "v_mfma_f32_16x16x32_bf16 v[8:11], a[44:47], v[92:95], v[8:11]\n\t"
      "v_readfirstlane_b32 s68, v40\n\t"
      "v_cvt_pk_bf16_f32 v56, v192, v193\n\t"
      "v_mfma_f32_16x16x32_bf16 v[12:15], a[108:111], v[92:95], v[12:15]\n\t"
      "v_cvt_pk_bf16_f32 v57, v194, v195\n\t"
      "v_cvt_pk_bf16_f32 v58, v196, v197\n\t"
      "s_waitcnt lgkmcnt(7)\n\t"
      "v_mfma_f32_16x16x32_bf16 v[0:3], a[48:51], v[96:99], v[0:3]\n\t"
      "v_cvt_pk_bf16_f32 v59, v198, v199\n\t"
      "s_add_u32 s67, s69, 8192\n\t"
      "s_lshl_b32 s73, s73, 10\n\t"
      "s_add_u32 s69, s73, s58\n\t"
      "s_add_u32 s70, s73, s59\n\t"
      "buffer_store_dwordx2 v[56:57], v249, s[44:47], s73 offen sc1\n\t"
      "v_mfma_f32_16x16x32_bf16 v[4:7], a[112:115], v[96:99], v[4:7]\n\t"
      "buffer_store_dwordx2 v[52:53], v249, s[36:39], s69 offen sc1\n\t"
      "buffer_store_dwordx2 v[200:201], v249, s[36:39], s70 offen sc1\n\t"
      "s_waitcnt lgkmcnt(6)\n\t"
      "v_mfma_f32_16x16x32_bf16 v[8:11], a[48:51], v[100:103], v[8:11]\n\t"
      "buffer_store_dwordx2 v[58:59], v249, s[44:47], s73 offen offset:512 sc1\n\t"
      "buffer_store_dwordx2 v[54:55], v249, s[36:39], s69 offen offset:512 sc1\n\t"
      "v_mfma_f32_16x16x32_bf16 v[12:15], a[112:115], v[100:103], v[12:15]\n\t"
      "buffer_store_dwordx2 v[202:203], v249, s[36:39], s70 offen offset:512 sc1\n\t"
      "s_mov_b32 s49, s89\n\t"
      "s_add_u32 s89, s89, 16\n\t"
      "s_lshr_b32 s69, s89, 4\n\t"
      "s_cmp_eq_u32 s69, 3\n\t"
      "s_cselect_b32 s89, s93, s89\n\t"
      "s_cmp_ge_u32 s49, 48\n\t"
      "s_cselect_b32 s60, s94, s60\n\t"
      "s_cselect_b32 s54, s95, s54\n\t"
      "s_waitcnt lgkmcnt(5)\n\t"
      "v_mfma_f32_16x16x32_bf16 v[0:3], a[52:55], v[104:107], v[0:3]\n\t"
      "v_mfma_f32_16x16x32_bf16 v[4:7], a[116:119], v[104:107], v[4:7]\n\t"
      "s_waitcnt lgkmcnt(4)\n\t"
      "v_mfma_f32_16x16x32_bf16 v[8:11], a[52:55], v[108:111], v[8:11]\n\t"
      "v_mfma_f32_16x16x32_bf16 v[12:15], a[116:119], v[108:111], v[12:15]\n\t"
      "s_waitcnt lgkmcnt(3)\n\t"
      "v_mfma_f32_16x16x32_bf16 v[0:3], a[56:59], v[112:115], v[0:3]\n\t"
      "v_mfma_f32_16x16x32_bf16 v[4:7], a[120:123], v[112:115], v[4:7]\n\t"
      "s_waitcnt lgkmcnt(2)\n\t"
      "v_mfma_f32_16x16x32_bf16 v[8:11], a[56:59], v[116:119], v[8:11]\n\t"
      "v_mfma_f32_16x16x32_bf16 v[12:15], a[120:123], v[116:119], v[12:15]\n\t"
      "s_waitcnt lgkmcnt(1)\n\t"
      "v_mfma_f32_16x16x32_bf16 v[0:3], a[60:63], v[120:123], v[0:3]\n\t"
      "v_mfma_f32_16x16x32_bf16 v[4:7], a[124:127], v[120:123], v[4:7]\n\t"
      "s_waitcnt lgkmcnt(0)\n\t"
      "v_mfma_f32_16x16x32_bf16 v[8:11], a[60:63], v[124:127], v[8:11]\n\t"
      "v_mfma_f32_16x16x32_bf16 v[12:15], a[124:127], v[124:127], v[12:15]\n\t"
      "v_xor_b32 v254, 0x8000, v254\n\t"
      "v_xor_b32 v255, 0x8000, v255\n\t"
      "s_add_u32 s69, s53, 0x4000\n\t"
      "s_add_u32 s48, s48, 4\n\t"
      "s_waitcnt vmcnt(40)\n\t"
      "v_cvt_pk_bf16_f32 v0, v0, v1\n\t"
      "v_cvt_pk_bf16_f32 v1, v2, v3\n\t"
      "v_cvt_pk_bf16_f32 v2, v4, v5\n\t"
      "v_cvt_pk_bf16_f32 v3, v6, v7\n\t"
      "v_min_u32 v0, -2, v0\n\t"
      "v_min_u32 v1, -2, v1\n\t"
      "v_min_u32 v2, -2, v2\n\t"
      "v_min_u32 v3, -2, v3\n\t"
      "buffer_store_dwordx4 v[0:3], v238, s[36:39], s53 offen sc1\n\t"
      "v_cvt_pk_bf16_f32 v8, v8, v9\n\t"
      "v_cvt_pk_bf16_f32 v9, v10, v11\n\t"
      "v_cvt_pk_bf16_f32 v10, v12, v13\n\t"
      "v_cvt_pk_bf16_f32 v11, v14, v15\n\t"
      "v_min_u32 v8, -2, v8\n\t"
      "v_min_u32 v9, -2, v9\n\t"
      "v_min_u32 v10, -2, v10\n\t"
      "v_min_u32 v11, -2, v11\n\t"
      "buffer_store_dwordx4 v[8:11], v238, s[36:39], s69 offen sc1\n\t"
      "s_add_u32 s53, s53, 1048576\n\t"
      "s_nop 1\n\t"
      "s_branch WTOP%=\n\t"
      "WOL5%=:\n\t"
      "buffer_load_dwordx4 v[128:131], v252, s[36:39], s51 offen sc1\n\t"
      "s_waitcnt lgkmcnt(11)\n\t"
      "v_mfma_f32_16x16x32_bf16 v[0:3], a[0:3], v[64:67], 0\n\t"
      "v_cvt_pk_bf16_f32 v204, v204, v205\n\t"
      "v_and_b32 v205, s74, v204\n\t"
      "v_mfma_f32_16x16x32_bf16 v[4:7], a[64:67], v[64:67], 0\n\t"
      "v_lshlrev_b32 v204, 16, v204\n\t"
      "v_add_f32 v205, 0x3727c5ac, v205\n\t"
      "ds_read_b128 v[112:115], v255 offset:12288\n\t"
      "s_waitcnt lgkmcnt(11)\n\t"
      "v_mfma_f32_16x16x32_bf16 v[8:11], a[0:3], v[68:71], 0\n\t"
      "v_sub_f32 v40, v192, v204\n\t"
      "v_sub_f32 v41, v193, v204\n\t"
      "v_mfma_f32_16x16x32_bf16 v[12:15], a[64:67], v[68:71], 0\n\t"
      "v_sub_f32 v42, v194, v204\n\t"
      "v_sub_f32 v43, v195, v204\n\t"
      "ds_read_b128 v[116:119], v255 offset:13312\n\t"
      "s_waitcnt lgkmcnt(11)\n\t"
      "v_mfma_f32_16x16x32_bf16 v[0:3], a[4:7], v[72:75], v[0:3]\n\t"
      "v_sub_f32 v44, v196, v204\n\t"
      "v_sub_f32 v45, v197, v204\n\t"
      "v_mfma_f32_16x16x32_bf16 v[4:7], a[68:71], v[72:75], v[4:7]\n\t"
      "v_sub_f32 v46, v198, v204\n\t"
      "v_sub_f32 v47, v199, v204\n\t"
      "ds_read_b128 v[120:123], v255 offset:14336\n\t"
      "s_waitcnt lgkmcnt(11)\n\t"
      "v_mfma_f32_16x16x32_bf16 v[8:11], a[4:7], v[76:79], v[8:11]\n\t"
      "v_cvt_pk_bf16_f32 v205, v205, v205\n\t"
      "v_and_b32 v205, s74, v205\n\t"
      "v_mfma_f32_16x16x32_bf16 v[12:15], a[68:71], v[76:79], v[12:15]\n\t"
      "buffer_load_dwordx4 v[132:135], v252, s[36:39], s51 offen offset:512 sc1\n\t"
      "ds_read_b128 v[124:127], v255 offset:15360\n\t"
      "s_waitcnt lgkmcnt(11)\n\t"
      "v_mfma_f32_16x16x32_bf16 v[0:3], a[8:11], v[80:83], v[0:3]\n\t"
      "v_sqrt_f32 v205, v205\n\t"
      "v_mfma_f32_16x16x32_bf16 v[4:7], a[72:75], v[80:83], v[4:7]\n\t"
      "v_cvt_pk_bf16_f32 v40, v40, v41\n\t"
      "v_cvt_pk_bf16_f32 v42, v42, v43\n\t"
      "ds_read_b128 v[64:67], v255 offset:16384\n\t"
      "s_waitcnt lgkmcnt(11)\n\t"
      "v_mfma_f32_16x16x32_bf16 v[8:11], a[8:11], v[84:87], v[8:11]\n\t"
      "v_cvt_pk_bf16_f32 v44, v44, v45\n\t"
      "v_cvt_pk_bf16_f32 v46, v46, v47\n\t"
      "v_mfma_f32_16x16x32_bf16 v[12:15], a[72:75], v[84:87], v[12:15]\n\t"
      "v_cvt_pk_bf16_f32 v205, v205, v205\n\t"
      "v_and_b32 v205, s74, v205\n\t"
      "ds_read_b128 v[68:71], v255 offset:17408\n\t"
      "s_waitcnt lgkmcnt(11)\n\t"
      "v_mfma_f32_16x16x32_bf16 v[0:3], a[12:15], v[88:91], v[0:3]\n\t"
      "v_rcp_f32 v204, v205\n\t"
      "v_mfma_f32_16x16x32_bf16 v[4:7], a[76:79], v[88:91], v[4:7]\n\t"
      "v_and_b32 v41, s74, v40\n\t"
      "v_lshlrev_b32 v40, 16, v40\n\t"
      "ds_read_b128 v[72:75], v255 offset:18432\n\t"
      "s_waitcnt lgkmcnt(11)\n\t"
      "v_mfma_f32_16x16x32_bf16 v[8:11], a[12:15], v[92:95], v[8:11]\n\t"
      "v_and_b32 v43, s74, v42\n\t"
      "v_lshlrev_b32 v42, 16, v42\n\t"
      "v_mfma_f32_16x16x32_bf16 v[12:15], a[76:79], v[92:95], v[12:15]\n\t"
      "buffer_load_dwordx4 v[136:139], v252, s[36:39], s51 offen offset:1024 sc1\n\t"
      "ds_read_b128 v[76:79], v255 offset:19456\n\t"
      "s_waitcnt lgkmcnt(11)\n\t"
      "v_mfma_f32_16x16x32_bf16 v[0:3], a[16:19], v[96:99], v[0:3]\n\t"
      "v_and_b32 v45, s74, v44\n\t"
      "v_lshlrev_b32 v44, 16, v44\n\t"
      "v_mfma_f32_16x16x32_bf16 v[4:7], a[80:83], v[96:99], v[4:7]\n\t"
      "v_and_b32 v47, s74, v46\n\t"
      "v_lshlrev_b32 v46, 16, v46\n\t"
      "ds_read_b128 v[80:83], v255 offset:20480\n\t"
      "s_waitcnt lgkmcnt(11)\n\t"
      "v_mfma_f32_16x16x32_bf16 v[8:11], a[16:19], v[100:103], v[8:11]\n\t"
      "v_mul_f32 v40, v40, v204\n\t"
      "v_mul_f32 v41, v41, v204\n\t"
      "v_mfma_f32_16x16x32_bf16 v[12:15], a[80:83], v[100:103], v[12:15]\n\t"
      "v_mul_f32 v42, v42, v204\n\t"
      "v_mul_f32 v43, v43, v204\n\t"
      "ds_read_b128 v[84:87], v255 offset:21504\n\t"
      "s_waitcnt lgkmcnt(11)\n\t"
      "v_mfma_f32_16x16x32_bf16 v[0:3], a[20:23], v[104:107], v[0:3]\n\t"
      "v_mul_f32 v44, v44, v204\n\t"
      "v_mul_f32 v45, v45, v204\n\t"
      "v_mfma_f32_16x16x32_bf16 v[4:7], a[84:87], v[104:107], v[4:7]\n\t"
      "v_mul_f32 v46, v46, v204\n\t"
      "v_mul_f32 v47, v47, v204\n\t"
      "ds_read_b128 v[88:91], v255 offset:22528\n\t"
      "s_waitcnt lgkmcnt(11)\n\t"
      "v_mfma_f32_16x16x32_bf16 v[8:11], a[20:23], v[108:111], v[8:11]\n\t"
      "v_cvt_pk_bf16_f32 v52, v40, v41\n\t"
      "v_cvt_pk_bf16_f32 v53, v42, v43\n\t"
      "v_mfma_f32_16x16x32_bf16 v[12:15], a[84:87], v[108:111], v[12:15]\n\t"
      "buffer_load_dwordx4 v[140:143], v252, s[36:39], s51 offen offset:1536 sc1\n\t"
      "ds_read_b128 v[92:95], v255 offset:23552\n\t"
      "s_waitcnt lgkmcnt(11)\n\t"
      "v_mfma_f32_16x16x32_bf16 v[0:3], a[24:27], v[112:115], v[0:3]\n\t"
      "v_cvt_pk_bf16_f32 v54, v44, v45\n\t"
      "v_cvt_pk_bf16_f32 v55, v46, v47\n\t"
      "v_mfma_f32_16x16x32_bf16 v[4:7], a[88:91], v[112:115], v[4:7]\n\t"
      "v_and_b32 v33, s74, v52\n\t"
      "v_lshlrev_b32 v32, 16, v52\n\t"
      "ds_read_b128 v[96:99], v255 offset:24576\n\t"
      "s_waitcnt lgkmcnt(11)\n\t"
      "v_mfma_f32_16x16x32_bf16 v[8:11], a[24:27], v[116:119], v[8:11]\n\t"
      "v_and_b32 v35, s74, v53\n\t"
      "v_lshlrev_b32 v34, 16, v53\n\t"
      "v_mfma_f32_16x16x32_bf16 v[12:15], a[88:91], v[116:119], v[12:15]\n\t"
      "v_and_b32 v37, s74, v54\n\t"
      "v_lshlrev_b32 v36, 16, v54\n\t"
      "ds_read_b128 v[100:103], v255 offset:25600\n\t"
      "s_waitcnt lgkmcnt(11)\n\t"
      "v_mfma_f32_16x16x32_bf16 v[0:3], a[28:31], v[120:123], v[0:3]\n\t"
      "v_and_b32 v39, s74, v55\n\t"
      "v_lshlrev_b32 v38, 16, v55\n\t"
      "v_mfma_f32_16x16x32_bf16 v[4:7], a[92:95], v[120:123], v[4:7]\n\t"
      "v_mul_f32 v40, v32, v240\n\t"
      "v_mul_f32 v41, v33, v241\n\t"
      "ds_read_b128 v[104:107], v255 offset:26624\n\t"
      "s_waitcnt lgkmcnt(11)\n\t"
      "v_mfma_f32_16x16x32_bf16 v[8:11], a[28:31], v[124:127], v[8:11]\n\t"
      "buffer_load_dwordx4 v[144:147], v253, s[36:39], s52 offen sc1\n\t"
      "v_mfma_f32_16x16x32_bf16 v[12:15], a[92:95], v[124:127], v[12:15]\n\t"
      "v_fma_f32 v40, v34, v242, v40\n\t"
      "v_fma_f32 v41, v35, v243, v41\n\t"
      "ds_read_b128 v[108:111], v255 offset:27648\n\t"
      "s_waitcnt lgkmcnt(11)\n\t"
      "v_mfma_f32_16x16x32_bf16 v[0:3], a[32:35], v[64:67], v[0:3]\n\t"
      "v_fma_f32 v40, v36, v244, v40\n\t"
      "v_fma_f32 v41, v37, v245, v41\n\t"
      "v_mfma_f32_16x16x32_bf16 v[4:7], a[96:99], v[64:67], v[4:7]\n\t"
      "v_fma_f32 v40, v38, v246, v40\n\t"
      "v_fma_f32 v41, v39, v247, v41\n\t"
      "ds_read_b128 v[112:115], v255 offset:28672\n\t"
      "s_waitcnt lgkmcnt(11)\n\t"
      "v_mfma_f32_16x16x32_bf16 v[8:11], a[32:35], v[68:71], v[8:11]\n\t"
      "v_add_f32 v40, v40, v41\n\t"
      "s_lshl_b32 s73, s49, 5\n\t"
      "s_add_u32 s73, s73, s60\n\t"
      "s_lshl_b32 s69, s73, 2\n\t"
      "s_add_u32 s69, s69, s57\n\t"
      "s_lshl_b32 s70, s73, 7\n\t"
      "s_add_u32 s70, s70, s57\n\t"
      "s_sub_u32 s70, s70, 212992\n\t"
      "s_cmp_ge_u32 s49, 56\n\t"
      "s_cselect_b32 s70, s70, s69\n\t"
      "v_add_f32_dpp v40, v40, v40 quad_perm:[1,0,3,2] row_mask:0xf bank_mask:0xf\n\t"
      "v_mfma_f32_16x16x32_bf16 v[12:15], a[96:99], v[68:71], v[12:15]\n\t"
      "ds_read_b128 v[116:119], v255 offset:29696\n\t"
      "s_waitcnt lgkmcnt(11)\n\t"
      "v_mfma_f32_16x16x32_bf16 v[0:3], a[36:39], v[72:75], v[0:3]\n\t"
      "v_add_f32_dpp v40, v40, v40 quad_perm:[2,3,0,1] row_mask:0xf bank_mask:0xf\n\t"
      "v_mfma_f32_16x16x32_bf16 v[4:7], a[100:103], v[72:75], v[4:7]\n\t"
      "ds_read_b128 v[120:123], v255 offset:30720\n\t"
      "s_waitcnt lgkmcnt(11)\n\t"
      "v_mfma_f32_16x16x32_bf16 v[8:11], a[36:39], v[76:79], v[8:11]\n\t"
      "buffer_load_dwordx4 v[148:151], v253, s[36:39], s52 offen offset:512 sc1\n\t"
      "v_mfma_f32_16x16x32_bf16 v[12:15], a[100:103], v[76:79], v[12:15]\n\t"
      "v_add_f32_dpp v40, v40, v40 row_ror:4 row_mask:0xf bank_mask:0xf\n\t"
      "ds_read_b128 v[124:127], v255 offset:31744\n\t"
      "s_waitcnt lgkmcnt(11)\n\t"
      "v_mfma_f32_16x16x32_bf16 v[0:3], a[40:43], v[80:83], v[0:3]\n\t"
      "v_add_f32_dpp v40, v40, v40 row_ror:8 row_mask:0xf bank_mask:0xf\n\t"
      "v_mov_b32 v41, v40\n\t"
      "v_mfma_f32_16x16x32_bf16 v[4:7], a[104:107], v[80:83], v[4:7]\n\t"
      "s_waitcnt lgkmcnt(10)\n\t"
      "v_mfma_f32_16x16x32_bf16 v[8:11], a[40:43], v[84:87], v[8:11]\n\t"
      "v_permlane16_swap_b32 v40, v41\n\t"
      "v_add_f32 v40, v40, v41\n\t"
      "v_mfma_f32_16x16x32_bf16 v[12:15], a[104:107], v[84:87], v[12:15]\n\t"
      "v_mov_b32 v41, v40\n\t"
      "s_waitcnt lgkmcnt(9)\n\t"
      "v_mfma_f32_16x16x32_bf16 v[0:3], a[44:47], v[88:91], v[0:3]\n\t"
      "v_permlane32_swap_b32 v40, v41\n\t"
      "v_add_f32 v40, v40, v41\n\t"
      "v_mfma_f32_16x16x32_bf16 v[4:7], a[108:111], v[88:91], v[4:7]\n\t"
      "v_cvt_pk_bf16_f32 v40, v40, v40\n\t"
      "v_and_b32 v40, s74, v40\n\t"
      "s_waitcnt lgkmcnt(8)\n\t"
      "v_mfma_f32_16x16x32_bf16 v[8:11], a[44:47], v[92:95], v[8:11]\n\t"
      "buffer_load_dwordx4 v[152:155], v253, s[36:39], s52 offen offset:1024 sc1\n\t"
      "v_mfma_f32_16x16x32_bf16 v[12:15], a[108:111], v[92:95], v[12:15]\n\t"
      "s_mov_b64 exec, 1\n\t"
      "buffer_store_dword v40, off, s[36:39], s70 sc1\n\t"
      "s_mov_b64 exec, -1\n\t"
      "v_add_f32 v192, v192, v32\n\t"
      "v_add_f32 v193, v193, v33\n\t"
      "s_waitcnt lgkmcnt(7)\n\t"
      "v_mfma_f32_16x16x32_bf16 v[0:3], a[48:51], v[96:99], v[0:3]\n\t"
      "v_add_f32 v194, v194, v34\n\t"
      "v_add_f32 v195, v195, v35\n\t"
      "v_mfma_f32_16x16x32_bf16 v[4:7], a[112:115], v[96:99], v[4:7]\n\t"
      "v_add_f32 v196, v196, v36\n\t"
      "v_add_f32 v197, v197, v37\n\t"
      "s_waitcnt lgkmcnt(6)\n\t"
      "v_mfma_f32_16x16x32_bf16 v[8:11], a[48:51], v[100:103], v[8:11]\n\t"
      "v_add_f32 v198, v198, v38\n\t"
      "v_add_f32 v199, v199, v39\n\t"
      "v_mfma_f32_16x16x32_bf16 v[12:15], a[112:115], v[100:103], v[12:15]\n\t"
      "v_readfirstlane_b32 s68, v40\n\t"
      "v_cvt_pk_bf16_f32 v56, v192, v193\n\t"
      "s_waitcnt lgkmcnt(5)\n\t"
      "v_mfma_f32_16x16x32_bf16 v[0:3], a[52:55], v[104:107], v[0:3]\n\t"
      "v_cvt_pk_bf16_f32 v57, v194, v195\n\t"
      "v_cvt_pk_bf16_f32 v58, v196, v197\n\t"
      "v_mfma_f32_16x16x32_bf16 v[4:7], a[116:119], v[104:107], v[4:7]\n\t"
      "v_cvt_pk_bf16_f32 v59, v198, v199\n\t"
      "s_add_u32 s67, s69, 8192\n\t"
      "s_lshl_b32 s73, s73, 10\n\t"
      "s_add_u32 s69, s73, s58\n\t"
      "s_add_u32 s70, s73, s59\n\t"
      "buffer_store_dwordx2 v[56:57], v249, s[44:47], s73 offen sc1\n\t"
      "s_waitcnt lgkmcnt(4)\n\t"
      "v_mfma_f32_16x16x32_bf16 v[8:11], a[52:55], v[108:111], v[8:11]\n\t"
      "buffer_store_dwordx2 v[52:53], v249, s[36:39], s69 offen sc1\n\t"
      "buffer_store_dwordx2 v[200:201], v249, s[36:39], s70 offen sc1\n\t"
      "v_mfma_f32_16x16x32_bf16 v[12:15], a[116:119], v[108:111], v[12:15]\n\t"
      "buffer_load_dwordx4 v[156:159], v253, s[36:39], s52 offen offset:1536 sc1\n\t"
      "s_waitcnt lgkmcnt(3)\n\t"
      "v_mfma_f32_16x16x32_bf16 v[0:3], a[56:59], v[112:115], v[0:3]\n\t"
      "buffer_store_dwordx2 v[58:59], v249, s[44:47], s73 offen offset:512 sc1\n\t"
      "buffer_store_dwordx2 v[54:55], v249, s[36:39], s69 offen offset:512 sc1\n\t"
      "v_mfma_f32_16x16x32_bf16 v[4:7], a[120:123], v[112:115], v[4:7]\n\t"
      "buffer_store_dwordx2 v[202:203], v249, s[36:39], s70 offen offset:512 sc1\n\t"
      "s_mov_b32 s49, s89\n\t"
      "s_add_u32 s89, s89, 16\n\t"
      "s_lshr_b32 s69, s89, 4\n\t"
      "s_cmp_eq_u32 s69, 3\n\t"
      "s_cselect_b32 s89, s93, s89\n\t"
      "s_cmp_ge_u32 s49, 48\n\t"
      "s_cselect_b32 s60, s94, s60\n\t"
      "s_cselect_b32 s54, s95, s54\n\t"
      "s_waitcnt lgkmcnt(2)\n\t"
      "v_mfma_f32_16x16x32_bf16 v[8:11], a[56:59], v[116:119], v[8:11]\n\t"
      "v_mfma_f32_16x16x32_bf16 v[12:15], a[120:123], v[116:119], v[12:15]\n\t"
      "s_waitcnt lgkmcnt(1)\n\t"
      "v_mfma_f32_16x16x32_bf16 v[0:3], a[60:63], v[120:123], v[0:3]\n\t"
      "v_mfma_f32_16x16x32_bf16 v[4:7], a[124:127], v[120:123], v[4:7]\n\t"
      "s_waitcnt lgkmcnt(0)\n\t"
      "v_mfma_f32_16x16x32_bf16 v[8:11], a[60:63], v[124:127], v[8:11]\n\t"
      "v_mfma_f32_16x16x32_bf16 v[12:15], a[124:127], v[124:127], v[12:15]\n\t"
      "v_xor_b32 v254, 0x8000, v254\n\t"
      "v_xor_b32 v255, 0x8000, v255\n\t"
      "s_add_u32 s69, s53, 0x4000\n\t"
      "s_add_u32 s48, s48, 4\n\t"
      "s_waitcnt vmcnt(40)\n\t"
      "v_cvt_pk_bf16_f32 v0, v0, v1\n\t"
      "v_cvt_pk_bf16_f32 v1, v2, v3\n\t"
      "v_cvt_pk_bf16_f32 v2, v4, v5\n\t"
      "v_cvt_pk_bf16_f32 v3, v6, v7\n\t"
      "v_min_u32 v0, -2, v0\n\t"
      "v_min_u32 v1, -2, v1\n\t"
      "v_min_u32 v2, -2, v2\n\t"
      "v_min_u32 v3, -2, v3\n\t"
      "buffer_store_dwordx4 v[0:3], v238, s[36:39], s53 offen sc1\n\t"
      "v_cvt_pk_bf16_f32 v8, v8, v9\n\t"
      "v_cvt_pk_bf16_f32 v9, v10, v11\n\t"
      "v_cvt_pk_bf16_f32 v10, v12, v13\n\t"
      "v_cvt_pk_bf16_f32 v11, v14, v15\n\t"
      "v_min_u32 v8, -2, v8\n\t"
      "v_min_u32 v9, -2, v9\n\t"
      "v_min_u32 v10, -2, v10\n\t"
      "v_min_u32 v11, -2, v11\n\t"
      "buffer_store_dwordx4 v[8:11], v238, s[36:39], s69 offen sc1\n\t"
      "s_add_u32 s53, s53, 1048576\n\t"
      "s_nop 1\n\t"
      "s_branch WTOP%=\n\t"
      "WKF%=:\n\t"
      "s_cmp_eq_u32 s48, 63\n\t"
      "s_cselect_b32 s69, 0, 46\n\t"
      "s_add_u32 s88, s88, s69\n\t"
      "s_add_u32 s88, s88, -16\n\t"
      "WKT%=:\n\t"
      "s_memrealtime s[90:91]\n\t"
      "s_waitcnt lgkmcnt(0)\n\t"
      "s_sub_u32 s69, s88, s90\n\t"
      "s_and_b32 s69, s69, 0xffff\n\t"
      "s_cmp_lt_u32 s69, 400\n\t"
      "s_cbranch_scc1 WKT%=\n\t"
      "WKL%=:\n\t"
      "buffer_load_dwordx4 v[128:131], v252, s[36:39], s51 offen sc1\n\t"
      "buffer_load_dwordx4 v[132:135], v252, s[36:39], s51 offen offset:512 sc1\n\t"
      "buffer_load_dwordx4 v[136:139], v252, s[36:39], s51 offen offset:1024 sc1\n\t"
      "buffer_load_dwordx4 v[140:143], v252, s[36:39], s51 offen offset:1536 sc1\n\t"
      "buffer_load_dwordx4 v[144:147], v253, s[36:39], s52 offen sc1\n\t"
      "buffer_load_dwordx4 v[148:151], v253, s[36:39], s52 offen offset:512 sc1\n\t"
      "buffer_load_dwordx4 v[152:155], v253, s[36:39], s52 offen offset:1024 sc1\n\t"
      "buffer_load_dwordx4 v[156:159], v253, s[36:39], s52 offen offset:1536 sc1\n\t"
      "v_lshlrev_b32 v6, 4, v248\n\t"
      "v_mov_b64 v[4:5], 0\n\t"
      "s_cmp_ge_u32 s49, s48\n\t"
      "s_cbranch_scc1 WKL1%=\n\t"
      "buffer_load_dword v4, v6, s[36:39], s55 offen sc1\n\t"
      "buffer_load_dword v5, v6, s[36:39], s55 offen offset:128 sc1\n\t"
      "WKL1%=:\n\t"
      "s_waitcnt vmcnt(0)\n\t"
      "v_min_u32 v4, v4, v5\n\t"
      "v_cmp_ge_u32 vcc, s49, v4\n\t"
      "s_nop 0\n\t"
      "s_cmp_eq_u64 vcc, 0\n\t"
      "s_cselect_b32 s66, 1, 0\n\t"
      "s_cmp_lt_u32 s48, 40\n\t"
      "s_cbranch_scc1 WKV%=\n\t"
      "WKC%=:\n\t"
      "v_max3_u32 v0, v128, v129, v130\n\t"
      "v_max3_u32 v0, v0, v131, v132\n\t"
      "v_max3_u32 v0, v0, v133, v134\n\t"
      "v_max3_u32 v0, v0, v135, v136\n\t"
      "v_max3_u32 v0, v0, v137, v138\n\t"
      "v_max3_u32 v0, v0, v139, v140\n\t"
      "v_max3_u32 v0, v0, v141, v142\n\t"
      "v_max3_u32 v0, v0, v143, v144\n\t"
      "v_max3_u32 v0, v0, v145, v146\n\t"
      "v_max3_u32 v0, v0, v147, v148\n\t"
      "v_max3_u32 v0, v0, v149, v150\n\t"
      "v_max3_u32 v0, v0, v151, v152\n\t"
      "v_max3_u32 v0, v0, v153, v154\n\t"
      "v_max3_u32 v0, v0, v155, v156\n\t"
      "v_max3_u32 v0, v0, v157, v158\n\t"
      "v_max_u32 v0, v0, v159\n\t"
      "v_cmp_eq_u32 vcc, -1, v0\n\t"
      "s_nop 0\n\t"
      "s_cbranch_vccz WKV%=\n\t"
      "s_sleep 2\n\t"
      "s_sub_u32 s72, s72, 1\n\t"
      "s_cmp_gt_i32 s72, 0\n\t"
      "s_cbranch_scc1 WKL%=\n\t"
      "WKV%=:\n\t"
      "s_cmp_lt_u32 s48, 60\n\t"
      "s_cbranch_scc1 WKV1%=\n\t"
      "s_cmp_eq_u32 s66, 0\n\t"
      "s_cbranch_scc1 WB0%=\n\t"
      "s_lshl_b32 s70, s49, 5\n\t"
      "s_add_u32 s70, s70, s60\n\t"
      "s_lshl_b32 s70, s70, 10\n\t"
      "buffer_load_dwordx2 v[224:225], v249, s[40:43], s70 offen \n\t"
      "buffer_load_dwordx2 v[226:227], v249, s[40:43], s70 offen offset:512 \n\t"
      "s_lshl_b32 s69, s49, 18\n\t"
      "s_add_u32 s69, s69, s54\n\t"
      "buffer_load_dwordx4 v[160:163], v248, s[36:39], s69 offen sc1\n\t"
      "s_add_u32 s69, s69, 0x8000\n\t"
      "buffer_load_dwordx4 v[164:167], v248, s[36:39], s69 offen sc1\n\t"
      "s_add_u32 s69, s69, 0x8000\n\t"
      "buffer_load_dwordx4 v[168:171], v248, s[36:39], s69 offen sc1\n\t"
      "s_add_u32 s69, s69, 0x8000\n\t"
      "buffer_load_dwordx4 v[172:175], v248, s[36:39], s69 offen sc1\n\t"
      "s_add_u32 s69, s69, 0x8000\n\t"
      "buffer_load_dwordx4 v[176:179], v248, s[36:39], s69 offen sc1\n\t"
      "s_add_u32 s69, s69, 0x8000\n\t"
      "buffer_load_dwordx4 v[180:183], v248, s[36:39], s69 offen sc1\n\t"
      "s_add_u32 s69, s69, 0x8000\n\t"
      "buffer_load_dwordx4 v[184:187], v248, s[36:39], s69 offen sc1\n\t"
      "s_add_u32 s69, s69, 0x8000\n\t"
      "buffer_load_dwordx4 v[188:191], v248, s[36:39], s69 offen sc1\n\t"
      "s_mov_b32 s65, 1\n\t"
      "WB0%=:\n\t"
      "v_mfma_f32_16x16x32_bf16 v[0:3], v[128:131], a[160:163], 0\n\t"
      "v_mfma_f32_16x16x32_bf16 v[4:7], v[128:131], a[164:167], 0\n\t"
      "v_mfma_f32_16x16x32_bf16 v[0:3], v[132:135], a[168:171], v[0:3]\n\t"
      "v_mfma_f32_16x16x32_bf16 v[4:7], v[132:135], a[172:175], v[4:7]\n\t"
      "v_mfma_f32_16x16x32_bf16 v[0:3], v[136:139], a[176:179], v[0:3]\n\t"
      "v_mfma_f32_16x16x32_bf16 v[4:7], v[136:139], a[180:183], v[4:7]\n\t"
      "v_mfma_f32_16x16x32_bf16 v[0:3], v[140:143], a[184:187], v[0:3]\n\t"
      "v_mfma_f32_16x16x32_bf16 v[4:7], v[140:143], a[188:191], v[4:7]\n\t"
      "s_add_u32 s51, s51, 262144\n\t"
      "s_add_u32 s52, s52, 262144\n\t"
      "s_nop 4\n\t"
      "v_cvt_pk_bf16_f32 v0, v0, v1\n\t"
      "v_cvt_pk_bf16_f32 v2, v2, v3\n\t"
      "v_cvt_pk_bf16_f32 v4, v4, v5\n\t"
      "v_cvt_pk_bf16_f32 v6, v6, v7\n\t"
      "v_and_b32 v1, s74, v0\n\t"
      "v_lshlrev_b32 v0, 16, v0\n\t"
      "v_and_b32 v3, s74, v2\n\t"
      "v_lshlrev_b32 v2, 16, v2\n\t"
      "v_and_b32 v5, s74, v4\n\t"
      "v_lshlrev_b32 v4, 16, v4\n\t"
      "v_and_b32 v7, s74, v6\n\t"
      "v_lshlrev_b32 v6, 16, v6\n\t"
      "v_pk_mul_f32 v[0:1], v[0:1], s[84:85]\n\t"
      "v_pk_mul_f32 v[2:3], v[2:3], s[84:85]\n\t"
      "v_pk_mul_f32 v[4:5], v[4:5], s[84:85]\n\t"
      "v_pk_mul_f32 v[6:7], v[6:7], s[84:85]\n\t"
      "v_cvt_pk_bf16_f32 v0, v0, v1\n\t"
      "v_cvt_pk_bf16_f32 v2, v2, v3\n\t"
      "v_cvt_pk_bf16_f32 v4, v4, v5\n\t"
      "v_cvt_pk_bf16_f32 v6, v6, v7\n\t"
      "v_and_b32 v1, s74, v0\n\t"
      "v_lshlrev_b32 v0, 16, v0\n\t"
      "v_and_b32 v3, s74, v2\n\t"
      "v_lshlrev_b32 v2, 16, v2\n\t"
      "v_and_b32 v5, s74, v4\n\t"
      "v_lshlrev_b32 v4, 16, v4\n\t"
      "v_and_b32 v7, s74, v6\n\t"
      "v_lshlrev_b32 v6, 16, v6\n\t"
      "v_max3_f32 v8, v0, v1, v2\n\t"
      "v_max3_f32 v8, v8, v3, v234\n\t"
      "v_max3_f32 v9, v4, v5, v6\n\t"
      "v_max3_f32 v9, v9, v7, v235\n\t"
      "s_nop 1\n\t"
      "v_permlane16_swap_b32 v8, v9\n\t"
      "v_max_f32 v8, v8, v9\n\t"
      "v_mov_b32 v9, v8\n\t"
      "s_nop 1\n\t"
      "v_permlane16_swap_b32 v8, v9\n\t"
      "v_pk_mul_f32 v[8:9], v[8:9], s[86:87]\n\t"
      "v_pk_fma_f32 v[10:11], v[234:235], s[86:87], v[8:9] neg_lo:[0,0,1] neg_hi:[0,0,1]\n\t"
      "v_pk_fma_f32 v[0:1], v[0:1], s[86:87], v[8:9] op_sel:[0,0,0] op_sel_hi:[1,1,0] neg_lo:[0,0,1] neg_hi:[0,0,1]\n\t"
      "v_pk_fma_f32 v[2:3], v[2:3], s[86:87], v[8:9] op_sel:[0,0,0] op_sel_hi:[1,1,0] neg_lo:[0,0,1] neg_hi:[0,0,1]\n\t"
      "v_pk_fma_f32 v[4:5], v[4:5], s[86:87], v[8:9] op_sel:[0,0,1] op_sel_hi:[1,1,1] neg_lo:[0,0,1] neg_hi:[0,0,1]\n\t"
      "v_pk_fma_f32 v[6:7], v[6:7], s[86:87], v[8:9] op_sel:[0,0,1] op_sel_hi:[1,1,1] neg_lo:[0,0,1] neg_hi:[0,0,1]\n\t"
      "v_exp_f32 v10, v10\n\t"
      "v_exp_f32 v11, v11\n\t"
      "v_exp_f32 v0, v0\n\t"
      "v_exp_f32 v1, v1\n\t"
      "v_exp_f32 v2, v2\n\t"
      "v_exp_f32 v3, v3\n\t"
      "v_exp_f32 v4, v4\n\t"
      "v_exp_f32 v5, v5\n\t"
      "v_exp_f32 v6, v6\n\t"
      "v_exp_f32 v7, v7\n\t"
      "v_pk_mul_f32 v[12:13], v[236:237], v[10:11]\n\t"
      "v_pk_add_f32 v[14:15], v[0:1], v[2:3]\n\t"
      "v_pk_add_f32 v[16:17], v[4:5], v[6:7]\n\t"
      "v_add_f32 v14, v14, v15\n\t"
      "v_add_f32 v15, v16, v17\n\t"
      "v_fma_f32 v12, v14, 0.5, v12\n\t"
      "v_fma_f32 v13, v15, 0.5, v13\n\t"
      "s_nop 1\n\t"
      "v_permlane16_swap_b32 v12, v13\n\t"
      "v_add_f32 v12, v12, v13\n\t"
      "v_mov_b32 v13, v12\n\t"
      "s_nop 1\n\t"
      "v_permlane32_swap_b32 v12, v13\n\t"
      "v_add_f32 v12, v12, v13\n\t"
      "v_mov_b32 v13, v12\n\t"
      "s_nop 1\n\t"
      "v_permlane16_swap_b32 v12, v13\n\t"
      "v_rcp_f32 v12, v12\n\t"
      "v_rcp_f32 v13, v13\n\t"
      "s_nop 0\n\t"
      "v_pk_mul_f32 v[10:11], v[10:11], v[12:13]\n\t"
      "v_pk_mul_f32 v[12:13], v[12:13], 0.5 op_sel_hi:[1,0]\n\t"
      "v_pk_mul_f32 v[0:1], v[0:1], v[12:13] op_sel:[0,0] op_sel_hi:[1,0]\n\t"
      "v_pk_mul_f32 v[2:3], v[2:3], v[12:13] op_sel:[0,0] op_sel_hi:[1,0]\n\t"
      "v_pk_mul_f32 v[4:5], v[4:5], v[12:13] op_sel:[0,1] op_sel_hi:[1,1]\n\t"
      "v_pk_mul_f32 v[6:7], v[6:7], v[12:13] op_sel:[0,1] op_sel_hi:[1,1]\n\t"
      "v_cvt_pk_bf16_f32 v16, v0, v1\n\t"
      "v_cvt_pk_bf16_f32 v17, v2, v3\n\t"
      "v_cvt_pk_bf16_f32 v18, v4, v5\n\t"
      "v_cvt_pk_bf16_f32 v19, v6, v7\n\t"
      "v_pk_mul_f32 v[64:65], v[64:65], v[10:11] op_sel:[0,0] op_sel_hi:[1,0]\n\t"
      "v_pk_mul_f32 v[66:67], v[66:67], v[10:11] op_sel:[0,0] op_sel_hi:[1,0]\n\t"
      "v_pk_mul_f32 v[68:69], v[68:69], v[10:11] op_sel:[0,0] op_sel_hi:[1,0]\n\t"
      "v_pk_mul_f32 v[70:71], v[70:71], v[10:11] op_sel:[0,0] op_sel_hi:[1,0]\n\t"
      "v_mfma_f32_16x16x16_bf16 v[64:67], v[144:145], v[16:17], v[64:67]\n\t"
      "v_pk_mul_f32 v[72:73], v[72:73], v[10:11] op_sel:[0,0] op_sel_hi:[1,0]\n\t"
      "v_pk_mul_f32 v[74:75], v[74:75], v[10:11] op_sel:[0,0] op_sel_hi:[1,0]\n\t"
      "v_mfma_f32_16x16x16_bf16 v[68:71], v[146:147], v[16:17], v[68:71]\n\t"
      "v_pk_mul_f32 v[76:77], v[76:77], v[10:11] op_sel:[0,0] op_sel_hi:[1,0]\n\t"
      "v_pk_mul_f32 v[78:79], v[78:79], v[10:11] op_sel:[0,0] op_sel_hi:[1,0]\n\t"
      "v_mfma_f32_16x16x16_bf16 v[72:75], v[148:149], v[16:17], v[72:75]\n\t"
      "v_pk_mul_f32 v[80:81], v[80:81], v[10:11] op_sel:[0,0] op_sel_hi:[1,0]\n\t"
      "v_pk_mul_f32 v[82:83], v[82:83], v[10:11] op_sel:[0,0] op_sel_hi:[1,0]\n\t"
      "v_mfma_f32_16x16x16_bf16 v[76:79], v[150:151], v[16:17], v[76:79]\n\t"
      "v_pk_mul_f32 v[84:85], v[84:85], v[10:11] op_sel:[0,0] op_sel_hi:[1,0]\n\t"
      "v_pk_mul_f32 v[86:87], v[86:87], v[10:11] op_sel:[0,0] op_sel_hi:[1,0]\n\t"
      "v_mfma_f32_16x16x16_bf16 v[80:83], v[152:153], v[16:17], v[80:83]\n\t"
      "v_pk_mul_f32 v[88:89], v[88:89], v[10:11] op_sel:[0,0] op_sel_hi:[1,0]\n\t"
      "v_pk_mul_f32 v[90:91], v[90:91], v[10:11] op_sel:[0,0] op_sel_hi:[1,0]\n\t"
      "v_mfma_f32_16x16x16_bf16 v[84:87], v[154:155], v[16:17], v[84:87]\n\t"
      "v_pk_mul_f32 v[92:93], v[92:93], v[10:11] op_sel:[0,0] op_sel_hi:[1,0]\n\t"
      "v_pk_mul_f32 v[94:95], v[94:95], v[10:11] op_sel:[0,0] op_sel_hi:[1,0]\n\t"
      "v_cvt_pk_bf16_f32 v24, v64, v65\n\t"
      "v_cvt_pk_bf16_f32 v25, v66, v67\n\t"
      "v_cvt_pk_bf16_f32 v26, v68, v69\n\t"
      "v_cvt_pk_bf16_f32 v27, v70, v71\n\t"
      "ds_write_b128 v254, v[24:27]\n\t"
      "v_mfma_f32_16x16x16_bf16 v[88:91], v[156:157], v[16:17], v[88:91]\n\t"
      "v_pk_mul_f32 v[96:97], v[96:97], v[10:11] op_sel:[0,1] op_sel_hi:[1,1]\n\t"
      "v_pk_mul_f32 v[98:99], v[98:99], v[10:11] op_sel:[0,1] op_sel_hi:[1,1]\n\t"
      "v_mfma_f32_16x16x16_bf16 v[92:95], v[158:159], v[16:17], v[92:95]\n\t"
      "v_pk_mul_f32 v[100:101], v[100:101], v[10:11] op_sel:[0,1] op_sel_hi:[1,1]\n\t"
      "v_pk_mul_f32 v[102:103], v[102:103], v[10:11] op_sel:[0,1] op_sel_hi:[1,1]\n\t"
      "v_cvt_pk_bf16_f32 v28, v72, v73\n\t"
      "v_cvt_pk_bf16_f32 v29, v74, v75\n\t"
      "v_cvt_pk_bf16_f32 v30, v76, v77\n\t"
      "v_cvt_pk_bf16_f32 v31, v78, v79\n\t"
      "ds_write_b128 v254, v[28:31] offset:2048\n\t"
      "v_mfma_f32_16x16x16_bf16 v[96:99], v[144:145], v[18:19], v[96:99]\n\t"
      "v_pk_mul_f32 v[104:105], v[104:105], v[10:11] op_sel:[0,1] op_sel_hi:[1,1]\n\t"
      "v_pk_mul_f32 v[106:107], v[106:107], v[10:11] op_sel:[0,1] op_sel_hi:[1,1]\n\t"
      "v_mfma_f32_16x16x16_bf16 v[100:103], v[146:147], v[18:19], v[100:103]\n\t"
      "v_pk_mul_f32 v[108:109], v[108:109], v[10:11] op_sel:[0,1] op_sel_hi:[1,1]\n\t"
      "v_pk_mul_f32 v[110:111], v[110:111], v[10:11] op_sel:[0,1] op_sel_hi:[1,1]\n\t"
      "v_cvt_pk_bf16_f32 v24, v80, v81\n\t"
      "v_cvt_pk_bf16_f32 v25, v82, v83\n\t"
      "v_cvt_pk_bf16_f32 v26, v84, v85\n\t"
      "v_cvt_pk_bf16_f32 v27, v86, v87\n\t"
      "ds_write_b128 v254, v[24:27] offset:4096\n\t"
      "v_mfma_f32_16x16x16_bf16 v[104:107], v[148:149], v[18:19], v[104:107]\n\t"
      "v_pk_mul_f32 v[112:113], v[112:113], v[10:11] op_sel:[0,1] op_sel_hi:[1,1]\n\t"
      "v_pk_mul_f32 v[114:115], v[114:115], v[10:11] op_sel:[0,1] op_sel_hi:[1,1]\n\t"
      "v_mfma_f32_16x16x16_bf16 v[108:111], v[150:151], v[18:19], v[108:111]\n\t"
      "v_pk_mul_f32 v[116:117], v[116:117], v[10:11] op_sel:[0,1] op_sel_hi:[1,1]\n\t"
      "v_pk_mul_f32 v[118:119], v[118:119], v[10:11] op_sel:[0,1] op_sel_hi:[1,1]\n\t"
      "v_cvt_pk_bf16_f32 v28, v88, v89\n\t"
      "v_cvt_pk_bf16_f32 v29, v90, v91\n\t"
      "v_cvt_pk_bf16_f32 v30, v92, v93\n\t"
      "v_cvt_pk_bf16_f32 v31, v94, v95\n\t"
      "ds_write_b128 v254, v[28:31] offset:6144\n\t"
      "v_mfma_f32_16x16x16_bf16 v[112:115], v[152:153], v[18:19], v[112:115]\n\t"
      "v_pk_mul_f32 v[120:121], v[120:121], v[10:11] op_sel:[0,1] op_sel_hi:[1,1]\n\t"
      "v_pk_mul_f32 v[122:123], v[122:123], v[10:11] op_sel:[0,1] op_sel_hi:[1,1]\n\t"
      "v_mfma_f32_16x16x16_bf16 v[116:119], v[154:155], v[18:19], v[116:119]\n\t"
      "v_pk_mul_f32 v[124:125], v[124:125], v[10:11] op_sel:[0,1] op_sel_hi:[1,1]\n\t"
      "v_pk_mul_f32 v[126:127], v[126:127], v[10:11] op_sel:[0,1] op_sel_hi:[1,1]\n\t"
      "v_cvt_pk_bf16_f32 v24, v96, v97\n\t"
      "v_cvt_pk_bf16_f32 v25, v98, v99\n\t"
      "v_cvt_pk_bf16_f32 v26, v100, v101\n\t"
      "v_cvt_pk_bf16_f32 v27, v102, v103\n\t"
      "ds_write_b128 v254, v[24:27] offset:1024\n\t"
      "v_mfma_f32_16x16x16_bf16 v[120:123], v[156:157], v[18:19], v[120:123]\n\t"
      "v_mfma_f32_16x16x16_bf16 v[124:127], v[158:159], v[18:19], v[124:127]\n\t"
      "v_cvt_pk_bf16_f32 v28, v104, v105\n\t"
      "v_cvt_pk_bf16_f32 v29, v106, v107\n\t"
      "v_cvt_pk_bf16_f32 v30, v108, v109\n\t"
      "v_cvt_pk_bf16_f32 v31, v110, v111\n\t"
      "ds_write_b128 v254, v[28:31] offset:3072\n\t"
      "v_cvt_pk_bf16_f32 v24, v112, v113\n\t"
      "v_cvt_pk_bf16_f32 v25, v114, v115\n\t"
      "v_cvt_pk_bf16_f32 v26, v116, v117\n\t"
      "v_cvt_pk_bf16_f32 v27, v118, v119\n\t"
      "ds_write_b128 v254, v[24:27] offset:5120\n\t"
      "v_cvt_pk_bf16_f32 v28, v120, v121\n\t"
      "v_cvt_pk_bf16_f32 v29, v122, v123\n\t"
      "v_cvt_pk_bf16_f32 v30, v124, v125\n\t"
      "v_cvt_pk_bf16_f32 v31, v126, v127\n\t"
      "ds_write_b128 v254, v[28:31] offset:7168\n\t"
      "s_waitcnt vmcnt(40) lgkmcnt(0)\n\t"
      "s_barrier\n\t"
      "WO%=:\n\t"
      "s_waitcnt vmcnt(16)\n\t"
      "ds_read_b128 v[64:67], v255\n\t"
      "ds_read_b128 v[68:71], v255 offset:1024\n\t"
      "ds_read_b128 v[72:75], v255 offset:2048\n\t"
      "ds_read_b128 v[76:79], v255 offset:3072\n\t"
      "ds_read_b128 v[80:83], v255 offset:4096\n\t"
      "ds_read_b128 v[84:87], v255 offset:5120\n\t"
      "ds_read_b128 v[88:91], v255 offset:6144\n\t"
      "ds_read_b128 v[92:95], v255 offset:7168\n\t"
      "ds_read_b128 v[96:99], v255 offset:8192\n\t"
      "ds_read_b128 v[100:103], v255 offset:9216\n\t"
      "ds_read_b128 v[104:107], v255 offset:10240\n\t"
      "ds_read_b128 v[108:111], v255 offset:11264\n\t"
      "s_cmp_ge_u32 s48, 32\n\t"
      "s_cbranch_scc1 WO3%=\n\t"
      "v_mov_b64 v[20:21], -1\n\t"
      "v_mov_b64 v[22:23], -1\n\t"
      "buffer_store_dwordx4 v[20:23], v238, s[36:39], s98 offen sc1\n\t"
      "s_bitcmp0_b32 s48, 2\n\t"
      "s_cselect_b32 s69, 1, 63\n\t"
      "s_lshl_b32 s69, s69, 14\n\t"
      "s_add_u32 s98, s98, s69\n\t"
      "WO3%=:\n\t"
      "v_lshlrev_b32 v16, 4, v248\n\t"
      "s_cmp_ge_u32 s48, 60\n\t"
      "s_cbranch_scc1 WO1%=\n\t"
      "buffer_load_dwordx4 v[228:231], off, s[36:39], s56 sc1\n\t"
      "s_mov_b32 s97, s96\n\t"
      "v_mov_b64 v[232:233], 0\n\t"
      "s_cmp_lg_u32 s65, 0\n\t"
      "s_cselect_b32 s70, s89, s49\n\t"
      "s_cmp_ge_u32 s70, s48\n\t"
      "s_cbranch_scc1 WO1%=\n\t"
      "buffer_load_dword v232, v16, s[36:39], s55 offen sc1\n\t"
      "buffer_load_dword v233, v16, s[36:39], s55 offen offset:128 sc1\n\t"
      "WO1%=:\n\t"
      "s_cmp_lt_u32 s48, 56\n\t"
      "s_cselect_b32 s69, 3145728, 0\n\t"
      "s_add_u32 s50, s50, s69\n\t"
      "s_mov_b32 s24, 0\n\t"
      "s_cmp_eq_u32 s65, 2\n\t"
      "s_cbranch_scc1 WOM2%=\n\t"
      "s_cmp_lt_u32 s48, 60\n\t"
      "s_cselect_b32 s69, s65, 0\n\t"
      "s_cmp_lg_u32 s69, 0\n\t"
      "s_cbranch_scc1 WOM1%=\n\t"
      "s_cmp_lg_u32 s96, 0\n\t"
      "s_cbranch_scc1 WOL1%=\n\t"
      "WOL0%=:\n\t"
      "s_waitcnt lgkmcnt(11)\n\t"
      "v_mfma_f32_16x16x32_bf16 v[0:3], a[0:3], v[64:67], 0\n\t"
      "v_mfma_f32_16x16x32_bf16 v[4:7], a[64:67], v[64:67], 0\n\t"
      "ds_read_b128 v[112:115], v255 offset:12288\n\t"
      "s_waitcnt lgkmcnt(11)\n\t"
      "v_mfma_f32_16x16x32_bf16 v[8:11], a[0:3], v[68:71], 0\n\t"
      "v_mfma_f32_16x16x32_bf16 v[12:15], a[64:67], v[68:71], 0\n\t"
      "ds_read_b128 v[116:119], v255 offset:13312\n\t"
      "s_waitcnt lgkmcnt(11)\n\t"
      "v_mfma_f32_16x16x32_bf16 v[0:3], a[4:7], v[72:75], v[0:3]\n\t"
      "v_mfma_f32_16x16x32_bf16 v[4:7], a[68:71], v[72:75], v[4:7]\n\t"
      "ds_read_b128 v[120:123], v255 offset:14336\n\t"
      "s_waitcnt lgkmcnt(11)\n\t"
      "v_mfma_f32_16x16x32_bf16 v[8:11], a[4:7], v[76:79], v[8:11]\n\t"
      "v_mfma_f32_16x16x32_bf16 v[12:15], a[68:71], v[76:79], v[12:15]\n\t"
      "ds_read_b128 v[124:127], v255 offset:15360\n\t"
      "s_waitcnt lgkmcnt(11)\n\t"
      "v_mfma_f32_16x16x32_bf16 v[0:3], a[8:11], v[80:83], v[0:3]\n\t"
      "v_mfma_f32_16x16x32_bf16 v[4:7], a[72:75], v[80:83], v[4:7]\n\t"
      "ds_read_b128 v[64:67], v255 offset:16384\n\t"
      "s_waitcnt lgkmcnt(11)\n\t"
      "v_mfma_f32_16x16x32_bf16 v[8:11], a[8:11], v[84:87], v[8:11]\n\t"
      "v_mfma_f32_16x16x32_bf16 v[12:15], a[72:75], v[84:87], v[12:15]\n\t"
      "ds_read_b128 v[68:71], v255 offset:17408\n\t"
      "s_waitcnt lgkmcnt(11)\n\t"
      "v_mfma_f32_16x16x32_bf16 v[0:3], a[12:15], v[88:91], v[0:3]\n\t"
      "v_mfma_f32_16x16x32_bf16 v[4:7], a[76:79], v[88:91], v[4:7]\n\t"
      "ds_read_b128 v[72:75], v255 offset:18432\n\t"
      "s_waitcnt lgkmcnt(11)\n\t"
      "v_mfma_f32_16x16x32_bf16 v[8:11], a[12:15], v[92:95], v[8:11]\n\t"
      "v_mfma_f32_16x16x32_bf16 v[12:15], a[76:79], v[92:95], v[12:15]\n\t"
      "ds_read_b128 v[76:79], v255 offset:19456\n\t"
      "s_waitcnt lgkmcnt(11)\n\t"
      "v_mfma_f32_16x16x32_bf16 v[0:3], a[16:19], v[96:99], v[0:3]\n\t"
      "v_mfma_f32_16x16x32_bf16 v[4:7], a[80:83], v[96:99], v[4:7]\n\t"
      "ds_read_b128 v[80:83], v255 offset:20480\n\t"
      "s_waitcnt lgkmcnt(11)\n\t"
      "v_mfma_f32_16x16x32_bf16 v[8:11], a[16:19], v[100:103], v[8:11]\n\t"
      "v_mfma_f32_16x16x32_bf16 v[12:15], a[80:83], v[100:103], v[12:15]\n\t"
      "ds_read_b128 v[84:87], v255 offset:21504\n\t"
      "s_waitcnt lgkmcnt(11)\n\t"
      "v_mfma_f32_16x16x32_bf16 v[0:3], a[20:23], v[104:107], v[0:3]\n\t"
      "v_mfma_f32_16x16x32_bf16 v[4:7], a[84:87], v[104:107], v[4:7]\n\t"
      "ds_read_b128 v[88:91], v255 offset:22528\n\t"
      "s_waitcnt lgkmcnt(11)\n\t"
      "v_mfma_f32_16x16x32_bf16 v[8:11], a[20:23], v[108:111], v[8:11]\n\t"
      "v_mfma_f32_16x16x32_bf16 v[12:15], a[84:87], v[108:111], v[12:15]\n\t"
      "ds_read_b128 v[92:95], v255 offset:23552\n\t"
      "s_waitcnt lgkmcnt(11)\n\t"
      "v_mfma_f32_16x16x32_bf16 v[0:3], a[24:27], v[112:115], v[0:3]\n\t"
      "v_mfma_f32_16x16x32_bf16 v[4:7], a[88:91], v[112:115], v[4:7]\n\t"
      "ds_read_b128 v[96:99], v255 offset:24576\n\t"
      "s_waitcnt lgkmcnt(11)\n\t"
      "v_mfma_f32_16x16x32_bf16 v[8:11], a[24:27], v[116:119], v[8:11]\n\t"
      "v_mfma_f32_16x16x32_bf16 v[12:15], a[88:91], v[116:119], v[12:15]\n\t"
      "ds_read_b128 v[100:103], v255 offset:25600\n\t"
      "s_waitcnt lgkmcnt(11)\n\t"
      "v_mfma_f32_16x16x32_bf16 v[0:3], a[28:31], v[120:123], v[0:3]\n\t"
      "v_mfma_f32_16x16x32_bf16 v[4:7], a[92:95], v[120:123], v[4:7]\n\t"
      "ds_read_b128 v[104:107], v255 offset:26624\n\t"
      "s_waitcnt lgkmcnt(11)\n\t"
      "v_mfma_f32_16x16x32_bf16 v[8:11], a[28:31], v[124:127], v[8:11]\n\t"
      "v_mfma_f32_16x16x32_bf16 v[12:15], a[92:95], v[124:127], v[12:15]\n\t"
      "ds_read_b128 v[108:111], v255 offset:27648\n\t"
      "s_waitcnt lgkmcnt(11)\n\t"
      "v_mfma_f32_16x16x32_bf16 v[0:3], a[32:35], v[64:67], v[0:3]\n\t"
      "v_mfma_f32_16x16x32_bf16 v[4:7], a[96:99], v[64:67], v[4:7]\n\t"
      "ds_read_b128 v[112:115], v255 offset:28672\n\t"
      "s_waitcnt lgkmcnt(11)\n\t"
      "v_mfma_f32_16x16x32_bf16 v[8:11], a[32:35], v[68:71], v[8:11]\n\t"
      "v_mfma_f32_16x16x32_bf16 v[12:15], a[96:99], v[68:71], v[12:15]\n\t"
      "ds_read_b128 v[116:119], v255 offset:29696\n\t"
      "s_waitcnt lgkmcnt(11)\n\t"
      "v_mfma_f32_16x16x32_bf16 v[0:3], a[36:39], v[72:75], v[0:3]\n\t"
      "v_mfma_f32_16x16x32_bf16 v[4:7], a[100:103], v[72:75], v[4:7]\n\t"
      "ds_read_b128 v[120:123], v255 offset:30720\n\t"
      "s_waitcnt lgkmcnt(11)\n\t"
      "v_mfma_f32_16x16x32_bf16 v[8:11], a[36:39], v[76:79], v[8:11]\n\t"
      "v_mfma_f32_16x16x32_bf16 v[12:15], a[100:103], v[76:79], v[12:15]\n\t"
      "ds_read_b128 v[124:127], v255 offset:31744\n\t"
      "s_waitcnt lgkmcnt(11)\n\t"
      "v_mfma_f32_16x16x32_bf16 v[0:3], a[40:43], v[80:83], v[0:3]\n\t"
      "v_mfma_f32_16x16x32_bf16 v[4:7], a[104:107], v[80:83], v[4:7]\n\t"
      "s_waitcnt lgkmcnt(10)\n\t"
      "v_mfma_f32_16x16x32_bf16 v[8:11], a[40:43], v[84:87], v[8:11]\n\t"
      "v_mfma_f32_16x16x32_bf16 v[12:15], a[104:107], v[84:87], v[12:15]\n\t"
      "s_waitcnt lgkmcnt(9)\n\t"
      "v_mfma_f32_16x16x32_bf16 v[0:3], a[44:47], v[88:91], v[0:3]\n\t"
      "v_mfma_f32_16x16x32_bf16 v[4:7], a[108:111], v[88:91], v[4:7]\n\t"
      "s_waitcnt lgkmcnt(8)\n\t"
      "v_mfma_f32_16x16x32_bf16 v[8:11], a[44:47], v[92:95], v[8:11]\n\t"
      "v_mfma_f32_16x16x32_bf16 v[12:15], a[108:111], v[92:95], v[12:15]\n\t"
      "s_waitcnt lgkmcnt(7)\n\t"
      "v_mfma_f32_16x16x32_bf16 v[0:3], a[48:51], v[96:99], v[0:3]\n\t"
      "v_mfma_f32_16x16x32_bf16 v[4:7], a[112:115], v[96:99], v[4:7]\n\t"
      "s_waitcnt lgkmcnt(6)\n\t"
      "v_mfma_f32_16x16x32_bf16 v[8:11], a[48:51], v[100:103], v[8:11]\n\t"
      "v_mfma_f32_16x16x32_bf16 v[12:15], a[112:115], v[100:103], v[12:15]\n\t"
      "s_waitcnt lgkmcnt(5)\n\t"
      "v_mfma_f32_16x16x32_bf16 v[0:3], a[52:55], v[104:107], v[0:3]\n\t"
      "v_mfma_f32_16x16x32_bf16 v[4:7], a[116:119], v[104:107], v[4:7]\n\t"
      "s_waitcnt lgkmcnt(4)\n\t"
      "v_mfma_f32_16x16x32_bf16 v[8:11], a[52:55], v[108:111], v[8:11]\n\t"
      "v_mfma_f32_16x16x32_bf16 v[12:15], a[116:119], v[108:111], v[12:15]\n\t"
      "s_waitcnt lgkmcnt(3)\n\t"
      "v_mfma_f32_16x16x32_bf16 v[0:3], a[56:59], v[112:115], v[0:3]\n\t"
      "v_mfma_f32_16x16x32_bf16 v[4:7], a[120:123], v[112:115], v[4:7]\n\t"
      "s_waitcnt lgkmcnt(2)\n\t"
      "v_mfma_f32_16x16x32_bf16 v[8:11], a[56:59], v[116:119], v[8:11]\n\t"
      "v_mfma_f32_16x16x32_bf16 v[12:15], a[120:123], v[116:119], v[12:15]\n\t"
      "s_waitcnt lgkmcnt(1)\n\t"
      "v_mfma_f32_16x16x32_bf16 v[0:3], a[60:63], v[120:123], v[0:3]\n\t"
      "v_mfma_f32_16x16x32_bf16 v[4:7], a[124:127], v[120:123], v[4:7]\n\t"
      "s_waitcnt lgkmcnt(0)\n\t"
      "v_mfma_f32_16x16x32_bf16 v[8:11], a[60:63], v[124:127], v[8:11]\n\t"
      "v_mfma_f32_16x16x32_bf16 v[12:15], a[124:127], v[124:127], v[12:15]\n\t"
      "v_xor_b32 v254, 0x8000, v254\n\t"
      "v_xor_b32 v255, 0x8000, v255\n\t"
      "s_add_u32 s69, s53, 0x4000\n\t"
      "s_add_u32 s48, s48, 4\n\t"
      "s_waitcnt vmcnt(40)\n\t"
      "v_cvt_pk_bf16_f32 v0, v0, v1\n\t"
      "v_cvt_pk_bf16_f32 v1, v2, v3\n\t"
      "v_cvt_pk_bf16_f32 v2, v4, v5\n\t"
      "v_cvt_pk_bf16_f32 v3, v6, v7\n\t"
      "v_min_u32 v0, -2, v0\n\t"
      "v_min_u32 v1, -2, v1\n\t"
      "v_min_u32 v2, -2, v2\n\t"
      "v_min_u32 v3, -2, v3\n\t"
      "buffer_store_dwordx4 v[0:3], v238, s[36:39], s53 offen sc1\n\t"
      "v_cvt_pk_bf16_f32 v8, v8, v9\n\t"
      "v_cvt_pk_bf16_f32 v9, v10, v11\n\t"
      "v_cvt_pk_bf16_f32 v10, v12, v13\n\t"
      "v_cvt_pk_bf16_f32 v11, v14, v15\n\t"
      "v_min_u32 v8, -2, v8\n\t"
      "v_min_u32 v9, -2, v9\n\t"
      "v_min_u32 v10, -2, v10\n\t"
      "v_min_u32 v11, -2, v11\n\t"
      "buffer_store_dwordx4 v[8:11], v238, s[36:39], s69 offen sc1\n\t"
      "s_add_u32 s53, s53, 1048576\n\t"
      "s_nop 1\n\t"
      "s_branch WTOP%=\n\t"
      "WEND%=:\n\t"
      : "+{v[0:15]}"(c0), "+{v[16:31]}"(c1), "+{v[240:255]}"(c2), "+{v238}"(c3), "+{v[32:39]}"(c4), "+s"(w), "+s"(wv), "+s"(A),
        "+{a[0:31]}"(wo.a), "+{a[32:63]}"(wo.b), "+{a[64:95]}"(wo.c), "+{a[96:127]}"(wo.d)
      :
      : "memory", "vcc", "scc", "v40", "v41", "v42", "v43", "v44", "v45", "v46", "v47", "v48", "v49", "v50", "v51", "v52", "v53", "v54", "v55", "v56", "v57", "v58", "v59", "v60", "v61", "v62", "v63", "v64", "v65", "v66", "v67", "v68", "v69", "v70", "v71", "v72", "v73", "v74", "v75", "v76", "v77", "v78", "v79", "v80", "v81", "v82", "v83", "v84", "v85", "v86", "v87", "v88", "v89", "v90", "v91", "v92", "v93", "v94", "v95", "v96", "v97", "v98", "v99", "v100", "v101", "v102", "v103", "v104", "v105", "v106", "v107", "v108", "v109", "v110", "v111", "v112", "v113", "v114", "v115", "v116", "v117", "v118", "v119", "v120", "v121", "v122", "v123", "v124", "v125", "v126", "v127", "v128", "v129", "v130", "v131", "v132", "v133", "v134", "v135", "v136", "v137", "v138", "v139", "v140", "v141", "v142", "v143", "v144", "v145", "v146", "v147", "v148", "v149", "v150", "v151", "v152", "v153", "v154", "v155", "v156", "v157", "v158", "v159", "v160", "v161", "v162", "v163", "v164", "v165", "v166", "v167", "v168", "v169", "v170", "v171", "v172", "v173", "v174", "v175", "v176", "v177", "v178", "v179", "v180", "v181", "v182", "v183", "v184", "v185", "v186", "v187", "v188", "v189", "v190", "v191", "v192", "v193", "v194", "v195", "v196", "v197", "v198", "v199", "v200", "v201", "v202", "v203", "v204", "v205", "v206", "v207", "v208", "v209", "v210", "v211", "v212", "v213", "v214", "v215", "v216", "v217", "v218", "v219", "v220", "v221", "v222", "v223", "v224", "v225", "v226", "v227", "v228", "v229", "v230", "v231", "v232", "v233", "v234", "v235", "v236", "v237", "v239", "a128", "a129", "a130", "a131", "a132", "a133", "a134", "a135", "a136", "a137", "a138", "a139", "a140", "a141", "a142", "a143", "a144", "a145", "a146", "a147", "a148", "a149", "a150", "a151", "a152", "a153", "a154", "a155", "a156", "a157", "a158", "a159", "a160", "a161", "a162", "a163", "a164", "a165", "a166", "a167", "a168", "a169", "a170", "a171", "a172", "a173", "a174", "a175", "a176", "a177", "a178", "a179", "a180", "a181", "a182", "a183", "a184", "a185", "a186", "a187", "a188", "a189", "a190", "a191", "a192", "a193", "a194", "a195", "a196", "a197", "a198", "a199", "a200", "a201", "a202", "a203", "a204", "a205", "a206", "a207", "a208", "a209", "a210", "a211", "a212", "a213", "a214", "a215", "a216", "a217", "a218", "a219", "a220", "a221", "a222", "a223", "a224", "a225", "a226", "a227", "a228", "a229", "a230", "a231", "a232", "a233", "a234", "a235", "a236", "a237", "a238", "a239", "a240", "a241", "a242", "a243", "a244", "a245", "a246", "a247", "a248", "a249", "a250", "a251", "a252", "a253", "a254", "a255", "s24", "s36", "s37", "s38", "s39", "s40", "s41", "s42", "s43", "s44", "s45", "s46", "s47", "s48", "s49", "s50", "s51", "s52", "s53", "s54", "s55", "s56", "s57", "s58", "s59", "s60", "s61", "s62", "s63", "s64", "s65", "s66", "s67", "s68", "s69", "s70", "s71", "s72", "s73", "s74", "s75", "s76", "s77", "s78", "s79", "s80", "s81", "s82", "s83", "s84", "s85", "s86", "s87", "s88", "s89", "s90", "s91", "s92", "s93", "s94", "s95", "s96", "s97", "s98", "s99");
}

__global__ void __launch_bounds__(256, 1) mega_kernel(MegaParams P) {
  __shared__ __attribute__((aligned(16))) char lds[LDS_BYTES];
  int b, wv;
  KArgs A = (KArgs)__builtin_amdgcn_kernarg_segment_ptr();
  u32x16 ka; u32x8 kb;
  asm volatile("s_load_dwordx16 s[36:51], %4, 0\n\t"
               "s_load_dwordx8 s[52:59], %4, 64\n\t"
               "s_getpc_b64 s[60:61]\n"
               "KPC:\n\t"
               "s_lshr_b32 %0, %5, 3\n\t"
               "s_and_b32 %0, %0, 7\n\t"
               "s_lshl_b32 %0, %0, 14\n\t"
               "v_lshl_add_u32 v254, %6, 6, %0\n\t"
               "v_min_u32 v254, KENDW-KPC, v254\n\t"
               "global_load_dword v255, v254, s[60:61]\n\t"
               "v_readfirstlane_b32 %1, %6\n\t"
               "s_lshr_b32 %0, %5, 1\n\t"
               "s_andn2_b32 %0, %0, 3\n\t"
               "s_and_b32 s60, %5, 3\n\t"
               "s_or_b32 %0, %0, s60\n\t"
               "s_lshr_b32 %1, %1, 6\n\t"
               "s_bitcmp1_b32 %5, 2\n\t"
               "s_cbranch_scc1 GSTART\n\t"
               "s_waitcnt lgkmcnt(0)"
               : "=&{s34}"(b), "=&{s33}"(wv), "=&{s[36:51]}"(ka), "=&{s[52:59]}"(kb), "+{s[0:1]}"(A)
               : "s"(blockIdx.x), "v"(threadIdx.x) : "v252", "v253", "v254", "v255", "s60", "s61", "s62", "s63", "scc");
  static_assert(XS == 0, "");
  if (b >= NREC) {
    int w = b, wv2 = wv;
    WoRegs wo;
    const unsigned tag = worker_role(w, wv2, A, wo);
    worker_tasks(lds, w, wv2, A, tag, wo);
    if (w == 0 && wv2 == 0) {
      const int lane = fresh_lane();
#pragma unroll 1
      for (unsigned spins = 0; spins < (1u << 16); ++spins) {
        const u32x2 sel = top2_wave((const float*)(A->ws + O_RBUF) + S_, lane, spins == 0, spins < 8);
        if (spins && sel[1]) {
          if (lane < 8) __hip_atomic_store((gu32*)(A->ws + O_CNT + lane * 64 + 8), (unsigned)__builtin_amdgcn_s_memrealtime(), RLX_AGENT);
          if (lane < 8) __hip_atomic_store((gu64*)(A->ws + O_CNT + lane * 64), (u64)sel[0] | ((u64)sel[1] << 32), RLX_AGENT);
          break;
        }
        __builtin_amdgcn_s_sleep(SSLEEP);
        if (spins == 0 && SWAIT) {
          unsigned f = 0;
#pragma unroll 1
          for (unsigned n = 0; n < 4096; ++n) {
            f = __builtin_amdgcn_readfirstlane(__hip_atomic_load((gu32*)(A->ws + O_KVSTEP), RLX_AGENT));
            if ((f >> 16) >= 61) break;
            __builtin_amdgcn_s_sleep(4);
          }
          f += SWAIT - ((f >> 16) - 61) * 156;
#pragma unroll 1
          while (((f - (unsigned)__builtin_amdgcn_s_memrealtime()) & 0xffffu) < 400u) __builtin_amdgcn_s_sleep(1);
        }
      }
    }
    __syncthreads();
    if ((w & 2) == 0) mlp2_role(A, lds, (w & 1) * 32 + (w >> 2), wv2 * 64 + fresh_lane());
    asm volatile("KENDW:");
    __builtin_amdgcn_endpgm();
    __builtin_unreachable();
  } else {
    KArgs kp = A;
    rec_role(lds, kp, b, wv, ka, kb);
    const int tid = wv * 64 + fresh_lane();
    __syncthreads();
    if ((b & 31) < M1_WGS / 4) mlp1_role(kp, lds, (b & 31) * 4 + (b >> 5), tid);
    else if (SW2 && b == M1_WGS / 4 + 1) {
      if (wv == 0) {
        const unsigned t0 = (unsigned)__builtin_amdgcn_s_memrealtime();
#pragma unroll 1
        for (unsigned spins = 0; spins < (1u << 16); ++spins) {
          const u32x2 sel = top2_wave((const float*)(kp->ws + O_RBUF) + S_, tid, spins == 0, false);
          if (spins && sel[1]) {
            if (tid < 32) __hip_atomic_store((gu64*)(kp->ws + O_SEL2 + tid * 128), (u64)sel[0] | ((u64)sel[1] << 32), RLX_AGENT);
            break;
          }
#pragma unroll 1
          while ((unsigned)__builtin_amdgcn_s_memrealtime() - t0 < SW2) __builtin_amdgcn_s_sleep(1);
        }
      }
    }
    else if (b == M1_WGS / 4) {
      if (tid == 64) {
        gu32* e = (gu32*)(kp->ws + O_EPOCH);
        __hip_atomic_store(e, mktag(__hip_atomic_load(e, RLX_AGENT)), RLX_AGENT);
      }
    }
    __builtin_amdgcn_endpgm();
    __builtin_unreachable();
  }
}

extern "C" void kernel_launch(void* const* d_in, const int* in_sizes, int n_in, void* d_out, int out_size, void* d_ws, size_t ws_size,
                              hipStream_t stream) {
  MegaParams P;
  P.x = (const u16*)d_in[0]; P.mem0 = (const u16*)d_in[1];
  P.Wq = (const u16*)d_in[2]; P.Wk = (const u16*)d_in[3]; P.Wv = (const u16*)d_in[4]; P.Wo = (const u16*)d_in[5];
  P.w_r = (const u16*)d_in[8]; P.wg = (const u16*)d_in[9]; P.wu = (const u16*)d_in[10]; P.wd = (const u16*)d_in[11];
  P.out = (u16*)d_out; P.ws = (char*)d_ws;
  mega_kernel<<<256, 256, 0, stream>>>(P);
}
```

```cpp
#include <hip/hip_runtime.h>
#include <hip/hip_bf16.h>
#define SWAIT 0
#define SW2 0
#define ASLEEP 32
#define ASALL 0
#define PSLEEP 8
#define FSLEEP 6
#define RDLY 250
#define RDL2 100
#define ADLY 130
#define CPF 1
#define TCH 0
#define SNEED 0
#define MPF 1

typedef unsigned short u16;
typedef unsigned long long u64;
typedef __attribute__((ext_vector_type(8))) short bf16x8;
typedef __attribute__((ext_vector_type(4))) float f32x4;
typedef __attribute__((ext_vector_type(4))) unsigned u32x4;
#define GAS __attribute__((address_space(1)))
typedef GAS u64 gu64;
typedef GAS unsigned gu32;
#define RLX_AGENT __ATOMIC_RELAXED, __HIP_MEMORY_SCOPE_AGENT

constexpr int S_ = 2048, DM = 512, NH = 32, DH = 128, MM = 8, SEG = 32, NSEG = 64;
constexpr int HD = NH * DH;
constexpr int QKVN = 3 * HD;
constexpr int FF = 1792;

__device__ __forceinline__ float bf2f(u16 v) { return __builtin_bit_cast(float, (unsigned)v << 16); }
__device__ __forceinline__ u16 f2bf(float f) { unsigned u = __builtin_bit_cast(unsigned, f); return (u16)((u + 0x7fffu + ((u >> 16) & 1u)) >> 16); }
__device__ __forceinline__ float rbf(float f) { return bf2f(f2bf(f)); }

typedef __attribute__((ext_vector_type(2))) float f32x2;
typedef __attribute__((ext_vector_type(2))) __bf16 bf16x2_t;
typedef __attribute__((ext_vector_type(2))) unsigned u32x2;
__device__ __forceinline__ unsigned pk2(float a, float b) { return __builtin_bit_cast(unsigned, __builtin_convertvector((f32x2){a, b}, bf16x2_t)); }
__device__ __forceinline__ unsigned f2u(float f) { return __builtin_bit_cast(unsigned, f); }
__device__ __forceinline__ float u2f(unsigned u) { return __builtin_bit_cast(float, u); }
__device__ __forceinline__ float lo2f(unsigned u) { return __builtin_bit_cast(float, u << 16); }
__device__ __forceinline__ float hi2f(unsigned u) { return __builtin_bit_cast(float, u & 0xffff0000u); }
__device__ __forceinline__ void unpack8(const u32x4 v, float (&f)[8]) {
#pragma unroll
  for (int i = 0; i < 4; ++i) { f[2 * i] = lo2f(v[i]); f[2 * i + 1] = hi2f(v[i]); }
}

constexpr unsigned O_EPOCH = 0;
constexpr unsigned O_TFLAG = 256;
constexpr unsigned O_MFLAG = O_TFLAG + 16 * 32 * 64;
constexpr unsigned O_LFLAG = O_MFLAG + 32 * 128;
constexpr unsigned O_WM = O_LFLAG + 128 * 128;
constexpr unsigned O_KVSTEP = O_WM + 128 * 128;
constexpr unsigned O_CNT = O_KVSTEP + 2048;
constexpr unsigned O_PFLAG = O_CNT + 2048;
constexpr unsigned O_SEL2 = O_PFLAG + 4 * 128 * 128;
constexpr unsigned O_ACT = O_SEL2 + 32 * 128;
constexpr unsigned O_RBUF = O_ACT + 3 * 112 * 128;
constexpr unsigned O_INIT_END = O_RBUF + 2 * S_ * 4 + 256 * 128;
constexpr unsigned O_STAMPS = 196608 + 65 * 32768;
constexpr unsigned O_XN = O_STAMPS + (4 << 20);
constexpr unsigned O_QKV = O_XN + S_ * DM * 2;
constexpr unsigned O_KVM = O_QKV + S_ * QKVN * 2;
constexpr unsigned O_VTM = O_KVM + NSEG * 8 * HD * 2;
constexpr unsigned O_PART = O_VTM + NSEG * 8 * HD * 2;
constexpr unsigned O_HBUF = O_PART + S_ * 8 * DM * 4;
constexpr unsigned O_LNH = O_HBUF + S_ * DM * 2;
constexpr unsigned O_MFRAG = O_LNH + S_ * DM * 2;
constexpr unsigned O_SLOTS = (O_MFRAG + 2 * DM * FF * 2 + 0x1fffff) & ~0x1fffffu;
constexpr unsigned WS_BYTES = O_SLOTS + 65 * 32768;
#define QO(i) (((i) & 1) * 16384 + ((i) >> 1) * 8192)
#define CAL(b) (O_SLOTS + 128 + ((b) >> 3) * 16384 + ((b) & 7) * 1024)
static_assert(O_INIT_END <= O_STAMPS && (O_INIT_END - O_KVSTEP) / 16 <= 128 * 128, "layout");
static_assert(O_TFLAG == 256 && O_MFLAG == 33024 && O_LFLAG == 37120 && O_STAMPS == 2326528 && O_XN == 6520832 && O_QKV == 8617984 && O_KVM == 58949632 && O_VTM == 63143936 && O_PART == 67338240 && O_MFRAG == 105086976 && O_SLOTS == 109051904 && O_CNT + 1024 == 72960 && O_INIT_END <= 262144 && O_WM == 53504, "layout");
constexpr int L_BS = 520, L_QN = 49152, L_TB = 98304, L_VT = 32768;
struct MegaParams {
  const u16* x; const u16* mem0; const u16* Wq; const u16* Wk; const u16* Wv; const u16* Wo;
  const u16* w_r; const u16* wg; const u16* wu; const u16* wd;
  u16* out;
  char* ws;
};
__device__ __forceinline__ unsigned mktag(unsigned e) { return e * 0x9e3779b1u + 0x7f4a7c15u; }

typedef __attribute__((ext_vector_type(4))) short s16x4;
#define LAS __attribute__((address_space(3)))
constexpr int LDS_BYTES = 163840;
#define SB() __builtin_amdgcn_sched_barrier(0)
#define LDS_BARRIER() asm volatile("s_waitcnt lgkmcnt(0)\n\ts_barrier" ::: "memory")
#define VM_DRAIN() asm volatile("s_waitcnt vmcnt(0)" ::: "memory")
__device__ __forceinline__ float xmax16_32(float m) {
  u32x2 p = __builtin_amdgcn_permlane16_swap(f2u(m), f2u(m), false, false);
  m = fmaxf(u2f(p[0]), u2f(p[1]));
  p = __builtin_amdgcn_permlane32_swap(f2u(m), f2u(m), false, false);
  return fmaxf(u2f(p[0]), u2f(p[1]));
}
__device__ __forceinline__ float xsum16_32(float m) {
  u32x2 p = __builtin_amdgcn_permlane16_swap(f2u(m), f2u(m), false, false);
  m = u2f(p[0]) + u2f(p[1]);
  p = __builtin_amdgcn_permlane32_swap(f2u(m), f2u(m), false, false);
  return u2f(p[0]) + u2f(p[1]);
}
__device__ __forceinline__ unsigned wave_max_u32(unsigned v) {
  v = max(v, (unsigned)__builtin_amdgcn_mov_dpp(v, 0xb1, 0xf, 0xf, true));
  v = max(v, (unsigned)__builtin_amdgcn_mov_dpp(v, 0x4e, 0xf, 0xf, true));
  v = max(v, (unsigned)__builtin_amdgcn_mov_dpp(v, 0x124, 0xf, 0xf, true));
  v = max(v, (unsigned)__builtin_amdgcn_mov_dpp(v, 0x128, 0xf, 0xf, true));
  u32x2 p = __builtin_amdgcn_permlane16_swap(v, v, false, false);
  v = max(p[0], p[1]);
  p = __builtin_amdgcn_permlane32_swap(v, v, false, false);
  return max(p[0], p[1]);
}
__device__ __forceinline__ u32x4 ld_sc1(__amdgpu_buffer_rsrc_t rs, unsigned off) { return __builtin_amdgcn_raw_buffer_load_b128(rs, off, 0, 16); }
__device__ __forceinline__ u32x2 ld8_sc1(__amdgpu_buffer_rsrc_t rs, unsigned off) { return __builtin_amdgcn_raw_buffer_load_b64(rs, off, 0, 16); }

#define XS 0
#define VBX __builtin_amdgcn_readfirstlane((int)(((blockIdx.x & 4) << 5) + (blockIdx.x >> 3) * 4 + (blockIdx.x & 3)))
constexpr int NREC = 128, NWORK = 128;
typedef __attribute__((ext_vector_type(8))) unsigned u32x8;
typedef __attribute__((ext_vector_type(16))) unsigned u32x16;
typedef __attribute__((ext_vector_type(32))) unsigned u32x32;
#define RSRC(p, n) __builtin_amdgcn_make_buffer_rsrc((void*)(p), 0, (n), 0x00020000)

__device__ __forceinline__ int fresh_lane() {
  int l;
  asm volatile("v_mbcnt_lo_u32_b32 %0, -1, 0\n\tv_mbcnt_hi_u32_b32 %0, -1, %0" : "=v"(l));
  return l;
}
typedef const __attribute__((address_space(4))) MegaParams* KArgs;
#define ARGS() KArgs A = (KArgs)__builtin_amdgcn_kernarg_segment_ptr(); asm volatile("" : "+s"(A))
#define MS(k)
__device__ __forceinline__ void rec_role(char* lds, KArgs& A, int& b, int& wv, const u32x16 ka, const u32x8 kb) {
#define HMAP 0
  const int h = HMAP == 1 ? (b & 3) * 8 + ((b >> 2) & 7) : (HMAP == 2 ? (b & 1) * 16 + (b & 2) / 2 + ((b >> 2) & 7) * 2 : b & 31), part = b >> 5;
  const int lane = fresh_lane(), l15 = lane & 15, l4 = lane >> 4;
  const int b1 = (l15 >> 1) & 1, mrow = 2 * (l15 >> 2) + (l15 & 1);
  const int r4 = l15 >> 2, c4 = l15 & 3;
#define KA(v, i) ((u64)v[i] | (u64)v[(i) + 1] << 32)
  const u64 wsa = KA(kb, 6), woa = KA(ka, 10), wva = KA(ka, 8), wqa = KA(ka, 4), wka = KA(ka, 6), m0a = KA(ka, 2);
  const unsigned awv = (unsigned)((8 * wv + l4) * 8192 + h * 256 + ((l15 ^ (l4 | (wv << 2))) << 4)), rot = 1 ? part : 0;
  const int sig = 1 ? (h >> 2) & 1 : 0;
  const int sig2 = 1 ? (h >> 3) & 1 : 0;
  unsigned tch;
  asm volatile(
      "s_lshl_b32 s91, %5, 4\n\t"
      "s_add_u32 m0, %5, 0\n\t"
      "s_xor_b32 s90, s91, 0\n\t"
      "buffer_load_dwordx4 %1, %3, s90 offen lds\n\t"
      "s_add_u32 m0, %5, 1024\n\t"
      "s_xor_b32 s90, s91, 16384\n\t"
      "buffer_load_dwordx4 %1, %3, s90 offen lds\n\t"
      "s_add_u32 m0, %5, 8192\n\t"
      "s_xor_b32 s90, s91, 32768\n\t"
      "buffer_load_dwordx4 %1, %3, s90 offen lds\n\t"
      "s_add_u32 m0, %5, 9216\n\t"
      "s_xor_b32 s90, s91, 49152\n\t"
      "buffer_load_dwordx4 %1, %3, s90 offen lds\n\t"
      "s_add_u32 m0, %5, 16384\n\t"
      "s_xor_b32 s90, s91, 65536\n\t"
      "buffer_load_dwordx4 %1, %3, s90 offen lds\n\t"
      "s_add_u32 m0, %5, 17408\n\t"
      "s_xor_b32 s90, s91, 81920\n\t"
      "buffer_load_dwordx4 %1, %3, s90 offen lds\n\t"
      "s_add_u32 m0, %5, 24576\n\t"
      "s_xor_b32 s90, s91, 98304\n\t"
      "buffer_load_dwordx4 %1, %3, s90 offen lds\n\t"
      "s_add_u32 m0, %5, 25600\n\t"
      "s_xor_b32 s90, s91, 114688\n\t"
      "buffer_load_dwordx4 %1, %3, s90 offen lds\n\t"
      : "=&{s87}"(tch)
      : "v"((unsigned)((h * DH + (lane >> 2)) * DM + 128 * part + 32 * wv + 8 * (2 * (lane & 1) + (((lane >> 1) & 1) ^ sig))) * 2), "v"(awv), "s"(RSRC(woa, DM * HD * 2)), "s"(RSRC(wva, DM * HD * 2)),
        "s"(wv * 2048), "s"(rot << 19), "s"(wva), "s"(wqa), "s"(wka), "s"(wsa), "s"(m0a)
      : "memory", "scc", "s90", "s91", "s92");
  SB();
  const unsigned i0 = 1024 + b * 512 + wv * 64 + lane, o1 = O_KVSTEP + ((wv >> 1) * 8192 + b * 64 + lane) * 16;
  const unsigned oz = wv == 2 ? CAL(b ^ 1) + (lane >> 5) * 8192 + ((lane >> 3) & 3) * 256 + (lane & 7) * 16 : ((wv & 1) && o1 < O_INIT_END ? o1 : 0xffffff00u);
  const int sl = lane ^ ((l4 & 1) << 1), su = ((l15 >> 3) * 16 + c4 * 4 + l4) ^ ((l15 >> 3) << 1);
  const int jt = wv ^ rot;
  u32x8 cc = {oz, (unsigned)lane, (unsigned)lane,
              wv == 2 || o1 < O_ACT ? 0u : ~0u,
              (unsigned)(QO(jt >> 1) + (((jt & 1) * 2 + (l4 >> 1)) * 8 + (r4 & 1) * 2 + (l15 >> 3)) * 256 + ((l15 & 1) ^ sig2) * 64 + (2 * b1 + (l4 & 1)) * 16),
              (unsigned)(h * 2048 + (2 * (part & 1) + (l4 >> 1)) * 512 + (2 * b1 + (l4 & 1)) * 128 + mrow * 16),
              (unsigned)(h * 2048 + part * 512 + (l4 >> 1) * 256 + (8 * wv + 4 * (l15 >> 3) + c4) * 16 + (r4 & 1) * 8 + (l4 & 1) * 4),
              (unsigned)((2 * (wv >> 1) + (l4 >> 1)) * 2048 + ((2 * (wv & 1) + (l4 & 1)) * 16 + l15) * 16)};
  u32x8 c3 = {(unsigned)lane, O_SLOTS + (i0 >> 3) * 256 + (i0 & 7) * 16,
              (unsigned)(wv * 2048 + (8 * l4 + r4) * 64 + c4 * 16),
              (unsigned)(wv * 2048 + (r4 >> 1) * 1024 + (l4 * 16 + 8 * (c4 >> 1) + 4 * (r4 & 1) + (c4 & 1)) * 16),
              (unsigned)(L_VT + (8 * r4 + c4) * 256 + (((4 * wv + l4) ^ l15) << 4)),
              awv,
              (unsigned)(L_VT + ((wv & 1) * 32 + 8 * l4 + r4) * 256 + (((4 * part + 2 * (wv >> 1) + (c4 >> 1)) ^ (r4 | (l4 << 2))) << 4) + (c4 & 1) * 8),
              (unsigned)(QO(part) + h * 256 + (lane & 7) * 16)};
  const int L = (h & 7) * 8 + (lane & 7);
  const u64 mp = 1 ? (u64)((const u16*)m0a + (4 * (l15 >> 3) + 2 * ((l15 & 1) ^ sig2) + (r4 & 1)) * DM + 64 * jt + 16 * l4 + 2 * b1) :
                (u64)((const u16*)m0a + (2 * ((L >> 2) & 3) + ((L >> 4) & 1)) * DM + 128 * part + 32 * (h >> 3) + 8 * (L >> 5) + 16 * (L & 1) + (L & 2));
  u32x8 c0 = {(unsigned)((8 * wv + l4) * 8192 + h * 256 + ((l15 ^ (2 * l4 + 8 * (wv & 1))) << 4)),
              (unsigned)(L_TB + (8 * l4 + r4) * 256 + (((4 * wv + (c4 >> 1)) ^ (2 * r4 + 8 * (l4 & 1))) << 4) + (c4 & 1) * 8),
              (unsigned)mp, (unsigned)(mp >> 32), (unsigned)(CAL(b) + wv * 256 + ((wv ^ (b >> 2)) & 1) * 8192) & (unsigned)((b - 32) >> 31) & (unsigned)((1 + 1 - wv) >> 31), (unsigned)(QO(part) + sig * 1024 + sig2 * 512),
              b1 ? 0xEEEEu : 0x4444u, (unsigned)((wv * 8 + (lane >> 5)) * 256 + (lane & 31) * 4)};
  u32x4 c1 = {(unsigned)(sl * 8),
                    (unsigned)(L_QN + ((wv >> 1) * 2 + (r4 & 1)) * L_BS + ((wv & 1) * 32 + su) * 8),
                    (unsigned)lane, (unsigned)lane * 16};
  const unsigned k0 = O_QKV + h * 24576, v0 = O_QKV + 8192 + SEG * QKVN * 2 + h * 24576 + wv * 2048;
  const unsigned role = wv < 2 ? wv : (wv == 3 ? 4 : (part < 2 ? 2 + part : 5));
  u32x16 sc = {(unsigned)lane, (unsigned)wv, role, O_SLOTS, k0, O_MFRAG + (min(h, 27) * 4 + part) * 32768 + wv * 4096, v0, rot << 19, O_KVM, O_VTM,
                     O_TFLAG + h * 64, O_KVSTEP + h * 64 + part * 4, O_STAMPS + (b * 4 + wv) * 65 * 64,
                     O_EPOCH, O_MFLAG + h * 128, (unsigned)wv * 2048};
  u32x16 s2 = {(unsigned)woa, (unsigned)(woa >> 32) & 0xffffu, DM * HD * 2, 0x00020000u, (unsigned)wva, (unsigned)(wva >> 32) & 0xffffu, O_WM, O_WM + b * 128,
               (unsigned)wsa, (unsigned)(wsa >> 32) & 0xffffu, WS_BYTES, 0x00020000u,
               (unsigned)wqa, (unsigned)(wqa >> 32) & 0xffffu, (unsigned)wka, (unsigned)(wka >> 32) & 0xffffu};
  asm volatile(
      "s_nop 1\n\t"
      "v_readfirstlane_b32 s40, v0\n\t"
      "v_readfirstlane_b32 s41, v1\n\t"
      "v_readfirstlane_b32 s42, v2\n\t"
      "v_readfirstlane_b32 s43, v3\n\t"
      "v_readfirstlane_b32 s44, v4\n\t"
      "v_readfirstlane_b32 s45, v5\n\t"
      "v_readfirstlane_b32 s46, v6\n\t"
      "v_readfirstlane_b32 s47, v7\n\t"
      "v_readfirstlane_b32 s48, v8\n\t"
      "v_readfirstlane_b32 s49, v9\n\t"
      "v_readfirstlane_b32 s50, v10\n\t"
      "v_readfirstlane_b32 s51, v11\n\t"
      "v_readfirstlane_b32 s52, v12\n\t"
      "v_readfirstlane_b32 s53, v13\n\t"
      "v_readfirstlane_b32 s54, v14\n\t"
      "v_readfirstlane_b32 s55, v15\n\t"
      "v_readfirstlane_b32 s88, v16\n\t"
      "v_readfirstlane_b32 s89, v17\n\t"
      "v_readfirstlane_b32 s90, v18\n\t"
      "v_readfirstlane_b32 s91, v19\n\t"
      "v_readfirstlane_b32 s92, v20\n\t"
      "v_readfirstlane_b32 s93, v21\n\t"
      "v_readfirstlane_b32 s94, v22\n\t"
      "v_readfirstlane_b32 s95, v23\n\t"
      "v_readfirstlane_b32 s36, v24\n\t"
      "v_readfirstlane_b32 s37, v25\n\t"
      "v_readfirstlane_b32 s38, v26\n\t"
      "v_readfirstlane_b32 s39, v27\n\t"
      "v_readfirstlane_b32 s96, v28\n\t"
      "v_readfirstlane_b32 s97, v29\n\t"
      "v_readfirstlane_b32 s98, v30\n\t"
      "v_readfirstlane_b32 s99, v31\n\t"
      "s_nop 4\n\t"
      "s_memtime s[80:81]\n\t"
      "s_memrealtime s[82:83]\n\t"
      "v_readfirstlane_b32 s86, v120\n\t"
      "v_add_u32 v100, s55, v127\n\t"
      "s_mov_b64 s[88:89], s[92:93]\n\t"
      "s_add_u32 m0, s55, 32768\n\t"
      "s_xor_b32 s61, s47, 0\n\t"
      "buffer_load_dwordx4 v105, s[88:91], s61 offen lds\n\t"
      "s_add_u32 m0, s55, 33792\n\t"
      "s_xor_b32 s61, s47, 32768\n\t"
      "buffer_load_dwordx4 v105, s[88:91], s61 offen lds\n\t"
      "s_add_u32 m0, s55, 40960\n\t"
      "s_xor_b32 s61, s47, 262144\n\t"
      "buffer_load_dwordx4 v105, s[88:91], s61 offen lds\n\t"
      "s_add_u32 m0, s55, 41984\n\t"
      "s_xor_b32 s61, s47, 294912\n\t"
      "buffer_load_dwordx4 v105, s[88:91], s61 offen lds\n\t"
      "s_add_u32 m0, s55, 49152\n\t"
      "s_xor_b32 s61, s47, 524288\n\t"
      "buffer_load_dwordx4 v105, s[88:91], s61 offen lds\n\t"
      "s_add_u32 m0, s55, 50176\n\t"
      "s_xor_b32 s61, s47, 557056\n\t"
      "buffer_load_dwordx4 v105, s[88:91], s61 offen lds\n\t"
      "s_add_u32 m0, s55, 57344\n\t"
      "s_xor_b32 s61, s47, 786432\n\t"
      "buffer_load_dwordx4 v105, s[88:91], s61 offen lds\n\t"
      "s_add_u32 m0, s55, 58368\n\t"
      "s_xor_b32 s61, s47, 819200\n\t"
      "buffer_load_dwordx4 v105, s[88:91], s61 offen lds\n\t"
      "buffer_load_dword v126, off, s[36:39], s53 sc1\n\t"
      "global_load_dword a172, v[118:119], off offset:0\n\t"
      "global_load_dword a173, v[118:119], off offset:8\n\t"
      "global_load_dword a174, v[118:119], off offset:16\n\t"
      "global_load_dword a175, v[118:119], off offset:24\n\t"
      "global_load_dword a236, v[118:119], off offset:512\n\t"
      "global_load_dword a237, v[118:119], off offset:520\n\t"
      "global_load_dword a238, v[118:119], off offset:528\n\t"
      "global_load_dword a239, v[118:119], off offset:536\n\t"
      "s_xor_b32 s61, s86, 0x2000\n\t"
      "s_buffer_load_dword s87, s[36:39], s61\n\t"
      "s_waitcnt vmcnt(17)\n\t"
      "s_waitcnt lgkmcnt(0)\n\t"
      "ds_read_b64_tr_b16 v[64:65], v102\n\t"
      "ds_read_b64_tr_b16 v[66:67], v102 offset:256\n\t"
      "ds_read_b64_tr_b16 v[68:69], v102 offset:8\n\t"
      "ds_read_b64_tr_b16 v[70:71], v102 offset:264\n\t"
      "ds_read_b64_tr_b16 v[72:73], v102 offset:8192\n\t"
      "ds_read_b64_tr_b16 v[74:75], v102 offset:8448\n\t"
      "ds_read_b64_tr_b16 v[76:77], v102 offset:8200\n\t"
      "ds_read_b64_tr_b16 v[78:79], v102 offset:8456\n\t"
      "ds_read_b64_tr_b16 v[80:81], v102 offset:16384\n\t"
      "ds_read_b64_tr_b16 v[82:83], v102 offset:16640\n\t"
      "ds_read_b64_tr_b16 v[84:85], v102 offset:16392\n\t"
      "ds_read_b64_tr_b16 v[86:87], v102 offset:16648\n\t"
      "s_waitcnt lgkmcnt(4)\n\t"
      "ds_read_b64_tr_b16 v[88:89], v102 offset:24576\n\t"
      "ds_read_b64_tr_b16 v[90:91], v102 offset:24832\n\t"
      "ds_read_b64_tr_b16 v[92:93], v102 offset:24584\n\t"
      "ds_read_b64_tr_b16 v[94:95], v102 offset:24840\n\t"
      "s_mov_b64 s[88:89], s[96:97]\n\t"
      "s_add_u32 m0, s55, 98304\n\t"
      "s_xor_b32 s61, s47, 0\n\t"
      "buffer_load_dwordx4 v116, s[88:91], s61 offen lds\n\t"
      "s_add_u32 m0, s55, 99328\n\t"
      "s_xor_b32 s61, s47, 32768\n\t"
      "buffer_load_dwordx4 v116, s[88:91], s61 offen lds\n\t"
      "s_add_u32 m0, s55, 106496\n\t"
      "s_xor_b32 s61, s47, 262144\n\t"
      "buffer_load_dwordx4 v116, s[88:91], s61 offen lds\n\t"
      "s_add_u32 m0, s55, 107520\n\t"
      "s_xor_b32 s61, s47, 294912\n\t"
      "buffer_load_dwordx4 v116, s[88:91], s61 offen lds\n\t"
      "s_waitcnt lgkmcnt(0)\n\t"
      "ds_write_b128 v103, v[64:67]\n\t"
      "ds_write_b128 v103, v[68:71] offset:32\n\t"
      "ds_write_b128 v103, v[72:75] offset:8192\n\t"
      "ds_write_b128 v103, v[76:79] offset:8224\n\t"
      "ds_write_b128 v103, v[80:83] offset:16384\n\t"
      "ds_write_b128 v103, v[84:87] offset:16416\n\t"
      "ds_write_b128 v103, v[88:91] offset:24576\n\t"
      "ds_write_b128 v103, v[92:95] offset:24608\n\t"
      "s_waitcnt lgkmcnt(4)\n\t"
      "ds_read_b128 v[32:35], v100\n\t"
      "ds_read_b128 v[36:39], v100 offset:1024\n\t"
      "ds_read_b128 v[40:43], v100 offset:8192\n\t"
      "ds_read_b128 v[44:47], v100 offset:9216\n\t"
      "ds_read_b128 v[48:51], v100 offset:16384\n\t"
      "ds_read_b128 v[52:55], v100 offset:17408\n\t"
      "ds_read_b128 v[56:59], v100 offset:24576\n\t"
      "ds_read_b128 v[60:63], v100 offset:25600\n\t"
      "v_xor_b32 v118, 32, v117\n\t"
      "v_xor_b32 v119, 64, v104\n\t"
      "v_xor_b32 v102, 128, v104\n\t"
      "v_xor_b32 v103, 192, v104\n\t"
      "s_mov_b64 s[88:89], s[92:93]\n\t"
      "s_add_u32 m0, s55, 65536\n\t"
      "s_xor_b32 s61, s47, 1048576\n\t"
      "buffer_load_dwordx4 v105, s[88:91], s61 offen lds\n\t"
      "s_add_u32 m0, s55, 66560\n\t"
      "s_xor_b32 s61, s47, 1081344\n\t"
      "buffer_load_dwordx4 v105, s[88:91], s61 offen lds\n\t"
      "s_mov_b64 s[88:89], s[96:97]\n\t"
      "s_add_u32 m0, s55, 114688\n\t"
      "s_xor_b32 s61, s47, 524288\n\t"
      "buffer_load_dwordx4 v116, s[88:91], s61 offen lds\n\t"
      "s_add_u32 m0, s55, 115712\n\t"
      "s_xor_b32 s61, s47, 557056\n\t"
      "buffer_load_dwordx4 v116, s[88:91], s61 offen lds\n\t"
      "s_add_u32 m0, s55, 122880\n\t"
      "s_xor_b32 s61, s47, 786432\n\t"
      "buffer_load_dwordx4 v116, s[88:91], s61 offen lds\n\t"
      "s_add_u32 m0, s55, 123904\n\t"
      "s_xor_b32 s61, s47, 819200\n\t"
      "buffer_load_dwordx4 v116, s[88:91], s61 offen lds\n\t"
      "s_waitcnt lgkmcnt(0)\n\t"
      "s_waitcnt vmcnt(25)\n\t"
      "s_barrier\n\t"
      "ds_read_b128 v[0:3], v104\n\t"
      "ds_read_b128 v[4:7], v119\n\t"
      "ds_read_b128 v[8:11], v102\n\t"
      "ds_read_b128 v[12:15], v103\n\t"
      "ds_read_b128 v[64:67], v104 offset:1024\n\t"
      "ds_read_b128 v[68:71], v119 offset:1024\n\t"
      "ds_read_b128 v[72:75], v102 offset:1024\n\t"
      "ds_read_b128 v[76:79], v103 offset:1024\n\t"
      "s_mov_b64 s[88:89], s[92:93]\n\t"
      "s_add_u32 m0, s55, 73728\n\t"
      "s_xor_b32 s61, s47, 1310720\n\t"
      "buffer_load_dwordx4 v105, s[88:91], s61 offen lds\n\t"
      "s_add_u32 m0, s55, 74752\n\t"
      "s_xor_b32 s61, s47, 1343488\n\t"
      "buffer_load_dwordx4 v105, s[88:91], s61 offen lds\n\t"
      "s_mov_b64 s[88:89], s[96:97]\n\t"
      "s_add_u32 m0, s55, 131072\n\t"
      "s_xor_b32 s61, s47, 1048576\n\t"
      "buffer_load_dwordx4 v116, s[88:91], s61 offen lds\n\t"
      "s_add_u32 m0, s55, 132096\n\t"
      "s_xor_b32 s61, s47, 1081344\n\t"
      "buffer_load_dwordx4 v116, s[88:91], s61 offen lds\n\t"
      "s_add_u32 m0, s55, 139264\n\t"
      "s_xor_b32 s61, s47, 1310720\n\t"
      "buffer_load_dwordx4 v116, s[88:91], s61 offen lds\n\t"
      "s_add_u32 m0, s55, 140288\n\t"
      "s_xor_b32 s61, s47, 1343488\n\t"
      "buffer_load_dwordx4 v116, s[88:91], s61 offen lds\n\t"
      "s_mov_b64 s[88:89], s[92:93]\n\t"
      "s_add_u32 m0, s55, 81920\n\t"
      "s_xor_b32 s61, s47, 1572864\n\t"
      "buffer_load_dwordx4 v105, s[88:91], s61 offen lds\n\t"
      "s_add_u32 m0, s55, 82944\n\t"
      "s_xor_b32 s61, s47, 1605632\n\t"
      "buffer_load_dwordx4 v105, s[88:91], s61 offen lds\n\t"
      "s_waitcnt vmcnt(31)\n\t"
      "s_barrier\n\t"
      "s_waitcnt lgkmcnt(7)\n\t"
      "v_mfma_f32_16x16x32_bf16 v[16:19], v[0:3], v[32:35], 0\n\t"
      "ds_read_b128 v[80:83], v104 offset:8192\n\t"
      "v_mfma_f32_16x16x32_bf16 v[20:23], v[0:3], v[36:39], 0\n\t"
      "s_mov_b64 s[88:89], s[96:97]\n\t"
      "s_add_u32 m0, s55, 147456\n\t"
      "s_xor_b32 s61, s47, 1572864\n\t"
      "buffer_load_dwordx4 v116, s[88:91], s61 offen lds\n\t"
      "ds_read_b64_tr_b16 a[176:177], v106\n\t"
      "s_waitcnt lgkmcnt(8)\n\t"
      "v_mfma_f32_16x16x32_bf16 v[16:19], v[4:7], v[40:43], v[16:19]\n\t"
      "ds_read_b128 v[84:87], v119 offset:8192\n\t"
      "v_mfma_f32_16x16x32_bf16 v[20:23], v[4:7], v[44:47], v[20:23]\n\t"
      "s_add_u32 m0, s55, 148480\n\t"
      "s_xor_b32 s61, s47, 1605632\n\t"
      "buffer_load_dwordx4 v116, s[88:91], s61 offen lds\n\t"
      "ds_read_b64_tr_b16 a[178:179], v106 offset:1024\n\t"
      "s_waitcnt lgkmcnt(9)\n\t"
      "v_mfma_f32_16x16x32_bf16 v[16:19], v[8:11], v[48:51], v[16:19]\n\t"
      "ds_read_b128 v[88:91], v102 offset:8192\n\t"
      "v_mfma_f32_16x16x32_bf16 v[20:23], v[8:11], v[52:55], v[20:23]\n\t"
      "s_add_u32 m0, s55, 155648\n\t"
      "s_xor_b32 s61, s47, 1835008\n\t"
      "buffer_load_dwordx4 v116, s[88:91], s61 offen lds\n\t"
      "s_add_u32 m0, s55, 156672\n\t"
      "s_xor_b32 s61, s47, 1867776\n\t"
      "buffer_load_dwordx4 v116, s[88:91], s61 offen lds\n\t"
      "s_waitcnt lgkmcnt(9)\n\t"
      "v_mfma_f32_16x16x32_bf16 v[16:19], v[12:15], v[56:59], v[16:19]\n\t"
      "ds_read_b128 v[92:95], v103 offset:8192\n\t"
      "v_mfma_f32_16x16x32_bf16 v[20:23], v[12:15], v[60:63], v[20:23]\n\t"
      "s_mov_b64 s[88:89], s[92:93]\n\t"
      "s_add_u32 m0, s55, 90112\n\t"
      "s_xor_b32 s61, s47, 1835008\n\t"
      "buffer_load_dwordx4 v105, s[88:91], s61 offen lds\n\t"
      "s_add_u32 m0, s55, 91136\n\t"
      "s_xor_b32 s61, s47, 1867776\n\t"
      "buffer_load_dwordx4 v105, s[88:91], s61 offen lds\n\t"
      "s_waitcnt lgkmcnt(9)\n\t"
      "v_mfma_f32_16x16x32_bf16 v[24:27], v[64:67], v[32:35], 0\n\t"
      "ds_read_b128 v[0:3], v104 offset:9216\n\t"
      "v_mfma_f32_16x16x32_bf16 v[28:31], v[64:67], v[36:39], 0\n\t"
      "s_waitcnt lgkmcnt(9)\n\t"
      "v_mfma_f32_16x16x32_bf16 v[24:27], v[68:71], v[40:43], v[24:27]\n\t"
      "ds_read_b128 v[4:7], v119 offset:9216\n\t"
      "v_cvt_pk_bf16_f32 v128, v16, v17\n\t"
      "v_mfma_f32_16x16x32_bf16 v[28:31], v[68:71], v[44:47], v[28:31]\n\t"
      "v_cvt_pk_bf16_f32 v129, v18, v19\n\t"
      "s_waitcnt lgkmcnt(9)\n\t"
      "v_mfma_f32_16x16x32_bf16 v[24:27], v[72:75], v[48:51], v[24:27]\n\t"
      "ds_read_b128 v[8:11], v102 offset:9216\n\t"
      "v_cvt_pk_bf16_f32 v192, v20, v21\n\t"
      "v_mfma_f32_16x16x32_bf16 v[28:31], v[72:75], v[52:55], v[28:31]\n\t"
      "v_cvt_pk_bf16_f32 v193, v22, v23\n\t"
      "s_waitcnt lgkmcnt(9)\n\t"
      "v_mfma_f32_16x16x32_bf16 v[24:27], v[76:79], v[56:59], v[24:27]\n\t"
      "ds_read_b128 v[12:15], v103 offset:9216\n\t"
      "v_mfma_f32_16x16x32_bf16 v[28:31], v[76:79], v[60:63], v[28:31]\n\t"
      "s_waitcnt vmcnt(35)\n\t"
      "s_waitcnt lgkmcnt(6)\n\t"
      "s_barrier\n\t"
      "v_mfma_f32_16x16x32_bf16 v[16:19], v[80:83], v[32:35], 0\n\t"
      "ds_read_b128 v[64:67], v104 offset:16384\n\t"
      "v_mfma_f32_16x16x32_bf16 v[20:23], v[80:83], v[36:39], 0\n\t"
      "s_add_u32 m0, s55, 32768\n\t"
      "s_xor_b32 s61, s47, 2097152\n\t"
      "buffer_load_dwordx4 v105, s[88:91], s61 offen lds\n\t"
      "s_add_u32 m0, s55, 33792\n\t"
      "s_xor_b32 s61, s47, 2129920\n\t"
      "buffer_load_dwordx4 v105, s[88:91], s61 offen lds\n\t"
      "v_mfma_f32_16x16x32_bf16 v[16:19], v[84:87], v[40:43], v[16:19]\n\t"
      "ds_read_b128 v[68:71], v119 offset:16384\n\t"
      "v_cvt_pk_bf16_f32 v130, v24, v25\n\t"
      "v_mfma_f32_16x16x32_bf16 v[20:23], v[84:87], v[44:47], v[20:23]\n\t"
      "v_cvt_pk_bf16_f32 v131, v26, v27\n\t"
      "s_waitcnt lgkmcnt(7)\n\t"
      "v_mfma_f32_16x16x32_bf16 v[16:19], v[88:91], v[48:51], v[16:19]\n\t"
      "ds_read_b128 v[72:75], v102 offset:16384\n\t"
      "v_cvt_pk_bf16_f32 v194, v28, v29\n\t"
      "v_mfma_f32_16x16x32_bf16 v[20:23], v[88:91], v[52:55], v[20:23]\n\t"
      "v_cvt_pk_bf16_f32 v195, v30, v31\n\t"
      "s_waitcnt lgkmcnt(7)\n\t"
      "v_mfma_f32_16x16x32_bf16 v[16:19], v[92:95], v[56:59], v[16:19]\n\t"
      "ds_read_b128 v[76:79], v103 offset:16384\n\t"
      "v_mfma_f32_16x16x32_bf16 v[20:23], v[92:95], v[60:63], v[20:23]\n\t"
      "v_mfma_f32_16x16x32_bf16 a[168:171], v[128:131], v[128:131], 0\n\t"
      "s_waitcnt lgkmcnt(7)\n\t"
      "v_mfma_f32_16x16x32_bf16 v[24:27], v[0:3], v[32:35], 0\n\t"
      "ds_read_b128 v[80:83], v104 offset:17408\n\t"
      "v_mfma_f32_16x16x32_bf16 v[28:31], v[0:3], v[36:39], 0\n\t"
      "s_waitcnt lgkmcnt(7)\n\t"
      "v_mfma_f32_16x16x32_bf16 v[24:27], v[4:7], v[40:43], v[24:27]\n\t"
      "ds_read_b128 v[84:87], v119 offset:17408\n\t"
      "v_cvt_pk_bf16_f32 v132, v16, v17\n\t"
      "v_mfma_f32_16x16x32_bf16 v[28:31], v[4:7], v[44:47], v[28:31]\n\t"
      "v_cvt_pk_bf16_f32 v133, v18, v19\n\t"
      "s_waitcnt lgkmcnt(7)\n\t"
      "v_mfma_f32_16x16x32_bf16 v[24:27], v[8:11], v[48:51], v[24:27]\n\t"
      "ds_read_b128 v[88:91], v102 offset:17408\n\t"
      "v_cvt_pk_bf16_f32 v196, v20, v21\n\t"
      "v_mfma_f32_16x16x32_bf16 v[28:31], v[8:11], v[52:55], v[28:31]\n\t"
      "v_cvt_pk_bf16_f32 v197, v22, v23\n\t"
      "s_waitcnt lgkmcnt(7)\n\t"
      "v_mfma_f32_16x16x32_bf16 v[24:27], v[12:15], v[56:59], v[24:27]\n\t"
      "ds_read_b128 v[92:95], v103 offset:17408\n\t"
      "v_mfma_f32_16x16x32_bf16 v[28:31], v[12:15], v[60:63], v[28:31]\n\t"
      "v_mfma_f32_16x16x32_bf16 a[232:235], v[192:195], v[192:195], 0\n\t"
      "s_waitcnt vmcnt(35)\n\t"
      "s_waitcnt vmcnt(22)\n\t"
      "s_barrier\n\t"
      "s_waitcnt lgkmcnt(7)\n\t"
      "v_mfma_f32_16x16x32_bf16 v[16:19], v[64:67], v[32:35], 0\n\t"
      "ds_read_b128 v[0:3], v104 offset:24576\n\t"
      "v_mfma_f32_16x16x32_bf16 v[20:23], v[64:67], v[36:39], 0\n\t"
      "s_add_u32 m0, s55, 40960\n\t"
      "s_xor_b32 s61, s47, 2359296\n\t"
      "buffer_load_dwordx4 v105, s[88:91], s61 offen lds\n\t"
      "ds_read_b64_tr_b16 a[180:181], v106 offset:16384\n\t"
      "s_waitcnt lgkmcnt(8)\n\t"
      "v_mfma_f32_16x16x32_bf16 v[16:19], v[68:71], v[40:43], v[16:19]\n\t"
      "ds_read_b128 v[4:7], v119 offset:24576\n\t"
      "v_cvt_pk_bf16_f32 v134, v24, v25\n\t"
      "v_mfma_f32_16x16x32_bf16 v[20:23], v[68:71], v[44:47], v[20:23]\n\t"
      "s_add_u32 m0, s55, 41984\n\t"
      "s_xor_b32 s61, s47, 2392064\n\t"
      "buffer_load_dwordx4 v105, s[88:91], s61 offen lds\n\t"
      "ds_read_b64_tr_b16 a[182:183], v106 offset:17408\n\t"
      "v_cvt_pk_bf16_f32 v135, v26, v27\n\t"
      "s_waitcnt lgkmcnt(9)\n\t"
      "v_mfma_f32_16x16x32_bf16 v[16:19], v[72:75], v[48:51], v[16:19]\n\t"
      "ds_read_b128 v[8:11], v102 offset:24576\n\t"
      "v_cvt_pk_bf16_f32 v198, v28, v29\n\t"
      "v_mfma_f32_16x16x32_bf16 v[20:23], v[72:75], v[52:55], v[20:23]\n\t"
      "ds_read_b64_tr_b16 a[0:1], v117\n\t"
      "ds_read_b64_tr_b16 a[2:3], v117 offset:1024\n\t"
      "v_cvt_pk_bf16_f32 v199, v30, v31\n\t"
      "s_waitcnt lgkmcnt(11)\n\t"
      "v_mfma_f32_16x16x32_bf16 v[16:19], v[76:79], v[56:59], v[16:19]\n\t"
      "ds_read_b128 v[12:15], v103 offset:24576\n\t"
      "v_mfma_f32_16x16x32_bf16 v[20:23], v[76:79], v[60:63], v[20:23]\n\t"
      "ds_read_b64_tr_b16 a[64:65], v118\n\t"
      "ds_read_b64_tr_b16 a[66:67], v118 offset:1024\n\t"
      "v_mfma_f32_16x16x32_bf16 a[168:171], v[132:135], v[132:135], a[168:171]\n\t"
      "s_waitcnt lgkmcnt(13)\n\t"
      "v_mfma_f32_16x16x32_bf16 v[24:27], v[80:83], v[32:35], 0\n\t"
      "ds_read_b128 v[64:67], v104 offset:25600\n\t"
      "v_mfma_f32_16x16x32_bf16 v[28:31], v[80:83], v[36:39], 0\n\t"
      "ds_read_b64_tr_b16 a[4:5], v117 offset:8192\n\t"
      "s_waitcnt lgkmcnt(12)\n\t"
      "ds_read_b64_tr_b16 a[6:7], v117 offset:9216\n\t"
      "v_mfma_f32_16x16x32_bf16 v[24:27], v[84:87], v[40:43], v[24:27]\n\t"
      "ds_read_b128 v[68:71], v119 offset:25600\n\t"
      "v_cvt_pk_bf16_f32 v136, v16, v17\n\t"
      "v_mfma_f32_16x16x32_bf16 v[28:31], v[84:87], v[44:47], v[28:31]\n\t"
      "ds_read_b64_tr_b16 a[68:69], v118 offset:8192\n\t"
      "s_waitcnt lgkmcnt(12)\n\t"
      "ds_read_b64_tr_b16 a[70:71], v118 offset:9216\n\t"
      "v_cvt_pk_bf16_f32 v137, v18, v19\n\t"
      "v_mfma_f32_16x16x32_bf16 v[24:27], v[88:91], v[48:51], v[24:27]\n\t"
      "ds_read_b128 v[72:75], v102 offset:25600\n\t"
      "v_cvt_pk_bf16_f32 v200, v20, v21\n\t"
      "v_mfma_f32_16x16x32_bf16 v[28:31], v[88:91], v[52:55], v[28:31]\n\t"
      "v_cvt_pk_bf16_f32 v201, v22, v23\n\t"
      "v_mfma_f32_16x16x32_bf16 v[24:27], v[92:95], v[56:59], v[24:27]\n\t"
      "ds_read_b128 v[76:79], v103 offset:25600\n\t"
      "v_mfma_f32_16x16x32_bf16 v[28:31], v[92:95], v[60:63], v[28:31]\n\t"
      "v_mfma_f32_16x16x32_bf16 a[232:235], v[196:199], v[196:199], a[232:235]\n\t"
      "s_waitcnt vmcnt(22)\n\t"
      "s_waitcnt vmcnt(18)\n\t"
      "s_waitcnt lgkmcnt(2)\n\t"
      "s_barrier\n\t"
      "v_mfma_f32_16x16x32_bf16 v[16:19], v[0:3], v[32:35], 0\n\t"
      "ds_read_b128 v[80:83], v104 offset:32768\n\t"
      "v_mfma_f32_16x16x32_bf16 v[20:23], v[0:3], v[36:39], 0\n\t"
      "s_add_u32 m0, s55, 49152\n\t"
      "s_xor_b32 s61, s47, 2621440\n\t"
      "buffer_load_dwordx4 v105, s[88:91], s61 offen lds\n\t"
      "ds_read_b64_tr_b16 a[8:9], v117 offset:16384\n\t"
      "v_mfma_f32_16x16x32_bf16 v[16:19], v[4:7], v[40:43], v[16:19]\n\t"
      "ds_read_b128 v[84:87], v119 offset:32768\n\t"
      "v_cvt_pk_bf16_f32 v138, v24, v25\n\t"
      "v_mfma_f32_16x16x32_bf16 v[20:23], v[4:7], v[44:47], v[20:23]\n\t"
      "s_add_u32 m0, s55, 50176\n\t"
      "s_xor_b32 s61, s47, 2654208\n\t"
      "buffer_load_dwordx4 v105, s[88:91], s61 offen lds\n\t"
      "ds_read_b64_tr_b16 a[10:11], v117 offset:17408\n\t"
      "v_cvt_pk_bf16_f32 v139, v26, v27\n\t"
      "v_mfma_f32_16x16x32_bf16 v[16:19], v[8:11], v[48:51], v[16:19]\n\t"
      "ds_read_b128 v[88:91], v102 offset:32768\n\t"
      "v_cvt_pk_bf16_f32 v202, v28, v29\n\t"
      "v_mfma_f32_16x16x32_bf16 v[20:23], v[8:11], v[52:55], v[20:23]\n\t"
      "s_mov_b64 s[88:89], s[96:97]\n\t"
      "s_add_u32 m0, s55, 98304\n\t"
      "s_xor_b32 s61, s47, 2097152\n\t"
      "buffer_load_dwordx4 v116, s[88:91], s61 offen lds\n\t"
      "ds_read_b64_tr_b16 a[72:73], v118 offset:16384\n\t"
      "v_cvt_pk_bf16_f32 v203, v30, v31\n\t"
      "v_mfma_f32_16x16x32_bf16 v[16:19], v[12:15], v[56:59], v[16:19]\n\t"
      "ds_read_b128 v[92:95], v103 offset:32768\n\t"
      "v_mfma_f32_16x16x32_bf16 v[20:23], v[12:15], v[60:63], v[20:23]\n\t"
      "s_add_u32 m0, s55, 99328\n\t"
      "s_xor_b32 s61, s47, 2129920\n\t"
      "buffer_load_dwordx4 v116, s[88:91], s61 offen lds\n\t"
      "ds_read_b64_tr_b16 a[74:75], v118 offset:17408\n\t"
      "v_mfma_f32_16x16x32_bf16 a[168:171], v[136:139], v[136:139], a[168:171]\n\t"
      "v_mfma_f32_16x16x32_bf16 v[24:27], v[64:67], v[32:35], 0\n\t"
      "ds_read_b128 v[0:3], v104 offset:33792\n\t"
      "v_mfma_f32_16x16x32_bf16 v[28:31], v[64:67], v[36:39], 0\n\t"
      "s_add_u32 m0, s55, 106496\n\t"
      "s_xor_b32 s61, s47, 2359296\n\t"
      "buffer_load_dwordx4 v116, s[88:91], s61 offen lds\n\t"
      "ds_read_b64_tr_b16 a[12:13], v117 offset:24576\n\t"
      "v_mfma_f32_16x16x32_bf16 v[24:27], v[68:71], v[40:43], v[24:27]\n\t"
      "ds_read_b128 v[4:7], v119 offset:33792\n\t"
      "v_cvt_pk_bf16_f32 v140, v16, v17\n\t"
      "v_mfma_f32_16x16x32_bf16 v[28:31], v[68:71], v[44:47], v[28:31]\n\t"
      "s_add_u32 m0, s55, 107520\n\t"
      "s_xor_b32 s61, s47, 2392064\n\t"
      "buffer_load_dwordx4 v116, s[88:91], s61 offen lds\n\t"
      "ds_read_b64_tr_b16 a[14:15], v117 offset:25600\n\t"
      "v_cvt_pk_bf16_f32 v141, v18, v19\n\t"
      "s_waitcnt lgkmcnt(13)\n\t"
      "v_mfma_f32_16x16x32_bf16 v[24:27], v[72:75], v[48:51], v[24:27]\n\t"
      "ds_read_b128 v[8:11], v102 offset:33792\n\t"
      "v_cvt_pk_bf16_f32 v204, v20, v21\n\t"
      "v_mfma_f32_16x16x32_bf16 v[28:31], v[72:75], v[52:55], v[28:31]\n\t"
      "ds_read_b64_tr_b16 a[76:77], v118 offset:24576\n\t"
      "s_waitcnt lgkmcnt(12)\n\t"
      "ds_read_b64_tr_b16 a[78:79], v118 offset:25600\n\t"
      "v_cvt_pk_bf16_f32 v205, v22, v23\n\t"
      "v_mfma_f32_16x16x32_bf16 v[24:27], v[76:79], v[56:59], v[24:27]\n\t"
      "ds_read_b128 v[12:15], v103 offset:33792\n\t"
      "v_mfma_f32_16x16x32_bf16 v[28:31], v[76:79], v[60:63], v[28:31]\n\t"
      "v_mfma_f32_16x16x32_bf16 a[232:235], v[200:203], v[200:203], a[232:235]\n\t"
      "s_waitcnt vmcnt(22)\n\t"
      "s_waitcnt vmcnt(18)\n\t"
      "s_waitcnt lgkmcnt(1)\n\t"
      "s_barrier\n\t"
      "v_mfma_f32_16x16x32_bf16 v[16:19], v[80:83], v[32:35], 0\n\t"
      "ds_read_b128 v[64:67], v104 offset:40960\n\t"
      "v_mfma_f32_16x16x32_bf16 v[20:23], v[80:83], v[36:39], 0\n\t"
      "s_mov_b64 s[88:89], s[92:93]\n\t"
      "s_add_u32 m0, s55, 57344\n\t"
      "s_xor_b32 s61, s47, 2883584\n\t"
      "buffer_load_dwordx4 v105, s[88:91], s61 offen lds\n\t"
      "ds_read_b64_tr_b16 a[184:185], v106 offset:32768\n\t"
      "v_mfma_f32_16x16x32_bf16 v[16:19], v[84:87], v[40:43], v[16:19]\n\t"
      "ds_read_b128 v[68:71], v119 offset:40960\n\t"
      "v_cvt_pk_bf16_f32 v142, v24, v25\n\t"
      "v_mfma_f32_16x16x32_bf16 v[20:23], v[84:87], v[44:47], v[20:23]\n\t"
      "s_add_u32 m0, s55, 58368\n\t"
      "s_xor_b32 s61, s47, 2916352\n\t"
      "buffer_load_dwordx4 v105, s[88:91], s61 offen lds\n\t"
      "ds_read_b64_tr_b16 a[186:187], v106 offset:33792\n\t"
      "v_cvt_pk_bf16_f32 v143, v26, v27\n\t"
      "v_mfma_f32_16x16x32_bf16 v[16:19], v[88:91], v[48:51], v[16:19]\n\t"
      "ds_read_b128 v[72:75], v102 offset:40960\n\t"
      "v_cvt_pk_bf16_f32 v206, v28, v29\n\t"
      "v_mfma_f32_16x16x32_bf16 v[20:23], v[88:91], v[52:55], v[20:23]\n\t"
      "s_mov_b64 s[88:89], s[96:97]\n\t"
      "s_add_u32 m0, s55, 114688\n\t"
      "s_xor_b32 s61, s47, 2621440\n\t"
      "buffer_load_dwordx4 v116, s[88:91], s61 offen lds\n\t"
      "ds_read_b64_tr_b16 a[16:17], v117 offset:32768\n\t"
      "v_cvt_pk_bf16_f32 v207, v30, v31\n\t"
      "v_mfma_f32_16x16x32_bf16 v[16:19], v[92:95], v[56:59], v[16:19]\n\t"
      "ds_read_b128 v[76:79], v103 offset:40960\n\t"
      "v_mfma_f32_16x16x32_bf16 v[20:23], v[92:95], v[60:63], v[20:23]\n\t"
      "s_add_u32 m0, s55, 115712\n\t"
      "s_xor_b32 s61, s47, 2654208\n\t"
      "buffer_load_dwordx4 v116, s[88:91], s61 offen lds\n\t"
      "ds_read_b64_tr_b16 a[18:19], v117 offset:33792\n\t"
      "v_mfma_f32_16x16x32_bf16 a[168:171], v[140:143], v[140:143], a[168:171]\n\t"
      "v_mfma_f32_16x16x32_bf16 v[24:27], v[0:3], v[32:35], 0\n\t"
      "ds_read_b128 v[80:83], v104 offset:41984\n\t"
      "v_mfma_f32_16x16x32_bf16 v[28:31], v[0:3], v[36:39], 0\n\t"
      "s_add_u32 m0, s55, 122880\n\t"
      "s_xor_b32 s61, s47, 2883584\n\t"
      "buffer_load_dwordx4 v116, s[88:91], s61 offen lds\n\t"
      "ds_read_b64_tr_b16 a[80:81], v118 offset:32768\n\t"
      "v_mfma_f32_16x16x32_bf16 v[24:27], v[4:7], v[40:43], v[24:27]\n\t"
      "ds_read_b128 v[84:87], v119 offset:41984\n\t"
      "v_cvt_pk_bf16_f32 v144, v16, v17\n\t"
      "v_mfma_f32_16x16x32_bf16 v[28:31], v[4:7], v[44:47], v[28:31]\n\t"
      "s_add_u32 m0, s55, 123904\n\t"
      "s_xor_b32 s61, s47, 2916352\n\t"
      "buffer_load_dwordx4 v116, s[88:91], s61 offen lds\n\t"
      "ds_read_b64_tr_b16 a[82:83], v118 offset:33792\n\t"
      "v_cvt_pk_bf16_f32 v145, v18, v19\n\t"
      "v_mfma_f32_16x16x32_bf16 v[24:27], v[8:11], v[48:51], v[24:27]\n\t"
      "ds_read_b128 v[88:91], v102 offset:41984\n\t"
      "v_cvt_pk_bf16_f32 v208, v20, v21\n\t"
      "v_mfma_f32_16x16x32_bf16 v[28:31], v[8:11], v[52:55], v[28:31]\n\t"
      "ds_read_b64_tr_b16 a[20:21], v117 offset:40960\n\t"
      "s_waitcnt lgkmcnt(12)\n\t"
      "ds_read_b64_tr_b16 a[22:23], v117 offset:41984\n\t"
      "v_cvt_pk_bf16_f32 v209, v22, v23\n\t"
      "v_mfma_f32_16x16x32_bf16 v[24:27], v[12:15], v[56:59], v[24:27]\n\t"
      "ds_read_b128 v[92:95], v103 offset:41984\n\t"
      "v_mfma_f32_16x16x32_bf16 v[28:31], v[12:15], v[60:63], v[28:31]\n\t"
      "ds_read_b64_tr_b16 a[84:85], v118 offset:40960\n\t"
      "s_waitcnt lgkmcnt(12)\n\t"
      "ds_read_b64_tr_b16 a[86:87], v118 offset:41984\n\t"
      "v_mfma_f32_16x16x32_bf16 a[232:235], v[204:207], v[204:207], a[232:235]\n\t"
      "s_waitcnt vmcnt(22)\n\t"
      "s_waitcnt vmcnt(18)\n\t"
      "s_waitcnt lgkmcnt(0)\n\t"
      "s_barrier\n\t"
      "v_mfma_f32_16x16x32_bf16 v[16:19], v[64:67], v[32:35], 0\n\t"
      "ds_read_b128 v[0:3], v104 offset:49152\n\t"
      "v_mfma_f32_16x16x32_bf16 v[20:23], v[64:67], v[36:39], 0\n\t"
      "s_mov_b64 s[88:89], s[92:93]\n\t"
      "s_add_u32 m0, s55, 65536\n\t"
      "s_xor_b32 s61, s47, 3145728\n\t"
      "buffer_load_dwordx4 v105, s[88:91], s61 offen lds\n\t"
      "ds_read_b64_tr_b16 a[24:25], v117 offset:49152\n\t"
      "v_mfma_f32_16x16x32_bf16 v[16:19], v[68:71], v[40:43], v[16:19]\n\t"
      "ds_read_b128 v[4:7], v119 offset:49152\n\t"
      "v_cvt_pk_bf16_f32 v146, v24, v25\n\t"
      "v_mfma_f32_16x16x32_bf16 v[20:23], v[68:71], v[44:47], v[20:23]\n\t"
      "s_add_u32 m0, s55, 66560\n\t"
      "s_xor_b32 s61, s47, 3178496\n\t"
      "buffer_load_dwordx4 v105, s[88:91], s61 offen lds\n\t"
      "ds_read_b64_tr_b16 a[26:27], v117 offset:50176\n\t"
      "v_cvt_pk_bf16_f32 v147, v26, v27\n\t"
      "v_mfma_f32_16x16x32_bf16 v[16:19], v[72:75], v[48:51], v[16:19]\n\t"
      "ds_read_b128 v[8:11], v102 offset:49152\n\t"
      "v_cvt_pk_bf16_f32 v210, v28, v29\n\t"
      "v_mfma_f32_16x16x32_bf16 v[20:23], v[72:75], v[52:55], v[20:23]\n\t"
      "s_mov_b64 s[88:89], s[96:97]\n\t"
      "s_add_u32 m0, s55, 131072\n\t"
      "s_xor_b32 s61, s47, 3145728\n\t"
      "buffer_load_dwordx4 v116, s[88:91], s61 offen lds\n\t"
      "ds_read_b64_tr_b16 a[88:89], v118 offset:49152\n\t"
      "v_cvt_pk_bf16_f32 v211, v30, v31\n\t"
      "v_mfma_f32_16x16x32_bf16 v[16:19], v[76:79], v[56:59], v[16:19]\n\t"
      "ds_read_b128 v[12:15], v103 offset:49152\n\t"
      "v_mfma_f32_16x16x32_bf16 v[20:23], v[76:79], v[60:63], v[20:23]\n\t"
      "s_add_u32 m0, s55, 132096\n\t"
      "s_xor_b32 s61, s47, 3178496\n\t"
      "buffer_load_dwordx4 v116, s[88:91], s61 offen lds\n\t"
      "ds_read_b64_tr_b16 a[90:91], v118 offset:50176\n\t"
      "v_mfma_f32_16x16x32_bf16 a[168:171], v[144:147], v[144:147], a[168:171]\n\t"
      "v_mfma_f32_16x16x32_bf16 v[24:27], v[80:83], v[32:35], 0\n\t"
      "ds_read_b128 v[64:67], v104 offset:50176\n\t"
      "v_mfma_f32_16x16x32_bf16 v[28:31], v[80:83], v[36:39], 0\n\t"
      "s_add_u32 m0, s55, 139264\n\t"
      "s_xor_b32 s61, s47, 3407872\n\t"
      "buffer_load_dwordx4 v116, s[88:91], s61 offen lds\n\t"
      "ds_read_b64_tr_b16 a[28:29], v117 offset:57344\n\t"
      "v_mfma_f32_16x16x32_bf16 v[24:27], v[84:87], v[40:43], v[24:27]\n\t"
      "ds_read_b128 v[68:71], v119 offset:50176\n\t"
      "v_cvt_pk_bf16_f32 v148, v16, v17\n\t"
      "v_mfma_f32_16x16x32_bf16 v[28:31], v[84:87], v[44:47], v[28:31]\n\t"
      "s_add_u32 m0, s55, 140288\n\t"
      "s_xor_b32 s61, s47, 3440640\n\t"
      "buffer_load_dwordx4 v116, s[88:91], s61 offen lds\n\t"
      "ds_read_b64_tr_b16 a[30:31], v117 offset:58368\n\t"
      "v_cvt_pk_bf16_f32 v149, v18, v19\n\t"
      "v_mfma_f32_16x16x32_bf16 v[24:27], v[88:91], v[48:51], v[24:27]\n\t"
      "ds_read_b128 v[72:75], v102 offset:50176\n\t"
      "v_cvt_pk_bf16_f32 v212, v20, v21\n\t"
      "v_mfma_f32_16x16x32_bf16 v[28:31], v[88:91], v[52:55], v[28:31]\n\t"
      "ds_read_b64_tr_b16 a[92:93], v118 offset:57344\n\t"
      "ds_read_b64_tr_b16 a[94:95], v118 offset:58368\n\t"
      "v_cvt_pk_bf16_f32 v213, v22, v23\n\t"
      "v_mfma_f32_16x16x32_bf16 v[24:27], v[92:95], v[56:59], v[24:27]\n\t"
      "s_waitcnt lgkmcnt(12)\n\t"
      "ds_read_b128 v[76:79], v103 offset:50176\n\t"
      "v_mfma_f32_16x16x32_bf16 v[28:31], v[92:95], v[60:63], v[28:31]\n\t"
      "v_mfma_f32_16x16x32_bf16 a[232:235], v[208:211], v[208:211], a[232:235]\n\t"
      "s_waitcnt vmcnt(22)\n\t"
      "s_waitcnt vmcnt(12)\n\t"
      "s_waitcnt lgkmcnt(1)\n\t"
      "s_barrier\n\t"
      "v_mfma_f32_16x16x32_bf16 v[16:19], v[0:3], v[32:35], 0\n\t"
      "ds_read_b128 v[80:83], v104 offset:57344\n\t"
      "v_mfma_f32_16x16x32_bf16 v[20:23], v[0:3], v[36:39], 0\n\t"
      "s_mov_b64 s[88:89], s[92:93]\n\t"
      "s_add_u32 m0, s55, 73728\n\t"
      "s_xor_b32 s61, s47, 3407872\n\t"
      "buffer_load_dwordx4 v105, s[88:91], s61 offen lds\n\t"
      "ds_read_b64_tr_b16 a[188:189], v106 offset:49152\n\t"
      "v_mfma_f32_16x16x32_bf16 v[16:19], v[4:7], v[40:43], v[16:19]\n\t"
      "ds_read_b128 v[84:87], v119 offset:57344\n\t"
      "v_cvt_pk_bf16_f32 v150, v24, v25\n\t"
      "v_mfma_f32_16x16x32_bf16 v[20:23], v[4:7], v[44:47], v[20:23]\n\t"
      "s_add_u32 m0, s55, 74752\n\t"
      "s_xor_b32 s61, s47, 3440640\n\t"
      "buffer_load_dwordx4 v105, s[88:91], s61 offen lds\n\t"
      "ds_read_b64_tr_b16 a[190:191], v106 offset:50176\n\t"
      "v_cvt_pk_bf16_f32 v151, v26, v27\n\t"
      "v_mfma_f32_16x16x32_bf16 v[16:19], v[8:11], v[48:51], v[16:19]\n\t"
      "ds_read_b128 v[88:91], v102 offset:57344\n\t"
      "v_cvt_pk_bf16_f32 v214, v28, v29\n\t"
      "v_mfma_f32_16x16x32_bf16 v[20:23], v[8:11], v[52:55], v[20:23]\n\t"
      "s_mov_b64 s[88:89], s[96:97]\n\t"
      "s_add_u32 m0, s55, 147456\n\t"
      "s_xor_b32 s61, s47, 3670016\n\t"
      "buffer_load_dwordx4 v116, s[88:91], s61 offen lds\n\t"
      "ds_read_b64_tr_b16 a[32:33], v117\n\t"
      "v_cvt_pk_bf16_f32 v215, v30, v31\n\t"
      "v_mfma_f32_16x16x32_bf16 v[16:19], v[12:15], v[56:59], v[16:19]\n\t"
      "ds_read_b128 v[92:95], v103 offset:57344\n\t"
      "v_mfma_f32_16x16x32_bf16 v[20:23], v[12:15], v[60:63], v[20:23]\n\t"
      "s_add_u32 m0, s55, 148480\n\t"
      "s_xor_b32 s61, s47, 3702784\n\t"
      "buffer_load_dwordx4 v116, s[88:91], s61 offen lds\n\t"
      "ds_read_b64_tr_b16 a[34:35], v117 offset:1024\n\t"
      "v_mfma_f32_16x16x32_bf16 a[168:171], v[148:151], v[148:151], a[168:171]\n\t"
      "v_readfirstlane_b32 s53, v126\n\t"
      "s_nop 0\n\t"
      "s_mul_i32 s53, s53, 0x9e3779b1\n\t"
      "s_add_u32 s53, s53, 0x7f4a7c15\n\t"
      "v_mov_b32 v96, 0x20000000\n\t"
      "v_mov_b32 v97, 0x20000000\n\t"
      "v_mov_b32 v98, 0x20000000\n\t"
      "v_mov_b32 v99, 0x20000000\n\t"
      "s_mov_b32 s61, 8192\n\t"
      "buffer_store_dwordx4 v[96:99], v101, s[36:39], 0 offen sc1\n\t"
      "buffer_store_dwordx4 v[96:99], v101, s[36:39], s61 offen sc1\n\t"
      "s_nop 1\n\t"
      "v_mov_b32 v96, v111\n\t"
      "v_mov_b32 v97, v111\n\t"
      "v_mov_b32 v98, v111\n\t"
      "v_mov_b32 v99, v111\n\t"
      "buffer_store_dwordx4 v[96:99], v108, s[36:39], 0 offen sc1\n\t"
      "v_mfma_f32_16x16x32_bf16 v[24:27], v[64:67], v[32:35], 0\n\t"
      "ds_read_b128 v[0:3], v104 offset:58368\n\t"
      "v_mfma_f32_16x16x32_bf16 v[28:31], v[64:67], v[36:39], 0\n\t"
      "s_add_u32 m0, s55, 155648\n\t"
      "s_xor_b32 s61, s47, 3932160\n\t"
      "buffer_load_dwordx4 v116, s[88:91], s61 offen lds\n\t"
      "ds_read_b64_tr_b16 a[96:97], v118\n\t"
      "v_mfma_f32_16x16x32_bf16 v[24:27], v[68:71], v[40:43], v[24:27]\n\t"
      "ds_read_b128 v[4:7], v119 offset:58368\n\t"
      "v_cvt_pk_bf16_f32 v152, v16, v17\n\t"
      "v_mfma_f32_16x16x32_bf16 v[28:31], v[68:71], v[44:47], v[28:31]\n\t"
      "s_add_u32 m0, s55, 156672\n\t"
      "s_xor_b32 s61, s47, 3964928\n\t"
      "buffer_load_dwordx4 v116, s[88:91], s61 offen lds\n\t"
      "ds_read_b64_tr_b16 a[98:99], v118 offset:1024\n\t"
      "v_cvt_pk_bf16_f32 v153, v18, v19\n\t"
      "v_mfma_f32_16x16x32_bf16 v[24:27], v[72:75], v[48:51], v[24:27]\n\t"
      "ds_read_b128 v[8:11], v102 offset:58368\n\t"
      "v_cvt_pk_bf16_f32 v216, v20, v21\n\t"
      "v_mfma_f32_16x16x32_bf16 v[28:31], v[72:75], v[52:55], v[28:31]\n\t"
      "ds_read_b64_tr_b16 a[36:37], v117 offset:8192\n\t"
      "s_waitcnt lgkmcnt(12)\n\t"
      "ds_read_b64_tr_b16 a[38:39], v117 offset:9216\n\t"
      "v_cvt_pk_bf16_f32 v217, v22, v23\n\t"
      "v_mfma_f32_16x16x32_bf16 v[24:27], v[76:79], v[56:59], v[24:27]\n\t"
      "ds_read_b128 v[12:15], v103 offset:58368\n\t"
      "v_mfma_f32_16x16x32_bf16 v[28:31], v[76:79], v[60:63], v[28:31]\n\t"
      "ds_read_b64_tr_b16 a[100:101], v118 offset:8192\n\t"
      "s_waitcnt lgkmcnt(12)\n\t"
      "ds_read_b64_tr_b16 a[102:103], v118 offset:9216\n\t"
      "v_mfma_f32_16x16x32_bf16 a[232:235], v[212:215], v[212:215], a[232:235]\n\t"
      "s_waitcnt vmcnt(15)\n\t"
      "s_waitcnt lgkmcnt(0)\n\t"
      "s_barrier\n\t"
      "v_mfma_f32_16x16x32_bf16 v[16:19], v[80:83], v[32:35], 0\n\t"
      "ds_read_b128 v[64:67], v104\n\t"
      "v_mfma_f32_16x16x32_bf16 v[20:23], v[80:83], v[36:39], 0\n\t"
      "s_mov_b64 s[88:89], s[92:93]\n\t"
      "s_add_u32 m0, s55, 81920\n\t"
      "s_xor_b32 s61, s47, 3670016\n\t"
      "buffer_load_dwordx4 v105, s[88:91], s61 offen lds\n\t"
      "ds_read_b64_tr_b16 a[40:41], v117 offset:16384\n\t"
      "v_mfma_f32_16x16x32_bf16 v[16:19], v[84:87], v[40:43], v[16:19]\n\t"
      "ds_read_b128 v[68:71], v119\n\t"
      "v_cvt_pk_bf16_f32 v154, v24, v25\n\t"
      "v_mfma_f32_16x16x32_bf16 v[20:23], v[84:87], v[44:47], v[20:23]\n\t"
      "s_add_u32 m0, s55, 82944\n\t"
      "s_xor_b32 s61, s47, 3702784\n\t"
      "buffer_load_dwordx4 v105, s[88:91], s61 offen lds\n\t"
      "ds_read_b64_tr_b16 a[42:43], v117 offset:17408\n\t"
      "v_cvt_pk_bf16_f32 v155, v26, v27\n\t"
      "v_mfma_f32_16x16x32_bf16 v[16:19], v[88:91], v[48:51], v[16:19]\n\t"
      "ds_read_b128 v[72:75], v102\n\t"
      "v_cvt_pk_bf16_f32 v218, v28, v29\n\t"
      "v_mfma_f32_16x16x32_bf16 v[20:23], v[88:91], v[52:55], v[20:23]\n\t"
      "s_mov_b64 s[88:89], s[98:99]\n\t"
      "s_add_u32 m0, s55, 98304\n\t"
      "s_xor_b32 s61, s47, 0\n\t"
      "buffer_load_dwordx4 v116, s[88:91], s61 offen lds\n\t"
      "ds_read_b64_tr_b16 a[104:105], v118 offset:16384\n\t"
      "v_cvt_pk_bf16_f32 v219, v30, v31\n\t"
      "v_mfma_f32_16x16x32_bf16 v[16:19], v[92:95], v[56:59], v[16:19]\n\t"
      "ds_read_b128 v[76:79], v103\n\t"
      "v_mfma_f32_16x16x32_bf16 v[20:23], v[92:95], v[60:63], v[20:23]\n\t"
      "s_add_u32 m0, s55, 99328\n\t"
      "s_xor_b32 s61, s47, 32768\n\t"
      "buffer_load_dwordx4 v116, s[88:91], s61 offen lds\n\t"
      "ds_read_b64_tr_b16 a[106:107], v118 offset:17408\n\t"
      "v_mfma_f32_16x16x32_bf16 a[168:171], v[152:155], v[152:155], a[168:171]\n\t"
      "v_mfma_f32_16x16x32_bf16 v[24:27], v[0:3], v[32:35], 0\n\t"
      "ds_read_b128 v[80:83], v104 offset:1024\n\t"
      "v_mfma_f32_16x16x32_bf16 v[28:31], v[0:3], v[36:39], 0\n\t"
      "s_add_u32 m0, s55, 106496\n\t"
      "s_xor_b32 s61, s47, 262144\n\t"
      "buffer_load_dwordx4 v116, s[88:91], s61 offen lds\n\t"
      "ds_read_b64_tr_b16 a[44:45], v117 offset:24576\n\t"
      "v_mfma_f32_16x16x32_bf16 v[24:27], v[4:7], v[40:43], v[24:27]\n\t"
      "ds_read_b128 v[84:87], v119 offset:1024\n\t"
      "v_cvt_pk_bf16_f32 v156, v16, v17\n\t"
      "v_mfma_f32_16x16x32_bf16 v[28:31], v[4:7], v[44:47], v[28:31]\n\t"
      "s_add_u32 m0, s55, 107520\n\t"
      "s_xor_b32 s61, s47, 294912\n\t"
      "buffer_load_dwordx4 v116, s[88:91], s61 offen lds\n\t"
      "ds_read_b64_tr_b16 a[46:47], v117 offset:25600\n\t"
      "v_cvt_pk_bf16_f32 v157, v18, v19\n\t"
      "v_mfma_f32_16x16x32_bf16 v[24:27], v[8:11], v[48:51], v[24:27]\n\t"
      "ds_read_b128 v[88:91], v102 offset:1024\n\t"
      "v_cvt_pk_bf16_f32 v220, v20, v21\n\t"
      "v_mfma_f32_16x16x32_bf16 v[28:31], v[8:11], v[52:55], v[28:31]\n\t"
      "ds_read_b64_tr_b16 a[108:109], v118 offset:24576\n\t"
      "ds_read_b64_tr_b16 a[110:111], v118 offset:25600\n\t"
      "v_cvt_pk_bf16_f32 v221, v22, v23\n\t"
      "v_mfma_f32_16x16x32_bf16 v[24:27], v[12:15], v[56:59], v[24:27]\n\t"
      "s_waitcnt lgkmcnt(12)\n\t"
      "ds_read_b128 v[92:95], v103 offset:1024\n\t"
      "v_mfma_f32_16x16x32_bf16 v[28:31], v[12:15], v[60:63], v[28:31]\n\t"
      "v_mfma_f32_16x16x32_bf16 a[232:235], v[216:219], v[216:219], a[232:235]\n\t"
      "s_waitcnt vmcnt(15)\n\t"
      "s_waitcnt lgkmcnt(1)\n\t"
      "s_barrier\n\t"
      "v_mfma_f32_16x16x32_bf16 v[16:19], v[64:67], v[32:35], 0\n\t"
      "ds_read_b128 v[0:3], v104 offset:8192\n\t"
      "v_mfma_f32_16x16x32_bf16 v[20:23], v[64:67], v[36:39], 0\n\t"
      "s_mov_b64 s[88:89], s[92:93]\n\t"
      "s_add_u32 m0, s55, 90112\n\t"
      "s_xor_b32 s61, s47, 3932160\n\t"
      "buffer_load_dwordx4 v105, s[88:91], s61 offen lds\n\t"
      "ds_read_b64_tr_b16 a[240:241], v106\n\t"
      "v_mfma_f32_16x16x32_bf16 v[16:19], v[68:71], v[40:43], v[16:19]\n\t"
      "ds_read_b128 v[4:7], v119 offset:8192\n\t"
      "v_cvt_pk_bf16_f32 v158, v24, v25\n\t"
      "v_mfma_f32_16x16x32_bf16 v[20:23], v[68:71], v[44:47], v[20:23]\n\t"
      "s_add_u32 m0, s55, 91136\n\t"
      "s_xor_b32 s61, s47, 3964928\n\t"
      "buffer_load_dwordx4 v105, s[88:91], s61 offen lds\n\t"
      "ds_read_b64_tr_b16 a[242:243], v106 offset:1024\n\t"
      "v_cvt_pk_bf16_f32 v159, v26, v27\n\t"
      "v_mfma_f32_16x16x32_bf16 v[16:19], v[72:75], v[48:51], v[16:19]\n\t"
      "ds_read_b128 v[8:11], v102 offset:8192\n\t"
      "v_cvt_pk_bf16_f32 v222, v28, v29\n\t"
      "v_mfma_f32_16x16x32_bf16 v[20:23], v[72:75], v[52:55], v[20:23]\n\t"
      "s_mov_b64 s[88:89], s[98:99]\n\t"
      "s_add_u32 m0, s55, 114688\n\t"
      "s_xor_b32 s61, s47, 524288\n\t"
      "buffer_load_dwordx4 v116, s[88:91], s61 offen lds\n\t"
      "ds_read_b64_tr_b16 a[48:49], v117 offset:32768\n\t"
      "v_cvt_pk_bf16_f32 v223, v30, v31\n\t"
      "v_mfma_f32_16x16x32_bf16 v[16:19], v[76:79], v[56:59], v[16:19]\n\t"
      "ds_read_b128 v[12:15], v103 offset:8192\n\t"
      "v_mfma_f32_16x16x32_bf16 v[20:23], v[76:79], v[60:63], v[20:23]\n\t"
      "s_add_u32 m0, s55, 115712\n\t"
      "s_xor_b32 s61, s47, 557056\n\t"
      "buffer_load_dwordx4 v116, s[88:91], s61 offen lds\n\t"
      "ds_read_b64_tr_b16 a[50:51], v117 offset:33792\n\t"
      "v_mfma_f32_16x16x32_bf16 a[168:171], v[156:159], v[156:159], a[168:171]\n\t"
      "v_mfma_f32_16x16x32_bf16 v[24:27], v[80:83], v[32:35], 0\n\t"
      "ds_read_b128 v[64:67], v104 offset:9216\n\t"
      "v_mfma_f32_16x16x32_bf16 v[28:31], v[80:83], v[36:39], 0\n\t"
      "s_add_u32 m0, s55, 122880\n\t"
      "s_xor_b32 s61, s47, 786432\n\t"
      "buffer_load_dwordx4 v116, s[88:91], s61 offen lds\n\t"
      "ds_read_b64_tr_b16 a[112:113], v118 offset:32768\n\t"
      "v_mfma_f32_16x16x32_bf16 v[24:27], v[84:87], v[40:43], v[24:27]\n\t"
      "ds_read_b128 v[68:71], v119 offset:9216\n\t"
      "v_cvt_pk_bf16_f32 v160, v16, v17\n\t"
      "v_mfma_f32_16x16x32_bf16 v[28:31], v[84:87], v[44:47], v[28:31]\n\t"
      "s_add_u32 m0, s55, 123904\n\t"
      "s_xor_b32 s61, s47, 819200\n\t"
      "buffer_load_dwordx4 v116, s[88:91], s61 offen lds\n\t"
      "ds_read_b64_tr_b16 a[114:115], v118 offset:33792\n\t"
      "v_cvt_pk_bf16_f32 v161, v18, v19\n\t"
      "v_mfma_f32_16x16x32_bf16 v[24:27], v[88:91], v[48:51], v[24:27]\n\t"
      "ds_read_b128 v[72:75], v102 offset:9216\n\t"
      "v_cvt_pk_bf16_f32 v224, v20, v21\n\t"
      "v_mfma_f32_16x16x32_bf16 v[28:31], v[88:91], v[52:55], v[28:31]\n\t"
      "ds_read_b64_tr_b16 a[52:53], v117 offset:40960\n\t"
      "s_waitcnt lgkmcnt(12)\n\t"
      "ds_read_b64_tr_b16 a[54:55], v117 offset:41984\n\t"
      "v_cvt_pk_bf16_f32 v225, v22, v23\n\t"
      "v_mfma_f32_16x16x32_bf16 v[24:27], v[92:95], v[56:59], v[24:27]\n\t"
      "ds_read_b128 v[76:79], v103 offset:9216\n\t"
      "v_mfma_f32_16x16x32_bf16 v[28:31], v[92:95], v[60:63], v[28:31]\n\t"
      "ds_read_b64_tr_b16 a[116:117], v118 offset:40960\n\t"
      "s_waitcnt lgkmcnt(12)\n\t"
      "ds_read_b64_tr_b16 a[118:119], v118 offset:41984\n\t"
      "v_mfma_f32_16x16x32_bf16 a[232:235], v[220:223], v[220:223], a[232:235]\n\t"
      "s_waitcnt vmcnt(12)\n\t"
      "s_waitcnt lgkmcnt(0)\n\t"
      "s_barrier\n\t"
      "v_mfma_f32_16x16x32_bf16 v[16:19], v[0:3], v[32:35], 0\n\t"
      "ds_read_b128 v[80:83], v104 offset:16384\n\t"
      "v_mfma_f32_16x16x32_bf16 v[20:23], v[0:3], v[36:39], 0\n\t"
      "s_add_u32 m0, s55, 131072\n\t"
      "s_xor_b32 s61, s47, 1048576\n\t"
      "buffer_load_dwordx4 v116, s[88:91], s61 offen lds\n\t"
      "ds_read_b64_tr_b16 a[56:57], v117 offset:49152\n\t"
      "v_mfma_f32_16x16x32_bf16 v[16:19], v[4:7], v[40:43], v[16:19]\n\t"
      "ds_read_b128 v[84:87], v119 offset:16384\n\t"
      "v_cvt_pk_bf16_f32 v162, v24, v25\n\t"
      "v_mfma_f32_16x16x32_bf16 v[20:23], v[4:7], v[44:47], v[20:23]\n\t"
      "s_add_u32 m0, s55, 132096\n\t"
      "s_xor_b32 s61, s47, 1081344\n\t"
      "buffer_load_dwordx4 v116, s[88:91], s61 offen lds\n\t"
      "ds_read_b64_tr_b16 a[58:59], v117 offset:50176\n\t"
      "v_cvt_pk_bf16_f32 v163, v26, v27\n\t"
      "v_mfma_f32_16x16x32_bf16 v[16:19], v[8:11], v[48:51], v[16:19]\n\t"
      "ds_read_b128 v[88:91], v102 offset:16384\n\t"
      "v_cvt_pk_bf16_f32 v226, v28, v29\n\t"
      "v_mfma_f32_16x16x32_bf16 v[20:23], v[8:11], v[52:55], v[20:23]\n\t"
      "s_add_u32 m0, s55, 139264\n\t"
      "s_xor_b32 s61, s47, 1310720\n\t"
      "buffer_load_dwordx4 v116, s[88:91], s61 offen lds\n\t"
      "ds_read_b64_tr_b16 a[120:121], v118 offset:49152\n\t"
      "v_cvt_pk_bf16_f32 v227, v30, v31\n\t"
      "v_mfma_f32_16x16x32_bf16 v[16:19], v[12:15], v[56:59], v[16:19]\n\t"
      "ds_read_b128 v[92:95], v103 offset:16384\n\t"
      "v_mfma_f32_16x16x32_bf16 v[20:23], v[12:15], v[60:63], v[20:23]\n\t"
      "s_add_u32 m0, s55, 140288\n\t"
      "s_xor_b32 s61, s47, 1343488\n\t"
      "buffer_load_dwordx4 v116, s[88:91], s61 offen lds\n\t"
      "ds_read_b64_tr_b16 a[122:123], v118 offset:50176\n\t"
      "v_mfma_f32_16x16x32_bf16 a[168:171], v[160:163], v[160:163], a[168:171]\n\t"
      "v_mfma_f32_16x16x32_bf16 v[24:27], v[64:67], v[32:35], 0\n\t"
      "ds_read_b128 v[0:3], v104 offset:17408\n\t"
      "v_mfma_f32_16x16x32_bf16 v[28:31], v[64:67], v[36:39], 0\n\t"
      "ds_read_b64_tr_b16 a[60:61], v117 offset:57344\n\t"
      "ds_read_b64_tr_b16 a[62:63], v117 offset:58368\n\t"
      "v_mfma_f32_16x16x32_bf16 v[24:27], v[68:71], v[40:43], v[24:27]\n\t"
      "ds_read_b128 v[4:7], v119 offset:17408\n\t"
      "v_cvt_pk_bf16_f32 v164, v16, v17\n\t"
      "v_mfma_f32_16x16x32_bf16 v[28:31], v[68:71], v[44:47], v[28:31]\n\t"
      "ds_read_b64_tr_b16 a[124:125], v118 offset:57344\n\t"
      "ds_read_b64_tr_b16 a[126:127], v118 offset:58368\n\t"
      "v_cvt_pk_bf16_f32 v165, v18, v19\n\t"
      "v_mfma_f32_16x16x32_bf16 v[24:27], v[72:75], v[48:51], v[24:27]\n\t"
      "ds_read_b128 v[8:11], v102 offset:17408\n\t"
      "v_cvt_pk_bf16_f32 v228, v20, v21\n\t"
      "v_mfma_f32_16x16x32_bf16 v[28:31], v[72:75], v[52:55], v[28:31]\n\t"
      "v_cvt_pk_bf16_f32 v229, v22, v23\n\t"
      "v_mfma_f32_16x16x32_bf16 v[24:27], v[76:79], v[56:59], v[24:27]\n\t"
      "s_waitcnt lgkmcnt(12)\n\t"
      "ds_read_b128 v[12:15], v103 offset:17408\n\t"
      "v_mfma_f32_16x16x32_bf16 v[28:31], v[76:79], v[60:63], v[28:31]\n\t"
      "v_mfma_f32_16x16x32_bf16 a[232:235], v[224:227], v[224:227], a[232:235]\n\t"
      "s_waitcnt vmcnt(10)\n\t"
      "s_waitcnt lgkmcnt(2)\n\t"
      "s_barrier\n\t"
      "v_mfma_f32_16x16x32_bf16 v[16:19], v[80:83], v[32:35], 0\n\t"
      "ds_read_b128 v[64:67], v104 offset:24576\n\t"
      "v_mfma_f32_16x16x32_bf16 v[20:23], v[80:83], v[36:39], 0\n\t"
      "s_add_u32 m0, s55, 147456\n\t"
      "s_xor_b32 s61, s47, 1572864\n\t"
      "buffer_load_dwordx4 v116, s[88:91], s61 offen lds\n\t"
      "ds_read_b64_tr_b16 a[244:245], v106 offset:16384\n\t"
      "v_mfma_f32_16x16x32_bf16 v[16:19], v[84:87], v[40:43], v[16:19]\n\t"
      "ds_read_b128 v[68:71], v119 offset:24576\n\t"
      "v_cvt_pk_bf16_f32 v166, v24, v25\n\t"
      "v_mfma_f32_16x16x32_bf16 v[20:23], v[84:87], v[44:47], v[20:23]\n\t"
      "s_add_u32 m0, s55, 148480\n\t"
      "s_xor_b32 s61, s47, 1605632\n\t"
      "buffer_load_dwordx4 v116, s[88:91], s61 offen lds\n\t"
      "ds_read_b64_tr_b16 a[246:247], v106 offset:17408\n\t"
      "v_cvt_pk_bf16_f32 v167, v26, v27\n\t"
      "v_mfma_f32_16x16x32_bf16 v[16:19], v[88:91], v[48:51], v[16:19]\n\t"
      "ds_read_b128 v[72:75], v102 offset:24576\n\t"
      "v_cvt_pk_bf16_f32 v230, v28, v29\n\t"
      "v_mfma_f32_16x16x32_bf16 v[20:23], v[88:91], v[52:55], v[20:23]\n\t"
      "s_add_u32 m0, s55, 155648\n\t"
      "s_xor_b32 s61, s47, 1835008\n\t"
      "buffer_load_dwordx4 v116, s[88:91], s61 offen lds\n\t"
      "ds_read_b64_tr_b16 a[128:129], v117\n\t"
      "v_cvt_pk_bf16_f32 v231, v30, v31\n\t"
      "v_mfma_f32_16x16x32_bf16 v[16:19], v[92:95], v[56:59], v[16:19]\n\t"
      "ds_read_b128 v[76:79], v103 offset:24576\n\t"
      "v_mfma_f32_16x16x32_bf16 v[20:23], v[92:95], v[60:63], v[20:23]\n\t"
      "s_add_u32 m0, s55, 156672\n\t"
      "s_xor_b32 s61, s47, 1867776\n\t"
      "buffer_load_dwordx4 v116, s[88:91], s61 offen lds\n\t"
      "ds_read_b64_tr_b16 a[130:131], v117 offset:1024\n\t"
      "v_mfma_f32_16x16x32_bf16 a[168:171], v[164:167], v[164:167], a[168:171]\n\t"
      "v_mfma_f32_16x16x32_bf16 v[24:27], v[0:3], v[32:35], 0\n\t"
      "ds_read_b128 v[80:83], v104 offset:25600\n\t"
      "v_mfma_f32_16x16x32_bf16 v[28:31], v[0:3], v[36:39], 0\n\t"
      "ds_read_b64_tr_b16 a[192:193], v118\n\t"
      "ds_read_b64_tr_b16 a[194:195], v118 offset:1024\n\t"
      "v_mfma_f32_16x16x32_bf16 v[24:27], v[4:7], v[40:43], v[24:27]\n\t"
      "ds_read_b128 v[84:87], v119 offset:25600\n\t"
      "v_cvt_pk_bf16_f32 v168, v16, v17\n\t"
      "v_mfma_f32_16x16x32_bf16 v[28:31], v[4:7], v[44:47], v[28:31]\n\t"
      "ds_read_b64_tr_b16 a[132:133], v117 offset:8192\n\t"
      "s_waitcnt lgkmcnt(12)\n\t"
      "ds_read_b64_tr_b16 a[134:135], v117 offset:9216\n\t"
      "v_cvt_pk_bf16_f32 v169, v18, v19\n\t"
      "v_mfma_f32_16x16x32_bf16 v[24:27], v[8:11], v[48:51], v[24:27]\n\t"
      "ds_read_b128 v[88:91], v102 offset:25600\n\t"
      "v_cvt_pk_bf16_f32 v232, v20, v21\n\t"
      "v_mfma_f32_16x16x32_bf16 v[28:31], v[8:11], v[52:55], v[28:31]\n\t"
      "ds_read_b64_tr_b16 a[196:197], v118 offset:8192\n\t"
      "s_waitcnt lgkmcnt(12)\n\t"
      "ds_read_b64_tr_b16 a[198:199], v118 offset:9216\n\t"
      "v_cvt_pk_bf16_f32 v233, v22, v23\n\t"
      "v_mfma_f32_16x16x32_bf16 v[24:27], v[12:15], v[56:59], v[24:27]\n\t"
      "ds_read_b128 v[92:95], v103 offset:25600\n\t"
      "v_mfma_f32_16x16x32_bf16 v[28:31], v[12:15], v[60:63], v[28:31]\n\t"
      "v_mfma_f32_16x16x32_bf16 a[232:235], v[228:231], v[228:231], a[232:235]\n\t"
      "s_waitcnt vmcnt(8)\n\t"
      "s_waitcnt lgkmcnt(1)\n\t"
      "s_barrier\n\t"
      "v_mfma_f32_16x16x32_bf16 v[16:19], v[64:67], v[32:35], 0\n\t"
      "ds_read_b128 v[0:3], v104 offset:32768\n\t"
      "v_mfma_f32_16x16x32_bf16 v[20:23], v[64:67], v[36:39], 0\n\t"
      "s_add_u32 m0, s55, 98304\n\t"
      "s_xor_b32 s61, s47, 2097152\n\t"
      "buffer_load_dwordx4 v116, s[88:91], s61 offen lds\n\t"
      "ds_read_b64_tr_b16 a[136:137], v117 offset:16384\n\t"
      "v_mfma_f32_16x16x32_bf16 v[16:19], v[68:71], v[40:43], v[16:19]\n\t"
      "ds_read_b128 v[4:7], v119 offset:32768\n\t"
      "v_cvt_pk_bf16_f32 v170, v24, v25\n\t"
      "v_mfma_f32_16x16x32_bf16 v[20:23], v[68:71], v[44:47], v[20:23]\n\t"
      "s_add_u32 m0, s55, 99328\n\t"
      "s_xor_b32 s61, s47, 2129920\n\t"
      "buffer_load_dwordx4 v116, s[88:91], s61 offen lds\n\t"
      "ds_read_b64_tr_b16 a[138:139], v117 offset:17408\n\t"
      "v_cvt_pk_bf16_f32 v171, v26, v27\n\t"
      "v_mfma_f32_16x16x32_bf16 v[16:19], v[72:75], v[48:51], v[16:19]\n\t"
      "ds_read_b128 v[8:11], v102 offset:32768\n\t"
      "v_cvt_pk_bf16_f32 v234, v28, v29\n\t"
      "v_mfma_f32_16x16x32_bf16 v[20:23], v[72:75], v[52:55], v[20:23]\n\t"
      "s_add_u32 m0, s55, 106496\n\t"
      "s_xor_b32 s61, s47, 2359296\n\t"
      "buffer_load_dwordx4 v116, s[88:91], s61 offen lds\n\t"
      "ds_read_b64_tr_b16 a[200:201], v118 offset:16384\n\t"
      "v_cvt_pk_bf16_f32 v235, v30, v31\n\t"
      "v_mfma_f32_16x16x32_bf16 v[16:19], v[76:79], v[56:59], v[16:19]\n\t"
      "ds_read_b128 v[12:15], v103 offset:32768\n\t"
      "v_mfma_f32_16x16x32_bf16 v[20:23], v[76:79], v[60:63], v[20:23]\n\t"
      "s_add_u32 m0, s55, 107520\n\t"
      "s_xor_b32 s61, s47, 2392064\n\t"
      "buffer_load_dwordx4 v116, s[88:91], s61 offen lds\n\t"
      "ds_read_b64_tr_b16 a[202:203], v118 offset:17408\n\t"
      "v_mfma_f32_16x16x32_bf16 a[168:171], v[168:171], v[168:171], a[168:171]\n\t"
      "v_mfma_f32_16x16x32_bf16 v[24:27], v[80:83], v[32:35], 0\n\t"
      "ds_read_b128 v[64:67], v104 offset:33792\n\t"
      "v_mfma_f32_16x16x32_bf16 v[28:31], v[80:83], v[36:39], 0\n\t"
      "ds_read_b64_tr_b16 a[140:141], v117 offset:24576\n\t"
      "ds_read_b64_tr_b16 a[142:143], v117 offset:25600\n\t"
      "v_mfma_f32_16x16x32_bf16 v[24:27], v[84:87], v[40:43], v[24:27]\n\t"
      "ds_read_b128 v[68:71], v119 offset:33792\n\t"
      "v_cvt_pk_bf16_f32 v172, v16, v17\n\t"
      "v_mfma_f32_16x16x32_bf16 v[28:31], v[84:87], v[44:47], v[28:31]\n\t"
      "ds_read_b64_tr_b16 a[204:205], v118 offset:24576\n\t"
      "ds_read_b64_tr_b16 a[206:207], v118 offset:25600\n\t"
      "v_cvt_pk_bf16_f32 v173, v18, v19\n\t"
      "v_mfma_f32_16x16x32_bf16 v[24:27], v[88:91], v[48:51], v[24:27]\n\t"
      "s_waitcnt lgkmcnt(12)\n\t"
      "ds_read_b128 v[72:75], v102 offset:33792\n\t"
      "v_cvt_pk_bf16_f32 v236, v20, v21\n\t"
      "v_mfma_f32_16x16x32_bf16 v[28:31], v[88:91], v[52:55], v[28:31]\n\t"
      "v_cvt_pk_bf16_f32 v237, v22, v23\n\t"
      "v_mfma_f32_16x16x32_bf16 v[24:27], v[92:95], v[56:59], v[24:27]\n\t"
      "ds_read_b128 v[76:79], v103 offset:33792\n\t"
      "v_mfma_f32_16x16x32_bf16 v[28:31], v[92:95], v[60:63], v[28:31]\n\t"
      "v_mfma_f32_16x16x32_bf16 a[232:235], v[232:235], v[232:235], a[232:235]\n\t"
      "s_waitcnt vmcnt(8)\n\t"
      "s_waitcnt lgkmcnt(2)\n\t"
      "s_barrier\n\t"
      "v_mfma_f32_16x16x32_bf16 v[16:19], v[0:3], v[32:35], 0\n\t"
      "ds_read_b128 v[80:83], v104 offset:40960\n\t"
      "v_mfma_f32_16x16x32_bf16 v[20:23], v[0:3], v[36:39], 0\n\t"
      "s_add_u32 m0, s55, 114688\n\t"
      "s_xor_b32 s61, s47, 2621440\n\t"
      "buffer_load_dwordx4 v116, s[88:91], s61 offen lds\n\t"
      "ds_read_b64_tr_b16 a[248:249], v106 offset:32768\n\t"
      "v_mfma_f32_16x16x32_bf16 v[16:19], v[4:7], v[40:43], v[16:19]\n\t"
      "ds_read_b128 v[84:87], v119 offset:40960\n\t"
      "v_cvt_pk_bf16_f32 v174, v24, v25\n\t"
      "v_mfma_f32_16x16x32_bf16 v[20:23], v[4:7], v[44:47], v[20:23]\n\t"
      "s_add_u32 m0, s55, 115712\n\t"
      "s_xor_b32 s61, s47, 2654208\n\t"
      "buffer_load_dwordx4 v116, s[88:91], s61 offen lds\n\t"
      "ds_read_b64_tr_b16 a[250:251], v106 offset:33792\n\t"
      "v_cvt_pk_bf16_f32 v175, v26, v27\n\t"
      "v_mfma_f32_16x16x32_bf16 v[16:19], v[8:11], v[48:51], v[16:19]\n\t"
      "ds_read_b128 v[88:91], v102 offset:40960\n\t"
      "v_cvt_pk_bf16_f32 v238, v28, v29\n\t"
      "v_mfma_f32_16x16x32_bf16 v[20:23], v[8:11], v[52:55], v[20:23]\n\t"
      "s_add_u32 m0, s55, 122880\n\t"
      "s_xor_b32 s61, s47, 2883584\n\t"
      "buffer_load_dwordx4 v116, s[88:91], s61 offen lds\n\t"
      "ds_read_b64_tr_b16 a[144:145], v117 offset:32768\n\t"
      "v_cvt_pk_bf16_f32 v239, v30, v31\n\t"
      "v_mfma_f32_16x16x32_bf16 v[16:19], v[12:15], v[56:59], v[16:19]\n\t"
      "ds_read_b128 v[92:95], v103 offset:40960\n\t"
      "v_mfma_f32_16x16x32_bf16 v[20:23], v[12:15], v[60:63], v[20:23]\n\t"
      "s_add_u32 m0, s55, 123904\n\t"
      "s_xor_b32 s61, s47, 2916352\n\t"
      "buffer_load_dwordx4 v116, s[88:91], s61 offen lds\n\t"
      "ds_read_b64_tr_b16 a[146:147], v117 offset:33792\n\t"
      "v_mfma_f32_16x16x32_bf16 a[168:171], v[172:175], v[172:175], a[168:171]\n\t"
      "v_mfma_f32_16x16x32_bf16 v[24:27], v[64:67], v[32:35], 0\n\t"
      "ds_read_b128 v[0:3], v104 offset:41984\n\t"
      "v_mfma_f32_16x16x32_bf16 v[28:31], v[64:67], v[36:39], 0\n\t"
      "ds_read_b64_tr_b16 a[208:209], v118 offset:32768\n\t"
      "ds_read_b64_tr_b16 a[210:211], v118 offset:33792\n\t"
      "v_mfma_f32_16x16x32_bf16 v[24:27], v[68:71], v[40:43], v[24:27]\n\t"
      "ds_read_b128 v[4:7], v119 offset:41984\n\t"
      "v_cvt_pk_bf16_f32 v176, v16, v17\n\t"
      "v_mfma_f32_16x16x32_bf16 v[28:31], v[68:71], v[44:47], v[28:31]\n\t"
      "ds_read_b64_tr_b16 a[148:149], v117 offset:40960\n\t"
      "s_waitcnt lgkmcnt(12)\n\t"
      "ds_read_b64_tr_b16 a[150:151], v117 offset:41984\n\t"
      "v_cvt_pk_bf16_f32 v177, v18, v19\n\t"
      "v_mfma_f32_16x16x32_bf16 v[24:27], v[72:75], v[48:51], v[24:27]\n\t"
      "ds_read_b128 v[8:11], v102 offset:41984\n\t"
      "v_cvt_pk_bf16_f32 v240, v20, v21\n\t"
      "v_mfma_f32_16x16x32_bf16 v[28:31], v[72:75], v[52:55], v[28:31]\n\t"
      "ds_read_b64_tr_b16 a[212:213], v118 offset:40960\n\t"
      "s_waitcnt lgkmcnt(12)\n\t"
      "ds_read_b64_tr_b16 a[214:215], v118 offset:41984\n\t"
      "v_cvt_pk_bf16_f32 v241, v22, v23\n\t"
      "v_mfma_f32_16x16x32_bf16 v[24:27], v[76:79], v[56:59], v[24:27]\n\t"
      "ds_read_b128 v[12:15], v103 offset:41984\n\t"
      "v_mfma_f32_16x16x32_bf16 v[28:31], v[76:79], v[60:63], v[28:31]\n\t"
      "v_mfma_f32_16x16x32_bf16 a[232:235], v[236:239], v[236:239], a[232:235]\n\t"
      "s_waitcnt vmcnt(8)\n\t"
      "s_waitcnt lgkmcnt(1)\n\t"
      "s_barrier\n\t"
      "v_mfma_f32_16x16x32_bf16 v[16:19], v[80:83], v[32:35], 0\n\t"
      "ds_read_b128 v[64:67], v104 offset:49152\n\t"
      "v_mfma_f32_16x16x32_bf16 v[20:23], v[80:83], v[36:39], 0\n\t"
      "s_add_u32 m0, s55, 131072\n\t"
      "s_xor_b32 s61, s47, 3145728\n\t"
      "buffer_load_dwordx4 v116, s[88:91], s61 offen lds\n\t"
      "ds_read_b64_tr_b16 a[152:153], v117 offset:49152\n\t"
      "v_mfma_f32_16x16x32_bf16 v[16:19], v[84:87], v[40:43], v[16:19]\n\t"
      "ds_read_b128 v[68:71], v119 offset:49152\n\t"
      "v_cvt_pk_bf16_f32 v178, v24, v25\n\t"
      "v_mfma_f32_16x16x32_bf16 v[20:23], v[84:87], v[44:47], v[20:23]\n\t"
      "s_add_u32 m0, s55, 132096\n\t"
      "s_xor_b32 s61, s47, 3178496\n\t"
      "buffer_load_dwordx4 v116, s[88:91], s61 offen lds\n\t"
      "ds_read_b64_tr_b16 a[154:155], v117 offset:50176\n\t"
      "v_cvt_pk_bf16_f32 v179, v26, v27\n\t"
      "v_mfma_f32_16x16x32_bf16 v[16:19], v[88:91], v[48:51], v[16:19]\n\t"
      "ds_read_b128 v[72:75], v102 offset:49152\n\t"
      "v_cvt_pk_bf16_f32 v242, v28, v29\n\t"
      "v_mfma_f32_16x16x32_bf16 v[20:23], v[88:91], v[52:55], v[20:23]\n\t"
      "s_add_u32 m0, s55, 139264\n\t"
      "s_xor_b32 s61, s47, 3407872\n\t"
      "buffer_load_dwordx4 v116, s[88:91], s61 offen lds\n\t"
      "ds_read_b64_tr_b16 a[216:217], v118 offset:49152\n\t"
      "v_cvt_pk_bf16_f32 v243, v30, v31\n\t"
      "v_mfma_f32_16x16x32_bf16 v[16:19], v[92:95], v[56:59], v[16:19]\n\t"
      "ds_read_b128 v[76:79], v103 offset:49152\n\t"
      "v_mfma_f32_16x16x32_bf16 v[20:23], v[92:95], v[60:63], v[20:23]\n\t"
      "s_add_u32 m0, s55, 140288\n\t"
      "s_xor_b32 s61, s47, 3440640\n\t"
      "buffer_load_dwordx4 v116, s[88:91], s61 offen lds\n\t"
      "ds_read_b64_tr_b16 a[218:219], v118 offset:50176\n\t"
      "v_mfma_f32_16x16x32_bf16 a[168:171], v[176:179], v[176:179], a[168:171]\n\t"
      "v_mfma_f32_16x16x32_bf16 v[24:27], v[0:3], v[32:35], 0\n\t"
      "ds_read_b128 v[80:83], v104 offset:50176\n\t"
      "v_mfma_f32_16x16x32_bf16 v[28:31], v[0:3], v[36:39], 0\n\t"
      "ds_read_b64_tr_b16 a[156:157], v117 offset:57344\n\t"
      "ds_read_b64_tr_b16 a[158:159], v117 offset:58368\n\t"
      "v_mfma_f32_16x16x32_bf16 v[24:27], v[4:7], v[40:43], v[24:27]\n\t"
      "ds_read_b128 v[84:87], v119 offset:50176\n\t"
      "v_cvt_pk_bf16_f32 v180, v16, v17\n\t"
      "v_mfma_f32_16x16x32_bf16 v[28:31], v[4:7], v[44:47], v[28:31]\n\t"
      "ds_read_b64_tr_b16 a[220:221], v118 offset:57344\n\t"
      "ds_read_b64_tr_b16 a[222:223], v118 offset:58368\n\t"
      "v_cvt_pk_bf16_f32 v181, v18, v19\n\t"
      "v_mfma_f32_16x16x32_bf16 v[24:27], v[8:11], v[48:51], v[24:27]\n\t"
      "s_waitcnt lgkmcnt(12)\n\t"
      "ds_read_b128 v[88:91], v102 offset:50176\n\t"
      "v_cvt_pk_bf16_f32 v244, v20, v21\n\t"
      "v_mfma_f32_16x16x32_bf16 v[28:31], v[8:11], v[52:55], v[28:31]\n\t"
      "v_cvt_pk_bf16_f32 v245, v22, v23\n\t"
      "v_mfma_f32_16x16x32_bf16 v[24:27], v[12:15], v[56:59], v[24:27]\n\t"
      "ds_read_b128 v[92:95], v103 offset:50176\n\t"
      "v_mfma_f32_16x16x32_bf16 v[28:31], v[12:15], v[60:63], v[28:31]\n\t"
      "v_mfma_f32_16x16x32_bf16 a[232:235], v[240:243], v[240:243], a[232:235]\n\t"
      "s_waitcnt vmcnt(8)\n\t"
      "s_waitcnt lgkmcnt(2)\n\t"
      "s_barrier\n\t"
      "v_mfma_f32_16x16x32_bf16 v[16:19], v[64:67], v[32:35], 0\n\t"
      "ds_read_b128 v[0:3], v104 offset:57344\n\t"
      "v_mfma_f32_16x16x32_bf16 v[20:23], v[64:67], v[36:39], 0\n\t"
      "s_add_u32 m0, s55, 147456\n\t"
      "s_xor_b32 s61, s47, 3670016\n\t"
      "buffer_load_dwordx4 v116, s[88:91], s61 offen lds\n\t"
      "ds_read_b64_tr_b16 a[252:253], v106 offset:49152\n\t"
      "v_mfma_f32_16x16x32_bf16 v[16:19], v[68:71], v[40:43], v[16:19]\n\t"
      "ds_read_b128 v[4:7], v119 offset:57344\n\t"
      "v_cvt_pk_bf16_f32 v182, v24, v25\n\t"
      "v_mfma_f32_16x16x32_bf16 v[20:23], v[68:71], v[44:47], v[20:23]\n\t"
      "s_add_u32 m0, s55, 148480\n\t"
      "s_xor_b32 s61, s47, 3702784\n\t"
      "buffer_load_dwordx4 v116, s[88:91], s61 offen lds\n\t"
      "ds_read_b64_tr_b16 a[254:255], v106 offset:50176\n\t"
      "v_cvt_pk_bf16_f32 v183, v26, v27\n\t"
      "v_mfma_f32_16x16x32_bf16 v[16:19], v[72:75], v[48:51], v[16:19]\n\t"
      "ds_read_b128 v[8:11], v102 offset:57344\n\t"
      "v_cvt_pk_bf16_f32 v246, v28, v29\n\t"
      "v_mfma_f32_16x16x32_bf16 v[20:23], v[72:75], v[52:55], v[20:23]\n\t"
      "s_add_u32 m0, s55, 155648\n\t"
      "s_xor_b32 s61, s47, 3932160\n\t"
      "buffer_load_dwordx4 v116, s[88:91], s61 offen lds\n\t"
      "ds_read_b64_tr_b16 a[160:161], v117\n\t"
      "v_cvt_pk_bf16_f32 v247, v30, v31\n\t"
      "v_mfma_f32_16x16x32_bf16 v[16:19], v[76:79], v[56:59], v[16:19]\n\t"
      "ds_read_b128 v[12:15], v103 offset:57344\n\t"
      "v_mfma_f32_16x16x32_bf16 v[20:23], v[76:79], v[60:63], v[20:23]\n\t"
      "s_add_u32 m0, s55, 156672\n\t"
      "s_xor_b32 s61, s47, 3964928\n\t"
      "buffer_load_dwordx4 v116, s[88:91], s61 offen lds\n\t"
      "ds_read_b64_tr_b16 a[162:163], v117 offset:1024\n\t"
      "v_mfma_f32_16x16x32_bf16 a[168:171], v[180:183], v[180:183], a[168:171]\n\t"
      "v_mfma_f32_16x16x32_bf16 v[24:27], v[80:83], v[32:35], 0\n\t"
      "ds_read_b128 v[64:67], v104 offset:58368\n\t"
      "v_mfma_f32_16x16x32_bf16 v[28:31], v[80:83], v[36:39], 0\n\t"
      "ds_read_b64_tr_b16 a[224:225], v118\n\t"
      "ds_read_b64_tr_b16 a[226:227], v118 offset:1024\n\t"
      "v_mfma_f32_16x16x32_bf16 v[24:27], v[84:87], v[40:43], v[24:27]\n\t"
      "ds_read_b128 v[68:71], v119 offset:58368\n\t"
      "v_cvt_pk_bf16_f32 v184, v16, v17\n\t"
      "v_mfma_f32_16x16x32_bf16 v[28:31], v[84:87], v[44:47], v[28:31]\n\t"
      "ds_read_b64_tr_b16 a[164:165], v117 offset:8192\n\t"
      "s_waitcnt lgkmcnt(12)\n\t"
      "ds_read_b64_tr_b16 a[166:167], v117 offset:9216\n\t"
      "v_cvt_pk_bf16_f32 v185, v18, v19\n\t"
      "v_mfma_f32_16x16x32_bf16 v[24:27], v[88:91], v[48:51], v[24:27]\n\t"
      "ds_read_b128 v[72:75], v102 offset:58368\n\t"
      "v_cvt_pk_bf16_f32 v248, v20, v21\n\t"
      "v_mfma_f32_16x16x32_bf16 v[28:31], v[88:91], v[52:55], v[28:31]\n\t"
      "ds_read_b64_tr_b16 a[228:229], v118 offset:8192\n\t"
      "s_waitcnt lgkmcnt(12)\n\t"
      "ds_read_b64_tr_b16 a[230:231], v118 offset:9216\n\t"
      "v_cvt_pk_bf16_f32 v249, v22, v23\n\t"
      "v_mfma_f32_16x16x32_bf16 v[24:27], v[92:95], v[56:59], v[24:27]\n\t"
      "ds_read_b128 v[76:79], v103 offset:58368\n\t"
      "v_mfma_f32_16x16x32_bf16 v[28:31], v[92:95], v[60:63], v[28:31]\n\t"
      "v_mfma_f32_16x16x32_bf16 a[232:235], v[244:247], v[244:247], a[232:235]\n\t"
      "v_mfma_f32_16x16x32_bf16 v[16:19], v[0:3], v[32:35], 0\n\t"
      "v_mfma_f32_16x16x32_bf16 v[20:23], v[0:3], v[36:39], 0\n\t"
      "v_mfma_f32_16x16x32_bf16 v[16:19], v[4:7], v[40:43], v[16:19]\n\t"
      "v_cvt_pk_bf16_f32 v186, v24, v25\n\t"
      "v_mfma_f32_16x16x32_bf16 v[20:23], v[4:7], v[44:47], v[20:23]\n\t"
      "v_cvt_pk_bf16_f32 v187, v26, v27\n\t"
      "s_waitcnt lgkmcnt(13)\n\t"
      "v_mfma_f32_16x16x32_bf16 v[16:19], v[8:11], v[48:51], v[16:19]\n\t"
      "v_cvt_pk_bf16_f32 v250, v28, v29\n\t"
      "v_mfma_f32_16x16x32_bf16 v[20:23], v[8:11], v[52:55], v[20:23]\n\t"
      "v_cvt_pk_bf16_f32 v251, v30, v31\n\t"
      "s_waitcnt lgkmcnt(11)\n\t"
      "v_mfma_f32_16x16x32_bf16 v[16:19], v[12:15], v[56:59], v[16:19]\n\t"
      "v_mfma_f32_16x16x32_bf16 v[20:23], v[12:15], v[60:63], v[20:23]\n\t"
      "v_mfma_f32_16x16x32_bf16 a[168:171], v[184:187], v[184:187], a[168:171]\n\t"
      "buffer_load_dwordx4 v[96:99], off, s[36:39], s54 sc1\n\t"
      "s_waitcnt lgkmcnt(9)\n\t"
      "v_mfma_f32_16x16x32_bf16 v[24:27], v[64:67], v[32:35], 0\n\t"
      "v_mfma_f32_16x16x32_bf16 v[28:31], v[64:67], v[36:39], 0\n\t"
      "s_waitcnt lgkmcnt(6)\n\t"
      "v_mfma_f32_16x16x32_bf16 v[24:27], v[68:71], v[40:43], v[24:27]\n\t"
      "v_cvt_pk_bf16_f32 v188, v16, v17\n\t"
      "v_mfma_f32_16x16x32_bf16 v[28:31], v[68:71], v[44:47], v[28:31]\n\t"
      "v_cvt_pk_bf16_f32 v189, v18, v19\n\t"
      "s_waitcnt lgkmcnt(3)\n\t"
      "v_mfma_f32_16x16x32_bf16 v[24:27], v[72:75], v[48:51], v[24:27]\n\t"
      "v_cvt_pk_bf16_f32 v252, v20, v21\n\t"
      "v_mfma_f32_16x16x32_bf16 v[28:31], v[72:75], v[52:55], v[28:31]\n\t"
      "v_cvt_pk_bf16_f32 v253, v22, v23\n\t"
      "s_waitcnt lgkmcnt(0)\n\t"
      "v_mfma_f32_16x16x32_bf16 v[24:27], v[76:79], v[56:59], v[24:27]\n\t"
      "v_mfma_f32_16x16x32_bf16 v[28:31], v[76:79], v[60:63], v[28:31]\n\t"
      "v_mfma_f32_16x16x32_bf16 a[232:235], v[248:251], v[248:251], a[232:235]\n\t"
      "v_lshl_add_u32 v8, s41, 10, v127\n\t"
      "v_add_u32 v8, 65536, v8\n\t"
      "v_mov_b32 v12, 0\n\t"
      "v_mov_b32 v9, s41\n\t"
      "v_lshlrev_b32 v9, 2, v9\n\t"
      "v_cvt_pk_bf16_f32 v190, v24, v25\n\t"
      "v_cvt_pk_bf16_f32 v191, v26, v27\n\t"
      "v_cvt_pk_bf16_f32 v254, v28, v29\n\t"
      "v_cvt_pk_bf16_f32 v255, v30, v31\n\t"
      "s_nop 1\n\t"
      "v_mfma_f32_16x16x32_bf16 a[168:171], v[188:191], v[188:191], a[168:171]\n\t"
      "v_mfma_f32_16x16x32_bf16 a[232:235], v[252:255], v[252:255], a[232:235]\n\t"
      "s_sub_u32 s64, s46, 786432\n\t"
      "s_mov_b32 s71, 98304\n\t"
      "s_mov_b32 s63, 0x10000\n\t"
      "s_nop 4\n\t"
      "v_accvgpr_read_b32 v0, a168\n\t"
      "v_accvgpr_read_b32 v1, a169\n\t"
      "v_accvgpr_read_b32 v2, a170\n\t"
      "v_accvgpr_read_b32 v3, a171\n\t"
      "v_accvgpr_read_b32 v4, a232\n\t"
      "v_accvgpr_read_b32 v5, a233\n\t"
      "v_accvgpr_read_b32 v6, a234\n\t"
      "v_accvgpr_read_b32 v7, a235\n\t"
      "v_and_b32 v0, 0x7fffffff, v0\n\t"
      "v_and_b32 v1, 0x7fffffff, v1\n\t"
      "v_and_b32 v2, 0x7fffffff, v2\n\t"
      "v_and_b32 v3, 0x7fffffff, v3\n\t"
      "v_and_b32 v4, 0x7fffffff, v4\n\t"
      "v_and_b32 v5, 0x7fffffff, v5\n\t"
      "v_and_b32 v6, 0x7fffffff, v6\n\t"
      "v_and_b32 v7, 0x7fffffff, v7\n\t"
      "v_max3_u32 v0, v0, v1, v2\n\t"
      "v_max3_u32 v3, v3, v4, v5\n\t"
      "v_max3_u32 v0, v0, v6, v7\n\t"
      "v_max_u32 v0, v0, v3\n\t"
      "s_nop 1\n\t"
      "v_max_u32_dpp v0, v0, v0 quad_perm:[1,0,3,2] row_mask:0xf bank_mask:0xf\n\t"
      "s_nop 1\n\t"
      "v_max_u32_dpp v0, v0, v0 quad_perm:[2,3,0,1] row_mask:0xf bank_mask:0xf\n\t"
      "s_nop 1\n\t"
      "v_max_u32_dpp v0, v0, v0 row_ror:4 row_mask:0xf bank_mask:0xf\n\t"
      "s_nop 1\n\t"
      "v_max_u32_dpp v0, v0, v0 row_ror:8 row_mask:0xf bank_mask:0xf\n\t"
      "v_mov_b32 v1, v0\n\t"
      "s_nop 1\n\t"
      "v_permlane16_swap_b32 v0, v1\n\t"
      "v_max_u32 v0, v0, v1\n\t"
      "v_mov_b32 v1, v0\n\t"
      "s_nop 1\n\t"
      "v_permlane32_swap_b32 v0, v1\n\t"
      "v_max_u32 v0, v0, v1\n\t"
      "ds_write_b32 v9, v0 offset:32768\n\t"
      "s_waitcnt lgkmcnt(0)\n\t"
      "s_barrier\n\t"
      "ds_read_b128 v[4:7], v12 offset:32768\n\t"
      "s_waitcnt lgkmcnt(0)\n\t"
      "v_max_u32 v4, v4, v5\n\t"
      "v_max3_u32 v4, v4, v6, v7\n\t"
      "v_mov_b32 v5, s53\n\t"
      "s_memrealtime s[92:93]\n\t"
      "v_add_u32 v4, 0x10000, v4\n\t"
      "s_mov_b64 exec, 1\n\t"
      "s_mov_b32 s61, 0xffff\n\t"
      "s_waitcnt lgkmcnt(0)\n\t"
      "v_mov_b32 v6, s92\n\t"
      "v_bfi_b32 v4, s61, v6, v4\n\t"
      "buffer_store_dwordx2 v[4:5], off, s[36:39], s95 sc1\n\t"
      "s_mov_b64 exec, -1\n\t"
      "s_waitcnt vmcnt(1)\n\t"
      "v_bitop3_b32 v96, v96, v97, s53 bitop3:0x7e\n\t"
      "v_bitop3_b32 v98, v98, v99, s53 bitop3:0x7e\n\t"
      "v_or_b32 v96, v96, v98\n\t"
      "s_add_u32 m0, s55, 98304\n\t"
      "v_readfirstlane_b32 s56, v96\n\t"
      "s_nop 1\n\t"
      "s_cmp_eq_u32 s56, 0\n\t"
      "s_cbranch_scc0 LF2%=\n\t"
      "buffer_load_dwordx4 v127, s[36:39], s64 offen sc1 lds\n\t"
      "buffer_load_dwordx4 v127, s[36:39], s64 offen offset:1024 sc1 lds\n\t"
      "LF2%=:\n\t"
      "buffer_load_dwordx4 v[96:99], off, s[36:39], s54 sc1\n\t"
      "ds_write_b128 v8, a[176:179]\n\t"
      "ds_write_b128 v8, a[180:183] offset:4096\n\t"
      "ds_write_b128 v8, a[184:187] offset:8192\n\t"
      "ds_write_b128 v8, a[188:191] offset:12288\n\t"
      "ds_write_b128 v8, a[240:243] offset:16384\n\t"
      "ds_write_b128 v8, a[244:247] offset:20480\n\t"
      "ds_write_b128 v8, a[248:251] offset:24576\n\t"
      "ds_write_b128 v8, a[252:255] offset:28672\n\t"
      "ds_write_b128 v100, v[112:115] offset:57344\n\t"
      "s_cbranch_scc1 LF3%=\n\t"
      "LF0%=:\n\t"
      "s_waitcnt vmcnt(0)\n\t"
      "v_bitop3_b32 v96, v96, v97, s53 bitop3:0x7e\n\t"
      "v_bitop3_b32 v98, v98, v99, s53 bitop3:0x7e\n\t"
      "v_or_b32 v96, v96, v98\n\t"
      "s_add_u32 m0, s55, 98304\n\t"
      "v_readfirstlane_b32 s56, v96\n\t"
      "s_nop 1\n\t"
      "s_cmp_eq_u32 s56, 0\n\t"
      "s_cbranch_scc1 LF1%=\n\t"
      "buffer_load_dwordx4 v[96:99], off, s[36:39], s54 sc1\n\t"
      "s_sub_u32 s63, s63, 1\n\t"
      "s_cmp_lg_u32 s63, 0\n\t"
      "s_cbranch_scc1 LF0%=\n\t"
      "LF1%=:\n\t"
      "buffer_load_dwordx4 v127, s[36:39], s64 offen sc1 lds\n\t"
      "buffer_load_dwordx4 v127, s[36:39], s64 offen offset:1024 sc1 lds\n\t"
      "LF3%=:\n\t"
      "v_lshlrev_b32 v52, 4, v127\n\t"
      "s_mov_b32 s62, 0xffff0000\n\t"
      "v_accvgpr_read_b32 v85, a172\n\t"
      "v_accvgpr_read_b32 v87, a173\n\t"
      "v_accvgpr_read_b32 v89, a174\n\t"
      "v_accvgpr_read_b32 v91, a175\n\t"
      "v_accvgpr_read_b32 v93, a236\n\t"
      "v_accvgpr_read_b32 v95, a237\n\t"
      "v_lshl_or_b32 v84, v85, 16, 1\n\t"
      "v_and_or_b32 v85, v85, s62, 1\n\t"
      "v_lshl_or_b32 v86, v87, 16, 1\n\t"
      "v_and_or_b32 v87, v87, s62, 1\n\t"
      "v_lshl_or_b32 v88, v89, 16, 1\n\t"
      "v_and_or_b32 v89, v89, s62, 1\n\t"
      "v_lshl_or_b32 v90, v91, 16, 1\n\t"
      "v_and_or_b32 v91, v91, s62, 1\n\t"
      "v_lshl_or_b32 v92, v93, 16, 1\n\t"
      "v_and_or_b32 v93, v93, s62, 1\n\t"
      "v_lshl_or_b32 v94, v95, 16, 1\n\t"
      "v_and_or_b32 v95, v95, s62, 1\n\t"
      "s_cmp_eq_u32 s86, 0\n\t"
      "s_cbranch_scc1 LC9%=\n\t"
      "s_waitcnt lgkmcnt(0)\n\t"
      "s_memtime s[96:97]\n\t"
      "s_waitcnt lgkmcnt(0)\n\t"
      "s_buffer_load_dword s87, s[36:39], s86\n\t"
      "s_waitcnt lgkmcnt(0)\n\t"
      "s_memtime s[98:99]\n\t"
      "s_waitcnt lgkmcnt(0)\n\t"
      "LC9%=:\n\t"
      "s_sub_u32 s61, s98, s96\n\t"
      "s_lshr_b32 s61, s61, 2\n\t"
      "s_min_u32 s61, s61, 511\n\t"
      "s_sub_u32 s62, 0, s61\n\t"
      "s_bitcmp1_b32 s86, 13\n\t"
      "s_cselect_b32 s61, s62, s61\n\t"
      "s_cmp_eq_u32 s86, 0\n\t"
      "s_cselect_b32 s65, 0, s61\n\t"
      "v_readfirstlane_b32 s97, v121\n\t"
      "v_mov_b32 v65, v115\n\t"
      "v_mov_b32 v64, 0\n\t"
      "v_mov_b32 v66, 0\n\t"
      "v_mov_b32 v67, 0\n\t"
      "v_mov_b32 v109, 0\n\t"
      "v_mov_b32 v110, 0\n\t"
      "v_mov_b32 v111, 0\n\t"
      "s_mov_b32 s40, -1\n\t"
      "s_add_u32 s45, s55, 32768\n\t"
      "v_mov_b32 v126, 0\n\t"
      "s_mov_b32 s56, 0\n\t"
      "s_mov_b32 s58, 1\n\t"
      "s_mov_b32 s59, 0x3e0293ee\n\t"
      "s_mov_b32 s96, 0x40820000\n\t"
      "s_mov_b32 s57, 1.0\n\t"
      "s_add_u32 s68, s43, 8192\n\t"
      "s_add_u32 s97, s97, s43\n\t"
      "s_add_u32 s97, s97, 0x8000\n\t"
      "s_mov_b32 s99, 0\n\t"
      "s_mov_b32 s84, 3\n\t"
      "s_mov_b32 s85, 0x80400000\n\t"
      "s_mov_b32 s26, 2460\n\t"
      "s_mov_b32 s25, 0\n\t"
      "s_memtime s[78:79]\n\t"
      "s_waitcnt lgkmcnt(0)\n\t"
      "s_sub_u32 s78, s78, 4000\n\t"
      "s_mov_b32 s88, s78\n\t"
      "s_mov_b32 s27, s78\n\t"
      "s_mov_b32 s24, s26\n\t"
      "s_add_u32 s50, s50, 32\n\t"
      "s_mov_b32 s72, 0xcccccccc\n\t"
      "s_mov_b32 s73, 0xcccccccc\n\t"
      "s_waitcnt vmcnt(0) lgkmcnt(0)\n\t"
      "s_barrier\n\t"
      "s_mov_b32 m0, s45\n\t"
      "s_sub_u32 s61, 204800, s71\n\t"
      "buffer_load_dwordx4 v65, s[36:39], s44 offen sc1 lds\n\t"
      "buffer_load_dwordx4 v65, s[36:39], s44 offen offset:1024 sc1 lds\n\t"
      "s_add_u32 m0, s55, s61\n\t"
      "s_nop 0\n\t"
      "buffer_load_dwordx4 v127, s[36:39], s46 offen sc1 lds\n\t"
      "buffer_load_dwordx4 v127, s[36:39], s46 offen offset:1024 sc1 lds\n\t"
      "buffer_load_dwordx2 v[48:49], v52, s[36:39], s94 offen sc1\n\t"
      "buffer_load_dwordx2 v[50:51], v52, s[36:39], s94 offen offset:128 sc1\n\t"
      "s_mov_b32 s62, 0xffff0000\n\t"
      "v_accvgpr_read_b32 v97, a238\n\t"
      "v_accvgpr_read_b32 v99, a239\n\t"
      "v_lshl_or_b32 v96, v97, 16, 1\n\t"
      "v_and_or_b32 v97, v97, s62, 1\n\t"
      "v_lshl_or_b32 v98, v99, 16, 1\n\t"
      "v_and_or_b32 v99, v99, s62, 1\n\t"
      "ds_read_b64_tr_b16 a[168:169], v117 offset:16384\n\t"
      "ds_read_b64_tr_b16 a[170:171], v117 offset:17408\n\t"
      "ds_read_b64_tr_b16 a[232:233], v118 offset:16384\n\t"
      "ds_read_b64_tr_b16 a[234:235], v118 offset:17408\n\t"
      "ds_read_b64_tr_b16 a[172:173], v117 offset:24576\n\t"
      "ds_read_b64_tr_b16 a[174:175], v117 offset:25600\n\t"
      "ds_read_b64_tr_b16 a[236:237], v118 offset:24576\n\t"
      "ds_read_b64_tr_b16 a[238:239], v118 offset:25600\n\t"
      "ds_read_b64_tr_b16 a[176:177], v117 offset:32768\n\t"
      "ds_read_b64_tr_b16 a[178:179], v117 offset:33792\n\t"
      "ds_read_b64_tr_b16 a[240:241], v118 offset:32768\n\t"
      "ds_read_b64_tr_b16 a[242:243], v118 offset:33792\n\t"
      "ds_read_b64_tr_b16 a[180:181], v117 offset:40960\n\t"
      "ds_read_b64_tr_b16 a[182:183], v117 offset:41984\n\t"
      "ds_read_b64_tr_b16 a[244:245], v118 offset:40960\n\t"
      "s_waitcnt lgkmcnt(12)\n\t"
      "ds_read_b64_tr_b16 a[246:247], v118 offset:41984\n\t"
      "ds_read_b64_tr_b16 a[184:185], v117 offset:49152\n\t"
      "ds_read_b64_tr_b16 a[186:187], v117 offset:50176\n\t"
      "s_waitcnt lgkmcnt(12)\n\t"
      "ds_read_b64_tr_b16 a[248:249], v118 offset:49152\n\t"
      "ds_read_b64_tr_b16 a[250:251], v118 offset:50176\n\t"
      "ds_read_b64_tr_b16 a[188:189], v117 offset:57344\n\t"
      "s_waitcnt lgkmcnt(12)\n\t"
      "ds_read_b64_tr_b16 a[190:191], v117 offset:58368\n\t"
      "ds_read_b64_tr_b16 a[252:253], v118 offset:57344\n\t"
      "ds_read_b64_tr_b16 a[254:255], v118 offset:58368\n\t"
      "s_waitcnt lgkmcnt(6)\n\t"
      "s_cmp_eq_u32 s41, 1\n\t"
      "s_cbranch_scc1 LWIN_1%=\n\t"
      "s_cmp_eq_u32 s41, 2\n\t"
      "s_cbranch_scc1 LWIN_2%=\n\t"
      "s_cmp_eq_u32 s41, 3\n\t"
      "s_cbranch_scc1 LWIN_3%=\n\t"
      "s_branch LWIN_0%=\n\t"
      ".p2align 6\n\t.fill 16, 4, 0xbf800000\n\t"
      "LPOLL_0%=:\n\t"
      "buffer_load_dwordx4 v[84:87], v60, s[36:39], s43 offen sc1\n\t"
      "buffer_load_dwordx4 v[88:91], v60, s[36:39], s43 offen offset:1024 sc1\n\t"
      "s_waitcnt lgkmcnt(4)\n\t"
      "v_mfma_f32_16x16x32_bf16 v[32:35], v[0:3], v[8:11], v[32:35]\n\t"
      "v_mfma_f32_16x16x32_bf16 v[36:39], v[4:7], v[8:11], v[36:39]\n\t"
      "v_mfma_f32_16x16x32_bf16 v[40:43], v[0:3], v[12:15], v[40:43]\n\t"
      "v_mfma_f32_16x16x32_bf16 v[44:47], v[4:7], v[12:15], v[44:47]\n\t"
      "buffer_load_dwordx4 v[92:95], v60, s[36:39], s68 offen sc1\n\t"
      "buffer_load_dwordx4 v[96:99], v60, s[36:39], s68 offen offset:1024 sc1\n\t"
      "ds_read_b64 v[54:55], v124 offset:53312\n\t"
      "s_xor_b32 s69, s97, 1024\n\t"
      "s_xor_b32 s98, s97, 512\n\t"
      "s_xor_b32 s23, s97, 1536\n\t"
      "s_waitcnt lgkmcnt(1)\n\t"
      "v_mfma_f32_16x16x32_bf16 v[32:35], v[76:79], v[112:115], v[32:35]\n\t"
      "v_mfma_f32_16x16x32_bf16 v[36:39], v[80:83], v[112:115], v[36:39]\n\t"
      "v_mfma_f32_16x16x32_bf16 v[40:43], v[76:79], v[116:119], v[40:43]\n\t"
      "v_mfma_f32_16x16x32_bf16 v[44:47], v[80:83], v[116:119], v[44:47]\n\t"
      "s_waitcnt lgkmcnt(0)\n\t"
      "v_add_f32 v48, v48, v54\n\t"
      "v_add_f32_e64 v49, v49, v55\n\t"
      "v_cvt_pk_bf16_f32 v48, v48, v49\n\t"
      "buffer_store_dword v48, v62, s[36:39], s28 offen sc1\n\t"
      "s_mov_b64 exec, 1\n\t"
      "ds_write_b64 v109, v[110:111] offset:55392\n\t"
      "s_mov_b64 exec, -1\n\t"
      "s_nop 4\n\t"
      "v_cvt_pk_bf16_f32 v68, v32, v33\n\t"
      "v_cvt_pk_bf16_f32 v69, v34, v35\n\t"
      "v_cvt_pk_bf16_f32 v70, v36, v37\n\t"
      "v_cvt_pk_bf16_f32 v71, v38, v39\n\t"
      "v_cvt_pk_bf16_f32 v74, v40, v41\n\t"
      "v_cvt_pk_bf16_f32 v75, v42, v43\n\t"
      "v_cvt_pk_bf16_f32 v72, v44, v45\n\t"
      "v_cvt_pk_bf16_f32 v73, v46, v47\n\t"
      "v_mov_b64 v[32:33], 0\n\t"
      "v_mov_b64 v[34:35], 0\n\t"
      "v_mov_b64 v[36:37], 0\n\t"
      "v_mov_b64 v[38:39], 0\n\t"
      "v_mov_b64 v[40:41], 0\n\t"
      "v_mov_b64 v[42:43], 0\n\t"
      "v_mov_b64 v[44:45], 0\n\t"
      "v_mov_b64 v[46:47], 0\n\t"
      "v_mov_b64 v[48:49], 0\n\t"
      "v_mov_b64 v[50:51], 0\n\t"
      "v_mov_b64 v[52:53], 0\n\t"
      "v_mov_b64 v[54:55], 0\n\t"
      "LGO_0%=:\n\t"
      "s_nop 0\n\t"
      "s_nop 0\n\t"
      "s_nop 0\n\t"
      "s_nop 0\n\t"
      "s_waitcnt vmcnt(4)\n\t"
      "v_pk_fma_f32 v[6:7], v[86:87], s[56:57], v[126:127] op_sel:[0,1,0] op_sel_hi:[1,1,0]\n\t"
      "v_pk_fma_f32 v[4:5], v[84:85], s[56:57], v[126:127] op_sel:[0,1,0] op_sel_hi:[1,1,0]\n\t"
      "v_cvt_pk_bf16_f32 v0, v4, v5\n\t"
      "v_cvt_pk_bf16_f32 v1, v6, v7\n\t"
      "s_nop 0\n\t"
      "ds_write_b64 v127, v[0:1] offset:40960\n\t"
      "s_waitcnt vmcnt(3)\n\t"
      "v_pk_fma_f32 v[8:9], v[88:89], s[56:57], v[126:127] op_sel:[0,1,0] op_sel_hi:[1,1,0]\n\t"
      "v_pk_fma_f32 v[10:11], v[90:91], s[56:57], v[126:127] op_sel:[0,1,0] op_sel_hi:[1,1,0]\n\t"
      "v_cvt_pk_bf16_f32 v2, v8, v9\n\t"
      "v_cvt_pk_bf16_f32 v3, v10, v11\n\t"
      "ds_write_b64 v127, v[2:3] offset:40968\n\t"
      "v_bitop3_b32 v64, v84, v85, v86 bitop3:0x80\n\t"
      "v_mfma_f32_16x16x32_bf16 v[56:59], v[0:3], v[0:3], 0\n\t"
      "v_bitop3_b32 v64, v64, v87, v88 bitop3:0x80\n\t"
      "v_bitop3_b32 v64, v64, v89, v90 bitop3:0x80\n\t"
      "v_bitop3_b32 v64, v64, v91, s58 bitop3:0x80\n\t"
      "v_cmp_eq_u32_e64 vcc, 0, v64\n\t"
      "v_smfmac_f32_16x16x64_bf16 v[36:39], v[0:3], a[64:71], v122\n\t"
      "s_nop 0\n\t"
      "v_bitop3_b32 v120, v120, v121, s53 bitop3:0x7e\n\t"
      "v_smfmac_f32_16x16x64_bf16 v[44:47], v[0:3], a[192:199], v122\n\t"
      "s_cbranch_vccnz LR0_0%=\n\t"
      "LC0_0%=:\n\t"
      "s_waitcnt lgkmcnt(0)\n\t"
      "s_barrier\n\t"
      "ds_read_b128 v[4:7], v127 offset:41984\n\t"
      "v_smfmac_f32_16x16x64_bf16 v[52:55], v[0:3], v[192:199], v122\n\t"
      "ds_read_b128 v[8:11], v127 offset:43008\n\t"
      "v_smfmac_f32_16x16x64_bf16 v[48:51], v[0:3], v[128:135], v122\n\t"
      "ds_read_b128 v[12:15], v127 offset:44032\n\t"
      "v_smfmac_f32_16x16x64_bf16 v[32:35], v[0:3], a[0:7], v122\n\t"
      "ds_read_b128 v[84:87], v127 offset:36864\n\t"
      "v_smfmac_f32_16x16x64_bf16 v[40:43], v[0:3], a[128:135], v122\n\t"
      "s_waitcnt lgkmcnt(3)\n\t"
      "v_smfmac_f32_16x16x64_bf16 v[32:35], v[4:7], a[8:15], v122\n\t"
      "ds_read_b128 v[80:83], v127 offset:35840\n\t"
      "v_smfmac_f32_16x16x64_bf16 v[40:43], v[4:7], a[136:143], v122\n\t"
      "s_waitcnt vmcnt(2)\n\t"
      "v_fma_f32 v23, v95, s57, v126\n\t"
      "v_fma_f32 v20, v92, s57, v126\n\t"
      "v_smfmac_f32_16x16x64_bf16 v[36:39], v[4:7], a[72:79], v122\n\t"
      "v_fma_f32 v22, v94, s57, v126\n\t"
      "v_cvt_pk_bf16_f32 v17, v22, v23\n\t"
      "v_bitop3_b32 v64, v92, v93, v94 bitop3:0x80\n\t"
      "v_smfmac_f32_16x16x64_bf16 v[44:47], v[4:7], a[200:207], v122\n\t"
      "s_waitcnt lgkmcnt(2)\n\t"
      "v_fma_f32 v21, v93, s57, v126\n\t"
      "v_cvt_pk_bf16_f32 v16, v20, v21\n\t"
      "v_smfmac_f32_16x16x64_bf16 v[36:39], v[8:11], a[80:87], v122\n\t"
      "s_waitcnt vmcnt(1)\n\t"
      "ds_write_b64 v127, v[16:17] offset:45056\n\t"
      "v_fma_f32 v27, v99, s57, v126\n\t"
      "v_fma_f32 v26, v98, s57, v126\n\t"
      "v_cvt_pk_bf16_f32 v19, v26, v27\n\t"
      "v_fma_f32 v25, v97, s57, v126\n\t"
      "v_fma_f32 v24, v96, s57, v126\n\t"
      "v_cvt_pk_bf16_f32 v18, v24, v25\n\t"
      "v_smfmac_f32_16x16x64_bf16 v[44:47], v[8:11], a[208:215], v122\n\t"
      "ds_write_b64 v127, v[18:19] offset:45064\n\t"
      "v_smfmac_f32_16x16x64_bf16 v[40:43], v[8:11], a[144:151], v122\n\t"
      "ds_read_b128 v[88:91], v127 offset:39936\n\t"
      "v_bitop3_b32 v64, v64, v95, v96 bitop3:0x80\n\t"
      "v_bitop3_b32 v64, v64, v97, v98 bitop3:0x80\n\t"
      "v_smfmac_f32_16x16x64_bf16 v[40:43], v[12:15], a[152:159], v122\n\t"
      "v_bitop3_b32 v64, v64, v99, s60 bitop3:0x80\n\t"
      "v_smfmac_f32_16x16x64_bf16 v[36:39], v[12:15], a[88:95], v122\n\t"
      "v_cmp_eq_u32_e64 vcc, 0, v64\n\t"
      "v_smfmac_f32_16x16x64_bf16 v[44:47], v[12:15], a[216:223], v122\n\t"
      "s_waitcnt lgkmcnt(1)\n\t"
      "v_smfmac_f32_16x16x64_bf16 v[32:35], v[8:11], a[16:23], v122\n\t"
      "s_cbranch_vccnz LR1_0%=\n\t"
      "LC1_0%=:\n\t"
      "v_smfmac_f32_16x16x64_bf16 v[40:43], v[16:19], a[160:167], v122\n\t"
      "s_barrier\n\t"
      "v_mfma_f32_16x16x32_bf16 v[56:59], v[16:19], v[16:19], v[56:59]\n\t"
      "ds_read_b128 v[20:23], v127 offset:46080\n\t"
      "s_nop 0\n\t"
      "v_smfmac_f32_16x16x64_bf16 v[32:35], v[12:15], a[24:31], v122\n\t"
      "ds_read_b128 v[24:27], v127 offset:47104\n\t"
      "v_smfmac_f32_16x16x64_bf16 v[44:47], v[16:19], a[224:231], v122\n\t"
      "ds_read_b128 v[28:31], v127 offset:48128\n\t"
      "v_max3_f32 v56, v56, v57, v58\n\t"
      "v_smfmac_f32_16x16x64_bf16 v[36:39], v[16:19], a[96:103], v122\n\t"
      "ds_read_b128 v[76:79], v127 offset:32768\n\t"
      "v_max_f32_e64 v56, v56, v59\n\t"
      "v_smfmac_f32_16x16x64_bf16 v[32:35], v[16:19], a[32:39], v122\n\t"
      "s_waitcnt lgkmcnt(3)\n\t"
      "v_readfirstlane_b32 s56, v120\n\t"
      "v_smfmac_f32_16x16x64_bf16 v[32:35], v[20:23], a[40:47], v122\n\t"
      "s_waitcnt lgkmcnt(2)\n\t"
      "v_max_f32_dpp v56, v56, v56 quad_perm:[1,0,3,2] row_mask:0xf bank_mask:0xf\n\t"
      "v_smfmac_f32_16x16x64_bf16 v[32:35], v[24:27], a[48:55], v122\n\t"
      "s_waitcnt lgkmcnt(1)\n\t"
      "v_max_f32_dpp v56, v56, v56 quad_perm:[2,3,0,1] row_mask:0xf bank_mask:0xf\n\t"
      "v_smfmac_f32_16x16x64_bf16 v[32:35], v[28:31], a[56:63], v122\n\t"
      "s_mov_b32 exec_lo, 0x100001\n\t"
      "s_mov_b32 exec_hi, 0x10000100\n\t"
      "v_smfmac_f32_16x16x64_bf16 v[36:39], v[20:23], a[104:111], v122\n\t"
      "ds_max_i32 v109, v56 offset:55392\n\t"
      "v_smfmac_f32_16x16x64_bf16 v[40:43], v[20:23], a[168:175], v122\n\t"
      "s_mov_b64 exec, -1\n\t"
      "v_add_f32 v33, v33, v35\n\t"
      "v_smfmac_f32_16x16x64_bf16 v[36:39], v[24:27], a[112:119], v122\n\t"
      "ds_read_b128 v[60:63], v127 offset:37888\n\t"
      "v_mul_f32_e64 v33, s59, v33\n\t"
      "v_smfmac_f32_16x16x64_bf16 v[36:39], v[28:31], a[120:127], v122\n\t"
      "v_add_f32_e64 v32, v32, v34\n\t"
      "v_smfmac_f32_16x16x64_bf16 v[44:47], v[20:23], a[232:239], v122\n\t"
      "ds_read_b128 v[56:59], v127 offset:38912\n\t"
      "v_mul_f32_e64 v32, s59, v32\n\t"
      "v_smfmac_f32_16x16x64_bf16 v[44:47], v[24:27], a[240:247], v122\n\t"
      "v_add_f32 v36, v36, v38\n\t"
      "v_add_f32 v37, v37, v39\n\t"
      "v_mul_f32_e64 v37, s59, v37\n\t"
      "v_smfmac_f32_16x16x64_bf16 v[40:43], v[24:27], a[176:183], v122\n\t"
      "ds_read_b128 v[92:95], v127 offset:34816\n\t"
      "v_cvt_pk_bf16_f32 v112, v32, v33\n\t"
      "v_smfmac_f32_16x16x64_bf16 v[44:47], v[28:31], a[248:255], v122\n\t"
      "ds_read_b128 v[96:99], v127 offset:33792\n\t"
      "v_mul_f32_e64 v36, s59, v36\n\t"
      "v_cvt_pk_bf16_f32 v113, v36, v37\n\t"
      "v_smfmac_f32_16x16x64_bf16 v[40:43], v[28:31], a[184:191], v122\n\t"
      "ds_write_b64 v125, v[112:113]\n\t"
      "v_smfmac_f32_16x16x64_bf16 v[48:51], v[4:7], v[136:143], v122\n\t"
      "s_nop 0\n\t"
      "v_add_f32 v45, v45, v47\n\t"
      "v_add_f32_e64 v44, v44, v46\n\t"
      "v_cvt_pk_bf16_f32 v121, v44, v45\n\t"
      "v_add_f32 v41, v41, v43\n\t"
      "v_add_f32 v40, v40, v42\n\t"
      "v_cvt_pk_bf16_f32 v120, v40, v41\n\t"
      "v_smfmac_f32_16x16x64_bf16 v[48:51], v[8:11], v[144:151], v122\n\t"
      "s_mov_b32 s58, 0x10000000\n\t"
      "ds_write_b64 v125, v[120:121] offset:2080\n\t"
      "v_smfmac_f32_16x16x64_bf16 v[48:51], v[12:15], v[152:159], v122\n\t"
      "s_waitcnt lgkmcnt(1)\n\t"
      "s_barrier\n\t"
      "ds_read_b64_tr_b16 v[36:37], v124 offset:50192\n\t"
      "v_smfmac_f32_16x16x64_bf16 v[48:51], v[16:19], v[160:167], v122\n\t"
      "ds_read_b32 v108, v109 offset:55392\n\t"
      "ds_read_b64_tr_b16 v[32:33], v124 offset:49152\n\t"
      "v_smfmac_f32_16x16x64_bf16 v[52:55], v[4:7], v[200:207], v122\n\t"
      "ds_read_b64_tr_b16 v[34:35], v124 offset:49672\n\t"
      "s_waitcnt vmcnt(0) lgkmcnt(4)\n\t"
      "ds_read_b64_tr_b16 v[38:39], v124 offset:50712\n\t"
      "v_smfmac_f32_16x16x64_bf16 v[48:51], v[20:23], v[168:175], v122\n\t"
      "s_barrier\n\t"
      "ds_read_b64_tr_b16 v[46:47], v124 offset:52792\n\t"
      "v_smfmac_f32_16x16x64_bf16 v[48:51], v[24:27], v[176:183], v122\n\t"
      "s_waitcnt lgkmcnt(4)\n\t"
      "ds_read_b64_tr_b16 v[44:45], v124 offset:52272\n\t"
      "v_readfirstlane_b32 s61, v108\n\t"
      "v_smfmac_f32_16x16x64_bf16 v[48:51], v[28:31], v[184:191], v122\n\t"
      "s_waitcnt lgkmcnt(3)\n\t"
      "ds_read_b64_tr_b16 v[42:43], v124 offset:51752\n\t"
      "ds_read_b64_tr_b16 v[40:41], v124 offset:51232\n\t"
      "s_waitcnt lgkmcnt(4)\n\t"
      "v_mfma_f32_16x16x32_bf16 v[112:115], v[76:79], v[32:35], 0\n\t"
      "s_lshr_b32 s61, s61, 23\n\t"
      "s_add_u32 s61, s61, s67\n\t"
      "v_mov_b32_dpp v102, v34 quad_perm:[2,3,0,1] row_mask:0xf bank_mask:0xf\n\t"
      "v_mov_b32_dpp v106, v38 quad_perm:[2,3,0,1] row_mask:0xf bank_mask:0xf\n\t"
      "s_max_u32 s61, s61, s66\n\t"
      "v_mfma_f32_16x16x32_bf16 v[116:119], v[80:83], v[32:35], 0\n\t"
      "s_lshr_b32 s61, s61, 1\n\t"
      "v_add_f32_e64 v48, v48, v50\n\t"
      "v_mov_b32_dpp v100, v32 quad_perm:[2,3,0,1] row_mask:0xf bank_mask:0xf\n\t"
      "v_mfma_f32_16x16x32_bf16 v[116:119], v[88:91], v[36:39], v[116:119]\n\t"
      "s_min_u32 s61, s61, 200\n\t"
      "v_mov_b32_dpp v107, v39 quad_perm:[2,3,0,1] row_mask:0xf bank_mask:0xf\n\t"
      "s_waitcnt lgkmcnt(0)\n\t"
      "v_mfma_f32_16x16x32_bf16 v[76:79], v[40:43], v[32:35], 0\n\t"
      "buffer_load_dwordx4 v127, s[36:39], s46 offen sc1 lds\n\t"
      "s_lshl_b32 s61, s61, 23\n\t"
      "v_mfma_f32_16x16x32_bf16 v[76:79], v[44:47], v[36:39], v[76:79]\n\t"
      "s_add_u32 s65, s61, 33554432\n\t"
      "v_mov_b32_dpp v104, v36 quad_perm:[2,3,0,1] row_mask:0xf bank_mask:0xf\n\t"
      "v_mov_b32_dpp v103, v35 quad_perm:[2,3,0,1] row_mask:0xf bank_mask:0xf\n\t"
      "v_mov_b32_dpp v101, v33 quad_perm:[2,3,0,1] row_mask:0xf bank_mask:0xf\n\t"
      "v_mfma_f32_16x16x32_bf16 v[112:115], v[84:87], v[36:39], v[112:115]\n\t"
      "s_pack_hh_b32_b16 s75, s65, s65\n\t"
      "v_add_f32 v49, v49, v51\n\t"
      "v_cvt_pk_bf16_f32 v64, v48, v49\n\t"
      "v_cndmask_b32_e64 v120, v76, v78, s[72:73]\n\t"
      "v_cndmask_b32_e64 v121, v77, v79, s[72:73]\n\t"
      "v_mov_b32_dpp v105, v37 quad_perm:[2,3,0,1] row_mask:0xf bank_mask:0xf\n\t"
      "v_mfma_f32_16x16x32_bf16 v[112:115], v[92:95], v[100:103], v[112:115]\n\t"
      "buffer_load_dwordx4 v127, s[36:39], s46 offen offset:1024 sc1 lds\n\t"
      "v_mfma_f32_16x16x32_bf16 v[116:119], v[96:99], v[100:103], v[116:119]\n\t"
      "s_pack_hh_b32_b16 s74, s65, s65\n\t"
      "v_add_f32_dpp v121, v121, v121 quad_perm:[2,3,0,1] row_mask:0xf bank_mask:0xf\n\t"
      "v_add_f32_dpp v120, v120, v120 quad_perm:[2,3,0,1] row_mask:0xf bank_mask:0xf\n\t"
      "v_mfma_f32_16x16x32_bf16 v[112:115], v[56:59], v[104:107], v[112:115]\n\t"
      "v_exp_f32 v81, v121\n\t"
      "v_mfma_f32_16x16x32_bf16 v[116:119], v[60:63], v[104:107], v[116:119]\n\t"
      "s_mov_b32 m0, s45\n\t"
      "v_mov_b64 v[90:91], s[74:75]\n\t"
      "v_exp_f32 v80, v120\n\t"
      "v_mov_b64 v[88:89], s[74:75]\n\t"
      "v_cvt_pk_bf16_f32 v108, v80, v81\n\t"
      "v_smfmac_f32_16x16x64_bf16 v[52:55], v[8:11], v[208:215], v122\n\t"
      "buffer_load_dwordx4 v65, s[36:39], s44 offen offset:1024 sc1 lds\n\t"
      "v_mfma_f32_16x16x32_bf16 v[84:87], v[108:111], v[64:67], 0\n\t"
      "s_add_u32 s57, s61, 310378496\n\t"
      "v_cndmask_b32_e64 v112, v112, v116, s[72:73]\n\t"
      "v_exp_f32_e64 v56, v112\n\t"
      "v_mfma_f32_16x16x32_bf16 v[48:51], v[108:111], v[88:91], 0\n\t"
      "s_add_u32 s43, s43, 0x8000\n\t"
      "v_cndmask_b32_e64 v113, v113, v117, s[72:73]\n\t"
      "v_cndmask_b32_e64 v114, v114, v118, s[72:73]\n\t"
      "v_exp_f32_e64 v58, v114\n\t"
      "v_cndmask_b32_e64 v115, v115, v119, s[72:73]\n\t"
      "v_exp_f32 v57, v113\n\t"
      "v_exp_f32 v59, v115\n\t"
      "v_cvt_pk_bf16_f32 v96, v56, v57\n\t"
      "v_cvt_pk_bf16_f32 v97, v58, v59\n\t"
      "v_max3_f32 v82, |v112|, |v113|, |v114|\n\t"
      "v_mov_b32_dpp v98, v96 quad_perm:[2,3,0,1] row_mask:0xf bank_mask:0xf\n\t"
      "v_mov_b32_dpp v99, v97 quad_perm:[2,3,0,1] row_mask:0xf bank_mask:0xf\n\t"
      "v_smfmac_f32_16x16x64_bf16 v[52:55], v[12:15], v[216:223], v122\n\t"
      "v_max3_f32 v83, |v115|, |v120|, |v121|\n\t"
      "v_mfma_f32_16x16x32_bf16 v[48:51], v[96:99], v[88:91], v[48:51]\n\t"
      "v_max_f32_e64 v82, v82, v83\n\t"
      "v_cmp_nge_f32 vcc, 0x42000000, v82\n\t"
      "v_mfma_f32_16x16x32_bf16 v[84:87], v[96:99], v[68:71], v[84:87]\n\t"
      "buffer_load_dwordx4 v65, s[36:39], s44 offen sc1 lds\n\t"
      "v_smfmac_f32_16x16x64_bf16 v[52:55], v[16:19], v[224:231], v122\n\t"
      "s_add_u32 s68, s68, 0x8000\n\t"
      "s_cbranch_vccnz LMX_0%=\n\t"
      "v_rcp_f32 v49, v49\n\t"
      "v_rcp_f32_e64 v48, v48\n\t"
      "v_smfmac_f32_16x16x64_bf16 v[52:55], v[20:23], v[232:239], v122\n\t"
      "v_fma_f32 v85, v85, v49, s96\n\t"
      "v_fma_f32 v84, v84, v48, s96\n\t"
      "v_smfmac_f32_16x16x64_bf16 v[52:55], v[24:27], v[240:247], v122\n\t"
      "buffer_atomic_add v84, v123, s[36:39], s97 offen\n\t"
      "buffer_atomic_add v85, v123, s[36:39], s98 offen\n\t"
      "v_smfmac_f32_16x16x64_bf16 v[52:55], v[28:31], v[248:255], v122\n\t"
      "v_mfma_f32_16x16x32_bf16 v[92:95], v[96:99], v[72:75], 0\n\t"
      "s_nop 3\n\t"
      "v_add_f32 v53, v53, v55\n\t"
      "v_add_f32_e64 v52, v52, v54\n\t"
      "v_cvt_pk_bf16_f32 v66, v52, v53\n\t"
      "s_add_u32 s62, s61, 0x83400000\n\t"
      "v_mov_b32_e64 v126, s62\n\t"
      "v_mfma_f32_16x16x32_bf16 v[92:95], v[108:111], v[66:69], v[92:95]\n\t"
      "s_add_u32 s97, s97, 0x8000\n\t"
      "s_nop 6\n\t"
      "v_fma_f32 v95, v95, v49, s96\n\t"
      "buffer_atomic_add v95, v123, s[36:39], s23 offen\n\t"
      "v_fma_f32 v94, v94, v48, s96\n\t"
      "buffer_atomic_add v94, v123, s[36:39], s69 offen\n\t"
      "LWIN_0%=:\n\t"
      "v_add_u32 v100, s55, v127\n\t"
      "ds_read_b128 v[60:63], v100 offset:57344\n\t"
      "s_cmp_eq_u32 s56, 0\n\t"
      "s_cbranch_scc0 LSLOW_0%=\n\t"
      "LWIN1_0%=:\n\t"
      "buffer_load_dwordx2 v[120:121], off, s[36:39], s50 sc1\n\t"
      "s_mov_b32 s64, s50\n\t"
      "s_bitcmp1_b64 s[84:85], s40\n\t"
      "s_cbranch_scc1 LEARLY_0%=\n\t"
      "LROLE_0%=:\n\t"
      "v_add_u32 v52, 65536, v127\n\t"
      "ds_read_b128 v[88:91], v52\n\t"
      "ds_read_b128 v[92:95], v52 offset:1024\n\t"
      "ds_read_b128 v[32:35], v52 offset:4096\n\t"
      "ds_read_b128 v[36:39], v52 offset:5120\n\t"
      "ds_read_b128 v[40:43], v52 offset:8192\n\t"
      "ds_read_b128 v[44:47], v52 offset:9216\n\t"
      "v_mov_b64 v[48:49], 0\n\t"
      "v_mov_b64 v[50:51], 0\n\t"
      "v_add_u32 v104, s71, v100\n\t"
      "ds_read_b128 v[76:79], v104\n\t"
      "ds_read_b128 v[80:83], v104 offset:1024\n\t"
      "ds_read_b128 v[112:115], v100\n\t"
      "ds_read_b128 v[116:119], v100 offset:1024\n\t"
      "v_xor_b32 v101, 2048, v104\n\t"
      "v_xor_b32 v102, 4096, v104\n\t"
      "v_xor_b32 v103, 6144, v104\n\t"
      "s_waitcnt lgkmcnt(8)\n\t"
      "v_smfmac_f32_16x16x64_bf16 v[48:51], v[0:3], v[88:95], v122\n\t"
      "ds_read_b128 v[88:91], v52 offset:12288\n\t"
      "ds_read_b128 v[92:95], v52 offset:13312\n\t"
      "s_add_u32 m0, s55, s71\n\t"
      "s_sub_u32 s71, 204800, s71\n\t"
      "s_waitcnt lgkmcnt(8)\n\t"
      "v_smfmac_f32_16x16x64_bf16 v[48:51], v[4:7], v[32:39], v122\n\t"
      "ds_read_b128 v[16:19], v101\n\t"
      "ds_read_b128 v[20:23], v101 offset:1024\n\t"
      "s_add_u32 s40, s40, 1\n\t"
      "s_cmp_lt_u32 s40, 63\n\t"
      "s_waitcnt lgkmcnt(8)\n\t"
      "v_smfmac_f32_16x16x64_bf16 v[48:51], v[8:11], v[40:47], v122\n\t"
      "ds_read_b128 v[24:27], v100 offset:8192\n\t"
      "ds_read_b128 v[28:31], v100 offset:9216\n\t"
      "s_cselect_b32 s61, 786432, 0\n\t"
      "s_cselect_b32 s60, s58, 0\n\t"
      "s_waitcnt lgkmcnt(4)\n\t"
      "v_smfmac_f32_16x16x64_bf16 v[48:51], v[12:15], v[88:95], v122\n\t"
      "s_add_u32 s44, s44, s61\n\t"
      "s_cmp_lt_u32 s40, 62\n\t"
      "ds_read_b128 v[0:3], v102\n\t"
      "ds_read_b128 v[4:7], v102 offset:1024\n\t"
      "ds_read_b128 v[8:11], v100 offset:16384\n\t"
      "ds_read_b128 v[12:15], v100 offset:17408\n\t"
      "s_cselect_b32 s61, 786432, 0\n\t"
      "s_add_u32 s46, s46, s61\n\t"
      "s_add_u32 s61, s40, 3\n\t"
      "s_and_b32 s62, s61, 3\n\t"
      "s_cselect_b32 s62, 16, 2000\n\t"
      "s_cmp_lt_u32 s61, 64\n\t"
      "s_cselect_b32 s62, s62, 0\n\t"
      "s_add_u32 s50, s50, s62\n\t"
      "s_mov_b32 s63, 0x2000\n\t"
      "s_mov_b32 s28, s49\n\t"
      "s_add_u32 s49, s49, 65536\n\t"
      "s_cmp_eq_u32 s40, 64\n\t"
      "v_add_f32 v48, v48, v50\n\t"
      "v_add_f32 v49, v49, v51\n\t"
      "s_cbranch_scc1 LFIN_0%=\n\t"
      "v_mfma_f32_16x16x32_bf16 v[32:35], v[76:79], v[112:115], 0\n\t"
      "v_mfma_f32_16x16x32_bf16 v[36:39], v[80:83], v[112:115], 0\n\t"
      "v_mfma_f32_16x16x32_bf16 v[40:43], v[76:79], v[116:119], 0\n\t"
      "v_mfma_f32_16x16x32_bf16 v[44:47], v[80:83], v[116:119], 0\n\t"
      "ds_read_b128 v[76:79], v103\n\t"
      "ds_read_b128 v[80:83], v103 offset:1024\n\t"
      "ds_read_b128 v[112:115], v100 offset:24576\n\t"
      "ds_read_b128 v[116:119], v100 offset:25600\n\t"
      "s_waitcnt lgkmcnt(8)\n\t"
      "v_mfma_f32_16x16x32_bf16 v[32:35], v[16:19], v[24:27], v[32:35]\n\t"
      "v_mfma_f32_16x16x32_bf16 v[36:39], v[20:23], v[24:27], v[36:39]\n\t"
      "v_mfma_f32_16x16x32_bf16 v[40:43], v[16:19], v[28:31], v[40:43]\n\t"
      "v_mfma_f32_16x16x32_bf16 v[44:47], v[20:23], v[28:31], v[44:47]\n\t"
      "LGATE_0%=:\n\t"
      "LBAR_0%=:\n\t"
      "s_cmp_gt_u32 s40, 1\n\t"
      "s_barrier\n\t"
      "s_cbranch_scc1 LPOLL_0%=\n\t"
      "s_branch LFIRST_0%=\n\t"
      ".p2align 6\n\t.fill 16, 4, 0xbf800000\n\t"
      "LPOLL_1%=:\n\t"
      "buffer_load_dwordx4 v[84:87], v60, s[36:39], s43 offen sc1\n\t"
      "buffer_load_dwordx4 v[88:91], v60, s[36:39], s43 offen offset:1024 sc1\n\t"
      "s_waitcnt lgkmcnt(4)\n\t"
      "v_mfma_f32_16x16x32_bf16 v[32:35], v[0:3], v[8:11], v[32:35]\n\t"
      "v_mfma_f32_16x16x32_bf16 v[36:39], v[4:7], v[8:11], v[36:39]\n\t"
      "v_mfma_f32_16x16x32_bf16 v[40:43], v[0:3], v[12:15], v[40:43]\n\t"
      "v_mfma_f32_16x16x32_bf16 v[44:47], v[4:7], v[12:15], v[44:47]\n\t"
      "buffer_load_dwordx4 v[92:95], v60, s[36:39], s68 offen sc1\n\t"
      "buffer_load_dwordx4 v[96:99], v60, s[36:39], s68 offen offset:1024 sc1\n\t"
      "ds_read_b64 v[54:55], v124 offset:53832\n\t"
      "s_xor_b32 s69, s97, 1024\n\t"
      "s_xor_b32 s98, s97, 512\n\t"
      "s_xor_b32 s23, s97, 1536\n\t"
      "s_waitcnt lgkmcnt(1)\n\t"
      "v_mfma_f32_16x16x32_bf16 v[32:35], v[76:79], v[112:115], v[32:35]\n\t"
      "v_mfma_f32_16x16x32_bf16 v[36:39], v[80:83], v[112:115], v[36:39]\n\t"
      "v_mfma_f32_16x16x32_bf16 v[40:43], v[76:79], v[116:119], v[40:43]\n\t"
      "v_mfma_f32_16x16x32_bf16 v[44:47], v[80:83], v[116:119], v[44:47]\n\t"
      "s_waitcnt lgkmcnt(0)\n\t"
      "v_add_f32 v48, v48, v54\n\t"
      "v_add_f32_e64 v49, v49, v55\n\t"
      "v_cvt_pk_bf16_f32 v48, v48, v49\n\t"
      "buffer_store_dword v48, v62, s[36:39], s28 offen sc1\n\t"
      "s_mov_b64 exec, 1\n\t"
      "ds_write_b64 v109, v[110:111] offset:55392\n\t"
      "s_mov_b64 exec, -1\n\t"
      "s_nop 4\n\t"
      "v_cvt_pk_bf16_f32 v68, v32, v33\n\t"
      "v_cvt_pk_bf16_f32 v69, v34, v35\n\t"
      "v_cvt_pk_bf16_f32 v70, v36, v37\n\t"
      "v_cvt_pk_bf16_f32 v71, v38, v39\n\t"
      "v_cvt_pk_bf16_f32 v74, v40, v41\n\t"
      "v_cvt_pk_bf16_f32 v75, v42, v43\n\t"
      "v_cvt_pk_bf16_f32 v72, v44, v45\n\t"
      "v_cvt_pk_bf16_f32 v73, v46, v47\n\t"
      "v_mov_b64 v[32:33], 0\n\t"
      "v_mov_b64 v[34:35], 0\n\t"
      "v_mov_b64 v[36:37], 0\n\t"
      "v_mov_b64 v[38:39], 0\n\t"
      "v_mov_b64 v[40:41], 0\n\t"
      "v_mov_b64 v[42:43], 0\n\t"
      "v_mov_b64 v[44:45], 0\n\t"
      "v_mov_b64 v[46:47], 0\n\t"
      "v_mov_b64 v[48:49], 0\n\t"
      "v_mov_b64 v[50:51], 0\n\t"
      "v_mov_b64 v[52:53], 0\n\t"
      "v_mov_b64 v[54:55], 0\n\t"
      "LGO_1%=:\n\t"
      "s_nop 0\n\t"
      "s_nop 0\n\t"
      "s_nop 0\n\t"
      "s_nop 0\n\t"
      "s_waitcnt vmcnt(4)\n\t"
      "v_pk_fma_f32 v[2:3], v[86:87], s[56:57], v[126:127] op_sel:[0,1,0] op_sel_hi:[1,1,0]\n\t"
      "v_pk_fma_f32 v[0:1], v[84:85], s[56:57], v[126:127] op_sel:[0,1,0] op_sel_hi:[1,1,0]\n\t"
      "v_cvt_pk_bf16_f32 v4, v0, v1\n\t"
      "v_cvt_pk_bf16_f32 v5, v2, v3\n\t"
      "s_nop 0\n\t"
      "ds_write_b64 v127, v[4:5] offset:41984\n\t"
      "s_waitcnt vmcnt(3)\n\t"
      "v_pk_fma_f32 v[12:13], v[88:89], s[56:57], v[126:127] op_sel:[0,1,0] op_sel_hi:[1,1,0]\n\t"
      "v_pk_fma_f32 v[14:15], v[90:91], s[56:57], v[126:127] op_sel:[0,1,0] op_sel_hi:[1,1,0]\n\t"
      "v_cvt_pk_bf16_f32 v6, v12, v13\n\t"
      "v_cvt_pk_bf16_f32 v7, v14, v15\n\t"
      "ds_write_b64 v127, v[6:7] offset:41992\n\t"
      "v_bitop3_b32 v64, v84, v85, v86 bitop3:0x80\n\t"
      "v_mfma_f32_16x16x32_bf16 v[56:59], v[4:7], v[4:7], 0\n\t"
      "v_bitop3_b32 v64, v64, v87, v88 bitop3:0x80\n\t"
      "v_bitop3_b32 v64, v64, v89, v90 bitop3:0x80\n\t"
      "v_bitop3_b32 v64, v64, v91, s58 bitop3:0x80\n\t"
      "v_cmp_eq_u32_e64 vcc, 0, v64\n\t"
      "v_smfmac_f32_16x16x64_bf16 v[36:39], v[4:7], a[72:79], v122\n\t"
      "s_nop 0\n\t"
      "v_bitop3_b32 v120, v120, v121, s53 bitop3:0x7e\n\t"
      "v_smfmac_f32_16x16x64_bf16 v[44:47], v[4:7], a[200:207], v122\n\t"
      "s_cbranch_vccnz LR0_1%=\n\t"
      "LC0_1%=:\n\t"
      "s_waitcnt lgkmcnt(0)\n\t"
      "s_barrier\n\t"
      "ds_read_b128 v[0:3], v127 offset:40960\n\t"
      "v_smfmac_f32_16x16x64_bf16 v[52:55], v[4:7], v[200:207], v122\n\t"
      "ds_read_b128 v[12:15], v127 offset:44032\n\t"
      "v_smfmac_f32_16x16x64_bf16 v[48:51], v[4:7], v[136:143], v122\n\t"
      "ds_read_b128 v[8:11], v127 offset:43008\n\t"
      "v_smfmac_f32_16x16x64_bf16 v[32:35], v[4:7], a[8:15], v122\n\t"
      "ds_read_b128 v[84:87], v127 offset:36864\n\t"
      "v_smfmac_f32_16x16x64_bf16 v[40:43], v[4:7], a[136:143], v122\n\t"
      "s_waitcnt lgkmcnt(3)\n\t"
      "v_smfmac_f32_16x16x64_bf16 v[32:35], v[0:3], a[0:7], v122\n\t"
      "ds_read_b128 v[80:83], v127 offset:35840\n\t"
      "v_smfmac_f32_16x16x64_bf16 v[40:43], v[0:3], a[128:135], v122\n\t"
      "s_waitcnt vmcnt(2)\n\t"
      "v_fma_f32 v19, v95, s57, v126\n\t"
      "v_fma_f32 v16, v92, s57, v126\n\t"
      "v_smfmac_f32_16x16x64_bf16 v[36:39], v[0:3], a[64:71], v122\n\t"
      "v_fma_f32 v18, v94, s57, v126\n\t"
      "v_cvt_pk_bf16_f32 v21, v18, v19\n\t"
      "v_bitop3_b32 v64, v92, v93, v94 bitop3:0x80\n\t"
      "v_smfmac_f32_16x16x64_bf16 v[44:47], v[0:3], a[192:199], v122\n\t"
      "s_waitcnt lgkmcnt(2)\n\t"
      "v_fma_f32 v17, v93, s57, v126\n\t"
      "v_cvt_pk_bf16_f32 v20, v16, v17\n\t"
      "v_smfmac_f32_16x16x64_bf16 v[36:39], v[12:15], a[88:95], v122\n\t"
      "s_waitcnt vmcnt(1)\n\t"
      "ds_write_b64 v127, v[20:21] offset:46080\n\t"
      "v_fma_f32 v31, v99, s57, v126\n\t"
      "v_fma_f32 v30, v98, s57, v126\n\t"
      "v_cvt_pk_bf16_f32 v23, v30, v31\n\t"
      "v_fma_f32 v29, v97, s57, v126\n\t"
      "v_fma_f32 v28, v96, s57, v126\n\t"
      "v_cvt_pk_bf16_f32 v22, v28, v29\n\t"
      "v_smfmac_f32_16x16x64_bf16 v[44:47], v[12:15], a[216:223], v122\n\t"
      "ds_write_b64 v127, v[22:23] offset:46088\n\t"
      "v_smfmac_f32_16x16x64_bf16 v[40:43], v[12:15], a[152:159], v122\n\t"
      "ds_read_b128 v[88:91], v127 offset:39936\n\t"
      "v_bitop3_b32 v64, v64, v95, v96 bitop3:0x80\n\t"
      "v_bitop3_b32 v64, v64, v97, v98 bitop3:0x80\n\t"
      "v_smfmac_f32_16x16x64_bf16 v[40:43], v[8:11], a[144:151], v122\n\t"
      "v_bitop3_b32 v64, v64, v99, s60 bitop3:0x80\n\t"
      "v_smfmac_f32_16x16x64_bf16 v[36:39], v[8:11], a[80:87], v122\n\t"
      "v_cmp_eq_u32_e64 vcc, 0, v64\n\t"
      "v_smfmac_f32_16x16x64_bf16 v[44:47], v[8:11], a[208:215], v122\n\t"
      "s_waitcnt lgkmcnt(1)\n\t"
      "v_smfmac_f32_16x16x64_bf16 v[32:35], v[12:15], a[24:31], v122\n\t"
      "s_cbranch_vccnz LR1_1%=\n\t"
      "LC1_1%=:\n\t"
      "v_smfmac_f32_16x16x64_bf16 v[40:43], v[20:23], a[168:175], v122\n\t"
      "s_barrier\n\t"
      "v_mfma_f32_16x16x32_bf16 v[56:59], v[20:23], v[20:23], v[56:59]\n\t"
      "ds_read_b128 v[16:19], v127 offset:45056\n\t"
      "s_nop 0\n\t"
      "v_smfmac_f32_16x16x64_bf16 v[32:35], v[8:11], a[16:23], v122\n\t"
      "ds_read_b128 v[28:31], v127 offset:48128\n\t"
      "v_smfmac_f32_16x16x64_bf16 v[44:47], v[20:23], a[232:239], v122\n\t"
      "ds_read_b128 v[24:27], v127 offset:47104\n\t"
      "v_max3_f32 v56, v56, v57, v58\n\t"
      "v_smfmac_f32_16x16x64_bf16 v[36:39], v[20:23], a[104:111], v122\n\t"
      "ds_read_b128 v[76:79], v127 offset:32768\n\t"
      "v_max_f32_e64 v56, v56, v59\n\t"
      "v_smfmac_f32_16x16x64_bf16 v[32:35], v[20:23], a[40:47], v122\n\t"
      "s_waitcnt lgkmcnt(3)\n\t"
      "v_readfirstlane_b32 s56, v120\n\t"
      "v_smfmac_f32_16x16x64_bf16 v[32:35], v[16:19], a[32:39], v122\n\t"
      "s_waitcnt lgkmcnt(2)\n\t"
      "v_max_f32_dpp v56, v56, v56 quad_perm:[1,0,3,2] row_mask:0xf bank_mask:0xf\n\t"
      "v_smfmac_f32_16x16x64_bf16 v[32:35], v[28:31], a[56:63], v122\n\t"
      "s_waitcnt lgkmcnt(1)\n\t"
      "v_max_f32_dpp v56, v56, v56 quad_perm:[2,3,0,1] row_mask:0xf bank_mask:0xf\n\t"
      "v_smfmac_f32_16x16x64_bf16 v[32:35], v[24:27], a[48:55], v122\n\t"
      "s_mov_b32 exec_lo, 0x100001\n\t"
      "s_mov_b32 exec_hi, 0x10000100\n\t"
      "v_smfmac_f32_16x16x64_bf16 v[36:39], v[16:19], a[96:103], v122\n\t"
      "ds_max_i32 v109, v56 offset:55392\n\t"
      "v_smfmac_f32_16x16x64_bf16 v[40:43], v[16:19], a[160:167], v122\n\t"
      "s_mov_b64 exec, -1\n\t"
      "v_add_f32 v33, v33, v35\n\t"
      "v_smfmac_f32_16x16x64_bf16 v[36:39], v[28:31], a[120:127], v122\n\t"
      "ds_read_b128 v[60:63], v127 offset:37888\n\t"
      "v_mul_f32_e64 v33, s59, v33\n\t"
      "v_smfmac_f32_16x16x64_bf16 v[36:39], v[24:27], a[112:119], v122\n\t"
      "v_add_f32_e64 v32, v32, v34\n\t"
      "v_smfmac_f32_16x16x64_bf16 v[44:47], v[16:19], a[224:231], v122\n\t"
      "ds_read_b128 v[56:59], v127 offset:38912\n\t"
      "v_mul_f32_e64 v32, s59, v32\n\t"
      "v_smfmac_f32_16x16x64_bf16 v[44:47], v[28:31], a[248:255], v122\n\t"
      "v_add_f32 v36, v36, v38\n\t"
      "v_add_f32 v37, v37, v39\n\t"
      "v_mul_f32_e64 v37, s59, v37\n\t"
      "v_smfmac_f32_16x16x64_bf16 v[40:43], v[28:31], a[184:191], v122\n\t"
      "ds_read_b128 v[92:95], v127 offset:34816\n\t"
      "v_cvt_pk_bf16_f32 v112, v32, v33\n\t"
      "v_smfmac_f32_16x16x64_bf16 v[44:47], v[24:27], a[240:247], v122\n\t"
      "ds_read_b128 v[96:99], v127 offset:33792\n\t"
      "v_mul_f32_e64 v36, s59, v36\n\t"
      "v_cvt_pk_bf16_f32 v113, v36, v37\n\t"
      "v_smfmac_f32_16x16x64_bf16 v[40:43], v[24:27], a[176:183], v122\n\t"
      "ds_write_b64 v125, v[112:113]\n\t"
      "v_smfmac_f32_16x16x64_bf16 v[48:51], v[0:3], v[128:135], v122\n\t"
      "s_nop 0\n\t"
      "v_add_f32 v45, v45, v47\n\t"
      "v_add_f32_e64 v44, v44, v46\n\t"
      "v_cvt_pk_bf16_f32 v121, v44, v45\n\t"
      "v_add_f32 v41, v41, v43\n\t"
      "v_add_f32 v40, v40, v42\n\t"
      "v_cvt_pk_bf16_f32 v120, v40, v41\n\t"
      "v_smfmac_f32_16x16x64_bf16 v[48:51], v[12:15], v[152:159], v122\n\t"
      "s_mov_b32 s58, 0x10000000\n\t"
      "ds_write_b64 v125, v[120:121] offset:2080\n\t"
      "v_smfmac_f32_16x16x64_bf16 v[48:51], v[8:11], v[144:151], v122\n\t"
      "s_waitcnt lgkmcnt(1)\n\t"
      "s_barrier\n\t"
      "ds_read_b64_tr_b16 v[36:37], v124 offset:50192\n\t"
      "v_smfmac_f32_16x16x64_bf16 v[48:51], v[20:23], v[168:175], v122\n\t"
      "ds_read_b32 v108, v109 offset:55392\n\t"
      "ds_read_b64_tr_b16 v[32:33], v124 offset:49152\n\t"
      "v_smfmac_f32_16x16x64_bf16 v[52:55], v[0:3], v[192:199], v122\n\t"
      "ds_read_b64_tr_b16 v[34:35], v124 offset:49672\n\t"
      "s_waitcnt vmcnt(0) lgkmcnt(4)\n\t"
      "ds_read_b64_tr_b16 v[38:39], v124 offset:50712\n\t"
      "v_smfmac_f32_16x16x64_bf16 v[48:51], v[16:19], v[160:167], v122\n\t"
      "s_barrier\n\t"
      "ds_read_b64_tr_b16 v[46:47], v124 offset:52792\n\t"
      "v_smfmac_f32_16x16x64_bf16 v[48:51], v[28:31], v[184:191], v122\n\t"
      "s_waitcnt lgkmcnt(4)\n\t"
      "ds_read_b64_tr_b16 v[44:45], v124 offset:52272\n\t"
      "v_readfirstlane_b32 s61, v108\n\t"
      "v_smfmac_f32_16x16x64_bf16 v[48:51], v[24:27], v[176:183], v122\n\t"
      "s_waitcnt lgkmcnt(3)\n\t"
      "ds_read_b64_tr_b16 v[42:43], v124 offset:51752\n\t"
      "ds_read_b64_tr_b16 v[40:41], v124 offset:51232\n\t"
      "s_waitcnt lgkmcnt(4)\n\t"
      "v_mfma_f32_16x16x32_bf16 v[112:115], v[76:79], v[32:35], 0\n\t"
      "s_lshr_b32 s61, s61, 23\n\t"
      "s_add_u32 s61, s61, s67\n\t"
      "v_mov_b32_dpp v102, v34 quad_perm:[2,3,0,1] row_mask:0xf bank_mask:0xf\n\t"
      "v_mov_b32_dpp v106, v38 quad_perm:[2,3,0,1] row_mask:0xf bank_mask:0xf\n\t"
      "s_max_u32 s61, s61, s66\n\t"
      "v_mfma_f32_16x16x32_bf16 v[116:119], v[80:83], v[32:35], 0\n\t"
      "s_lshr_b32 s61, s61, 1\n\t"
      "v_add_f32_e64 v48, v48, v50\n\t"
      "v_mov_b32_dpp v100, v32 quad_perm:[2,3,0,1] row_mask:0xf bank_mask:0xf\n\t"
      "v_mfma_f32_16x16x32_bf16 v[116:119], v[88:91], v[36:39], v[116:119]\n\t"
      "s_min_u32 s61, s61, 200\n\t"
      "v_mov_b32_dpp v107, v39 quad_perm:[2,3,0,1] row_mask:0xf bank_mask:0xf\n\t"
      "s_waitcnt lgkmcnt(0)\n\t"
      "v_mfma_f32_16x16x32_bf16 v[76:79], v[40:43], v[32:35], 0\n\t"
      "buffer_load_dwordx4 v127, s[36:39], s46 offen sc1 lds\n\t"
      "s_lshl_b32 s61, s61, 23\n\t"
      "v_mfma_f32_16x16x32_bf16 v[76:79], v[44:47], v[36:39], v[76:79]\n\t"
      "s_add_u32 s65, s61, 33554432\n\t"
      "v_mov_b32_dpp v104, v36 quad_perm:[2,3,0,1] row_mask:0xf bank_mask:0xf\n\t"
      "v_mov_b32_dpp v103, v35 quad_perm:[2,3,0,1] row_mask:0xf bank_mask:0xf\n\t"
      "v_mov_b32_dpp v101, v33 quad_perm:[2,3,0,1] row_mask:0xf bank_mask:0xf\n\t"
      "v_mfma_f32_16x16x32_bf16 v[112:115], v[84:87], v[36:39], v[112:115]\n\t"
      "s_pack_hh_b32_b16 s75, s65, s65\n\t"
      "v_add_f32 v49, v49, v51\n\t"
      "v_cvt_pk_bf16_f32 v64, v48, v49\n\t"
      "v_cndmask_b32_e64 v120, v76, v78, s[72:73]\n\t"
      "v_cndmask_b32_e64 v121, v77, v79, s[72:73]\n\t"
      "v_mov_b32_dpp v105, v37 quad_perm:[2,3,0,1] row_mask:0xf bank_mask:0xf\n\t"
      "v_mfma_f32_16x16x32_bf16 v[112:115], v[92:95], v[100:103], v[112:115]\n\t"
      "buffer_load_dwordx4 v127, s[36:39], s46 offen offset:1024 sc1 lds\n\t"
      "v_mfma_f32_16x16x32_bf16 v[116:119], v[96:99], v[100:103], v[116:119]\n\t"
      "s_pack_hh_b32_b16 s74, s65, s65\n\t"
      "v_add_f32_dpp v121, v121, v121 quad_perm:[2,3,0,1] row_mask:0xf bank_mask:0xf\n\t"
      "v_add_f32_dpp v120, v120, v120 quad_perm:[2,3,0,1] row_mask:0xf bank_mask:0xf\n\t"
      "v_mfma_f32_16x16x32_bf16 v[112:115], v[56:59], v[104:107], v[112:115]\n\t"
      "v_exp_f32 v81, v121\n\t"
      "v_mfma_f32_16x16x32_bf16 v[116:119], v[60:63], v[104:107], v[116:119]\n\t"
      "s_mov_b32 m0, s45\n\t"
      "v_mov_b64 v[90:91], s[74:75]\n\t"
      "v_exp_f32 v80, v120\n\t"
      "v_mov_b64 v[88:89], s[74:75]\n\t"
      "v_cvt_pk_bf16_f32 v108, v80, v81\n\t"
      "v_smfmac_f32_16x16x64_bf16 v[52:55], v[12:15], v[216:223], v122\n\t"
      "buffer_load_dwordx4 v65, s[36:39], s44 offen offset:1024 sc1 lds\n\t"
      "v_mfma_f32_16x16x32_bf16 v[84:87], v[108:111], v[64:67], 0\n\t"
      "s_add_u32 s57, s61, 310378496\n\t"
      "v_cndmask_b32_e64 v112, v112, v116, s[72:73]\n\t"
      "v_exp_f32_e64 v56, v112\n\t"
      "v_mfma_f32_16x16x32_bf16 v[48:51], v[108:111], v[88:91], 0\n\t"
      "s_add_u32 s43, s43, 0x8000\n\t"
      "v_cndmask_b32_e64 v113, v113, v117, s[72:73]\n\t"
      "v_cndmask_b32_e64 v114, v114, v118, s[72:73]\n\t"
      "v_exp_f32_e64 v58, v114\n\t"
      "v_cndmask_b32_e64 v115, v115, v119, s[72:73]\n\t"
      "v_exp_f32 v57, v113\n\t"
      "v_exp_f32 v59, v115\n\t"
      "v_cvt_pk_bf16_f32 v96, v56, v57\n\t"
      "v_cvt_pk_bf16_f32 v97, v58, v59\n\t"
      "v_max3_f32 v82, |v112|, |v113|, |v114|\n\t"
      "v_mov_b32_dpp v98, v96 quad_perm:[2,3,0,1] row_mask:0xf bank_mask:0xf\n\t"
      "v_mov_b32_dpp v99, v97 quad_perm:[2,3,0,1] row_mask:0xf bank_mask:0xf\n\t"
      "v_smfmac_f32_16x16x64_bf16 v[52:55], v[8:11], v[208:215], v122\n\t"
      "v_max3_f32 v83, |v115|, |v120|, |v121|\n\t"
      "v_mfma_f32_16x16x32_bf16 v[48:51], v[96:99], v[88:91], v[48:51]\n\t"
      "v_max_f32_e64 v82, v82, v83\n\t"
      "v_cmp_nge_f32 vcc, 0x42000000, v82\n\t"
      "v_mfma_f32_16x16x32_bf16 v[84:87], v[96:99], v[68:71], v[84:87]\n\t"
      "buffer_load_dwordx4 v65, s[36:39], s44 offen sc1 lds\n\t"
      "v_smfmac_f32_16x16x64_bf16 v[52:55], v[20:23], v[232:239], v122\n\t"
      "s_add_u32 s68, s68, 0x8000\n\t"
      "s_cbranch_vccnz LMX_1%=\n\t"
      "v_rcp_f32 v49, v49\n\t"
      "v_rcp_f32_e64 v48, v48\n\t"
      "v_smfmac_f32_16x16x64_bf16 v[52:55], v[16:19], v[224:231], v122\n\t"
      "v_fma_f32 v85, v85, v49, s96\n\t"
      "v_fma_f32 v84, v84, v48, s96\n\t"
      "v_smfmac_f32_16x16x64_bf16 v[52:55], v[28:31], v[248:255], v122\n\t"
      "buffer_atomic_add v84, v123, s[36:39], s97 offen\n\t"
      "buffer_atomic_add v85, v123, s[36:39], s98 offen\n\t"
      "v_smfmac_f32_16x16x64_bf16 v[52:55], v[24:27], v[240:247], v122\n\t"
      "v_mfma_f32_16x16x32_bf16 v[92:95], v[96:99], v[72:75], 0\n\t"
      "s_nop 3\n\t"
      "v_add_f32 v53, v53, v55\n\t"
      "v_add_f32_e64 v52, v52, v54\n\t"
      "v_cvt_pk_bf16_f32 v66, v52, v53\n\t"
      "s_add_u32 s62, s61, 0x83400000\n\t"
      "v_mov_b32_e64 v126, s62\n\t"
      "v_mfma_f32_16x16x32_bf16 v[92:95], v[108:111], v[66:69], v[92:95]\n\t"
      "s_add_u32 s97, s97, 0x8000\n\t"
      "s_nop 6\n\t"
      "v_fma_f32 v95, v95, v49, s96\n\t"
      "buffer_atomic_add v95, v123, s[36:39], s23 offen\n\t"
      "v_fma_f32 v94, v94, v48, s96\n\t"
      "buffer_atomic_add v94, v123, s[36:39], s69 offen\n\t"
      "LWIN_1%=:\n\t"
      "v_add_u32 v100, s55, v127\n\t"
      "ds_read_b128 v[60:63], v100 offset:57344\n\t"
      "s_cmp_eq_u32 s56, 0\n\t"
      "s_cbranch_scc0 LSLOW_1%=\n\t"
      "LWIN1_1%=:\n\t"
      "buffer_load_dwordx2 v[120:121], off, s[36:39], s50 sc1\n\t"
      "s_mov_b32 s64, s50\n\t"
      "s_bitcmp1_b64 s[84:85], s40\n\t"
      "s_cbranch_scc1 LEARLY_1%=\n\t"
      "LROLE_1%=:\n\t"
      "v_add_u32 v52, 67584, v127\n\t"
      "ds_read_b128 v[88:91], v52\n\t"
      "ds_read_b128 v[92:95], v52 offset:1024\n\t"
      "ds_read_b128 v[32:35], v52 offset:4096\n\t"
      "ds_read_b128 v[36:39], v52 offset:5120\n\t"
      "ds_read_b128 v[40:43], v52 offset:8192\n\t"
      "ds_read_b128 v[44:47], v52 offset:9216\n\t"
      "v_mov_b64 v[48:49], 0\n\t"
      "v_mov_b64 v[50:51], 0\n\t"
      "v_add_u32 v104, s71, v100\n\t"
      "ds_read_b128 v[76:79], v104\n\t"
      "ds_read_b128 v[80:83], v104 offset:1024\n\t"
      "ds_read_b128 v[112:115], v100\n\t"
      "ds_read_b128 v[116:119], v100 offset:1024\n\t"
      "v_xor_b32 v101, 2048, v104\n\t"
      "v_xor_b32 v102, 4096, v104\n\t"
      "v_xor_b32 v103, 6144, v104\n\t"
      "s_waitcnt lgkmcnt(8)\n\t"
      "v_smfmac_f32_16x16x64_bf16 v[48:51], v[0:3], v[88:95], v122\n\t"
      "ds_read_b128 v[88:91], v52 offset:12288\n\t"
      "ds_read_b128 v[92:95], v52 offset:13312\n\t"
      "s_add_u32 m0, s55, s71\n\t"
      "s_sub_u32 s71, 204800, s71\n\t"
      "s_waitcnt lgkmcnt(8)\n\t"
      "v_smfmac_f32_16x16x64_bf16 v[48:51], v[4:7], v[32:39], v122\n\t"
      "ds_read_b128 v[16:19], v101\n\t"
      "ds_read_b128 v[20:23], v101 offset:1024\n\t"
      "s_add_u32 s40, s40, 1\n\t"
      "s_cmp_lt_u32 s40, 63\n\t"
      "s_waitcnt lgkmcnt(8)\n\t"
      "v_smfmac_f32_16x16x64_bf16 v[48:51], v[8:11], v[40:47], v122\n\t"
      "ds_read_b128 v[24:27], v100 offset:8192\n\t"
      "ds_read_b128 v[28:31], v100 offset:9216\n\t"
      "s_cselect_b32 s61, 786432, 0\n\t"
      "s_cselect_b32 s60, s58, 0\n\t"
      "s_waitcnt lgkmcnt(4)\n\t"
      "v_smfmac_f32_16x16x64_bf16 v[48:51], v[12:15], v[88:95], v122\n\t"
      "s_add_u32 s44, s44, s61\n\t"
      "s_cmp_lt_u32 s40, 62\n\t"
      "ds_read_b128 v[0:3], v102\n\t"
      "ds_read_b128 v[4:7], v102 offset:1024\n\t"
      "ds_read_b128 v[8:11], v100 offset:16384\n\t"
      "ds_read_b128 v[12:15], v100 offset:17408\n\t"
      "s_cselect_b32 s61, 786432, 0\n\t"
      "s_add_u32 s46, s46, s61\n\t"
      "s_add_u32 s61, s40, 3\n\t"
      "s_and_b32 s62, s61, 3\n\t"
      "s_cselect_b32 s62, 16, 2000\n\t"
      "s_cmp_lt_u32 s61, 64\n\t"
      "s_cselect_b32 s62, s62, 0\n\t"
      "s_add_u32 s50, s50, s62\n\t"
      "s_mov_b32 s63, 0x2000\n\t"
      "s_mov_b32 s28, s49\n\t"
      "s_add_u32 s49, s49, 65536\n\t"
      "s_cmp_eq_u32 s40, 64\n\t"
      "v_add_f32 v48, v48, v50\n\t"
      "v_add_f32 v49, v49, v51\n\t"
      "s_cbranch_scc1 LFIN_1%=\n\t"
      "v_mfma_f32_16x16x32_bf16 v[32:35], v[76:79], v[112:115], 0\n\t"
      "v_mfma_f32_16x16x32_bf16 v[36:39], v[80:83], v[112:115], 0\n\t"
      "v_mfma_f32_16x16x32_bf16 v[40:43], v[76:79], v[116:119], 0\n\t"
      "v_mfma_f32_16x16x32_bf16 v[44:47], v[80:83], v[116:119], 0\n\t"
      "ds_read_b128 v[76:79], v103\n\t"
      "ds_read_b128 v[80:83], v103 offset:1024\n\t"
      "ds_read_b128 v[112:115], v100 offset:24576\n\t"
      "ds_read_b128 v[116:119], v100 offset:25600\n\t"
      "s_waitcnt lgkmcnt(8)\n\t"
      "v_mfma_f32_16x16x32_bf16 v[32:35], v[16:19], v[24:27], v[32:35]\n\t"
      "v_mfma_f32_16x16x32_bf16 v[36:39], v[20:23], v[24:27], v[36:39]\n\t"
      "v_mfma_f32_16x16x32_bf16 v[40:43], v[16:19], v[28:31], v[40:43]\n\t"
      "v_mfma_f32_16x16x32_bf16 v[44:47], v[20:23], v[28:31], v[44:47]\n\t"
      "LGATE_1%=:\n\t"
      "LBAR_1%=:\n\t"
      "s_cmp_gt_u32 s40, 1\n\t"
      "s_barrier\n\t"
      "s_cbranch_scc1 LPOLL_1%=\n\t"
      "s_branch LFIRST_1%=\n\t"
      ".p2align 6\n\t.fill 16, 4, 0xbf800000\n\t"
      "LPOLL_2%=:\n\t"
      "buffer_load_dwordx4 v[84:87], v60, s[36:39], s43 offen sc1\n\t"
      "buffer_load_dwordx4 v[88:91], v60, s[36:39], s43 offen offset:1024 sc1\n\t"
      "s_waitcnt lgkmcnt(8)\n\t"
      "v_mfma_f32_16x16x32_bf16 v[32:35], v[0:3], v[8:11], v[32:35]\n\t"
      "v_mfma_f32_16x16x32_bf16 v[36:39], v[4:7], v[8:11], v[36:39]\n\t"
      "v_mfma_f32_16x16x32_bf16 v[40:43], v[0:3], v[12:15], v[40:43]\n\t"
      "v_mfma_f32_16x16x32_bf16 v[44:47], v[4:7], v[12:15], v[44:47]\n\t"
      "buffer_load_dwordx4 v[92:95], v60, s[36:39], s68 offen sc1\n\t"
      "buffer_load_dwordx4 v[96:99], v60, s[36:39], s68 offen offset:1024 sc1\n\t"
      "v_mov_b64 v[48:49], 0\n\t"
      "v_mov_b64 v[50:51], 0\n\t"
      "v_mov_b64 v[52:53], 0\n\t"
      "v_mov_b64 v[54:55], 0\n\t"
      "s_xor_b32 s69, s97, 1024\n\t"
      "s_xor_b32 s98, s97, 512\n\t"
      "s_xor_b32 s23, s97, 1536\n\t"
      "s_waitcnt lgkmcnt(4)\n\t"
      "v_mfma_f32_16x16x32_bf16 v[32:35], v[16:19], v[24:27], v[32:35]\n\t"
      "v_mfma_f32_16x16x32_bf16 v[36:39], v[20:23], v[24:27], v[36:39]\n\t"
      "v_mfma_f32_16x16x32_bf16 v[40:43], v[16:19], v[28:31], v[40:43]\n\t"
      "v_mfma_f32_16x16x32_bf16 v[44:47], v[20:23], v[28:31], v[44:47]\n\t"
      "s_waitcnt lgkmcnt(0)\n\t"
      "v_mfma_f32_16x16x32_bf16 v[32:35], v[76:79], v[112:115], v[32:35]\n\t"
      "v_mfma_f32_16x16x32_bf16 v[36:39], v[80:83], v[112:115], v[36:39]\n\t"
      "v_mfma_f32_16x16x32_bf16 v[40:43], v[76:79], v[116:119], v[40:43]\n\t"
      "v_mfma_f32_16x16x32_bf16 v[44:47], v[80:83], v[116:119], v[44:47]\n\t"
      "s_mov_b64 exec, 1\n\t"
      "ds_write_b64 v109, v[110:111] offset:55392\n\t"
      "s_mov_b64 exec, -1\n\t"
      "s_nop 4\n\t"
      "v_cvt_pk_bf16_f32 v68, v32, v33\n\t"
      "v_cvt_pk_bf16_f32 v69, v34, v35\n\t"
      "v_cvt_pk_bf16_f32 v70, v36, v37\n\t"
      "v_cvt_pk_bf16_f32 v71, v38, v39\n\t"
      "v_cvt_pk_bf16_f32 v74, v40, v41\n\t"
      "v_cvt_pk_bf16_f32 v75, v42, v43\n\t"
      "v_cvt_pk_bf16_f32 v72, v44, v45\n\t"
      "v_cvt_pk_bf16_f32 v73, v46, v47\n\t"
      "v_mov_b64 v[32:33], 0\n\t"
      "v_mov_b64 v[34:35], 0\n\t"
      "v_mov_b64 v[36:37], 0\n\t"
      "v_mov_b64 v[38:39], 0\n\t"
      "v_mov_b64 v[40:41], 0\n\t"
      "v_mov_b64 v[42:43], 0\n\t"
      "v_mov_b64 v[44:45], 0\n\t"
      "v_mov_b64 v[46:47], 0\n\t"
      "LGO_2%=:\n\t"
      "s_nop 0\n\t"
      "s_nop 0\n\t"
      "s_nop 0\n\t"
      "s_nop 0\n\t"
      "s_nop 0\n\t"
      "s_waitcnt vmcnt(3)\n\t"
      "v_pk_fma_f32 v[14:15], v[86:87], s[56:57], v[126:127] op_sel:[0,1,0] op_sel_hi:[1,1,0]\n\t"
      "v_pk_fma_f32 v[12:13], v[84:85], s[56:57], v[126:127] op_sel:[0,1,0] op_sel_hi:[1,1,0]\n\t"
      "v_cvt_pk_bf16_f32 v8, v12, v13\n\t"
      "v_cvt_pk_bf16_f32 v9, v14, v15\n\t"
      "s_nop 0\n\t"
      "ds_write_b64 v127, v[8:9] offset:43008\n\t"
      "s_waitcnt vmcnt(2)\n\t"
      "v_pk_fma_f32 v[0:1], v[88:89], s[56:57], v[126:127] op_sel:[0,1,0] op_sel_hi:[1,1,0]\n\t"
      "v_pk_fma_f32 v[2:3], v[90:91], s[56:57], v[126:127] op_sel:[0,1,0] op_sel_hi:[1,1,0]\n\t"
      "v_cvt_pk_bf16_f32 v10, v0, v1\n\t"
      "v_cvt_pk_bf16_f32 v11, v2, v3\n\t"
      "ds_write_b64 v127, v[10:11] offset:43016\n\t"
      "v_bitop3_b32 v64, v84, v85, v86 bitop3:0x80\n\t"
      "v_mfma_f32_16x16x32_bf16 v[56:59], v[8:11], v[8:11], 0\n\t"
      "v_bitop3_b32 v64, v64, v87, v88 bitop3:0x80\n\t"
      "v_bitop3_b32 v64, v64, v89, v90 bitop3:0x80\n\t"
      "v_bitop3_b32 v64, v64, v91, s58 bitop3:0x80\n\t"
      "v_cmp_eq_u32_e64 vcc, 0, v64\n\t"
      "v_smfmac_f32_16x16x64_bf16 v[36:39], v[8:11], a[80:87], v122\n\t"
      "s_nop 0\n\t"
      "v_bitop3_b32 v120, v120, v121, s53 bitop3:0x7e\n\t"
      "v_smfmac_f32_16x16x64_bf16 v[44:47], v[8:11], a[208:215], v122\n\t"
      "s_cbranch_vccnz LR0_2%=\n\t"
      "LC0_2%=:\n\t"
      "s_waitcnt lgkmcnt(0)\n\t"
      "s_barrier\n\t"
      "ds_read_b128 v[12:15], v127 offset:44032\n\t"
      "v_smfmac_f32_16x16x64_bf16 v[52:55], v[8:11], v[208:215], v122\n\t"
      "ds_read_b128 v[0:3], v127 offset:40960\n\t"
      "v_smfmac_f32_16x16x64_bf16 v[48:51], v[8:11], v[144:151], v122\n\t"
      "ds_read_b128 v[4:7], v127 offset:41984\n\t"
      "v_smfmac_f32_16x16x64_bf16 v[32:35], v[8:11], a[16:23], v122\n\t"
      "ds_read_b128 v[84:87], v127 offset:36864\n\t"
      "v_smfmac_f32_16x16x64_bf16 v[40:43], v[8:11], a[144:151], v122\n\t"
      "s_waitcnt lgkmcnt(3)\n\t"
      "v_smfmac_f32_16x16x64_bf16 v[32:35], v[12:15], a[24:31], v122\n\t"
      "ds_read_b128 v[80:83], v127 offset:35840\n\t"
      "v_smfmac_f32_16x16x64_bf16 v[40:43], v[12:15], a[152:159], v122\n\t"
      "s_waitcnt vmcnt(1)\n\t"
      "v_fma_f32 v31, v95, s57, v126\n\t"
      "v_fma_f32 v28, v92, s57, v126\n\t"
      "v_smfmac_f32_16x16x64_bf16 v[36:39], v[12:15], a[88:95], v122\n\t"
      "v_fma_f32 v30, v94, s57, v126\n\t"
      "v_cvt_pk_bf16_f32 v25, v30, v31\n\t"
      "v_bitop3_b32 v64, v92, v93, v94 bitop3:0x80\n\t"
      "v_smfmac_f32_16x16x64_bf16 v[44:47], v[12:15], a[216:223], v122\n\t"
      "s_waitcnt lgkmcnt(2)\n\t"
      "v_fma_f32 v29, v93, s57, v126\n\t"
      "v_cvt_pk_bf16_f32 v24, v28, v29\n\t"
      "v_smfmac_f32_16x16x64_bf16 v[36:39], v[0:3], a[64:71], v122\n\t"
      "s_waitcnt vmcnt(0)\n\t"
      "ds_write_b64 v127, v[24:25] offset:47104\n\t"
      "v_fma_f32 v19, v99, s57, v126\n\t"
      "v_fma_f32 v18, v98, s57, v126\n\t"
      "v_cvt_pk_bf16_f32 v27, v18, v19\n\t"
      "v_fma_f32 v17, v97, s57, v126\n\t"
      "v_fma_f32 v16, v96, s57, v126\n\t"
      "v_cvt_pk_bf16_f32 v26, v16, v17\n\t"
      "v_smfmac_f32_16x16x64_bf16 v[44:47], v[0:3], a[192:199], v122\n\t"
      "ds_write_b64 v127, v[26:27] offset:47112\n\t"
      "v_smfmac_f32_16x16x64_bf16 v[40:43], v[0:3], a[128:135], v122\n\t"
      "ds_read_b128 v[88:91], v127 offset:39936\n\t"
      "v_bitop3_b32 v64, v64, v95, v96 bitop3:0x80\n\t"
      "v_bitop3_b32 v64, v64, v97, v98 bitop3:0x80\n\t"
      "v_smfmac_f32_16x16x64_bf16 v[40:43], v[4:7], a[136:143], v122\n\t"
      "v_bitop3_b32 v64, v64, v99, s60 bitop3:0x80\n\t"
      "v_smfmac_f32_16x16x64_bf16 v[36:39], v[4:7], a[72:79], v122\n\t"
      "v_cmp_eq_u32_e64 vcc, 0, v64\n\t"
      "v_smfmac_f32_16x16x64_bf16 v[44:47], v[4:7], a[200:207], v122\n\t"
      "s_waitcnt lgkmcnt(1)\n\t"
      "v_smfmac_f32_16x16x64_bf16 v[32:35], v[0:3], a[0:7], v122\n\t"
      "s_cbranch_vccnz LR1_2%=\n\t"
      "LC1_2%=:\n\t"
      "v_smfmac_f32_16x16x64_bf16 v[40:43], v[24:27], a[176:183], v122\n\t"
      "s_barrier\n\t"
      "v_mfma_f32_16x16x32_bf16 v[56:59], v[24:27], v[24:27], v[56:59]\n\t"
      "ds_read_b128 v[28:31], v127 offset:48128\n\t"
      "s_nop 0\n\t"
      "v_smfmac_f32_16x16x64_bf16 v[32:35], v[4:7], a[8:15], v122\n\t"
      "ds_read_b128 v[16:19], v127 offset:45056\n\t"
      "v_smfmac_f32_16x16x64_bf16 v[44:47], v[24:27], a[240:247], v122\n\t"
      "ds_read_b128 v[20:23], v127 offset:46080\n\t"
      "v_max3_f32 v56, v56, v57, v58\n\t"
      "v_smfmac_f32_16x16x64_bf16 v[36:39], v[24:27], a[112:119], v122\n\t"
      "ds_read_b128 v[76:79], v127 offset:32768\n\t"
      "v_max_f32_e64 v56, v56, v59\n\t"
      "v_smfmac_f32_16x16x64_bf16 v[32:35], v[24:27], a[48:55], v122\n\t"
      "s_waitcnt lgkmcnt(3)\n\t"
      "v_readfirstlane_b32 s56, v120\n\t"
      "v_smfmac_f32_16x16x64_bf16 v[32:35], v[28:31], a[56:63], v122\n\t"
      "s_waitcnt lgkmcnt(2)\n\t"
      "v_max_f32_dpp v56, v56, v56 quad_perm:[1,0,3,2] row_mask:0xf bank_mask:0xf\n\t"
      "v_smfmac_f32_16x16x64_bf16 v[32:35], v[16:19], a[32:39], v122\n\t"
      "s_waitcnt lgkmcnt(1)\n\t"
      "v_max_f32_dpp v56, v56, v56 quad_perm:[2,3,0,1] row_mask:0xf bank_mask:0xf\n\t"
      "v_smfmac_f32_16x16x64_bf16 v[32:35], v[20:23], a[40:47], v122\n\t"
      "s_mov_b32 exec_lo, 0x100001\n\t"
      "s_mov_b32 exec_hi, 0x10000100\n\t"
      "v_smfmac_f32_16x16x64_bf16 v[36:39], v[28:31], a[120:127], v122\n\t"
      "ds_max_i32 v109, v56 offset:55392\n\t"
      "v_smfmac_f32_16x16x64_bf16 v[40:43], v[28:31], a[184:191], v122\n\t"
      "s_mov_b64 exec, -1\n\t"
      "v_add_f32 v33, v33, v35\n\t"
      "v_smfmac_f32_16x16x64_bf16 v[36:39], v[16:19], a[96:103], v122\n\t"
      "ds_read_b128 v[60:63], v127 offset:37888\n\t"
      "v_mul_f32_e64 v33, s59, v33\n\t"
      "v_smfmac_f32_16x16x64_bf16 v[36:39], v[20:23], a[104:111], v122\n\t"
      "v_add_f32_e64 v32, v32, v34\n\t"
      "v_smfmac_f32_16x16x64_bf16 v[44:47], v[28:31], a[248:255], v122\n\t"
      "ds_read_b128 v[56:59], v127 offset:38912\n\t"
      "v_mul_f32_e64 v32, s59, v32\n\t"
      "v_smfmac_f32_16x16x64_bf16 v[44:47], v[16:19], a[224:231], v122\n\t"
      "v_add_f32 v36, v36, v38\n\t"
      "v_add_f32 v37, v37, v39\n\t"
      "v_mul_f32_e64 v37, s59, v37\n\t"
      "v_smfmac_f32_16x16x64_bf16 v[40:43], v[16:19], a[160:167], v122\n\t"
      "ds_read_b128 v[92:95], v127 offset:34816\n\t"
      "v_cvt_pk_bf16_f32 v112, v32, v33\n\t"
      "v_smfmac_f32_16x16x64_bf16 v[44:47], v[20:23], a[232:239], v122\n\t"
      "ds_read_b128 v[96:99], v127 offset:33792\n\t"
      "v_mul_f32_e64 v36, s59, v36\n\t"
      "v_cvt_pk_bf16_f32 v113, v36, v37\n\t"
      "v_smfmac_f32_16x16x64_bf16 v[40:43], v[20:23], a[168:175], v122\n\t"
      "ds_write_b64 v125, v[112:113]\n\t"
      "v_smfmac_f32_16x16x64_bf16 v[48:51], v[12:15], v[152:159], v122\n\t"
      "s_nop 0\n\t"
      "v_add_f32 v45, v45, v47\n\t"
      "v_add_f32_e64 v44, v44, v46\n\t"
      "v_cvt_pk_bf16_f32 v121, v44, v45\n\t"
      "v_add_f32 v41, v41, v43\n\t"
      "v_add_f32 v40, v40, v42\n\t"
      "v_cvt_pk_bf16_f32 v120, v40, v41\n\t"
      "v_smfmac_f32_16x16x64_bf16 v[48:51], v[0:3], v[128:135], v122\n\t"
      "s_mov_b32 s58, 0x10000000\n\t"
      "ds_write_b64 v125, v[120:121] offset:2080\n\t"
      "v_smfmac_f32_16x16x64_bf16 v[48:51], v[4:7], v[136:143], v122\n\t"
      "s_waitcnt lgkmcnt(1)\n\t"
      "s_barrier\n\t"
      "ds_read_b64_tr_b16 v[36:37], v124 offset:50192\n\t"
      "v_smfmac_f32_16x16x64_bf16 v[48:51], v[24:27], v[176:183], v122\n\t"
      "ds_read_b32 v108, v109 offset:55392\n\t"
      "ds_read_b64_tr_b16 v[32:33], v124 offset:49152\n\t"
      "v_smfmac_f32_16x16x64_bf16 v[52:55], v[12:15], v[216:223], v122\n\t"
      "ds_read_b64_tr_b16 v[34:35], v124 offset:49672\n\t"
      "s_waitcnt lgkmcnt(4)\n\t"
      "ds_read_b64_tr_b16 v[38:39], v124 offset:50712\n\t"
      "v_smfmac_f32_16x16x64_bf16 v[48:51], v[28:31], v[184:191], v122\n\t"
      "s_barrier\n\t"
      "ds_read_b64_tr_b16 v[46:47], v124 offset:52792\n\t"
      "v_smfmac_f32_16x16x64_bf16 v[48:51], v[16:19], v[160:167], v122\n\t"
      "s_waitcnt lgkmcnt(4)\n\t"
      "ds_read_b64_tr_b16 v[44:45], v124 offset:52272\n\t"
      "v_readfirstlane_b32 s61, v108\n\t"
      "v_smfmac_f32_16x16x64_bf16 v[48:51], v[20:23], v[168:175], v122\n\t"
      "s_waitcnt lgkmcnt(3)\n\t"
      "ds_read_b64_tr_b16 v[42:43], v124 offset:51752\n\t"
      "ds_read_b64_tr_b16 v[40:41], v124 offset:51232\n\t"
      "s_waitcnt lgkmcnt(4)\n\t"
      "v_mfma_f32_16x16x32_bf16 v[112:115], v[76:79], v[32:35], 0\n\t"
      "s_lshr_b32 s61, s61, 23\n\t"
      "s_add_u32 s61, s61, s67\n\t"
      "v_mov_b32_dpp v102, v34 quad_perm:[2,3,0,1] row_mask:0xf bank_mask:0xf\n\t"
      "v_mov_b32_dpp v106, v38 quad_perm:[2,3,0,1] row_mask:0xf bank_mask:0xf\n\t"
      "s_max_u32 s61, s61, s66\n\t"
      "v_mfma_f32_16x16x32_bf16 v[116:119], v[80:83], v[32:35], 0\n\t"
      "s_lshr_b32 s61, s61, 1\n\t"
      "v_add_f32_e64 v48, v48, v50\n\t"
      "v_mov_b32_dpp v100, v32 quad_perm:[2,3,0,1] row_mask:0xf bank_mask:0xf\n\t"
      "v_mfma_f32_16x16x32_bf16 v[116:119], v[88:91], v[36:39], v[116:119]\n\t"
      "s_min_u32 s61, s61, 200\n\t"
      "v_mov_b32_dpp v107, v39 quad_perm:[2,3,0,1] row_mask:0xf bank_mask:0xf\n\t"
      "s_waitcnt lgkmcnt(0)\n\t"
      "v_mfma_f32_16x16x32_bf16 v[76:79], v[40:43], v[32:35], 0\n\t"
      "buffer_load_dwordx4 v127, s[36:39], s46 offen sc1 lds\n\t"
      "s_lshl_b32 s61, s61, 23\n\t"
      "v_mfma_f32_16x16x32_bf16 v[76:79], v[44:47], v[36:39], v[76:79]\n\t"
      "s_add_u32 s65, s61, 33554432\n\t"
      "v_mov_b32_dpp v104, v36 quad_perm:[2,3,0,1] row_mask:0xf bank_mask:0xf\n\t"
      "v_mov_b32_dpp v103, v35 quad_perm:[2,3,0,1] row_mask:0xf bank_mask:0xf\n\t"
      "v_mov_b32_dpp v101, v33 quad_perm:[2,3,0,1] row_mask:0xf bank_mask:0xf\n\t"
      "v_mfma_f32_16x16x32_bf16 v[112:115], v[84:87], v[36:39], v[112:115]\n\t"
      "s_pack_hh_b32_b16 s75, s65, s65\n\t"
      "v_add_f32 v49, v49, v51\n\t"
      "v_cvt_pk_bf16_f32 v64, v48, v49\n\t"
      "v_cndmask_b32_e64 v120, v76, v78, s[72:73]\n\t"
      "v_cndmask_b32_e64 v121, v77, v79, s[72:73]\n\t"
      "v_mov_b32_dpp v105, v37 quad_perm:[2,3,0,1] row_mask:0xf bank_mask:0xf\n\t"
      "v_mfma_f32_16x16x32_bf16 v[112:115], v[92:95], v[100:103], v[112:115]\n\t"
      "buffer_load_dwordx4 v127, s[36:39], s46 offen offset:1024 sc1 lds\n\t"
      "v_mfma_f32_16x16x32_bf16 v[116:119], v[96:99], v[100:103], v[116:119]\n\t"
      "s_pack_hh_b32_b16 s74, s65, s65\n\t"
      "v_add_f32_dpp v121, v121, v121 quad_perm:[2,3,0,1] row_mask:0xf bank_mask:0xf\n\t"
      "v_add_f32_dpp v120, v120, v120 quad_perm:[2,3,0,1] row_mask:0xf bank_mask:0xf\n\t"
      "v_mfma_f32_16x16x32_bf16 v[112:115], v[56:59], v[104:107], v[112:115]\n\t"
      "v_exp_f32 v81, v121\n\t"
      "v_mfma_f32_16x16x32_bf16 v[116:119], v[60:63], v[104:107], v[116:119]\n\t"
      "s_mov_b32 m0, s45\n\t"
      "v_mov_b64 v[90:91], s[74:75]\n\t"
      "v_exp_f32 v80, v120\n\t"
      "v_mov_b64 v[88:89], s[74:75]\n\t"
      "v_cvt_pk_bf16_f32 v108, v80, v81\n\t"
      "v_smfmac_f32_16x16x64_bf16 v[52:55], v[0:3], v[192:199], v122\n\t"
      "buffer_load_dwordx4 v65, s[36:39], s44 offen offset:1024 sc1 lds\n\t"
      "v_mfma_f32_16x16x32_bf16 v[84:87], v[108:111], v[64:67], 0\n\t"
      "s_add_u32 s57, s61, 310378496\n\t"
      "v_cndmask_b32_e64 v112, v112, v116, s[72:73]\n\t"
      "v_exp_f32_e64 v56, v112\n\t"
      "v_mfma_f32_16x16x32_bf16 v[48:51], v[108:111], v[88:91], 0\n\t"
      "s_add_u32 s43, s43, 0x8000\n\t"
      "v_cndmask_b32_e64 v113, v113, v117, s[72:73]\n\t"
      "v_cndmask_b32_e64 v114, v114, v118, s[72:73]\n\t"
      "v_exp_f32_e64 v58, v114\n\t"
      "v_cndmask_b32_e64 v115, v115, v119, s[72:73]\n\t"
      "v_exp_f32 v57, v113\n\t"
      "v_exp_f32 v59, v115\n\t"
      "v_cvt_pk_bf16_f32 v96, v56, v57\n\t"
      "v_cvt_pk_bf16_f32 v97, v58, v59\n\t"
      "v_max3_f32 v82, |v112|, |v113|, |v114|\n\t"
      "v_mov_b32_dpp v98, v96 quad_perm:[2,3,0,1] row_mask:0xf bank_mask:0xf\n\t"
      "v_mov_b32_dpp v99, v97 quad_perm:[2,3,0,1] row_mask:0xf bank_mask:0xf\n\t"
      "v_smfmac_f32_16x16x64_bf16 v[52:55], v[4:7], v[200:207], v122\n\t"
      "v_max3_f32 v83, |v115|, |v120|, |v121|\n\t"
      "v_mfma_f32_16x16x32_bf16 v[48:51], v[96:99], v[88:91], v[48:51]\n\t"
      "v_max_f32_e64 v82, v82, v83\n\t"
      "v_cmp_nge_f32 vcc, 0x42000000, v82\n\t"
      "v_mfma_f32_16x16x32_bf16 v[84:87], v[96:99], v[68:71], v[84:87]\n\t"
      "buffer_load_dwordx4 v65, s[36:39], s44 offen sc1 lds\n\t"
      "v_smfmac_f32_16x16x64_bf16 v[52:55], v[24:27], v[240:247], v122\n\t"
      "s_add_u32 s68, s68, 0x8000\n\t"
      "s_cbranch_vccnz LMX_2%=\n\t"
      "v_rcp_f32 v49, v49\n\t"
      "v_rcp_f32_e64 v48, v48\n\t"
      "v_smfmac_f32_16x16x64_bf16 v[52:55], v[28:31], v[248:255], v122\n\t"
      "v_fma_f32 v85, v85, v49, s96\n\t"
      "v_fma_f32 v84, v84, v48, s96\n\t"
      "v_smfmac_f32_16x16x64_bf16 v[52:55], v[16:19], v[224:231], v122\n\t"
      "buffer_atomic_add v84, v123, s[36:39], s97 offen\n\t"
      "buffer_atomic_add v85, v123, s[36:39], s98 offen\n\t"
      "v_smfmac_f32_16x16x64_bf16 v[52:55], v[20:23], v[232:239], v122\n\t"
      "v_mfma_f32_16x16x32_bf16 v[92:95], v[96:99], v[72:75], 0\n\t"
      "s_nop 3\n\t"
      "v_add_f32 v53, v53, v55\n\t"
      "v_add_f32_e64 v52, v52, v54\n\t"
      "v_cvt_pk_bf16_f32 v66, v52, v53\n\t"
      "s_add_u32 s62, s61, 0x83400000\n\t"
      "v_mov_b32_e64 v126, s62\n\t"
      "v_mfma_f32_16x16x32_bf16 v[92:95], v[108:111], v[66:69], v[92:95]\n\t"
      "s_add_u32 s97, s97, 0x8000\n\t"
      "s_nop 6\n\t"
      "v_fma_f32 v95, v95, v49, s96\n\t"
      "buffer_atomic_add v95, v123, s[36:39], s23 offen\n\t"
      "v_fma_f32 v94, v94, v48, s96\n\t"
      "buffer_atomic_add v94, v123, s[36:39], s69 offen\n\t"
      "LWIN_2%=:\n\t"
      "v_add_u32 v100, s55, v127\n\t"
      "ds_read_b128 v[60:63], v100 offset:57344\n\t"
      "s_cmp_eq_u32 s56, 0\n\t"
      "s_cbranch_scc0 LSLOW_2%=\n\t"
      "LWIN1_2%=:\n\t"
      "buffer_load_dwordx2 v[120:121], off, s[36:39], s50 sc1\n\t"
      "s_mov_b32 s64, s50\n\t"
      "s_bitcmp1_b64 s[84:85], s40\n\t"
      "s_cbranch_scc1 LEARLY_2%=\n\t"
      "LROLE_2%=:\n\t"
      "v_add_u32 v52, 65536, v127\n\t"
      "ds_read_b128 v[88:91], v52 offset:16384\n\t"
      "ds_read_b128 v[92:95], v52 offset:17408\n\t"
      "ds_read_b128 v[32:35], v52 offset:20480\n\t"
      "ds_read_b128 v[36:39], v52 offset:21504\n\t"
      "s_cmp_eq_u32 s42, 3\n\t"
      "s_waitcnt lgkmcnt(4)\n\t"
      "s_cbranch_scc1 LWK1_2%=\n\t"
      "s_cmp_eq_u32 s42, 2\n\t"
      "s_cbranch_scc0 LW4_2%=\n\t"
      "buffer_store_dwordx4 v[40:43], v61, s[36:39], s48 offen sc1\n\t"
      "LW4_2%=:\n\t"
      "s_add_u32 s48, s48, 65536\n\t"
      "s_nop 0\n\t"
      "ds_read_b128 v[40:43], v52 offset:24576\n\t"
      "ds_read_b128 v[44:47], v52 offset:25600\n\t"
      "v_mov_b64 v[48:49], 0\n\t"
      "v_mov_b64 v[50:51], 0\n\t"
      "s_memrealtime s[92:93]\n\t"
      "s_waitcnt lgkmcnt(0)\n\t"
      "s_sub_u32 s61, s92, s95\n\t"
      "s_mov_b32 s95, s92\n\t"
      "s_min_u32 s61, s61, 190\n\t"
      "s_add_u32 s61, s61, s92\n\t"
      "s_pack_ll_b32_b16 s61, s61, s40\n\t"
      "v_mov_b32 v63, s61\n\t"
      "s_mov_b64 exec, 1\n\t"
      "buffer_store_dword v63, off, s[36:39], s51 sc1\n\t"
      "s_mov_b64 exec, -1\n\t"
      "v_add_u32 v104, s71, v100\n\t"
      "ds_read_b128 v[76:79], v104\n\t"
      "ds_read_b128 v[80:83], v104 offset:1024\n\t"
      "ds_read_b128 v[112:115], v100\n\t"
      "ds_read_b128 v[116:119], v100 offset:1024\n\t"
      "v_xor_b32 v101, 2048, v104\n\t"
      "v_xor_b32 v102, 4096, v104\n\t"
      "v_xor_b32 v103, 6144, v104\n\t"
      "v_smfmac_f32_16x16x64_bf16 v[48:51], v[16:19], v[88:95], v122\n\t"
      "ds_read_b128 v[88:91], v52 offset:28672\n\t"
      "ds_read_b128 v[92:95], v52 offset:29696\n\t"
      "s_add_u32 m0, s55, s71\n\t"
      "s_sub_u32 s71, 204800, s71\n\t"
      "v_smfmac_f32_16x16x64_bf16 v[48:51], v[20:23], v[32:39], v122\n\t"
      "ds_read_b128 v[0:3], v101\n\t"
      "ds_read_b128 v[4:7], v101 offset:1024\n\t"
      "s_add_u32 s40, s40, 1\n\t"
      "s_cmp_lt_u32 s40, 63\n\t"
      "v_smfmac_f32_16x16x64_bf16 v[48:51], v[24:27], v[40:47], v122\n\t"
      "ds_read_b128 v[8:11], v100 offset:8192\n\t"
      "ds_read_b128 v[12:15], v100 offset:9216\n\t"
      "s_cselect_b32 s61, 786432, 0\n\t"
      "s_cselect_b32 s60, s58, 0\n\t"
      "s_waitcnt lgkmcnt(4)\n\t"
      "v_smfmac_f32_16x16x64_bf16 v[48:51], v[28:31], v[88:95], v122\n\t"
      "s_add_u32 s44, s44, s61\n\t"
      "s_cmp_lt_u32 s40, 62\n\t"
      "ds_read_b128 v[16:19], v102\n\t"
      "ds_read_b128 v[20:23], v102 offset:1024\n\t"
      "ds_read_b128 v[24:27], v100 offset:16384\n\t"
      "ds_read_b128 v[28:31], v100 offset:17408\n\t"
      "s_cselect_b32 s61, 786432, 0\n\t"
      "s_add_u32 s46, s46, s61\n\t"
      "s_add_u32 s61, s40, 3\n\t"
      "s_and_b32 s62, s61, 3\n\t"
      "s_cselect_b32 s62, 16, 2000\n\t"
      "s_cmp_lt_u32 s61, 64\n\t"
      "s_cselect_b32 s62, s62, 0\n\t"
      "s_add_u32 s50, s50, s62\n\t"
      "s_mov_b32 s63, 0x2000\n\t"
      "s_cmp_eq_u32 s40, 64\n\t"
      "v_add_f32 v48, v48, v50\n\t"
      "v_add_f32 v49, v49, v51\n\t"
      "ds_write_b64 v124, v[48:49] offset:53312\n\t"
      "s_cbranch_scc1 LFIN_2%=\n\t"
      "v_mfma_f32_16x16x32_bf16 v[32:35], v[76:79], v[112:115], 0\n\t"
      "v_mfma_f32_16x16x32_bf16 v[36:39], v[80:83], v[112:115], 0\n\t"
      "v_mfma_f32_16x16x32_bf16 v[40:43], v[76:79], v[116:119], 0\n\t"
      "v_mfma_f32_16x16x32_bf16 v[44:47], v[80:83], v[116:119], 0\n\t"
      "ds_read_b128 v[76:79], v103\n\t"
      "ds_read_b128 v[80:83], v103 offset:1024\n\t"
      "ds_read_b128 v[112:115], v100 offset:24576\n\t"
      "ds_read_b128 v[116:119], v100 offset:25600\n\t"
      "LGATE_2%=:\n\t"
      "LBAR_2%=:\n\t"
      "s_cmp_gt_u32 s40, 1\n\t"
      "s_waitcnt lgkmcnt(0)\n\t"
      "s_barrier\n\t"
      "s_cbranch_scc1 LPOLL_2%=\n\t"
      "s_branch LFIRST_2%=\n\t"
      ".p2align 6\n\t.fill 16, 4, 0xbf800000\n\t"
      "LPOLL_3%=:\n\t"
      "s_memtime s[78:79]\n\t"
      "buffer_load_dwordx4 v[84:87], v60, s[36:39], s43 offen sc1\n\t"
      "buffer_load_dwordx4 v[88:91], v60, s[36:39], s43 offen offset:1024 sc1\n\t"
      "s_waitcnt lgkmcnt(8)\n\t"
      "v_mfma_f32_16x16x32_bf16 v[32:35], v[0:3], v[8:11], v[32:35]\n\t"
      "v_mfma_f32_16x16x32_bf16 v[36:39], v[4:7], v[8:11], v[36:39]\n\t"
      "v_mfma_f32_16x16x32_bf16 v[40:43], v[0:3], v[12:15], v[40:43]\n\t"
      "v_mfma_f32_16x16x32_bf16 v[44:47], v[4:7], v[12:15], v[44:47]\n\t"
      "buffer_load_dwordx4 v[92:95], v60, s[36:39], s68 offen sc1\n\t"
      "buffer_load_dwordx4 v[96:99], v60, s[36:39], s68 offen offset:1024 sc1\n\t"
      "v_mov_b64 v[48:49], 0\n\t"
      "v_mov_b64 v[50:51], 0\n\t"
      "v_mov_b64 v[52:53], 0\n\t"
      "v_mov_b64 v[54:55], 0\n\t"
      "s_xor_b32 s69, s97, 1024\n\t"
      "s_xor_b32 s98, s97, 512\n\t"
      "s_xor_b32 s23, s97, 1536\n\t"
      "s_waitcnt lgkmcnt(4)\n\t"
      "v_mfma_f32_16x16x32_bf16 v[32:35], v[16:19], v[24:27], v[32:35]\n\t"
      "v_mfma_f32_16x16x32_bf16 v[36:39], v[20:23], v[24:27], v[36:39]\n\t"
      "v_mfma_f32_16x16x32_bf16 v[40:43], v[16:19], v[28:31], v[40:43]\n\t"
      "v_mfma_f32_16x16x32_bf16 v[44:47], v[20:23], v[28:31], v[44:47]\n\t"
      "s_waitcnt lgkmcnt(0)\n\t"
      "v_mfma_f32_16x16x32_bf16 v[32:35], v[76:79], v[112:115], v[32:35]\n\t"
      "v_mfma_f32_16x16x32_bf16 v[36:39], v[80:83], v[112:115], v[36:39]\n\t"
      "v_mfma_f32_16x16x32_bf16 v[40:43], v[76:79], v[116:119], v[40:43]\n\t"
      "v_mfma_f32_16x16x32_bf16 v[44:47], v[80:83], v[116:119], v[44:47]\n\t"
      "s_mov_b64 exec, 1\n\t"
      "ds_write_b64 v109, v[110:111] offset:55392\n\t"
      "s_mov_b64 exec, -1\n\t"
      "s_nop 4\n\t"
      "v_cvt_pk_bf16_f32 v68, v32, v33\n\t"
      "v_cvt_pk_bf16_f32 v69, v34, v35\n\t"
      "v_cvt_pk_bf16_f32 v70, v36, v37\n\t"
      "v_cvt_pk_bf16_f32 v71, v38, v39\n\t"
      "v_cvt_pk_bf16_f32 v74, v40, v41\n\t"
      "v_cvt_pk_bf16_f32 v75, v42, v43\n\t"
      "v_cvt_pk_bf16_f32 v72, v44, v45\n\t"
      "v_cvt_pk_bf16_f32 v73, v46, v47\n\t"
      "v_mov_b64 v[32:33], 0\n\t"
      "v_mov_b64 v[34:35], 0\n\t"
      "v_mov_b64 v[36:37], 0\n\t"
      "v_mov_b64 v[38:39], 0\n\t"
      "v_mov_b64 v[40:41], 0\n\t"
      "v_mov_b64 v[42:43], 0\n\t"
      "v_mov_b64 v[44:45], 0\n\t"
      "v_mov_b64 v[46:47], 0\n\t"
      "LGO_3%=:\n\t"
      "s_nop 0\n\t"
      "s_waitcnt vmcnt(3)\n\t"
      "s_memtime s[88:89]\n\t"
      "v_pk_fma_f32 v[10:11], v[86:87], s[56:57], v[126:127] op_sel:[0,1,0] op_sel_hi:[1,1,0]\n\t"
      "v_pk_fma_f32 v[8:9], v[84:85], s[56:57], v[126:127] op_sel:[0,1,0] op_sel_hi:[1,1,0]\n\t"
      "v_cvt_pk_bf16_f32 v12, v8, v9\n\t"
      "v_cvt_pk_bf16_f32 v13, v10, v11\n\t"
      "s_nop 0\n\t"
      "ds_write_b64 v127, v[12:13] offset:44032\n\t"
      "s_waitcnt vmcnt(2)\n\t"
      "v_pk_fma_f32 v[4:5], v[88:89], s[56:57], v[126:127] op_sel:[0,1,0] op_sel_hi:[1,1,0]\n\t"
      "v_pk_fma_f32 v[6:7], v[90:91], s[56:57], v[126:127] op_sel:[0,1,0] op_sel_hi:[1,1,0]\n\t"
      "v_cvt_pk_bf16_f32 v14, v4, v5\n\t"
      "v_cvt_pk_bf16_f32 v15, v6, v7\n\t"
      "ds_write_b64 v127, v[14:15] offset:44040\n\t"
      "v_bitop3_b32 v64, v84, v85, v86 bitop3:0x80\n\t"
      "v_mfma_f32_16x16x32_bf16 v[56:59], v[12:15], v[12:15], 0\n\t"
      "v_bitop3_b32 v64, v64, v87, v88 bitop3:0x80\n\t"
      "v_bitop3_b32 v64, v64, v89, v90 bitop3:0x80\n\t"
      "v_bitop3_b32 v64, v64, v91, s58 bitop3:0x80\n\t"
      "v_cmp_eq_u32_e64 vcc, 0, v64\n\t"
      "v_smfmac_f32_16x16x64_bf16 v[36:39], v[12:15], a[88:95], v122\n\t"
      "s_nop 0\n\t"
      "v_bitop3_b32 v120, v120, v121, s53 bitop3:0x7e\n\t"
      "v_smfmac_f32_16x16x64_bf16 v[44:47], v[12:15], a[216:223], v122\n\t"
      "s_cbranch_vccnz LR0_3%=\n\t"
      "LC0_3%=:\n\t"
      "s_waitcnt lgkmcnt(0)\n\t"
      "s_barrier\n\t"
      "ds_read_b128 v[8:11], v127 offset:43008\n\t"
      "v_smfmac_f32_16x16x64_bf16 v[52:55], v[12:15], v[216:223], v122\n\t"
      "ds_read_b128 v[4:7], v127 offset:41984\n\t"
      "v_smfmac_f32_16x16x64_bf16 v[48:51], v[12:15], v[152:159], v122\n\t"
      "ds_read_b128 v[0:3], v127 offset:40960\n\t"
      "v_smfmac_f32_16x16x64_bf16 v[32:35], v[12:15], a[24:31], v122\n\t"
      "ds_read_b128 v[84:87], v127 offset:36864\n\t"
      "v_smfmac_f32_16x16x64_bf16 v[40:43], v[12:15], a[152:159], v122\n\t"
      "s_waitcnt lgkmcnt(3)\n\t"
      "v_smfmac_f32_16x16x64_bf16 v[32:35], v[8:11], a[16:23], v122\n\t"
      "ds_read_b128 v[80:83], v127 offset:35840\n\t"
      "v_smfmac_f32_16x16x64_bf16 v[40:43], v[8:11], a[144:151], v122\n\t"
      "s_waitcnt vmcnt(1)\n\t"
      "v_fma_f32 v27, v95, s57, v126\n\t"
      "v_fma_f32 v24, v92, s57, v126\n\t"
      "v_smfmac_f32_16x16x64_bf16 v[36:39], v[8:11], a[80:87], v122\n\t"
      "v_fma_f32 v26, v94, s57, v126\n\t"
      "v_cvt_pk_bf16_f32 v29, v26, v27\n\t"
      "v_bitop3_b32 v64, v92, v93, v94 bitop3:0x80\n\t"
      "v_smfmac_f32_16x16x64_bf16 v[44:47], v[8:11], a[208:215], v122\n\t"
      "s_waitcnt lgkmcnt(2)\n\t"
      "v_fma_f32 v25, v93, s57, v126\n\t"
      "v_cvt_pk_bf16_f32 v28, v24, v25\n\t"
      "v_smfmac_f32_16x16x64_bf16 v[36:39], v[4:7], a[72:79], v122\n\t"
      "s_waitcnt vmcnt(0)\n\t"
      "ds_write_b64 v127, v[28:29] offset:48128\n\t"
      "v_fma_f32 v23, v99, s57, v126\n\t"
      "v_fma_f32 v22, v98, s57, v126\n\t"
      "v_cvt_pk_bf16_f32 v31, v22, v23\n\t"
      "v_fma_f32 v21, v97, s57, v126\n\t"
      "v_fma_f32 v20, v96, s57, v126\n\t"
      "v_cvt_pk_bf16_f32 v30, v20, v21\n\t"
      "v_smfmac_f32_16x16x64_bf16 v[44:47], v[4:7], a[200:207], v122\n\t"
      "ds_write_b64 v127, v[30:31] offset:48136\n\t"
      "v_smfmac_f32_16x16x64_bf16 v[40:43], v[4:7], a[136:143], v122\n\t"
      "ds_read_b128 v[88:91], v127 offset:39936\n\t"
      "v_bitop3_b32 v64, v64, v95, v96 bitop3:0x80\n\t"
      "v_bitop3_b32 v64, v64, v97, v98 bitop3:0x80\n\t"
      "v_smfmac_f32_16x16x64_bf16 v[40:43], v[0:3], a[128:135], v122\n\t"
      "v_bitop3_b32 v64, v64, v99, s60 bitop3:0x80\n\t"
      "v_smfmac_f32_16x16x64_bf16 v[36:39], v[0:3], a[64:71], v122\n\t"
      "v_cmp_eq_u32_e64 vcc, 0, v64\n\t"
      "v_smfmac_f32_16x16x64_bf16 v[44:47], v[0:3], a[192:199], v122\n\t"
      "s_waitcnt lgkmcnt(1)\n\t"
      "v_smfmac_f32_16x16x64_bf16 v[32:35], v[4:7], a[8:15], v122\n\t"
      "s_cbranch_vccnz LR1_3%=\n\t"
      "LC1_3%=:\n\t"
      "v_smfmac_f32_16x16x64_bf16 v[40:43], v[28:31], a[184:191], v122\n\t"
      "s_barrier\n\t"
      "v_mfma_f32_16x16x32_bf16 v[56:59], v[28:31], v[28:31], v[56:59]\n\t"
      "ds_read_b128 v[24:27], v127 offset:47104\n\t"
      "s_nop 0\n\t"
      "v_smfmac_f32_16x16x64_bf16 v[32:35], v[0:3], a[0:7], v122\n\t"
      "ds_read_b128 v[20:23], v127 offset:46080\n\t"
      "v_smfmac_f32_16x16x64_bf16 v[44:47], v[28:31], a[248:255], v122\n\t"
      "ds_read_b128 v[16:19], v127 offset:45056\n\t"
      "v_max3_f32 v56, v56, v57, v58\n\t"
      "v_smfmac_f32_16x16x64_bf16 v[36:39], v[28:31], a[120:127], v122\n\t"
      "ds_read_b128 v[76:79], v127 offset:32768\n\t"
      "v_max_f32_e64 v56, v56, v59\n\t"
      "v_smfmac_f32_16x16x64_bf16 v[32:35], v[28:31], a[56:63], v122\n\t"
      "s_waitcnt lgkmcnt(3)\n\t"
      "v_readfirstlane_b32 s56, v120\n\t"
      "v_smfmac_f32_16x16x64_bf16 v[32:35], v[24:27], a[48:55], v122\n\t"
      "s_waitcnt lgkmcnt(2)\n\t"
      "v_max_f32_dpp v56, v56, v56 quad_perm:[1,0,3,2] row_mask:0xf bank_mask:0xf\n\t"
      "v_smfmac_f32_16x16x64_bf16 v[32:35], v[20:23], a[40:47], v122\n\t"
      "s_waitcnt lgkmcnt(1)\n\t"
      "v_max_f32_dpp v56, v56, v56 quad_perm:[2,3,0,1] row_mask:0xf bank_mask:0xf\n\t"
      "v_smfmac_f32_16x16x64_bf16 v[32:35], v[16:19], a[32:39], v122\n\t"
      "s_mov_b32 exec_lo, 0x100001\n\t"
      "s_mov_b32 exec_hi, 0x10000100\n\t"
      "v_smfmac_f32_16x16x64_bf16 v[36:39], v[24:27], a[112:119], v122\n\t"
      "ds_max_i32 v109, v56 offset:55392\n\t"
      "v_smfmac_f32_16x16x64_bf16 v[40:43], v[24:27], a[176:183], v122\n\t"
      "s_mov_b64 exec, -1\n\t"
      "v_add_f32 v33, v33, v35\n\t"
      "v_smfmac_f32_16x16x64_bf16 v[36:39], v[20:23], a[104:111], v122\n\t"
      "ds_read_b128 v[60:63], v127 offset:37888\n\t"
      "v_mul_f32_e64 v33, s59, v33\n\t"
      "v_smfmac_f32_16x16x64_bf16 v[36:39], v[16:19], a[96:103], v122\n\t"
      "v_add_f32_e64 v32, v32, v34\n\t"
      "v_smfmac_f32_16x16x64_bf16 v[44:47], v[24:27], a[240:247], v122\n\t"
      "ds_read_b128 v[56:59], v127 offset:38912\n\t"
      "v_mul_f32_e64 v32, s59, v32\n\t"
      "v_smfmac_f32_16x16x64_bf16 v[44:47], v[20:23], a[232:239], v122\n\t"
      "v_add_f32 v36, v36, v38\n\t"
      "v_add_f32 v37, v37, v39\n\t"
      "v_mul_f32_e64 v37, s59, v37\n\t"
      "v_smfmac_f32_16x16x64_bf16 v[40:43], v[20:23], a[168:175], v122\n\t"
      "ds_read_b128 v[92:95], v127 offset:34816\n\t"
      "v_cvt_pk_bf16_f32 v112, v32, v33\n\t"
      "v_smfmac_f32_16x16x64_bf16 v[44:47], v[16:19], a[224:231], v122\n\t"
      "ds_read_b128 v[96:99], v127 offset:33792\n\t"
      "v_mul_f32_e64 v36, s59, v36\n\t"
      "v_cvt_pk_bf16_f32 v113, v36, v37\n\t"
      "v_smfmac_f32_16x16x64_bf16 v[40:43], v[16:19], a[160:167], v122\n\t"
      "ds_write_b64 v125, v[112:113]\n\t"
      "v_smfmac_f32_16x16x64_bf16 v[48:51], v[8:11], v[144:151], v122\n\t"
      "s_nop 0\n\t"
      "v_add_f32 v45, v45, v47\n\t"
      "v_add_f32_e64 v44, v44, v46\n\t"
      "v_cvt_pk_bf16_f32 v121, v44, v45\n\t"
      "v_add_f32 v41, v41, v43\n\t"
      "v_add_f32 v40, v40, v42\n\t"
      "v_cvt_pk_bf16_f32 v120, v40, v41\n\t"
      "v_smfmac_f32_16x16x64_bf16 v[48:51], v[4:7], v[136:143], v122\n\t"
      "s_mov_b32 s58, 0x10000000\n\t"
      "ds_write_b64 v125, v[120:121] offset:2080\n\t"
      "v_smfmac_f32_16x16x64_bf16 v[48:51], v[0:3], v[128:135], v122\n\t"
      "s_waitcnt lgkmcnt(1)\n\t"
      "s_barrier\n\t"
      "ds_read_b64_tr_b16 v[36:37], v124 offset:50192\n\t"
      "v_smfmac_f32_16x16x64_bf16 v[48:51], v[28:31], v[184:191], v122\n\t"
      "ds_read_b32 v108, v109 offset:55392\n\t"
      "ds_read_b64_tr_b16 v[32:33], v124 offset:49152\n\t"
      "v_smfmac_f32_16x16x64_bf16 v[52:55], v[8:11], v[208:215], v122\n\t"
      "ds_read_b64_tr_b16 v[34:35], v124 offset:49672\n\t"
      "s_waitcnt lgkmcnt(4)\n\t"
      "ds_read_b64_tr_b16 v[38:39], v124 offset:50712\n\t"
      "v_smfmac_f32_16x16x64_bf16 v[48:51], v[24:27], v[176:183], v122\n\t"
      "s_barrier\n\t"
      "ds_read_b64_tr_b16 v[46:47], v124 offset:52792\n\t"
      "v_smfmac_f32_16x16x64_bf16 v[48:51], v[20:23], v[168:175], v122\n\t"
      "s_waitcnt lgkmcnt(4)\n\t"
      "ds_read_b64_tr_b16 v[44:45], v124 offset:52272\n\t"
      "v_readfirstlane_b32 s61, v108\n\t"
      "v_smfmac_f32_16x16x64_bf16 v[48:51], v[16:19], v[160:167], v122\n\t"
      "s_waitcnt lgkmcnt(3)\n\t"
      "ds_read_b64_tr_b16 v[42:43], v124 offset:51752\n\t"
      "ds_read_b64_tr_b16 v[40:41], v124 offset:51232\n\t"
      "s_waitcnt lgkmcnt(4)\n\t"
      "v_mfma_f32_16x16x32_bf16 v[112:115], v[76:79], v[32:35], 0\n\t"
      "s_lshr_b32 s61, s61, 23\n\t"
      "s_add_u32 s61, s61, s67\n\t"
      "v_mov_b32_dpp v102, v34 quad_perm:[2,3,0,1] row_mask:0xf bank_mask:0xf\n\t"
      "v_mov_b32_dpp v106, v38 quad_perm:[2,3,0,1] row_mask:0xf bank_mask:0xf\n\t"
      "s_max_u32 s61, s61, s66\n\t"
      "v_mfma_f32_16x16x32_bf16 v[116:119], v[80:83], v[32:35], 0\n\t"
      "s_lshr_b32 s61, s61, 1\n\t"
      "v_add_f32_e64 v48, v48, v50\n\t"
      "v_mov_b32_dpp v100, v32 quad_perm:[2,3,0,1] row_mask:0xf bank_mask:0xf\n\t"
      "v_mfma_f32_16x16x32_bf16 v[116:119], v[88:91], v[36:39], v[116:119]\n\t"
      "s_min_u32 s61, s61, 200\n\t"
      "v_mov_b32_dpp v107, v39 quad_perm:[2,3,0,1] row_mask:0xf bank_mask:0xf\n\t"
      "s_waitcnt lgkmcnt(0)\n\t"
      "v_mfma_f32_16x16x32_bf16 v[76:79], v[40:43], v[32:35], 0\n\t"
      "buffer_load_dwordx4 v127, s[36:39], s46 offen sc1 lds\n\t"
      "s_lshl_b32 s61, s61, 23\n\t"
      "v_mfma_f32_16x16x32_bf16 v[76:79], v[44:47], v[36:39], v[76:79]\n\t"
      "s_add_u32 s65, s61, 33554432\n\t"
      "v_mov_b32_dpp v104, v36 quad_perm:[2,3,0,1] row_mask:0xf bank_mask:0xf\n\t"
      "v_mov_b32_dpp v103, v35 quad_perm:[2,3,0,1] row_mask:0xf bank_mask:0xf\n\t"
      "v_mov_b32_dpp v101, v33 quad_perm:[2,3,0,1] row_mask:0xf bank_mask:0xf\n\t"
      "v_mfma_f32_16x16x32_bf16 v[112:115], v[84:87], v[36:39], v[112:115]\n\t"
      "s_pack_hh_b32_b16 s75, s65, s65\n\t"
      "v_add_f32 v49, v49, v51\n\t"
      "v_cvt_pk_bf16_f32 v64, v48, v49\n\t"
      "v_cndmask_b32_e64 v120, v76, v78, s[72:73]\n\t"
      "v_cndmask_b32_e64 v121, v77, v79, s[72:73]\n\t"
      "v_mov_b32_dpp v105, v37 quad_perm:[2,3,0,1] row_mask:0xf bank_mask:0xf\n\t"
      "v_mfma_f32_16x16x32_bf16 v[112:115], v[92:95], v[100:103], v[112:115]\n\t"
      "buffer_load_dwordx4 v127, s[36:39], s46 offen offset:1024 sc1 lds\n\t"
      "v_mfma_f32_16x16x32_bf16 v[116:119], v[96:99], v[100:103], v[116:119]\n\t"
      "s_pack_hh_b32_b16 s74, s65, s65\n\t"
      "v_add_f32_dpp v121, v121, v121 quad_perm:[2,3,0,1] row_mask:0xf bank_mask:0xf\n\t"
      "v_add_f32_dpp v120, v120, v120 quad_perm:[2,3,0,1] row_mask:0xf bank_mask:0xf\n\t"
      "v_mfma_f32_16x16x32_bf16 v[112:115], v[56:59], v[104:107], v[112:115]\n\t"
      "v_exp_f32 v81, v121\n\t"
      "v_mfma_f32_16x16x32_bf16 v[116:119], v[60:63], v[104:107], v[116:119]\n\t"
      "s_mov_b32 m0, s45\n\t"
      "v_mov_b64 v[90:91], s[74:75]\n\t"
      "v_exp_f32 v80, v120\n\t"
      "v_mov_b64 v[88:89], s[74:75]\n\t"
      "v_cvt_pk_bf16_f32 v108, v80, v81\n\t"
      "v_smfmac_f32_16x16x64_bf16 v[52:55], v[4:7], v[200:207], v122\n\t"
      "buffer_load_dwordx4 v65, s[36:39], s44 offen offset:1024 sc1 lds\n\t"
      "v_mfma_f32_16x16x32_bf16 v[84:87], v[108:111], v[64:67], 0\n\t"
      "s_add_u32 s57, s61, 310378496\n\t"
      "v_cndmask_b32_e64 v112, v112, v116, s[72:73]\n\t"
      "v_exp_f32_e64 v56, v112\n\t"
      "v_mfma_f32_16x16x32_bf16 v[48:51], v[108:111], v[88:91], 0\n\t"
      "s_add_u32 s43, s43, 0x8000\n\t"
      "v_cndmask_b32_e64 v113, v113, v117, s[72:73]\n\t"
      "v_cndmask_b32_e64 v114, v114, v118, s[72:73]\n\t"
      "v_exp_f32_e64 v58, v114\n\t"
      "v_cndmask_b32_e64 v115, v115, v119, s[72:73]\n\t"
      "v_exp_f32 v57, v113\n\t"
      "v_exp_f32 v59, v115\n\t"
      "v_cvt_pk_bf16_f32 v96, v56, v57\n\t"
      "v_cvt_pk_bf16_f32 v97, v58, v59\n\t"
      "v_max3_f32 v82, |v112|, |v113|, |v114|\n\t"
      "v_mov_b32_dpp v98, v96 quad_perm:[2,3,0,1] row_mask:0xf bank_mask:0xf\n\t"
      "v_mov_b32_dpp v99, v97 quad_perm:[2,3,0,1] row_mask:0xf bank_mask:0xf\n\t"
      "v_smfmac_f32_16x16x64_bf16 v[52:55], v[0:3], v[192:199], v122\n\t"
      "v_max3_f32 v83, |v115|, |v120|, |v121|\n\t"
      "v_mfma_f32_16x16x32_bf16 v[48:51], v[96:99], v[88:91], v[48:51]\n\t"
      "v_max_f32_e64 v82, v82, v83\n\t"
      "v_cmp_nge_f32 vcc, 0x42000000, v82\n\t"
      "v_mfma_f32_16x16x32_bf16 v[84:87], v[96:99], v[68:71], v[84:87]\n\t"
      "buffer_load_dwordx4 v65, s[36:39], s44 offen sc1 lds\n\t"
      "v_smfmac_f32_16x16x64_bf16 v[52:55], v[28:31], v[248:255], v122\n\t"
      "s_add_u32 s68, s68, 0x8000\n\t"
      "s_cbranch_vccnz LMX_3%=\n\t"
      "v_rcp_f32 v49, v49\n\t"
      "v_rcp_f32_e64 v48, v48\n\t"
      "v_smfmac_f32_16x16x64_bf16 v[52:55], v[24:27], v[240:247], v122\n\t"
      "v_fma_f32 v85, v85, v49, s96\n\t"
      "v_fma_f32 v84, v84, v48, s96\n\t"
      "v_smfmac_f32_16x16x64_bf16 v[52:55], v[20:23], v[232:239], v122\n\t"
      "buffer_atomic_add v84, v123, s[36:39], s97 offen\n\t"
      "buffer_atomic_add v85, v123, s[36:39], s98 offen\n\t"
      "v_smfmac_f32_16x16x64_bf16 v[52:55], v[16:19], v[224:231], v122\n\t"
      "v_mfma_f32_16x16x32_bf16 v[92:95], v[96:99], v[72:75], 0\n\t"
      "s_nop 3\n\t"
      "v_add_f32 v53, v53, v55\n\t"
      "v_add_f32_e64 v52, v52, v54\n\t"
      "v_cvt_pk_bf16_f32 v66, v52, v53\n\t"
      "s_add_u32 s62, s61, 0x83400000\n\t"
      "v_mov_b32_e64 v126, s62\n\t"
      "v_mfma_f32_16x16x32_bf16 v[92:95], v[108:111], v[66:69], v[92:95]\n\t"
      "s_add_u32 s97, s97, 0x8000\n\t"
      "s_nop 6\n\t"
      "v_fma_f32 v95, v95, v49, s96\n\t"
      "buffer_atomic_add v95, v123, s[36:39], s23 offen\n\t"
      "v_fma_f32 v94, v94, v48, s96\n\t"
      "buffer_atomic_add v94, v123, s[36:39], s69 offen\n\t"
      "LWIN_3%=:\n\t"
      "v_add_u32 v100, s55, v127\n\t"
      "ds_read_b128 v[60:63], v100 offset:57344\n\t"
      "s_cmp_eq_u32 s56, 0\n\t"
      "s_cbranch_scc0 LSLOW_3%=\n\t"
      "LWIN1_3%=:\n\t"
      "buffer_load_dwordx2 v[120:121], off, s[36:39], s50 sc1\n\t"
      "s_mov_b32 s64, s50\n\t"
      "s_bitcmp1_b64 s[84:85], s40\n\t"
      "s_cbranch_scc1 LEARLY_3%=\n\t"
      "LROLE_3%=:\n\t"
      "v_add_u32 v52, 67584, v127\n\t"
      "ds_read_b128 v[88:91], v52 offset:16384\n\t"
      "ds_read_b128 v[92:95], v52 offset:17408\n\t"
      "ds_read_b128 v[32:35], v52 offset:20480\n\t"
      "ds_read_b128 v[36:39], v52 offset:21504\n\t"
      "ds_read_b128 v[40:43], v52 offset:24576\n\t"
      "ds_read_b128 v[44:47], v52 offset:25600\n\t"
      "v_mov_b64 v[48:49], 0\n\t"
      "v_mov_b64 v[50:51], 0\n\t"
      "v_add_u32 v104, s71, v100\n\t"
      "ds_read_b128 v[76:79], v104\n\t"
      "ds_read_b128 v[80:83], v104 offset:1024\n\t"
      "ds_read_b128 v[112:115], v100\n\t"
      "ds_read_b128 v[116:119], v100 offset:1024\n\t"
      "v_xor_b32 v101, 2048, v104\n\t"
      "v_xor_b32 v102, 4096, v104\n\t"
      "v_xor_b32 v103, 6144, v104\n\t"
      "s_waitcnt lgkmcnt(8)\n\t"
      "v_smfmac_f32_16x16x64_bf16 v[48:51], v[16:19], v[88:95], v122\n\t"
      "ds_read_b128 v[88:91], v52 offset:28672\n\t"
      "ds_read_b128 v[92:95], v52 offset:29696\n\t"
      "s_sub_u32 s61, s88, s78\n\t"
      "s_sub_i32 s61, s61, 556\n\t"
      "s_waitcnt lgkmcnt(8)\n\t"
      "v_smfmac_f32_16x16x64_bf16 v[48:51], v[20:23], v[32:39], v122\n\t"
      "ds_read_b128 v[0:3], v101\n\t"
      "ds_read_b128 v[4:7], v101 offset:1024\n\t"
      "s_lshr_b32 s62, s25, 3\n\t"
      "s_sub_u32 s25, s25, s62\n\t"
      "s_waitcnt lgkmcnt(8)\n\t"
      "v_smfmac_f32_16x16x64_bf16 v[48:51], v[24:27], v[40:47], v122\n\t"
      "ds_read_b128 v[8:11], v100 offset:8192\n\t"
      "ds_read_b128 v[12:15], v100 offset:9216\n\t"
      "s_sub_i32 s25, s25, 24\n\t"
      "s_max_i32 s25, s25, 0\n\t"
      "s_waitcnt lgkmcnt(4)\n\t"
      "v_smfmac_f32_16x16x64_bf16 v[48:51], v[28:31], v[88:95], v122\n\t"
      "s_sub_i32 s62, s61, 48\n\t"
      "s_max_i32 s62, s62, 0\n\t"
      "ds_read_b128 v[16:19], v102\n\t"
      "ds_read_b128 v[20:23], v102 offset:1024\n\t"
      "ds_read_b128 v[24:27], v100 offset:16384\n\t"
      "ds_read_b128 v[28:31], v100 offset:17408\n\t"
      "s_min_i32 s62, s62, 320\n\t"
      "s_lshr_b32 s62, s62, 2\n\t"
      "s_add_u32 s25, s25, s62\n\t"
      "s_min_i32 s61, s61, 48\n\t"
      "s_max_i32 s61, s61, -48\n\t"
      "s_ashr_i32 s61, s61, 2\n\t"
      "s_add_u32 s26, s26, s61\n\t"
      "s_max_u32 s26, s26, 2100\n\t"
      "s_min_u32 s26, s26, 2900\n\t"
      "s_sub_u32 s62, s88, s27\n\t"
      "s_mov_b32 s27, s88\n\t"
      "s_sub_i32 s62, s62, s24\n\t"
      "s_cmp_gt_i32 s62, 1150\n\t"
      "s_cselect_b32 s62, 200, 0\n\t"
      "s_max_u32 s25, s25, s62\n\t"
      "s_add_u32 s24, s26, s25\n\t"
      "s_add_u32 m0, s55, s71\n\t"
      "s_sub_u32 s71, 204800, s71\n\t"
      "s_add_u32 s40, s40, 1\n\t"
      "s_cmp_lt_u32 s40, 63\n\t"
      "s_cselect_b32 s61, 786432, 0\n\t"
      "s_cselect_b32 s60, s58, 0\n\t"
      "s_add_u32 s44, s44, s61\n\t"
      "s_cmp_lt_u32 s40, 62\n\t"
      "s_cselect_b32 s61, 786432, 0\n\t"
      "s_add_u32 s46, s46, s61\n\t"
      "s_add_u32 s61, s40, 3\n\t"
      "s_and_b32 s62, s61, 3\n\t"
      "s_cselect_b32 s62, 16, 2000\n\t"
      "s_cmp_lt_u32 s61, 64\n\t"
      "s_cselect_b32 s62, s62, 0\n\t"
      "s_add_u32 s50, s50, s62\n\t"
      "s_mov_b32 s63, 0x2000\n\t"
      "s_cmp_eq_u32 s40, 64\n\t"
      "v_add_f32 v48, v48, v50\n\t"
      "v_add_f32 v49, v49, v51\n\t"
      "ds_write_b64 v124, v[48:49] offset:53832\n\t"
      "s_cbranch_scc1 LFIN_3%=\n\t"
      "v_mfma_f32_16x16x32_bf16 v[32:35], v[76:79], v[112:115], 0\n\t"
      "v_mfma_f32_16x16x32_bf16 v[36:39], v[80:83], v[112:115], 0\n\t"
      "v_mfma_f32_16x16x32_bf16 v[40:43], v[76:79], v[116:119], 0\n\t"
      "v_mfma_f32_16x16x32_bf16 v[44:47], v[80:83], v[116:119], 0\n\t"
      "ds_read_b128 v[76:79], v103\n\t"
      "ds_read_b128 v[80:83], v103 offset:1024\n\t"
      "ds_read_b128 v[112:115], v100 offset:24576\n\t"
      "ds_read_b128 v[116:119], v100 offset:25600\n\t"
      "LGATE_3%=:\n\t"
      "s_memtime s[92:93]\n\t"
      "s_waitcnt lgkmcnt(0)\n\t"
      "s_sub_u32 s61, s92, s88\n\t"
      "s_sub_i32 s61, s24, s61\n\t"
      "s_sub_i32 s61, s61, 70\n\t"
      "s_min_i32 s61, s61, 3000\n\t"
      "s_getpc_b64 s[92:93]\n\t"
      "LDPC_3%=:\n\t"
      "s_max_i32 s61, s61, 0\n\t"
      "s_andn2_b32 s62, 28, s61\n\t"
      "s_add_u32 s62, s62, LDSL_3%=-LDPC_3%=\n\t"
      "s_add_u32 s92, s92, s62\n\t"
      "s_addc_u32 s93, s93, 0\n\t"
      "LDLY_3%=:\n\t"
      "s_sub_i32 s61, s61, 32\n\t"
      "s_cmp_ge_i32 s61, 0\n\t"
      "s_cbranch_scc1 LDLY_3%=\n\t"
      "s_setpc_b64 s[92:93]\n\t"
      "LDSL_3%=:\n\t"
      "s_nop 0\n\t"
      "s_nop 0\n\t"
      "s_nop 0\n\t"
      "s_nop 0\n\t"
      "s_nop 0\n\t"
      "s_nop 0\n\t"
      "s_nop 0\n\t"
      "LBAR_3%=:\n\t"
      "s_cmp_gt_u32 s40, 1\n\t"
      "s_barrier\n\t"
      "s_cbranch_scc1 LPOLL_3%=\n\t"
      "s_branch LFIRST_3%=\n\t"
      "LUTOK_0%=:\n\t"
      "v_add_u32 v104, s71, v100\n\t"
      "ds_read_b128 v[76:79], v104\n\t"
      "ds_read_b128 v[80:83], v104 offset:1024\n\t"
      "ds_read_b128 v[112:115], v100\n\t"
      "ds_read_b128 v[116:119], v100 offset:1024\n\t"
      "v_xor_b32 v101, 2048, v104\n\t"
      "v_xor_b32 v102, 4096, v104\n\t"
      "v_xor_b32 v103, 6144, v104\n\t"
      "ds_read_b128 v[16:19], v101\n\t"
      "ds_read_b128 v[20:23], v101 offset:1024\n\t"
      "ds_read_b128 v[24:27], v100 offset:8192\n\t"
      "ds_read_b128 v[28:31], v100 offset:9216\n\t"
      "ds_read_b128 v[0:3], v102\n\t"
      "ds_read_b128 v[4:7], v102 offset:1024\n\t"
      "ds_read_b128 v[8:11], v100 offset:16384\n\t"
      "ds_read_b128 v[12:15], v100 offset:17408\n\t"
      "s_mov_b32 s28, s52\n\t"
      "s_add_u32 m0, s55, s71\n\t"
      "s_sub_u32 s71, 204800, s71\n\t"
      "s_add_u32 s40, s40, 1\n\t"
      "s_cmp_lt_u32 s40, 63\n\t"
      "s_cselect_b32 s61, 786432, 0\n\t"
      "s_cselect_b32 s60, s58, 0\n\t"
      "s_add_u32 s44, s44, s61\n\t"
      "s_cmp_lt_u32 s40, 62\n\t"
      "s_cselect_b32 s61, 786432, 0\n\t"
      "s_add_u32 s46, s46, s61\n\t"
      "s_add_u32 s61, s40, 3\n\t"
      "s_and_b32 s62, s61, 3\n\t"
      "s_cselect_b32 s62, 16, 2000\n\t"
      "s_cmp_lt_u32 s61, 64\n\t"
      "s_cselect_b32 s62, s62, 0\n\t"
      "s_add_u32 s50, s50, s62\n\t"
      "s_mov_b32 s63, 0x2000\n\t"
      "s_waitcnt lgkmcnt(8)\n\t"
      "v_mfma_f32_16x16x32_bf16 v[32:35], v[76:79], v[112:115], 0\n\t"
      "v_mfma_f32_16x16x32_bf16 v[36:39], v[80:83], v[112:115], 0\n\t"
      "v_mfma_f32_16x16x32_bf16 v[40:43], v[76:79], v[116:119], 0\n\t"
      "v_mfma_f32_16x16x32_bf16 v[44:47], v[80:83], v[116:119], 0\n\t"
      "ds_read_b128 v[76:79], v103\n\t"
      "ds_read_b128 v[80:83], v103 offset:1024\n\t"
      "ds_read_b128 v[112:115], v100 offset:24576\n\t"
      "ds_read_b128 v[116:119], v100 offset:25600\n\t"
      "s_waitcnt lgkmcnt(8)\n\t"
      "v_mfma_f32_16x16x32_bf16 v[32:35], v[16:19], v[24:27], v[32:35]\n\t"
      "v_mfma_f32_16x16x32_bf16 v[36:39], v[20:23], v[24:27], v[36:39]\n\t"
      "v_mfma_f32_16x16x32_bf16 v[40:43], v[16:19], v[28:31], v[40:43]\n\t"
      "v_mfma_f32_16x16x32_bf16 v[44:47], v[20:23], v[28:31], v[44:47]\n\t"
      "s_branch LGATE_0%=\n\t"
      "LFIRST_0%=:\n\t"
      "s_cmp_eq_u32 s40, 0\n\t"
      "s_cbranch_scc1 LWM0_0%=\n\t"
      "s_barrier\n\t"
      "ds_read_b32 v99, v109 offset:55392\n\t"
      "s_waitcnt lgkmcnt(0)\n\t"
      "v_readfirstlane_b32 s61, v99\n\t"
      "s_nop 1\n\t"
      "s_cmp_gt_i32 s61, 0\n\t"
      "s_cselect_b32 s99, 0x2000, 0\n\t"
      "s_xor_b32 s97, s97, s99\n\t"
      "s_barrier\n\t"
      "s_mov_b32 s63, 0x2000\n\t"
      "s_branch LPOLL_0%=\n\t"
      "LWM0_0%=:\n\t"
      "s_mov_b32 s63, 0x10000\n\t"
      "LWM_0%=:\n\t"
      "s_waitcnt vmcnt(0) lgkmcnt(0)\n\t"
      "v_bitop3_b32 v49, v49, v51, s53 bitop3:0x7e\n\t"
      "v_max_u32 v48, v48, v50\n\t"
      "v_cmp_ne_u32 vcc, 0, v49\n\t"
      "s_nop 1\n\t"
      "v_max_u32_dpp v48, v48, v48 quad_perm:[1,0,3,2] row_mask:0xf bank_mask:0xf\n\t"
      "s_nop 1\n\t"
      "v_max_u32_dpp v48, v48, v48 quad_perm:[2,3,0,1] row_mask:0xf bank_mask:0xf\n\t"
      "s_nop 1\n\t"
      "v_max_u32_dpp v48, v48, v48 row_ror:4 row_mask:0xf bank_mask:0xf\n\t"
      "s_nop 1\n\t"
      "v_max_u32_dpp v48, v48, v48 row_ror:8 row_mask:0xf bank_mask:0xf\n\t"
      "v_mov_b32 v49, v48\n\t"
      "s_nop 1\n\t"
      "v_permlane16_swap_b32 v48, v49\n\t"
      "v_max_u32 v48, v48, v49\n\t"
      "v_mov_b32 v49, v48\n\t"
      "s_nop 1\n\t"
      "v_permlane32_swap_b32 v48, v49\n\t"
      "v_max_u32 v48, v48, v49\n\t"
      "v_mul_f32 v49, 0x40a00000, v48\n\t"
      "s_nop 0\n\t"
      "v_readfirstlane_b32 s67, v49\n\t"
      "v_mul_f32 v49, 0x43030000, v49\n\t"
      "s_nop 0\n\t"
      "v_readfirstlane_b32 s66, v49\n\t"
      "s_cbranch_vccz LWM1_0%=\n\t"
      "s_sleep 4\n\t"
      "buffer_load_dwordx2 v[48:49], v52, s[36:39], s94 offen sc1\n\t"
      "buffer_load_dwordx2 v[50:51], v52, s[36:39], s94 offen offset:128 sc1\n\t"
      "s_sub_u32 s63, s63, 1\n\t"
      "s_cmp_lg_u32 s63, 0\n\t"
      "s_cbranch_scc1 LWM_0%=\n\t"
      "s_waitcnt vmcnt(0)\n\t"
      "LWM1_0%=:\n\t"
      "buffer_load_dwordx2 v[120:121], off, s[36:39], s64 sc1\n\t"
      "s_lshr_b32 s67, s67, 23\n\t"
      "s_add_u32 s67, s67, 5\n\t"
      "s_lshr_b32 s66, s66, 23\n\t"
      "s_add_u32 s66, s66, 129\n\t"
      "s_max_u32 s66, s66, 80\n\t"
      "s_mov_b32 s63, 0x2000\n\t"
      "LPOL0_0%=:\n\t"
      "s_waitcnt lgkmcnt(4)\n\t"
      "v_mfma_f32_16x16x32_bf16 v[32:35], v[0:3], v[8:11], v[32:35]\n\t"
      "v_mfma_f32_16x16x32_bf16 v[36:39], v[4:7], v[8:11], v[36:39]\n\t"
      "v_mfma_f32_16x16x32_bf16 v[40:43], v[0:3], v[12:15], v[40:43]\n\t"
      "v_mfma_f32_16x16x32_bf16 v[44:47], v[4:7], v[12:15], v[44:47]\n\t"
      "v_mov_b64 v[48:49], 0\n\t"
      "v_mov_b64 v[50:51], 0\n\t"
      "v_mov_b64 v[52:53], 0\n\t"
      "v_mov_b64 v[54:55], 0\n\t"
      "s_xor_b32 s69, s97, 1024\n\t"
      "s_xor_b32 s98, s97, 512\n\t"
      "s_xor_b32 s23, s97, 1536\n\t"
      "s_waitcnt lgkmcnt(0)\n\t"
      "v_mfma_f32_16x16x32_bf16 v[32:35], v[76:79], v[112:115], v[32:35]\n\t"
      "v_mfma_f32_16x16x32_bf16 v[36:39], v[80:83], v[112:115], v[36:39]\n\t"
      "v_mfma_f32_16x16x32_bf16 v[40:43], v[76:79], v[116:119], v[40:43]\n\t"
      "v_mfma_f32_16x16x32_bf16 v[44:47], v[80:83], v[116:119], v[44:47]\n\t"
      "s_mov_b64 exec, 1\n\t"
      "ds_write_b64 v109, v[110:111] offset:55392\n\t"
      "s_mov_b64 exec, -1\n\t"
      "s_nop 4\n\t"
      "v_cvt_pk_bf16_f32 v68, v32, v33\n\t"
      "v_cvt_pk_bf16_f32 v69, v34, v35\n\t"
      "v_cvt_pk_bf16_f32 v70, v36, v37\n\t"
      "v_cvt_pk_bf16_f32 v71, v38, v39\n\t"
      "v_cvt_pk_bf16_f32 v74, v40, v41\n\t"
      "v_cvt_pk_bf16_f32 v75, v42, v43\n\t"
      "v_cvt_pk_bf16_f32 v72, v44, v45\n\t"
      "v_cvt_pk_bf16_f32 v73, v46, v47\n\t"
      "v_mov_b64 v[32:33], 0\n\t"
      "v_mov_b64 v[34:35], 0\n\t"
      "v_mov_b64 v[36:37], 0\n\t"
      "v_mov_b64 v[38:39], 0\n\t"
      "v_mov_b64 v[40:41], 0\n\t"
      "v_mov_b64 v[42:43], 0\n\t"
      "v_mov_b64 v[44:45], 0\n\t"
      "v_mov_b64 v[46:47], 0\n\t"
      "s_waitcnt vmcnt(0)\n\t"
      "s_branch LGO_0%=\n\t"
      "LEARLY_0%=:\n\t"
      "s_cmp_eq_u32 s40, 54\n\t"
      "s_cbranch_scc1 LPW_0%=\n\t"
      "s_cmp_lt_i32 s40, 0\n\t"
      "s_waitcnt lgkmcnt(0)\n\t"
      "s_cbranch_scc1 LUTOK_0%=\n\t"
      "s_xor_b32 s43, s43, s99\n\t"
      "s_xor_b32 s68, s68, s99\n\t"
      "s_branch LROLE_0%=\n\t"
      "LPW_0%=:\n\t"
      "s_and_b32 s61, s51, 0x70c\n\t"
      "s_cmp_lg_u32 s61, 0x100\n\t"
      "s_cbranch_scc1 LROLE_0%=\n\t"
      "s_getpc_b64 s[92:93]\n\t"
      "LPX_0%=:\n\t"
      "s_add_u32 s92, s92, LCOLD%=-LPX_0%=+0\n\t"
      "s_addc_u32 s93, s93, (LCOLD%=-LPX_0%=+0)>>32\n\t"
      "v_lshlrev_b32 v99, 2, v127\n\t"
      "global_load_dword v98, v99, s[92:93]\n\t"
      "v_add_u32 v99, 4096, v99\n\t"
      "global_load_dword v98, v99, s[92:93]\n\t"
      "v_add_u32 v99, 4096, v99\n\t"
      "s_branch LROLE_0%=\n\t"
      "LSLOW_0%=:\n\t"
      "s_mov_b32 s63, 0x10000\n\t"
      "LSLOW1_0%=:\n\t"
      "buffer_load_dwordx2 v[92:93], off, s[36:39], s64 sc1\n\t"
      "s_waitcnt vmcnt(0)\n\t"
      "v_bitop3_b32 v92, v92, v93, s53 bitop3:0x7e\n\t"
      "s_nop 0\n\t"
      "v_readfirstlane_b32 s56, v92\n\t"
      "s_nop 1\n\t"
      "s_cmp_eq_u32 s56, 0\n\t"
      "s_cbranch_scc1 LSLOW2_0%=\n\t"
      "s_sleep 8\n\t"
      "s_sub_u32 s63, s63, 1\n\t"
      "s_cmp_lg_u32 s63, 0\n\t"
      "s_cbranch_scc1 LSLOW1_0%=\n\t"
      "LSLOW2_0%=:\n\t"
      "s_mov_b32 m0, s45\n\t"
      "s_sub_u32 s61, 204800, s71\n\t"
      "buffer_load_dwordx4 v65, s[36:39], s44 offen sc1 lds\n\t"
      "buffer_load_dwordx4 v65, s[36:39], s44 offen offset:1024 sc1 lds\n\t"
      "s_add_u32 m0, s55, s61\n\t"
      "s_nop 0\n\t"
      "buffer_load_dwordx4 v127, s[36:39], s46 offen sc1 lds\n\t"
      "buffer_load_dwordx4 v127, s[36:39], s46 offen offset:1024 sc1 lds\n\t"
      "s_branch LWIN1_0%=\n\t"
      "LMX_0%=:\n\t"
      "s_waitcnt lgkmcnt(0)\n\t"
      "s_nop 7\n\t"
      "v_smfmac_f32_16x16x64_bf16 v[52:55], v[20:23], v[232:239], v122\n\t"
      "v_smfmac_f32_16x16x64_bf16 v[52:55], v[24:27], v[240:247], v122\n\t"
      "v_smfmac_f32_16x16x64_bf16 v[52:55], v[28:31], v[248:255], v122\n\t"
      "s_nop 7\n\t"
      "v_add_f32 v53, v53, v55\n\t"
      "v_add_f32_e64 v52, v52, v54\n\t"
      "v_cvt_pk_bf16_f32 v66, v52, v53\n\t"
      "s_add_u32 s62, s61, 0x83400000\n\t"
      "v_mov_b32_e64 v126, s62\n\t"
      "v_max3_f32 v82, v112, v113, v114\n\t"
      "v_max_f32 v82, v82, v115\n\t"
      "s_nop 1\n\t"
      "v_max_f32_dpp v82, v82, v82 quad_perm:[2,3,0,1] row_mask:0xf bank_mask:0xf\n\t"
      "v_max3_f32 v83, v82, v120, v121\n\t"
      "v_max3_f32 v60, v82, v120, v121\n\t"
      "s_nop 1\n\t"
      "v_permlane16_swap_b32 v83, v60\n\t"
      "v_max_f32 v61, v83, v60\n\t"
      "v_max_f32 v60, v83, v60\n\t"
      "s_nop 1\n\t"
      "v_permlane32_swap_b32 v61, v60\n\t"
      "v_max_f32 v83, v61, v60\n\t"
      "v_sub_f32 v112, v112, v83\n\t"
      "v_sub_f32 v113, v113, v83\n\t"
      "v_sub_f32 v114, v114, v83\n\t"
      "v_sub_f32 v115, v115, v83\n\t"
      "v_sub_f32 v120, v120, v83\n\t"
      "v_sub_f32 v121, v121, v83\n\t"
      "v_exp_f32_e64 v56, v112\n\t"
      "v_exp_f32 v57, v113\n\t"
      "v_exp_f32_e64 v58, v114\n\t"
      "v_exp_f32 v59, v115\n\t"
      "v_exp_f32 v80, v120\n\t"
      "v_exp_f32 v81, v121\n\t"
      "v_cvt_pk_bf16_f32 v96, v56, v57\n\t"
      "v_cvt_pk_bf16_f32 v97, v58, v59\n\t"
      "v_cvt_pk_bf16_f32 v108, v80, v81\n\t"
      "v_mov_b32_dpp v98, v96 quad_perm:[2,3,0,1] row_mask:0xf bank_mask:0xf\n\t"
      "v_mov_b32_dpp v99, v97 quad_perm:[2,3,0,1] row_mask:0xf bank_mask:0xf\n\t"
      "v_mfma_f32_16x16x32_bf16 v[48:51], v[108:111], v[88:91], 0\n\t"
      "s_nop 0\n\t"
      "v_mfma_f32_16x16x32_bf16 v[48:51], v[96:99], v[88:91], v[48:51]\n\t"
      "v_mfma_f32_16x16x32_bf16 v[84:87], v[108:111], v[64:67], 0\n\t"
      "v_mfma_f32_16x16x32_bf16 v[84:87], v[96:99], v[68:71], v[84:87]\n\t"
      "v_mfma_f32_16x16x32_bf16 v[92:95], v[96:99], v[72:75], 0\n\t"
      "v_mfma_f32_16x16x32_bf16 v[92:95], v[108:111], v[66:69], v[92:95]\n\t"
      "s_nop 3\n\t"
      "v_rcp_f32_e64 v48, v48\n\t"
      "v_rcp_f32 v49, v49\n\t"
      "v_fma_f32 v84, v84, v48, s96\n\t"
      "buffer_atomic_add v84, v123, s[36:39], s97 offen\n\t"
      "v_fma_f32 v85, v85, v49, s96\n\t"
      "buffer_atomic_add v85, v123, s[36:39], s98 offen\n\t"
      "v_fma_f32 v94, v94, v48, s96\n\t"
      "buffer_atomic_add v94, v123, s[36:39], s69 offen\n\t"
      "v_fma_f32 v95, v95, v49, s96\n\t"
      "buffer_atomic_add v95, v123, s[36:39], s23 offen\n\t"
      "s_add_u32 s97, s97, 0x8000\n\t"
      "s_branch LWIN_0%=\n\t"
      "LR0_0%=:\n\t"
      "buffer_load_dwordx4 v[84:87], v60, s[36:39], s43 offen sc1\n\t"
      "buffer_load_dwordx4 v[88:91], v60, s[36:39], s43 offen offset:1024 sc1\n\t"
      "s_waitcnt vmcnt(2)\n\t"
      "buffer_load_dwordx4 v[92:95], v60, s[36:39], s68 offen sc1\n\t"
      "buffer_load_dwordx4 v[96:99], v60, s[36:39], s68 offen offset:1024 sc1\n\t"
      "s_sub_u32 s63, s63, 1\n\t"
      "s_mov_b64 exec, 1\n\t"
      "buffer_store_dword v109, off, s[36:39], s52 sc1\n\t"
      "s_mov_b64 exec, -1\n\t"
      "s_waitcnt vmcnt(3) lgkmcnt(0)\n\t"
      "v_bitop3_b32 v64, v84, v85, v86 bitop3:0x80\n\t"
      "v_bitop3_b32 v64, v64, v87, v88 bitop3:0x80\n\t"
      "v_bitop3_b32 v64, v64, v89, v90 bitop3:0x80\n\t"
      "v_bitop3_b32 v64, v64, v91, s58 bitop3:0x80\n\t"
      "v_cmp_eq_u32 vcc, 0, v64\n\t"
      "v_pk_fma_f32 v[4:5], v[84:85], s[56:57], v[126:127] op_sel:[0,1,0] op_sel_hi:[1,1,0]\n\t"
      "v_pk_fma_f32 v[6:7], v[86:87], s[56:57], v[126:127] op_sel:[0,1,0] op_sel_hi:[1,1,0]\n\t"
      "v_cvt_pk_bf16_f32 v0, v4, v5\n\t"
      "v_cvt_pk_bf16_f32 v1, v6, v7\n\t"
      "ds_write_b64 v127, v[0:1] offset:40960\n\t"
      "v_pk_fma_f32 v[8:9], v[88:89], s[56:57], v[126:127] op_sel:[0,1,0] op_sel_hi:[1,1,0]\n\t"
      "v_pk_fma_f32 v[10:11], v[90:91], s[56:57], v[126:127] op_sel:[0,1,0] op_sel_hi:[1,1,0]\n\t"
      "v_cvt_pk_bf16_f32 v2, v8, v9\n\t"
      "v_cvt_pk_bf16_f32 v3, v10, v11\n\t"
      "ds_write_b64 v127, v[2:3] offset:40968\n\t"
      "s_cmp_le_i32 s63, 0\n\t"
      "s_waitcnt lgkmcnt(0)\n\t"
      "s_cbranch_scc1 LRZ_0%=\n\t"
      "s_cbranch_vccnz LR0_0%=\n\t"
      "LRZ_0%=:\n\t"
      "v_mov_b64 v[32:33], 0\n\t"
      "v_mov_b64 v[34:35], 0\n\t"
      "v_mov_b64 v[36:37], 0\n\t"
      "v_mov_b64 v[38:39], 0\n\t"
      "v_mov_b64 v[40:41], 0\n\t"
      "v_mov_b64 v[42:43], 0\n\t"
      "v_mov_b64 v[44:45], 0\n\t"
      "v_mov_b64 v[46:47], 0\n\t"
      "v_mov_b64 v[48:49], 0\n\t"
      "v_mov_b64 v[50:51], 0\n\t"
      "v_mov_b64 v[52:53], 0\n\t"
      "v_mov_b64 v[54:55], 0\n\t"
      "v_mfma_f32_16x16x32_bf16 v[56:59], v[0:3], v[0:3], 0\n\t"
      "v_smfmac_f32_16x16x64_bf16 v[36:39], v[0:3], a[64:71], v122\n\t"
      "v_smfmac_f32_16x16x64_bf16 v[44:47], v[0:3], a[192:199], v122\n\t"
      "s_branch LC0_0%=\n\t"
      "LUTOK_1%=:\n\t"
      "v_add_u32 v104, s71, v100\n\t"
      "ds_read_b128 v[76:79], v104\n\t"
      "ds_read_b128 v[80:83], v104 offset:1024\n\t"
      "ds_read_b128 v[112:115], v100\n\t"
      "ds_read_b128 v[116:119], v100 offset:1024\n\t"
      "v_xor_b32 v101, 2048, v104\n\t"
      "v_xor_b32 v102, 4096, v104\n\t"
      "v_xor_b32 v103, 6144, v104\n\t"
      "ds_read_b128 v[16:19], v101\n\t"
      "ds_read_b128 v[20:23], v101 offset:1024\n\t"
      "ds_read_b128 v[24:27], v100 offset:8192\n\t"
      "ds_read_b128 v[28:31], v100 offset:9216\n\t"
      "ds_read_b128 v[0:3], v102\n\t"
      "ds_read_b128 v[4:7], v102 offset:1024\n\t"
      "ds_read_b128 v[8:11], v100 offset:16384\n\t"
      "ds_read_b128 v[12:15], v100 offset:17408\n\t"
      "s_mov_b32 s28, s52\n\t"
      "s_add_u32 m0, s55, s71\n\t"
      "s_sub_u32 s71, 204800, s71\n\t"
      "s_add_u32 s40, s40, 1\n\t"
      "s_cmp_lt_u32 s40, 63\n\t"
      "s_cselect_b32 s61, 786432, 0\n\t"
      "s_cselect_b32 s60, s58, 0\n\t"
      "s_add_u32 s44, s44, s61\n\t"
      "s_cmp_lt_u32 s40, 62\n\t"
      "s_cselect_b32 s61, 786432, 0\n\t"
      "s_add_u32 s46, s46, s61\n\t"
      "s_add_u32 s61, s40, 3\n\t"
      "s_and_b32 s62, s61, 3\n\t"
      "s_cselect_b32 s62, 16, 2000\n\t"
      "s_cmp_lt_u32 s61, 64\n\t"
      "s_cselect_b32 s62, s62, 0\n\t"
      "s_add_u32 s50, s50, s62\n\t"
      "s_mov_b32 s63, 0x2000\n\t"
      "s_waitcnt lgkmcnt(8)\n\t"
      "v_mfma_f32_16x16x32_bf16 v[32:35], v[76:79], v[112:115], 0\n\t"
      "v_mfma_f32_16x16x32_bf16 v[36:39], v[80:83], v[112:115], 0\n\t"
      "v_mfma_f32_16x16x32_bf16 v[40:43], v[76:79], v[116:119], 0\n\t"
      "v_mfma_f32_16x16x32_bf16 v[44:47], v[80:83], v[116:119], 0\n\t"
      "ds_read_b128 v[76:79], v103\n\t"
      "ds_read_b128 v[80:83], v103 offset:1024\n\t"
      "ds_read_b128 v[112:115], v100 offset:24576\n\t"
      "ds_read_b128 v[116:119], v100 offset:25600\n\t"
      "s_waitcnt lgkmcnt(8)\n\t"
      "v_mfma_f32_16x16x32_bf16 v[32:35], v[16:19], v[24:27], v[32:35]\n\t"
      "v_mfma_f32_16x16x32_bf16 v[36:39], v[20:23], v[24:27], v[36:39]\n\t"
      "v_mfma_f32_16x16x32_bf16 v[40:43], v[16:19], v[28:31], v[40:43]\n\t"
      "v_mfma_f32_16x16x32_bf16 v[44:47], v[20:23], v[28:31], v[44:47]\n\t"
      "s_branch LGATE_1%=\n\t"
      "LFIRST_1%=:\n\t"
      "s_cmp_eq_u32 s40, 0\n\t"
      "s_cbranch_scc1 LWM0_1%=\n\t"
      "s_barrier\n\t"
      "ds_read_b32 v99, v109 offset:55392\n\t"
      "s_waitcnt lgkmcnt(0)\n\t"
      "v_readfirstlane_b32 s61, v99\n\t"
      "s_nop 1\n\t"
      "s_cmp_gt_i32 s61, 0\n\t"
      "s_cselect_b32 s99, 0x2000, 0\n\t"
      "s_xor_b32 s97, s97, s99\n\t"
      "s_barrier\n\t"
      "s_mov_b32 s63, 0x2000\n\t"
      "s_branch LPOLL_1%=\n\t"
      "LWM0_1%=:\n\t"
      "s_mov_b32 s63, 0x10000\n\t"
      "LWM_1%=:\n\t"
      "s_waitcnt vmcnt(0) lgkmcnt(0)\n\t"
      "v_bitop3_b32 v49, v49, v51, s53 bitop3:0x7e\n\t"
      "v_max_u32 v48, v48, v50\n\t"
      "v_cmp_ne_u32 vcc, 0, v49\n\t"
      "s_nop 1\n\t"
      "v_max_u32_dpp v48, v48, v48 quad_perm:[1,0,3,2] row_mask:0xf bank_mask:0xf\n\t"
      "s_nop 1\n\t"
      "v_max_u32_dpp v48, v48, v48 quad_perm:[2,3,0,1] row_mask:0xf bank_mask:0xf\n\t"
      "s_nop 1\n\t"
      "v_max_u32_dpp v48, v48, v48 row_ror:4 row_mask:0xf bank_mask:0xf\n\t"
      "s_nop 1\n\t"
      "v_max_u32_dpp v48, v48, v48 row_ror:8 row_mask:0xf bank_mask:0xf\n\t"
      "v_mov_b32 v49, v48\n\t"
      "s_nop 1\n\t"
      "v_permlane16_swap_b32 v48, v49\n\t"
      "v_max_u32 v48, v48, v49\n\t"
      "v_mov_b32 v49, v48\n\t"
      "s_nop 1\n\t"
      "v_permlane32_swap_b32 v48, v49\n\t"
      "v_max_u32 v48, v48, v49\n\t"
      "v_mul_f32 v49, 0x40a00000, v48\n\t"
      "s_nop 0\n\t"
      "v_readfirstlane_b32 s67, v49\n\t"
      "v_mul_f32 v49, 0x43030000, v49\n\t"
      "s_nop 0\n\t"
      "v_readfirstlane_b32 s66, v49\n\t"
      "s_cbranch_vccz LWM1_1%=\n\t"
      "s_sleep 4\n\t"
      "buffer_load_dwordx2 v[48:49], v52, s[36:39], s94 offen sc1\n\t"
      "buffer_load_dwordx2 v[50:51], v52, s[36:39], s94 offen offset:128 sc1\n\t"
      "s_sub_u32 s63, s63, 1\n\t"
      "s_cmp_lg_u32 s63, 0\n\t"
      "s_cbranch_scc1 LWM_1%=\n\t"
      "s_waitcnt vmcnt(0)\n\t"
      "LWM1_1%=:\n\t"
      "buffer_load_dwordx2 v[120:121], off, s[36:39], s64 sc1\n\t"
      "s_lshr_b32 s67, s67, 23\n\t"
      "s_add_u32 s67, s67, 5\n\t"
      "s_lshr_b32 s66, s66, 23\n\t"
      "s_add_u32 s66, s66, 129\n\t"
      "s_max_u32 s66, s66, 80\n\t"
      "s_mov_b32 s63, 0x2000\n\t"
      "LPOL0_1%=:\n\t"
      "s_waitcnt lgkmcnt(4)\n\t"
      "v_mfma_f32_16x16x32_bf16 v[32:35], v[0:3], v[8:11], v[32:35]\n\t"
      "v_mfma_f32_16x16x32_bf16 v[36:39], v[4:7], v[8:11], v[36:39]\n\t"
      "v_mfma_f32_16x16x32_bf16 v[40:43], v[0:3], v[12:15], v[40:43]\n\t"
      "v_mfma_f32_16x16x32_bf16 v[44:47], v[4:7], v[12:15], v[44:47]\n\t"
      "v_mov_b64 v[48:49], 0\n\t"
      "v_mov_b64 v[50:51], 0\n\t"
      "v_mov_b64 v[52:53], 0\n\t"
      "v_mov_b64 v[54:55], 0\n\t"
      "s_xor_b32 s69, s97, 1024\n\t"
      "s_xor_b32 s98, s97, 512\n\t"
      "s_xor_b32 s23, s97, 1536\n\t"
      "s_waitcnt lgkmcnt(0)\n\t"
      "v_mfma_f32_16x16x32_bf16 v[32:35], v[76:79], v[112:115], v[32:35]\n\t"
      "v_mfma_f32_16x16x32_bf16 v[36:39], v[80:83], v[112:115], v[36:39]\n\t"
      "v_mfma_f32_16x16x32_bf16 v[40:43], v[76:79], v[116:119], v[40:43]\n\t"
      "v_mfma_f32_16x16x32_bf16 v[44:47], v[80:83], v[116:119], v[44:47]\n\t"
      "s_mov_b64 exec, 1\n\t"
      "ds_write_b64 v109, v[110:111] offset:55392\n\t"
      "s_mov_b64 exec, -1\n\t"
      "s_nop 4\n\t"
      "v_cvt_pk_bf16_f32 v68, v32, v33\n\t"
      "v_cvt_pk_bf16_f32 v69, v34, v35\n\t"
      "v_cvt_pk_bf16_f32 v70, v36, v37\n\t"
      "v_cvt_pk_bf16_f32 v71, v38, v39\n\t"
      "v_cvt_pk_bf16_f32 v74, v40, v41\n\t"
      "v_cvt_pk_bf16_f32 v75, v42, v43\n\t"
      "v_cvt_pk_bf16_f32 v72, v44, v45\n\t"
      "v_cvt_pk_bf16_f32 v73, v46, v47\n\t"
      "v_mov_b64 v[32:33], 0\n\t"
      "v_mov_b64 v[34:35], 0\n\t"
      "v_mov_b64 v[36:37], 0\n\t"
      "v_mov_b64 v[38:39], 0\n\t"
      "v_mov_b64 v[40:41], 0\n\t"
      "v_mov_b64 v[42:43], 0\n\t"
      "v_mov_b64 v[44:45], 0\n\t"
      "v_mov_b64 v[46:47], 0\n\t"
      "s_waitcnt vmcnt(0)\n\t"
      "s_branch LGO_1%=\n\t"
      "LEARLY_1%=:\n\t"
      "s_cmp_eq_u32 s40, 54\n\t"
      "s_cbranch_scc1 LROLE_1%=\n\t"
      "s_cmp_lt_i32 s40, 0\n\t"
      "s_waitcnt lgkmcnt(0)\n\t"
      "s_cbranch_scc1 LUTOK_1%=\n\t"
      "s_xor_b32 s43, s43, s99\n\t"
      "s_xor_b32 s68, s68, s99\n\t"
      "s_branch LROLE_1%=\n\t"
      "LSLOW_1%=:\n\t"
      "s_mov_b32 s63, 0x10000\n\t"
      "LSLOW1_1%=:\n\t"
      "buffer_load_dwordx2 v[92:93], off, s[36:39], s64 sc1\n\t"
      "s_waitcnt vmcnt(0)\n\t"
      "v_bitop3_b32 v92, v92, v93, s53 bitop3:0x7e\n\t"
      "s_nop 0\n\t"
      "v_readfirstlane_b32 s56, v92\n\t"
      "s_nop 1\n\t"
      "s_cmp_eq_u32 s56, 0\n\t"
      "s_cbranch_scc1 LSLOW2_1%=\n\t"
      "s_sleep 8\n\t"
      "s_sub_u32 s63, s63, 1\n\t"
      "s_cmp_lg_u32 s63, 0\n\t"
      "s_cbranch_scc1 LSLOW1_1%=\n\t"
      "LSLOW2_1%=:\n\t"
      "s_mov_b32 m0, s45\n\t"
      "s_sub_u32 s61, 204800, s71\n\t"
      "buffer_load_dwordx4 v65, s[36:39], s44 offen sc1 lds\n\t"
      "buffer_load_dwordx4 v65, s[36:39], s44 offen offset:1024 sc1 lds\n\t"
      "s_add_u32 m0, s55, s61\n\t"
      "s_nop 0\n\t"
      "buffer_load_dwordx4 v127, s[36:39], s46 offen sc1 lds\n\t"
      "buffer_load_dwordx4 v127, s[36:39], s46 offen offset:1024 sc1 lds\n\t"
      "s_branch LWIN1_1%=\n\t"
      "LMX_1%=:\n\t"
      "s_waitcnt lgkmcnt(0)\n\t"
      "s_nop 7\n\t"
      "v_smfmac_f32_16x16x64_bf16 v[52:55], v[16:19], v[224:231], v122\n\t"
      "v_smfmac_f32_16x16x64_bf16 v[52:55], v[28:31], v[248:255], v122\n\t"
      "v_smfmac_f32_16x16x64_bf16 v[52:55], v[24:27], v[240:247], v122\n\t"
      "s_nop 7\n\t"
      "v_add_f32 v53, v53, v55\n\t"
      "v_add_f32_e64 v52, v52, v54\n\t"
      "v_cvt_pk_bf16_f32 v66, v52, v53\n\t"
      "s_add_u32 s62, s61, 0x83400000\n\t"
      "v_mov_b32_e64 v126, s62\n\t"
      "v_max3_f32 v82, v112, v113, v114\n\t"
      "v_max_f32 v82, v82, v115\n\t"
      "s_nop 1\n\t"
      "v_max_f32_dpp v82, v82, v82 quad_perm:[2,3,0,1] row_mask:0xf bank_mask:0xf\n\t"
      "v_max3_f32 v83, v82, v120, v121\n\t"
      "v_max3_f32 v60, v82, v120, v121\n\t"
      "s_nop 1\n\t"
      "v_permlane16_swap_b32 v83, v60\n\t"
      "v_max_f32 v61, v83, v60\n\t"
      "v_max_f32 v60, v83, v60\n\t"
      "s_nop 1\n\t"
      "v_permlane32_swap_b32 v61, v60\n\t"
      "v_max_f32 v83, v61, v60\n\t"
      "v_sub_f32 v112, v112, v83\n\t"
      "v_sub_f32 v113, v113, v83\n\t"
      "v_sub_f32 v114, v114, v83\n\t"
      "v_sub_f32 v115, v115, v83\n\t"
      "v_sub_f32 v120, v120, v83\n\t"
      "v_sub_f32 v121, v121, v83\n\t"
      "v_exp_f32_e64 v56, v112\n\t"
      "v_exp_f32 v57, v113\n\t"
      "v_exp_f32_e64 v58, v114\n\t"
      "v_exp_f32 v59, v115\n\t"
      "v_exp_f32 v80, v120\n\t"
      "v_exp_f32 v81, v121\n\t"
      "v_cvt_pk_bf16_f32 v96, v56, v57\n\t"
      "v_cvt_pk_bf16_f32 v97, v58, v59\n\t"
      "v_cvt_pk_bf16_f32 v108, v80, v81\n\t"
      "v_mov_b32_dpp v98, v96 quad_perm:[2,3,0,1] row_mask:0xf bank_mask:0xf\n\t"
      "v_mov_b32_dpp v99, v97 quad_perm:[2,3,0,1] row_mask:0xf bank_mask:0xf\n\t"
      "v_mfma_f32_16x16x32_bf16 v[48:51], v[108:111], v[88:91], 0\n\t"
      "s_nop 0\n\t"
      "v_mfma_f32_16x16x32_bf16 v[48:51], v[96:99], v[88:91], v[48:51]\n\t"
      "v_mfma_f32_16x16x32_bf16 v[84:87], v[108:111], v[64:67], 0\n\t"
      "v_mfma_f32_16x16x32_bf16 v[84:87], v[96:99], v[68:71], v[84:87]\n\t"
      "v_mfma_f32_16x16x32_bf16 v[92:95], v[96:99], v[72:75], 0\n\t"
      "v_mfma_f32_16x16x32_bf16 v[92:95], v[108:111], v[66:69], v[92:95]\n\t"
      "s_nop 3\n\t"
      "v_rcp_f32_e64 v48, v48\n\t"
      "v_rcp_f32 v49, v49\n\t"
      "v_fma_f32 v84, v84, v48, s96\n\t"
      "buffer_atomic_add v84, v123, s[36:39], s97 offen\n\t"
      "v_fma_f32 v85, v85, v49, s96\n\t"
      "buffer_atomic_add v85, v123, s[36:39], s98 offen\n\t"
      "v_fma_f32 v94, v94, v48, s96\n\t"
      "buffer_atomic_add v94, v123, s[36:39], s69 offen\n\t"
      "v_fma_f32 v95, v95, v49, s96\n\t"
      "buffer_atomic_add v95, v123, s[36:39], s23 offen\n\t"
      "s_add_u32 s97, s97, 0x8000\n\t"
      "s_branch LWIN_1%=\n\t"
      "LR0_1%=:\n\t"
      "buffer_load_dwordx4 v[84:87], v60, s[36:39], s43 offen sc1\n\t"
      "buffer_load_dwordx4 v[88:91], v60, s[36:39], s43 offen offset:1024 sc1\n\t"
      "s_waitcnt vmcnt(2)\n\t"
      "buffer_load_dwordx4 v[92:95], v60, s[36:39], s68 offen sc1\n\t"
      "buffer_load_dwordx4 v[96:99], v60, s[36:39], s68 offen offset:1024 sc1\n\t"
      "s_sub_u32 s63, s63, 1\n\t"
      "s_mov_b64 exec, 1\n\t"
      "buffer_store_dword v109, off, s[36:39], s52 sc1\n\t"
      "s_mov_b64 exec, -1\n\t"
      "s_waitcnt vmcnt(3) lgkmcnt(0)\n\t"
      "v_bitop3_b32 v64, v84, v85, v86 bitop3:0x80\n\t"
      "v_bitop3_b32 v64, v64, v87, v88 bitop3:0x80\n\t"
      "v_bitop3_b32 v64, v64, v89, v90 bitop3:0x80\n\t"
      "v_bitop3_b32 v64, v64, v91, s58 bitop3:0x80\n\t"
      "v_cmp_eq_u32 vcc, 0, v64\n\t"
      "v_pk_fma_f32 v[0:1], v[84:85], s[56:57], v[126:127] op_sel:[0,1,0] op_sel_hi:[1,1,0]\n\t"
      "v_pk_fma_f32 v[2:3], v[86:87], s[56:57], v[126:127] op_sel:[0,1,0] op_sel_hi:[1,1,0]\n\t"
      "v_cvt_pk_bf16_f32 v4, v0, v1\n\t"
      "v_cvt_pk_bf16_f32 v5, v2, v3\n\t"
      "ds_write_b64 v127, v[4:5] offset:41984\n\t"
      "v_pk_fma_f32 v[12:13], v[88:89], s[56:57], v[126:127] op_sel:[0,1,0] op_sel_hi:[1,1,0]\n\t"
      "v_pk_fma_f32 v[14:15], v[90:91], s[56:57], v[126:127] op_sel:[0,1,0] op_sel_hi:[1,1,0]\n\t"
      "v_cvt_pk_bf16_f32 v6, v12, v13\n\t"
      "v_cvt_pk_bf16_f32 v7, v14, v15\n\t"
      "ds_write_b64 v127, v[6:7] offset:41992\n\t"
      "s_cmp_le_i32 s63, 0\n\t"
      "s_waitcnt lgkmcnt(0)\n\t"
      "s_cbranch_scc1 LRZ_1%=\n\t"
      "s_cbranch_vccnz LR0_1%=\n\t"
      "LRZ_1%=:\n\t"
      "v_mov_b64 v[32:33], 0\n\t"
      "v_mov_b64 v[34:35], 0\n\t"
      "v_mov_b64 v[36:37], 0\n\t"
      "v_mov_b64 v[38:39], 0\n\t"
      "v_mov_b64 v[40:41], 0\n\t"
      "v_mov_b64 v[42:43], 0\n\t"
      "v_mov_b64 v[44:45], 0\n\t"
      "v_mov_b64 v[46:47], 0\n\t"
      "v_mov_b64 v[48:49], 0\n\t"
      "v_mov_b64 v[50:51], 0\n\t"
      "v_mov_b64 v[52:53], 0\n\t"
      "v_mov_b64 v[54:55], 0\n\t"
      "v_mfma_f32_16x16x32_bf16 v[56:59], v[4:7], v[4:7], 0\n\t"
      "v_smfmac_f32_16x16x64_bf16 v[36:39], v[4:7], a[72:79], v122\n\t"
      "v_smfmac_f32_16x16x64_bf16 v[44:47], v[4:7], a[200:207], v122\n\t"
      "s_branch LC0_1%=\n\t"
      "LUTOK_2%=:\n\t"
      "v_add_u32 v104, s71, v100\n\t"
      "ds_read_b128 v[76:79], v104\n\t"
      "ds_read_b128 v[80:83], v104 offset:1024\n\t"
      "ds_read_b128 v[112:115], v100\n\t"
      "ds_read_b128 v[116:119], v100 offset:1024\n\t"
      "v_xor_b32 v101, 2048, v104\n\t"
      "v_xor_b32 v102, 4096, v104\n\t"
      "v_xor_b32 v103, 6144, v104\n\t"
      "ds_read_b128 v[0:3], v101\n\t"
      "ds_read_b128 v[4:7], v101 offset:1024\n\t"
      "ds_read_b128 v[8:11], v100 offset:8192\n\t"
      "ds_read_b128 v[12:15], v100 offset:9216\n\t"
      "ds_read_b128 v[16:19], v102\n\t"
      "ds_read_b128 v[20:23], v102 offset:1024\n\t"
      "ds_read_b128 v[24:27], v100 offset:16384\n\t"
      "ds_read_b128 v[28:31], v100 offset:17408\n\t"
      "s_add_u32 m0, s55, s71\n\t"
      "s_sub_u32 s71, 204800, s71\n\t"
      "s_add_u32 s40, s40, 1\n\t"
      "s_cmp_lt_u32 s40, 63\n\t"
      "s_cselect_b32 s61, 786432, 0\n\t"
      "s_cselect_b32 s60, s58, 0\n\t"
      "s_add_u32 s44, s44, s61\n\t"
      "s_cmp_lt_u32 s40, 62\n\t"
      "s_cselect_b32 s61, 786432, 0\n\t"
      "s_add_u32 s46, s46, s61\n\t"
      "s_add_u32 s61, s40, 3\n\t"
      "s_and_b32 s62, s61, 3\n\t"
      "s_cselect_b32 s62, 16, 2000\n\t"
      "s_cmp_lt_u32 s61, 64\n\t"
      "s_cselect_b32 s62, s62, 0\n\t"
      "s_add_u32 s50, s50, s62\n\t"
      "s_mov_b32 s63, 0x2000\n\t"
      "s_waitcnt lgkmcnt(8)\n\t"
      "v_mfma_f32_16x16x32_bf16 v[32:35], v[76:79], v[112:115], 0\n\t"
      "v_mfma_f32_16x16x32_bf16 v[36:39], v[80:83], v[112:115], 0\n\t"
      "v_mfma_f32_16x16x32_bf16 v[40:43], v[76:79], v[116:119], 0\n\t"
      "v_mfma_f32_16x16x32_bf16 v[44:47], v[80:83], v[116:119], 0\n\t"
      "ds_read_b128 v[76:79], v103\n\t"
      "ds_read_b128 v[80:83], v103 offset:1024\n\t"
      "ds_read_b128 v[112:115], v100 offset:24576\n\t"
      "ds_read_b128 v[116:119], v100 offset:25600\n\t"
      "s_branch LGATE_2%=\n\t"
      "LFIRST_2%=:\n\t"
      "s_cmp_eq_u32 s40, 0\n\t"
      "s_cbranch_scc1 LWM0_2%=\n\t"
      "s_barrier\n\t"
      "ds_read_b32 v99, v109 offset:55392\n\t"
      "s_waitcnt lgkmcnt(0)\n\t"
      "v_readfirstlane_b32 s61, v99\n\t"
      "s_nop 1\n\t"
      "s_cmp_gt_i32 s61, 0\n\t"
      "s_cselect_b32 s99, 0x2000, 0\n\t"
      "s_xor_b32 s97, s97, s99\n\t"
      "s_barrier\n\t"
      "s_mov_b32 s63, 0x2000\n\t"
      "s_branch LPOLL_2%=\n\t"
      "LWM0_2%=:\n\t"
      "s_mov_b32 s63, 0x10000\n\t"
      "LWM_2%=:\n\t"
      "s_waitcnt vmcnt(0) lgkmcnt(0)\n\t"
      "v_bitop3_b32 v49, v49, v51, s53 bitop3:0x7e\n\t"
      "v_max_u32 v48, v48, v50\n\t"
      "v_cmp_ne_u32 vcc, 0, v49\n\t"
      "s_nop 1\n\t"
      "v_max_u32_dpp v48, v48, v48 quad_perm:[1,0,3,2] row_mask:0xf bank_mask:0xf\n\t"
      "s_nop 1\n\t"
      "v_max_u32_dpp v48, v48, v48 quad_perm:[2,3,0,1] row_mask:0xf bank_mask:0xf\n\t"
      "s_nop 1\n\t"
      "v_max_u32_dpp v48, v48, v48 row_ror:4 row_mask:0xf bank_mask:0xf\n\t"
      "s_nop 1\n\t"
      "v_max_u32_dpp v48, v48, v48 row_ror:8 row_mask:0xf bank_mask:0xf\n\t"
      "v_mov_b32 v49, v48\n\t"
      "s_nop 1\n\t"
      "v_permlane16_swap_b32 v48, v49\n\t"
      "v_max_u32 v48, v48, v49\n\t"
      "v_mov_b32 v49, v48\n\t"
      "s_nop 1\n\t"
      "v_permlane32_swap_b32 v48, v49\n\t"
      "v_max_u32 v48, v48, v49\n\t"
      "v_mul_f32 v49, 0x40a00000, v48\n\t"
      "s_nop 0\n\t"
      "v_readfirstlane_b32 s67, v49\n\t"
      "v_mul_f32 v49, 0x43030000, v49\n\t"
      "s_nop 0\n\t"
      "v_readfirstlane_b32 s66, v49\n\t"
      "s_cbranch_vccz LWM1_2%=\n\t"
      "s_sleep 4\n\t"
      "buffer_load_dwordx2 v[48:49], v52, s[36:39], s94 offen sc1\n\t"
      "buffer_load_dwordx2 v[50:51], v52, s[36:39], s94 offen offset:128 sc1\n\t"
      "s_sub_u32 s63, s63, 1\n\t"
      "s_cmp_lg_u32 s63, 0\n\t"
      "s_cbranch_scc1 LWM_2%=\n\t"
      "s_waitcnt vmcnt(0)\n\t"
      "LWM1_2%=:\n\t"
      "buffer_load_dwordx2 v[120:121], off, s[36:39], s64 sc1\n\t"
      "s_lshr_b32 s67, s67, 23\n\t"
      "s_add_u32 s67, s67, 5\n\t"
      "s_lshr_b32 s66, s66, 23\n\t"
      "s_add_u32 s66, s66, 129\n\t"
      "s_max_u32 s66, s66, 80\n\t"
      "s_mov_b32 s63, 0x2000\n\t"
      "LPOL0_2%=:\n\t"
      "s_waitcnt lgkmcnt(8)\n\t"
      "v_mfma_f32_16x16x32_bf16 v[32:35], v[0:3], v[8:11], v[32:35]\n\t"
      "v_mfma_f32_16x16x32_bf16 v[36:39], v[4:7], v[8:11], v[36:39]\n\t"
      "v_mfma_f32_16x16x32_bf16 v[40:43], v[0:3], v[12:15], v[40:43]\n\t"
      "v_mfma_f32_16x16x32_bf16 v[44:47], v[4:7], v[12:15], v[44:47]\n\t"
      "v_mov_b64 v[48:49], 0\n\t"
      "v_mov_b64 v[50:51], 0\n\t"
      "v_mov_b64 v[52:53], 0\n\t"
      "v_mov_b64 v[54:55], 0\n\t"
      "s_xor_b32 s69, s97, 1024\n\t"
      "s_xor_b32 s98, s97, 512\n\t"
      "s_xor_b32 s23, s97, 1536\n\t"
      "s_waitcnt lgkmcnt(4)\n\t"
      "v_mfma_f32_16x16x32_bf16 v[32:35], v[16:19], v[24:27], v[32:35]\n\t"
      "v_mfma_f32_16x16x32_bf16 v[36:39], v[20:23], v[24:27], v[36:39]\n\t"
      "v_mfma_f32_16x16x32_bf16 v[40:43], v[16:19], v[28:31], v[40:43]\n\t"
      "v_mfma_f32_16x16x32_bf16 v[44:47], v[20:23], v[28:31], v[44:47]\n\t"
      "s_waitcnt lgkmcnt(0)\n\t"
      "v_mfma_f32_16x16x32_bf16 v[32:35], v[76:79], v[112:115], v[32:35]\n\t"
      "v_mfma_f32_16x16x32_bf16 v[36:39], v[80:83], v[112:115], v[36:39]\n\t"
      "v_mfma_f32_16x16x32_bf16 v[40:43], v[76:79], v[116:119], v[40:43]\n\t"
      "v_mfma_f32_16x16x32_bf16 v[44:47], v[80:83], v[116:119], v[44:47]\n\t"
      "s_mov_b64 exec, 1\n\t"
      "ds_write_b64 v109, v[110:111] offset:55392\n\t"
      "s_mov_b64 exec, -1\n\t"
      "s_nop 4\n\t"
      "v_cvt_pk_bf16_f32 v68, v32, v33\n\t"
      "v_cvt_pk_bf16_f32 v69, v34, v35\n\t"
      "v_cvt_pk_bf16_f32 v70, v36, v37\n\t"
      "v_cvt_pk_bf16_f32 v71, v38, v39\n\t"
      "v_cvt_pk_bf16_f32 v74, v40, v41\n\t"
      "v_cvt_pk_bf16_f32 v75, v42, v43\n\t"
      "v_cvt_pk_bf16_f32 v72, v44, v45\n\t"
      "v_cvt_pk_bf16_f32 v73, v46, v47\n\t"
      "v_mov_b64 v[32:33], 0\n\t"
      "v_mov_b64 v[34:35], 0\n\t"
      "v_mov_b64 v[36:37], 0\n\t"
      "v_mov_b64 v[38:39], 0\n\t"
      "v_mov_b64 v[40:41], 0\n\t"
      "v_mov_b64 v[42:43], 0\n\t"
      "v_mov_b64 v[44:45], 0\n\t"
      "v_mov_b64 v[46:47], 0\n\t"
      "s_waitcnt vmcnt(0)\n\t"
      "s_branch LGO_2%=\n\t"
      "LEARLY_2%=:\n\t"
      "s_cmp_eq_u32 s40, 54\n\t"
      "s_cbranch_scc1 LROLE_2%=\n\t"
      "s_cmp_lt_i32 s40, 0\n\t"
      "s_waitcnt lgkmcnt(0)\n\t"
      "s_cbranch_scc1 LUTOK_2%=\n\t"
      "s_xor_b32 s43, s43, s99\n\t"
      "s_xor_b32 s68, s68, s99\n\t"
      "s_branch LROLE_2%=\n\t"
      "LSLOW_2%=:\n\t"
      "s_mov_b32 s63, 0x10000\n\t"
      "LSLOW1_2%=:\n\t"
      "buffer_load_dwordx2 v[92:93], off, s[36:39], s64 sc1\n\t"
      "s_waitcnt vmcnt(0)\n\t"
      "v_bitop3_b32 v92, v92, v93, s53 bitop3:0x7e\n\t"
      "s_nop 0\n\t"
      "v_readfirstlane_b32 s56, v92\n\t"
      "s_nop 1\n\t"
      "s_cmp_eq_u32 s56, 0\n\t"
      "s_cbranch_scc1 LSLOW2_2%=\n\t"
      "s_sleep 8\n\t"
      "s_sub_u32 s63, s63, 1\n\t"
      "s_cmp_lg_u32 s63, 0\n\t"
      "s_cbranch_scc1 LSLOW1_2%=\n\t"
      "LSLOW2_2%=:\n\t"
      "s_mov_b32 m0, s45\n\t"
      "s_sub_u32 s61, 204800, s71\n\t"
      "buffer_load_dwordx4 v65, s[36:39], s44 offen sc1 lds\n\t"
      "buffer_load_dwordx4 v65, s[36:39], s44 offen offset:1024 sc1 lds\n\t"
      "s_add_u32 m0, s55, s61\n\t"
      "s_nop 0\n\t"
      "buffer_load_dwordx4 v127, s[36:39], s46 offen sc1 lds\n\t"
      "buffer_load_dwordx4 v127, s[36:39], s46 offen offset:1024 sc1 lds\n\t"
      "s_branch LWIN1_2%=\n\t"
      "LMX_2%=:\n\t"
      "s_waitcnt lgkmcnt(0)\n\t"
      "s_nop 7\n\t"
      "v_smfmac_f32_16x16x64_bf16 v[52:55], v[28:31], v[248:255], v122\n\t"
      "v_smfmac_f32_16x16x64_bf16 v[52:55], v[16:19], v[224:231], v122\n\t"
      "v_smfmac_f32_16x16x64_bf16 v[52:55], v[20:23], v[232:239], v122\n\t"
      "s_nop 7\n\t"
      "v_add_f32 v53, v53, v55\n\t"
      "v_add_f32_e64 v52, v52, v54\n\t"
      "v_cvt_pk_bf16_f32 v66, v52, v53\n\t"
      "s_add_u32 s62, s61, 0x83400000\n\t"
      "v_mov_b32_e64 v126, s62\n\t"
      "v_max3_f32 v82, v112, v113, v114\n\t"
      "v_max_f32 v82, v82, v115\n\t"
      "s_nop 1\n\t"
      "v_max_f32_dpp v82, v82, v82 quad_perm:[2,3,0,1] row_mask:0xf bank_mask:0xf\n\t"
      "v_max3_f32 v83, v82, v120, v121\n\t"
      "v_max3_f32 v60, v82, v120, v121\n\t"
      "s_nop 1\n\t"
      "v_permlane16_swap_b32 v83, v60\n\t"
      "v_max_f32 v61, v83, v60\n\t"
      "v_max_f32 v60, v83, v60\n\t"
      "s_nop 1\n\t"
      "v_permlane32_swap_b32 v61, v60\n\t"
      "v_max_f32 v83, v61, v60\n\t"
      "v_sub_f32 v112, v112, v83\n\t"
      "v_sub_f32 v113, v113, v83\n\t"
      "v_sub_f32 v114, v114, v83\n\t"
      "v_sub_f32 v115, v115, v83\n\t"
      "v_sub_f32 v120, v120, v83\n\t"
      "v_sub_f32 v121, v121, v83\n\t"
      "v_exp_f32_e64 v56, v112\n\t"
      "v_exp_f32 v57, v113\n\t"
      "v_exp_f32_e64 v58, v114\n\t"
      "v_exp_f32 v59, v115\n\t"
      "v_exp_f32 v80, v120\n\t"
      "v_exp_f32 v81, v121\n\t"
      "v_cvt_pk_bf16_f32 v96, v56, v57\n\t"
      "v_cvt_pk_bf16_f32 v97, v58, v59\n\t"
      "v_cvt_pk_bf16_f32 v108, v80, v81\n\t"
      "v_mov_b32_dpp v98, v96 quad_perm:[2,3,0,1] row_mask:0xf bank_mask:0xf\n\t"
      "v_mov_b32_dpp v99, v97 quad_perm:[2,3,0,1] row_mask:0xf bank_mask:0xf\n\t"
      "v_mfma_f32_16x16x32_bf16 v[48:51], v[108:111], v[88:91], 0\n\t"
      "s_nop 0\n\t"
      "v_mfma_f32_16x16x32_bf16 v[48:51], v[96:99], v[88:91], v[48:51]\n\t"
      "v_mfma_f32_16x16x32_bf16 v[84:87], v[108:111], v[64:67], 0\n\t"
      "v_mfma_f32_16x16x32_bf16 v[84:87], v[96:99], v[68:71], v[84:87]\n\t"
      "v_mfma_f32_16x16x32_bf16 v[92:95], v[96:99], v[72:75], 0\n\t"
      "v_mfma_f32_16x16x32_bf16 v[92:95], v[108:111], v[66:69], v[92:95]\n\t"
      "s_nop 3\n\t"
      "v_rcp_f32_e64 v48, v48\n\t"
      "v_rcp_f32 v49, v49\n\t"
      "v_fma_f32 v84, v84, v48, s96\n\t"
      "buffer_atomic_add v84, v123, s[36:39], s97 offen\n\t"
      "v_fma_f32 v85, v85, v49, s96\n\t"
      "buffer_atomic_add v85, v123, s[36:39], s98 offen\n\t"
      "v_fma_f32 v94, v94, v48, s96\n\t"
      "buffer_atomic_add v94, v123, s[36:39], s69 offen\n\t"
      "v_fma_f32 v95, v95, v49, s96\n\t"
      "buffer_atomic_add v95, v123, s[36:39], s23 offen\n\t"
      "s_add_u32 s97, s97, 0x8000\n\t"
      "s_branch LWIN_2%=\n\t"
      "LR0_2%=:\n\t"
      "buffer_load_dwordx4 v[84:87], v60, s[36:39], s43 offen sc1\n\t"
      "buffer_load_dwordx4 v[88:91], v60, s[36:39], s43 offen offset:1024 sc1\n\t"
      "s_waitcnt vmcnt(2)\n\t"
      "buffer_load_dwordx4 v[92:95], v60, s[36:39], s68 offen sc1\n\t"
      "buffer_load_dwordx4 v[96:99], v60, s[36:39], s68 offen offset:1024 sc1\n\t"
      "s_sub_u32 s63, s63, 1\n\t"
      "s_waitcnt vmcnt(2) lgkmcnt(0)\n\t"
      "v_bitop3_b32 v64, v84, v85, v86 bitop3:0x80\n\t"
      "v_bitop3_b32 v64, v64, v87, v88 bitop3:0x80\n\t"
      "v_bitop3_b32 v64, v64, v89, v90 bitop3:0x80\n\t"
      "v_bitop3_b32 v64, v64, v91, s58 bitop3:0x80\n\t"
      "v_cmp_eq_u32 vcc, 0, v64\n\t"
      "v_pk_fma_f32 v[12:13], v[84:85], s[56:57], v[126:127] op_sel:[0,1,0] op_sel_hi:[1,1,0]\n\t"
      "v_pk_fma_f32 v[14:15], v[86:87], s[56:57], v[126:127] op_sel:[0,1,0] op_sel_hi:[1,1,0]\n\t"
      "v_cvt_pk_bf16_f32 v8, v12, v13\n\t"
      "v_cvt_pk_bf16_f32 v9, v14, v15\n\t"
      "ds_write_b64 v127, v[8:9] offset:43008\n\t"
      "v_pk_fma_f32 v[0:1], v[88:89], s[56:57], v[126:127] op_sel:[0,1,0] op_sel_hi:[1,1,0]\n\t"
      "v_pk_fma_f32 v[2:3], v[90:91], s[56:57], v[126:127] op_sel:[0,1,0] op_sel_hi:[1,1,0]\n\t"
      "v_cvt_pk_bf16_f32 v10, v0, v1\n\t"
      "v_cvt_pk_bf16_f32 v11, v2, v3\n\t"
      "ds_write_b64 v127, v[10:11] offset:43016\n\t"
      "s_cmp_le_i32 s63, 0\n\t"
      "s_waitcnt lgkmcnt(0)\n\t"
      "s_cbranch_scc1 LRZ_2%=\n\t"
      "s_cbranch_vccnz LR0_2%=\n\t"
      "LRZ_2%=:\n\t"
      "v_mov_b64 v[32:33], 0\n\t"
      "v_mov_b64 v[34:35], 0\n\t"
      "v_mov_b64 v[36:37], 0\n\t"
      "v_mov_b64 v[38:39], 0\n\t"
      "v_mov_b64 v[40:41], 0\n\t"
      "v_mov_b64 v[42:43], 0\n\t"
      "v_mov_b64 v[44:45], 0\n\t"
      "v_mov_b64 v[46:47], 0\n\t"
      "v_mov_b64 v[48:49], 0\n\t"
      "v_mov_b64 v[50:51], 0\n\t"
      "v_mov_b64 v[52:53], 0\n\t"
      "v_mov_b64 v[54:55], 0\n\t"
      "v_mfma_f32_16x16x32_bf16 v[56:59], v[8:11], v[8:11], 0\n\t"
      "v_smfmac_f32_16x16x64_bf16 v[36:39], v[8:11], a[80:87], v122\n\t"
      "v_smfmac_f32_16x16x64_bf16 v[44:47], v[8:11], a[208:215], v122\n\t"
      "s_branch LC0_2%=\n\t"
      "LUTOK_3%=:\n\t"
      "v_add_u32 v104, s71, v100\n\t"
      "ds_read_b128 v[76:79], v104\n\t"
      "ds_read_b128 v[80:83], v104 offset:1024\n\t"
      "ds_read_b128 v[112:115], v100\n\t"
      "ds_read_b128 v[116:119], v100 offset:1024\n\t"
      "v_xor_b32 v101, 2048, v104\n\t"
      "v_xor_b32 v102, 4096, v104\n\t"
      "v_xor_b32 v103, 6144, v104\n\t"
      "ds_read_b128 v[0:3], v101\n\t"
      "ds_read_b128 v[4:7], v101 offset:1024\n\t"
      "ds_read_b128 v[8:11], v100 offset:8192\n\t"
      "ds_read_b128 v[12:15], v100 offset:9216\n\t"
      "ds_read_b128 v[16:19], v102\n\t"
      "ds_read_b128 v[20:23], v102 offset:1024\n\t"
      "ds_read_b128 v[24:27], v100 offset:16384\n\t"
      "ds_read_b128 v[28:31], v100 offset:17408\n\t"
      "s_sub_u32 s61, s88, s78\n\t"
      "s_sub_i32 s61, s61, 556\n\t"
      "s_lshr_b32 s62, s25, 3\n\t"
      "s_sub_u32 s25, s25, s62\n\t"
      "s_sub_i32 s25, s25, 24\n\t"
      "s_max_i32 s25, s25, 0\n\t"
      "s_sub_i32 s62, s61, 48\n\t"
      "s_max_i32 s62, s62, 0\n\t"
      "s_min_i32 s62, s62, 320\n\t"
      "s_lshr_b32 s62, s62, 2\n\t"
      "s_add_u32 s25, s25, s62\n\t"
      "s_min_i32 s61, s61, 48\n\t"
      "s_max_i32 s61, s61, -48\n\t"
      "s_ashr_i32 s61, s61, 2\n\t"
      "s_add_u32 s26, s26, s61\n\t"
      "s_max_u32 s26, s26, 2100\n\t"
      "s_min_u32 s26, s26, 2900\n\t"
      "s_sub_u32 s62, s88, s27\n\t"
      "s_mov_b32 s27, s88\n\t"
      "s_sub_i32 s62, s62, s24\n\t"
      "s_cmp_gt_i32 s62, 1150\n\t"
      "s_cselect_b32 s62, 200, 0\n\t"
      "s_max_u32 s25, s25, s62\n\t"
      "s_add_u32 s24, s26, s25\n\t"
      "s_add_u32 m0, s55, s71\n\t"
      "s_sub_u32 s71, 204800, s71\n\t"
      "s_add_u32 s40, s40, 1\n\t"
      "s_cmp_lt_u32 s40, 63\n\t"
      "s_cselect_b32 s61, 786432, 0\n\t"
      "s_cselect_b32 s60, s58, 0\n\t"
      "s_add_u32 s44, s44, s61\n\t"
      "s_cmp_lt_u32 s40, 62\n\t"
      "s_cselect_b32 s61, 786432, 0\n\t"
      "s_add_u32 s46, s46, s61\n\t"
      "s_add_u32 s61, s40, 3\n\t"
      "s_and_b32 s62, s61, 3\n\t"
      "s_cselect_b32 s62, 16, 2000\n\t"
      "s_cmp_lt_u32 s61, 64\n\t"
      "s_cselect_b32 s62, s62, 0\n\t"
      "s_add_u32 s50, s50, s62\n\t"
      "s_mov_b32 s63, 0x2000\n\t"
      "s_waitcnt lgkmcnt(8)\n\t"
      "v_mfma_f32_16x16x32_bf16 v[32:35], v[76:79], v[112:115], 0\n\t"
      "v_mfma_f32_16x16x32_bf16 v[36:39], v[80:83], v[112:115], 0\n\t"
      "v_mfma_f32_16x16x32_bf16 v[40:43], v[76:79], v[116:119], 0\n\t"
      "v_mfma_f32_16x16x32_bf16 v[44:47], v[80:83], v[116:119], 0\n\t"
      "ds_read_b128 v[76:79], v103\n\t"
      "ds_read_b128 v[80:83], v103 offset:1024\n\t"
      "ds_read_b128 v[112:115], v100 offset:24576\n\t"
      "ds_read_b128 v[116:119], v100 offset:25600\n\t"
      "s_branch LGATE_3%=\n\t"
      "LFIRST_3%=:\n\t"
      "s_cmp_eq_u32 s40, 0\n\t"
      "s_cbranch_scc1 LWM0_3%=\n\t"
      "s_mov_b32 s63, 0x10000\n\t"
      "LST0_3%=:\n\t"
      "s_waitcnt vmcnt(0)\n\t"
      "v_bitop3_b32 v97, v57, v85, s53 bitop3:0x7e\n\t"
      "v_cmp_ne_u32 vcc, 0, v97\n\t"
      "v_add_u32 v99, v58, v86\n\t"
      "v_subrev_u32 v97, s87, v56\n\t"
      "v_subrev_u32 v98, s87, v84\n\t"
      "v_max_i32 v97, v97, v98\n\t"
      "s_cbranch_vccz LST3_3%=\n\t"
      "buffer_load_dwordx4 v[56:59], v96, s[36:39], s86 offen sc1\n\t"
      "buffer_load_dwordx4 v[84:87], v96, s[36:39], s86 offen offset:128 sc1\n\t"
      "s_sub_u32 s63, s63, 1\n\t"
      "s_cmp_eq_u32 s63, 0\n\t"
      "s_cbranch_scc0 LST0_3%=\n\t"
      "s_waitcnt vmcnt(0)\n\t"
      "LST3_3%=:\n\t"
      "s_nop 1\n\t"
      "v_add_u32_dpp v99, v99, v99 quad_perm:[1,0,3,2] row_mask:0xf bank_mask:0xf\n\t"
      "s_nop 1\n\t"
      "v_add_u32_dpp v99, v99, v99 quad_perm:[2,3,0,1] row_mask:0xf bank_mask:0xf\n\t"
      "s_nop 1\n\t"
      "v_add_u32_dpp v99, v99, v99 row_ror:4 row_mask:0xf bank_mask:0xf\n\t"
      "s_nop 1\n\t"
      "v_add_u32_dpp v99, v99, v99 row_ror:8 row_mask:0xf bank_mask:0xf\n\t"
      "v_mov_b32 v98, v99\n\t"
      "s_nop 1\n\t"
      "v_permlane16_swap_b32 v99, v98\n\t"
      "v_add_u32 v99, v99, v98\n\t"
      "v_mov_b32 v98, v99\n\t"
      "s_nop 1\n\t"
      "v_permlane32_swap_b32 v99, v98\n\t"
      "v_add_u32 v99, v99, v98\n\t"
      "ds_write_b32 v109, v99 offset:55392\n\t"
      "s_nop 1\n\t"
      "v_max_i32_dpp v97, v97, v97 quad_perm:[1,0,3,2] row_mask:0xf bank_mask:0xf\n\t"
      "s_nop 1\n\t"
      "v_max_i32_dpp v97, v97, v97 quad_perm:[2,3,0,1] row_mask:0xf bank_mask:0xf\n\t"
      "s_nop 1\n\t"
      "v_max_i32_dpp v97, v97, v97 row_ror:4 row_mask:0xf bank_mask:0xf\n\t"
      "s_nop 1\n\t"
      "v_max_i32_dpp v97, v97, v97 row_ror:8 row_mask:0xf bank_mask:0xf\n\t"
      "v_mov_b32 v98, v97\n\t"
      "s_nop 1\n\t"
      "v_permlane16_swap_b32 v97, v98\n\t"
      "v_max_i32 v97, v97, v98\n\t"
      "v_mov_b32 v98, v97\n\t"
      "s_nop 1\n\t"
      "v_permlane32_swap_b32 v97, v98\n\t"
      "v_max_i32 v97, v97, v98\n\t"
      "v_max_u32 v99, v59, v87\n\t"
      "s_nop 1\n\t"
      "v_max_u32_dpp v99, v99, v99 quad_perm:[1,0,3,2] row_mask:0xf bank_mask:0xf\n\t"
      "s_nop 1\n\t"
      "v_max_u32_dpp v99, v99, v99 quad_perm:[2,3,0,1] row_mask:0xf bank_mask:0xf\n\t"
      "s_nop 1\n\t"
      "v_max_u32_dpp v99, v99, v99 row_ror:4 row_mask:0xf bank_mask:0xf\n\t"
      "s_nop 1\n\t"
      "v_max_u32_dpp v99, v99, v99 row_ror:8 row_mask:0xf bank_mask:0xf\n\t"
      "v_mov_b32 v98, v99\n\t"
      "s_nop 1\n\t"
      "v_permlane16_swap_b32 v99, v98\n\t"
      "v_max_u32 v99, v99, v98\n\t"
      "v_mov_b32 v98, v99\n\t"
      "s_nop 1\n\t"
      "v_permlane32_swap_b32 v99, v98\n\t"
      "v_max_u32 v99, v99, v98\n\t"
      "v_rcp_f32 v99, v99\n\t"
      "s_nop 0\n\t"
      "v_mul_f32 v99, s83, v99\n\t"
      "v_fma_f32 v99, v99, 0.5, 0.5\n\t"
      "s_mov_b32 s25, 0\n\t"
      "v_mul_f32 v99, 0x4519c000, v99\n\t"
      "v_cvt_u32_f32 v99, v99\n\t"
      "s_nop 0\n\t"
      "v_readfirstlane_b32 s26, v99\n\t"
      "s_waitcnt lgkmcnt(0)\n\t"
      "s_barrier\n\t"
      "ds_read_b32 v99, v109 offset:55392\n\t"
      "v_readfirstlane_b32 s62, v97\n\t"
      "s_waitcnt lgkmcnt(0)\n\t"
      "v_readfirstlane_b32 s61, v99\n\t"
      "s_nop 1\n\t"
      "s_cmp_gt_i32 s61, 0\n\t"
      "s_cselect_b32 s99, 0x2000, 0\n\t"
      "s_xor_b32 s97, s97, s99\n\t"
      "s_min_u32 s62, s62, 500\n\t"
      "s_add_u32 s62, s62, 195\n\t"
      "LST1_3%=:\n\t"
      "s_memrealtime s[92:93]\n\t"
      "s_waitcnt lgkmcnt(0)\n\t"
      "s_sub_u32 s61, s92, s87\n\t"
      "s_cmp_lt_u32 s61, s62\n\t"
      "s_cbranch_scc1 LST1_3%=\n\t"
      "s_barrier\n\t"
      "s_mov_b32 s63, 0x2000\n\t"
      "s_branch LPOLL_3%=\n\t"
      "LWM0_3%=:\n\t"
      "s_mov_b32 s63, 0x10000\n\t"
      "LWM_3%=:\n\t"
      "s_waitcnt vmcnt(0) lgkmcnt(0)\n\t"
      "v_bitop3_b32 v49, v49, v51, s53 bitop3:0x7e\n\t"
      "v_max_u32 v48, v48, v50\n\t"
      "v_cmp_ne_u32 vcc, 0, v49\n\t"
      "s_nop 1\n\t"
      "v_max_u32_dpp v48, v48, v48 quad_perm:[1,0,3,2] row_mask:0xf bank_mask:0xf\n\t"
      "s_nop 1\n\t"
      "v_max_u32_dpp v48, v48, v48 quad_perm:[2,3,0,1] row_mask:0xf bank_mask:0xf\n\t"
      "s_nop 1\n\t"
      "v_max_u32_dpp v48, v48, v48 row_ror:4 row_mask:0xf bank_mask:0xf\n\t"
      "s_nop 1\n\t"
      "v_max_u32_dpp v48, v48, v48 row_ror:8 row_mask:0xf bank_mask:0xf\n\t"
      "v_mov_b32 v49, v48\n\t"
      "s_nop 1\n\t"
      "v_permlane16_swap_b32 v48, v49\n\t"
      "v_max_u32 v48, v48, v49\n\t"
      "v_mov_b32 v49, v48\n\t"
      "s_nop 1\n\t"
      "v_permlane32_swap_b32 v48, v49\n\t"
      "v_max_u32 v48, v48, v49\n\t"
      "v_mul_f32 v49, 0x40a00000, v48\n\t"
      "s_nop 0\n\t"
      "v_readfirstlane_b32 s67, v49\n\t"
      "v_mul_f32 v49, 0x43030000, v49\n\t"
      "s_nop 0\n\t"
      "v_readfirstlane_b32 s66, v49\n\t"
      "s_cbranch_vccz LWM1_3%=\n\t"
      "s_sleep 4\n\t"
      "buffer_load_dwordx2 v[48:49], v52, s[36:39], s94 offen sc1\n\t"
      "buffer_load_dwordx2 v[50:51], v52, s[36:39], s94 offen offset:128 sc1\n\t"
      "s_sub_u32 s63, s63, 1\n\t"
      "s_cmp_lg_u32 s63, 0\n\t"
      "s_cbranch_scc1 LWM_3%=\n\t"
      "s_waitcnt vmcnt(0)\n\t"
      "LWM1_3%=:\n\t"
      "buffer_load_dwordx2 v[120:121], off, s[36:39], s64 sc1\n\t"
      "s_lshr_b32 s67, s67, 23\n\t"
      "s_add_u32 s67, s67, 5\n\t"
      "s_lshr_b32 s66, s66, 23\n\t"
      "s_add_u32 s66, s66, 129\n\t"
      "s_max_u32 s66, s66, 80\n\t"
      "s_mov_b32 s63, 0x2000\n\t"
      "LPOL0_3%=:\n\t"
      "s_memtime s[78:79]\n\t"
      "s_waitcnt lgkmcnt(8)\n\t"
      "v_mfma_f32_16x16x32_bf16 v[32:35], v[0:3], v[8:11], v[32:35]\n\t"
      "v_mfma_f32_16x16x32_bf16 v[36:39], v[4:7], v[8:11], v[36:39]\n\t"
      "v_mfma_f32_16x16x32_bf16 v[40:43], v[0:3], v[12:15], v[40:43]\n\t"
      "v_mfma_f32_16x16x32_bf16 v[44:47], v[4:7], v[12:15], v[44:47]\n\t"
      "v_mov_b64 v[48:49], 0\n\t"
      "v_mov_b64 v[50:51], 0\n\t"
      "v_mov_b64 v[52:53], 0\n\t"
      "v_mov_b64 v[54:55], 0\n\t"
      "s_xor_b32 s69, s97, 1024\n\t"
      "s_xor_b32 s98, s97, 512\n\t"
      "s_xor_b32 s23, s97, 1536\n\t"
      "s_waitcnt lgkmcnt(4)\n\t"
      "v_mfma_f32_16x16x32_bf16 v[32:35], v[16:19], v[24:27], v[32:35]\n\t"
      "v_mfma_f32_16x16x32_bf16 v[36:39], v[20:23], v[24:27], v[36:39]\n\t"
      "v_mfma_f32_16x16x32_bf16 v[40:43], v[16:19], v[28:31], v[40:43]\n\t"
      "v_mfma_f32_16x16x32_bf16 v[44:47], v[20:23], v[28:31], v[44:47]\n\t"
      "s_waitcnt lgkmcnt(0)\n\t"
      "v_mfma_f32_16x16x32_bf16 v[32:35], v[76:79], v[112:115], v[32:35]\n\t"
      "v_mfma_f32_16x16x32_bf16 v[36:39], v[80:83], v[112:115], v[36:39]\n\t"
      "v_mfma_f32_16x16x32_bf16 v[40:43], v[76:79], v[116:119], v[40:43]\n\t"
      "v_mfma_f32_16x16x32_bf16 v[44:47], v[80:83], v[116:119], v[44:47]\n\t"
      "s_mov_b64 exec, 1\n\t"
      "ds_write_b64 v109, v[110:111] offset:55392\n\t"
      "s_mov_b64 exec, -1\n\t"
      "s_nop 4\n\t"
      "v_cvt_pk_bf16_f32 v68, v32, v33\n\t"
      "v_cvt_pk_bf16_f32 v69, v34, v35\n\t"
      "v_cvt_pk_bf16_f32 v70, v36, v37\n\t"
      "v_cvt_pk_bf16_f32 v71, v38, v39\n\t"
      "v_cvt_pk_bf16_f32 v74, v40, v41\n\t"
      "v_cvt_pk_bf16_f32 v75, v42, v43\n\t"
      "v_cvt_pk_bf16_f32 v72, v44, v45\n\t"
      "v_cvt_pk_bf16_f32 v73, v46, v47\n\t"
      "v_mov_b64 v[32:33], 0\n\t"
      "v_mov_b64 v[34:35], 0\n\t"
      "v_mov_b64 v[36:37], 0\n\t"
      "v_mov_b64 v[38:39], 0\n\t"
      "v_mov_b64 v[40:41], 0\n\t"
      "v_mov_b64 v[42:43], 0\n\t"
      "v_mov_b64 v[44:45], 0\n\t"
      "v_mov_b64 v[46:47], 0\n\t"
      "s_waitcnt vmcnt(0)\n\t"
      "s_branch LGO_3%=\n\t"
      "LEARLY_3%=:\n\t"
      "s_cmp_eq_u32 s40, 54\n\t"
      "s_cbranch_scc1 LROLE_3%=\n\t"
      "s_cmp_lt_i32 s40, 0\n\t"
      "s_waitcnt lgkmcnt(0)\n\t"
      "s_cbranch_scc1 LSTART_3%=\n\t"
      "s_xor_b32 s43, s43, s99\n\t"
      "s_xor_b32 s68, s68, s99\n\t"
      "s_cmp_lg_u32 s40, 0\n\t"
      "s_cbranch_scc1 LROLE_3%=\n\t"
      "v_lshlrev_b32 v96, 4, v127\n\t"
      "buffer_load_dwordx4 v[56:59], v96, s[36:39], s86 offen sc1\n\t"
      "buffer_load_dwordx4 v[84:87], v96, s[36:39], s86 offen offset:128 sc1\n\t"
      "s_branch LROLE_3%=\n\t"
      "LSTART_3%=:\n\t"
      "s_memrealtime s[92:93]\n\t"
      "s_memtime s[90:91]\n\t"
      "s_mov_b64 exec, 1\n\t"
      "v_mov_b32 v57, s53\n\t"
      "v_mov_b32 v58, s65\n\t"
      "s_add_u32 s86, s94, 208640\n\t"
      "s_add_u32 s61, s95, 208640\n\t"
      "s_waitcnt lgkmcnt(0)\n\t"
      "v_mov_b32 v56, s92\n\t"
      "s_mov_b32 s87, s92\n\t"
      "s_sub_u32 s80, s90, s80\n\t"
      "s_sub_u32 s82, s92, s82\n\t"
      "v_cvt_f32_u32 v59, s82\n\t"
      "v_cvt_f32_u32 v63, s80\n\t"
      "v_rcp_f32 v59, v59\n\t"
      "s_nop 0\n\t"
      "v_mul_f32 v59, v59, v63\n\t"
      "s_nop 0\n\t"
      "v_readfirstlane_b32 s83, v59\n\t"
      "buffer_store_dwordx4 v[56:59], off, s[36:39], s61 sc1\n\t"
      "s_mov_b64 exec, -1\n\t"
      "s_branch LUTOK_3%=\n\t"
      "LSLOW_3%=:\n\t"
      "s_mov_b32 s63, 0x10000\n\t"
      "LSLOW1_3%=:\n\t"
      "buffer_load_dwordx2 v[92:93], off, s[36:39], s64 sc1\n\t"
      "s_waitcnt vmcnt(0)\n\t"
      "v_bitop3_b32 v92, v92, v93, s53 bitop3:0x7e\n\t"
      "s_nop 0\n\t"
      "v_readfirstlane_b32 s56, v92\n\t"
      "s_nop 1\n\t"
      "s_cmp_eq_u32 s56, 0\n\t"
      "s_cbranch_scc1 LSLOW2_3%=\n\t"
      "s_sleep 8\n\t"
      "s_sub_u32 s63, s63, 1\n\t"
      "s_cmp_lg_u32 s63, 0\n\t"
      "s_cbranch_scc1 LSLOW1_3%=\n\t"
      "LSLOW2_3%=:\n\t"
      "s_mov_b32 m0, s45\n\t"
      "s_sub_u32 s61, 204800, s71\n\t"
      "buffer_load_dwordx4 v65, s[36:39], s44 offen sc1 lds\n\t"
      "buffer_load_dwordx4 v65, s[36:39], s44 offen offset:1024 sc1 lds\n\t"
      "s_add_u32 m0, s55, s61\n\t"
      "s_nop 0\n\t"
      "buffer_load_dwordx4 v127, s[36:39], s46 offen sc1 lds\n\t"
      "buffer_load_dwordx4 v127, s[36:39], s46 offen offset:1024 sc1 lds\n\t"
      "s_branch LWIN1_3%=\n\t"
      "LMX_3%=:\n\t"
      "s_waitcnt lgkmcnt(0)\n\t"
      "s_nop 7\n\t"
      "v_smfmac_f32_16x16x64_bf16 v[52:55], v[24:27], v[240:247], v122\n\t"
      "v_smfmac_f32_16x16x64_bf16 v[52:55], v[20:23], v[232:239], v122\n\t"
      "v_smfmac_f32_16x16x64_bf16 v[52:55], v[16:19], v[224:231], v122\n\t"
      "s_nop 7\n\t"
      "v_add_f32 v53, v53, v55\n\t"
      "v_add_f32_e64 v52, v52, v54\n\t"
      "v_cvt_pk_bf16_f32 v66, v52, v53\n\t"
      "s_add_u32 s62, s61, 0x83400000\n\t"
      "v_mov_b32_e64 v126, s62\n\t"
      "v_max3_f32 v82, v112, v113, v114\n\t"
      "v_max_f32 v82, v82, v115\n\t"
      "s_nop 1\n\t"
      "v_max_f32_dpp v82, v82, v82 quad_perm:[2,3,0,1] row_mask:0xf bank_mask:0xf\n\t"
      "v_max3_f32 v83, v82, v120, v121\n\t"
      "v_max3_f32 v60, v82, v120, v121\n\t"
      "s_nop 1\n\t"
      "v_permlane16_swap_b32 v83, v60\n\t"
      "v_max_f32 v61, v83, v60\n\t"
      "v_max_f32 v60, v83, v60\n\t"
      "s_nop 1\n\t"
      "v_permlane32_swap_b32 v61, v60\n\t"
      "v_max_f32 v83, v61, v60\n\t"
      "v_sub_f32 v112, v112, v83\n\t"
      "v_sub_f32 v113, v113, v83\n\t"
      "v_sub_f32 v114, v114, v83\n\t"
      "v_sub_f32 v115, v115, v83\n\t"
      "v_sub_f32 v120, v120, v83\n\t"
      "v_sub_f32 v121, v121, v83\n\t"
      "v_exp_f32_e64 v56, v112\n\t"
      "v_exp_f32 v57, v113\n\t"
      "v_exp_f32_e64 v58, v114\n\t"
      "v_exp_f32 v59, v115\n\t"
      "v_exp_f32 v80, v120\n\t"
      "v_exp_f32 v81, v121\n\t"
      "v_cvt_pk_bf16_f32 v96, v56, v57\n\t"
      "v_cvt_pk_bf16_f32 v97, v58, v59\n\t"
      "v_cvt_pk_bf16_f32 v108, v80, v81\n\t"
      "v_mov_b32_dpp v98, v96 quad_perm:[2,3,0,1] row_mask:0xf bank_mask:0xf\n\t"
      "v_mov_b32_dpp v99, v97 quad_perm:[2,3,0,1] row_mask:0xf bank_mask:0xf\n\t"
      "v_mfma_f32_16x16x32_bf16 v[48:51], v[108:111], v[88:91], 0\n\t"
      "s_nop 0\n\t"
      "v_mfma_f32_16x16x32_bf16 v[48:51], v[96:99], v[88:91], v[48:51]\n\t"
      "v_mfma_f32_16x16x32_bf16 v[84:87], v[108:111], v[64:67], 0\n\t"
      "v_mfma_f32_16x16x32_bf16 v[84:87], v[96:99], v[68:71], v[84:87]\n\t"
      "v_mfma_f32_16x16x32_bf16 v[92:95], v[96:99], v[72:75], 0\n\t"
      "v_mfma_f32_16x16x32_bf16 v[92:95], v[108:111], v[66:69], v[92:95]\n\t"
      "s_nop 3\n\t"
      "v_rcp_f32_e64 v48, v48\n\t"
      "v_rcp_f32 v49, v49\n\t"
      "v_fma_f32 v84, v84, v48, s96\n\t"
      "buffer_atomic_add v84, v123, s[36:39], s97 offen\n\t"
      "v_fma_f32 v85, v85, v49, s96\n\t"
      "buffer_atomic_add v85, v123, s[36:39], s98 offen\n\t"
      "v_fma_f32 v94, v94, v48, s96\n\t"
      "buffer_atomic_add v94, v123, s[36:39], s69 offen\n\t"
      "v_fma_f32 v95, v95, v49, s96\n\t"
      "buffer_atomic_add v95, v123, s[36:39], s23 offen\n\t"
      "s_add_u32 s97, s97, 0x8000\n\t"
      "s_branch LWIN_3%=\n\t"
      "LR0_3%=:\n\t"
      "buffer_load_dwordx4 v[84:87], v60, s[36:39], s43 offen sc1\n\t"
      "buffer_load_dwordx4 v[88:91], v60, s[36:39], s43 offen offset:1024 sc1\n\t"
      "s_waitcnt vmcnt(2)\n\t"
      "buffer_load_dwordx4 v[92:95], v60, s[36:39], s68 offen sc1\n\t"
      "buffer_load_dwordx4 v[96:99], v60, s[36:39], s68 offen offset:1024 sc1\n\t"
      "s_sub_u32 s63, s63, 1\n\t"
      "s_waitcnt vmcnt(2) lgkmcnt(0)\n\t"
      "v_bitop3_b32 v64, v84, v85, v86 bitop3:0x80\n\t"
      "v_bitop3_b32 v64, v64, v87, v88 bitop3:0x80\n\t"
      "v_bitop3_b32 v64, v64, v89, v90 bitop3:0x80\n\t"
      "v_bitop3_b32 v64, v64, v91, s58 bitop3:0x80\n\t"
      "v_cmp_eq_u32 vcc, 0, v64\n\t"
      "v_pk_fma_f32 v[8:9], v[84:85], s[56:57], v[126:127] op_sel:[0,1,0] op_sel_hi:[1,1,0]\n\t"
      "v_pk_fma_f32 v[10:11], v[86:87], s[56:57], v[126:127] op_sel:[0,1,0] op_sel_hi:[1,1,0]\n\t"
      "v_cvt_pk_bf16_f32 v12, v8, v9\n\t"
      "v_cvt_pk_bf16_f32 v13, v10, v11\n\t"
      "ds_write_b64 v127, v[12:13] offset:44032\n\t"
      "v_pk_fma_f32 v[4:5], v[88:89], s[56:57], v[126:127] op_sel:[0,1,0] op_sel_hi:[1,1,0]\n\t"
      "v_pk_fma_f32 v[6:7], v[90:91], s[56:57], v[126:127] op_sel:[0,1,0] op_sel_hi:[1,1,0]\n\t"
      "v_cvt_pk_bf16_f32 v14, v4, v5\n\t"
      "v_cvt_pk_bf16_f32 v15, v6, v7\n\t"
      "ds_write_b64 v127, v[14:15] offset:44040\n\t"
      "s_memtime s[88:89]\n\t"
      "s_cmp_le_i32 s63, 0\n\t"
      "s_waitcnt lgkmcnt(0)\n\t"
      "s_cbranch_scc1 LRZ_3%=\n\t"
      "s_cbranch_vccnz LR0_3%=\n\t"
      "LRZ_3%=:\n\t"
      "s_sub_u32 s88, s88, 0\n\t"
      "v_mov_b64 v[32:33], 0\n\t"
      "v_mov_b64 v[34:35], 0\n\t"
      "v_mov_b64 v[36:37], 0\n\t"
      "v_mov_b64 v[38:39], 0\n\t"
      "v_mov_b64 v[40:41], 0\n\t"
      "v_mov_b64 v[42:43], 0\n\t"
      "v_mov_b64 v[44:45], 0\n\t"
      "v_mov_b64 v[46:47], 0\n\t"
      "v_mov_b64 v[48:49], 0\n\t"
      "v_mov_b64 v[50:51], 0\n\t"
      "v_mov_b64 v[52:53], 0\n\t"
      "v_mov_b64 v[54:55], 0\n\t"
      "v_mfma_f32_16x16x32_bf16 v[56:59], v[12:15], v[12:15], 0\n\t"
      "v_smfmac_f32_16x16x64_bf16 v[36:39], v[12:15], a[88:95], v122\n\t"
      "v_smfmac_f32_16x16x64_bf16 v[44:47], v[12:15], a[216:223], v122\n\t"
      "s_branch LC0_3%=\n\t"
      "LCOLD%=:\n\t"
      "LR1_0%=:\n\t"
      "v_bitop3_b32 v64, v92, v93, v94 bitop3:0x80\n\t"
      "v_bitop3_b32 v64, v64, v95, v96 bitop3:0x80\n\t"
      "v_bitop3_b32 v64, v64, v97, v98 bitop3:0x80\n\t"
      "v_bitop3_b32 v64, v64, v99, s58 bitop3:0x80\n\t"
      "v_cmp_eq_u32 vcc, 0, v64\n\t"
      "s_nop 0\n\t"
      "s_cbranch_vccz LRX_0%=\n\t"
      "LRY_0%=:\n\t"
      "buffer_load_dwordx4 v[92:95], v60, s[36:39], s68 offen sc1\n\t"
      "buffer_load_dwordx4 v[96:99], v60, s[36:39], s68 offen offset:1024 sc1\n\t"
      "s_sub_u32 s63, s63, 1\n\t"
      "s_waitcnt vmcnt(0) lgkmcnt(0)\n\t"
      "v_bitop3_b32 v64, v92, v93, v94 bitop3:0x80\n\t"
      "v_bitop3_b32 v64, v64, v95, v96 bitop3:0x80\n\t"
      "v_bitop3_b32 v64, v64, v97, v98 bitop3:0x80\n\t"
      "v_bitop3_b32 v64, v64, v99, s58 bitop3:0x80\n\t"
      "v_cmp_eq_u32 vcc, 0, v64\n\t"
      "v_pk_fma_f32 v[20:21], v[92:93], s[56:57], v[126:127] op_sel:[0,1,0] op_sel_hi:[1,1,0]\n\t"
      "v_pk_fma_f32 v[22:23], v[94:95], s[56:57], v[126:127] op_sel:[0,1,0] op_sel_hi:[1,1,0]\n\t"
      "v_cvt_pk_bf16_f32 v16, v20, v21\n\t"
      "v_cvt_pk_bf16_f32 v17, v22, v23\n\t"
      "ds_write_b64 v127, v[16:17] offset:45056\n\t"
      "v_pk_fma_f32 v[24:25], v[96:97], s[56:57], v[126:127] op_sel:[0,1,0] op_sel_hi:[1,1,0]\n\t"
      "v_pk_fma_f32 v[26:27], v[98:99], s[56:57], v[126:127] op_sel:[0,1,0] op_sel_hi:[1,1,0]\n\t"
      "v_cvt_pk_bf16_f32 v18, v24, v25\n\t"
      "v_cvt_pk_bf16_f32 v19, v26, v27\n\t"
      "ds_write_b64 v127, v[18:19] offset:45064\n\t"
      "s_cmp_le_i32 s63, 0\n\t"
      "s_waitcnt lgkmcnt(0)\n\t"
      "s_cbranch_scc1 LRX_0%=\n\t"
      "s_cbranch_vccnz LRY_0%=\n\t"
      "LRX_0%=:\n\t"
      "s_cmp_eq_u32 s40, 63\n\t"
      "s_cbranch_scc1 LLAST_0%=\n\t"
      "s_branch LC1_0%=\n\t"
      "LLAST_0%=:\n\t"
      "s_waitcnt vmcnt(0) lgkmcnt(0)\n\t"
      "s_barrier\n\t"
      "ds_read_b128 v[20:23], v127 offset:46080\n\t"
      "ds_read_b128 v[24:27], v127 offset:47104\n\t"
      "ds_read_b128 v[28:31], v127 offset:48128\n\t"
      "v_smfmac_f32_16x16x64_bf16 v[40:43], v[16:19], a[160:167], v122\n\t"
      "v_smfmac_f32_16x16x64_bf16 v[44:47], v[16:19], a[224:231], v122\n\t"
      "s_waitcnt lgkmcnt(2)\n\t"
      "v_smfmac_f32_16x16x64_bf16 v[40:43], v[20:23], a[168:175], v122\n\t"
      "v_smfmac_f32_16x16x64_bf16 v[44:47], v[20:23], a[232:239], v122\n\t"
      "s_waitcnt lgkmcnt(1)\n\t"
      "v_smfmac_f32_16x16x64_bf16 v[44:47], v[24:27], a[240:247], v122\n\t"
      "v_smfmac_f32_16x16x64_bf16 v[40:43], v[24:27], a[176:183], v122\n\t"
      "s_waitcnt lgkmcnt(0)\n\t"
      "v_smfmac_f32_16x16x64_bf16 v[44:47], v[28:31], a[248:255], v122\n\t"
      "v_smfmac_f32_16x16x64_bf16 v[40:43], v[28:31], a[184:191], v122\n\t"
      "s_nop 7\n\t"
      "v_add_f32 v40, v40, v42\n\t"
      "v_add_f32 v41, v41, v43\n\t"
      "v_cvt_pk_bf16_f32 v120, v40, v41\n\t"
      "v_add_f32 v44, v44, v46\n\t"
      "v_add_f32 v45, v45, v47\n\t"
      "v_cvt_pk_bf16_f32 v121, v44, v45\n\t"
      "ds_write_b64 v125, v[120:121] offset:2080\n\t"
      "s_waitcnt lgkmcnt(0)\n\t"
      "s_barrier\n\t"
      "ds_read_b64_tr_b16 v[40:41], v124 offset:51232\n\t"
      "ds_read_b64_tr_b16 v[42:43], v124 offset:51752\n\t"
      "ds_read_b64_tr_b16 v[44:45], v124 offset:52272\n\t"
      "ds_read_b64_tr_b16 v[46:47], v124 offset:52792\n\t"
      "v_add_u32 v100, s55, v127\n\t"
      "ds_read_b128 v[60:63], v100 offset:57344\n\t"
      "s_waitcnt lgkmcnt(0)\n\t"
      "s_branch LROLE_0%=\n\t"
      "LFIN_0%=:\n\t"
      "s_waitcnt lgkmcnt(0)\n\t"
      "s_barrier\n\t"
      "ds_read_b64 v[54:55], v124 offset:53312\n\t"
      "s_waitcnt lgkmcnt(0)\n\t"
      "v_add_f32 v48, v48, v54\n\t"
      "v_add_f32 v49, v49, v55\n\t"
      "v_cvt_pk_bf16_f32 v48, v48, v49\n\t"
      "buffer_store_dword v48, v62, s[36:39], s28 offen sc1\n\t"
      "s_branch LEND%=\n\t"
      "LR1_1%=:\n\t"
      "v_bitop3_b32 v64, v92, v93, v94 bitop3:0x80\n\t"
      "v_bitop3_b32 v64, v64, v95, v96 bitop3:0x80\n\t"
      "v_bitop3_b32 v64, v64, v97, v98 bitop3:0x80\n\t"
      "v_bitop3_b32 v64, v64, v99, s58 bitop3:0x80\n\t"
      "v_cmp_eq_u32 vcc, 0, v64\n\t"
      "s_nop 0\n\t"
      "s_cbranch_vccz LRX_1%=\n\t"
      "LRY_1%=:\n\t"
      "buffer_load_dwordx4 v[92:95], v60, s[36:39], s68 offen sc1\n\t"
      "buffer_load_dwordx4 v[96:99], v60, s[36:39], s68 offen offset:1024 sc1\n\t"
      "s_sub_u32 s63, s63, 1\n\t"
      "s_waitcnt vmcnt(0) lgkmcnt(0)\n\t"
      "v_bitop3_b32 v64, v92, v93, v94 bitop3:0x80\n\t"
      "v_bitop3_b32 v64, v64, v95, v96 bitop3:0x80\n\t"
      "v_bitop3_b32 v64, v64, v97, v98 bitop3:0x80\n\t"
      "v_bitop3_b32 v64, v64, v99, s58 bitop3:0x80\n\t"
      "v_cmp_eq_u32 vcc, 0, v64\n\t"
      "v_pk_fma_f32 v[16:17], v[92:93], s[56:57], v[126:127] op_sel:[0,1,0] op_sel_hi:[1,1,0]\n\t"
      "v_pk_fma_f32 v[18:19], v[94:95], s[56:57], v[126:127] op_sel:[0,1,0] op_sel_hi:[1,1,0]\n\t"
      "v_cvt_pk_bf16_f32 v20, v16, v17\n\t"
      "v_cvt_pk_bf16_f32 v21, v18, v19\n\t"
      "ds_write_b64 v127, v[20:21] offset:46080\n\t"
      "v_pk_fma_f32 v[28:29], v[96:97], s[56:57], v[126:127] op_sel:[0,1,0] op_sel_hi:[1,1,0]\n\t"
      "v_pk_fma_f32 v[30:31], v[98:99], s[56:57], v[126:127] op_sel:[0,1,0] op_sel_hi:[1,1,0]\n\t"
      "v_cvt_pk_bf16_f32 v22, v28, v29\n\t"
      "v_cvt_pk_bf16_f32 v23, v30, v31\n\t"
      "ds_write_b64 v127, v[22:23] offset:46088\n\t"
      "s_cmp_le_i32 s63, 0\n\t"
      "s_waitcnt lgkmcnt(0)\n\t"
      "s_cbranch_scc1 LRX_1%=\n\t"
      "s_cbranch_vccnz LRY_1%=\n\t"
      "LRX_1%=:\n\t"
      "s_cmp_eq_u32 s40, 63\n\t"
      "s_cbranch_scc1 LLAST_1%=\n\t"
      "s_branch LC1_1%=\n\t"
      "LLAST_1%=:\n\t"
      "s_waitcnt vmcnt(0) lgkmcnt(0)\n\t"
      "s_barrier\n\t"
      "ds_read_b128 v[16:19], v127 offset:45056\n\t"
      "ds_read_b128 v[28:31], v127 offset:48128\n\t"
      "ds_read_b128 v[24:27], v127 offset:47104\n\t"
      "v_smfmac_f32_16x16x64_bf16 v[40:43], v[20:23], a[168:175], v122\n\t"
      "v_smfmac_f32_16x16x64_bf16 v[44:47], v[20:23], a[232:239], v122\n\t"
      "s_waitcnt lgkmcnt(2)\n\t"
      "v_smfmac_f32_16x16x64_bf16 v[40:43], v[16:19], a[160:167], v122\n\t"
      "v_smfmac_f32_16x16x64_bf16 v[44:47], v[16:19], a[224:231], v122\n\t"
      "s_waitcnt lgkmcnt(1)\n\t"
      "v_smfmac_f32_16x16x64_bf16 v[44:47], v[28:31], a[248:255], v122\n\t"
      "v_smfmac_f32_16x16x64_bf16 v[40:43], v[28:31], a[184:191], v122\n\t"
      "s_waitcnt lgkmcnt(0)\n\t"
      "v_smfmac_f32_16x16x64_bf16 v[44:47], v[24:27], a[240:247], v122\n\t"
      "v_smfmac_f32_16x16x64_bf16 v[40:43], v[24:27], a[176:183], v122\n\t"
      "s_nop 7\n\t"
      "v_add_f32 v40, v40, v42\n\t"
      "v_add_f32 v41, v41, v43\n\t"
      "v_cvt_pk_bf16_f32 v120, v40, v41\n\t"
      "v_add_f32 v44, v44, v46\n\t"
      "v_add_f32 v45, v45, v47\n\t"
      "v_cvt_pk_bf16_f32 v121, v44, v45\n\t"
      "ds_write_b64 v125, v[120:121] offset:2080\n\t"
      "s_waitcnt lgkmcnt(0)\n\t"
      "s_barrier\n\t"
      "ds_read_b64_tr_b16 v[40:41], v124 offset:51232\n\t"
      "ds_read_b64_tr_b16 v[42:43], v124 offset:51752\n\t"
      "ds_read_b64_tr_b16 v[44:45], v124 offset:52272\n\t"
      "ds_read_b64_tr_b16 v[46:47], v124 offset:52792\n\t"
      "v_add_u32 v100, s55, v127\n\t"
      "ds_read_b128 v[60:63], v100 offset:57344\n\t"
      "s_waitcnt lgkmcnt(0)\n\t"
      "s_branch LROLE_1%=\n\t"
      "LFIN_1%=:\n\t"
      "s_waitcnt lgkmcnt(0)\n\t"
      "s_barrier\n\t"
      "ds_read_b64 v[54:55], v124 offset:53832\n\t"
      "s_waitcnt lgkmcnt(0)\n\t"
      "v_add_f32 v48, v48, v54\n\t"
      "v_add_f32 v49, v49, v55\n\t"
      "v_cvt_pk_bf16_f32 v48, v48, v49\n\t"
      "buffer_store_dword v48, v62, s[36:39], s28 offen sc1\n\t"
      "s_branch LEND%=\n\t"
      "LWK1_2%=:\n\t"
      "buffer_store_dwordx4 v[44:47], v61, s[36:39], s48 offen sc1\n\t"
      "s_branch LW4_2%=\n\t"
      "LR1_2%=:\n\t"
      "v_bitop3_b32 v64, v92, v93, v94 bitop3:0x80\n\t"
      "v_bitop3_b32 v64, v64, v95, v96 bitop3:0x80\n\t"
      "v_bitop3_b32 v64, v64, v97, v98 bitop3:0x80\n\t"
      "v_bitop3_b32 v64, v64, v99, s58 bitop3:0x80\n\t"
      "v_cmp_eq_u32 vcc, 0, v64\n\t"
      "s_nop 0\n\t"
      "s_cbranch_vccz LRX_2%=\n\t"
      "LRY_2%=:\n\t"
      "buffer_load_dwordx4 v[92:95], v60, s[36:39], s68 offen sc1\n\t"
      "buffer_load_dwordx4 v[96:99], v60, s[36:39], s68 offen offset:1024 sc1\n\t"
      "s_sub_u32 s63, s63, 1\n\t"
      "s_waitcnt vmcnt(0) lgkmcnt(0)\n\t"
      "v_bitop3_b32 v64, v92, v93, v94 bitop3:0x80\n\t"
      "v_bitop3_b32 v64, v64, v95, v96 bitop3:0x80\n\t"
      "v_bitop3_b32 v64, v64, v97, v98 bitop3:0x80\n\t"
      "v_bitop3_b32 v64, v64, v99, s58 bitop3:0x80\n\t"
      "v_cmp_eq_u32 vcc, 0, v64\n\t"
      "v_pk_fma_f32 v[28:29], v[92:93], s[56:57], v[126:127] op_sel:[0,1,0] op_sel_hi:[1,1,0]\n\t"
      "v_pk_fma_f32 v[30:31], v[94:95], s[56:57], v[126:127] op_sel:[0,1,0] op_sel_hi:[1,1,0]\n\t"
      "v_cvt_pk_bf16_f32 v24, v28, v29\n\t"
      "v_cvt_pk_bf16_f32 v25, v30, v31\n\t"
      "ds_write_b64 v127, v[24:25] offset:47104\n\t"
      "v_pk_fma_f32 v[16:17], v[96:97], s[56:57], v[126:127] op_sel:[0,1,0] op_sel_hi:[1,1,0]\n\t"
      "v_pk_fma_f32 v[18:19], v[98:99], s[56:57], v[126:127] op_sel:[0,1,0] op_sel_hi:[1,1,0]\n\t"
      "v_cvt_pk_bf16_f32 v26, v16, v17\n\t"
      "v_cvt_pk_bf16_f32 v27, v18, v19\n\t"
      "ds_write_b64 v127, v[26:27] offset:47112\n\t"
      "s_cmp_le_i32 s63, 0\n\t"
      "s_waitcnt lgkmcnt(0)\n\t"
      "s_cbranch_scc1 LRX_2%=\n\t"
      "s_cbranch_vccnz LRY_2%=\n\t"
      "LRX_2%=:\n\t"
      "s_cmp_eq_u32 s40, 63\n\t"
      "s_cbranch_scc1 LLAST_2%=\n\t"
      "s_branch LC1_2%=\n\t"
      "LLAST_2%=:\n\t"
      "s_waitcnt vmcnt(0) lgkmcnt(0)\n\t"
      "s_barrier\n\t"
      "ds_read_b128 v[28:31], v127 offset:48128\n\t"
      "ds_read_b128 v[16:19], v127 offset:45056\n\t"
      "ds_read_b128 v[20:23], v127 offset:46080\n\t"
      "v_smfmac_f32_16x16x64_bf16 v[40:43], v[24:27], a[176:183], v122\n\t"
      "v_smfmac_f32_16x16x64_bf16 v[44:47], v[24:27], a[240:247], v122\n\t"
      "s_waitcnt lgkmcnt(2)\n\t"
      "v_smfmac_f32_16x16x64_bf16 v[40:43], v[28:31], a[184:191], v122\n\t"
      "v_smfmac_f32_16x16x64_bf16 v[44:47], v[28:31], a[248:255], v122\n\t"
      "s_waitcnt lgkmcnt(1)\n\t"
      "v_smfmac_f32_16x16x64_bf16 v[44:47], v[16:19], a[224:231], v122\n\t"
      "v_smfmac_f32_16x16x64_bf16 v[40:43], v[16:19], a[160:167], v122\n\t"
      "s_waitcnt lgkmcnt(0)\n\t"
      "v_smfmac_f32_16x16x64_bf16 v[44:47], v[20:23], a[232:239], v122\n\t"
      "v_smfmac_f32_16x16x64_bf16 v[40:43], v[20:23], a[168:175], v122\n\t"
      "s_nop 7\n\t"
      "v_add_f32 v40, v40, v42\n\t"
      "v_add_f32 v41, v41, v43\n\t"
      "v_cvt_pk_bf16_f32 v120, v40, v41\n\t"
      "v_add_f32 v44, v44, v46\n\t"
      "v_add_f32 v45, v45, v47\n\t"
      "v_cvt_pk_bf16_f32 v121, v44, v45\n\t"
      "ds_write_b64 v125, v[120:121] offset:2080\n\t"
      "s_waitcnt lgkmcnt(0)\n\t"
      "s_barrier\n\t"
      "ds_read_b64_tr_b16 v[40:41], v124 offset:51232\n\t"
      "ds_read_b64_tr_b16 v[42:43], v124 offset:51752\n\t"
      "ds_read_b64_tr_b16 v[44:45], v124 offset:52272\n\t"
      "ds_read_b64_tr_b16 v[46:47], v124 offset:52792\n\t"
      "v_add_u32 v100, s55, v127\n\t"
      "ds_read_b128 v[60:63], v100 offset:57344\n\t"
      "s_waitcnt lgkmcnt(0)\n\t"
      "s_branch LROLE_2%=\n\t"
      "LFIN_2%=:\n\t"
      "s_waitcnt lgkmcnt(0)\n\t"
      "s_barrier\n\t"
      "s_branch LEND%=\n\t"
      "LR1_3%=:\n\t"
      "v_bitop3_b32 v64, v92, v93, v94 bitop3:0x80\n\t"
      "v_bitop3_b32 v64, v64, v95, v96 bitop3:0x80\n\t"
      "v_bitop3_b32 v64, v64, v97, v98 bitop3:0x80\n\t"
      "v_bitop3_b32 v64, v64, v99, s58 bitop3:0x80\n\t"
      "v_cmp_eq_u32 vcc, 0, v64\n\t"
      "s_nop 0\n\t"
      "s_cbranch_vccz LRX_3%=\n\t"
      "LRY_3%=:\n\t"
      "buffer_load_dwordx4 v[92:95], v60, s[36:39], s68 offen sc1\n\t"
      "buffer_load_dwordx4 v[96:99], v60, s[36:39], s68 offen offset:1024 sc1\n\t"
      "s_sub_u32 s63, s63, 1\n\t"
      "s_waitcnt vmcnt(0) lgkmcnt(0)\n\t"
      "v_bitop3_b32 v64, v92, v93, v94 bitop3:0x80\n\t"
      "v_bitop3_b32 v64, v64, v95, v96 bitop3:0x80\n\t"
      "v_bitop3_b32 v64, v64, v97, v98 bitop3:0x80\n\t"
      "v_bitop3_b32 v64, v64, v99, s58 bitop3:0x80\n\t"
      "v_cmp_eq_u32 vcc, 0, v64\n\t"
      "v_pk_fma_f32 v[24:25], v[92:93], s[56:57], v[126:127] op_sel:[0,1,0] op_sel_hi:[1,1,0]\n\t"
      "v_pk_fma_f32 v[26:27], v[94:95], s[56:57], v[126:127] op_sel:[0,1,0] op_sel_hi:[1,1,0]\n\t"
      "v_cvt_pk_bf16_f32 v28, v24, v25\n\t"
      "v_cvt_pk_bf16_f32 v29, v26, v27\n\t"
      "ds_write_b64 v127, v[28:29] offset:48128\n\t"
      "v_pk_fma_f32 v[20:21], v[96:97], s[56:57], v[126:127] op_sel:[0,1,0] op_sel_hi:[1,1,0]\n\t"
      "v_pk_fma_f32 v[22:23], v[98:99], s[56:57], v[126:127] op_sel:[0,1,0] op_sel_hi:[1,1,0]\n\t"
      "v_cvt_pk_bf16_f32 v30, v20, v21\n\t"
      "v_cvt_pk_bf16_f32 v31, v22, v23\n\t"
      "ds_write_b64 v127, v[30:31] offset:48136\n\t"
      "s_memtime s[88:89]\n\t"
      "s_cmp_le_i32 s63, 0\n\t"
      "s_waitcnt lgkmcnt(0)\n\t"
      "s_cbranch_scc1 LRX_3%=\n\t"
      "s_cbranch_vccnz LRY_3%=\n\t"
      "s_sub_u32 s88, s88, 336\n\t"
      "LRX_3%=:\n\t"
      "s_cmp_eq_u32 s40, 63\n\t"
      "s_cbranch_scc1 LLAST_3%=\n\t"
      "s_branch LC1_3%=\n\t"
      "LLAST_3%=:\n\t"
      "s_waitcnt vmcnt(0) lgkmcnt(0)\n\t"
      "s_barrier\n\t"
      "ds_read_b128 v[24:27], v127 offset:47104\n\t"
      "ds_read_b128 v[20:23], v127 offset:46080\n\t"
      "ds_read_b128 v[16:19], v127 offset:45056\n\t"
      "v_smfmac_f32_16x16x64_bf16 v[40:43], v[28:31], a[184:191], v122\n\t"
      "v_smfmac_f32_16x16x64_bf16 v[44:47], v[28:31], a[248:255], v122\n\t"
      "s_waitcnt lgkmcnt(2)\n\t"
      "v_smfmac_f32_16x16x64_bf16 v[40:43], v[24:27], a[176:183], v122\n\t"
      "v_smfmac_f32_16x16x64_bf16 v[44:47], v[24:27], a[240:247], v122\n\t"
      "s_waitcnt lgkmcnt(1)\n\t"
      "v_smfmac_f32_16x16x64_bf16 v[44:47], v[20:23], a[232:239], v122\n\t"
      "v_smfmac_f32_16x16x64_bf16 v[40:43], v[20:23], a[168:175], v122\n\t"
      "s_waitcnt lgkmcnt(0)\n\t"
      "v_smfmac_f32_16x16x64_bf16 v[44:47], v[16:19], a[224:231], v122\n\t"
      "v_smfmac_f32_16x16x64_bf16 v[40:43], v[16:19], a[160:167], v122\n\t"
      "s_nop 7\n\t"
      "v_add_f32 v40, v40, v42\n\t"
      "v_add_f32 v41, v41, v43\n\t"
      "v_cvt_pk_bf16_f32 v120, v40, v41\n\t"
      "v_add_f32 v44, v44, v46\n\t"
      "v_add_f32 v45, v45, v47\n\t"
      "v_cvt_pk_bf16_f32 v121, v44, v45\n\t"
      "ds_write_b64 v125, v[120:121] offset:2080\n\t"
      "s_waitcnt lgkmcnt(0)\n\t"
      "s_barrier\n\t"
      "ds_read_b64_tr_b16 v[40:41], v124 offset:51232\n\t"
      "ds_read_b64_tr_b16 v[42:43], v124 offset:51752\n\t"
      "ds_read_b64_tr_b16 v[44:45], v124 offset:52272\n\t"
      "ds_read_b64_tr_b16 v[46:47], v124 offset:52792\n\t"
      "v_add_u32 v100, s55, v127\n\t"
      "ds_read_b128 v[60:63], v100 offset:57344\n\t"
      "s_waitcnt lgkmcnt(0)\n\t"
      "s_branch LROLE_3%=\n\t"
      "LFIN_3%=:\n\t"
      "s_waitcnt lgkmcnt(0)\n\t"
      "s_barrier\n\t"
      "s_branch LEND%=\n\t"
      "LEND%=:\n\t"
      "s_waitcnt vmcnt(2) lgkmcnt(0)\n\t"
      "s_cmp_lg_u32 s42, 9\n\t"
      "s_cbranch_scc1 LOUT%=\n\t"
      "s_waitcnt vmcnt(0)\n\t"
      "LOUT%=:\n\t"
      : "+{v[116:123]}"(c0), "+{v[124:127]}"(c1), "+{v[0:15]}"(sc), "+{v[16:31]}"(s2), "+{v[100:107]}"(c3), "+{v[108:115]}"(cc), "+s"(A), "+s"(b), "+s"(wv), "+{s87}"(tch)
      :
      : "memory", "vcc", "scc", "v32", "v33", "v34", "v35", "v36", "v37", "v38", "v39", "v40", "v41", "v42", "v43", "v44", "v45", "v46", "v47", "v48", "v49", "v50", "v51", "v52", "v53", "v54", "v55", "v56", "v57", "v58", "v59", "v60", "v61", "v62", "v63", "v64", "v65", "v66", "v67", "v68", "v69", "v70", "v71", "v72", "v73", "v74", "v75", "v76", "v77", "v78", "v79", "v80", "v81", "v82", "v83", "v84", "v85", "v86", "v87", "v88", "v89", "v90", "v91", "v92", "v93", "v94", "v95", "v96", "v97", "v98", "v99", "v128", "v129", "v130", "v131", "v132", "v133", "v134", "v135", "v136", "v137", "v138", "v139", "v140", "v141", "v142", "v143", "v144", "v145", "v146", "v147", "v148", "v149", "v150", "v151", "v152", "v153", "v154", "v155", "v156", "v157", "v158", "v159", "v160", "v161", "v162", "v163", "v164", "v165", "v166", "v167", "v168", "v169", "v170", "v171", "v172", "v173", "v174", "v175", "v176", "v177", "v178", "v179", "v180", "v181", "v182", "v183", "v184", "v185", "v186", "v187", "v188", "v189", "v190", "v191", "v192", "v193", "v194", "v195", "v196", "v197", "v198", "v199", "v200", "v201", "v202", "v203", "v204", "v205", "v206", "v207", "v208", "v209", "v210", "v211", "v212", "v213", "v214", "v215", "v216", "v217", "v218", "v219", "v220", "v221", "v222", "v223", "v224", "v225", "v226", "v227", "v228", "v229", "v230", "v231", "v232", "v233", "v234", "v235", "v236", "v237", "v238", "v239", "v240", "v241", "v242", "v243", "v244", "v245", "v246", "v247", "v248", "v249", "v250", "v251", "v252", "v253", "v254", "v255", "a0", "a1", "a2", "a3", "a4", "a5", "a6", "a7", "a8", "a9", "a10", "a11", "a12", "a13", "a14", "a15", "a16", "a17", "a18", "a19", "a20", "a21", "a22", "a23", "a24", "a25", "a26", "a27", "a28", "a29", "a30", "a31", "a32", "a33", "a34", "a35", "a36", "a37", "a38", "a39", "a40", "a41", "a42", "a43", "a44", "a45", "a46", "a47", "a48", "a49", "a50", "a51", "a52", "a53", "a54", "a55", "a56", "a57", "a58", "a59", "a60", "a61", "a62", "a63", "a64", "a65", "a66", "a67", "a68", "a69", "a70", "a71", "a72", "a73", "a74", "a75", "a76", "a77", "a78", "a79", "a80", "a81", "a82", "a83", "a84", "a85", "a86", "a87", "a88", "a89", "a90", "a91", "a92", "a93", "a94", "a95", "a96", "a97", "a98", "a99", "a100", "a101", "a102", "a103", "a104", "a105", "a106", "a107", "a108", "a109", "a110", "a111", "a112", "a113", "a114", "a115", "a116", "a117", "a118", "a119", "a120", "a121", "a122", "a123", "a124", "a125", "a126", "a127", "a128", "a129", "a130", "a131", "a132", "a133", "a134", "a135", "a136", "a137", "a138", "a139", "a140", "a141", "a142", "a143", "a144", "a145", "a146", "a147", "a148", "a149", "a150", "a151", "a152", "a153", "a154", "a155", "a156", "a157", "a158", "a159", "a160", "a161", "a162", "a163", "a164", "a165", "a166", "a167", "a168", "a169", "a170", "a171", "a172", "a173", "a174", "a175", "a176", "a177", "a178", "a179", "a180", "a181", "a182", "a183", "a184", "a185", "a186", "a187", "a188", "a189", "a190", "a191", "a192", "a193", "a194", "a195", "a196", "a197", "a198", "a199", "a200", "a201", "a202", "a203", "a204", "a205", "a206", "a207", "a208", "a209", "a210", "a211", "a212", "a213", "a214", "a215", "a216", "a217", "a218", "a219", "a220", "a221", "a222", "a223", "a224", "a225", "a226", "a227", "a228", "a229", "a230", "a231", "a232", "a233", "a234", "a235", "a236", "a237", "a238", "a239", "a240", "a241", "a242", "a243", "a244", "a245", "a246", "a247", "a248", "a249", "a250", "a251", "a252", "a253", "a254", "a255", "s22", "s23", "s24", "s25", "s26", "s27", "s28", "s29", "s30", "s31", "s36", "s37", "s38", "s39", "s40", "s41", "s42", "s43", "s44", "s45", "s46", "s47", "s48", "s49", "s50", "s51", "s52", "s53", "s54", "s55", "s56", "s57", "s58", "s59", "s60", "s61", "s62", "s63", "s64", "s65", "s66", "s67", "s68", "s69", "s70", "s71", "s72", "s73", "s74", "s75", "s76", "s77", "s78", "s79", "s80", "s81", "s82", "s83", "s84", "s85", "s86", "s88", "s89", "s90", "s91", "s92", "s93", "s94", "s95", "s96", "s97", "s98", "s99");
}

#define SSLEEP 1
#define TSLEEP 4
__device__ __forceinline__ unsigned mkkey(unsigned w, int idx) {
  unsigned u = w >> 16;
  u = (u & 0x7fffu) == 0 ? 0u : u;
  unsigned o = (u & 0x8000u) ? (~u & 0xffffu) : (u | 0x8000u);
  o = (u & 0x7fffu) > 0x7f80u ? 0u : o;
  return (o << 16) | (0xffffu - (unsigned)idx);
}
template <bool spread>
__device__ __forceinline__ u32x2 top2(const float* rbuf, int tid, int need, unsigned* sk) {
  unsigned key[9];
  const __amdgpu_buffer_rsrc_t rrs = __builtin_amdgcn_make_buffer_rsrc((void*)rbuf, 0, spread ? 2 * S_ * 4 + 256 * 128 : S_ * 4, 0x00020000);
  const int t8 = spread && tid >= 224 ? 0 : tid * 8;
#pragma unroll 1
  for (unsigned spins = 0; spins < (1u << 20); ++spins) {
    asm volatile("" ::: "memory");
    const u32x4 a = ld_sc1(rrs, t8 * 4), b = ld_sc1(rrs, t8 * 4 + 16);
    key[8] = spread ? __builtin_amdgcn_raw_buffer_load_b32(rrs, 2 * S_ * 4 + tid * 128, 0, 16) : a[0];
    unsigned mx = key[8];
#pragma unroll
    for (int k = 0; k < 4; ++k) { key[k] = a[k]; key[4 + k] = b[k]; mx = max(mx, max(a[k], b[k])); }
    if (mx != ~0u || tid * 8 >= need) break;
    __builtin_amdgcn_s_sleep(TSLEEP);
  }
  unsigned m = 0;
#pragma unroll
  for (int k = 0; k < 9; ++k) {
    key[k] = mkkey(key[k], k < 8 ? t8 + k : (spread ? S_ - 256 + tid : t8));
    m = max(m, key[k]);
  }
  m = wave_max_u32(m);
  __syncthreads();
  if ((tid & 63) == 0) sk[tid >> 6] = m;
  __syncthreads();
  const unsigned k0 = max(max(sk[0], sk[1]), max(sk[2], sk[3]));
  m = 0;
#pragma unroll
  for (int k = 0; k < 9; ++k) m = max(m, key[k] == k0 ? 0u : key[k]);
  m = wave_max_u32(m);
  if ((tid & 63) == 0) sk[4 + (tid >> 6)] = m;
  __syncthreads();
  const unsigned k1 = max(max(sk[4], sk[5]), max(sk[6], sk[7]));
  return (u32x2){(unsigned)__builtin_amdgcn_readfirstlane(k0), (unsigned)__builtin_amdgcn_readfirstlane(k1)};
}
__device__ __forceinline__ u32x2 top2_wave(const float* rbuf, int lane, bool dry, bool strict) {
  const __amdgpu_buffer_rsrc_t rrs = __builtin_amdgcn_make_buffer_rsrc((void*)rbuf, 0, S_ * 4, 0x00020000);
  asm volatile("" ::: "memory");
  u32x4 a[8];
#pragma unroll
  for (int i = 0; i < 8; ++i) a[i] = (u32x4){0u, 0u, 0u, 0u};
  if (!dry) {
#pragma unroll
    for (int i = 0; i < 8; ++i) a[i] = ld_sc1(rrs, (i * 64 + lane) * 16);
  }
  if (SNEED) {
    unsigned mx = 0;
#pragma unroll
    for (int i = 0; i < 8; ++i) {
      const unsigned x = max(max(a[i][0], a[i][1]), max(a[i][2], a[i][3]));
      mx = max(mx, i * 8 + (lane >> 3) < SNEED ? x : 0u);
    }
    if (strict && __builtin_amdgcn_ballot_w64(mx == ~0u)) return (u32x2){0u, 0u};
  }
  unsigned m1 = 0, m2 = 0;
#pragma unroll
  for (int i = 0; i < 32; ++i) {
    const unsigned w = a[i >> 2][i & 3], o = w ^ ((unsigned)((int)w >> 31) | 0x80000000u);
    const unsigned k = (o & 0xffff0000u) | ((0xffffu - (unsigned)(((i >> 2) * 64 + lane) * 4 + (i & 3))) & ~w);
    m2 = max(min(m1, k), m2);
    m1 = max(m1, k);
  }
  unsigned k[2];
  k[0] = wave_max_u32(m1);
  k[1] = wave_max_u32(m1 == k[0] ? m2 : m1);
  if (!k[1]) return (u32x2){0u, 0u};
#pragma unroll
  for (int i = 0; i < 2; ++i) k[i] = mkkey((k[i] >> 31) ? k[i] & 0x7fffffffu : ~k[i], 0xffff - (k[i] & 0xffff));
  return (u32x2){max(k[0], k[1]), min(k[0], k[1])};
}
__device__ __forceinline__ bool other_top2a(const float* rbuf, int tid, unsigned k0, unsigned k1) {
  const __amdgpu_buffer_rsrc_t rrs = __builtin_amdgcn_make_buffer_rsrc((void*)rbuf, 0, S_ * 4, 0x00020000);
  const int t8 = tid >= 224 ? 0 : tid * 8;
  bool bad = false;
#pragma unroll 1
  for (unsigned spins = 0; spins < (1u << 20); ++spins) {
    asm volatile("" ::: "memory");
    const u32x4 a = ld_sc1(rrs, t8 * 4), b = ld_sc1(rrs, t8 * 4 + 16);
    unsigned mx = 0;
    bad = false;
#pragma unroll
    for (int k = 0; k < 4; ++k) {
      const unsigned ka = mkkey(a[k], t8 + k), kb = mkkey(b[k], t8 + 4 + k);
      bad |= (ka > k1 && ka != k0) || (kb > k1 && kb != k0);
      mx = max(mx, max(a[k], b[k]));
    }
    if (__builtin_amdgcn_ballot_w64(mx == ~0u) == 0) break;
    __builtin_amdgcn_s_sleep(TSLEEP);
  }
  return __builtin_amdgcn_ballot_w64(bad) != 0;
}
__device__ __forceinline__ bool other_top2b(const float* rbuf, int tid, unsigned k0, unsigned k1, bool bad, unsigned* sk, unsigned& tmp, unsigned f) {
  if (RDLY && (f >> 16) == 63) {
    const unsigned t = f + RDLY - (3 - (tid >> 6)) * RDL2;
#pragma unroll 1
    while (((t - (unsigned)__builtin_amdgcn_s_memrealtime()) & 0xffffu) < 400u) __builtin_amdgcn_s_sleep(1);
  }
  const __amdgpu_buffer_rsrc_t rrs = __builtin_amdgcn_make_buffer_rsrc((void*)rbuf, 0, 2 * S_ * 4 + 256 * 128, 0x00020000);
  unsigned w = RDLY ? __builtin_amdgcn_raw_buffer_load_b32(rrs, 2 * S_ * 4 + tid * 128, 0, 16) : ~0u, n;
  tmp = 0;
  if (!RDLY || __builtin_amdgcn_ballot_w64(w == ~0u)) {
  asm volatile("buffer_load_dword %1, %3, %4, 0 offen sc1\n\ts_sleep %5\n\t"
               "buffer_load_dword %1, %3, %4, 0 offen sc1\n\ts_sleep %5\n\t"
               "s_mov_b32 %2, 0x100000\n"
               "TP%=:\n\t"
               "buffer_load_dword %1, %3, %4, 0 offen sc1\n\t"
               "s_sub_u32 %2, %2, 1\n\t"
               "s_waitcnt vmcnt(2)\n\t"
               "v_mov_b32 %0, %1\n\t"
               "v_cmp_eq_u32 vcc, -1, %0\n\t"
               "s_cbranch_scc1 TQ%=\n\t"
               "s_cbranch_vccnz TP%=\n"
               "TQ%=:"
               : "=&v"(w), "=&v"(tmp), "=&s"(n) : "v"(2 * S_ * 4 + tid * 128), "s"(rrs), "n"(PSLEEP) : "memory", "vcc", "scc");
  }
  const unsigned k = mkkey(w, S_ - 256 + tid);
  bad |= k > k1 && k != k0;
  const unsigned any = __builtin_amdgcn_ballot_w64(bad) != 0;
  if ((tid & 63) == 0) sk[tid >> 6] = any;
  LDS_BARRIER();
  const u32x4 v = *(const u32x4*)sk;
  return __builtin_amdgcn_readfirstlane(v[0] | v[1] | v[2] | v[3]) != 0;
}

constexpr int M1_WGS = FF / 16;
constexpr int L_WD = 131072;
__device__ __forceinline__ void wait_set(gu32* f) {
#pragma unroll 1
  for (unsigned spins = 0; spins < (1u << 18); ++spins) {
    if (__hip_atomic_load(f, RLX_AGENT) != ~0u) return;
    __builtin_amdgcn_s_sleep(4);
  }
}
__device__ __forceinline__ u32x2 wait_selt(char* ws, int b, unsigned& tm) {
  const __amdgpu_buffer_rsrc_t rs = __builtin_amdgcn_make_buffer_rsrc((void*)(ws + O_CNT), 0, 1024, 0x00020000);
  u32x4 v;
#pragma unroll 1
  for (unsigned spins = 0; spins < (1u << 18); ++spins) {
    asm volatile("" ::: "memory");
    v = ld_sc1(rs, (b & 7) * 64);
    if (v[1]) break;
    __builtin_amdgcn_s_sleep(FSLEEP);
  }
  tm = __builtin_amdgcn_readfirstlane(v[2]);
  return (u32x2){(unsigned)__builtin_amdgcn_readfirstlane(v[0]), (unsigned)__builtin_amdgcn_readfirstlane(v[1])};
}
__device__ __forceinline__ u32x2 wait_sel(gu32* f, u64 v = 0) {
#pragma unroll 1
  for (unsigned spins = 0; spins < (1u << 18) && !v; ++spins) {
    v = __hip_atomic_load((gu64*)f, RLX_AGENT);
    if (v) break;
    __builtin_amdgcn_s_sleep(FSLEEP);
  }
  return (u32x2){(unsigned)__builtin_amdgcn_readfirstlane((unsigned)v), (unsigned)__builtin_amdgcn_readfirstlane((unsigned)(v >> 32))};
}
__device__ __forceinline__ unsigned code_ahead(int tid) {
  unsigned d = 0;
  if (MPF) { u64 pc; asm volatile("s_getpc_b64 %1\n\tglobal_load_dword %0, %2, %1" : "=v"(d), "=&s"(pc) : "v"((tid & 127) * 64) : "memory"); }
  return d;
}
__device__ __forceinline__ bf16x8 as_frag(u32x4 v) { return __builtin_bit_cast(bf16x8, v); }
__device__ __forceinline__ u32x4 tr_frag(char* p0, char* p1) {
  const s16x4 a = __builtin_amdgcn_ds_read_tr16_b64_v4i16((LAS s16x4*)p0), b = __builtin_amdgcn_ds_read_tr16_b64_v4i16((LAS s16x4*)p1);
  const u32x2 x = __builtin_bit_cast(u32x2, a), y = __builtin_bit_cast(u32x2, b);
  return (u32x4){x[0], x[1], y[0], y[1]};
}
__device__ __forceinline__ void mlp1_role(KArgs P, char* lds, int b, int tid) {
  float* part = (float*)lds;
  unsigned* si = (unsigned*)(lds + 4096);
  const int lane = tid & 63, wv = tid >> 6, l15 = lane & 15, l4 = lane >> 4;
  const __amdgpu_buffer_rsrc_t lrs = __builtin_amdgcn_make_buffer_rsrc((void*)(P->ws + O_LNH), 0, S_ * DM * 2, 0x00020000);
  const __amdgpu_buffer_rsrc_t mrs = __builtin_amdgcn_make_buffer_rsrc((void*)(P->ws + O_MFRAG), 0, 2 * DM * FF * 2, 0x00020000);
  u32x4 aw[2][4];
  MS(0);
  unsigned ca = code_ahead(tid);
  const u64 s0 = __hip_atomic_load((gu64*)(P->ws + O_CNT + (b & 7) * 64), RLX_AGENT);
  const unsigned fl = __hip_atomic_load((gu32*)(P->ws + O_KVSTEP + (b >> 2) * 64 + (b & 3) * 4), RLX_AGENT);
#pragma unroll
  for (int i = 0; i < 8; ++i) aw[i >> 2][i & 3] = ld_sc1(mrs, (unsigned)(b * 32768 + (i >> 2) * 16384 + (wv * 4 + (i & 3)) * 1024 + lane * 16));
  u32x2 sel = wait_sel((gu32*)(P->ws + O_CNT) + (b & 7) * 16, s0);
  asm volatile("" :: "v"(ca));
  MS(1);
  int round = 0;
#pragma unroll 1
  for (;;) {
    const int idx = 0xffff - (((l15 & 1) ? sel[1] : sel[0]) & 0xffff);
    if (round == 2) wait_set((gu32*)(P->ws + O_RBUF) + S_ + idx);
    u32x4 bx[4];
#pragma unroll
    for (int i = 0; i < 4; ++i) bx[i] = ld_sc1(lrs, (unsigned)(idx * 1024 + (wv * 4 + i) * 64 + l4 * 16));
    f32x4 ag = {0.f, 0.f, 0.f, 0.f}, au = {0.f, 0.f, 0.f, 0.f};
#pragma unroll
    for (int i = 0; i < 4; ++i) {
      ag = __builtin_amdgcn_mfma_f32_16x16x32_bf16(as_frag(aw[0][i]), as_frag(bx[i]), ag, 0, 0, 0);
      au = __builtin_amdgcn_mfma_f32_16x16x32_bf16(as_frag(aw[1][i]), as_frag(bx[i]), au, 0, 0, 0);
    }
    if (l15 < 2) {
      *(f32x4*)(part + wv * 64 + l15 * 16 + l4 * 4) = ag;
      *(f32x4*)(part + wv * 64 + 32 + l15 * 16 + l4 * 4) = au;
    }
    LDS_BARRIER();
    MS(2);
    if (tid < 16) {
      float a[2];
#pragma unroll
      for (int e = 0; e < 2; ++e) {
        const float* q = part + tid * 2 + e;
        const float g = rbf((q[0] + q[64]) + (q[128] + q[192])), u = rbf((q[32] + q[96]) + (q[160] + q[224]));
        a[e] = rbf(g / (1.0f + __expf(-g))) * u;
      }
      unsigned av = pk2(a[0], a[1]);
      av = av == ~0u ? 0x7fc07fc0u : av;
      __hip_atomic_store((gu32*)(P->ws + O_ACT) + (round * M1_WGS + b) * 32 + tid, av, RLX_AGENT);
    }
    MS(4);
    if (round == 2) break;
    if (SW2 && !round) {
      const u32x2 s2 = wait_sel((gu32*)(P->ws + O_SEL2) + (b & 31) * 32);
      if (s2[0] != sel[0] || s2[1] != sel[1]) { sel = s2; round = 1; __syncthreads(); continue; }
    }
    unsigned tmp;
    const bool again = other_top2b((const float*)(P->ws + O_RBUF), tid, sel[0], sel[1], other_top2a((const float*)(P->ws + O_RBUF), tid, sel[0], sel[1]), si, tmp, __builtin_amdgcn_readfirstlane(fl));
    MS(5);
    asm volatile("s_waitcnt vmcnt(0)" :: "v"(tmp) : "memory");
    MS(6);
    if (!again) break;
    sel = top2<true>((const float*)(P->ws + O_RBUF), tid, S_, si + 4);
    round = 2;
  }
}
__device__ __forceinline__ void mlp2_role(KArgs P, char* lds, int b, int tid) {
  float* part = (float*)lds;
  unsigned* si = (unsigned*)(lds + 4096);
  const int lane = tid & 63, wv = tid >> 6, l15 = lane & 15, l4 = lane >> 4;
  const __amdgpu_buffer_rsrc_t hrs = __builtin_amdgcn_make_buffer_rsrc((void*)(P->ws + O_HBUF), 0, S_ * DM * 2, 0x00020000);
  const __amdgpu_buffer_rsrc_t lrs = __builtin_amdgcn_make_buffer_rsrc((void*)(P->ws + O_LNH), 0, S_ * DM * 2, 0x00020000);
  const __amdgpu_buffer_rsrc_t ors = __builtin_amdgcn_make_buffer_rsrc((void*)P->out, 0, S_ * DM * 2, 0x00020000);
  const __amdgpu_buffer_rsrc_t ars = __builtin_amdgcn_make_buffer_rsrc((void*)(P->ws + O_ACT), 0, 3 * M1_WGS * 128, 0x00020000);
  u32x4 aw[7];
  const int c0 = b * 8;
  MS(0);
  unsigned ca = code_ahead(tid);
  {
    char* p = lds + L_WD + (((l15 >> 1) & 1) * 896 + wv * 224 + l4 * 8 + (l15 >> 2)) * 16 + (l15 & 1) * 8;
#pragma unroll
    for (int i = 0; i < 7; ++i) aw[i] = tr_frag(p + i * 512, p + i * 512 + 64);
  }
  unsigned tsel;
  u32x2 sel = wait_selt(P->ws, b, tsel);
  asm volatile("" :: "v"(ca));
  const unsigned fl = __hip_atomic_load((gu32*)(P->ws + O_KVSTEP + (c0 >> 4) * 64), RLX_AGENT);
  MS(1);
  int round = 0;
#pragma unroll 1
  for (;;) {
    const int idx = 0xffff - (((tid & 8) ? sel[1] : sel[0]) & 0xffff), col = c0 + (tid & 7);
    if (round == 2) wait_set((gu32*)(P->ws + O_RBUF) + S_ + idx);
    const float rr = __hip_atomic_load((const GAS float*)(P->ws + O_RBUF) + S_ + idx, RLX_AGENT);
    const float lv = bf2f(__builtin_amdgcn_raw_buffer_load_b16(lrs, (unsigned)(idx * DM + col) * 2, 0, 16));
    const float hv = bf2f(__builtin_amdgcn_raw_buffer_load_b16(hrs, (unsigned)(idx * DM + col) * 2, 0, 16));
    const float sg = rbf(1.0f / (1.0f + __expf(-rr)));
    const bool bad = other_top2a((const float*)(P->ws + O_RBUF), tid, sel[0], sel[1]);
    if (ADLY && !round) {
#pragma unroll 1
      while (tsel + ADLY - (unsigned)__builtin_amdgcn_s_memrealtime() < 400u) __builtin_amdgcn_s_sleep(1);
    } else if (!round && (ASALL || c0 < 256)) __builtin_amdgcn_s_sleep(ASLEEP);
    u32x4 av[7];
    u64 v2 = 0;
#pragma unroll 1
    for (unsigned spins = 0; spins < (1u << 20); ++spins) {
      asm volatile("" ::: "memory");
      unsigned mx = 0;
      if (SW2) v2 = __hip_atomic_load((gu64*)(P->ws + O_SEL2 + (b & 31) * 128), RLX_AGENT);
#pragma unroll
      for (int i = 0; i < 7; ++i) {
        av[i] = ld_sc1(ars, (unsigned)((round * M1_WGS + ((l15 >> 1) & 1) * 56 + (wv * 7 + i) * 2 + (l4 >> 1)) * 128 + (l15 & 1) * 32 + (l4 & 1) * 16));
        mx = max(max(mx, max(av[i][0], av[i][1])), max(av[i][2], av[i][3]));
      }
      if (__builtin_amdgcn_ballot_w64(mx == ~0u) == 0) break;
      __builtin_amdgcn_s_sleep(4);
    }
    MS(2);
    if (SW2 && !round) {
      const u32x2 s2 = wait_sel((gu32*)(P->ws + O_SEL2) + (b & 31) * 32, v2);
      if (s2[0] != sel[0] || s2[1] != sel[1]) { sel = s2; round = 1; continue; }
    }
    f32x4 a0 = {0.f, 0.f, 0.f, 0.f}, a1 = {0.f, 0.f, 0.f, 0.f};
#pragma unroll
    for (int i = 0; i < 7; ++i) {
      if (i & 1) a1 = __builtin_amdgcn_mfma_f32_16x16x32_bf16(as_frag(aw[i]), as_frag(av[i]), a1, 0, 0, 0);
      else a0 = __builtin_amdgcn_mfma_f32_16x16x32_bf16(as_frag(aw[i]), as_frag(av[i]), a0, 0, 0, 0);
    }
    if ((l15 >> 1) == (l4 >> 1)) *(f32x4*)(part + wv * 32 + l15 * 8 + (l4 & 1) * 4) = a0 + a1;
    LDS_BARRIER();
    const float* q = part + (tid & 15);
    const float y = rbf(((q[0] + q[16]) + (q[32] + q[48])) + ((q[64] + q[80]) + (q[96] + q[112])));
    const float upd = rbf(y * sg);
    const float bb = rbf(lv + upd);
    const u16 res = f2bf(hv + bb);
    MS(4);
    if (tid < 16) __builtin_amdgcn_raw_buffer_store_b16(res, ors, (unsigned)(idx * DM + col) * 2, 0, 16);
    if (round == 2) break;
    unsigned tmp;
    const bool again = other_top2b((const float*)(P->ws + O_RBUF), tid, sel[0], sel[1], bad, si, tmp, __builtin_amdgcn_readfirstlane(fl));
    MS(5);
    asm volatile("s_waitcnt vmcnt(0)" :: "v"(tmp) : "memory");
    MS(6);
    if (!again) break;
    if (tid < 16) __builtin_amdgcn_raw_buffer_store_b16(f2bf(hv + lv), ors, (unsigned)(idx * DM + col) * 2, 0, 16);
    VM_DRAIN();
    sel = top2<true>((const float*)(P->ws + O_RBUF), tid, S_, si + 4);
    round = 2;
  }
}

struct WoRegs { u32x32 a, b, c, d; };
__device__ __forceinline__ unsigned worker_role(int& w, int& wv, KArgs& A, WoRegs& wo) {
  unsigned tag;
  asm volatile(
      "GSTART:\n\t"
      "v_mbcnt_lo_u32_b32 v96, -1, 0\n\t"
      "v_mbcnt_hi_u32_b32 v96, -1, v96\n\t"
      "s_and_b32 s82, %0, 31\n\t"
      "s_mov_b32 s66, 4194304\n\t"
      "s_mov_b32 s67, 0x20000\n\t"
      "s_mov_b32 s62, 111181824\n\t"
      "s_mov_b32 s63, 0x20000\n\t"
      "v_and_b32 v104, 3, v96\n\t"
      "v_lshrrev_b32 v105, 2, v96\n\t"
      "s_lshl_b32 s78, %0, 14\n\t"
      "s_lshl_b32 s79, %1, 12\n\t"
      "s_add_u32 s79, s79, s78\n\t"
      "v_lshlrev_b32 v106, 10, v104\n\t"
      "v_lshl_add_u32 v106, v105, 4, v106\n\t"
      "v_lshlrev_b32 v99, 13, v105\n\t"
      "v_lshl_add_u32 v99, v104, 4, v99\n\t"
      "s_lshl_b32 s80, s82, 8\n\t"
      "s_lshl_b32 s72, %1, 6\n\t"
      "s_add_u32 s80, s80, s72\n\t"
      "v_add_u32 v99, s80, v99\n\t"
      "s_bitcmp1_b32 %0, 5\n\t"
      "s_waitcnt lgkmcnt(0)\n\t"
      "s_mov_b64 s[64:65], s[36:37]\n\t"
      "s_mov_b64 s[60:61], s[58:59]\n\t"
      "buffer_load_dwordx4 v[64:67], v106, s[64:67], s79 offen \n\t"
      "buffer_load_dwordx4 v[68:71], v106, s[64:67], s79 offen offset:256 \n\t"
      "buffer_load_dwordx4 v[72:75], v106, s[64:67], s79 offen offset:512 \n\t"
      "buffer_load_dwordx4 v[76:79], v106, s[64:67], s79 offen offset:768 \n\t"
      "s_cselect_b64 s[38:39], s[44:45], s[42:43]\n\t"
      "s_mov_b32 s78, 0\n\t"
      "s_load_dword s87, s[38:39], s78\n\t"
      "s_mov_b32 s78, 4194300\n\t"
      "s_load_dword s87, s[38:39], s78\n\t"
      "s_mov_b32 s78, 128\n\t"
      "s_load_dword s87, s[60:61], s78\n\t"
      "s_mov_b32 s78, 6520704\n\t"
      "s_load_dword s87, s[60:61], s78\n\t"
      "v_mov_b32 v118, v106\n\t"
      "s_mov_b64 s[64:65], s[36:37]\n\t"
      "s_lshr_b32 s78, %0, 6\n\t"
      "s_lshl_b32 s78, s78, 15\n\t"
      "s_lshl_b32 s79, %1, 12\n\t"
      "s_add_u32 s79, s79, s78\n\t"
      "buffer_load_dwordx4 v[128:131], v118, s[64:67], s79 offen \n\t"
      "buffer_load_dwordx4 v[132:135], v118, s[64:67], s79 offen offset:256 \n\t"
      "buffer_load_dwordx4 v[136:139], v118, s[64:67], s79 offen offset:512 \n\t"
      "buffer_load_dwordx4 v[140:143], v118, s[64:67], s79 offen offset:768 \n\t"
      "s_add_u32 s79, s79, 16384\n\t"
      "buffer_load_dwordx4 v[144:147], v118, s[64:67], s79 offen \n\t"
      "buffer_load_dwordx4 v[148:151], v118, s[64:67], s79 offen offset:256 \n\t"
      "buffer_load_dwordx4 v[152:155], v118, s[64:67], s79 offen offset:512 \n\t"
      "buffer_load_dwordx4 v[156:159], v118, s[64:67], s79 offen offset:768 \n\t"
      "s_mov_b64 s[64:65], s[38:39]\n\t"
      "s_mov_b64 s[64:65], s[38:39]\n\t"
      "buffer_load_dword v107, off, s[60:63], 0 sc1\n\t"
      "v_lshrrev_b32 v101, 4, v96\n\t"
      "v_and_b32 v102, 15, v96\n\t"
      "v_lshlrev_b32 v103, 2, v101\n\t"
      "v_xor_b32 v102, v102, v103\n\t"
      "v_lshlrev_b32 v108, 13, v101\n\t"
      "v_lshl_add_u32 v108, v102, 4, v108\n\t"
      "s_lshl_b32 s78, s82, 8\n\t"
      "s_lshl_b32 s79, %1, 16\n\t"
      "s_add_u32 s78, s78, s79\n\t"
      "v_add_u32 v108, s78, v108\n\t"
      "s_lshl_b32 s76, %1, 11\n\t"
      "s_mov_b32 s77, 0\n\t"
      "s_add_u32 m0, s76, 0\n\t"
      "s_add_u32 s68, s77, 0\n\t"
      "buffer_load_dwordx4 v108, s[64:67], s68 offen lds\n\t"
      "s_add_u32 m0, s76, 1024\n\t"
      "s_add_u32 s68, s77, 32768\n\t"
      "s_add_u32 s76, s76, 8192\n\t"
      "s_add_u32 s77, s77, 0x40000\n\t"
      "buffer_load_dwordx4 v108, s[64:67], s68 offen lds\n\t"
      "s_mov_b32 s84, 0xffff0000\n\t"
      "s_mov_b32 s86, 0\n\t"
      "s_lshl_b32 s85, %1, 13\n\t"
      "v_lshlrev_b32 v97, 4, v96\n\t"
      "v_lshrrev_b32 v101, 4, v96\n\t"
      "v_and_b32 v102, 3, v105\n\t"
      "v_lshlrev_b32 v100, 9, v101\n\t"
      "v_lshl_add_u32 v100, v102, 6, v100\n\t"
      "v_lshl_add_u32 v100, v104, 4, v100\n\t"
      "v_lshlrev_b32 v106, 10, v101\n\t"
      "v_lshl_add_u32 v106, v102, 8, v106\n\t"
      "v_lshl_add_u32 v106, v104, 4, v106\n\t"
      "s_lshl_b32 s74, %0, 14\n\t"
      "s_add_u32 s74, s74, s72\n\t"
      "s_add_u32 s74, s74, 6520832\n\t"
      "s_lshr_b32 s78, %0, 6\n\t"
      "v_min_u32 v104, 31, v96\n\t"
      "v_lshl_add_u32 v104, v104, 1, s78\n\t"
      "s_lshr_b32 s79, %0, 5\n\t"
      "s_and_b32 s83, s79, 1\n\t"
      "s_add_u32 s80, s85, 6520832\n\t"
      "v_lshl_add_u32 v101, v104, 15, s80\n\t"
      "s_mul_i32 s80, s82, 24576\n\t"
      "s_lshl_b32 s72, %1, 11\n\t"
      "s_add_u32 s80, s80, s72\n\t"
      "s_add_u32 s80, s80, 8617984\n\t"
      "s_lshl_b32 s72, s83, 13\n\t"
      "s_add_u32 s80, s80, s72\n\t"
      "v_mov_b32 v102, s80\n\t"
      "s_mov_b32 s72, 786432\n\t"
      "v_mad_u32_u24 v102, v104, s72, v102\n\t"
      "s_lshl_b32 s80, s82, 6\n\t"
      "s_lshl_b32 s72, s83, 2\n\t"
      "s_add_u32 s80, s80, s72\n\t"
      "s_add_u32 s80, s80, 256\n\t"
      "v_lshrrev_b32 v105, 2, v104\n\t"
      "v_lshl_add_u32 v103, v105, 11, s80\n\t"
      "v_and_b32 v104, 3, v104\n\t"
      "v_lshl_add_u32 v103, v104, 4, v103\n\t"
      "s_lshl_b32 s80, s82, 7\n\t"
      "s_lshl_b32 s79, s79, 2\n\t"
      "s_add_u32 s80, s80, s79\n\t"
      "s_add_u32 s80, s80, 33024\n\t"
      "v_writelane_b32 v103, s80, 0\n\t"
      "v_writelane_b32 v103, 0, 63\n\t"
      "s_cmp_eq_u32 %1, 0\n\t"
      "s_cselect_b64 s[90:91], 1, 0\n\t"
      "v_lshrrev_b32 v104, 4, v96\n\t"
      "v_bfe_u32 v105, v96, 2, 2\n\t"
      "v_and_b32 v110, 3, v96\n\t"
      "s_lshl_b32 s78, %1, 2\n\t"
      "v_lshl_add_u32 v109, v104, 3, v105\n\t"
      "v_lshlrev_b32 v109, 8, v109\n\t"
      "v_or_b32 v110, s78, v110\n\t"
      "v_lshlrev_b32 v105, 2, v105\n\t"
      "v_xor_b32 v110, v110, v105\n\t"
      "v_lshl_add_u32 v109, v110, 4, v109\n\t"
      "s_lshl_b32 s78, %1, 6\n\t"
      "s_add_u32 s78, s78, 131072\n\t"
      "v_add_u32 v110, s78, v106\n\t"
      "s_bfe_u32 s80, %0, 0x10005\n\t"
      "s_and_b32 s79, %1, 1\n\t"
      "v_bfe_u32 v105, v96, 4, 1\n\t"
      "v_lshlrev_b32 v105, 1, v105\n\t"
      "v_and_b32 v116, 7, v96\n\t"
      "v_xor_b32 v116, v116, v105\n\t"
      "s_lshl_b32 s78, s79, 2\n\t"
      "v_xor_b32 v116, s78, v116\n\t"
      "v_lshlrev_b32 v116, 4, v116\n\t"
      "v_lshrrev_b32 v105, 3, v96\n\t"
      "v_lshl_add_u32 v116, v105, 13, v116\n\t"
      "s_lshl_b32 s78, s82, 8\n\t"
      "s_lshl_b32 s72, s80, 7\n\t"
      "s_add_u32 s78, s78, s72\n\t"
      "s_lshl_b32 s72, %1, 16\n\t"
      "s_add_u32 s78, s78, s72\n\t"
      "v_add_u32 v116, s78, v116\n\t"
      "v_bfe_u32 v105, v96, 2, 2\n\t"
      "v_lshl_add_u32 v117, v104, 3, v105\n\t"
      "v_lshlrev_b32 v117, 7, v117\n\t"
      "v_bfe_u32 v105, v96, 3, 2\n\t"
      "v_xor_b32 v105, %1, v105\n\t"
      "v_lshl_add_u32 v117, v105, 5, v117\n\t"
      "v_and_b32 v105, 3, v96\n\t"
      "v_lshl_add_u32 v117, v105, 3, v117\n\t"
      "v_and_b32 v111, 15, v96\n\t"
      "v_lshlrev_b32 v111, 4, v111\n\t"
      "v_and_b32 v105, 1, v104\n\t"
      "v_lshl_add_u32 v111, v105, 10, v111\n\t"
      "v_lshrrev_b32 v105, 1, v104\n\t"
      "v_lshl_add_u32 v111, v105, 8, v111\n\t"
      "s_lshl_b32 s78, s79, 9\n\t"
      "s_add_u32 s78, s78, 16384\n\t"
      "s_lshl_b32 s80, s80, 12\n\t"
      "s_sub_u32 s78, s78, s80\n\t"
      "s_add_u32 s80, %1, 1\n\t"
      "s_lshr_b32 s80, s80, 1\n\t"
      "s_lshl_b32 s80, s80, 11\n\t"
      "s_sub_u32 s78, s78, s80\n\t"
      "v_add_u32 v111, s78, v111\n\t"
      "s_mov_b32 s69, 0\n\t"
      "s_add_u32 m0, s76, 0\n\t"
      "s_add_u32 s68, s77, 0\n\t"
      "buffer_load_dwordx4 v108, s[64:67], s68 offen lds\n\t"
      "s_add_u32 m0, s76, 1024\n\t"
      "s_add_u32 s68, s77, 32768\n\t"
      "s_add_u32 s76, s76, 8192\n\t"
      "s_add_u32 s77, s77, 0x40000\n\t"
      "buffer_load_dwordx4 v108, s[64:67], s68 offen lds\n\t"
      "s_add_u32 m0, s76, 0\n\t"
      "s_add_u32 s68, s77, 0\n\t"
      "buffer_load_dwordx4 v108, s[64:67], s68 offen lds\n\t"
      "s_add_u32 m0, s76, 1024\n\t"
      "s_add_u32 s68, s77, 32768\n\t"
      "s_add_u32 s76, s76, 8192\n\t"
      "s_add_u32 s77, s77, 0x40000\n\t"
      "buffer_load_dwordx4 v108, s[64:67], s68 offen lds\n\t"
      "s_add_u32 m0, s76, 0\n\t"
      "s_add_u32 s68, s77, 0\n\t"
      "buffer_load_dwordx4 v108, s[64:67], s68 offen lds\n\t"
      "s_add_u32 m0, s76, 1024\n\t"
      "s_add_u32 s68, s77, 32768\n\t"
      "s_add_u32 s76, s76, 8192\n\t"
      "s_add_u32 s77, s77, 0x40000\n\t"
      "buffer_load_dwordx4 v108, s[64:67], s68 offen lds\n\t"
      "s_mov_b64 s[88:89], 0\n\t"
      "s_waitcnt vmcnt(8)\n\t"
      "GLN%=:\n\t"
      "v_and_b32 v1, s84, v64\n\t"
      "v_lshlrev_b32 v0, 16, v64\n\t"
      "v_and_b32 v3, s84, v65\n\t"
      "v_lshlrev_b32 v2, 16, v65\n\t"
      "v_and_b32 v5, s84, v66\n\t"
      "v_lshlrev_b32 v4, 16, v66\n\t"
      "v_and_b32 v7, s84, v67\n\t"
      "v_lshlrev_b32 v6, 16, v67\n\t"
      "v_and_b32 v9, s84, v68\n\t"
      "v_lshlrev_b32 v8, 16, v68\n\t"
      "v_and_b32 v11, s84, v69\n\t"
      "v_lshlrev_b32 v10, 16, v69\n\t"
      "v_and_b32 v13, s84, v70\n\t"
      "v_lshlrev_b32 v12, 16, v70\n\t"
      "v_and_b32 v15, s84, v71\n\t"
      "v_lshlrev_b32 v14, 16, v71\n\t"
      "s_mov_b64 exec, s[88:89]\n\t"
      "s_add_u32 m0, s76, 0\n\t"
      "s_add_u32 s68, s77, 0\n\t"
      "buffer_load_dwordx4 v108, s[64:67], s68 offen lds\n\t"
      "s_mov_b64 exec, -1\n\t"
      "v_and_b32 v17, s84, v72\n\t"
      "v_lshlrev_b32 v16, 16, v72\n\t"
      "v_and_b32 v19, s84, v73\n\t"
      "v_lshlrev_b32 v18, 16, v73\n\t"
      "v_and_b32 v21, s84, v74\n\t"
      "v_lshlrev_b32 v20, 16, v74\n\t"
      "v_and_b32 v23, s84, v75\n\t"
      "v_lshlrev_b32 v22, 16, v75\n\t"
      "v_and_b32 v25, s84, v76\n\t"
      "v_lshlrev_b32 v24, 16, v76\n\t"
      "v_and_b32 v27, s84, v77\n\t"
      "v_lshlrev_b32 v26, 16, v77\n\t"
      "v_and_b32 v29, s84, v78\n\t"
      "v_lshlrev_b32 v28, 16, v78\n\t"
      "v_and_b32 v31, s84, v79\n\t"
      "v_lshlrev_b32 v30, 16, v79\n\t"
      "s_mov_b64 exec, s[88:89]\n\t"
      "s_add_u32 m0, s76, 1024\n\t"
      "s_add_u32 s68, s77, 32768\n\t"
      "s_add_u32 s76, s76, 8192\n\t"
      "s_add_u32 s77, s77, 0x40000\n\t"
      "buffer_load_dwordx4 v108, s[64:67], s68 offen lds\n\t"
      "s_mov_b64 exec, -1\n\t"
      "v_pk_add_f32 v[32:33], v[0:1], v[2:3]\n\t"
      "v_pk_add_f32 v[34:35], v[4:5], v[6:7]\n\t"
      "v_pk_add_f32 v[36:37], v[8:9], v[10:11]\n\t"
      "v_pk_add_f32 v[38:39], v[12:13], v[14:15]\n\t"
      "v_pk_add_f32 v[40:41], v[16:17], v[18:19]\n\t"
      "v_pk_add_f32 v[42:43], v[20:21], v[22:23]\n\t"
      "v_pk_add_f32 v[44:45], v[24:25], v[26:27]\n\t"
      "v_pk_add_f32 v[46:47], v[28:29], v[30:31]\n\t"
      "v_pk_add_f32 v[32:33], v[32:33], v[40:41]\n\t"
      "v_pk_add_f32 v[34:35], v[34:35], v[42:43]\n\t"
      "v_pk_add_f32 v[36:37], v[36:37], v[44:45]\n\t"
      "v_pk_add_f32 v[38:39], v[38:39], v[46:47]\n\t"
      "v_pk_add_f32 v[32:33], v[32:33], v[36:37]\n\t"
      "v_pk_add_f32 v[34:35], v[34:35], v[38:39]\n\t"
      "v_pk_add_f32 v[32:33], v[32:33], v[34:35]\n\t"
      "v_add_f32 v32, v32, v33\n\t"
      "s_mov_b64 exec, s[88:89]\n\t"
      "s_add_u32 m0, s76, 0\n\t"
      "s_add_u32 s68, s77, 0\n\t"
      "buffer_load_dwordx4 v108, s[64:67], s68 offen lds\n\t"
      "s_mov_b64 exec, -1\n\t"
      "v_add_f32_dpp v32, v32, v32 row_ror:4 row_mask:0xf bank_mask:0xf\n\t"
      "s_nop 1\n\t"
      "v_add_f32_dpp v32, v32, v32 row_ror:8 row_mask:0xf bank_mask:0xf\n\t"
      "v_mov_b32 v33, v32\n\t"
      "s_nop 1\n\t"
      "v_permlane16_swap_b32 v32, v33\n\t"
      "v_add_f32 v32, v32, v33\n\t"
      "v_mov_b32 v33, v32\n\t"
      "s_nop 1\n\t"
      "v_permlane32_swap_b32 v32, v33\n\t"
      "v_add_f32 v32, v32, v33\n\t"
      "v_mul_f32 v80, 0x3b000000, v32\n\t"
      "v_pk_add_f32 v[32:33], v[0:1], v[80:81] op_sel_hi:[1,0] neg_lo:[0,1] neg_hi:[0,1]\n\t"
      "v_pk_add_f32 v[34:35], v[2:3], v[80:81] op_sel_hi:[1,0] neg_lo:[0,1] neg_hi:[0,1]\n\t"
      "v_pk_add_f32 v[36:37], v[4:5], v[80:81] op_sel_hi:[1,0] neg_lo:[0,1] neg_hi:[0,1]\n\t"
      "v_pk_add_f32 v[38:39], v[6:7], v[80:81] op_sel_hi:[1,0] neg_lo:[0,1] neg_hi:[0,1]\n\t"
      "v_pk_add_f32 v[40:41], v[8:9], v[80:81] op_sel_hi:[1,0] neg_lo:[0,1] neg_hi:[0,1]\n\t"
      "v_pk_add_f32 v[42:43], v[10:11], v[80:81] op_sel_hi:[1,0] neg_lo:[0,1] neg_hi:[0,1]\n\t"
      "v_pk_add_f32 v[44:45], v[12:13], v[80:81] op_sel_hi:[1,0] neg_lo:[0,1] neg_hi:[0,1]\n\t"
      "s_mov_b64 exec, s[88:89]\n\t"
      "s_add_u32 m0, s76, 1024\n\t"
      "s_add_u32 s68, s77, 32768\n\t"
      "s_add_u32 s76, s76, 8192\n\t"
      "s_add_u32 s77, s77, 0x40000\n\t"
      "buffer_load_dwordx4 v108, s[64:67], s68 offen lds\n\t"
      "s_mov_b64 exec, -1\n\t"
      "v_pk_add_f32 v[46:47], v[14:15], v[80:81] op_sel_hi:[1,0] neg_lo:[0,1] neg_hi:[0,1]\n\t"
      "v_pk_add_f32 v[48:49], v[16:17], v[80:81] op_sel_hi:[1,0] neg_lo:[0,1] neg_hi:[0,1]\n\t"
      "v_pk_add_f32 v[50:51], v[18:19], v[80:81] op_sel_hi:[1,0] neg_lo:[0,1] neg_hi:[0,1]\n\t"
      "v_pk_add_f32 v[52:53], v[20:21], v[80:81] op_sel_hi:[1,0] neg_lo:[0,1] neg_hi:[0,1]\n\t"
      "v_pk_add_f32 v[54:55], v[22:23], v[80:81] op_sel_hi:[1,0] neg_lo:[0,1] neg_hi:[0,1]\n\t"
      "v_pk_add_f32 v[56:57], v[24:25], v[80:81] op_sel_hi:[1,0] neg_lo:[0,1] neg_hi:[0,1]\n\t"
      "v_pk_add_f32 v[58:59], v[26:27], v[80:81] op_sel_hi:[1,0] neg_lo:[0,1] neg_hi:[0,1]\n\t"
      "v_pk_add_f32 v[60:61], v[28:29], v[80:81] op_sel_hi:[1,0] neg_lo:[0,1] neg_hi:[0,1]\n\t"
      "v_pk_add_f32 v[62:63], v[30:31], v[80:81] op_sel_hi:[1,0] neg_lo:[0,1] neg_hi:[0,1]\n\t"
      "v_pk_mul_f32 v[64:65], v[32:33], v[32:33]\n\t"
      "v_pk_mul_f32 v[66:67], v[34:35], v[34:35]\n\t"
      "v_pk_mul_f32 v[68:69], v[36:37], v[36:37]\n\t"
      "v_pk_mul_f32 v[70:71], v[38:39], v[38:39]\n\t"
      "v_pk_fma_f32 v[64:65], v[40:41], v[40:41], v[64:65]\n\t"
      "v_pk_fma_f32 v[66:67], v[42:43], v[42:43], v[66:67]\n\t"
      "v_pk_fma_f32 v[68:69], v[44:45], v[44:45], v[68:69]\n\t"
      "s_mov_b64 exec, s[88:89]\n\t"
      "s_add_u32 m0, s76, 0\n\t"
      "s_add_u32 s68, s77, 0\n\t"
      "buffer_load_dwordx4 v108, s[64:67], s68 offen lds\n\t"
      "s_mov_b64 exec, -1\n\t"
      "v_pk_fma_f32 v[70:71], v[46:47], v[46:47], v[70:71]\n\t"
      "v_pk_fma_f32 v[64:65], v[48:49], v[48:49], v[64:65]\n\t"
      "v_pk_fma_f32 v[66:67], v[50:51], v[50:51], v[66:67]\n\t"
      "v_pk_fma_f32 v[68:69], v[52:53], v[52:53], v[68:69]\n\t"
      "v_pk_fma_f32 v[70:71], v[54:55], v[54:55], v[70:71]\n\t"
      "v_pk_fma_f32 v[64:65], v[56:57], v[56:57], v[64:65]\n\t"
      "v_pk_fma_f32 v[66:67], v[58:59], v[58:59], v[66:67]\n\t"
      "v_pk_fma_f32 v[68:69], v[60:61], v[60:61], v[68:69]\n\t"
      "v_pk_fma_f32 v[70:71], v[62:63], v[62:63], v[70:71]\n\t"
      "v_pk_add_f32 v[64:65], v[64:65], v[66:67]\n\t"
      "v_pk_add_f32 v[68:69], v[68:69], v[70:71]\n\t"
      "v_pk_add_f32 v[64:65], v[64:65], v[68:69]\n\t"
      "v_add_f32 v64, v64, v65\n\t"
      "s_nop 1\n\t"
      "v_add_f32_dpp v64, v64, v64 row_ror:4 row_mask:0xf bank_mask:0xf\n\t"
      "s_nop 1\n\t"
      "v_add_f32_dpp v64, v64, v64 row_ror:8 row_mask:0xf bank_mask:0xf\n\t"
      "v_mov_b32 v65, v64\n\t"
      "s_mov_b64 exec, s[88:89]\n\t"
      "s_add_u32 m0, s76, 1024\n\t"
      "s_add_u32 s68, s77, 32768\n\t"
      "s_add_u32 s76, s76, 8192\n\t"
      "s_add_u32 s77, s77, 0x40000\n\t"
      "buffer_load_dwordx4 v108, s[64:67], s68 offen lds\n\t"
      "s_mov_b64 exec, -1\n\t"
      "v_permlane16_swap_b32 v64, v65\n\t"
      "v_add_f32 v64, v64, v65\n\t"
      "v_mov_b32 v65, v64\n\t"
      "s_nop 1\n\t"
      "v_permlane32_swap_b32 v64, v65\n\t"
      "v_add_f32 v64, v64, v65\n\t"
      "v_mul_f32 v81, 0x3b000000, v64\n\t"
      "v_cvt_pk_bf16_f32 v80, v80, v81\n\t"
      "v_and_b32 v81, s84, v80\n\t"
      "v_lshlrev_b32 v80, 16, v80\n\t"
      "v_add_f32 v81, 0x3727c5ac, v81\n\t"
      "v_pk_add_f32 v[32:33], v[0:1], v[80:81] op_sel_hi:[1,0] neg_lo:[0,1] neg_hi:[0,1]\n\t"
      "v_pk_add_f32 v[34:35], v[2:3], v[80:81] op_sel_hi:[1,0] neg_lo:[0,1] neg_hi:[0,1]\n\t"
      "v_pk_add_f32 v[36:37], v[4:5], v[80:81] op_sel_hi:[1,0] neg_lo:[0,1] neg_hi:[0,1]\n\t"
      "v_pk_add_f32 v[38:39], v[6:7], v[80:81] op_sel_hi:[1,0] neg_lo:[0,1] neg_hi:[0,1]\n\t"
      "v_pk_add_f32 v[40:41], v[8:9], v[80:81] op_sel_hi:[1,0] neg_lo:[0,1] neg_hi:[0,1]\n\t"
      "v_pk_add_f32 v[42:43], v[10:11], v[80:81] op_sel_hi:[1,0] neg_lo:[0,1] neg_hi:[0,1]\n\t"
      "s_mov_b64 exec, s[88:89]\n\t"
      "s_add_u32 m0, s76, 0\n\t"
      "s_add_u32 s68, s77, 0\n\t"
      "buffer_load_dwordx4 v108, s[64:67], s68 offen lds\n\t"
      "s_mov_b64 exec, -1\n\t"
      "v_pk_add_f32 v[44:45], v[12:13], v[80:81] op_sel_hi:[1,0] neg_lo:[0,1] neg_hi:[0,1]\n\t"
      "v_pk_add_f32 v[46:47], v[14:15], v[80:81] op_sel_hi:[1,0] neg_lo:[0,1] neg_hi:[0,1]\n\t"
      "v_pk_add_f32 v[48:49], v[16:17], v[80:81] op_sel_hi:[1,0] neg_lo:[0,1] neg_hi:[0,1]\n\t"
      "v_pk_add_f32 v[50:51], v[18:19], v[80:81] op_sel_hi:[1,0] neg_lo:[0,1] neg_hi:[0,1]\n\t"
      "v_pk_add_f32 v[52:53], v[20:21], v[80:81] op_sel_hi:[1,0] neg_lo:[0,1] neg_hi:[0,1]\n\t"
      "v_pk_add_f32 v[54:55], v[22:23], v[80:81] op_sel_hi:[1,0] neg_lo:[0,1] neg_hi:[0,1]\n\t"
      "v_pk_add_f32 v[56:57], v[24:25], v[80:81] op_sel_hi:[1,0] neg_lo:[0,1] neg_hi:[0,1]\n\t"
      "v_pk_add_f32 v[58:59], v[26:27], v[80:81] op_sel_hi:[1,0] neg_lo:[0,1] neg_hi:[0,1]\n\t"
      "v_pk_add_f32 v[60:61], v[28:29], v[80:81] op_sel_hi:[1,0] neg_lo:[0,1] neg_hi:[0,1]\n\t"
      "v_pk_add_f32 v[62:63], v[30:31], v[80:81] op_sel_hi:[1,0] neg_lo:[0,1] neg_hi:[0,1]\n\t"
      "v_cvt_pk_bf16_f32 v81, v81, v81\n\t"
      "v_and_b32 v81, s84, v81\n\t"
      "v_sqrt_f32 v81, v81\n\t"
      "v_cvt_pk_bf16_f32 v32, v32, v33\n\t"
      "v_cvt_pk_bf16_f32 v34, v34, v35\n\t"
      "v_cvt_pk_bf16_f32 v36, v36, v37\n\t"
      "s_mov_b64 exec, s[88:89]\n\t"
      "s_add_u32 m0, s76, 1024\n\t"
      "s_add_u32 s68, s77, 32768\n\t"
      "s_add_u32 s76, s76, 8192\n\t"
      "s_add_u32 s77, s77, 0x40000\n\t"
      "buffer_load_dwordx4 v108, s[64:67], s68 offen lds\n\t"
      "s_mov_b64 exec, -1\n\t"
      "v_cvt_pk_bf16_f32 v38, v38, v39\n\t"
      "v_cvt_pk_bf16_f32 v40, v40, v41\n\t"
      "v_cvt_pk_bf16_f32 v42, v42, v43\n\t"
      "v_cvt_pk_bf16_f32 v44, v44, v45\n\t"
      "v_cvt_pk_bf16_f32 v46, v46, v47\n\t"
      "v_cvt_pk_bf16_f32 v48, v48, v49\n\t"
      "v_cvt_pk_bf16_f32 v50, v50, v51\n\t"
      "v_cvt_pk_bf16_f32 v52, v52, v53\n\t"
      "v_cvt_pk_bf16_f32 v54, v54, v55\n\t"
      "v_cvt_pk_bf16_f32 v56, v56, v57\n\t"
      "v_cvt_pk_bf16_f32 v58, v58, v59\n\t"
      "v_cvt_pk_bf16_f32 v60, v60, v61\n\t"
      "v_cvt_pk_bf16_f32 v62, v62, v63\n\t"
      "v_cvt_pk_bf16_f32 v81, v81, v81\n\t"
      "v_and_b32 v81, s84, v81\n\t"
      "v_rcp_f32 v80, v81\n\t"
      "s_mov_b64 exec, s[88:89]\n\t"
      "s_add_u32 m0, s76, 0\n\t"
      "s_add_u32 s68, s77, 0\n\t"
      "buffer_load_dwordx4 v108, s[64:67], s68 offen lds\n\t"
      "s_mov_b64 exec, -1\n\t"
      "v_and_b32 v33, s84, v32\n\t"
      "v_lshlrev_b32 v32, 16, v32\n\t"
      "v_and_b32 v35, s84, v34\n\t"
      "v_lshlrev_b32 v34, 16, v34\n\t"
      "v_and_b32 v37, s84, v36\n\t"
      "v_lshlrev_b32 v36, 16, v36\n\t"
      "v_and_b32 v39, s84, v38\n\t"
      "v_lshlrev_b32 v38, 16, v38\n\t"
      "v_pk_mul_f32 v[32:33], v[32:33], v[80:81] op_sel_hi:[1,0]\n\t"
      "v_pk_mul_f32 v[34:35], v[34:35], v[80:81] op_sel_hi:[1,0]\n\t"
      "v_pk_mul_f32 v[36:37], v[36:37], v[80:81] op_sel_hi:[1,0]\n\t"
      "v_pk_mul_f32 v[38:39], v[38:39], v[80:81] op_sel_hi:[1,0]\n\t"
      "v_cvt_pk_bf16_f32 v64, v32, v33\n\t"
      "v_cvt_pk_bf16_f32 v65, v34, v35\n\t"
      "v_cvt_pk_bf16_f32 v66, v36, v37\n\t"
      "v_cvt_pk_bf16_f32 v67, v38, v39\n\t"
      "s_mov_b64 exec, s[88:89]\n\t"
      "s_add_u32 m0, s76, 1024\n\t"
      "s_add_u32 s68, s77, 32768\n\t"
      "s_add_u32 s76, s76, 8192\n\t"
      "s_add_u32 s77, s77, 0x40000\n\t"
      "buffer_load_dwordx4 v108, s[64:67], s68 offen lds\n\t"
      "s_mov_b64 exec, -1\n\t"
      "v_and_b32 v41, s84, v40\n\t"
      "v_lshlrev_b32 v40, 16, v40\n\t"
      "v_and_b32 v43, s84, v42\n\t"
      "v_lshlrev_b32 v42, 16, v42\n\t"
      "v_and_b32 v45, s84, v44\n\t"
      "v_lshlrev_b32 v44, 16, v44\n\t"
      "v_and_b32 v47, s84, v46\n\t"
      "v_lshlrev_b32 v46, 16, v46\n\t"
      "v_pk_mul_f32 v[40:41], v[40:41], v[80:81] op_sel_hi:[1,0]\n\t"
      "v_pk_mul_f32 v[42:43], v[42:43], v[80:81] op_sel_hi:[1,0]\n\t"
      "v_pk_mul_f32 v[44:45], v[44:45], v[80:81] op_sel_hi:[1,0]\n\t"
      "v_pk_mul_f32 v[46:47], v[46:47], v[80:81] op_sel_hi:[1,0]\n\t"
      "v_cvt_pk_bf16_f32 v68, v40, v41\n\t"
      "v_cvt_pk_bf16_f32 v69, v42, v43\n\t"
      "v_cvt_pk_bf16_f32 v70, v44, v45\n\t"
      "v_cvt_pk_bf16_f32 v71, v46, v47\n\t"
      "s_mov_b64 exec, s[88:89]\n\t"
      "s_add_u32 m0, s76, 0\n\t"
      "s_add_u32 s68, s77, 0\n\t"
      "buffer_load_dwordx4 v108, s[64:67], s68 offen lds\n\t"
      "s_mov_b64 exec, -1\n\t"
      "v_and_b32 v49, s84, v48\n\t"
      "v_lshlrev_b32 v48, 16, v48\n\t"
      "v_and_b32 v51, s84, v50\n\t"
      "v_lshlrev_b32 v50, 16, v50\n\t"
      "v_and_b32 v53, s84, v52\n\t"
      "v_lshlrev_b32 v52, 16, v52\n\t"
      "v_and_b32 v55, s84, v54\n\t"
      "v_lshlrev_b32 v54, 16, v54\n\t"
      "v_pk_mul_f32 v[48:49], v[48:49], v[80:81] op_sel_hi:[1,0]\n\t"
      "v_pk_mul_f32 v[50:51], v[50:51], v[80:81] op_sel_hi:[1,0]\n\t"
      "v_pk_mul_f32 v[52:53], v[52:53], v[80:81] op_sel_hi:[1,0]\n\t"
      "v_pk_mul_f32 v[54:55], v[54:55], v[80:81] op_sel_hi:[1,0]\n\t"
      "v_cvt_pk_bf16_f32 v72, v48, v49\n\t"
      "v_cvt_pk_bf16_f32 v73, v50, v51\n\t"
      "v_cvt_pk_bf16_f32 v74, v52, v53\n\t"
      "v_cvt_pk_bf16_f32 v75, v54, v55\n\t"
      "s_mov_b64 exec, s[88:89]\n\t"
      "s_add_u32 m0, s76, 1024\n\t"
      "s_add_u32 s68, s77, 32768\n\t"
      "s_add_u32 s76, s76, 8192\n\t"
      "s_add_u32 s77, s77, 0x40000\n\t"
      "buffer_load_dwordx4 v108, s[64:67], s68 offen lds\n\t"
      "s_mov_b64 exec, -1\n\t"
      "v_and_b32 v57, s84, v56\n\t"
      "v_lshlrev_b32 v56, 16, v56\n\t"
      "v_and_b32 v59, s84, v58\n\t"
      "v_lshlrev_b32 v58, 16, v58\n\t"
      "v_and_b32 v61, s84, v60\n\t"
      "v_lshlrev_b32 v60, 16, v60\n\t"
      "v_and_b32 v63, s84, v62\n\t"
      "v_lshlrev_b32 v62, 16, v62\n\t"
      "v_pk_mul_f32 v[56:57], v[56:57], v[80:81] op_sel_hi:[1,0]\n\t"
      "v_pk_mul_f32 v[58:59], v[58:59], v[80:81] op_sel_hi:[1,0]\n\t"
      "v_pk_mul_f32 v[60:61], v[60:61], v[80:81] op_sel_hi:[1,0]\n\t"
      "v_pk_mul_f32 v[62:63], v[62:63], v[80:81] op_sel_hi:[1,0]\n\t"
      "v_cvt_pk_bf16_f32 v76, v56, v57\n\t"
      "v_cvt_pk_bf16_f32 v77, v58, v59\n\t"
      "v_cvt_pk_bf16_f32 v78, v60, v61\n\t"
      "v_cvt_pk_bf16_f32 v79, v62, v63\n\t"
      "s_cmp_lg_u32 s69, 0\n\t"
      "s_cbranch_scc1 GLN1%=\n\t"
      "buffer_store_dwordx4 v[64:67], v106, s[60:63], s74 offen sc1\n\t"
      "s_add_u32 s74, s74, 4096\n\t"
      "buffer_store_dwordx4 v[68:71], v106, s[60:63], s74 offen sc1\n\t"
      "s_add_u32 s74, s74, 4096\n\t"
      "buffer_store_dwordx4 v[72:75], v106, s[60:63], s74 offen sc1\n\t"
      "s_add_u32 s74, s74, 4096\n\t"
      "buffer_store_dwordx4 v[76:79], v106, s[60:63], s74 offen sc1\n\t"
      "s_add_u32 s74, s74, 4096\n\t"
      "s_sub_u32 s76, s76, 49152\n\t"
      "s_sub_u32 s77, s77, 1572864\n\t"
      "s_mov_b64 s[88:89], -1\n\t"
      "v_readfirstlane_b32 %3, v107\n\t"
      "v_mov_b64 v[64:65], v[128:129]\n\t"
      "v_mov_b64 v[66:67], v[130:131]\n\t"
      "v_mov_b64 v[68:69], v[132:133]\n\t"
      "v_mov_b64 v[70:71], v[134:135]\n\t"
      "v_mov_b64 v[72:73], v[136:137]\n\t"
      "v_mov_b64 v[74:75], v[138:139]\n\t"
      "v_mov_b64 v[76:77], v[140:141]\n\t"
      "v_mov_b64 v[78:79], v[142:143]\n\t"
      "s_mul_i32 %3, %3, 0x9e3779b1\n\t"
      "s_add_u32 %3, %3, 0x7f4a7c15\n\t"
      "s_mov_b32 s69, 1\n\t"
      "s_branch GLN%=\n\t"
      "GLN1%=:\n\t"
      "ds_write_b128 v110, v[64:67]\n\t"
      "ds_write_b128 v110, v[68:71] offset:4096\n\t"
      "ds_write_b128 v110, v[72:75] offset:8192\n\t"
      "ds_write_b128 v110, v[76:79] offset:12288\n\t"
      "s_cmp_eq_u32 s69, 2\n\t"
      "s_cbranch_scc1 GLN2%=\n\t"
      "v_mov_b32 v107, %3\n\t"
      "v_add_u32 v110, 0x4000, v110\n\t"
      "s_lshl_b32 s79, %0, 7\n\t"
      "s_add_u32 s79, s79, 37120\n\t"
      "s_waitcnt vmcnt(12) lgkmcnt(0)\n\t"
      "s_barrier\n\t"
      "s_mov_b64 exec, s[90:91]\n\t"
      "buffer_store_dword v107, off, s[60:63], s79 sc1\n\t"
      "s_mov_b64 exec, -1\n\t"
      "v_mov_b64 v[64:65], v[144:145]\n\t"
      "v_mov_b64 v[66:67], v[146:147]\n\t"
      "v_mov_b64 v[68:69], v[148:149]\n\t"
      "v_mov_b64 v[70:71], v[150:151]\n\t"
      "v_mov_b64 v[72:73], v[152:153]\n\t"
      "v_mov_b64 v[74:75], v[154:155]\n\t"
      "v_mov_b64 v[76:77], v[156:157]\n\t"
      "v_mov_b64 v[78:79], v[158:159]\n\t"
      "s_mov_b32 s69, 2\n\t"
      "s_branch GLN%=\n\t"
      "GLN2%=:\n\t"
      "s_waitcnt lgkmcnt(10)\n\t"
      "ds_read_b64_tr_b16 a[0:1], v109\n\t"
      "ds_read_b64_tr_b16 a[2:3], v109 offset:1024\n\t"
      "ds_read_b64_tr_b16 a[4:5], v109 offset:8\n\t"
      "ds_read_b64_tr_b16 a[6:7], v109 offset:1032\n\t"
      "s_waitcnt lgkmcnt(10)\n\t"
      "ds_read_b64_tr_b16 a[8:9], v109 offset:8192\n\t"
      "ds_read_b64_tr_b16 a[10:11], v109 offset:9216\n\t"
      "ds_read_b64_tr_b16 a[12:13], v109 offset:8200\n\t"
      "ds_read_b64_tr_b16 a[14:15], v109 offset:9224\n\t"
      "s_waitcnt lgkmcnt(10)\n\t"
      "ds_read_b64_tr_b16 a[16:17], v109 offset:16384\n\t"
      "ds_read_b64_tr_b16 a[18:19], v109 offset:17408\n\t"
      "ds_read_b64_tr_b16 a[20:21], v109 offset:16392\n\t"
      "ds_read_b64_tr_b16 a[22:23], v109 offset:17416\n\t"
      "s_waitcnt lgkmcnt(10)\n\t"
      "ds_read_b64_tr_b16 a[24:25], v109 offset:24576\n\t"
      "ds_read_b64_tr_b16 a[26:27], v109 offset:25600\n\t"
      "ds_read_b64_tr_b16 a[28:29], v109 offset:24584\n\t"
      "ds_read_b64_tr_b16 a[30:31], v109 offset:25608\n\t"
      "v_lshlrev_b32 v106, 8, v96\n\t"
      "s_mov_b32 s79, 37120\n\t"
      "s_mov_b32 s81, 0x40000\n\t"
      "v_add_u32 v98, 131072, v97\n\t"
      "v_readlane_b32 s73, v102, 0\n\t"
      "v_readlane_b32 s75, v103, 0\n\t"
      "s_mov_b64 s[64:65], s[40:41]\n\t"
      "s_lshl_b32 s74, %1, 10\n\t"
      "v_readlane_b32 s72, v101, 1\n\t"
      "s_add_u32 s76, s85, 0\n\t"
      "s_waitcnt vmcnt(12)\n\t"
      "s_barrier\n\t"
      "s_waitcnt lgkmcnt(10)\n\t"
      "ds_read_b64_tr_b16 a[32:33], v109 offset:32768\n\t"
      "ds_read_b64_tr_b16 a[34:35], v109 offset:33792\n\t"
      "ds_read_b64_tr_b16 a[36:37], v109 offset:32776\n\t"
      "ds_read_b64_tr_b16 a[38:39], v109 offset:33800\n\t"
      "s_waitcnt lgkmcnt(10)\n\t"
      "ds_read_b64_tr_b16 a[40:41], v109 offset:40960\n\t"
      "ds_read_b64_tr_b16 a[42:43], v109 offset:41984\n\t"
      "ds_read_b64_tr_b16 a[44:45], v109 offset:40968\n\t"
      "ds_read_b64_tr_b16 a[46:47], v109 offset:41992\n\t"
      "s_waitcnt lgkmcnt(10)\n\t"
      "ds_read_b64_tr_b16 a[48:49], v109 offset:49152\n\t"
      "ds_read_b64_tr_b16 a[50:51], v109 offset:50176\n\t"
      "ds_read_b64_tr_b16 a[52:53], v109 offset:49160\n\t"
      "ds_read_b64_tr_b16 a[54:55], v109 offset:50184\n\t"
      "s_waitcnt lgkmcnt(10)\n\t"
      "ds_read_b64_tr_b16 a[56:57], v109 offset:57344\n\t"
      "ds_read_b64_tr_b16 a[58:59], v109 offset:58368\n\t"
      "ds_read_b64_tr_b16 a[60:61], v109 offset:57352\n\t"
      "ds_read_b64_tr_b16 a[62:63], v109 offset:58376\n\t"
      "v_add_u32 v109, 0x10000, v109\n\t"
      "s_waitcnt lgkmcnt(10)\n\t"
      "ds_read_b64_tr_b16 a[64:65], v109\n\t"
      "ds_read_b64_tr_b16 a[66:67], v109 offset:1024\n\t"
      "ds_read_b64_tr_b16 a[68:69], v109 offset:8\n\t"
      "ds_read_b64_tr_b16 a[70:71], v109 offset:1032\n\t"
      "s_waitcnt lgkmcnt(10)\n\t"
      "ds_read_b64_tr_b16 a[72:73], v109 offset:8192\n\t"
      "ds_read_b64_tr_b16 a[74:75], v109 offset:9216\n\t"
      "ds_read_b64_tr_b16 a[76:77], v109 offset:8200\n\t"
      "ds_read_b64_tr_b16 a[78:79], v109 offset:9224\n\t"
      "s_waitcnt vmcnt(0)\n\t"
      "s_barrier\n\t"
      "buffer_load_dword v128, v106, s[60:63], s79 offen sc1\n\t"
      "buffer_load_dword v129, v106, s[60:63], s79 offen offset:128 sc1\n\t"
      "s_waitcnt lgkmcnt(10)\n\t"
      "ds_read_b64_tr_b16 a[80:81], v109 offset:16384\n\t"
      "ds_read_b64_tr_b16 a[82:83], v109 offset:17408\n\t"
      "ds_read_b64_tr_b16 a[84:85], v109 offset:16392\n\t"
      "ds_read_b64_tr_b16 a[86:87], v109 offset:17416\n\t"
      "s_waitcnt lgkmcnt(10)\n\t"
      "ds_read_b64_tr_b16 a[88:89], v109 offset:24576\n\t"
      "ds_read_b64_tr_b16 a[90:91], v109 offset:25600\n\t"
      "ds_read_b64_tr_b16 a[92:93], v109 offset:24584\n\t"
      "ds_read_b64_tr_b16 a[94:95], v109 offset:25608\n\t"
      "s_waitcnt lgkmcnt(10)\n\t"
      "ds_read_b64_tr_b16 a[96:97], v109 offset:32768\n\t"
      "ds_read_b64_tr_b16 a[98:99], v109 offset:33792\n\t"
      "ds_read_b64_tr_b16 a[100:101], v109 offset:32776\n\t"
      "ds_read_b64_tr_b16 a[102:103], v109 offset:33800\n\t"
      "s_waitcnt lgkmcnt(10)\n\t"
      "ds_read_b64_tr_b16 a[104:105], v109 offset:40960\n\t"
      "ds_read_b64_tr_b16 a[106:107], v109 offset:41984\n\t"
      "ds_read_b64_tr_b16 a[108:109], v109 offset:40968\n\t"
      "ds_read_b64_tr_b16 a[110:111], v109 offset:41992\n\t"
      "s_waitcnt lgkmcnt(10)\n\t"
      "ds_read_b64_tr_b16 a[112:113], v109 offset:49152\n\t"
      "ds_read_b64_tr_b16 a[114:115], v109 offset:50176\n\t"
      "ds_read_b64_tr_b16 a[116:117], v109 offset:49160\n\t"
      "ds_read_b64_tr_b16 a[118:119], v109 offset:50184\n\t"
      "s_waitcnt lgkmcnt(10)\n\t"
      "ds_read_b64_tr_b16 a[120:121], v109 offset:57344\n\t"
      "ds_read_b64_tr_b16 a[122:123], v109 offset:58368\n\t"
      "ds_read_b64_tr_b16 a[124:125], v109 offset:57352\n\t"
      "ds_read_b64_tr_b16 a[126:127], v109 offset:58376\n\t"
      "s_mov_b64 s[88:89], 0\n\t"
      "s_mov_b64 s[38:39], 0\n\t"
      "s_waitcnt lgkmcnt(4)\n\t"
      "s_mov_b32 s71, 0\n\t"
      "GKV%=:\n\t"
      "ds_read_b128 v[0:3], v98\n\t"
      "ds_read_b128 v[4:7], v98 offset:1024\n\t"
      "ds_read_b128 v[8:11], v98 offset:2048\n\t"
      "ds_read_b128 v[12:15], v98 offset:3072\n\t"
      "ds_read_b128 v[16:19], v98 offset:4096\n\t"
      "ds_read_b128 v[20:23], v98 offset:5120\n\t"
      "ds_read_b128 v[24:27], v98 offset:6144\n\t"
      "ds_read_b128 v[28:31], v98 offset:7168\n\t"
      "ds_read_b128 v[32:35], v98 offset:8192\n\t"
      "ds_read_b128 v[36:39], v98 offset:9216\n\t"
      "s_waitcnt lgkmcnt(9)\n\t"
      "v_mfma_f32_16x16x32_bf16 v[64:67], a[0:3], v[0:3], 0\n\t"
      "ds_read_b128 v[40:43], v98 offset:10240\n\t"
      "v_mfma_f32_16x16x32_bf16 v[68:71], a[4:7], v[0:3], 0\n\t"
      "s_waitcnt lgkmcnt(9)\n\t"
      "v_mfma_f32_16x16x32_bf16 v[64:67], a[8:11], v[4:7], v[64:67]\n\t"
      "ds_read_b128 v[44:47], v98 offset:11264\n\t"
      "v_mfma_f32_16x16x32_bf16 v[68:71], a[12:15], v[4:7], v[68:71]\n\t"
      "s_waitcnt lgkmcnt(9)\n\t"
      "v_mfma_f32_16x16x32_bf16 v[64:67], a[16:19], v[8:11], v[64:67]\n\t"
      "ds_read_b128 v[48:51], v98 offset:12288\n\t"
      "v_mfma_f32_16x16x32_bf16 v[68:71], a[20:23], v[8:11], v[68:71]\n\t"
      "s_waitcnt lgkmcnt(9)\n\t"
      "v_mfma_f32_16x16x32_bf16 v[64:67], a[24:27], v[12:15], v[64:67]\n\t"
      "ds_read_b128 v[52:55], v98 offset:13312\n\t"
      "v_mfma_f32_16x16x32_bf16 v[68:71], a[28:31], v[12:15], v[68:71]\n\t"
      "s_waitcnt lgkmcnt(9)\n\t"
      "v_mfma_f32_16x16x32_bf16 v[64:67], a[32:35], v[16:19], v[64:67]\n\t"
      "ds_read_b128 v[56:59], v98 offset:14336\n\t"
      "v_mfma_f32_16x16x32_bf16 v[68:71], a[36:39], v[16:19], v[68:71]\n\t"
      "s_waitcnt lgkmcnt(9)\n\t"
      "v_mfma_f32_16x16x32_bf16 v[64:67], a[40:43], v[20:23], v[64:67]\n\t"
      "ds_read_b128 v[60:63], v98 offset:15360\n\t"
      "v_mfma_f32_16x16x32_bf16 v[68:71], a[44:47], v[20:23], v[68:71]\n\t"
      "s_waitcnt lgkmcnt(9)\n\t"
      "v_mfma_f32_16x16x32_bf16 v[64:67], a[48:51], v[24:27], v[64:67]\n\t"
      "ds_read_b128 v[0:3], v98 offset:16384\n\t"
      "v_mfma_f32_16x16x32_bf16 v[68:71], a[52:55], v[24:27], v[68:71]\n\t"
      "s_waitcnt lgkmcnt(9)\n\t"
      "v_mfma_f32_16x16x32_bf16 v[64:67], a[56:59], v[28:31], v[64:67]\n\t"
      "ds_read_b128 v[4:7], v98 offset:17408\n\t"
      "v_mfma_f32_16x16x32_bf16 v[68:71], a[60:63], v[28:31], v[68:71]\n\t"
      "s_waitcnt lgkmcnt(9)\n\t"
      "v_mfma_f32_16x16x32_bf16 v[64:67], a[64:67], v[32:35], v[64:67]\n\t"
      "ds_read_b128 v[8:11], v98 offset:18432\n\t"
      "v_mfma_f32_16x16x32_bf16 v[68:71], a[68:71], v[32:35], v[68:71]\n\t"
      "s_waitcnt lgkmcnt(9)\n\t"
      "v_mfma_f32_16x16x32_bf16 v[64:67], a[72:75], v[36:39], v[64:67]\n\t"
      "ds_read_b128 v[12:15], v98 offset:19456\n\t"
      "v_mfma_f32_16x16x32_bf16 v[68:71], a[76:79], v[36:39], v[68:71]\n\t"
      "s_waitcnt lgkmcnt(9)\n\t"
      "v_mfma_f32_16x16x32_bf16 v[64:67], a[80:83], v[40:43], v[64:67]\n\t"
      "ds_read_b128 v[16:19], v98 offset:20480\n\t"
      "v_mfma_f32_16x16x32_bf16 v[68:71], a[84:87], v[40:43], v[68:71]\n\t"
      "s_waitcnt lgkmcnt(9)\n\t"
      "v_mfma_f32_16x16x32_bf16 v[64:67], a[88:91], v[44:47], v[64:67]\n\t"
      "ds_read_b128 v[20:23], v98 offset:21504\n\t"
      "v_mfma_f32_16x16x32_bf16 v[68:71], a[92:95], v[44:47], v[68:71]\n\t"
      "s_waitcnt lgkmcnt(9)\n\t"
      "v_mfma_f32_16x16x32_bf16 v[64:67], a[96:99], v[48:51], v[64:67]\n\t"
      "ds_read_b128 v[24:27], v98 offset:22528\n\t"
      "v_mfma_f32_16x16x32_bf16 v[68:71], a[100:103], v[48:51], v[68:71]\n\t"
      "s_cmp_lg_u32 s71, 0\n\t"
      "s_barrier\n\t"
      "s_cbranch_scc1 GKV0%=\n\t"
      "s_waitcnt vmcnt(0)\n\t"
      "v_bitop3_b32 v128, v128, v129, %3 bitop3:0x7e\n\t"
      "v_cmp_ne_u32 vcc, 0, v128\n\t"
      "s_nop 0\n\t"
      "s_cmp_eq_u64 vcc, 0\n\t"
      "s_cselect_b64 s[36:37], -1, 0\n\t"
      "GKV0%=:\n\t"
      "s_waitcnt lgkmcnt(9)\n\t"
      "v_mfma_f32_16x16x32_bf16 v[64:67], a[104:107], v[52:55], v[64:67]\n\t"
      "ds_read_b128 v[28:31], v98 offset:23552\n\t"
      "v_mfma_f32_16x16x32_bf16 v[68:71], a[108:111], v[52:55], v[68:71]\n\t"
      "s_add_u32 m0, s76, 0\n\t"
      "s_mov_b64 exec, s[36:37]\n\t"
      "buffer_load_dwordx4 v97, s[60:63], s72 offen sc1 lds\n\t"
      "s_mov_b64 exec, -1\n\t"
      "s_waitcnt lgkmcnt(9)\n\t"
      "v_mfma_f32_16x16x32_bf16 v[64:67], a[112:115], v[56:59], v[64:67]\n\t"
      "ds_read_b128 v[32:35], v98 offset:24576\n\t"
      "v_mfma_f32_16x16x32_bf16 v[68:71], a[116:119], v[56:59], v[68:71]\n\t"
      "s_waitcnt lgkmcnt(9)\n\t"
      "v_mfma_f32_16x16x32_bf16 v[64:67], a[120:123], v[60:63], v[64:67]\n\t"
      "ds_read_b128 v[36:39], v98 offset:25600\n\t"
      "v_mfma_f32_16x16x32_bf16 v[68:71], a[124:127], v[60:63], v[68:71]\n\t"
      "s_add_u32 m0, s76, 0\n\t"
      "s_mov_b64 exec, s[36:37]\n\t"
      "buffer_load_dwordx4 v97, s[60:63], s72 offen offset:1024 sc1 lds\n\t"
      "s_mov_b64 exec, -1\n\t"
      "s_waitcnt lgkmcnt(9)\n\t"
      "v_mfma_f32_16x16x32_bf16 v[72:75], a[0:3], v[0:3], 0\n\t"
      "ds_read_b128 v[40:43], v98 offset:26624\n\t"
      "v_mfma_f32_16x16x32_bf16 v[76:79], a[4:7], v[0:3], 0\n\t"
      "s_waitcnt lgkmcnt(9)\n\t"
      "v_mfma_f32_16x16x32_bf16 v[72:75], a[8:11], v[4:7], v[72:75]\n\t"
      "ds_read_b128 v[44:47], v98 offset:27648\n\t"
      "v_mfma_f32_16x16x32_bf16 v[76:79], a[12:15], v[4:7], v[76:79]\n\t"
      "s_add_u32 m0, s76, 0\n\t"
      "s_mov_b64 exec, s[36:37]\n\t"
      "buffer_load_dwordx4 v97, s[60:63], s72 offen offset:2048 sc1 lds\n\t"
      "s_mov_b64 exec, -1\n\t"
      "s_cmp_eq_u32 s71, 1\n\t"
      "s_cbranch_scc0 GKV2%=\n\t"
      "s_waitcnt vmcnt(3)\n\t"
      "s_barrier\n\t"
      "s_mov_b64 exec, s[38:39]\n\t"
      "buffer_store_dword v107, off, s[60:63], s75 sc1\n\t"
      "s_mov_b64 exec, -1\n\t"
      "GKV2%=:\n\t"
      "s_waitcnt lgkmcnt(9)\n\t"
      "v_mfma_f32_16x16x32_bf16 v[72:75], a[16:19], v[8:11], v[72:75]\n\t"
      "ds_read_b128 v[48:51], v98 offset:28672\n\t"
      "v_mfma_f32_16x16x32_bf16 v[76:79], a[20:23], v[8:11], v[76:79]\n\t"
      "s_add_u32 m0, s74, 65536\n\t"
      "s_mov_b32 s78, 0\n\t"
      "s_mov_b64 exec, s[88:89]\n\t"
      "buffer_load_dwordx4 v116, s[64:67], s78 offen lds\n\t"
      "s_mov_b64 exec, -1\n\t"
      "s_waitcnt lgkmcnt(9)\n\t"
      "v_mfma_f32_16x16x32_bf16 v[72:75], a[24:27], v[12:15], v[72:75]\n\t"
      "ds_read_b128 v[52:55], v98 offset:29696\n\t"
      "v_mfma_f32_16x16x32_bf16 v[76:79], a[28:31], v[12:15], v[76:79]\n\t"
      "s_add_u32 m0, s76, 0\n\t"
      "s_mov_b64 exec, s[36:37]\n\t"
      "buffer_load_dwordx4 v97, s[60:63], s72 offen offset:3072 sc1 lds\n\t"
      "s_mov_b64 exec, -1\n\t"
      "s_waitcnt lgkmcnt(9)\n\t"
      "v_mfma_f32_16x16x32_bf16 v[72:75], a[32:35], v[16:19], v[72:75]\n\t"
      "ds_read_b128 v[56:59], v98 offset:30720\n\t"
      "v_mfma_f32_16x16x32_bf16 v[76:79], a[36:39], v[16:19], v[76:79]\n\t"
      "s_add_u32 m0, s74, 69632\n\t"
      "s_mov_b32 s78, 262144\n\t"
      "s_mov_b64 exec, s[88:89]\n\t"
      "buffer_load_dwordx4 v116, s[64:67], s78 offen lds\n\t"
      "s_mov_b64 exec, -1\n\t"
      "s_waitcnt lgkmcnt(9)\n\t"
      "v_mfma_f32_16x16x32_bf16 v[72:75], a[40:43], v[20:23], v[72:75]\n\t"
      "ds_read_b128 v[60:63], v98 offset:31744\n\t"
      "v_mfma_f32_16x16x32_bf16 v[76:79], a[44:47], v[20:23], v[76:79]\n\t"
      "s_add_u32 m0, s76, 4096\n\t"
      "s_add_u32 s72, s72, 4096\n\t"
      "s_mov_b64 exec, s[36:37]\n\t"
      "buffer_load_dwordx4 v97, s[60:63], s72 offen sc1 lds\n\t"
      "s_mov_b64 exec, -1\n\t"
      "v_cvt_pk_bf16_f32 v80, v64, v65\n\t"
      "v_cvt_pk_bf16_f32 v81, v66, v67\n\t"
      "v_cvt_pk_bf16_f32 v82, v68, v69\n\t"
      "v_cvt_pk_bf16_f32 v83, v70, v71\n\t"
      "buffer_store_dwordx4 v[80:83], v97, s[60:63], s73 offen sc1\n\t"
      "s_waitcnt lgkmcnt(9)\n\t"
      "v_mfma_f32_16x16x32_bf16 v[72:75], a[48:51], v[24:27], v[72:75]\n\t"
      "v_mfma_f32_16x16x32_bf16 v[76:79], a[52:55], v[24:27], v[76:79]\n\t"
      "s_add_u32 m0, s74, 73728\n\t"
      "s_mov_b32 s78, 524288\n\t"
      "s_mov_b64 exec, s[88:89]\n\t"
      "buffer_load_dwordx4 v116, s[64:67], s78 offen lds\n\t"
      "s_mov_b64 exec, -1\n\t"
      "s_waitcnt lgkmcnt(8)\n\t"
      "v_mfma_f32_16x16x32_bf16 v[72:75], a[56:59], v[28:31], v[72:75]\n\t"
      "v_mfma_f32_16x16x32_bf16 v[76:79], a[60:63], v[28:31], v[76:79]\n\t"
      "s_add_u32 m0, s76, 4096\n\t"
      "s_mov_b64 exec, s[36:37]\n\t"
      "buffer_load_dwordx4 v97, s[60:63], s72 offen offset:1024 sc1 lds\n\t"
      "s_mov_b64 exec, -1\n\t"
      "s_waitcnt lgkmcnt(7)\n\t"
      "v_mfma_f32_16x16x32_bf16 v[72:75], a[64:67], v[32:35], v[72:75]\n\t"
      "v_mfma_f32_16x16x32_bf16 v[76:79], a[68:71], v[32:35], v[76:79]\n\t"
      "s_add_u32 m0, s74, 77824\n\t"
      "s_mov_b32 s78, 786432\n\t"
      "s_mov_b64 exec, s[88:89]\n\t"
      "buffer_load_dwordx4 v116, s[64:67], s78 offen lds\n\t"
      "s_mov_b64 exec, -1\n\t"
      "s_waitcnt lgkmcnt(6)\n\t"
      "v_mfma_f32_16x16x32_bf16 v[72:75], a[72:75], v[36:39], v[72:75]\n\t"
      "v_mfma_f32_16x16x32_bf16 v[76:79], a[76:79], v[36:39], v[76:79]\n\t"
      "s_add_u32 m0, s76, 4096\n\t"
      "s_mov_b64 exec, s[36:37]\n\t"
      "buffer_load_dwordx4 v97, s[60:63], s72 offen offset:2048 sc1 lds\n\t"
      "s_mov_b64 exec, -1\n\t"
      "s_waitcnt lgkmcnt(5)\n\t"
      "v_mfma_f32_16x16x32_bf16 v[72:75], a[80:83], v[40:43], v[72:75]\n\t"
      "v_mfma_f32_16x16x32_bf16 v[76:79], a[84:87], v[40:43], v[76:79]\n\t"
      "s_add_u32 m0, s74, 81920\n\t"
      "s_mov_b32 s78, 1048576\n\t"
      "s_mov_b64 exec, s[88:89]\n\t"
      "buffer_load_dwordx4 v116, s[64:67], s78 offen lds\n\t"
      "s_mov_b64 exec, -1\n\t"
      "s_waitcnt lgkmcnt(4)\n\t"
      "v_mfma_f32_16x16x32_bf16 v[72:75], a[88:91], v[44:47], v[72:75]\n\t"
      "v_mfma_f32_16x16x32_bf16 v[76:79], a[92:95], v[44:47], v[76:79]\n\t"
      "s_add_u32 m0, s76, 4096\n\t"
      "s_mov_b64 exec, s[36:37]\n\t"
      "buffer_load_dwordx4 v97, s[60:63], s72 offen offset:3072 sc1 lds\n\t"
      "s_mov_b64 exec, -1\n\t"
      "s_waitcnt lgkmcnt(3)\n\t"
      "v_mfma_f32_16x16x32_bf16 v[72:75], a[96:99], v[48:51], v[72:75]\n\t"
      "v_mfma_f32_16x16x32_bf16 v[76:79], a[100:103], v[48:51], v[76:79]\n\t"
      "s_add_u32 m0, s74, 86016\n\t"
      "s_mov_b32 s78, 1310720\n\t"
      "s_mov_b64 exec, s[88:89]\n\t"
      "buffer_load_dwordx4 v116, s[64:67], s78 offen lds\n\t"
      "s_mov_b64 exec, -1\n\t"
      "s_waitcnt lgkmcnt(2)\n\t"
      "v_mfma_f32_16x16x32_bf16 v[72:75], a[104:107], v[52:55], v[72:75]\n\t"
      "v_mfma_f32_16x16x32_bf16 v[76:79], a[108:111], v[52:55], v[76:79]\n\t"
      "s_waitcnt lgkmcnt(1)\n\t"
      "v_mfma_f32_16x16x32_bf16 v[72:75], a[112:115], v[56:59], v[72:75]\n\t"
      "v_mfma_f32_16x16x32_bf16 v[76:79], a[116:119], v[56:59], v[76:79]\n\t"
      "s_add_u32 m0, s74, 90112\n\t"
      "s_mov_b32 s78, 1572864\n\t"
      "s_mov_b64 exec, s[88:89]\n\t"
      "buffer_load_dwordx4 v116, s[64:67], s78 offen lds\n\t"
      "s_mov_b64 exec, -1\n\t"
      "s_waitcnt lgkmcnt(0)\n\t"
      "v_mfma_f32_16x16x32_bf16 v[72:75], a[120:123], v[60:63], v[72:75]\n\t"
      "v_mfma_f32_16x16x32_bf16 v[76:79], a[124:127], v[60:63], v[76:79]\n\t"
      "s_nop 6\n\t"
      "v_cvt_pk_bf16_f32 v84, v72, v73\n\t"
      "v_cvt_pk_bf16_f32 v85, v74, v75\n\t"
      "v_cvt_pk_bf16_f32 v86, v76, v77\n\t"
      "v_cvt_pk_bf16_f32 v87, v78, v79\n\t"
      "buffer_store_dwordx4 v[84:87], v97, s[60:63], s73 offen offset:1024 sc1\n\t"
      "s_cmp_eq_u32 s71, 2\n\t"
      "s_cbranch_scc1 GKV3%=\n\t"
      "s_cmp_lg_u32 s71, 0\n\t"
      "s_cbranch_scc1 GKV1%=\n\t"
      "s_cmp_lg_u64 s[36:37], 0\n\t"
      "s_cbranch_scc1 GL2%=\n\t"
      "GL0%=:\n\t"
      "buffer_load_dword v128, v106, s[60:63], s79 offen sc1\n\t"
      "buffer_load_dword v129, v106, s[60:63], s79 offen offset:128 sc1\n\t"
      "s_sub_u32 s81, s81, 1\n\t"
      "s_waitcnt vmcnt(0)\n\t"
      "v_bitop3_b32 v128, v128, v129, %3 bitop3:0x7e\n\t"
      "v_cmp_ne_u32 vcc, 0, v128\n\t"
      "s_cmp_eq_u32 s81, 0\n\t"
      "s_cbranch_scc1 GL1%=\n\t"
      "s_cbranch_vccnz GL0%=\n\t"
      "GL1%=:\n\t"
      "v_readlane_b32 s72, v101, 1\n\t"
      "s_add_u32 s76, s85, 0\n\t"
      "s_add_u32 m0, s76, 0\n\t"
      "s_nop 2\n\t"
      "buffer_load_dwordx4 v97, s[60:63], s72 offen sc1 lds\n\t"
      "buffer_load_dwordx4 v97, s[60:63], s72 offen offset:1024 sc1 lds\n\t"
      "buffer_load_dwordx4 v97, s[60:63], s72 offen offset:2048 sc1 lds\n\t"
      "buffer_load_dwordx4 v97, s[60:63], s72 offen offset:3072 sc1 lds\n\t"
      "s_add_u32 m0, s76, 4096\n\t"
      "s_add_u32 s72, s72, 4096\n\t"
      "buffer_load_dwordx4 v97, s[60:63], s72 offen sc1 lds\n\t"
      "buffer_load_dwordx4 v97, s[60:63], s72 offen offset:1024 sc1 lds\n\t"
      "buffer_load_dwordx4 v97, s[60:63], s72 offen offset:2048 sc1 lds\n\t"
      "buffer_load_dwordx4 v97, s[60:63], s72 offen offset:3072 sc1 lds\n\t"
      "s_waitcnt vmcnt(0)\n\t"
      "GL2%=:\n\t"
      "s_mov_b32 s77, s73\n\t"
      "v_readlane_b32 s73, v102, 1\n\t"
      "v_mov_b32 v98, v97\n\t"
      "s_mov_b64 s[88:89], -1\n\t"
      "s_mov_b64 s[38:39], s[90:91]\n\t"
      "s_mov_b64 s[36:37], -1\n\t"
      "s_mov_b32 s71, 1\n\t"
      "v_readlane_b32 s72, v101, 2\n\t"
      "s_add_u32 s76, s85, 32768\n\t"
      "s_waitcnt vmcnt(1)\n\t"
      "s_barrier\n\t"
      "s_branch GKV%=\n\t"
      "GKV1%=:\n\t"
      "v_readlane_b32 s75, v103, 1\n\t"
      "v_add_u32 v117, 0x10000, v117\n\t"
      "v_add_u32 v98, 131072, v97\n\t"
      "s_mov_b32 s68, 0\n\t"
      "s_add_u32 m0, s74, 94208\n\t"
      "s_mov_b32 s78, 1835008\n\t"
      "buffer_load_dwordx4 v116, s[64:67], s78 offen lds\n\t"
      "s_add_u32 m0, s74, 98304\n\t"
      "s_mov_b32 s78, 2097152\n\t"
      "buffer_load_dwordx4 v116, s[64:67], s78 offen lds\n\t"
      "s_add_u32 m0, s74, 102400\n\t"
      "s_mov_b32 s78, 2359296\n\t"
      "buffer_load_dwordx4 v116, s[64:67], s78 offen lds\n\t"
      "s_add_u32 m0, s74, 106496\n\t"
      "s_mov_b32 s78, 2621440\n\t"
      "buffer_load_dwordx4 v116, s[64:67], s78 offen lds\n\t"
      "s_add_u32 m0, s74, 110592\n\t"
      "s_mov_b32 s78, 2883584\n\t"
      "buffer_load_dwordx4 v116, s[64:67], s78 offen lds\n\t"
      "s_add_u32 m0, s74, 114688\n\t"
      "s_mov_b32 s78, 3145728\n\t"
      "buffer_load_dwordx4 v116, s[64:67], s78 offen lds\n\t"
      "s_add_u32 m0, s74, 118784\n\t"
      "s_mov_b32 s78, 3407872\n\t"
      "buffer_load_dwordx4 v116, s[64:67], s78 offen lds\n\t"
      "s_add_u32 m0, s74, 122880\n\t"
      "s_mov_b32 s78, 3670016\n\t"
      "buffer_load_dwordx4 v116, s[64:67], s78 offen lds\n\t"
      "s_add_u32 m0, s74, 126976\n\t"
      "s_mov_b32 s78, 3932160\n\t"
      "buffer_load_dwordx4 v116, s[64:67], s78 offen lds\n\t"
      "v_add_u32 v98, 0x8000, v97\n\t"
      "v_readlane_b32 s73, v102, 2\n\t"
      "s_mov_b64 s[88:89], 0\n\t"
      "s_mov_b64 s[36:37], 0\n\t"
      "s_mov_b32 s71, 2\n\t"
      "s_waitcnt vmcnt(9)\n\t"
      "s_barrier\n\t"
      "s_mov_b64 exec, s[90:91]\n\t"
      "buffer_store_dword v107, off, s[60:63], s75 sc1\n\t"
      "s_mov_b64 exec, -1\n\t"
      "s_branch GKV%=\n\t"
      "GKV3%=:\n\t"
      "v_readlane_b32 s75, v103, 2\n\t"
      "v_add_u32 v98, 131072, v97\n\t"
      "s_waitcnt vmcnt(0)\n\t"
      "s_barrier\n\t"
      "s_mov_b64 exec, s[90:91]\n\t"
      "s_nop 0\n\t"
      "buffer_store_dword v107, off, s[60:63], s75 sc1\n\t"
      "s_mov_b64 exec, -1\n\t"
      "s_waitcnt lgkmcnt(12)\n\t"
      "ds_read_b64_tr_b16 a[128:129], v117\n\t"
      "ds_read_b64_tr_b16 a[130:131], v117 offset:512\n\t"
      "s_waitcnt lgkmcnt(12)\n\t"
      "ds_read_b64_tr_b16 a[132:133], v117 offset:4096\n\t"
      "ds_read_b64_tr_b16 a[134:135], v117 offset:4608\n\t"
      "s_waitcnt lgkmcnt(12)\n\t"
      "ds_read_b64_tr_b16 a[136:137], v117 offset:8192\n\t"
      "ds_read_b64_tr_b16 a[138:139], v117 offset:8704\n\t"
      "s_waitcnt lgkmcnt(12)\n\t"
      "ds_read_b64_tr_b16 a[140:141], v117 offset:12288\n\t"
      "ds_read_b64_tr_b16 a[142:143], v117 offset:12800\n\t"
      "s_waitcnt lgkmcnt(12)\n\t"
      "ds_read_b64_tr_b16 a[144:145], v117 offset:16384\n\t"
      "ds_read_b64_tr_b16 a[146:147], v117 offset:16896\n\t"
      "s_waitcnt lgkmcnt(12)\n\t"
      "ds_read_b64_tr_b16 a[148:149], v117 offset:20480\n\t"
      "ds_read_b64_tr_b16 a[150:151], v117 offset:20992\n\t"
      "s_waitcnt lgkmcnt(12)\n\t"
      "ds_read_b64_tr_b16 a[152:153], v117 offset:24576\n\t"
      "ds_read_b64_tr_b16 a[154:155], v117 offset:25088\n\t"
      "s_waitcnt lgkmcnt(12)\n\t"
      "ds_read_b64_tr_b16 a[156:157], v117 offset:28672\n\t"
      "ds_read_b64_tr_b16 a[158:159], v117 offset:29184\n\t"
      "s_waitcnt lgkmcnt(12)\n\t"
      "ds_read_b64_tr_b16 a[160:161], v117 offset:32768\n\t"
      "ds_read_b64_tr_b16 a[162:163], v117 offset:33280\n\t"
      "s_waitcnt lgkmcnt(12)\n\t"
      "ds_read_b64_tr_b16 a[164:165], v117 offset:36864\n\t"
      "ds_read_b64_tr_b16 a[166:167], v117 offset:37376\n\t"
      "s_waitcnt lgkmcnt(12)\n\t"
      "ds_read_b64_tr_b16 a[168:169], v117 offset:40960\n\t"
      "ds_read_b64_tr_b16 a[170:171], v117 offset:41472\n\t"
      "s_waitcnt lgkmcnt(12)\n\t"
      "ds_read_b64_tr_b16 a[172:173], v117 offset:45056\n\t"
      "ds_read_b64_tr_b16 a[174:175], v117 offset:45568\n\t"
      "s_waitcnt lgkmcnt(12)\n\t"
      "ds_read_b64_tr_b16 a[176:177], v117 offset:49152\n\t"
      "ds_read_b64_tr_b16 a[178:179], v117 offset:49664\n\t"
      "s_waitcnt lgkmcnt(12)\n\t"
      "ds_read_b64_tr_b16 a[180:181], v117 offset:53248\n\t"
      "ds_read_b64_tr_b16 a[182:183], v117 offset:53760\n\t"
      "s_waitcnt lgkmcnt(12)\n\t"
      "ds_read_b64_tr_b16 a[184:185], v117 offset:57344\n\t"
      "ds_read_b64_tr_b16 a[186:187], v117 offset:57856\n\t"
      "s_waitcnt lgkmcnt(12)\n\t"
      "ds_read_b64_tr_b16 a[188:189], v117 offset:61440\n\t"
      "ds_read_b64_tr_b16 a[190:191], v117 offset:61952\n\t"
      "s_waitcnt lgkmcnt(4)\n\t"
      "GQ%=:\n\t"
      "ds_read_b128 v[0:3], v98\n\t"
      "ds_read_b128 v[4:7], v98 offset:1024\n\t"
      "ds_read_b128 v[8:11], v98 offset:2048\n\t"
      "ds_read_b128 v[12:15], v98 offset:3072\n\t"
      "ds_read_b128 v[16:19], v98 offset:4096\n\t"
      "ds_read_b128 v[20:23], v98 offset:5120\n\t"
      "ds_read_b128 v[24:27], v98 offset:6144\n\t"
      "ds_read_b128 v[28:31], v98 offset:7168\n\t"
      "ds_read_b128 v[32:35], v98 offset:8192\n\t"
      "ds_read_b128 v[36:39], v98 offset:9216\n\t"
      "s_waitcnt lgkmcnt(9)\n\t"
      "v_mfma_f32_16x16x32_bf16 v[88:91], a[128:131], v[0:3], 0\n\t"
      "ds_read_b128 v[40:43], v98 offset:10240\n\t"
      "s_waitcnt lgkmcnt(9)\n\t"
      "v_mfma_f32_16x16x32_bf16 v[88:91], a[132:135], v[4:7], v[88:91]\n\t"
      "ds_read_b128 v[44:47], v98 offset:11264\n\t"
      "s_waitcnt lgkmcnt(9)\n\t"
      "v_mfma_f32_16x16x32_bf16 v[88:91], a[136:139], v[8:11], v[88:91]\n\t"
      "ds_read_b128 v[48:51], v98 offset:12288\n\t"
      "s_waitcnt lgkmcnt(9)\n\t"
      "v_mfma_f32_16x16x32_bf16 v[88:91], a[140:143], v[12:15], v[88:91]\n\t"
      "ds_read_b128 v[52:55], v98 offset:13312\n\t"
      "s_waitcnt lgkmcnt(9)\n\t"
      "v_mfma_f32_16x16x32_bf16 v[88:91], a[144:147], v[16:19], v[88:91]\n\t"
      "ds_read_b128 v[56:59], v98 offset:14336\n\t"
      "s_waitcnt lgkmcnt(9)\n\t"
      "v_mfma_f32_16x16x32_bf16 v[88:91], a[148:151], v[20:23], v[88:91]\n\t"
      "ds_read_b128 v[60:63], v98 offset:15360\n\t"
      "s_waitcnt lgkmcnt(9)\n\t"
      "v_mfma_f32_16x16x32_bf16 v[88:91], a[152:155], v[24:27], v[88:91]\n\t"
      "ds_read_b128 v[0:3], v98 offset:16384\n\t"
      "s_waitcnt lgkmcnt(9)\n\t"
      "v_mfma_f32_16x16x32_bf16 v[88:91], a[156:159], v[28:31], v[88:91]\n\t"
      "ds_read_b128 v[4:7], v98 offset:17408\n\t"
      "s_waitcnt lgkmcnt(9)\n\t"
      "v_mfma_f32_16x16x32_bf16 v[88:91], a[160:163], v[32:35], v[88:91]\n\t"
      "ds_read_b128 v[8:11], v98 offset:18432\n\t"
      "s_waitcnt lgkmcnt(9)\n\t"
      "v_mfma_f32_16x16x32_bf16 v[88:91], a[164:167], v[36:39], v[88:91]\n\t"
      "ds_read_b128 v[12:15], v98 offset:19456\n\t"
      "s_waitcnt lgkmcnt(9)\n\t"
      "v_mfma_f32_16x16x32_bf16 v[88:91], a[168:171], v[40:43], v[88:91]\n\t"
      "ds_read_b128 v[16:19], v98 offset:20480\n\t"
      "s_waitcnt lgkmcnt(9)\n\t"
      "v_mfma_f32_16x16x32_bf16 v[88:91], a[172:175], v[44:47], v[88:91]\n\t"
      "ds_read_b128 v[20:23], v98 offset:21504\n\t"
      "s_waitcnt lgkmcnt(9)\n\t"
      "v_mfma_f32_16x16x32_bf16 v[88:91], a[176:179], v[48:51], v[88:91]\n\t"
      "ds_read_b128 v[24:27], v98 offset:22528\n\t"
      "s_waitcnt lgkmcnt(9)\n\t"
      "v_mfma_f32_16x16x32_bf16 v[88:91], a[180:183], v[52:55], v[88:91]\n\t"
      "ds_read_b128 v[28:31], v98 offset:23552\n\t"
      "s_waitcnt lgkmcnt(9)\n\t"
      "v_mfma_f32_16x16x32_bf16 v[88:91], a[184:187], v[56:59], v[88:91]\n\t"
      "ds_read_b128 v[32:35], v98 offset:24576\n\t"
      "s_waitcnt lgkmcnt(9)\n\t"
      "v_mfma_f32_16x16x32_bf16 v[88:91], a[188:191], v[60:63], v[88:91]\n\t"
      "ds_read_b128 v[36:39], v98 offset:25600\n\t"
      "s_waitcnt lgkmcnt(9)\n\t"
      "v_mfma_f32_16x16x32_bf16 v[92:95], a[128:131], v[0:3], 0\n\t"
      "ds_read_b128 v[40:43], v98 offset:26624\n\t"
      "s_waitcnt lgkmcnt(9)\n\t"
      "v_mfma_f32_16x16x32_bf16 v[92:95], a[132:135], v[4:7], v[92:95]\n\t"
      "ds_read_b128 v[44:47], v98 offset:27648\n\t"
      "s_waitcnt lgkmcnt(9)\n\t"
      "v_mfma_f32_16x16x32_bf16 v[92:95], a[136:139], v[8:11], v[92:95]\n\t"
      "ds_read_b128 v[48:51], v98 offset:28672\n\t"
      "s_waitcnt lgkmcnt(9)\n\t"
      "v_mfma_f32_16x16x32_bf16 v[92:95], a[140:143], v[12:15], v[92:95]\n\t"
      "ds_read_b128 v[52:55], v98 offset:29696\n\t"
      "s_waitcnt lgkmcnt(9)\n\t"
      "v_mfma_f32_16x16x32_bf16 v[92:95], a[144:147], v[16:19], v[92:95]\n\t"
      "ds_read_b128 v[56:59], v98 offset:30720\n\t"
      "s_waitcnt lgkmcnt(9)\n\t"
      "v_mfma_f32_16x16x32_bf16 v[92:95], a[148:151], v[20:23], v[92:95]\n\t"
      "ds_read_b128 v[60:63], v98 offset:31744\n\t"
      "s_waitcnt lgkmcnt(9)\n\t"
      "v_mfma_f32_16x16x32_bf16 v[92:95], a[152:155], v[24:27], v[92:95]\n\t"
      "s_waitcnt lgkmcnt(8)\n\t"
      "v_mfma_f32_16x16x32_bf16 v[92:95], a[156:159], v[28:31], v[92:95]\n\t"
      "s_waitcnt lgkmcnt(7)\n\t"
      "v_mfma_f32_16x16x32_bf16 v[92:95], a[160:163], v[32:35], v[92:95]\n\t"
      "s_waitcnt lgkmcnt(6)\n\t"
      "v_mfma_f32_16x16x32_bf16 v[92:95], a[164:167], v[36:39], v[92:95]\n\t"
      "s_waitcnt lgkmcnt(5)\n\t"
      "v_mfma_f32_16x16x32_bf16 v[92:95], a[168:171], v[40:43], v[92:95]\n\t"
      "s_waitcnt lgkmcnt(4)\n\t"
      "v_mfma_f32_16x16x32_bf16 v[92:95], a[172:175], v[44:47], v[92:95]\n\t"
      "s_waitcnt lgkmcnt(3)\n\t"
      "v_mfma_f32_16x16x32_bf16 v[92:95], a[176:179], v[48:51], v[92:95]\n\t"
      "s_waitcnt lgkmcnt(2)\n\t"
      "v_mfma_f32_16x16x32_bf16 v[92:95], a[180:183], v[52:55], v[92:95]\n\t"
      "s_waitcnt lgkmcnt(1)\n\t"
      "v_mfma_f32_16x16x32_bf16 v[92:95], a[184:187], v[56:59], v[92:95]\n\t"
      "s_waitcnt lgkmcnt(0)\n\t"
      "v_mfma_f32_16x16x32_bf16 v[92:95], a[188:191], v[60:63], v[92:95]\n\t"
      "v_cvt_pk_bf16_f32 v112, v88, v89\n\t"
      "v_cvt_pk_bf16_f32 v113, v90, v91\n\t"
      "s_cmp_eq_u32 s68, 3\n\t"
      "s_waitcnt vmcnt(17)\n\t"
      "s_barrier\n\t"
      "s_cbranch_scc1 GQ1%=\n\t"
      "s_nop 3\n\t"
      "v_cvt_pk_bf16_f32 v114, v92, v93\n\t"
      "v_cvt_pk_bf16_f32 v115, v94, v95\n\t"
      "s_nop 0\n\t"
      "v_permlane16_swap_b32 v112, v114\n\t"
      "v_permlane16_swap_b32 v113, v115\n\t"
      "buffer_store_dwordx4 v[112:115], v111, s[60:63], s77 offen sc1\n\t"
      "s_cmp_lg_u32 s68, 0\n\t"
      "s_cbranch_scc1 GQ2%=\n\t"
      "v_readlane_b32 s72, v101, 3\n\t"
      "s_add_u32 s76, s85, 65536\n\t"
      "s_add_u32 m0, s76, 0\n\t"
      "s_nop 2\n\t"
      "buffer_load_dwordx4 v97, s[60:63], s72 offen sc1 lds\n\t"
      "buffer_load_dwordx4 v97, s[60:63], s72 offen offset:1024 sc1 lds\n\t"
      "buffer_load_dwordx4 v97, s[60:63], s72 offen offset:2048 sc1 lds\n\t"
      "buffer_load_dwordx4 v97, s[60:63], s72 offen offset:3072 sc1 lds\n\t"
      "s_add_u32 m0, s76, 4096\n\t"
      "s_add_u32 s72, s72, 4096\n\t"
      "buffer_load_dwordx4 v97, s[60:63], s72 offen sc1 lds\n\t"
      "buffer_load_dwordx4 v97, s[60:63], s72 offen offset:1024 sc1 lds\n\t"
      "buffer_load_dwordx4 v97, s[60:63], s72 offen offset:2048 sc1 lds\n\t"
      "buffer_load_dwordx4 v97, s[60:63], s72 offen offset:3072 sc1 lds\n\t"
      "v_readlane_b32 s72, v101, 4\n\t"
      "s_add_u32 s76, s85, 98304\n\t"
      "s_add_u32 m0, s76, 0\n\t"
      "s_nop 2\n\t"
      "buffer_load_dwordx4 v97, s[60:63], s72 offen sc1 lds\n\t"
      "buffer_load_dwordx4 v97, s[60:63], s72 offen offset:1024 sc1 lds\n\t"
      "buffer_load_dwordx4 v97, s[60:63], s72 offen offset:2048 sc1 lds\n\t"
      "buffer_load_dwordx4 v97, s[60:63], s72 offen offset:3072 sc1 lds\n\t"
      "s_add_u32 m0, s76, 4096\n\t"
      "s_add_u32 s72, s72, 4096\n\t"
      "buffer_load_dwordx4 v97, s[60:63], s72 offen sc1 lds\n\t"
      "buffer_load_dwordx4 v97, s[60:63], s72 offen offset:1024 sc1 lds\n\t"
      "buffer_load_dwordx4 v97, s[60:63], s72 offen offset:2048 sc1 lds\n\t"
      "buffer_load_dwordx4 v97, s[60:63], s72 offen offset:3072 sc1 lds\n\t"
      "v_mov_b32 v98, v97\n\t"
      "v_readlane_b32 s77, v102, 1\n\t"
      "s_mov_b32 s68, 1\n\t"
      "s_branch GQ%=\n\t"
      "GQ2%=:\n\t"
      "v_readlane_b32 s72, v101, 5\n\t"
      "s_add_u32 s76, s85, 0\n\t"
      "s_add_u32 m0, s76, 0\n\t"
      "s_nop 2\n\t"
      "buffer_load_dwordx4 v97, s[60:63], s72 offen sc1 lds\n\t"
      "buffer_load_dwordx4 v97, s[60:63], s72 offen offset:1024 sc1 lds\n\t"
      "buffer_load_dwordx4 v97, s[60:63], s72 offen offset:2048 sc1 lds\n\t"
      "buffer_load_dwordx4 v97, s[60:63], s72 offen offset:3072 sc1 lds\n\t"
      "s_add_u32 m0, s76, 4096\n\t"
      "s_add_u32 s72, s72, 4096\n\t"
      "buffer_load_dwordx4 v97, s[60:63], s72 offen sc1 lds\n\t"
      "buffer_load_dwordx4 v97, s[60:63], s72 offen offset:1024 sc1 lds\n\t"
      "buffer_load_dwordx4 v97, s[60:63], s72 offen offset:2048 sc1 lds\n\t"
      "buffer_load_dwordx4 v97, s[60:63], s72 offen offset:3072 sc1 lds\n\t"
      "v_add_u32 v98, 0x8000, v97\n\t"
      "s_mov_b32 s68, 3\n\t"
      "s_branch GQ%=\n\t"
      "GQ1%=:\n\t"
      "s_mov_b32 s69, 6\n\t"
      "s_mov_b32 s70, 63\n\t"
      "s_waitcnt vmcnt(9)\n\t"
      "s_mov_b32 s77, 0x10000\n\t"
      "s_mov_b32 s71, 5\n\t"
      "s_getpc_b64 s[88:89]\n\t"
      "GPC%=:\n\t"
      "s_add_u32 s88, s88, GJOBS%=-GPC%=\n\t"
      "s_addc_u32 s89, s89, 0\n\t"
      "v_add_u32 v98, s77, v97\n\t"
      "s_add_u32 s76, s85, 0x8000\n\t"
      "ds_read_b128 v[0:3], v98\n\t"
      "ds_read_b128 v[4:7], v98 offset:1024\n\t"
      "ds_read_b128 v[8:11], v98 offset:2048\n\t"
      "ds_read_b128 v[12:15], v98 offset:3072\n\t"
      "ds_read_b128 v[16:19], v98 offset:4096\n\t"
      "ds_read_b128 v[20:23], v98 offset:5120\n\t"
      "ds_read_b128 v[24:27], v98 offset:6144\n\t"
      "ds_read_b128 v[28:31], v98 offset:7168\n\t"
      "ds_read_b128 v[32:35], v98 offset:8192\n\t"
      "ds_read_b128 v[36:39], v98 offset:9216\n\t"
      ".p2align 6\n\t"
      "GU%=:\n\t"
      "s_cmp_eq_u32 s68, s71\n\t"
      "s_cbranch_scc1 GCALL%=\n\t"
      "GBACK%=:\n\t"
      "s_waitcnt lgkmcnt(9)\n\t"
      "v_mfma_f32_16x16x32_bf16 v[64:67], a[0:3], v[0:3], 0\n\t"
      "ds_read_b128 v[40:43], v98 offset:10240\n\t"
      "v_mfma_f32_16x16x32_bf16 v[68:71], a[4:7], v[0:3], 0\n\t"
      "v_readlane_b32 s72, v101, s69\n\t"
      "v_mfma_f32_16x16x32_bf16 v[88:91], a[128:131], v[0:3], 0\n\t"
      "s_waitcnt lgkmcnt(9)\n\t"
      "v_mfma_f32_16x16x32_bf16 v[64:67], a[8:11], v[4:7], v[64:67]\n\t"
      "ds_read_b128 v[44:47], v98 offset:11264\n\t"
      "v_mfma_f32_16x16x32_bf16 v[68:71], a[12:15], v[4:7], v[68:71]\n\t"
      "v_readlane_b32 s75, v103, s70\n\t"
      "v_mfma_f32_16x16x32_bf16 v[88:91], a[132:135], v[4:7], v[88:91]\n\t"
      "s_waitcnt lgkmcnt(9)\n\t"
      "v_mfma_f32_16x16x32_bf16 v[64:67], a[16:19], v[8:11], v[64:67]\n\t"
      "ds_read_b128 v[48:51], v98 offset:12288\n\t"
      "v_mfma_f32_16x16x32_bf16 v[68:71], a[20:23], v[8:11], v[68:71]\n\t"
      "v_cvt_pk_bf16_f32 v84, v72, v73\n\t"
      "v_cvt_pk_bf16_f32 v85, v74, v75\n\t"
      "v_mfma_f32_16x16x32_bf16 v[88:91], a[136:139], v[8:11], v[88:91]\n\t"
      "v_cvt_pk_bf16_f32 v114, v92, v93\n\t"
      "v_cvt_pk_bf16_f32 v115, v94, v95\n\t"
      "s_waitcnt lgkmcnt(9)\n\t"
      "v_mfma_f32_16x16x32_bf16 v[64:67], a[24:27], v[12:15], v[64:67]\n\t"
      "ds_read_b128 v[52:55], v98 offset:13312\n\t"
      "v_mfma_f32_16x16x32_bf16 v[68:71], a[28:31], v[12:15], v[68:71]\n\t"
      "v_cvt_pk_bf16_f32 v86, v76, v77\n\t"
      "v_cvt_pk_bf16_f32 v87, v78, v79\n\t"
      "v_mfma_f32_16x16x32_bf16 v[88:91], a[140:143], v[12:15], v[88:91]\n\t"
      "v_permlane16_swap_b32 v112, v114\n\t"
      "v_permlane16_swap_b32 v113, v115\n\t"
      "s_waitcnt lgkmcnt(9)\n\t"
      "v_mfma_f32_16x16x32_bf16 v[64:67], a[32:35], v[16:19], v[64:67]\n\t"
      "ds_read_b128 v[56:59], v98 offset:14336\n\t"
      "v_mfma_f32_16x16x32_bf16 v[68:71], a[36:39], v[16:19], v[68:71]\n\t"
      "buffer_store_dwordx4 v[84:87], v97, s[60:63], s73 offen offset:1024 sc1\n\t"
      "buffer_store_dwordx4 v[112:115], v111, s[60:63], s73 offen sc1\n\t"
      "v_mfma_f32_16x16x32_bf16 v[88:91], a[144:147], v[16:19], v[88:91]\n\t"
      "s_add_u32 m0, s76, 0\n\t"
      "buffer_load_dwordx4 v97, s[60:63], s72 offen sc1 lds\n\t"
      "s_waitcnt lgkmcnt(9)\n\t"
      "v_mfma_f32_16x16x32_bf16 v[64:67], a[40:43], v[20:23], v[64:67]\n\t"
      "ds_read_b128 v[60:63], v98 offset:15360\n\t"
      "v_mfma_f32_16x16x32_bf16 v[68:71], a[44:47], v[20:23], v[68:71]\n\t"
      "v_mfma_f32_16x16x32_bf16 v[88:91], a[148:151], v[20:23], v[88:91]\n\t"
      "s_waitcnt lgkmcnt(9)\n\t"
      "v_mfma_f32_16x16x32_bf16 v[64:67], a[48:51], v[24:27], v[64:67]\n\t"
      "ds_read_b128 v[0:3], v98 offset:16384\n\t"
      "v_mfma_f32_16x16x32_bf16 v[68:71], a[52:55], v[24:27], v[68:71]\n\t"
      "v_mfma_f32_16x16x32_bf16 v[88:91], a[152:155], v[24:27], v[88:91]\n\t"
      "s_waitcnt lgkmcnt(9)\n\t"
      "v_mfma_f32_16x16x32_bf16 v[64:67], a[56:59], v[28:31], v[64:67]\n\t"
      "ds_read_b128 v[4:7], v98 offset:17408\n\t"
      "v_mfma_f32_16x16x32_bf16 v[68:71], a[60:63], v[28:31], v[68:71]\n\t"
      "buffer_load_dwordx4 v97, s[60:63], s72 offen offset:1024 sc1 lds\n\t"
      "v_mfma_f32_16x16x32_bf16 v[88:91], a[156:159], v[28:31], v[88:91]\n\t"
      "s_waitcnt lgkmcnt(9)\n\t"
      "v_mfma_f32_16x16x32_bf16 v[64:67], a[64:67], v[32:35], v[64:67]\n\t"
      "ds_read_b128 v[8:11], v98 offset:18432\n\t"
      "v_mfma_f32_16x16x32_bf16 v[68:71], a[68:71], v[32:35], v[68:71]\n\t"
      "v_mfma_f32_16x16x32_bf16 v[88:91], a[160:163], v[32:35], v[88:91]\n\t"
      "s_waitcnt lgkmcnt(9)\n\t"
      "v_mfma_f32_16x16x32_bf16 v[64:67], a[72:75], v[36:39], v[64:67]\n\t"
      "ds_read_b128 v[12:15], v98 offset:19456\n\t"
      "v_mfma_f32_16x16x32_bf16 v[68:71], a[76:79], v[36:39], v[68:71]\n\t"
      "v_readlane_b32 s73, v102, s68\n\t"
      "v_mfma_f32_16x16x32_bf16 v[88:91], a[164:167], v[36:39], v[88:91]\n\t"
      "s_waitcnt lgkmcnt(9)\n\t"
      "v_mfma_f32_16x16x32_bf16 v[64:67], a[80:83], v[40:43], v[64:67]\n\t"
      "ds_read_b128 v[16:19], v98 offset:20480\n\t"
      "v_mfma_f32_16x16x32_bf16 v[68:71], a[84:87], v[40:43], v[68:71]\n\t"
      "buffer_load_dwordx4 v97, s[60:63], s72 offen offset:2048 sc1 lds\n\t"
      "v_mfma_f32_16x16x32_bf16 v[88:91], a[168:171], v[40:43], v[88:91]\n\t"
      "s_waitcnt lgkmcnt(9)\n\t"
      "v_mfma_f32_16x16x32_bf16 v[64:67], a[88:91], v[44:47], v[64:67]\n\t"
      "ds_read_b128 v[20:23], v98 offset:21504\n\t"
      "v_mfma_f32_16x16x32_bf16 v[68:71], a[92:95], v[44:47], v[68:71]\n\t"
      "v_mfma_f32_16x16x32_bf16 v[88:91], a[172:175], v[44:47], v[88:91]\n\t"
      "s_waitcnt lgkmcnt(9)\n\t"
      "v_mfma_f32_16x16x32_bf16 v[64:67], a[96:99], v[48:51], v[64:67]\n\t"
      "ds_read_b128 v[24:27], v98 offset:22528\n\t"
      "v_mfma_f32_16x16x32_bf16 v[68:71], a[100:103], v[48:51], v[68:71]\n\t"
      "v_mfma_f32_16x16x32_bf16 v[88:91], a[176:179], v[48:51], v[88:91]\n\t"
      "s_waitcnt lgkmcnt(9)\n\t"
      "v_mfma_f32_16x16x32_bf16 v[64:67], a[104:107], v[52:55], v[64:67]\n\t"
      "ds_read_b128 v[28:31], v98 offset:23552\n\t"
      "v_mfma_f32_16x16x32_bf16 v[68:71], a[108:111], v[52:55], v[68:71]\n\t"
      "buffer_load_dwordx4 v97, s[60:63], s72 offen offset:3072 sc1 lds\n\t"
      "v_mfma_f32_16x16x32_bf16 v[88:91], a[180:183], v[52:55], v[88:91]\n\t"
      "s_waitcnt lgkmcnt(9)\n\t"
      "v_mfma_f32_16x16x32_bf16 v[64:67], a[112:115], v[56:59], v[64:67]\n\t"
      "ds_read_b128 v[32:35], v98 offset:24576\n\t"
      "v_mfma_f32_16x16x32_bf16 v[68:71], a[116:119], v[56:59], v[68:71]\n\t"
      "v_mfma_f32_16x16x32_bf16 v[88:91], a[184:187], v[56:59], v[88:91]\n\t"
      "s_waitcnt lgkmcnt(9)\n\t"
      "v_mfma_f32_16x16x32_bf16 v[64:67], a[120:123], v[60:63], v[64:67]\n\t"
      "ds_read_b128 v[36:39], v98 offset:25600\n\t"
      "v_mfma_f32_16x16x32_bf16 v[68:71], a[124:127], v[60:63], v[68:71]\n\t"
      "s_add_u32 s68, s68, 1\n\t"
      "v_mfma_f32_16x16x32_bf16 v[88:91], a[188:191], v[60:63], v[88:91]\n\t"
      "s_waitcnt lgkmcnt(9)\n\t"
      "v_mfma_f32_16x16x32_bf16 v[72:75], a[0:3], v[0:3], 0\n\t"
      "ds_read_b128 v[40:43], v98 offset:26624\n\t"
      "v_mfma_f32_16x16x32_bf16 v[76:79], a[4:7], v[0:3], 0\n\t"
      "s_add_u32 m0, s76, 4096\n\t"
      "s_add_u32 s72, s72, 4096\n\t"
      "buffer_load_dwordx4 v97, s[60:63], s72 offen sc1 lds\n\t"
      "v_mfma_f32_16x16x32_bf16 v[92:95], a[128:131], v[0:3], 0\n\t"
      "s_add_u32 s69, s69, 1\n\t"
      "s_add_u32 s70, s70, 1\n\t"
      "s_waitcnt lgkmcnt(9)\n\t"
      "v_mfma_f32_16x16x32_bf16 v[72:75], a[8:11], v[4:7], v[72:75]\n\t"
      "ds_read_b128 v[44:47], v98 offset:27648\n\t"
      "v_mfma_f32_16x16x32_bf16 v[76:79], a[12:15], v[4:7], v[76:79]\n\t"
      "s_cmp_lt_u32 s68, 7\n\t"
      "s_cselect_b32 s70, 1, 3\n\t"
      "v_mfma_f32_16x16x32_bf16 v[92:95], a[132:135], v[4:7], v[92:95]\n\t"
      "s_sub_u32 s70, s68, s70\n\t"
      "s_add_u32 s76, s77, s85\n\t"
      "s_add_u32 s77, s77, 0x8000\n\t"
      "s_and_b32 s77, s77, 0x1ffff\n\t"
      "s_waitcnt lgkmcnt(9)\n\t"
      "v_mfma_f32_16x16x32_bf16 v[72:75], a[16:19], v[8:11], v[72:75]\n\t"
      "ds_read_b128 v[48:51], v98 offset:28672\n\t"
      "v_mfma_f32_16x16x32_bf16 v[76:79], a[20:23], v[8:11], v[76:79]\n\t"
      "v_cvt_pk_bf16_f32 v80, v64, v65\n\t"
      "v_cvt_pk_bf16_f32 v81, v66, v67\n\t"
      "v_mfma_f32_16x16x32_bf16 v[92:95], a[136:139], v[8:11], v[92:95]\n\t"
      "v_cvt_pk_bf16_f32 v112, v88, v89\n\t"
      "s_waitcnt lgkmcnt(9)\n\t"
      "v_mfma_f32_16x16x32_bf16 v[72:75], a[24:27], v[12:15], v[72:75]\n\t"
      "ds_read_b128 v[52:55], v98 offset:29696\n\t"
      "v_mfma_f32_16x16x32_bf16 v[76:79], a[28:31], v[12:15], v[76:79]\n\t"
      "buffer_load_dwordx4 v97, s[60:63], s72 offen offset:1024 sc1 lds\n\t"
      "v_cvt_pk_bf16_f32 v82, v68, v69\n\t"
      "v_mfma_f32_16x16x32_bf16 v[92:95], a[140:143], v[12:15], v[92:95]\n\t"
      "v_cvt_pk_bf16_f32 v83, v70, v71\n\t"
      "v_cvt_pk_bf16_f32 v113, v90, v91\n\t"
      "s_waitcnt lgkmcnt(9)\n\t"
      "v_mfma_f32_16x16x32_bf16 v[72:75], a[32:35], v[16:19], v[72:75]\n\t"
      "ds_read_b128 v[56:59], v98 offset:30720\n\t"
      "v_mfma_f32_16x16x32_bf16 v[76:79], a[36:39], v[16:19], v[76:79]\n\t"
      "buffer_store_dwordx4 v[80:83], v97, s[60:63], s73 offen sc1\n\t"
      "v_mfma_f32_16x16x32_bf16 v[92:95], a[144:147], v[16:19], v[92:95]\n\t"
      "s_waitcnt lgkmcnt(9)\n\t"
      "v_mfma_f32_16x16x32_bf16 v[72:75], a[40:43], v[20:23], v[72:75]\n\t"
      "ds_read_b128 v[60:63], v98 offset:31744\n\t"
      "v_mfma_f32_16x16x32_bf16 v[76:79], a[44:47], v[20:23], v[76:79]\n\t"
      "v_mfma_f32_16x16x32_bf16 v[92:95], a[148:151], v[20:23], v[92:95]\n\t"
      "s_waitcnt lgkmcnt(9)\n\t"
      "v_mfma_f32_16x16x32_bf16 v[72:75], a[48:51], v[24:27], v[72:75]\n\t"
      "v_mfma_f32_16x16x32_bf16 v[76:79], a[52:55], v[24:27], v[76:79]\n\t"
      "buffer_load_dwordx4 v97, s[60:63], s72 offen offset:2048 sc1 lds\n\t"
      "v_add_u32 v98, s77, v97\n\t"
      "v_mfma_f32_16x16x32_bf16 v[92:95], a[152:155], v[24:27], v[92:95]\n\t"
      "s_cmp_lg_u32 s75, 0\n\t"
      "s_waitcnt lgkmcnt(8)\n\t"
      "v_mfma_f32_16x16x32_bf16 v[72:75], a[56:59], v[28:31], v[72:75]\n\t"
      "v_mfma_f32_16x16x32_bf16 v[76:79], a[60:63], v[28:31], v[76:79]\n\t"
      "s_cselect_b64 vcc, s[90:91], 0\n\t"
      "v_mfma_f32_16x16x32_bf16 v[92:95], a[156:159], v[28:31], v[92:95]\n\t"
      "s_waitcnt lgkmcnt(7)\n\t"
      "v_mfma_f32_16x16x32_bf16 v[72:75], a[64:67], v[32:35], v[72:75]\n\t"
      "v_mfma_f32_16x16x32_bf16 v[76:79], a[68:71], v[32:35], v[76:79]\n\t"
      "buffer_load_dwordx4 v97, s[60:63], s72 offen offset:3072 sc1 lds\n\t"
      "v_mfma_f32_16x16x32_bf16 v[92:95], a[160:163], v[32:35], v[92:95]\n\t"
      "s_cmp_gt_u32 s68, 7\n\t"
      "s_waitcnt lgkmcnt(6)\n\t"
      "v_mfma_f32_16x16x32_bf16 v[72:75], a[72:75], v[36:39], v[72:75]\n\t"
      "v_mfma_f32_16x16x32_bf16 v[76:79], a[76:79], v[36:39], v[76:79]\n\t"
      "v_mfma_f32_16x16x32_bf16 v[92:95], a[164:167], v[36:39], v[92:95]\n\t"
      "s_waitcnt vmcnt(22) lgkmcnt(0)\n\t"
      "s_cbranch_scc1 GEF%=\n\t"
      "s_waitcnt vmcnt(9)\n\t"
      "GEF%=:\n\t"
      "s_barrier\n\t"
      "v_mfma_f32_16x16x32_bf16 v[72:75], a[80:83], v[40:43], v[72:75]\n\t"
      "ds_read_b128 v[0:3], v98\n\t"
      "v_mfma_f32_16x16x32_bf16 v[76:79], a[84:87], v[40:43], v[76:79]\n\t"
      "ds_read_b128 v[4:7], v98 offset:1024\n\t"
      "v_mfma_f32_16x16x32_bf16 v[92:95], a[168:171], v[40:43], v[92:95]\n\t"
      "v_mfma_f32_16x16x32_bf16 v[72:75], a[88:91], v[44:47], v[72:75]\n\t"
      "ds_read_b128 v[8:11], v98 offset:2048\n\t"
      "v_mfma_f32_16x16x32_bf16 v[76:79], a[92:95], v[44:47], v[76:79]\n\t"
      "ds_read_b128 v[12:15], v98 offset:3072\n\t"
      "v_mfma_f32_16x16x32_bf16 v[92:95], a[172:175], v[44:47], v[92:95]\n\t"
      "v_mfma_f32_16x16x32_bf16 v[72:75], a[96:99], v[48:51], v[72:75]\n\t"
      "ds_read_b128 v[16:19], v98 offset:4096\n\t"
      "v_mfma_f32_16x16x32_bf16 v[76:79], a[100:103], v[48:51], v[76:79]\n\t"
      "ds_read_b128 v[20:23], v98 offset:5120\n\t"
      "v_mfma_f32_16x16x32_bf16 v[92:95], a[176:179], v[48:51], v[92:95]\n\t"
      "v_mfma_f32_16x16x32_bf16 v[72:75], a[104:107], v[52:55], v[72:75]\n\t"
      "ds_read_b128 v[24:27], v98 offset:6144\n\t"
      "v_mfma_f32_16x16x32_bf16 v[76:79], a[108:111], v[52:55], v[76:79]\n\t"
      "ds_read_b128 v[28:31], v98 offset:7168\n\t"
      "v_mfma_f32_16x16x32_bf16 v[92:95], a[180:183], v[52:55], v[92:95]\n\t"
      "v_mfma_f32_16x16x32_bf16 v[72:75], a[112:115], v[56:59], v[72:75]\n\t"
      "ds_read_b128 v[32:35], v98 offset:8192\n\t"
      "v_mfma_f32_16x16x32_bf16 v[76:79], a[116:119], v[56:59], v[76:79]\n\t"
      "ds_read_b128 v[36:39], v98 offset:9216\n\t"
      "v_mfma_f32_16x16x32_bf16 v[92:95], a[184:187], v[56:59], v[92:95]\n\t"
      "s_cbranch_vccnz GFLAG%=\n\t"
      "GFB%=:\n\t"
      "v_mfma_f32_16x16x32_bf16 v[72:75], a[120:123], v[60:63], v[72:75]\n\t"
      "v_mfma_f32_16x16x32_bf16 v[76:79], a[124:127], v[60:63], v[76:79]\n\t"
      "v_mfma_f32_16x16x32_bf16 v[92:95], a[188:191], v[60:63], v[92:95]\n\t"
      "s_branch GU%=\n\t"
      "GFLAG%=:\n\t"
      "s_mov_b64 exec, 1\n\t"
      "buffer_store_dword v107, off, s[60:63], s75 sc1\n\t"
      "s_mov_b64 exec, -1\n\t"
      "s_branch GFB%=\n\t"
      "GCALL%=:\n\t"
      "s_setpc_b64 s[88:89]\n\t"
      "GJOBS%=:\n\t"
      "s_nop 7\n\t"
      "v_readlane_b32 s75, v103, 3\n\t"
      "s_waitcnt vmcnt(9)\n\t"
      "s_barrier\n\t"
      "s_mov_b64 exec, s[90:91]\n\t"
      "s_nop 1\n\t"
      "buffer_store_dword v107, off, s[60:63], s75 sc1\n\t"
      "s_mov_b64 exec, -1\n\t"
      "s_mov_b32 s71, 6\n\t"
      "s_getpc_b64 s[88:89]\n\t"
      "s_add_u32 s88, s88, 12\n\t"
      "s_addc_u32 s89, s89, 0\n\t"
      "s_branch GBACK%=\n\t"
      "s_bitcmp0_b32 %0, 1\n\t"
      "s_cselect_b64 s[36:37], -1, 0\n\t"
      "s_cmp_lt_u32 s82, 28\n\t"
      "s_cselect_b64 s[38:39], -1, 0\n\t"
      "s_min_u32 s74, s82, 27\n\t"
      "v_lshrrev_b32 v104, 3, v96\n\t"
      "v_and_b32 v105, 7, v96\n\t"
      "s_mov_b32 s78, 3584\n\t"
      "v_mul_u32_u24 v104, s78, v104\n\t"
      "v_lshl_add_u32 v104, v105, 4, v104\n\t"
      "s_lshr_b32 s78, %0, 5\n\t"
      "s_lshl_b32 s78, s78, 2\n\t"
      "s_add_u32 s78, s78, %1\n\t"
      "s_mul_i32 s78, s78, 114688\n\t"
      "s_lshl_b32 s80, s74, 7\n\t"
      "s_add_u32 s80, s80, s78\n\t"
      "s_mov_b64 s[64:65], s[50:51]\n\t"
      "s_add_u32 s78, s80, 0\n\t"
      "buffer_load_dwordx4 v[128:131], v104, s[64:67], s78 offen \n\t"
      "s_add_u32 s78, s80, 28672\n\t"
      "buffer_load_dwordx4 v[132:135], v104, s[64:67], s78 offen \n\t"
      "s_add_u32 s78, s80, 57344\n\t"
      "buffer_load_dwordx4 v[136:139], v104, s[64:67], s78 offen \n\t"
      "s_add_u32 s71, s71, 1\n\t"
      "s_getpc_b64 s[88:89]\n\t"
      "s_add_u32 s88, s88, 12\n\t"
      "s_addc_u32 s89, s89, 0\n\t"
      "s_branch GBACK%=\n\t"
      "s_add_u32 s78, s80, 86016\n\t"
      "buffer_load_dwordx4 v[140:143], v104, s[64:67], s78 offen \n\t"
      "s_mov_b64 s[64:65], s[52:53]\n\t"
      "s_add_u32 s78, s80, 0\n\t"
      "buffer_load_dwordx4 v[144:147], v104, s[64:67], s78 offen \n\t"
      "s_add_u32 s78, s80, 28672\n\t"
      "buffer_load_dwordx4 v[148:151], v104, s[64:67], s78 offen \n\t"
      "s_add_u32 s71, s71, 1\n\t"
      "s_getpc_b64 s[88:89]\n\t"
      "s_add_u32 s88, s88, 12\n\t"
      "s_addc_u32 s89, s89, 0\n\t"
      "s_branch GBACK%=\n\t"
      "s_add_u32 s78, s80, 57344\n\t"
      "buffer_load_dwordx4 v[152:155], v104, s[64:67], s78 offen \n\t"
      "s_add_u32 s78, s80, 86016\n\t"
      "buffer_load_dwordx4 v[156:159], v104, s[64:67], s78 offen \n\t"
      "v_mov_b32 v120, -1\n\t"
      "v_mov_b32 v121, -1\n\t"
      "v_mov_b32 v122, -1\n\t"
      "v_mov_b32 v123, -1\n\t"
      "s_lshr_b32 s78, %0, 5\n\t"
      "s_lshl_b32 s78, s78, 2\n\t"
      "s_add_u32 s78, s78, %1\n\t"
      "s_lshr_b32 s79, s78, 1\n\t"
      "s_lshl_b32 s79, s79, 16\n\t"
      "s_lshl_b32 s80, s82, 11\n\t"
      "s_add_u32 s79, s79, s80\n\t"
      "s_and_b32 s78, s78, 1\n\t"
      "s_lshl_b32 s78, s78, 10\n\t"
      "s_add_u32 s79, s79, s78\n\t"
      "s_add_u32 s79, s79, 66813952\n\t"
      "buffer_store_dwordx4 v[120:123], v97, s[60:63], s79 offen sc1\n\t"
      "s_add_u32 s71, s71, 1\n\t"
      "s_getpc_b64 s[88:89]\n\t"
      "s_add_u32 s88, s88, 12\n\t"
      "s_addc_u32 s89, s89, 0\n\t"
      "s_branch GBACK%=\n\t"
      "s_sub_u32 s79, s79, 4194304\n\t"
      "buffer_store_dwordx4 v[120:123], v97, s[60:63], s79 offen sc1\n\t"
      "s_add_u32 s79, s79, 3670016\n\t"
      "buffer_store_dwordx4 v[120:123], v97, s[60:63], s79 offen sc1\n\t"
      "s_sub_u32 s79, s79, 4194304\n\t"
      "buffer_store_dwordx4 v[120:123], v97, s[60:63], s79 offen sc1\n\t"
      "s_add_u32 s71, s71, 1\n\t"
      "s_getpc_b64 s[88:89]\n\t"
      "s_add_u32 s88, s88, 12\n\t"
      "s_addc_u32 s89, s89, 0\n\t"
      "s_branch GBACK%=\n\t"
      "s_add_u32 s79, s79, 3670016\n\t"
      "buffer_store_dwordx4 v[120:123], v97, s[60:63], s79 offen sc1\n\t"
      "s_sub_u32 s79, s79, 4194304\n\t"
      "buffer_store_dwordx4 v[120:123], v97, s[60:63], s79 offen sc1\n\t"
      "s_mov_b64 s[64:65], s[54:55]\n\t"
      "v_lshrrev_b32 v104, 5, v96\n\t"
      "v_and_b32 v105, 31, v96\n\t"
      "v_lshlrev_b32 v104, 10, v104\n\t"
      "v_lshl_add_u32 v104, v105, 4, v104\n\t"
      "s_andn2_b32 s78, %0, 3\n\t"
      "s_add_u32 s78, s78, %1\n\t"
      "s_mul_i32 s78, s78, 14336\n\t"
      "s_and_b32 s79, %0, 1\n\t"
      "s_lshl_b32 s79, s79, 9\n\t"
      "s_add_u32 s78, s78, s79\n\t"
      "s_mov_b64 exec, s[36:37]\n\t"
      "buffer_load_dwordx4 v[160:163], v104, s[64:67], s78 offen \n\t"
      "s_mov_b64 exec, -1\n\t"
      "s_add_u32 s71, s71, 1\n\t"
      "s_getpc_b64 s[88:89]\n\t"
      "s_add_u32 s88, s88, 12\n\t"
      "s_addc_u32 s89, s89, 0\n\t"
      "s_branch GBACK%=\n\t"
      "s_mov_b64 exec, s[36:37]\n\t"
      "buffer_load_dwordx4 v[164:167], v104, s[64:67], s78 offen offset:2048 \n\t"
      "s_add_u32 s78, s78, 4096\n\t"
      "buffer_load_dwordx4 v[168:171], v104, s[64:67], s78 offen \n\t"
      "buffer_load_dwordx4 v[172:175], v104, s[64:67], s78 offen offset:2048 \n\t"
      "s_mov_b64 exec, -1\n\t"
      "s_add_u32 s71, s71, 1\n\t"
      "s_getpc_b64 s[88:89]\n\t"
      "s_add_u32 s88, s88, 12\n\t"
      "s_addc_u32 s89, s89, 0\n\t"
      "s_branch GBACK%=\n\t"
      "s_mov_b64 exec, s[36:37]\n\t"
      "s_add_u32 s78, s78, 4096\n\t"
      "buffer_load_dwordx4 v[176:179], v104, s[64:67], s78 offen \n\t"
      "buffer_load_dwordx4 v[180:183], v104, s[64:67], s78 offen offset:2048 \n\t"
      "s_add_u32 s78, s78, 4096\n\t"
      "buffer_load_dwordx4 v[184:187], v104, s[64:67], s78 offen \n\t"
      "s_mov_b64 exec, -1\n\t"
      "s_add_u32 s71, s71, 1\n\t"
      "s_getpc_b64 s[88:89]\n\t"
      "s_add_u32 s88, s88, 12\n\t"
      "s_addc_u32 s89, s89, 0\n\t"
      "s_branch GBACK%=\n\t"
      "s_mov_b64 exec, s[36:37]\n\t"
      "s_mov_b64 exec, -1\n\t"
      "v_and_b32 v106, 3, v96\n\t"
      "v_mul_u32_u24 v106, 24, v106\n\t"
      "v_lshlrev_b32 v104, 1, v100\n\t"
      "v_sub_u32 v106, v104, v106\n\t"
      "v_add_u32 v104, s76, v97\n\t"
      "v_add_u32 v105, s76, v106\n\t"
      "s_waitcnt vmcnt(30) lgkmcnt(0)\n\t"
      "ds_write_b128 v104, v[128:131]\n\t"
      "ds_write_b128 v104, v[132:135] offset:1024\n\t"
      "ds_write_b128 v104, v[136:139] offset:2048\n\t"
      "ds_write_b128 v104, v[140:143] offset:3072\n\t"
      "ds_write_b128 v104, v[144:147] offset:4096\n\t"
      "ds_write_b128 v104, v[148:151] offset:5120\n\t"
      "ds_write_b128 v104, v[152:155] offset:6144\n\t"
      "ds_write_b128 v104, v[156:159] offset:7168\n\t"
      "s_lshr_b32 s78, %0, 5\n\t"
      "s_lshl_b32 s78, s78, 2\n\t"
      "s_add_u32 s78, s78, %1\n\t"
      "s_lshl_b32 s78, s78, 10\n\t"
      "s_lshl_b32 s79, s74, 17\n\t"
      "s_add_u32 s78, s78, s79\n\t"
      "s_add_u32 s78, s78, 105086976\n\t"
      "s_waitcnt lgkmcnt(6)\n\t"
      "ds_read_b64_tr_b16 v[128:129], v105\n\t"
      "ds_read_b64_tr_b16 v[130:131], v105 offset:512\n\t"
      "ds_read_b64_tr_b16 v[132:133], v105 offset:4096\n\t"
      "ds_read_b64_tr_b16 v[134:135], v105 offset:4608\n\t"
      "ds_read_b64_tr_b16 v[136:137], v105 offset:32\n\t"
      "ds_read_b64_tr_b16 v[138:139], v105 offset:544\n\t"
      "ds_read_b64_tr_b16 v[140:141], v105 offset:4128\n\t"
      "ds_read_b64_tr_b16 v[142:143], v105 offset:4640\n\t"
      "s_waitcnt lgkmcnt(6)\n\t"
      "ds_read_b64_tr_b16 v[144:145], v105 offset:64\n\t"
      "ds_read_b64_tr_b16 v[146:147], v105 offset:576\n\t"
      "ds_read_b64_tr_b16 v[148:149], v105 offset:4160\n\t"
      "ds_read_b64_tr_b16 v[150:151], v105 offset:4672\n\t"
      "ds_read_b64_tr_b16 v[152:153], v105 offset:96\n\t"
      "ds_read_b64_tr_b16 v[154:155], v105 offset:608\n\t"
      "ds_read_b64_tr_b16 v[156:157], v105 offset:4192\n\t"
      "ds_read_b64_tr_b16 v[158:159], v105 offset:4704\n\t"
      "s_waitcnt lgkmcnt(0)\n\t"
      "s_mov_b64 exec, s[38:39]\n\t"
      "buffer_store_dwordx4 v[128:131], v97, s[60:63], s78 offen sc1\n\t"
      "s_add_u32 s78, s78, 16384\n\t"
      "buffer_store_dwordx4 v[132:135], v97, s[60:63], s78 offen sc1\n\t"
      "s_add_u32 s78, s78, 16384\n\t"
      "buffer_store_dwordx4 v[136:139], v97, s[60:63], s78 offen sc1\n\t"
      "s_mov_b64 exec, -1\n\t"
      "s_add_u32 s71, s71, 1\n\t"
      "s_getpc_b64 s[88:89]\n\t"
      "s_add_u32 s88, s88, 12\n\t"
      "s_addc_u32 s89, s89, 0\n\t"
      "s_branch GBACK%=\n\t"
      "s_mov_b64 exec, s[38:39]\n\t"
      "s_add_u32 s78, s78, 16384\n\t"
      "buffer_store_dwordx4 v[140:143], v97, s[60:63], s78 offen sc1\n\t"
      "s_add_u32 s78, s78, 16384\n\t"
      "buffer_store_dwordx4 v[144:147], v97, s[60:63], s78 offen sc1\n\t"
      "s_add_u32 s78, s78, 16384\n\t"
      "buffer_store_dwordx4 v[148:151], v97, s[60:63], s78 offen sc1\n\t"
      "s_mov_b64 exec, -1\n\t"
      "s_add_u32 s71, s71, 1\n\t"
      "s_getpc_b64 s[88:89]\n\t"
      "s_add_u32 s88, s88, 12\n\t"
      "s_addc_u32 s89, s89, 0\n\t"
      "s_branch GBACK%=\n\t"
      "s_mov_b64 exec, s[38:39]\n\t"
      "s_add_u32 s78, s78, 16384\n\t"
      "buffer_store_dwordx4 v[152:155], v97, s[60:63], s78 offen sc1\n\t"
      "s_add_u32 s78, s78, 16384\n\t"
      "buffer_store_dwordx4 v[156:159], v97, s[60:63], s78 offen sc1\n\t"
      "s_add_u32 s78, s78, 16384\n\t"
      "s_mov_b64 exec, -1\n\t"
      "v_lshlrev_b32 v104, 10, v96\n\t"
      "s_and_b32 s78, %0, 1\n\t"
      "s_lshl_b32 s78, s78, 5\n\t"
      "s_lshr_b32 s79, %0, 2\n\t"
      "s_add_u32 s78, s78, s79\n\t"
      "s_lshl_b32 s78, s78, 4\n\t"
      "s_lshl_b32 s79, %1, 16\n\t"
      "s_add_u32 s78, s78, s79\n\t"
      "s_lshl_b32 s79, %1, 10\n\t"
      "s_mov_b64 exec, s[36:37]\n\t"
      "s_add_u32 m0, s79, 131072\n\t"
      "s_nop 0\n\t"
      "buffer_load_dwordx4 v104, s[64:67], s78 offen lds\n\t"
      "s_mov_b64 exec, -1\n\t"
      "s_add_u32 s71, s71, 1\n\t"
      "s_getpc_b64 s[88:89]\n\t"
      "s_add_u32 s88, s88, 12\n\t"
      "s_addc_u32 s89, s89, 0\n\t"
      "s_branch GBACK%=\n\t"
      "s_mov_b64 exec, s[36:37]\n\t"
      "s_add_u32 s78, s78, 0x40000\n\t"
      "s_add_u32 m0, s79, 135168\n\t"
      "s_nop 0\n\t"
      "buffer_load_dwordx4 v104, s[64:67], s78 offen lds\n\t"
      "s_add_u32 s78, s78, 0x40000\n\t"
      "s_add_u32 m0, s79, 139264\n\t"
      "s_nop 0\n\t"
      "buffer_load_dwordx4 v104, s[64:67], s78 offen lds\n\t"
      "s_add_u32 s78, s78, 0x40000\n\t"
      "s_add_u32 m0, s79, 143360\n\t"
      "s_nop 0\n\t"
      "buffer_load_dwordx4 v104, s[64:67], s78 offen lds\n\t"
      "s_mov_b64 exec, -1\n\t"
      "s_add_u32 s71, s71, 1\n\t"
      "s_getpc_b64 s[88:89]\n\t"
      "s_add_u32 s88, s88, 12\n\t"
      "s_addc_u32 s89, s89, 0\n\t"
      "s_branch GBACK%=\n\t"
      "s_mov_b64 exec, s[36:37]\n\t"
      "s_add_u32 s78, s78, 0x40000\n\t"
      "s_add_u32 m0, s79, 147456\n\t"
      "s_nop 0\n\t"
      "buffer_load_dwordx4 v104, s[64:67], s78 offen lds\n\t"
      "s_add_u32 s78, s78, 0x40000\n\t"
      "s_add_u32 m0, s79, 151552\n\t"
      "s_nop 0\n\t"
      "buffer_load_dwordx4 v104, s[64:67], s78 offen lds\n\t"
      "s_add_u32 s78, s78, 0x40000\n\t"
      "s_add_u32 m0, s79, 155648\n\t"
      "s_nop 0\n\t"
      "buffer_load_dwordx4 v104, s[64:67], s78 offen lds\n\t"
      "s_mov_b64 exec, -1\n\t"
      "s_add_u32 s71, s71, 1\n\t"
      "s_getpc_b64 s[88:89]\n\t"
      "s_add_u32 s88, s88, 12\n\t"
      "s_addc_u32 s89, s89, 0\n\t"
      "s_branch GBACK%=\n\t"
      "s_mov_b64 exec, s[36:37]\n\t"
      "s_add_u32 s78, s78, 0x40000\n\t"
      "s_mov_b64 exec, -1\n\t"
      "s_mov_b64 s[64:65], s[46:47]\n\t"
      "v_lshrrev_b32 v99, 2, v96\n\t"
      "v_and_b32 v106, 3, v96\n\t"
      "v_lshlrev_b32 v99, 10, v99\n\t"
      "v_lshl_add_u32 v99, v106, 4, v99\n\t"
      "s_lshr_b32 s78, %0, 4\n\t"
      "s_lshl_b32 s78, s78, 19\n\t"
      "s_bfe_u32 s79, %0, 0x20002\n\t"
      "s_lshl_b32 s79, s79, 8\n\t"
      "s_add_u32 s78, s78, s79\n\t"
      "s_lshl_b32 s79, %1, 6\n\t"
      "s_add_u32 s78, s78, s79\n\t"
      "v_add_u32 v99, s78, v99\n\t"
      "s_mov_b32 s78, 262144\n\t"
      "buffer_load_dwordx4 v[192:195], v99, s[64:67], s78 offen \n\t"
      "s_mov_b32 s78, 278528\n\t"
      "buffer_load_dwordx4 v[196:199], v99, s[64:67], s78 offen \n\t"
      "s_mov_b32 s78, 294912\n\t"
      "buffer_load_dwordx4 v[200:203], v99, s[64:67], s78 offen \n\t"
      "s_add_u32 s71, s71, 1\n\t"
      "s_getpc_b64 s[88:89]\n\t"
      "s_add_u32 s88, s88, 12\n\t"
      "s_addc_u32 s89, s89, 0\n\t"
      "s_branch GBACK%=\n\t"
      "s_mov_b32 s78, 311296\n\t"
      "buffer_load_dwordx4 v[204:207], v99, s[64:67], s78 offen \n\t"
      "s_mov_b32 s78, 327680\n\t"
      "buffer_load_dwordx4 v[208:211], v99, s[64:67], s78 offen \n\t"
      "s_mov_b32 s78, 344064\n\t"
      "buffer_load_dwordx4 v[212:215], v99, s[64:67], s78 offen \n\t"
      "s_add_u32 s71, s71, 1\n\t"
      "s_getpc_b64 s[88:89]\n\t"
      "s_add_u32 s88, s88, 12\n\t"
      "s_addc_u32 s89, s89, 0\n\t"
      "s_branch GBACK%=\n\t"
      "s_mov_b32 s78, 360448\n\t"
      "buffer_load_dwordx4 v[216:219], v99, s[64:67], s78 offen \n\t"
      "s_mov_b32 s78, 376832\n\t"
      "buffer_load_dwordx4 v[220:223], v99, s[64:67], s78 offen \n\t"
      "s_mov_b32 s78, 393216\n\t"
      "buffer_load_dwordx4 v[224:227], v99, s[64:67], s78 offen \n\t"
      "s_add_u32 s71, s71, 1\n\t"
      "s_getpc_b64 s[88:89]\n\t"
      "s_add_u32 s88, s88, 12\n\t"
      "s_addc_u32 s89, s89, 0\n\t"
      "s_branch GBACK%=\n\t"
      "s_mov_b32 s78, 409600\n\t"
      "buffer_load_dwordx4 v[228:231], v99, s[64:67], s78 offen \n\t"
      "s_mov_b32 s78, 425984\n\t"
      "buffer_load_dwordx4 v[232:235], v99, s[64:67], s78 offen \n\t"
      "s_mov_b32 s78, 442368\n\t"
      "buffer_load_dwordx4 v[236:239], v99, s[64:67], s78 offen \n\t"
      "s_add_u32 s71, s71, 1\n\t"
      "s_getpc_b64 s[88:89]\n\t"
      "s_add_u32 s88, s88, 12\n\t"
      "s_addc_u32 s89, s89, 0\n\t"
      "s_branch GBACK%=\n\t"
      "s_mov_b32 s78, 458752\n\t"
      "buffer_load_dwordx4 v[240:243], v99, s[64:67], s78 offen \n\t"
      "s_mov_b32 s78, 475136\n\t"
      "buffer_load_dwordx4 v[244:247], v99, s[64:67], s78 offen \n\t"
      "s_mov_b32 s78, 491520\n\t"
      "buffer_load_dwordx4 v[248:251], v99, s[64:67], s78 offen \n\t"
      "s_add_u32 s71, s71, 1\n\t"
      "s_getpc_b64 s[88:89]\n\t"
      "s_add_u32 s88, s88, 12\n\t"
      "s_addc_u32 s89, s89, 0\n\t"
      "s_branch GBACK%=\n\t"
      "s_mov_b32 s78, 507904\n\t"
      "buffer_load_dwordx4 v[252:255], v99, s[64:67], s78 offen \n\t"
      "s_mov_b32 s78, 0\n\t"
      "buffer_load_dwordx4 v[128:131], v99, s[64:67], s78 offen \n\t"
      "s_mov_b32 s78, 16384\n\t"
      "buffer_load_dwordx4 v[132:135], v99, s[64:67], s78 offen \n\t"
      "s_add_u32 s71, s71, 1\n\t"
      "s_getpc_b64 s[88:89]\n\t"
      "s_add_u32 s88, s88, 12\n\t"
      "s_addc_u32 s89, s89, 0\n\t"
      "s_branch GBACK%=\n\t"
      "s_mov_b32 s78, 32768\n\t"
      "buffer_load_dwordx4 v[136:139], v99, s[64:67], s78 offen \n\t"
      "s_mov_b32 s78, 49152\n\t"
      "buffer_load_dwordx4 v[140:143], v99, s[64:67], s78 offen \n\t"
      "s_mov_b32 s78, 65536\n\t"
      "buffer_load_dwordx4 v[144:147], v99, s[64:67], s78 offen \n\t"
      "s_add_u32 s71, s71, 1\n\t"
      "s_getpc_b64 s[88:89]\n\t"
      "s_add_u32 s88, s88, 12\n\t"
      "s_addc_u32 s89, s89, 0\n\t"
      "s_branch GBACK%=\n\t"
      "s_mov_b32 s78, 81920\n\t"
      "buffer_load_dwordx4 v[148:151], v99, s[64:67], s78 offen \n\t"
      "s_mov_b32 s78, 98304\n\t"
      "buffer_load_dwordx4 v[152:155], v99, s[64:67], s78 offen \n\t"
      "s_mov_b32 s78, 114688\n\t"
      "buffer_load_dwordx4 v[156:159], v99, s[64:67], s78 offen \n\t"
      "s_add_u32 s71, s71, 1\n\t"
      "s_getpc_b64 s[88:89]\n\t"
      "s_add_u32 s88, s88, 12\n\t"
      "s_addc_u32 s89, s89, 0\n\t"
      "s_branch GBACK%=\n\t"
      "s_mov_b32 s78, 131072\n\t"
      "buffer_load_dwordx4 v[160:163], v99, s[64:67], s78 offen \n\t"
      "s_mov_b32 s78, 147456\n\t"
      "buffer_load_dwordx4 v[164:167], v99, s[64:67], s78 offen \n\t"
      "s_mov_b32 s78, 163840\n\t"
      "buffer_load_dwordx4 v[168:171], v99, s[64:67], s78 offen \n\t"
      "s_add_u32 s71, s71, 1\n\t"
      "s_getpc_b64 s[88:89]\n\t"
      "s_add_u32 s88, s88, 12\n\t"
      "s_addc_u32 s89, s89, 0\n\t"
      "s_branch GBACK%=\n\t"
      "s_mov_b32 s78, 180224\n\t"
      "buffer_load_dwordx4 v[172:175], v99, s[64:67], s78 offen \n\t"
      "s_mov_b32 s78, 196608\n\t"
      "buffer_load_dwordx4 v[176:179], v99, s[64:67], s78 offen \n\t"
      "s_mov_b32 s78, 212992\n\t"
      "buffer_load_dwordx4 v[180:183], v99, s[64:67], s78 offen \n\t"
      "s_add_u32 s71, s71, 1\n\t"
      "s_getpc_b64 s[88:89]\n\t"
      "s_add_u32 s88, s88, 12\n\t"
      "s_addc_u32 s89, s89, 0\n\t"
      "s_branch GBACK%=\n\t"
      "s_mov_b32 s78, 229376\n\t"
      "buffer_load_dwordx4 v[184:187], v99, s[64:67], s78 offen \n\t"
      "s_mov_b32 s78, 245760\n\t"
      "buffer_load_dwordx4 v[188:191], v99, s[64:67], s78 offen \n\t"
      "s_mov_b32 s71, 32\n\t"
      "s_getpc_b64 s[88:89]\n\t"
      "s_add_u32 s88, s88, 12\n\t"
      "s_addc_u32 s89, s89, 0\n\t"
      "s_branch GBACK%=\n\t"
      "s_nop 7\n\t"
      "v_add_u32 v104, s76, v97\n\t"
      "v_add_u32 v105, s76, v100\n\t"
      "s_sub_u32 s68, s68, 1\n\t"
      "s_waitcnt vmcnt(11) lgkmcnt(0)\n\t"
      "v_cvt_pk_bf16_f32 v84, v72, v73\n\t"
      "v_cvt_pk_bf16_f32 v85, v74, v75\n\t"
      "v_cvt_pk_bf16_f32 v86, v76, v77\n\t"
      "v_cvt_pk_bf16_f32 v87, v78, v79\n\t"
      "buffer_store_dwordx4 v[84:87], v97, s[60:63], s73 offen offset:1024 sc1\n\t"
      "v_cvt_pk_bf16_f32 v114, v92, v93\n\t"
      "v_cvt_pk_bf16_f32 v115, v94, v95\n\t"
      "s_nop 0\n\t"
      "v_permlane16_swap_b32 v112, v114\n\t"
      "v_permlane16_swap_b32 v113, v115\n\t"
      "buffer_store_dwordx4 v[112:115], v111, s[60:63], s73 offen sc1\n\t"
      "v_readlane_b32 s75, v103, s68\n\t"
      "s_waitcnt lgkmcnt(6)\n\t"
      "ds_write_b128 v104, v[128:131]\n\t"
      "ds_write_b128 v104, v[132:135] offset:1024\n\t"
      "ds_read_b64_tr_b16 a[0:1], v105\n\t"
      "ds_read_b64_tr_b16 a[2:3], v105 offset:256\n\t"
      "ds_read_b64_tr_b16 a[64:65], v105 offset:8\n\t"
      "ds_read_b64_tr_b16 a[66:67], v105 offset:264\n\t"
      "s_waitcnt lgkmcnt(6)\n\t"
      "ds_write_b128 v104, v[136:139] offset:2048\n\t"
      "ds_write_b128 v104, v[140:143] offset:3072\n\t"
      "ds_read_b64_tr_b16 a[4:5], v105 offset:2048\n\t"
      "ds_read_b64_tr_b16 a[6:7], v105 offset:2304\n\t"
      "ds_read_b64_tr_b16 a[68:69], v105 offset:2056\n\t"
      "ds_read_b64_tr_b16 a[70:71], v105 offset:2312\n\t"
      "s_waitcnt lgkmcnt(6)\n\t"
      "ds_write_b128 v104, v[144:147] offset:4096\n\t"
      "ds_write_b128 v104, v[148:151] offset:5120\n\t"
      "ds_read_b64_tr_b16 a[8:9], v105 offset:4096\n\t"
      "ds_read_b64_tr_b16 a[10:11], v105 offset:4352\n\t"
      "ds_read_b64_tr_b16 a[72:73], v105 offset:4104\n\t"
      "ds_read_b64_tr_b16 a[74:75], v105 offset:4360\n\t"
      "s_waitcnt lgkmcnt(6)\n\t"
      "ds_write_b128 v104, v[152:155] offset:6144\n\t"
      "ds_write_b128 v104, v[156:159] offset:7168\n\t"
      "ds_read_b64_tr_b16 a[12:13], v105 offset:6144\n\t"
      "ds_read_b64_tr_b16 a[14:15], v105 offset:6400\n\t"
      "ds_read_b64_tr_b16 a[76:77], v105 offset:6152\n\t"
      "ds_read_b64_tr_b16 a[78:79], v105 offset:6408\n\t"
      "s_waitcnt lgkmcnt(6)\n\t"
      "ds_write_b128 v104, v[160:163]\n\t"
      "ds_write_b128 v104, v[164:167] offset:1024\n\t"
      "ds_read_b64_tr_b16 a[16:17], v105\n\t"
      "ds_read_b64_tr_b16 a[18:19], v105 offset:256\n\t"
      "ds_read_b64_tr_b16 a[80:81], v105 offset:8\n\t"
      "ds_read_b64_tr_b16 a[82:83], v105 offset:264\n\t"
      "s_waitcnt lgkmcnt(6)\n\t"
      "ds_write_b128 v104, v[168:171] offset:2048\n\t"
      "ds_write_b128 v104, v[172:175] offset:3072\n\t"
      "ds_read_b64_tr_b16 a[20:21], v105 offset:2048\n\t"
      "ds_read_b64_tr_b16 a[22:23], v105 offset:2304\n\t"
      "ds_read_b64_tr_b16 a[84:85], v105 offset:2056\n\t"
      "ds_read_b64_tr_b16 a[86:87], v105 offset:2312\n\t"
      "s_waitcnt lgkmcnt(6)\n\t"
      "ds_write_b128 v104, v[176:179] offset:4096\n\t"
      "ds_write_b128 v104, v[180:183] offset:5120\n\t"
      "ds_read_b64_tr_b16 a[24:25], v105 offset:4096\n\t"
      "ds_read_b64_tr_b16 a[26:27], v105 offset:4352\n\t"
      "ds_read_b64_tr_b16 a[88:89], v105 offset:4104\n\t"
      "ds_read_b64_tr_b16 a[90:91], v105 offset:4360\n\t"
      "s_waitcnt lgkmcnt(6)\n\t"
      "ds_write_b128 v104, v[184:187] offset:6144\n\t"
      "ds_write_b128 v104, v[188:191] offset:7168\n\t"
      "ds_read_b64_tr_b16 a[28:29], v105 offset:6144\n\t"
      "ds_read_b64_tr_b16 a[30:31], v105 offset:6400\n\t"
      "ds_read_b64_tr_b16 a[92:93], v105 offset:6152\n\t"
      "ds_read_b64_tr_b16 a[94:95], v105 offset:6408\n\t"
      "s_waitcnt lgkmcnt(6)\n\t"
      "ds_write_b128 v104, v[192:195]\n\t"
      "ds_write_b128 v104, v[196:199] offset:1024\n\t"
      "ds_read_b64_tr_b16 a[32:33], v105\n\t"
      "ds_read_b64_tr_b16 a[34:35], v105 offset:256\n\t"
      "ds_read_b64_tr_b16 a[96:97], v105 offset:8\n\t"
      "ds_read_b64_tr_b16 a[98:99], v105 offset:264\n\t"
      "s_waitcnt lgkmcnt(6)\n\t"
      "ds_write_b128 v104, v[200:203] offset:2048\n\t"
      "ds_write_b128 v104, v[204:207] offset:3072\n\t"
      "ds_read_b64_tr_b16 a[36:37], v105 offset:2048\n\t"
      "ds_read_b64_tr_b16 a[38:39], v105 offset:2304\n\t"
      "ds_read_b64_tr_b16 a[100:101], v105 offset:2056\n\t"
      "ds_read_b64_tr_b16 a[102:103], v105 offset:2312\n\t"
      "s_waitcnt lgkmcnt(6)\n\t"
      "ds_write_b128 v104, v[208:211] offset:4096\n\t"
      "ds_write_b128 v104, v[212:215] offset:5120\n\t"
      "ds_read_b64_tr_b16 a[40:41], v105 offset:4096\n\t"
      "ds_read_b64_tr_b16 a[42:43], v105 offset:4352\n\t"
      "ds_read_b64_tr_b16 a[104:105], v105 offset:4104\n\t"
      "ds_read_b64_tr_b16 a[106:107], v105 offset:4360\n\t"
      "s_waitcnt lgkmcnt(6)\n\t"
      "ds_write_b128 v104, v[216:219] offset:6144\n\t"
      "ds_write_b128 v104, v[220:223] offset:7168\n\t"
      "ds_read_b64_tr_b16 a[44:45], v105 offset:6144\n\t"
      "ds_read_b64_tr_b16 a[46:47], v105 offset:6400\n\t"
      "ds_read_b64_tr_b16 a[108:109], v105 offset:6152\n\t"
      "ds_read_b64_tr_b16 a[110:111], v105 offset:6408\n\t"
      "s_waitcnt lgkmcnt(6)\n\t"
      "ds_write_b128 v104, v[224:227]\n\t"
      "ds_write_b128 v104, v[228:231] offset:1024\n\t"
      "ds_read_b64_tr_b16 a[48:49], v105\n\t"
      "ds_read_b64_tr_b16 a[50:51], v105 offset:256\n\t"
      "ds_read_b64_tr_b16 a[112:113], v105 offset:8\n\t"
      "ds_read_b64_tr_b16 a[114:115], v105 offset:264\n\t"
      "s_waitcnt lgkmcnt(6)\n\t"
      "ds_write_b128 v104, v[232:235] offset:2048\n\t"
      "ds_write_b128 v104, v[236:239] offset:3072\n\t"
      "ds_read_b64_tr_b16 a[52:53], v105 offset:2048\n\t"
      "ds_read_b64_tr_b16 a[54:55], v105 offset:2304\n\t"
      "ds_read_b64_tr_b16 a[116:117], v105 offset:2056\n\t"
      "ds_read_b64_tr_b16 a[118:119], v105 offset:2312\n\t"
      "s_waitcnt lgkmcnt(6)\n\t"
      "ds_write_b128 v104, v[240:243] offset:4096\n\t"
      "ds_write_b128 v104, v[244:247] offset:5120\n\t"
      "ds_read_b64_tr_b16 a[56:57], v105 offset:4096\n\t"
      "ds_read_b64_tr_b16 a[58:59], v105 offset:4352\n\t"
      "ds_read_b64_tr_b16 a[120:121], v105 offset:4104\n\t"
      "ds_read_b64_tr_b16 a[122:123], v105 offset:4360\n\t"
      "s_waitcnt lgkmcnt(6)\n\t"
      "ds_write_b128 v104, v[248:251] offset:6144\n\t"
      "ds_write_b128 v104, v[252:255] offset:7168\n\t"
      "ds_read_b64_tr_b16 a[60:61], v105 offset:6144\n\t"
      "ds_read_b64_tr_b16 a[62:63], v105 offset:6400\n\t"
      "ds_read_b64_tr_b16 a[124:125], v105 offset:6152\n\t"
      "ds_read_b64_tr_b16 a[126:127], v105 offset:6408\n\t"
      "s_waitcnt vmcnt(0) lgkmcnt(0)\n\t"
      "s_barrier\n\t"
      "s_mov_b64 exec, s[90:91]\n\t"
      "buffer_store_dword v107, off, s[60:63], s75 sc1\n\t"
      "s_sub_u32 s75, s75, 32\n\t"
      "buffer_store_dword v107, off, s[60:63], s75 sc1\n\t"
      "s_sub_u32 s75, s75, 2016\n\t"
      "buffer_store_dword v107, off, s[60:63], s75 sc1\n\t"
      "s_mov_b64 exec, -1\n\t"
      : "+{s34}"(w), "+{s33}"(wv), "+{s[0:1]}"(A), "=&s"(tag), "={a[0:31]}"(wo.a), "={a[32:63]}"(wo.b), "={a[64:95]}"(wo.c), "={a[96:127]}"(wo.d)
      :
      : "memory", "vcc", "scc", "v0", "v1", "v2", "v3", "v4", "v5", "v6", "v7", "v8", "v9", "v10", "v11", "v12", "v13", "v14", "v15", "v16", "v17", "v18", "v19", "v20", "v21", "v22", "v23", "v24", "v25", "v26", "v27", "v28", "v29", "v30", "v31", "v32", "v33", "v34", "v35", "v36", "v37", "v38", "v39", "v40", "v41", "v42", "v43", "v44", "v45", "v46", "v47", "v48", "v49", "v50", "v51", "v52", "v53", "v54", "v55", "v56", "v57", "v58", "v59", "v60", "v61", "v62", "v63", "v64", "v65", "v66", "v67", "v68", "v69", "v70", "v71", "v72", "v73", "v74", "v75", "v76", "v77", "v78", "v79", "v80", "v81", "v82", "v83", "v84", "v85", "v86", "v87", "v88", "v89", "v90", "v91", "v92", "v93", "v94", "v95", "v96", "v97", "v98", "v99", "v100", "v101", "v102", "v103", "v104", "v105", "v106", "v107", "v108", "v109", "v110", "v111", "v112", "v113", "v114", "v115", "v116", "v117", "v118", "v119", "v120", "v121", "v122", "v123", "v124", "v125", "v126", "v127", "v128", "v129", "v130", "v131", "v132", "v133", "v134", "v135", "v136", "v137", "v138", "v139", "v140", "v141", "v142", "v143", "v144", "v145", "v146", "v147", "v148", "v149", "v150", "v151", "v152", "v153", "v154", "v155", "v156", "v157", "v158", "v159", "v160", "v161", "v162", "v163", "v164", "v165", "v166", "v167", "v168", "v169", "v170", "v171", "v172", "v173", "v174", "v175", "v176", "v177", "v178", "v179", "v180", "v181", "v182", "v183", "v184", "v185", "v186", "v187", "v188", "v189", "v190", "v191", "v192", "v193", "v194", "v195", "v196", "v197", "v198", "v199", "v200", "v201", "v202", "v203", "v204", "v205", "v206", "v207", "v208", "v209", "v210", "v211", "v212", "v213", "v214", "v215", "v216", "v217", "v218", "v219", "v220", "v221", "v222", "v223", "v224", "v225", "v226", "v227", "v228", "v229", "v230", "v231", "v232", "v233", "v234", "v235", "v236", "v237", "v238", "v239", "v240", "v241", "v242", "v243", "v244", "v245", "v246", "v247", "v248", "v249", "v250", "v251", "v252", "v253", "v254", "v255", "a128", "a129", "a130", "a131", "a132", "a133", "a134", "a135", "a136", "a137", "a138", "a139", "a140", "a141", "a142", "a143", "a144", "a145", "a146", "a147", "a148", "a149", "a150", "a151", "a152", "a153", "a154", "a155", "a156", "a157", "a158", "a159", "a160", "a161", "a162", "a163", "a164", "a165", "a166", "a167", "a168", "a169", "a170", "a171", "a172", "a173", "a174", "a175", "a176", "a177", "a178", "a179", "a180", "a181", "a182", "a183", "a184", "a185", "a186", "a187", "a188", "a189", "a190", "a191", "a192", "a193", "a194", "a195", "a196", "a197", "a198", "a199", "a200", "a201", "a202", "a203", "a204", "a205", "a206", "a207", "a208", "a209", "a210", "a211", "a212", "a213", "a214", "a215", "a216", "a217", "a218", "a219", "a220", "a221", "a222", "a223", "a224", "a225", "a226", "a227", "a228", "a229", "a230", "a231", "a232", "a233", "a234", "a235", "a236", "a237", "a238", "a239", "a240", "a241", "a242", "a243", "a244", "a245", "a246", "a247", "a248", "a249", "a250", "a251", "a252", "a253", "a254", "a255", "s36", "s37", "s38", "s39", "s40", "s41", "s42", "s43", "s44", "s45", "s46", "s47", "s48", "s49", "s50", "s51", "s52", "s53", "s54", "s55", "s56", "s57", "s58", "s59", "s60", "s61", "s62", "s63", "s64", "s65", "s66", "s67", "s68", "s69", "s70", "s71", "s72", "s73", "s74", "s75", "s76", "s77", "s78", "s79", "s80", "s81", "s82", "s83", "s84", "s85", "s86", "s87", "s88", "s89", "s90", "s91", "s92", "s93", "s94", "s95");
  return tag;
}

__device__ __forceinline__ void worker_tasks(char* lds, int& w, int& wv, KArgs& A, unsigned tag, WoRegs& wo) {
  asm volatile("" : "+s"(w), "+s"(wv), "+s"(A));
  const int lane = fresh_lane(), l15 = lane & 15, l4 = lane >> 4;
  const int team = w & 3, j = w >> 2, g = j >> 2, c = j & 3, h = g * 4 + wv;
  const u64 wsa = (u64)A->ws, xa = (u64)A->x, oa = (u64)A->out;
  u32x16 c0 = {(unsigned)wsa, (unsigned)(wsa >> 32) & 0xffffu, WS_BYTES, 0x00020000u, (unsigned)xa, (unsigned)(xa >> 32) & 0xffffu, S_ * DM * 2, 0x00020000u,
               (unsigned)oa, (unsigned)(oa >> 32) & 0xffffu, S_ * DM * 2, 0x00020000u, O_STAMPS + (310000 + j * 4 + wv) * 8, (unsigned)(l15 - l4 * 4),
               (unsigned)(team + 4 * wv + 48), (unsigned)j};
  const int r0 = 0 ? h : j;
  u32x16 c1 = {(unsigned)team, (unsigned)(team + 4 * (0 ? c : wv)), O_QKV + (team * 32 + h) * 24576, O_KVM + (team * 32 + h) * 2048, O_VTM + (team * 32 + h) * 2048,
               O_PART + (team * 8 + g) * 32768, O_PART + r0 * 1024, O_PFLAG + team * 16384, O_KVSTEP + h * 64, O_RBUF, O_LNH, O_HBUF, (unsigned)r0,
               (unsigned)wv * 8192, tag, O_PFLAG + team * 16384 + (j * 4 + wv) * 128};
  u32x8 c4 = {(unsigned)lane, (unsigned)lane, (unsigned)lane, (unsigned)lane, O_TFLAG + (6 * 32 + h) * 64 + 32 + (l15 & 1) * 16, O_TFLAG + (15 * 32 + h) * 64 + 32, O_WM + lane * 256 + 4, (unsigned)lane};
  const int rc = 8 * (l15 + 16 * (l4 >> 1)) + 4 * (l4 & 1);
  const u32x2 w0 = *(const u32x2*)(A->w_r + rc), w1 = *(const u32x2*)(A->w_r + 256 + rc);
  u32x16 c2 = {w0[0] << 16, w0[0] & 0xffff0000u, w0[1] << 16, w0[1] & 0xffff0000u, w1[0] << 16, w1[0] & 0xffff0000u, w1[1] << 16, w1[1] & 0xffff0000u,
               (unsigned)lane * 16, (unsigned)rc * 2, (unsigned)(lane ^ ((l4 & 1) * 8)) * 16,
               (unsigned)(wv * 8192 + (l15 & 3) * 256 + (((l4 * 4 + (l15 >> 2)) ^ ((l15 & 1) * 8)) << 4)),
               (unsigned)(l4 * 128 + (l15 & 7) * 16), (unsigned)(lane & 31) * 16,
               (unsigned)(65536 + wv * 8192 + lane * 16), (unsigned)(65536 + lane * 16)};
  unsigned c3 = (unsigned)(l15 * DM + c * 128 + wv * 32 + l4 * 8) * 2;
  asm volatile(
      ".p2align 5\n\t"
      "s_nop 1\n\t"
      "v_readfirstlane_b32 s36, v0\n\t"
      "v_readfirstlane_b32 s37, v1\n\t"
      "v_readfirstlane_b32 s38, v2\n\t"
      "v_readfirstlane_b32 s39, v3\n\t"
      "v_readfirstlane_b32 s40, v4\n\t"
      "v_readfirstlane_b32 s41, v5\n\t"
      "v_readfirstlane_b32 s42, v6\n\t"
      "v_readfirstlane_b32 s43, v7\n\t"
      "v_readfirstlane_b32 s44, v8\n\t"
      "v_readfirstlane_b32 s45, v9\n\t"
      "v_readfirstlane_b32 s46, v10\n\t"
      "v_readfirstlane_b32 s47, v11\n\t"
      "v_readfirstlane_b32 s48, v16\n\t"
      "v_readfirstlane_b32 s49, v17\n\t"
      "v_readfirstlane_b32 s50, v18\n\t"
      "v_readfirstlane_b32 s51, v19\n\t"
      "v_readfirstlane_b32 s52, v20\n\t"
      "v_readfirstlane_b32 s53, v21\n\t"
      "v_readfirstlane_b32 s54, v22\n\t"
      "v_readfirstlane_b32 s55, v23\n\t"
      "v_readfirstlane_b32 s56, v24\n\t"
      "v_readfirstlane_b32 s57, v25\n\t"
      "v_readfirstlane_b32 s58, v26\n\t"
      "v_readfirstlane_b32 s59, v27\n\t"
      "v_readfirstlane_b32 s60, v28\n\t"
      "v_readfirstlane_b32 s61, v29\n\t"
      "v_readfirstlane_b32 s62, v30\n\t"
      "v_readfirstlane_b32 s63, v31\n\t"
      "v_readfirstlane_b32 s75, v12\n\t"
      "v_readfirstlane_b32 s93, v14\n\t"
      "v_readfirstlane_b32 s94, v15\n\t"
      "v_cmp_ge_i32 s[76:77], v13, 0\n\t"
      "v_cmp_ge_i32 s[78:79], v13, 1\n\t"
      "v_cmp_ge_i32 s[80:81], v13, 2\n\t"
      "v_cmp_ge_i32 s[82:83], v13, 3\n\t"
      "s_nop 4\n\t"
      "v_readfirstlane_b32 s99, v37\n\t"
      "s_add_u32 s98, s53, 12582912\n\t"
      "v_lshrrev_b32 v40, 4, v248\n\t"
      "v_and_b32 v41, 15, v40\n\t"
      "v_lshrrev_b32 v40, 1, v40\n\t"
      "v_and_b32 v40, 24, v40\n\t"
      "v_sub_u32 v41, v41, v40\n\t"
      "s_mov_b32 s69, 0x3f80\n\t"
      "s_mov_b32 s70, 0x3f800000\n\t"
      "v_mov_b32 v42, 0\n\t"
      "v_cmp_eq_i32 vcc, 0, v41\n\t"
      "v_mov_b32 v43, s69\n\t"
      "s_nop 0\n\t"
      "v_cndmask_b32 v42, v42, v43\n\t"
      "v_cmp_eq_i32 vcc, 1, v41\n\t"
      "v_mov_b32 v43, s70\n\t"
      "s_nop 0\n\t"
      "v_cndmask_b32 v42, v42, v43\n\t"
      "v_accvgpr_write_b32 a224, v42\n\t"
      "v_mov_b32 v42, 0\n\t"
      "v_cmp_eq_i32 vcc, 2, v41\n\t"
      "v_mov_b32 v43, s69\n\t"
      "s_nop 0\n\t"
      "v_cndmask_b32 v42, v42, v43\n\t"
      "v_cmp_eq_i32 vcc, 3, v41\n\t"
      "v_mov_b32 v43, s70\n\t"
      "s_nop 0\n\t"
      "v_cndmask_b32 v42, v42, v43\n\t"
      "v_accvgpr_write_b32 a225, v42\n\t"
      "v_mov_b32 v42, 0\n\t"
      "v_cmp_eq_i32 vcc, 4, v41\n\t"
      "v_mov_b32 v43, s69\n\t"
      "s_nop 0\n\t"
      "v_cndmask_b32 v42, v42, v43\n\t"
      "v_cmp_eq_i32 vcc, 5, v41\n\t"
      "v_mov_b32 v43, s70\n\t"
      "s_nop 0\n\t"
      "v_cndmask_b32 v42, v42, v43\n\t"
      "v_accvgpr_write_b32 a226, v42\n\t"
      "v_mov_b32 v42, 0\n\t"
      "v_cmp_eq_i32 vcc, 6, v41\n\t"
      "v_mov_b32 v43, s69\n\t"
      "s_nop 0\n\t"
      "v_cndmask_b32 v42, v42, v43\n\t"
      "v_cmp_eq_i32 vcc, 7, v41\n\t"
      "v_mov_b32 v43, s70\n\t"
      "s_nop 0\n\t"
      "v_cndmask_b32 v42, v42, v43\n\t"
      "v_accvgpr_write_b32 a227, v42\n\t"
      "v_mov_b32 v42, 0\n\t"
      "v_cmp_eq_i32 vcc, -16, v41\n\t"
      "v_mov_b32 v43, s69\n\t"
      "s_nop 0\n\t"
      "v_cndmask_b32 v42, v42, v43\n\t"
      "v_cmp_eq_i32 vcc, -15, v41\n\t"
      "v_mov_b32 v43, s70\n\t"
      "s_nop 0\n\t"
      "v_cndmask_b32 v42, v42, v43\n\t"
      "v_accvgpr_write_b32 a228, v42\n\t"
      "v_mov_b32 v42, 0\n\t"
      "v_cmp_eq_i32 vcc, -14, v41\n\t"
      "v_mov_b32 v43, s69\n\t"
      "s_nop 0\n\t"
      "v_cndmask_b32 v42, v42, v43\n\t"
      "v_cmp_eq_i32 vcc, -13, v41\n\t"
      "v_mov_b32 v43, s70\n\t"
      "s_nop 0\n\t"
      "v_cndmask_b32 v42, v42, v43\n\t"
      "v_accvgpr_write_b32 a229, v42\n\t"
      "v_mov_b32 v42, 0\n\t"
      "v_cmp_eq_i32 vcc, -12, v41\n\t"
      "v_mov_b32 v43, s69\n\t"
      "s_nop 0\n\t"
      "v_cndmask_b32 v42, v42, v43\n\t"
      "v_cmp_eq_i32 vcc, -11, v41\n\t"
      "v_mov_b32 v43, s70\n\t"
      "s_nop 0\n\t"
      "v_cndmask_b32 v42, v42, v43\n\t"
      "v_accvgpr_write_b32 a230, v42\n\t"
      "v_mov_b32 v42, 0\n\t"
      "v_cmp_eq_i32 vcc, -10, v41\n\t"
      "v_mov_b32 v43, s69\n\t"
      "s_nop 0\n\t"
      "v_cndmask_b32 v42, v42, v43\n\t"
      "v_cmp_eq_i32 vcc, -9, v41\n\t"
      "v_mov_b32 v43, s70\n\t"
      "s_nop 0\n\t"
      "v_cndmask_b32 v42, v42, v43\n\t"
      "v_accvgpr_write_b32 a231, v42\n\t"
      "s_mov_b32 s72, 0x40000\n\t"
      "WE0%=:\n\t"
      "buffer_load_dwordx4 v[40:43], v36, s[36:39], 0 offen sc1\n\t"
      "buffer_load_dword v48, v38, s[36:39], 0 offen sc1\n\t"
      "buffer_load_dword v49, v38, s[36:39], 0 offen offset:128 sc1\n\t"
      "s_waitcnt vmcnt(0)\n\t"
      "v_bitop3_b32 v40, v40, v41, s62 bitop3:0x7e\n\t"
      "v_bitop3_b32 v48, v48, v49, s62 bitop3:0x7e\n\t"
      "v_or_b32 v40, v40, v48\n\t"
      "v_cmp_ne_u32 vcc, 0, v40\n\t"
      "s_nop 0\n\t"
      "s_cbranch_vccz WE1%=\n\t"
      "s_sleep 8\n\t"
      "s_sub_u32 s72, s72, 1\n\t"
      "s_cmp_lg_u32 s72, 0\n\t"
      "s_cbranch_scc1 WE0%=\n\t"
      "WE1%=:\n\t"
      "buffer_load_dwordx4 a[128:131], v248, s[36:39], s50 offen  sc1\n\t"
      "buffer_load_dwordx4 a[132:135], v248, s[36:39], s50 offen offset:1024  sc1\n\t"
      "buffer_load_dwordx4 a[136:139], v248, s[36:39], s50 offen offset:2048  sc1\n\t"
      "buffer_load_dwordx4 a[140:143], v248, s[36:39], s50 offen offset:3072  sc1\n\t"
      "s_add_u32 s69, s50, 4096\n\t"
      "buffer_load_dwordx4 a[144:147], v248, s[36:39], s69 offen  sc1\n\t"
      "buffer_load_dwordx4 a[148:151], v248, s[36:39], s69 offen offset:1024  sc1\n\t"
      "buffer_load_dwordx4 a[152:155], v248, s[36:39], s69 offen offset:2048  sc1\n\t"
      "buffer_load_dwordx4 a[156:159], v248, s[36:39], s69 offen offset:3072  sc1\n\t"
      "s_add_u32 s69, s50, 8192\n\t"
      "s_add_u32 m0, s61, 0\n\t"
      "s_nop 0\n\t"
      "buffer_load_dwordx4 v250, s[36:39], s69 offen sc1 lds\n\t"
      "buffer_load_dwordx4 v250, s[36:39], s69 offen offset:1024 sc1 lds\n\t"
      "buffer_load_dwordx4 v250, s[36:39], s69 offen offset:2048 sc1 lds\n\t"
      "buffer_load_dwordx4 v250, s[36:39], s69 offen offset:3072 sc1 lds\n\t"
      "s_add_u32 s69, s50, 12288\n\t"
      "s_add_u32 m0, s61, 4096\n\t"
      "s_nop 0\n\t"
      "buffer_load_dwordx4 v250, s[36:39], s69 offen sc1 lds\n\t"
      "buffer_load_dwordx4 v250, s[36:39], s69 offen offset:1024 sc1 lds\n\t"
      "buffer_load_dwordx4 v250, s[36:39], s69 offen offset:2048 sc1 lds\n\t"
      "buffer_load_dwordx4 v250, s[36:39], s69 offen offset:3072 sc1 lds\n\t"
      "s_add_u32 s69, s50, 16384\n\t"
      "buffer_load_dwordx4 a[160:163], v248, s[36:39], s69 offen  sc1\n\t"
      "buffer_load_dwordx4 a[164:167], v248, s[36:39], s69 offen offset:1024  sc1\n\t"
      "buffer_load_dwordx4 a[168:171], v248, s[36:39], s69 offen offset:2048  sc1\n\t"
      "buffer_load_dwordx4 a[172:175], v248, s[36:39], s69 offen offset:3072  sc1\n\t"
      "s_add_u32 s69, s50, 20480\n\t"
      "buffer_load_dwordx4 a[176:179], v248, s[36:39], s69 offen  sc1\n\t"
      "buffer_load_dwordx4 a[180:183], v248, s[36:39], s69 offen offset:1024  sc1\n\t"
      "buffer_load_dwordx4 a[184:187], v248, s[36:39], s69 offen offset:2048  sc1\n\t"
      "buffer_load_dwordx4 a[188:191], v248, s[36:39], s69 offen offset:3072  sc1\n\t"
      "s_add_u32 s50, s50, 3145728\n\t"
      "s_mov_b32 s64, 0\n\t"
      "s_mov_b32 s65, 0\n\t"
      "s_mov_b32 s66, 0\n\t"
      "s_mov_b32 s96, 0\n\t"
      "s_mov_b32 s97, 0\n\t"
      "s_add_u32 s89, s49, 16\n\t"
      "s_sub_u32 s95, s94, s60\n\t"
      "s_lshl_b32 s95, s95, 10\n\t"
      "s_add_u32 s95, s95, s54\n\t"
      "s_mov_b32 s67, -1\n\t"
      "s_mov_b32 s74, 0xffff0000\n\t"
      "s_mov_b32 s84, 0x3db504f3\n\t"
      "s_mov_b32 s86, 0x3e0293ee\n\t"
      "s_mov_b32 s85, 0x3db504f3\n\t"
      "s_mov_b32 s87, 0x3e0293ee\n\t"
      "v_mov_b32 v239, 0xff61b1e6\n\t"
      "s_waitcnt vmcnt(0) lgkmcnt(0)\n\t"
      "s_branch WA%=\n\t"
      ".p2align 5\n\t"
      "WTOP%=:\n\t"
      "s_cmp_ge_u32 s48, 64\n\t"
      "s_cbranch_scc1 WFIN%=\n\t"
      "s_lshr_b32 s69, s48, 2\n\t"
      "s_cmp_eq_u32 s69, 5\n\t"
      "s_cbranch_scc1 WH2%=\n\t"
      "WT0%=:\n\t"
      "s_waitcnt vmcnt(17)\n\t"
      "s_cmp_eq_u32 s24, 9\n\t"
      "s_cbranch_scc1 WT1%=\n\t"
      "s_waitcnt vmcnt(10)\n\t"
      "s_cmp_lg_u32 s97, 0\n\t"
      "s_cbranch_scc1 WT1%=\n\t"
      "s_waitcnt vmcnt(9)\n\t"
      "s_cmp_eq_u32 s24, 8\n\t"
      "s_cbranch_scc1 WT1%=\n\t"
      "s_waitcnt vmcnt(2)\n\t"
      "WT1%=:\n\t"
      "v_min_u32 v228, v228, v229\n\t"
      "v_min3_u32 v228, v228, v230, v231\n\t"
      "v_min_u32 v232, v232, v233\n\t"
      "s_cmp_lg_u32 s65, 0\n\t"
      "s_cselect_b32 s70, s89, s49\n\t"
      "v_readfirstlane_b32 s88, v228\n\t"
      "s_memrealtime s[90:91]\n\t"
      "v_cmp_ge_u32 vcc, s70, v232\n\t"
      "s_lshr_b32 s69, s88, 16\n\t"
      "s_cmp_gt_u32 s69, s48\n\t"
      "s_cselect_b32 s64, 1, 0\n\t"
      "s_cmp_eq_u64 vcc, 0\n\t"
      "s_cselect_b32 s66, 1, 0\n\t"
      "s_cmp_eq_u32 s65, 2\n\t"
      "s_cselect_b32 s66, 0, s66\n\t"
      "s_add_u32 s70, s48, 4\n\t"
      "s_cmp_gt_u32 s69, s70\n\t"
      "s_cselect_b32 s96, 1, 0\n\t"
      "s_or_b32 s64, s64, s97\n\t"
      "s_cmp_lg_u32 s97, 0\n\t"
      "s_cbranch_scc1 WA%=\n\t"
      "s_cmp_lg_u32 s64, 0\n\t"
      "s_cbranch_scc1 WT2%=\n\t"
      "s_sub_u32 s69, s48, s69\n\t"
      "s_mul_i32 s69, s69, 170\n\t"
      "s_waitcnt lgkmcnt(0)\n\t"
      "s_sub_u32 s70, s90, s88\n\t"
      "s_add_u32 s70, s70, 200\n\t"
      "s_and_b32 s70, s70, 0xffff\n\t"
      "s_add_u32 s69, s69, 60\n\t"
      "s_cmp_ge_u32 s70, s69\n\t"
      "s_cselect_b32 s69, s48, 0\n\t"
      "s_cmp_ge_u32 s69, 40\n\t"
      "s_cselect_b32 s64, 2, 0\n\t"
      "s_cmp_eq_u32 s64, 0\n\t"
      "s_cbranch_scc1 WA%=\n\t"
      "WT2%=:\n\t"
      "buffer_load_dwordx4 v[128:131], v252, s[36:39], s51 offen sc1\n\t"
      "buffer_load_dwordx4 v[132:135], v252, s[36:39], s51 offen offset:512 sc1\n\t"
      "buffer_load_dwordx4 v[136:139], v252, s[36:39], s51 offen offset:1024 sc1\n\t"
      "buffer_load_dwordx4 v[140:143], v252, s[36:39], s51 offen offset:1536 sc1\n\t"
      "buffer_load_dwordx4 v[144:147], v253, s[36:39], s52 offen sc1\n\t"
      "buffer_load_dwordx4 v[148:151], v253, s[36:39], s52 offen offset:512 sc1\n\t"
      "buffer_load_dwordx4 v[152:155], v253, s[36:39], s52 offen offset:1024 sc1\n\t"
      "buffer_load_dwordx4 v[156:159], v253, s[36:39], s52 offen offset:1536 sc1\n\t"
      "WA%=:\n\t"
      "v_mfma_f32_16x16x32_bf16 v[0:3], a[128:131], a[160:163], 0\n\t"
      "ds_read_b64_tr_b16 a[192:193], v251\n\t"
      "ds_read_b64_tr_b16 a[194:195], v251 offset:1024\n\t"
      "v_mfma_f32_16x16x32_bf16 v[4:7], a[128:131], a[164:167], 0\n\t"
      "ds_read_b64_tr_b16 a[196:197], v251 offset:8\n\t"
      "ds_read_b64_tr_b16 a[198:199], v251 offset:1032\n\t"
      "v_mfma_f32_16x16x32_bf16 v[8:11], a[132:135], a[164:167], 0\n\t"
      "ds_read_b64_tr_b16 a[200:201], v251 offset:2048\n\t"
      "ds_read_b64_tr_b16 a[202:203], v251 offset:3072\n\t"
      "v_mfma_f32_16x16x32_bf16 v[0:3], a[136:139], a[168:171], v[0:3]\n\t"
      "ds_read_b64_tr_b16 a[204:205], v251 offset:2056\n\t"
      "ds_read_b64_tr_b16 a[206:207], v251 offset:3080\n\t"
      "v_mfma_f32_16x16x32_bf16 v[4:7], a[136:139], a[172:175], v[4:7]\n\t"
      "v_mfma_f32_16x16x32_bf16 v[8:11], a[140:143], a[172:175], v[8:11]\n\t"
      "s_waitcnt lgkmcnt(0)\n\t"
      "ds_read_b64_tr_b16 a[208:209], v251 offset:4096\n\t"
      "ds_read_b64_tr_b16 a[210:211], v251 offset:5120\n\t"
      "ds_read_b64_tr_b16 a[212:213], v251 offset:4104\n\t"
      "ds_read_b64_tr_b16 a[214:215], v251 offset:5128\n\t"
      "ds_read_b64_tr_b16 a[216:217], v251 offset:6144\n\t"
      "ds_read_b64_tr_b16 a[218:219], v251 offset:7168\n\t"
      "ds_read_b64_tr_b16 a[220:221], v251 offset:6152\n\t"
      "ds_read_b64_tr_b16 a[222:223], v251 offset:7176\n\t"
      "v_mfma_f32_16x16x32_bf16 v[0:3], a[144:147], a[176:179], v[0:3]\n\t"
      "v_mfma_f32_16x16x32_bf16 v[4:7], a[144:147], a[180:183], v[4:7]\n\t"
      "v_mfma_f32_16x16x32_bf16 v[8:11], a[148:151], a[180:183], v[8:11]\n\t"
      "v_mfma_f32_16x16x32_bf16 v[0:3], a[152:155], a[184:187], v[0:3]\n\t"
      "v_mfma_f32_16x16x32_bf16 v[4:7], a[152:155], a[188:191], v[4:7]\n\t"
      "v_mfma_f32_16x16x32_bf16 v[8:11], a[156:159], a[188:191], v[8:11]\n\t"
      "s_waitcnt lgkmcnt(0)\n\t"
      "buffer_load_dwordx4 a[128:131], v248, s[36:39], s50 offen  sc1\n\t"
      "s_nop 3\n\t"
      "v_cndmask_b32_e64 v0, v239, v0, s[76:77]\n\t"
      "s_nop 0\n\t"
      "v_cndmask_b32_e64 v8, v239, v8, s[76:77]\n\t"
      "v_cndmask_b32_e64 v1, v239, v1, s[78:79]\n\t"
      "v_cndmask_b32_e64 v9, v239, v9, s[78:79]\n\t"
      "v_cndmask_b32_e64 v2, v239, v2, s[80:81]\n\t"
      "v_cndmask_b32_e64 v10, v239, v10, s[80:81]\n\t"
      "v_cndmask_b32_e64 v3, v239, v3, s[82:83]\n\t"
      "v_cndmask_b32_e64 v11, v239, v11, s[82:83]\n\t"
      "v_max3_f32 v234, v0, v1, v2\n\t"
      "v_max3_f32 v235, v4, v5, v6\n\t"
      "v_max_f32 v234, v234, v3\n\t"
      "v_max3_f32 v235, v235, v7, v8\n\t"
      "v_max3_f32 v235, v235, v9, v10\n\t"
      "v_max_f32 v235, v235, v11\n\t"
      "s_nop 1\n\t"
      "v_permlane16_swap_b32 v234, v235\n\t"
      "v_max_f32 v234, v234, v235\n\t"
      "buffer_load_dwordx4 a[132:135], v248, s[36:39], s50 offen offset:1024  sc1\n\t"
      "v_mov_b32 v235, v234\n\t"
      "s_nop 1\n\t"
      "v_permlane32_swap_b32 v234, v235\n\t"
      "v_max_f32 v234, v234, v235\n\t"
      "v_mov_b32 v235, v234\n\t"
      "s_nop 1\n\t"
      "v_permlane16_swap_b32 v234, v235\n\t"
      "v_pk_mul_f32 v[12:13], v[234:235], s[86:87]\n\t"
      "v_pk_fma_f32 v[0:1], v[0:1], s[86:87], v[12:13] op_sel:[0,0,0] op_sel_hi:[1,1,0] neg_lo:[0,0,1] neg_hi:[0,0,1]\n\t"
      "v_pk_fma_f32 v[2:3], v[2:3], s[86:87], v[12:13] op_sel:[0,0,0] op_sel_hi:[1,1,0] neg_lo:[0,0,1] neg_hi:[0,0,1]\n\t"
      "v_pk_fma_f32 v[4:5], v[4:5], s[86:87], v[12:13] op_sel:[0,0,1] op_sel_hi:[1,1,1] neg_lo:[0,0,1] neg_hi:[0,0,1]\n\t"
      "v_pk_fma_f32 v[6:7], v[6:7], s[86:87], v[12:13] op_sel:[0,0,1] op_sel_hi:[1,1,1] neg_lo:[0,0,1] neg_hi:[0,0,1]\n\t"
      "v_pk_fma_f32 v[8:9], v[8:9], s[86:87], v[12:13] op_sel:[0,0,1] op_sel_hi:[1,1,1] neg_lo:[0,0,1] neg_hi:[0,0,1]\n\t"
      "v_pk_fma_f32 v[10:11], v[10:11], s[86:87], v[12:13] op_sel:[0,0,1] op_sel_hi:[1,1,1] neg_lo:[0,0,1] neg_hi:[0,0,1]\n\t"
      "v_exp_f32 v0, v0\n\t"
      "v_exp_f32 v1, v1\n\t"
      "buffer_load_dwordx4 a[136:139], v248, s[36:39], s50 offen offset:2048  sc1\n\t"
      "v_exp_f32 v2, v2\n\t"
      "v_exp_f32 v3, v3\n\t"
      "v_exp_f32 v4, v4\n\t"
      "v_exp_f32 v5, v5\n\t"
      "v_exp_f32 v6, v6\n\t"
      "v_exp_f32 v7, v7\n\t"
      "v_exp_f32 v8, v8\n\t"
      "v_exp_f32 v9, v9\n\t"
      "buffer_load_dwordx4 a[140:143], v248, s[36:39], s50 offen offset:3072  sc1\n\t"
      "v_exp_f32 v10, v10\n\t"
      "v_exp_f32 v11, v11\n\t"
      "v_mov_b64 v[18:19], 0\n\t"
      "v_cvt_pk_bf16_f32 v16, v0, v1\n\t"
      "v_cvt_pk_bf16_f32 v17, v2, v3\n\t"
      "v_cvt_pk_bf16_f32 v20, v4, v5\n\t"
      "v_cvt_pk_bf16_f32 v21, v6, v7\n\t"
      "v_cvt_pk_bf16_f32 v22, v8, v9\n\t"
      "v_cvt_pk_bf16_f32 v23, v10, v11\n\t"
      "v_mfma_f32_16x16x32_bf16 v[64:67], a[192:195], v[16:19], 0\n\t"
      "v_pk_add_f32 v[0:1], v[0:1], v[2:3]\n\t"
      "s_add_u32 s69, s50, 4096\n\t"
      "buffer_load_dwordx4 a[144:147], v248, s[36:39], s69 offen  sc1\n\t"
      "v_mfma_f32_16x16x32_bf16 v[96:99], a[192:195], v[20:23], 0\n\t"
      "v_pk_add_f32 v[4:5], v[4:5], v[6:7]\n\t"
      "v_mfma_f32_16x16x32_bf16 v[68:71], a[196:199], v[16:19], 0\n\t"
      "v_pk_add_f32 v[8:9], v[8:9], v[10:11]\n\t"
      "v_mfma_f32_16x16x32_bf16 v[100:103], a[196:199], v[20:23], 0\n\t"
      "v_add_f32 v236, v0, v1\n\t"
      "v_mfma_f32_16x16x32_bf16 v[72:75], a[200:203], v[16:19], 0\n\t"
      "buffer_load_dwordx4 a[148:151], v248, s[36:39], s69 offen offset:1024  sc1\n\t"
      "v_pk_add_f32 v[4:5], v[4:5], v[8:9]\n\t"
      "v_mfma_f32_16x16x32_bf16 v[104:107], a[200:203], v[20:23], 0\n\t"
      "v_add_f32 v237, v4, v5\n\t"
      "v_mfma_f32_16x16x32_bf16 v[76:79], a[204:207], v[16:19], 0\n\t"
      "v_mfma_f32_16x16x32_bf16 v[108:111], a[204:207], v[20:23], 0\n\t"
      "buffer_load_dwordx4 a[152:155], v248, s[36:39], s69 offen offset:2048  sc1\n\t"
      "v_mfma_f32_16x16x32_bf16 v[80:83], a[208:211], v[16:19], 0\n\t"
      "v_mfma_f32_16x16x32_bf16 v[112:115], a[208:211], v[20:23], 0\n\t"
      "v_mfma_f32_16x16x32_bf16 v[84:87], a[212:215], v[16:19], 0\n\t"
      "v_mfma_f32_16x16x32_bf16 v[116:119], a[212:215], v[20:23], 0\n\t"
      "buffer_load_dwordx4 a[156:159], v248, s[36:39], s69 offen offset:3072  sc1\n\t"
      "v_mfma_f32_16x16x32_bf16 v[88:91], a[216:219], v[16:19], 0\n\t"
      "v_mfma_f32_16x16x32_bf16 v[120:123], a[216:219], v[20:23], 0\n\t"
      "v_mfma_f32_16x16x32_bf16 v[92:95], a[220:223], v[16:19], 0\n\t"
      "v_mfma_f32_16x16x32_bf16 v[124:127], a[220:223], v[20:23], 0\n\t"
      "s_waitcnt vmcnt(8)\n\t"
      "s_cmp_lt_u32 s48, 4\n\t"
      "s_cbranch_scc1 WGO%=\n\t"
      "v_mov_b32 v0, s48\n\t"
      "s_mov_b64 exec, 1\n\t"
      "buffer_store_dword v0, off, s[36:39], s63 sc1\n\t"
      "s_mov_b64 exec, -1\n\t"
      "s_cmp_lt_i32 s67, 0\n\t"
      "s_cbranch_scc1 WPB1%=\n\t"
      "v_mov_b32 v8, s68\n\t"
      "s_mov_b64 exec, 1\n\t"
      "buffer_store_dword v8, off, s[36:39], s67 sc1\n\t"
      "s_mov_b64 exec, -1\n\t"
      "s_mov_b32 s67, -1\n\t"
      "WPB1%=:\n\t"
      "WGO%=:\n\t"
      "s_cmp_eq_u32 s64, 1\n\t"
      "s_cbranch_scc1 WKV%=\n\t"
      "s_cmp_lg_u32 s65, 0\n\t"
      "s_cbranch_scc1 WR2%=\n\t"
      "WGO2%=:\n\t"
      "s_cmp_lt_u32 s48, 40\n\t"
      "s_cselect_b32 s71, 1, -1\n\t"
      "s_add_u32 s71, s71, s48\n\t"
      "s_mov_b32 s72, 0x40000\n\t"
      "WLOOP%=:\n\t"
      "s_cmp_ge_u32 s49, 48\n\t"
      "s_cselect_b32 s69, s49, 99\n\t"
      "s_cmp_lt_u32 s69, s48\n\t"
      "s_cbranch_scc1 WRW%=\n\t"
      "s_cmp_eq_u32 s64, 2\n\t"
      "s_cbranch_scc1 WKC%=\n\t"
      "v_lshlrev_b32 v6, 4, v248\n\t"
      "v_mov_b64 v[4:5], 0\n\t"
      "buffer_load_dwordx4 v[0:3], off, s[36:39], s56 sc1\n\t"
      "s_cmp_ge_u32 s49, s48\n\t"
      "s_cbranch_scc1 WL1%=\n\t"
      "buffer_load_dword v4, v6, s[36:39], s55 offen sc1\n\t"
      "buffer_load_dword v5, v6, s[36:39], s55 offen offset:128 sc1\n\t"
      "WL1%=:\n\t"
      "s_waitcnt vmcnt(0)\n\t"
      "s_cmp_lt_i32 s67, 0\n\t"
      "s_cbranch_scc1 WL2%=\n\t"
      "v_mov_b32 v8, s68\n\t"
      "s_mov_b64 exec, 1\n\t"
      "buffer_store_dword v8, off, s[36:39], s67 sc1\n\t"
      "s_mov_b64 exec, -1\n\t"
      "s_mov_b32 s67, -1\n\t"
      "WL2%=:\n\t"
      "v_min_u32 v0, v0, v1\n\t"
      "v_min3_u32 v0, v0, v2, v3\n\t"
      "v_min_u32 v4, v4, v5\n\t"
      "v_readfirstlane_b32 s88, v0\n\t"
      "v_cmp_ge_u32 vcc, s49, v4\n\t"
      "s_lshr_b32 s69, s88, 16\n\t"
      "s_max_u32 s70, s71, s48\n\t"
      "s_cmp_ge_u32 s69, s70\n\t"
      "s_cbranch_scc1 WKL%=\n\t"
      "s_cmp_ge_u32 s69, s71\n\t"
      "s_cbranch_scc1 WKF%=\n\t"
      "s_cbranch_vccz WRW%=\n\t"
      "s_sleep 6\n\t"
      "s_cmp_lt_u32 s49, s48\n\t"
      "s_cbranch_scc1 WKM%=\n\t"
      "s_sub_u32 s69, s71, s69\n\t"
      "s_min_u32 s69, s69, 9\n\t"
      "WKN%=:\n\t"
      "s_sub_u32 s69, s69, 1\n\t"
      "s_cmp_eq_u32 s69, 0\n\t"
      "s_cbranch_scc1 WKM%=\n\t"
      "s_sleep 48\n\t"
      "s_branch WKN%=\n\t"
      "WKM%=:\n\t"
      "s_sub_u32 s72, s72, 1\n\t"
      "s_cmp_gt_i32 s72, 0\n\t"
      "s_cbranch_scc1 WLOOP%=\n\t"
      "s_branch WKL%=\n\t"
      ".p2align 5\n\t"
      "WFIN%=:\n\t"
      "s_mov_b32 s64, 0\n\t"
      "s_mov_b32 s72, 0x40000\n\t"
      "s_waitcnt vmcnt(2)\n\t"
      "WFIN1%=:\n\t"
      "s_cmp_lt_i32 s67, 0\n\t"
      "s_cbranch_scc1 WF1%=\n\t"
      "v_mov_b32 v8, s68\n\t"
      "s_mov_b64 exec, 1\n\t"
      "buffer_store_dword v8, off, s[36:39], s67 sc1\n\t"
      "s_mov_b64 exec, -1\n\t"
      "s_mov_b32 s67, -1\n\t"
      "WF1%=:\n\t"
      "s_cmp_lg_u32 s65, 0\n\t"
      "s_cbranch_scc1 WRC%=\n\t"
      "s_cmp_ge_u32 s49, 64\n\t"
      "s_cbranch_scc1 WEND%=\n\t"
      "s_cmp_ge_u32 s49, 48\n\t"
      "s_cbranch_scc1 WRS%=\n\t"
      "v_lshlrev_b32 v6, 4, v248\n\t"
      "WRF%=:\n\t"
      "buffer_load_dword v4, v6, s[36:39], s55 offen sc1\n\t"
      "buffer_load_dword v5, v6, s[36:39], s55 offen offset:128 sc1\n\t"
      "s_waitcnt vmcnt(0)\n\t"
      "v_min_u32 v4, v4, v5\n\t"
      "v_cmp_ge_u32 vcc, s49, v4\n\t"
      "s_nop 0\n\t"
      "s_cbranch_vccz WRW%=\n\t"
      "s_sleep 8\n\t"
      "s_sub_u32 s72, s72, 1\n\t"
      "s_cmp_gt_i32 s72, 0\n\t"
      "s_cbranch_scc1 WRF%=\n\t"
      "WRS%=:\n\t"
      "s_waitcnt vmcnt(0)\n\t"
      "WRW%=:\n\t"
      "s_lshl_b32 s70, s49, 5\n\t"
      "s_add_u32 s70, s70, s60\n\t"
      "s_lshl_b32 s70, s70, 10\n\t"
      "buffer_load_dwordx2 v[224:225], v249, s[40:43], s70 offen \n\t"
      "buffer_load_dwordx2 v[226:227], v249, s[40:43], s70 offen offset:512 \n\t"
      "WRL%=:\n\t"
      "s_lshl_b32 s69, s49, 18\n\t"
      "s_add_u32 s69, s69, s54\n\t"
      "buffer_load_dwordx4 v[160:163], v248, s[36:39], s69 offen sc1\n\t"
      "s_add_u32 s69, s69, 0x8000\n\t"
      "buffer_load_dwordx4 v[164:167], v248, s[36:39], s69 offen sc1\n\t"
      "s_add_u32 s69, s69, 0x8000\n\t"
      "buffer_load_dwordx4 v[168:171], v248, s[36:39], s69 offen sc1\n\t"
      "s_add_u32 s69, s69, 0x8000\n\t"
      "buffer_load_dwordx4 v[172:175], v248, s[36:39], s69 offen sc1\n\t"
      "s_add_u32 s69, s69, 0x8000\n\t"
      "buffer_load_dwordx4 v[176:179], v248, s[36:39], s69 offen sc1\n\t"
      "s_add_u32 s69, s69, 0x8000\n\t"
      "buffer_load_dwordx4 v[180:183], v248, s[36:39], s69 offen sc1\n\t"
      "s_add_u32 s69, s69, 0x8000\n\t"
      "buffer_load_dwordx4 v[184:187], v248, s[36:39], s69 offen sc1\n\t"
      "s_add_u32 s69, s69, 0x8000\n\t"
      "buffer_load_dwordx4 v[188:191], v248, s[36:39], s69 offen sc1\n\t"
      "s_waitcnt vmcnt(0)\n\t"
      "s_cmp_lt_i32 s67, 0\n\t"
      "s_cbranch_scc1 WF2%=\n\t"
      "v_mov_b32 v8, s68\n\t"
      "s_mov_b64 exec, 1\n\t"
      "buffer_store_dword v8, off, s[36:39], s67 sc1\n\t"
      "s_mov_b64 exec, -1\n\t"
      "s_mov_b32 s67, -1\n\t"
      "WF2%=:\n\t"
      "WRC%=:\n\t"
      "v_mfma_f32_16x16x32_bf16 v[16:19], a[224:227], v[160:163], 0\n\t"
      "v_mfma_f32_16x16x32_bf16 v[20:23], a[228:231], v[160:163], 0\n\t"
      "v_and_b32 v193, s74, v224\n\t"
      "v_lshlrev_b32 v192, 16, v224\n\t"
      "v_mfma_f32_16x16x32_bf16 v[16:19], a[224:227], v[164:167], v[16:19]\n\t"
      "v_mfma_f32_16x16x32_bf16 v[20:23], a[228:231], v[164:167], v[20:23]\n\t"
      "v_and_b32 v195, s74, v225\n\t"
      "v_lshlrev_b32 v194, 16, v225\n\t"
      "v_mfma_f32_16x16x32_bf16 v[16:19], a[224:227], v[168:171], v[16:19]\n\t"
      "v_mfma_f32_16x16x32_bf16 v[20:23], a[228:231], v[168:171], v[20:23]\n\t"
      "v_and_b32 v197, s74, v226\n\t"
      "v_lshlrev_b32 v196, 16, v226\n\t"
      "v_mfma_f32_16x16x32_bf16 v[16:19], a[224:227], v[172:175], v[16:19]\n\t"
      "v_mfma_f32_16x16x32_bf16 v[20:23], a[228:231], v[172:175], v[20:23]\n\t"
      "v_and_b32 v199, s74, v227\n\t"
      "v_lshlrev_b32 v198, 16, v227\n\t"
      "v_mfma_f32_16x16x32_bf16 v[16:19], a[224:227], v[176:179], v[16:19]\n\t"
      "v_mfma_f32_16x16x32_bf16 v[20:23], a[228:231], v[176:179], v[20:23]\n\t"
      "v_mfma_f32_16x16x32_bf16 v[16:19], a[224:227], v[180:183], v[16:19]\n\t"
      "v_mfma_f32_16x16x32_bf16 v[20:23], a[228:231], v[180:183], v[20:23]\n\t"
      "v_mfma_f32_16x16x32_bf16 v[16:19], a[224:227], v[184:187], v[16:19]\n\t"
      "v_mfma_f32_16x16x32_bf16 v[20:23], a[228:231], v[184:187], v[20:23]\n\t"
      "v_mfma_f32_16x16x32_bf16 v[16:19], a[224:227], v[188:191], v[16:19]\n\t"
      "v_mfma_f32_16x16x32_bf16 v[20:23], a[228:231], v[188:191], v[20:23]\n\t"
      "s_nop 6\n\t"
      "v_cvt_pk_bf16_f32 v32, v16, v17\n\t"
      "v_cvt_pk_bf16_f32 v34, v18, v19\n\t"
      "v_cvt_pk_bf16_f32 v36, v20, v21\n\t"
      "v_cvt_pk_bf16_f32 v38, v22, v23\n\t"
      "v_and_b32 v33, s74, v32\n\t"
      "v_lshlrev_b32 v32, 16, v32\n\t"
      "v_and_b32 v35, s74, v34\n\t"
      "v_lshlrev_b32 v34, 16, v34\n\t"
      "v_and_b32 v37, s74, v36\n\t"
      "v_lshlrev_b32 v36, 16, v36\n\t"
      "v_and_b32 v39, s74, v38\n\t"
      "v_lshlrev_b32 v38, 16, v38\n\t"
      "v_pk_add_f32 v[192:193], v[192:193], v[32:33]\n\t"
      "v_pk_add_f32 v[194:195], v[194:195], v[34:35]\n\t"
      "v_pk_add_f32 v[196:197], v[196:197], v[36:37]\n\t"
      "v_pk_add_f32 v[198:199], v[198:199], v[38:39]\n\t"
      "v_cvt_pk_bf16_f32 v200, v192, v193\n\t"
      "v_cvt_pk_bf16_f32 v201, v194, v195\n\t"
      "v_cvt_pk_bf16_f32 v202, v196, v197\n\t"
      "v_cvt_pk_bf16_f32 v203, v198, v199\n\t"
      "v_and_b32 v193, s74, v200\n\t"
      "v_lshlrev_b32 v192, 16, v200\n\t"
      "v_and_b32 v195, s74, v201\n\t"
      "v_lshlrev_b32 v194, 16, v201\n\t"
      "v_and_b32 v197, s74, v202\n\t"
      "v_lshlrev_b32 v196, 16, v202\n\t"
      "v_and_b32 v199, s74, v203\n\t"
      "v_lshlrev_b32 v198, 16, v203\n\t"
      "v_pk_add_f32 v[32:33], v[192:193], v[196:197]\n\t"
      "v_pk_add_f32 v[34:35], v[194:195], v[198:199]\n\t"
      "v_pk_add_f32 v[32:33], v[32:33], v[34:35]\n\t"
      "v_add_f32 v32, v32, v33\n\t"
      "s_nop 1\n\t"
      "v_add_f32_dpp v32, v32, v32 quad_perm:[1,0,3,2] row_mask:0xf bank_mask:0xf\n\t"
      "s_nop 1\n\t"
      "v_add_f32_dpp v32, v32, v32 quad_perm:[2,3,0,1] row_mask:0xf bank_mask:0xf\n\t"
      "s_nop 1\n\t"
      "v_add_f32_dpp v32, v32, v32 row_ror:4 row_mask:0xf bank_mask:0xf\n\t"
      "s_nop 1\n\t"
      "v_add_f32_dpp v32, v32, v32 row_ror:8 row_mask:0xf bank_mask:0xf\n\t"
      "v_mov_b32 v33, v32\n\t"
      "s_nop 1\n\t"
      "v_permlane16_swap_b32 v32, v33\n\t"
      "v_add_f32 v32, v32, v33\n\t"
      "v_mov_b32 v33, v32\n\t"
      "s_nop 1\n\t"
      "v_permlane32_swap_b32 v32, v33\n\t"
      "v_add_f32 v32, v32, v33\n\t"
      "v_cmp_u_f32 vcc, v32, v32\n\t"
      "v_mul_f32 v204, 0x3b000000, v32\n\t"
      "s_cbranch_vccnz WRN%=\n\t"
      "WRD%=:\n\t"
      "v_pk_add_f32 v[40:41], v[192:193], v[204:205] op_sel_hi:[1,0] neg_lo:[0,1] neg_hi:[0,1]\n\t"
      "v_pk_add_f32 v[42:43], v[194:195], v[204:205] op_sel_hi:[1,0] neg_lo:[0,1] neg_hi:[0,1]\n\t"
      "v_pk_add_f32 v[44:45], v[196:197], v[204:205] op_sel_hi:[1,0] neg_lo:[0,1] neg_hi:[0,1]\n\t"
      "v_pk_add_f32 v[46:47], v[198:199], v[204:205] op_sel_hi:[1,0] neg_lo:[0,1] neg_hi:[0,1]\n\t"
      "v_pk_mul_f32 v[32:33], v[40:41], v[40:41]\n\t"
      "v_pk_fma_f32 v[32:33], v[42:43], v[42:43], v[32:33]\n\t"
      "v_pk_fma_f32 v[32:33], v[44:45], v[44:45], v[32:33]\n\t"
      "v_pk_fma_f32 v[32:33], v[46:47], v[46:47], v[32:33]\n\t"
      "v_add_f32 v32, v32, v33\n\t"
      "s_nop 1\n\t"
      "v_add_f32_dpp v32, v32, v32 quad_perm:[1,0,3,2] row_mask:0xf bank_mask:0xf\n\t"
      "s_nop 1\n\t"
      "v_add_f32_dpp v32, v32, v32 quad_perm:[2,3,0,1] row_mask:0xf bank_mask:0xf\n\t"
      "s_nop 1\n\t"
      "v_add_f32_dpp v32, v32, v32 row_ror:4 row_mask:0xf bank_mask:0xf\n\t"
      "s_nop 1\n\t"
      "v_add_f32_dpp v32, v32, v32 row_ror:8 row_mask:0xf bank_mask:0xf\n\t"
      "v_mov_b32 v33, v32\n\t"
      "s_nop 1\n\t"
      "v_permlane16_swap_b32 v32, v33\n\t"
      "v_add_f32 v32, v32, v33\n\t"
      "v_mov_b32 v33, v32\n\t"
      "s_nop 1\n\t"
      "v_permlane32_swap_b32 v32, v33\n\t"
      "v_add_f32 v32, v32, v33\n\t"
      "v_mul_f32 v205, 0x3b000000, v32\n\t"
      "v_cvt_pk_bf16_f32 v204, v204, v205\n\t"
      "v_and_b32 v205, s74, v204\n\t"
      "v_lshlrev_b32 v204, 16, v204\n\t"
      "v_add_f32 v205, 0x3727c5ac, v205\n\t"
      "v_pk_add_f32 v[40:41], v[192:193], v[204:205] op_sel_hi:[1,0] neg_lo:[0,1] neg_hi:[0,1]\n\t"
      "v_pk_add_f32 v[42:43], v[194:195], v[204:205] op_sel_hi:[1,0] neg_lo:[0,1] neg_hi:[0,1]\n\t"
      "v_pk_add_f32 v[44:45], v[196:197], v[204:205] op_sel_hi:[1,0] neg_lo:[0,1] neg_hi:[0,1]\n\t"
      "v_pk_add_f32 v[46:47], v[198:199], v[204:205] op_sel_hi:[1,0] neg_lo:[0,1] neg_hi:[0,1]\n\t"
      "v_cvt_pk_bf16_f32 v205, v205, v205\n\t"
      "v_and_b32 v205, s74, v205\n\t"
      "v_sqrt_f32 v205, v205\n\t"
      "v_cvt_pk_bf16_f32 v40, v40, v41\n\t"
      "v_cvt_pk_bf16_f32 v42, v42, v43\n\t"
      "v_cvt_pk_bf16_f32 v44, v44, v45\n\t"
      "v_cvt_pk_bf16_f32 v46, v46, v47\n\t"
      "v_cvt_pk_bf16_f32 v205, v205, v205\n\t"
      "v_and_b32 v205, s74, v205\n\t"
      "v_rcp_f32 v204, v205\n\t"
      "v_and_b32 v41, s74, v40\n\t"
      "v_lshlrev_b32 v40, 16, v40\n\t"
      "v_and_b32 v43, s74, v42\n\t"
      "v_lshlrev_b32 v42, 16, v42\n\t"
      "v_and_b32 v45, s74, v44\n\t"
      "v_lshlrev_b32 v44, 16, v44\n\t"
      "v_and_b32 v47, s74, v46\n\t"
      "v_lshlrev_b32 v46, 16, v46\n\t"
      "v_pk_mul_f32 v[40:41], v[40:41], v[204:205] op_sel_hi:[1,0]\n\t"
      "v_pk_mul_f32 v[42:43], v[42:43], v[204:205] op_sel_hi:[1,0]\n\t"
      "v_pk_mul_f32 v[44:45], v[44:45], v[204:205] op_sel_hi:[1,0]\n\t"
      "v_pk_mul_f32 v[46:47], v[46:47], v[204:205] op_sel_hi:[1,0]\n\t"
      "v_cvt_pk_bf16_f32 v52, v40, v41\n\t"
      "v_cvt_pk_bf16_f32 v53, v42, v43\n\t"
      "v_cvt_pk_bf16_f32 v54, v44, v45\n\t"
      "v_cvt_pk_bf16_f32 v55, v46, v47\n\t"
      "v_and_b32 v33, s74, v52\n\t"
      "v_lshlrev_b32 v32, 16, v52\n\t"
      "v_and_b32 v35, s74, v53\n\t"
      "v_lshlrev_b32 v34, 16, v53\n\t"
      "v_and_b32 v37, s74, v54\n\t"
      "v_lshlrev_b32 v36, 16, v54\n\t"
      "v_and_b32 v39, s74, v55\n\t"
      "v_lshlrev_b32 v38, 16, v55\n\t"
      "v_pk_mul_f32 v[40:41], v[32:33], v[240:241]\n\t"
      "v_pk_fma_f32 v[40:41], v[34:35], v[242:243], v[40:41]\n\t"
      "v_pk_fma_f32 v[40:41], v[36:37], v[244:245], v[40:41]\n\t"
      "v_pk_fma_f32 v[40:41], v[38:39], v[246:247], v[40:41]\n\t"
      "v_add_f32 v40, v40, v41\n\t"
      "s_lshl_b32 s73, s49, 5\n\t"
      "s_add_u32 s73, s73, s60\n\t"
      "s_lshl_b32 s69, s73, 2\n\t"
      "s_add_u32 s69, s69, s57\n\t"
      "s_lshl_b32 s70, s73, 7\n\t"
      "s_add_u32 s70, s70, s57\n\t"
      "s_sub_u32 s70, s70, 212992\n\t"
      "s_cmp_ge_u32 s49, 56\n\t"
      "s_cselect_b32 s70, s70, s69\n\t"
      "v_add_f32_dpp v40, v40, v40 quad_perm:[1,0,3,2] row_mask:0xf bank_mask:0xf\n\t"
      "s_nop 1\n\t"
      "v_add_f32_dpp v40, v40, v40 quad_perm:[2,3,0,1] row_mask:0xf bank_mask:0xf\n\t"
      "s_nop 1\n\t"
      "v_add_f32_dpp v40, v40, v40 row_ror:4 row_mask:0xf bank_mask:0xf\n\t"
      "s_nop 1\n\t"
      "v_add_f32_dpp v40, v40, v40 row_ror:8 row_mask:0xf bank_mask:0xf\n\t"
      "v_mov_b32 v41, v40\n\t"
      "s_nop 1\n\t"
      "v_permlane16_swap_b32 v40, v41\n\t"
      "v_add_f32 v40, v40, v41\n\t"
      "v_mov_b32 v41, v40\n\t"
      "s_nop 1\n\t"
      "v_permlane32_swap_b32 v40, v41\n\t"
      "v_add_f32 v40, v40, v41\n\t"
      "v_cvt_pk_bf16_f32 v40, v40, v40\n\t"
      "v_and_b32 v40, s74, v40\n\t"
      "s_mov_b64 exec, 1\n\t"
      "buffer_store_dword v40, off, s[36:39], s70 sc1\n\t"
      "s_mov_b64 exec, -1\n\t"
      "v_pk_add_f32 v[192:193], v[192:193], v[32:33]\n\t"
      "v_pk_add_f32 v[194:195], v[194:195], v[34:35]\n\t"
      "v_pk_add_f32 v[196:197], v[196:197], v[36:37]\n\t"
      "v_pk_add_f32 v[198:199], v[198:199], v[38:39]\n\t"
      "v_readfirstlane_b32 s68, v40\n\t"
      "v_cvt_pk_bf16_f32 v56, v192, v193\n\t"
      "v_cvt_pk_bf16_f32 v57, v194, v195\n\t"
      "v_cvt_pk_bf16_f32 v58, v196, v197\n\t"
      "v_cvt_pk_bf16_f32 v59, v198, v199\n\t"
      "s_add_u32 s67, s69, 8192\n\t"
      "s_lshl_b32 s73, s73, 10\n\t"
      "s_add_u32 s69, s73, s58\n\t"
      "s_add_u32 s70, s73, s59\n\t"
      "buffer_store_dwordx2 v[56:57], v249, s[44:47], s73 offen sc1\n\t"
      "buffer_store_dwordx2 v[52:53], v249, s[36:39], s69 offen sc1\n\t"
      "buffer_store_dwordx2 v[200:201], v249, s[36:39], s70 offen sc1\n\t"
      "buffer_store_dwordx2 v[58:59], v249, s[44:47], s73 offen offset:512 sc1\n\t"
      "buffer_store_dwordx2 v[54:55], v249, s[36:39], s69 offen offset:512 sc1\n\t"
      "buffer_store_dwordx2 v[202:203], v249, s[36:39], s70 offen offset:512 sc1\n\t"
      "s_mov_b32 s49, s89\n\t"
      "s_add_u32 s89, s89, 16\n\t"
      "s_lshr_b32 s69, s89, 4\n\t"
      "s_cmp_eq_u32 s69, 3\n\t"
      "s_cselect_b32 s89, s93, s89\n\t"
      "s_cmp_ge_u32 s49, 48\n\t"
      "s_cselect_b32 s60, s94, s60\n\t"
      "s_cselect_b32 s54, s95, s54\n\t"
      "s_cmp_ge_u32 s48, 64\n\t"
      "s_cbranch_scc1 WRX%=\n\t"
      "s_cmp_lg_u32 s65, 0\n\t"
      "s_mov_b32 s65, 0\n\t"
      "s_cbranch_scc1 WGO%=\n\t"
      "s_mov_b32 s66, 0\n\t"
      "s_branch WLOOP%=\n\t"
      ".p2align 5\n\t"
      "WRX%=:\n\t"
      "s_mov_b32 s65, 0\n\t"
      "s_waitcnt vmcnt(0)\n\t"
      "s_branch WFIN1%=\n\t"
      ".p2align 5\n\t"
      "WRN%=:\n\t"
      "v_max3_u32 v40, v160, v161, v162\n\t"
      "v_max3_u32 v40, v40, v163, v164\n\t"
      "v_max3_u32 v40, v40, v165, v166\n\t"
      "v_max3_u32 v40, v40, v167, v168\n\t"
      "v_max3_u32 v40, v40, v169, v170\n\t"
      "v_max3_u32 v40, v40, v171, v172\n\t"
      "v_max3_u32 v40, v40, v173, v174\n\t"
      "v_max3_u32 v40, v40, v175, v176\n\t"
      "v_max3_u32 v40, v40, v177, v178\n\t"
      "v_max3_u32 v40, v40, v179, v180\n\t"
      "v_max3_u32 v40, v40, v181, v182\n\t"
      "v_max3_u32 v40, v40, v183, v184\n\t"
      "v_max3_u32 v40, v40, v185, v186\n\t"
      "v_max3_u32 v40, v40, v187, v188\n\t"
      "v_max3_u32 v40, v40, v189, v190\n\t"
      "v_max_u32 v40, v40, v191\n\t"
      "v_cmp_eq_u32 vcc, -1, v40\n\t"
      "s_nop 0\n\t"
      "s_cbranch_vccz WRD%=\n\t"
      "s_cmp_lg_u32 s65, 0\n\t"
      "s_cbranch_scc1 WRD%=\n\t"
      "s_sub_u32 s72, s72, 1\n\t"
      "s_cmp_gt_i32 s72, 0\n\t"
      "s_cbranch_scc1 WRL%=\n\t"
      "s_branch WRD%=\n\t"
      ".p2align 5\n\t"
      "WH2%=:\n\t"
      "s_mov_b32 s72, 0x40000\n\t"
      "WH3%=:\n\t"
      "buffer_load_dwordx2 v[0:1], off, s[36:39], s99 sc1\n\t"
      "buffer_load_dwordx2 v[2:3], off, s[36:39], s99 offset:16 sc1\n\t"
      "s_sub_u32 s72, s72, 1\n\t"
      "s_waitcnt vmcnt(0)\n\t"
      "v_bitop3_b32 v0, v0, v1, s62 bitop3:0x7e\n\t"
      "v_bitop3_b32 v2, v2, v3, s62 bitop3:0x7e\n\t"
      "v_or_b32 v0, v0, v2\n\t"
      "v_cmp_ne_u32 vcc, 0, v0\n\t"
      "s_cmp_eq_u32 s72, 0\n\t"
      "s_cbranch_scc1 WT0%=\n\t"
      "s_cbranch_vccnz WH3%=\n\t"
      "s_branch WT0%=\n\t"
      ".p2align 5\n\t"
      "WKV1%=:\n\t"
      "s_cmp_eq_u32 s66, 0\n\t"
      "s_cbranch_scc1 WB1%=\n\t"
      "WB2%=:\n\t"
      "s_mov_b32 s65, 1\n\t"
      "v_mfma_f32_16x16x32_bf16 v[0:3], v[128:131], a[160:163], 0\n\t"
      "v_mfma_f32_16x16x32_bf16 v[4:7], v[128:131], a[164:167], 0\n\t"
      "v_mfma_f32_16x16x32_bf16 v[0:3], v[132:135], a[168:171], v[0:3]\n\t"
      "v_mfma_f32_16x16x32_bf16 v[4:7], v[132:135], a[172:175], v[4:7]\n\t"
      "v_mfma_f32_16x16x32_bf16 v[0:3], v[136:139], a[176:179], v[0:3]\n\t"
      "v_mfma_f32_16x16x32_bf16 v[4:7], v[136:139], a[180:183], v[4:7]\n\t"
      "v_mfma_f32_16x16x32_bf16 v[0:3], v[140:143], a[184:187], v[0:3]\n\t"
      "v_mfma_f32_16x16x32_bf16 v[4:7], v[140:143], a[188:191], v[4:7]\n\t"
      "s_add_u32 s51, s51, 262144\n\t"
      "s_add_u32 s52, s52, 262144\n\t"
      "s_lshl_b32 s70, s49, 5\n\t"
      "s_add_u32 s70, s70, s60\n\t"
      "s_lshl_b32 s70, s70, 10\n\t"
      "buffer_load_dwordx2 v[224:225], v249, s[40:43], s70 offen \n\t"
      "s_nop 0\n\t"
      "v_max3_f32 v8, v0, v1, v2\n\t"
      "v_max3_f32 v8, v8, v3, v234\n\t"
      "v_max3_f32 v9, v4, v5, v6\n\t"
      "v_max3_f32 v9, v9, v7, v235\n\t"
      "s_nop 1\n\t"
      "v_permlane16_swap_b32 v8, v9\n\t"
      "v_max_f32 v8, v8, v9\n\t"
      "buffer_load_dwordx2 v[226:227], v249, s[40:43], s70 offen offset:512 \n\t"
      "v_mov_b32 v9, v8\n\t"
      "s_nop 1\n\t"
      "v_permlane16_swap_b32 v8, v9\n\t"
      "v_pk_mul_f32 v[8:9], v[8:9], s[86:87]\n\t"
      "v_pk_fma_f32 v[10:11], v[234:235], s[86:87], v[8:9] neg_lo:[0,0,1] neg_hi:[0,0,1]\n\t"
      "v_pk_fma_f32 v[0:1], v[0:1], s[86:87], v[8:9] op_sel:[0,0,0] op_sel_hi:[1,1,0] neg_lo:[0,0,1] neg_hi:[0,0,1]\n\t"
      "v_pk_fma_f32 v[2:3], v[2:3], s[86:87], v[8:9] op_sel:[0,0,0] op_sel_hi:[1,1,0] neg_lo:[0,0,1] neg_hi:[0,0,1]\n\t"
      "s_lshl_b32 s69, s49, 18\n\t"
      "s_add_u32 s69, s69, s54\n\t"
      "buffer_load_dwordx4 v[160:163], v248, s[36:39], s69 offen sc1\n\t"
      "v_pk_fma_f32 v[4:5], v[4:5], s[86:87], v[8:9] op_sel:[0,0,1] op_sel_hi:[1,1,1] neg_lo:[0,0,1] neg_hi:[0,0,1]\n\t"
      "v_pk_fma_f32 v[6:7], v[6:7], s[86:87], v[8:9] op_sel:[0,0,1] op_sel_hi:[1,1,1] neg_lo:[0,0,1] neg_hi:[0,0,1]\n\t"
      "v_exp_f32 v10, v10\n\t"
      "v_exp_f32 v11, v11\n\t"
      "s_add_u32 s69, s69, 0x8000\n\t"
      "buffer_load_dwordx4 v[164:167], v248, s[36:39], s69 offen sc1\n\t"
      "v_exp_f32 v0, v0\n\t"
      "v_exp_f32 v1, v1\n\t"
      "v_exp_f32 v2, v2\n\t"
      "v_exp_f32 v3, v3\n\t"
      "s_add_u32 s69, s69, 0x8000\n\t"
      "buffer_load_dwordx4 v[168:171], v248, s[36:39], s69 offen sc1\n\t"
      "v_exp_f32 v4, v4\n\t"
      "v_exp_f32 v5, v5\n\t"
      "v_exp_f32 v6, v6\n\t"
      "s_add_u32 s69, s69, 0x8000\n\t"
      "buffer_load_dwordx4 v[172:175], v248, s[36:39], s69 offen sc1\n\t"
      "v_exp_f32 v7, v7\n\t"
      "v_pk_mul_f32 v[12:13], v[236:237], v[10:11]\n\t"
      "v_pk_add_f32 v[14:15], v[0:1], v[2:3]\n\t"
      "v_pk_add_f32 v[16:17], v[4:5], v[6:7]\n\t"
      "s_add_u32 s69, s69, 0x8000\n\t"
      "buffer_load_dwordx4 v[176:179], v248, s[36:39], s69 offen sc1\n\t"
      "v_add_f32 v14, v14, v15\n\t"
      "v_add_f32 v15, v16, v17\n\t"
      "v_fma_f32 v12, v14, 0.5, v12\n\t"
      "v_fma_f32 v13, v15, 0.5, v13\n\t"
      "s_nop 1\n\t"
      "v_permlane16_swap_b32 v12, v13\n\t"
      "v_add_f32 v12, v12, v13\n\t"
      "s_add_u32 s69, s69, 0x8000\n\t"
      "buffer_load_dwordx4 v[180:183], v248, s[36:39], s69 offen sc1\n\t"
      "v_mov_b32 v13, v12\n\t"
      "s_nop 1\n\t"
      "v_permlane32_swap_b32 v12, v13\n\t"
      "v_add_f32 v12, v12, v13\n\t"
      "v_mov_b32 v13, v12\n\t"
      "s_nop 1\n\t"
      "v_permlane16_swap_b32 v12, v13\n\t"
      "v_rcp_f32 v12, v12\n\t"
      "s_add_u32 s69, s69, 0x8000\n\t"
      "buffer_load_dwordx4 v[184:187], v248, s[36:39], s69 offen sc1\n\t"
      "v_rcp_f32 v13, v13\n\t"
      "s_nop 0\n\t"
      "v_pk_mul_f32 v[10:11], v[10:11], v[12:13]\n\t"
      "v_pk_mul_f32 v[12:13], v[12:13], 0.5 op_sel_hi:[1,0]\n\t"
      "v_pk_mul_f32 v[0:1], v[0:1], v[12:13] op_sel:[0,0] op_sel_hi:[1,0]\n\t"
      "s_add_u32 s69, s69, 0x8000\n\t"
      "buffer_load_dwordx4 v[188:191], v248, s[36:39], s69 offen sc1\n\t"
      "v_pk_mul_f32 v[2:3], v[2:3], v[12:13] op_sel:[0,0] op_sel_hi:[1,0]\n\t"
      "v_pk_mul_f32 v[4:5], v[4:5], v[12:13] op_sel:[0,1] op_sel_hi:[1,1]\n\t"
      "v_pk_mul_f32 v[6:7], v[6:7], v[12:13] op_sel:[0,1] op_sel_hi:[1,1]\n\t"
      "v_cvt_pk_bf16_f32 v16, v0, v1\n\t"
      "v_cvt_pk_bf16_f32 v17, v2, v3\n\t"
      "v_cvt_pk_bf16_f32 v18, v4, v5\n\t"
      "s_add_u32 s69, s50, 8192\n\t"
      "s_add_u32 m0, s61, 0\n\t"
      "s_nop 0\n\t"
      "buffer_load_dwordx4 v250, s[36:39], s69 offen sc1 lds\n\t"
      "v_cvt_pk_bf16_f32 v19, v6, v7\n\t"
      "v_pk_mul_f32 v[64:65], v[64:65], v[10:11] op_sel:[0,0] op_sel_hi:[1,0]\n\t"
      "v_pk_mul_f32 v[66:67], v[66:67], v[10:11] op_sel:[0,0] op_sel_hi:[1,0]\n\t"
      "v_pk_mul_f32 v[68:69], v[68:69], v[10:11] op_sel:[0,0] op_sel_hi:[1,0]\n\t"
      "v_pk_mul_f32 v[70:71], v[70:71], v[10:11] op_sel:[0,0] op_sel_hi:[1,0]\n\t"
      "v_mfma_f32_16x16x16_bf16 v[64:67], v[144:145], v[16:17], v[64:67]\n\t"
      "buffer_load_dwordx4 v250, s[36:39], s69 offen offset:1024 sc1 lds\n\t"
      "v_pk_mul_f32 v[72:73], v[72:73], v[10:11] op_sel:[0,0] op_sel_hi:[1,0]\n\t"
      "v_pk_mul_f32 v[74:75], v[74:75], v[10:11] op_sel:[0,0] op_sel_hi:[1,0]\n\t"
      "v_mfma_f32_16x16x16_bf16 v[68:71], v[146:147], v[16:17], v[68:71]\n\t"
      "v_pk_mul_f32 v[76:77], v[76:77], v[10:11] op_sel:[0,0] op_sel_hi:[1,0]\n\t"
      "buffer_load_dwordx4 v250, s[36:39], s69 offen offset:2048 sc1 lds\n\t"
      "v_pk_mul_f32 v[78:79], v[78:79], v[10:11] op_sel:[0,0] op_sel_hi:[1,0]\n\t"
      "v_mfma_f32_16x16x16_bf16 v[72:75], v[148:149], v[16:17], v[72:75]\n\t"
      "v_pk_mul_f32 v[80:81], v[80:81], v[10:11] op_sel:[0,0] op_sel_hi:[1,0]\n\t"
      "v_pk_mul_f32 v[82:83], v[82:83], v[10:11] op_sel:[0,0] op_sel_hi:[1,0]\n\t"
      "v_mfma_f32_16x16x16_bf16 v[76:79], v[150:151], v[16:17], v[76:79]\n\t"
      "buffer_load_dwordx4 v250, s[36:39], s69 offen offset:3072 sc1 lds\n\t"
      "v_pk_mul_f32 v[84:85], v[84:85], v[10:11] op_sel:[0,0] op_sel_hi:[1,0]\n\t"
      "v_pk_mul_f32 v[86:87], v[86:87], v[10:11] op_sel:[0,0] op_sel_hi:[1,0]\n\t"
      "v_mfma_f32_16x16x16_bf16 v[80:83], v[152:153], v[16:17], v[80:83]\n\t"
      "v_pk_mul_f32 v[88:89], v[88:89], v[10:11] op_sel:[0,0] op_sel_hi:[1,0]\n\t"
      "s_add_u32 s69, s50, 12288\n\t"
      "s_add_u32 m0, s61, 4096\n\t"
      "s_nop 0\n\t"
      "buffer_load_dwordx4 v250, s[36:39], s69 offen sc1 lds\n\t"
      "v_pk_mul_f32 v[90:91], v[90:91], v[10:11] op_sel:[0,0] op_sel_hi:[1,0]\n\t"
      "v_mfma_f32_16x16x16_bf16 v[84:87], v[154:155], v[16:17], v[84:87]\n\t"
      "v_pk_mul_f32 v[92:93], v[92:93], v[10:11] op_sel:[0,0] op_sel_hi:[1,0]\n\t"
      "v_pk_mul_f32 v[94:95], v[94:95], v[10:11] op_sel:[0,0] op_sel_hi:[1,0]\n\t"
      "v_cvt_pk_bf16_f32 v24, v64, v65\n\t"
      "buffer_load_dwordx4 v250, s[36:39], s69 offen offset:1024 sc1 lds\n\t"
      "v_cvt_pk_bf16_f32 v25, v66, v67\n\t"
      "v_cvt_pk_bf16_f32 v26, v68, v69\n\t"
      "v_cvt_pk_bf16_f32 v27, v70, v71\n\t"
      "ds_write_b128 v254, v[24:27]\n\t"
      "v_mfma_f32_16x16x16_bf16 v[88:91], v[156:157], v[16:17], v[88:91]\n\t"
      "buffer_load_dwordx4 v250, s[36:39], s69 offen offset:2048 sc1 lds\n\t"
      "v_pk_mul_f32 v[96:97], v[96:97], v[10:11] op_sel:[0,1] op_sel_hi:[1,1]\n\t"
      "v_pk_mul_f32 v[98:99], v[98:99], v[10:11] op_sel:[0,1] op_sel_hi:[1,1]\n\t"
      "v_mfma_f32_16x16x16_bf16 v[92:95], v[158:159], v[16:17], v[92:95]\n\t"
      "v_pk_mul_f32 v[100:101], v[100:101], v[10:11] op_sel:[0,1] op_sel_hi:[1,1]\n\t"
      "v_pk_mul_f32 v[102:103], v[102:103], v[10:11] op_sel:[0,1] op_sel_hi:[1,1]\n\t"
      "buffer_load_dwordx4 v250, s[36:39], s69 offen offset:3072 sc1 lds\n\t"
      "v_cvt_pk_bf16_f32 v28, v72, v73\n\t"
      "v_cvt_pk_bf16_f32 v29, v74, v75\n\t"
      "v_cvt_pk_bf16_f32 v30, v76, v77\n\t"
      "v_cvt_pk_bf16_f32 v31, v78, v79\n\t"
      "ds_write_b128 v254, v[28:31] offset:2048\n\t"
      "v_mfma_f32_16x16x16_bf16 v[96:99], v[144:145], v[18:19], v[96:99]\n\t"
      "s_add_u32 s69, s50, 16384\n\t"
      "buffer_load_dwordx4 a[160:163], v248, s[36:39], s69 offen  sc1\n\t"
      "v_pk_mul_f32 v[104:105], v[104:105], v[10:11] op_sel:[0,1] op_sel_hi:[1,1]\n\t"
      "v_pk_mul_f32 v[106:107], v[106:107], v[10:11] op_sel:[0,1] op_sel_hi:[1,1]\n\t"
      "v_mfma_f32_16x16x16_bf16 v[100:103], v[146:147], v[18:19], v[100:103]\n\t"
      "v_pk_mul_f32 v[108:109], v[108:109], v[10:11] op_sel:[0,1] op_sel_hi:[1,1]\n\t"
      "buffer_load_dwordx4 a[164:167], v248, s[36:39], s69 offen offset:1024  sc1\n\t"
      "v_pk_mul_f32 v[110:111], v[110:111], v[10:11] op_sel:[0,1] op_sel_hi:[1,1]\n\t"
      "v_cvt_pk_bf16_f32 v24, v80, v81\n\t"
      "v_cvt_pk_bf16_f32 v25, v82, v83\n\t"
      "v_cvt_pk_bf16_f32 v26, v84, v85\n\t"
      "v_cvt_pk_bf16_f32 v27, v86, v87\n\t"
      "ds_write_b128 v254, v[24:27] offset:4096\n\t"
      "buffer_load_dwordx4 a[168:171], v248, s[36:39], s69 offen offset:2048  sc1\n\t"
      "v_mfma_f32_16x16x16_bf16 v[104:107], v[148:149], v[18:19], v[104:107]\n\t"
      "v_pk_mul_f32 v[112:113], v[112:113], v[10:11] op_sel:[0,1] op_sel_hi:[1,1]\n\t"
      "v_pk_mul_f32 v[114:115], v[114:115], v[10:11] op_sel:[0,1] op_sel_hi:[1,1]\n\t"
      "v_mfma_f32_16x16x16_bf16 v[108:111], v[150:151], v[18:19], v[108:111]\n\t"
      "buffer_load_dwordx4 a[172:175], v248, s[36:39], s69 offen offset:3072  sc1\n\t"
      "v_pk_mul_f32 v[116:117], v[116:117], v[10:11] op_sel:[0,1] op_sel_hi:[1,1]\n\t"
      "v_pk_mul_f32 v[118:119], v[118:119], v[10:11] op_sel:[0,1] op_sel_hi:[1,1]\n\t"
      "v_cvt_pk_bf16_f32 v28, v88, v89\n\t"
      "v_cvt_pk_bf16_f32 v29, v90, v91\n\t"
      "v_cvt_pk_bf16_f32 v30, v92, v93\n\t"
      "v_cvt_pk_bf16_f32 v31, v94, v95\n\t"
      "s_add_u32 s69, s50, 20480\n\t"
      "buffer_load_dwordx4 a[176:179], v248, s[36:39], s69 offen  sc1\n\t"
      "ds_write_b128 v254, v[28:31] offset:6144\n\t"
      "v_mfma_f32_16x16x16_bf16 v[112:115], v[152:153], v[18:19], v[112:115]\n\t"
      "v_pk_mul_f32 v[120:121], v[120:121], v[10:11] op_sel:[0,1] op_sel_hi:[1,1]\n\t"
      "v_pk_mul_f32 v[122:123], v[122:123], v[10:11] op_sel:[0,1] op_sel_hi:[1,1]\n\t"
      "v_mfma_f32_16x16x16_bf16 v[116:119], v[154:155], v[18:19], v[116:119]\n\t"
      "buffer_load_dwordx4 a[180:183], v248, s[36:39], s69 offen offset:1024  sc1\n\t"
      "v_pk_mul_f32 v[124:125], v[124:125], v[10:11] op_sel:[0,1] op_sel_hi:[1,1]\n\t"
      "v_pk_mul_f32 v[126:127], v[126:127], v[10:11] op_sel:[0,1] op_sel_hi:[1,1]\n\t"
      "v_cvt_pk_bf16_f32 v24, v96, v97\n\t"
      "v_cvt_pk_bf16_f32 v25, v98, v99\n\t"
      "v_cvt_pk_bf16_f32 v26, v100, v101\n\t"
      "buffer_load_dwordx4 a[184:187], v248, s[36:39], s69 offen offset:2048  sc1\n\t"
      "v_cvt_pk_bf16_f32 v27, v102, v103\n\t"
      "ds_write_b128 v254, v[24:27] offset:1024\n\t"
      "v_mfma_f32_16x16x16_bf16 v[120:123], v[156:157], v[18:19], v[120:123]\n\t"
      "v_mfma_f32_16x16x16_bf16 v[124:127], v[158:159], v[18:19], v[124:127]\n\t"
      "buffer_load_dwordx4 a[188:191], v248, s[36:39], s69 offen offset:3072  sc1\n\t"
      "v_cvt_pk_bf16_f32 v28, v104, v105\n\t"
      "v_cvt_pk_bf16_f32 v29, v106, v107\n\t"
      "v_cvt_pk_bf16_f32 v30, v108, v109\n\t"
      "v_cvt_pk_bf16_f32 v31, v110, v111\n\t"
      "ds_write_b128 v254, v[28:31] offset:3072\n\t"
      "v_cvt_pk_bf16_f32 v24, v112, v113\n\t"
      "v_cvt_pk_bf16_f32 v25, v114, v115\n\t"
      "v_cvt_pk_bf16_f32 v26, v116, v117\n\t"
      "v_cvt_pk_bf16_f32 v27, v118, v119\n\t"
      "ds_write_b128 v254, v[24:27] offset:5120\n\t"
      "v_cvt_pk_bf16_f32 v28, v120, v121\n\t"
      "v_cvt_pk_bf16_f32 v29, v122, v123\n\t"
      "v_cvt_pk_bf16_f32 v30, v124, v125\n\t"
      "v_cvt_pk_bf16_f32 v31, v126, v127\n\t"
      "ds_write_b128 v254, v[28:31] offset:7168\n\t"
      "s_waitcnt vmcnt(40) lgkmcnt(0)\n\t"
      "s_barrier\n\t"
      "s_branch WO%=\n\t"
      ".p2align 5\n\t"
      "WB1%=:\n\t"
      "v_mfma_f32_16x16x32_bf16 v[0:3], v[128:131], a[160:163], 0\n\t"
      "v_mfma_f32_16x16x32_bf16 v[4:7], v[128:131], a[164:167], 0\n\t"
      "v_mfma_f32_16x16x32_bf16 v[0:3], v[132:135], a[168:171], v[0:3]\n\t"
      "v_mfma_f32_16x16x32_bf16 v[4:7], v[132:135], a[172:175], v[4:7]\n\t"
      "v_mfma_f32_16x16x32_bf16 v[0:3], v[136:139], a[176:179], v[0:3]\n\t"
      "v_mfma_f32_16x16x32_bf16 v[4:7], v[136:139], a[180:183], v[4:7]\n\t"
      "v_mfma_f32_16x16x32_bf16 v[0:3], v[140:143], a[184:187], v[0:3]\n\t"
      "v_mfma_f32_16x16x32_bf16 v[4:7], v[140:143], a[188:191], v[4:7]\n\t"
      "s_add_u32 s51, s51, 262144\n\t"
      "s_add_u32 s52, s52, 262144\n\t"
      "s_add_u32 s69, s50, 8192\n\t"
      "s_add_u32 m0, s61, 0\n\t"
      "s_nop 0\n\t"
      "buffer_load_dwordx4 v250, s[36:39], s69 offen sc1 lds\n\t"
      "s_nop 0\n\t"
      "v_max3_f32 v8, v0, v1, v2\n\t"
      "v_max3_f32 v8, v8, v3, v234\n\t"
      "v_max3_f32 v9, v4, v5, v6\n\t"
      "v_max3_f32 v9, v9, v7, v235\n\t"
      "s_nop 1\n\t"
      "v_permlane16_swap_b32 v8, v9\n\t"
      "v_max_f32 v8, v8, v9\n\t"
      "v_mov_b32 v9, v8\n\t"
      "s_nop 1\n\t"
      "v_permlane16_swap_b32 v8, v9\n\t"
      "v_pk_mul_f32 v[8:9], v[8:9], s[86:87]\n\t"
      "v_pk_fma_f32 v[10:11], v[234:235], s[86:87], v[8:9] neg_lo:[0,0,1] neg_hi:[0,0,1]\n\t"
      "buffer_load_dwordx4 v250, s[36:39], s69 offen offset:1024 sc1 lds\n\t"
      "v_pk_fma_f32 v[0:1], v[0:1], s[86:87], v[8:9] op_sel:[0,0,0] op_sel_hi:[1,1,0] neg_lo:[0,0,1] neg_hi:[0,0,1]\n\t"
      "v_pk_fma_f32 v[2:3], v[2:3], s[86:87], v[8:9] op_sel:[0,0,0] op_sel_hi:[1,1,0] neg_lo:[0,0,1] neg_hi:[0,0,1]\n\t"
      "v_pk_fma_f32 v[4:5], v[4:5], s[86:87], v[8:9] op_sel:[0,0,1] op_sel_hi:[1,1,1] neg_lo:[0,0,1] neg_hi:[0,0,1]\n\t"
      "v_pk_fma_f32 v[6:7], v[6:7], s[86:87], v[8:9] op_sel:[0,0,1] op_sel_hi:[1,1,1] neg_lo:[0,0,1] neg_hi:[0,0,1]\n\t"
      "v_exp_f32 v10, v10\n\t"
      "v_exp_f32 v11, v11\n\t"
      "v_exp_f32 v0, v0\n\t"
      "buffer_load_dwordx4 v250, s[36:39], s69 offen offset:2048 sc1 lds\n\t"
      "v_exp_f32 v1, v1\n\t"
      "v_exp_f32 v2, v2\n\t"
      "v_exp_f32 v3, v3\n\t"
      "v_exp_f32 v4, v4\n\t"
      "v_exp_f32 v5, v5\n\t"
      "buffer_load_dwordx4 v250, s[36:39], s69 offen offset:3072 sc1 lds\n\t"
      "v_exp_f32 v6, v6\n\t"
      "v_exp_f32 v7, v7\n\t"
      "v_pk_mul_f32 v[12:13], v[236:237], v[10:11]\n\t"
      "v_pk_add_f32 v[14:15], v[0:1], v[2:3]\n\t"
      "v_pk_add_f32 v[16:17], v[4:5], v[6:7]\n\t"
      "v_add_f32 v14, v14, v15\n\t"
      "v_add_f32 v15, v16, v17\n\t"
      "v_fma_f32 v12, v14, 0.5, v12\n\t"
      "s_add_u32 s69, s50, 12288\n\t"
      "s_add_u32 m0, s61, 4096\n\t"
      "s_nop 0\n\t"
      "buffer_load_dwordx4 v250, s[36:39], s69 offen sc1 lds\n\t"
      "v_fma_f32 v13, v15, 0.5, v13\n\t"
      "s_nop 1\n\t"
      "v_permlane16_swap_b32 v12, v13\n\t"
      "v_add_f32 v12, v12, v13\n\t"
      "v_mov_b32 v13, v12\n\t"
      "s_nop 1\n\t"
      "v_permlane32_swap_b32 v12, v13\n\t"
      "v_add_f32 v12, v12, v13\n\t"
      "v_mov_b32 v13, v12\n\t"
      "s_nop 1\n\t"
      "v_permlane16_swap_b32 v12, v13\n\t"
      "v_rcp_f32 v12, v12\n\t"
      "buffer_load_dwordx4 v250, s[36:39], s69 offen offset:1024 sc1 lds\n\t"
      "v_rcp_f32 v13, v13\n\t"
      "s_nop 0\n\t"
      "v_pk_mul_f32 v[10:11], v[10:11], v[12:13]\n\t"
      "v_pk_mul_f32 v[12:13], v[12:13], 0.5 op_sel_hi:[1,0]\n\t"
      "v_pk_mul_f32 v[0:1], v[0:1], v[12:13] op_sel:[0,0] op_sel_hi:[1,0]\n\t"
      "v_pk_mul_f32 v[2:3], v[2:3], v[12:13] op_sel:[0,0] op_sel_hi:[1,0]\n\t"
      "v_pk_mul_f32 v[4:5], v[4:5], v[12:13] op_sel:[0,1] op_sel_hi:[1,1]\n\t"
      "v_pk_mul_f32 v[6:7], v[6:7], v[12:13] op_sel:[0,1] op_sel_hi:[1,1]\n\t"
      "v_cvt_pk_bf16_f32 v16, v0, v1\n\t"
      "buffer_load_dwordx4 v250, s[36:39], s69 offen offset:2048 sc1 lds\n\t"
      "v_cvt_pk_bf16_f32 v17, v2, v3\n\t"
      "v_cvt_pk_bf16_f32 v18, v4, v5\n\t"
      "v_cvt_pk_bf16_f32 v19, v6, v7\n\t"
      "v_pk_mul_f32 v[64:65], v[64:65], v[10:11] op_sel:[0,0] op_sel_hi:[1,0]\n\t"
      "v_pk_mul_f32 v[66:67], v[66:67], v[10:11] op_sel:[0,0] op_sel_hi:[1,0]\n\t"
      "v_pk_mul_f32 v[68:69], v[68:69], v[10:11] op_sel:[0,0] op_sel_hi:[1,0]\n\t"
      "v_pk_mul_f32 v[70:71], v[70:71], v[10:11] op_sel:[0,0] op_sel_hi:[1,0]\n\t"
      "v_mfma_f32_16x16x16_bf16 v[64:67], v[144:145], v[16:17], v[64:67]\n\t"
      "v_pk_mul_f32 v[72:73], v[72:73], v[10:11] op_sel:[0,0] op_sel_hi:[1,0]\n\t"
      "buffer_load_dwordx4 v250, s[36:39], s69 offen offset:3072 sc1 lds\n\t"
      "v_pk_mul_f32 v[74:75], v[74:75], v[10:11] op_sel:[0,0] op_sel_hi:[1,0]\n\t"
      "v_mfma_f32_16x16x16_bf16 v[68:71], v[146:147], v[16:17], v[68:71]\n\t"
      "v_pk_mul_f32 v[76:77], v[76:77], v[10:11] op_sel:[0,0] op_sel_hi:[1,0]\n\t"
      "v_pk_mul_f32 v[78:79], v[78:79], v[10:11] op_sel:[0,0] op_sel_hi:[1,0]\n\t"
      "v_mfma_f32_16x16x16_bf16 v[72:75], v[148:149], v[16:17], v[72:75]\n\t"
      "v_pk_mul_f32 v[80:81], v[80:81], v[10:11] op_sel:[0,0] op_sel_hi:[1,0]\n\t"
      "v_pk_mul_f32 v[82:83], v[82:83], v[10:11] op_sel:[0,0] op_sel_hi:[1,0]\n\t"
      "v_mfma_f32_16x16x16_bf16 v[76:79], v[150:151], v[16:17], v[76:79]\n\t"
      "s_add_u32 s69, s50, 16384\n\t"
      "buffer_load_dwordx4 a[160:163], v248, s[36:39], s69 offen  sc1\n\t"
      "v_pk_mul_f32 v[84:85], v[84:85], v[10:11] op_sel:[0,0] op_sel_hi:[1,0]\n\t"
      "v_pk_mul_f32 v[86:87], v[86:87], v[10:11] op_sel:[0,0] op_sel_hi:[1,0]\n\t"
      "v_mfma_f32_16x16x16_bf16 v[80:83], v[152:153], v[16:17], v[80:83]\n\t"
      "v_pk_mul_f32 v[88:89], v[88:89], v[10:11] op_sel:[0,0] op_sel_hi:[1,0]\n\t"
      "v_pk_mul_f32 v[90:91], v[90:91], v[10:11] op_sel:[0,0] op_sel_hi:[1,0]\n\t"
      "v_mfma_f32_16x16x16_bf16 v[84:87], v[154:155], v[16:17], v[84:87]\n\t"
      "v_pk_mul_f32 v[92:93], v[92:93], v[10:11] op_sel:[0,0] op_sel_hi:[1,0]\n\t"
      "buffer_load_dwordx4 a[164:167], v248, s[36:39], s69 offen offset:1024  sc1\n\t"
      "v_pk_mul_f32 v[94:95], v[94:95], v[10:11] op_sel:[0,0] op_sel_hi:[1,0]\n\t"
      "v_cvt_pk_bf16_f32 v24, v64, v65\n\t"
      "v_cvt_pk_bf16_f32 v25, v66, v67\n\t"
      "v_cvt_pk_bf16_f32 v26, v68, v69\n\t"
      "v_cvt_pk_bf16_f32 v27, v70, v71\n\t"
      "ds_write_b128 v254, v[24:27]\n\t"
      "v_mfma_f32_16x16x16_bf16 v[88:91], v[156:157], v[16:17], v[88:91]\n\t"
      "v_pk_mul_f32 v[96:97], v[96:97], v[10:11] op_sel:[0,1] op_sel_hi:[1,1]\n\t"
      "buffer_load_dwordx4 a[168:171], v248, s[36:39], s69 offen offset:2048  sc1\n\t"
      "v_pk_mul_f32 v[98:99], v[98:99], v[10:11] op_sel:[0,1] op_sel_hi:[1,1]\n\t"
      "v_mfma_f32_16x16x16_bf16 v[92:95], v[158:159], v[16:17], v[92:95]\n\t"
      "v_pk_mul_f32 v[100:101], v[100:101], v[10:11] op_sel:[0,1] op_sel_hi:[1,1]\n\t"
      "v_pk_mul_f32 v[102:103], v[102:103], v[10:11] op_sel:[0,1] op_sel_hi:[1,1]\n\t"
      "v_cvt_pk_bf16_f32 v28, v72, v73\n\t"
      "v_cvt_pk_bf16_f32 v29, v74, v75\n\t"
      "v_cvt_pk_bf16_f32 v30, v76, v77\n\t"
      "v_cvt_pk_bf16_f32 v31, v78, v79\n\t"
      "ds_write_b128 v254, v[28:31] offset:2048\n\t"
      "buffer_load_dwordx4 a[172:175], v248, s[36:39], s69 offen offset:3072  sc1\n\t"
      "v_mfma_f32_16x16x16_bf16 v[96:99], v[144:145], v[18:19], v[96:99]\n\t"
      "v_pk_mul_f32 v[104:105], v[104:105], v[10:11] op_sel:[0,1] op_sel_hi:[1,1]\n\t"
      "v_pk_mul_f32 v[106:107], v[106:107], v[10:11] op_sel:[0,1] op_sel_hi:[1,1]\n\t"
      "v_mfma_f32_16x16x16_bf16 v[100:103], v[146:147], v[18:19], v[100:103]\n\t"
      "v_pk_mul_f32 v[108:109], v[108:109], v[10:11] op_sel:[0,1] op_sel_hi:[1,1]\n\t"
      "v_pk_mul_f32 v[110:111], v[110:111], v[10:11] op_sel:[0,1] op_sel_hi:[1,1]\n\t"
      "v_cvt_pk_bf16_f32 v24, v80, v81\n\t"
      "v_cvt_pk_bf16_f32 v25, v82, v83\n\t"
      "s_add_u32 s69, s50, 20480\n\t"
      "buffer_load_dwordx4 a[176:179], v248, s[36:39], s69 offen  sc1\n\t"
      "v_cvt_pk_bf16_f32 v26, v84, v85\n\t"
      "v_cvt_pk_bf16_f32 v27, v86, v87\n\t"
      "ds_write_b128 v254, v[24:27] offset:4096\n\t"
      "v_mfma_f32_16x16x16_bf16 v[104:107], v[148:149], v[18:19], v[104:107]\n\t"
      "v_pk_mul_f32 v[112:113], v[112:113], v[10:11] op_sel:[0,1] op_sel_hi:[1,1]\n\t"
      "v_pk_mul_f32 v[114:115], v[114:115], v[10:11] op_sel:[0,1] op_sel_hi:[1,1]\n\t"
      "v_mfma_f32_16x16x16_bf16 v[108:111], v[150:151], v[18:19], v[108:111]\n\t"
      "buffer_load_dwordx4 a[180:183], v248, s[36:39], s69 offen offset:1024  sc1\n\t"
      "v_pk_mul_f32 v[116:117], v[116:117], v[10:11] op_sel:[0,1] op_sel_hi:[1,1]\n\t"
      "v_pk_mul_f32 v[118:119], v[118:119], v[10:11] op_sel:[0,1] op_sel_hi:[1,1]\n\t"
      "v_cvt_pk_bf16_f32 v28, v88, v89\n\t"
      "v_cvt_pk_bf16_f32 v29, v90, v91\n\t"
      "v_cvt_pk_bf16_f32 v30, v92, v93\n\t"
      "v_cvt_pk_bf16_f32 v31, v94, v95\n\t"
      "ds_write_b128 v254, v[28:31] offset:6144\n\t"
      "v_mfma_f32_16x16x16_bf16 v[112:115], v[152:153], v[18:19], v[112:115]\n\t"
      "v_pk_mul_f32 v[120:121], v[120:121], v[10:11] op_sel:[0,1] op_sel_hi:[1,1]\n\t"
      "buffer_load_dwordx4 a[184:187], v248, s[36:39], s69 offen offset:2048  sc1\n\t"
      "v_pk_mul_f32 v[122:123], v[122:123], v[10:11] op_sel:[0,1] op_sel_hi:[1,1]\n\t"
      "v_mfma_f32_16x16x16_bf16 v[116:119], v[154:155], v[18:19], v[116:119]\n\t"
      "v_pk_mul_f32 v[124:125], v[124:125], v[10:11] op_sel:[0,1] op_sel_hi:[1,1]\n\t"
      "v_pk_mul_f32 v[126:127], v[126:127], v[10:11] op_sel:[0,1] op_sel_hi:[1,1]\n\t"
      "v_cvt_pk_bf16_f32 v24, v96, v97\n\t"
      "v_cvt_pk_bf16_f32 v25, v98, v99\n\t"
      "v_cvt_pk_bf16_f32 v26, v100, v101\n\t"
      "v_cvt_pk_bf16_f32 v27, v102, v103\n\t"
      "ds_write_b128 v254, v[24:27] offset:1024\n\t"
      "buffer_load_dwordx4 a[188:191], v248, s[36:39], s69 offen offset:3072  sc1\n\t"
      "v_mfma_f32_16x16x16_bf16 v[120:123], v[156:157], v[18:19], v[120:123]\n\t"
      "v_mfma_f32_16x16x16_bf16 v[124:127], v[158:159], v[18:19], v[124:127]\n\t"
      "v_cvt_pk_bf16_f32 v28, v104, v105\n\t"
      "v_cvt_pk_bf16_f32 v29, v106, v107\n\t"
      "v_cvt_pk_bf16_f32 v30, v108, v109\n\t"
      "v_cvt_pk_bf16_f32 v31, v110, v111\n\t"
      "ds_write_b128 v254, v[28:31] offset:3072\n\t"
      "v_cvt_pk_bf16_f32 v24, v112, v113\n\t"
      "v_cvt_pk_bf16_f32 v25, v114, v115\n\t"
      "v_cvt_pk_bf16_f32 v26, v116, v117\n\t"
      "v_cvt_pk_bf16_f32 v27, v118, v119\n\t"
      "ds_write_b128 v254, v[24:27] offset:5120\n\t"
      "v_cvt_pk_bf16_f32 v28, v120, v121\n\t"
      "v_cvt_pk_bf16_f32 v29, v122, v123\n\t"
      "v_cvt_pk_bf16_f32 v30, v124, v125\n\t"
      "v_cvt_pk_bf16_f32 v31, v126, v127\n\t"
      "ds_write_b128 v254, v[28:31] offset:7168\n\t"
      "s_waitcnt vmcnt(40) lgkmcnt(0)\n\t"
      "s_barrier\n\t"
      "s_branch WO%=\n\t"
      ".p2align 5\n\t"
      "WOL1%=:\n\t"
      "buffer_load_dwordx4 v[128:131], v252, s[36:39], s51 offen sc1\n\t"
      "s_waitcnt lgkmcnt(11)\n\t"
      "v_mfma_f32_16x16x32_bf16 v[0:3], a[0:3], v[64:67], 0\n\t"
      "v_mfma_f32_16x16x32_bf16 v[4:7], a[64:67], v[64:67], 0\n\t"
      "ds_read_b128 v[112:115], v255 offset:12288\n\t"
      "s_waitcnt lgkmcnt(11)\n\t"
      "v_mfma_f32_16x16x32_bf16 v[8:11], a[0:3], v[68:71], 0\n\t"
      "v_mfma_f32_16x16x32_bf16 v[12:15], a[64:67], v[68:71], 0\n\t"
      "ds_read_b128 v[116:119], v255 offset:13312\n\t"
      "s_waitcnt lgkmcnt(11)\n\t"
      "v_mfma_f32_16x16x32_bf16 v[0:3], a[4:7], v[72:75], v[0:3]\n\t"
      "v_mfma_f32_16x16x32_bf16 v[4:7], a[68:71], v[72:75], v[4:7]\n\t"
      "ds_read_b128 v[120:123], v255 offset:14336\n\t"
      "s_waitcnt lgkmcnt(11)\n\t"
      "v_mfma_f32_16x16x32_bf16 v[8:11], a[4:7], v[76:79], v[8:11]\n\t"
      "v_mfma_f32_16x16x32_bf16 v[12:15], a[68:71], v[76:79], v[12:15]\n\t"
      "buffer_load_dwordx4 v[132:135], v252, s[36:39], s51 offen offset:512 sc1\n\t"
      "ds_read_b128 v[124:127], v255 offset:15360\n\t"
      "s_waitcnt lgkmcnt(11)\n\t"
      "v_mfma_f32_16x16x32_bf16 v[0:3], a[8:11], v[80:83], v[0:3]\n\t"
      "v_mfma_f32_16x16x32_bf16 v[4:7], a[72:75], v[80:83], v[4:7]\n\t"
      "ds_read_b128 v[64:67], v255 offset:16384\n\t"
      "s_waitcnt lgkmcnt(11)\n\t"
      "v_mfma_f32_16x16x32_bf16 v[8:11], a[8:11], v[84:87], v[8:11]\n\t"
      "v_mfma_f32_16x16x32_bf16 v[12:15], a[72:75], v[84:87], v[12:15]\n\t"
      "ds_read_b128 v[68:71], v255 offset:17408\n\t"
      "s_waitcnt lgkmcnt(11)\n\t"
      "v_mfma_f32_16x16x32_bf16 v[0:3], a[12:15], v[88:91], v[0:3]\n\t"
      "v_mfma_f32_16x16x32_bf16 v[4:7], a[76:79], v[88:91], v[4:7]\n\t"
      "ds_read_b128 v[72:75], v255 offset:18432\n\t"
      "s_waitcnt lgkmcnt(11)\n\t"
      "v_mfma_f32_16x16x32_bf16 v[8:11], a[12:15], v[92:95], v[8:11]\n\t"
      "v_mfma_f32_16x16x32_bf16 v[12:15], a[76:79], v[92:95], v[12:15]\n\t"
      "buffer_load_dwordx4 v[136:139], v252, s[36:39], s51 offen offset:1024 sc1\n\t"
      "ds_read_b128 v[76:79], v255 offset:19456\n\t"
      "s_waitcnt lgkmcnt(11)\n\t"
      "v_mfma_f32_16x16x32_bf16 v[0:3], a[16:19], v[96:99], v[0:3]\n\t"
      "v_mfma_f32_16x16x32_bf16 v[4:7], a[80:83], v[96:99], v[4:7]\n\t"
      "ds_read_b128 v[80:83], v255 offset:20480\n\t"
      "s_waitcnt lgkmcnt(11)\n\t"
      "v_mfma_f32_16x16x32_bf16 v[8:11], a[16:19], v[100:103], v[8:11]\n\t"
      "v_mfma_f32_16x16x32_bf16 v[12:15], a[80:83], v[100:103], v[12:15]\n\t"
      "ds_read_b128 v[84:87], v255 offset:21504\n\t"
      "s_waitcnt lgkmcnt(11)\n\t"
      "v_mfma_f32_16x16x32_bf16 v[0:3], a[20:23], v[104:107], v[0:3]\n\t"
      "v_mfma_f32_16x16x32_bf16 v[4:7], a[84:87], v[104:107], v[4:7]\n\t"
      "ds_read_b128 v[88:91], v255 offset:22528\n\t"
      "s_waitcnt lgkmcnt(11)\n\t"
      "v_mfma_f32_16x16x32_bf16 v[8:11], a[20:23], v[108:111], v[8:11]\n\t"
      "v_mfma_f32_16x16x32_bf16 v[12:15], a[84:87], v[108:111], v[12:15]\n\t"
      "buffer_load_dwordx4 v[140:143], v252, s[36:39], s51 offen offset:1536 sc1\n\t"
      "ds_read_b128 v[92:95], v255 offset:23552\n\t"
      "s_waitcnt lgkmcnt(11)\n\t"
      "v_mfma_f32_16x16x32_bf16 v[0:3], a[24:27], v[112:115], v[0:3]\n\t"
      "v_mfma_f32_16x16x32_bf16 v[4:7], a[88:91], v[112:115], v[4:7]\n\t"
      "ds_read_b128 v[96:99], v255 offset:24576\n\t"
      "s_waitcnt lgkmcnt(11)\n\t"
      "v_mfma_f32_16x16x32_bf16 v[8:11], a[24:27], v[116:119], v[8:11]\n\t"
      "v_mfma_f32_16x16x32_bf16 v[12:15], a[88:91], v[116:119], v[12:15]\n\t"
      "ds_read_b128 v[100:103], v255 offset:25600\n\t"
      "s_waitcnt lgkmcnt(11)\n\t"
      "v_mfma_f32_16x16x32_bf16 v[0:3], a[28:31], v[120:123], v[0:3]\n\t"
      "v_mfma_f32_16x16x32_bf16 v[4:7], a[92:95], v[120:123], v[4:7]\n\t"
      "ds_read_b128 v[104:107], v255 offset:26624\n\t"
      "s_waitcnt lgkmcnt(11)\n\t"
      "v_mfma_f32_16x16x32_bf16 v[8:11], a[28:31], v[124:127], v[8:11]\n\t"
      "buffer_load_dwordx4 v[144:147], v253, s[36:39], s52 offen sc1\n\t"
      "v_mfma_f32_16x16x32_bf16 v[12:15], a[92:95], v[124:127], v[12:15]\n\t"
      "ds_read_b128 v[108:111], v255 offset:27648\n\t"
      "s_waitcnt lgkmcnt(11)\n\t"
      "v_mfma_f32_16x16x32_bf16 v[0:3], a[32:35], v[64:67], v[0:3]\n\t"
      "v_mfma_f32_16x16x32_bf16 v[4:7], a[96:99], v[64:67], v[4:7]\n\t"
      "ds_read_b128 v[112:115], v255 offset:28672\n\t"
      "s_waitcnt lgkmcnt(11)\n\t"
      "v_mfma_f32_16x16x32_bf16 v[8:11], a[32:35], v[68:71], v[8:11]\n\t"
      "v_mfma_f32_16x16x32_bf16 v[12:15], a[96:99], v[68:71], v[12:15]\n\t"
      "ds_read_b128 v[116:119], v255 offset:29696\n\t"
      "s_waitcnt lgkmcnt(11)\n\t"
      "v_mfma_f32_16x16x32_bf16 v[0:3], a[36:39], v[72:75], v[0:3]\n\t"
      "v_mfma_f32_16x16x32_bf16 v[4:7], a[100:103], v[72:75], v[4:7]\n\t"
      "ds_read_b128 v[120:123], v255 offset:30720\n\t"
      "s_waitcnt lgkmcnt(11)\n\t"
      "v_mfma_f32_16x16x32_bf16 v[8:11], a[36:39], v[76:79], v[8:11]\n\t"
      "buffer_load_dwordx4 v[148:151], v253, s[36:39], s52 offen offset:512 sc1\n\t"
      "v_mfma_f32_16x16x32_bf16 v[12:15], a[100:103], v[76:79], v[12:15]\n\t"
      "ds_read_b128 v[124:127], v255 offset:31744\n\t"
      "s_waitcnt lgkmcnt(11)\n\t"
      "v_mfma_f32_16x16x32_bf16 v[0:3], a[40:43], v[80:83], v[0:3]\n\t"
      "v_mfma_f32_16x16x32_bf16 v[4:7], a[104:107], v[80:83], v[4:7]\n\t"
      "s_waitcnt lgkmcnt(10)\n\t"
      "v_mfma_f32_16x16x32_bf16 v[8:11], a[40:43], v[84:87], v[8:11]\n\t"
      "v_mfma_f32_16x16x32_bf16 v[12:15], a[104:107], v[84:87], v[12:15]\n\t"
      "s_waitcnt lgkmcnt(9)\n\t"
      "v_mfma_f32_16x16x32_bf16 v[0:3], a[44:47], v[88:91], v[0:3]\n\t"
      "v_mfma_f32_16x16x32_bf16 v[4:7], a[108:111], v[88:91], v[4:7]\n\t"
      "s_waitcnt lgkmcnt(8)\n\t"
      "v_mfma_f32_16x16x32_bf16 v[8:11], a[44:47], v[92:95], v[8:11]\n\t"
      "buffer_load_dwordx4 v[152:155], v253, s[36:39], s52 offen offset:1024 sc1\n\t"
      "v_mfma_f32_16x16x32_bf16 v[12:15], a[108:111], v[92:95], v[12:15]\n\t"
      "s_waitcnt lgkmcnt(7)\n\t"
      "v_mfma_f32_16x16x32_bf16 v[0:3], a[48:51], v[96:99], v[0:3]\n\t"
      "v_mfma_f32_16x16x32_bf16 v[4:7], a[112:115], v[96:99], v[4:7]\n\t"
      "s_waitcnt lgkmcnt(6)\n\t"
      "v_mfma_f32_16x16x32_bf16 v[8:11], a[48:51], v[100:103], v[8:11]\n\t"
      "v_mfma_f32_16x16x32_bf16 v[12:15], a[112:115], v[100:103], v[12:15]\n\t"
      "s_waitcnt lgkmcnt(5)\n\t"
      "v_mfma_f32_16x16x32_bf16 v[0:3], a[52:55], v[104:107], v[0:3]\n\t"
      "v_mfma_f32_16x16x32_bf16 v[4:7], a[116:119], v[104:107], v[4:7]\n\t"
      "s_waitcnt lgkmcnt(4)\n\t"
      "v_mfma_f32_16x16x32_bf16 v[8:11], a[52:55], v[108:111], v[8:11]\n\t"
      "v_mfma_f32_16x16x32_bf16 v[12:15], a[116:119], v[108:111], v[12:15]\n\t"
      "buffer_load_dwordx4 v[156:159], v253, s[36:39], s52 offen offset:1536 sc1\n\t"
      "s_waitcnt lgkmcnt(3)\n\t"
      "v_mfma_f32_16x16x32_bf16 v[0:3], a[56:59], v[112:115], v[0:3]\n\t"
      "v_mfma_f32_16x16x32_bf16 v[4:7], a[120:123], v[112:115], v[4:7]\n\t"
      "s_waitcnt lgkmcnt(2)\n\t"
      "v_mfma_f32_16x16x32_bf16 v[8:11], a[56:59], v[116:119], v[8:11]\n\t"
      "v_mfma_f32_16x16x32_bf16 v[12:15], a[120:123], v[116:119], v[12:15]\n\t"
      "s_waitcnt lgkmcnt(1)\n\t"
      "v_mfma_f32_16x16x32_bf16 v[0:3], a[60:63], v[120:123], v[0:3]\n\t"
      "v_mfma_f32_16x16x32_bf16 v[4:7], a[124:127], v[120:123], v[4:7]\n\t"
      "s_waitcnt lgkmcnt(0)\n\t"
      "v_mfma_f32_16x16x32_bf16 v[8:11], a[60:63], v[124:127], v[8:11]\n\t"
      "v_mfma_f32_16x16x32_bf16 v[12:15], a[124:127], v[124:127], v[12:15]\n\t"
      "v_xor_b32 v254, 0x8000, v254\n\t"
      "v_xor_b32 v255, 0x8000, v255\n\t"
      "s_add_u32 s69, s53, 0x4000\n\t"
      "s_add_u32 s48, s48, 4\n\t"
      "s_waitcnt vmcnt(40)\n\t"
      "v_cvt_pk_bf16_f32 v0, v0, v1\n\t"
      "v_cvt_pk_bf16_f32 v1, v2, v3\n\t"
      "v_cvt_pk_bf16_f32 v2, v4, v5\n\t"
      "v_cvt_pk_bf16_f32 v3, v6, v7\n\t"
      "v_min_u32 v0, -2, v0\n\t"
      "v_min_u32 v1, -2, v1\n\t"
      "v_min_u32 v2, -2, v2\n\t"
      "v_min_u32 v3, -2, v3\n\t"
      "buffer_store_dwordx4 v[0:3], v238, s[36:39], s53 offen sc1\n\t"
      "v_cvt_pk_bf16_f32 v8, v8, v9\n\t"
      "v_cvt_pk_bf16_f32 v9, v10, v11\n\t"
      "v_cvt_pk_bf16_f32 v10, v12, v13\n\t"
      "v_cvt_pk_bf16_f32 v11, v14, v15\n\t"
      "v_min_u32 v8, -2, v8\n\t"
      "v_min_u32 v9, -2, v9\n\t"
      "v_min_u32 v10, -2, v10\n\t"
      "v_min_u32 v11, -2, v11\n\t"
      "buffer_store_dwordx4 v[8:11], v238, s[36:39], s69 offen sc1\n\t"
      "s_add_u32 s53, s53, 1048576\n\t"
      "s_nop 1\n\t"
      "s_branch WTOP%=\n\t"
      ".p2align 5\n\t"
      "WR2%=:\n\t"
      "v_cvt_pk_bf16_f32 v204, v204, v205\n\t"
      "v_and_b32 v205, s74, v204\n\t"
      "v_lshlrev_b32 v204, 16, v204\n\t"
      "v_add_f32 v205, 0x3727c5ac, v205\n\t"
      "v_pk_add_f32 v[40:41], v[192:193], v[204:205] op_sel_hi:[1,0] neg_lo:[0,1] neg_hi:[0,1]\n\t"
      "v_pk_add_f32 v[42:43], v[194:195], v[204:205] op_sel_hi:[1,0] neg_lo:[0,1] neg_hi:[0,1]\n\t"
      "v_pk_add_f32 v[44:45], v[196:197], v[204:205] op_sel_hi:[1,0] neg_lo:[0,1] neg_hi:[0,1]\n\t"
      "v_pk_add_f32 v[46:47], v[198:199], v[204:205] op_sel_hi:[1,0] neg_lo:[0,1] neg_hi:[0,1]\n\t"
      "v_cvt_pk_bf16_f32 v205, v205, v205\n\t"
      "v_and_b32 v205, s74, v205\n\t"
      "v_sqrt_f32 v205, v205\n\t"
      "v_cvt_pk_bf16_f32 v40, v40, v41\n\t"
      "v_cvt_pk_bf16_f32 v42, v42, v43\n\t"
      "v_cvt_pk_bf16_f32 v44, v44, v45\n\t"
      "v_cvt_pk_bf16_f32 v46, v46, v47\n\t"
      "v_cvt_pk_bf16_f32 v205, v205, v205\n\t"
      "v_and_b32 v205, s74, v205\n\t"
      "v_rcp_f32 v204, v205\n\t"
      "v_and_b32 v41, s74, v40\n\t"
      "v_lshlrev_b32 v40, 16, v40\n\t"
      "v_and_b32 v43, s74, v42\n\t"
      "v_lshlrev_b32 v42, 16, v42\n\t"
      "v_and_b32 v45, s74, v44\n\t"
      "v_lshlrev_b32 v44, 16, v44\n\t"
      "v_and_b32 v47, s74, v46\n\t"
      "v_lshlrev_b32 v46, 16, v46\n\t"
      "v_pk_mul_f32 v[40:41], v[40:41], v[204:205] op_sel_hi:[1,0]\n\t"
      "v_pk_mul_f32 v[42:43], v[42:43], v[204:205] op_sel_hi:[1,0]\n\t"
      "v_pk_mul_f32 v[44:45], v[44:45], v[204:205] op_sel_hi:[1,0]\n\t"
      "v_pk_mul_f32 v[46:47], v[46:47], v[204:205] op_sel_hi:[1,0]\n\t"
      "v_cvt_pk_bf16_f32 v52, v40, v41\n\t"
      "v_cvt_pk_bf16_f32 v53, v42, v43\n\t"
      "v_cvt_pk_bf16_f32 v54, v44, v45\n\t"
      "v_cvt_pk_bf16_f32 v55, v46, v47\n\t"
      "v_and_b32 v33, s74, v52\n\t"
      "v_lshlrev_b32 v32, 16, v52\n\t"
      "v_and_b32 v35, s74, v53\n\t"
      "v_lshlrev_b32 v34, 16, v53\n\t"
      "v_and_b32 v37, s74, v54\n\t"
      "v_lshlrev_b32 v36, 16, v54\n\t"
      "v_and_b32 v39, s74, v55\n\t"
      "v_lshlrev_b32 v38, 16, v55\n\t"
      "v_pk_mul_f32 v[40:41], v[32:33], v[240:241]\n\t"
      "v_pk_fma_f32 v[40:41], v[34:35], v[242:243], v[40:41]\n\t"
      "v_pk_fma_f32 v[40:41], v[36:37], v[244:245], v[40:41]\n\t"
      "v_pk_fma_f32 v[40:41], v[38:39], v[246:247], v[40:41]\n\t"
      "v_add_f32 v40, v40, v41\n\t"
      "s_lshl_b32 s73, s49, 5\n\t"
      "s_add_u32 s73, s73, s60\n\t"
      "s_lshl_b32 s69, s73, 2\n\t"
      "s_add_u32 s69, s69, s57\n\t"
      "s_lshl_b32 s70, s73, 7\n\t"
      "s_add_u32 s70, s70, s57\n\t"
      "s_sub_u32 s70, s70, 212992\n\t"
      "s_cmp_ge_u32 s49, 56\n\t"
      "s_cselect_b32 s70, s70, s69\n\t"
      "v_add_f32_dpp v40, v40, v40 quad_perm:[1,0,3,2] row_mask:0xf bank_mask:0xf\n\t"
      "s_nop 1\n\t"
      "v_add_f32_dpp v40, v40, v40 quad_perm:[2,3,0,1] row_mask:0xf bank_mask:0xf\n\t"
      "s_nop 1\n\t"
      "v_add_f32_dpp v40, v40, v40 row_ror:4 row_mask:0xf bank_mask:0xf\n\t"
      "s_nop 1\n\t"
      "v_add_f32_dpp v40, v40, v40 row_ror:8 row_mask:0xf bank_mask:0xf\n\t"
      "v_mov_b32 v41, v40\n\t"
      "s_nop 1\n\t"
      "v_permlane16_swap_b32 v40, v41\n\t"
      "v_add_f32 v40, v40, v41\n\t"
      "v_mov_b32 v41, v40\n\t"
      "s_nop 1\n\t"
      "v_permlane32_swap_b32 v40, v41\n\t"
      "v_add_f32 v40, v40, v41\n\t"
      "v_cvt_pk_bf16_f32 v40, v40, v40\n\t"
      "v_and_b32 v40, s74, v40\n\t"
      "s_mov_b64 exec, 1\n\t"
      "buffer_store_dword v40, off, s[36:39], s70 sc1\n\t"
      "s_mov_b64 exec, -1\n\t"
      "v_pk_add_f32 v[192:193], v[192:193], v[32:33]\n\t"
      "v_pk_add_f32 v[194:195], v[194:195], v[34:35]\n\t"
      "v_pk_add_f32 v[196:197], v[196:197], v[36:37]\n\t"
      "v_pk_add_f32 v[198:199], v[198:199], v[38:39]\n\t"
      "v_readfirstlane_b32 s68, v40\n\t"
      "v_cvt_pk_bf16_f32 v56, v192, v193\n\t"
      "v_cvt_pk_bf16_f32 v57, v194, v195\n\t"
      "v_cvt_pk_bf16_f32 v58, v196, v197\n\t"
      "v_cvt_pk_bf16_f32 v59, v198, v199\n\t"
      "s_add_u32 s67, s69, 8192\n\t"
      "s_lshl_b32 s73, s73, 10\n\t"
      "s_add_u32 s69, s73, s58\n\t"
      "s_add_u32 s70, s73, s59\n\t"
      "buffer_store_dwordx2 v[56:57], v249, s[44:47], s73 offen sc1\n\t"
      "buffer_store_dwordx2 v[52:53], v249, s[36:39], s69 offen sc1\n\t"
      "buffer_store_dwordx2 v[200:201], v249, s[36:39], s70 offen sc1\n\t"
      "buffer_store_dwordx2 v[58:59], v249, s[44:47], s73 offen offset:512 sc1\n\t"
      "buffer_store_dwordx2 v[54:55], v249, s[36:39], s69 offen offset:512 sc1\n\t"
      "buffer_store_dwordx2 v[202:203], v249, s[36:39], s70 offen offset:512 sc1\n\t"
      "s_mov_b32 s49, s89\n\t"
      "s_add_u32 s89, s89, 16\n\t"
      "s_lshr_b32 s69, s89, 4\n\t"
      "s_cmp_eq_u32 s69, 3\n\t"
      "s_cselect_b32 s89, s93, s89\n\t"
      "s_cmp_ge_u32 s49, 48\n\t"
      "s_cselect_b32 s60, s94, s60\n\t"
      "s_cselect_b32 s54, s95, s54\n\t"
      "s_mov_b32 s65, 0\n\t"
      "s_branch WGO2%=\n\t"
      ".p2align 5\n\t"
      "WOM1%=:\n\t"
      "s_mov_b32 s65, 2\n\t"
      "s_cmp_lg_u32 s96, 0\n\t"
      "s_cbranch_scc1 WOL3%=\n\t"
      "WOL2%=:\n\t"
      "s_waitcnt lgkmcnt(11)\n\t"
      "v_mfma_f32_16x16x32_bf16 v[0:3], a[0:3], v[64:67], 0\n\t"
      "v_mfma_f32_16x16x32_bf16 v[16:19], a[224:227], v[160:163], 0\n\t"
      "v_mfma_f32_16x16x32_bf16 v[4:7], a[64:67], v[64:67], 0\n\t"
      "v_mfma_f32_16x16x32_bf16 v[20:23], a[228:231], v[160:163], 0\n\t"
      "v_and_b32 v193, s74, v224\n\t"
      "v_lshlrev_b32 v192, 16, v224\n\t"
      "ds_read_b128 v[112:115], v255 offset:12288\n\t"
      "s_waitcnt lgkmcnt(11)\n\t"
      "v_mfma_f32_16x16x32_bf16 v[8:11], a[0:3], v[68:71], 0\n\t"
      "v_mfma_f32_16x16x32_bf16 v[16:19], a[224:227], v[164:167], v[16:19]\n\t"
      "v_mfma_f32_16x16x32_bf16 v[12:15], a[64:67], v[68:71], 0\n\t"
      "v_mfma_f32_16x16x32_bf16 v[20:23], a[228:231], v[164:167], v[20:23]\n\t"
      "v_and_b32 v195, s74, v225\n\t"
      "v_lshlrev_b32 v194, 16, v225\n\t"
      "ds_read_b128 v[116:119], v255 offset:13312\n\t"
      "s_waitcnt lgkmcnt(11)\n\t"
      "v_mfma_f32_16x16x32_bf16 v[0:3], a[4:7], v[72:75], v[0:3]\n\t"
      "v_mfma_f32_16x16x32_bf16 v[16:19], a[224:227], v[168:171], v[16:19]\n\t"
      "v_mfma_f32_16x16x32_bf16 v[4:7], a[68:71], v[72:75], v[4:7]\n\t"
      "v_mfma_f32_16x16x32_bf16 v[20:23], a[228:231], v[168:171], v[20:23]\n\t"
      "v_and_b32 v197, s74, v226\n\t"
      "v_lshlrev_b32 v196, 16, v226\n\t"
      "ds_read_b128 v[120:123], v255 offset:14336\n\t"
      "s_waitcnt lgkmcnt(11)\n\t"
      "v_mfma_f32_16x16x32_bf16 v[8:11], a[4:7], v[76:79], v[8:11]\n\t"
      "v_mfma_f32_16x16x32_bf16 v[16:19], a[224:227], v[172:175], v[16:19]\n\t"
      "v_mfma_f32_16x16x32_bf16 v[12:15], a[68:71], v[76:79], v[12:15]\n\t"
      "v_mfma_f32_16x16x32_bf16 v[20:23], a[228:231], v[172:175], v[20:23]\n\t"
      "v_and_b32 v199, s74, v227\n\t"
      "v_lshlrev_b32 v198, 16, v227\n\t"
      "ds_read_b128 v[124:127], v255 offset:15360\n\t"
      "s_waitcnt lgkmcnt(11)\n\t"
      "v_mfma_f32_16x16x32_bf16 v[0:3], a[8:11], v[80:83], v[0:3]\n\t"
      "v_mfma_f32_16x16x32_bf16 v[16:19], a[224:227], v[176:179], v[16:19]\n\t"
      "v_mfma_f32_16x16x32_bf16 v[4:7], a[72:75], v[80:83], v[4:7]\n\t"
      "v_mfma_f32_16x16x32_bf16 v[20:23], a[228:231], v[176:179], v[20:23]\n\t"
      "ds_read_b128 v[64:67], v255 offset:16384\n\t"
      "s_waitcnt lgkmcnt(11)\n\t"
      "v_mfma_f32_16x16x32_bf16 v[8:11], a[8:11], v[84:87], v[8:11]\n\t"
      "v_mfma_f32_16x16x32_bf16 v[16:19], a[224:227], v[180:183], v[16:19]\n\t"
      "v_mfma_f32_16x16x32_bf16 v[12:15], a[72:75], v[84:87], v[12:15]\n\t"
      "v_mfma_f32_16x16x32_bf16 v[20:23], a[228:231], v[180:183], v[20:23]\n\t"
      "ds_read_b128 v[68:71], v255 offset:17408\n\t"
      "s_waitcnt lgkmcnt(11)\n\t"
      "v_mfma_f32_16x16x32_bf16 v[0:3], a[12:15], v[88:91], v[0:3]\n\t"
      "v_mfma_f32_16x16x32_bf16 v[16:19], a[224:227], v[184:187], v[16:19]\n\t"
      "v_mfma_f32_16x16x32_bf16 v[4:7], a[76:79], v[88:91], v[4:7]\n\t"
      "v_mfma_f32_16x16x32_bf16 v[20:23], a[228:231], v[184:187], v[20:23]\n\t"
      "ds_read_b128 v[72:75], v255 offset:18432\n\t"
      "s_waitcnt lgkmcnt(11)\n\t"
      "v_mfma_f32_16x16x32_bf16 v[8:11], a[12:15], v[92:95], v[8:11]\n\t"
      "v_mfma_f32_16x16x32_bf16 v[16:19], a[224:227], v[188:191], v[16:19]\n\t"
      "v_mfma_f32_16x16x32_bf16 v[12:15], a[76:79], v[92:95], v[12:15]\n\t"
      "v_mfma_f32_16x16x32_bf16 v[20:23], a[228:231], v[188:191], v[20:23]\n\t"
      "ds_read_b128 v[76:79], v255 offset:19456\n\t"
      "s_waitcnt lgkmcnt(11)\n\t"
      "v_mfma_f32_16x16x32_bf16 v[0:3], a[16:19], v[96:99], v[0:3]\n\t"
      "v_cvt_pk_bf16_f32 v32, v16, v17\n\t"
      "v_cvt_pk_bf16_f32 v34, v18, v19\n\t"
      "v_mfma_f32_16x16x32_bf16 v[4:7], a[80:83], v[96:99], v[4:7]\n\t"
      "ds_read_b128 v[80:83], v255 offset:20480\n\t"
      "s_waitcnt lgkmcnt(11)\n\t"
      "v_mfma_f32_16x16x32_bf16 v[8:11], a[16:19], v[100:103], v[8:11]\n\t"
      "v_cvt_pk_bf16_f32 v36, v20, v21\n\t"
      "v_cvt_pk_bf16_f32 v38, v22, v23\n\t"
      "v_mfma_f32_16x16x32_bf16 v[12:15], a[80:83], v[100:103], v[12:15]\n\t"
      "v_and_b32 v33, s74, v32\n\t"
      "v_lshlrev_b32 v32, 16, v32\n\t"
      "ds_read_b128 v[84:87], v255 offset:21504\n\t"
      "s_waitcnt lgkmcnt(11)\n\t"
      "v_mfma_f32_16x16x32_bf16 v[0:3], a[20:23], v[104:107], v[0:3]\n\t"
      "v_and_b32 v35, s74, v34\n\t"
      "v_lshlrev_b32 v34, 16, v34\n\t"
      "v_mfma_f32_16x16x32_bf16 v[4:7], a[84:87], v[104:107], v[4:7]\n\t"
      "v_and_b32 v37, s74, v36\n\t"
      "v_lshlrev_b32 v36, 16, v36\n\t"
      "ds_read_b128 v[88:91], v255 offset:22528\n\t"
      "s_waitcnt lgkmcnt(11)\n\t"
      "v_mfma_f32_16x16x32_bf16 v[8:11], a[20:23], v[108:111], v[8:11]\n\t"
      "v_and_b32 v39, s74, v38\n\t"
      "v_lshlrev_b32 v38, 16, v38\n\t"
      "v_mfma_f32_16x16x32_bf16 v[12:15], a[84:87], v[108:111], v[12:15]\n\t"
      "v_add_f32 v192, v192, v32\n\t"
      "v_add_f32 v193, v193, v33\n\t"
      "ds_read_b128 v[92:95], v255 offset:23552\n\t"
      "s_waitcnt lgkmcnt(11)\n\t"
      "v_mfma_f32_16x16x32_bf16 v[0:3], a[24:27], v[112:115], v[0:3]\n\t"
      "v_add_f32 v194, v194, v34\n\t"
      "v_add_f32 v195, v195, v35\n\t"
      "v_mfma_f32_16x16x32_bf16 v[4:7], a[88:91], v[112:115], v[4:7]\n\t"
      "v_add_f32 v196, v196, v36\n\t"
      "v_add_f32 v197, v197, v37\n\t"
      "ds_read_b128 v[96:99], v255 offset:24576\n\t"
      "s_waitcnt lgkmcnt(11)\n\t"
      "v_mfma_f32_16x16x32_bf16 v[8:11], a[24:27], v[116:119], v[8:11]\n\t"
      "v_add_f32 v198, v198, v38\n\t"
      "v_add_f32 v199, v199, v39\n\t"
      "v_mfma_f32_16x16x32_bf16 v[12:15], a[88:91], v[116:119], v[12:15]\n\t"
      "v_cvt_pk_bf16_f32 v200, v192, v193\n\t"
      "v_cvt_pk_bf16_f32 v201, v194, v195\n\t"
      "ds_read_b128 v[100:103], v255 offset:25600\n\t"
      "s_waitcnt lgkmcnt(11)\n\t"
      "v_mfma_f32_16x16x32_bf16 v[0:3], a[28:31], v[120:123], v[0:3]\n\t"
      "v_cvt_pk_bf16_f32 v202, v196, v197\n\t"
      "v_cvt_pk_bf16_f32 v203, v198, v199\n\t"
      "v_mfma_f32_16x16x32_bf16 v[4:7], a[92:95], v[120:123], v[4:7]\n\t"
      "v_and_b32 v193, s74, v200\n\t"
      "v_lshlrev_b32 v192, 16, v200\n\t"
      "ds_read_b128 v[104:107], v255 offset:26624\n\t"
      "s_waitcnt lgkmcnt(11)\n\t"
      "v_mfma_f32_16x16x32_bf16 v[8:11], a[28:31], v[124:127], v[8:11]\n\t"
      "v_and_b32 v195, s74, v201\n\t"
      "v_lshlrev_b32 v194, 16, v201\n\t"
      "v_mfma_f32_16x16x32_bf16 v[12:15], a[92:95], v[124:127], v[12:15]\n\t"
      "v_and_b32 v197, s74, v202\n\t"
      "v_lshlrev_b32 v196, 16, v202\n\t"
      "ds_read_b128 v[108:111], v255 offset:27648\n\t"
      "s_waitcnt lgkmcnt(11)\n\t"
      "v_mfma_f32_16x16x32_bf16 v[0:3], a[32:35], v[64:67], v[0:3]\n\t"
      "v_and_b32 v199, s74, v203\n\t"
      "v_lshlrev_b32 v198, 16, v203\n\t"
      "v_mfma_f32_16x16x32_bf16 v[4:7], a[96:99], v[64:67], v[4:7]\n\t"
      "v_add_f32 v32, v192, v196\n\t"
      "v_add_f32 v33, v193, v197\n\t"
      "ds_read_b128 v[112:115], v255 offset:28672\n\t"
      "s_waitcnt lgkmcnt(11)\n\t"
      "v_mfma_f32_16x16x32_bf16 v[8:11], a[32:35], v[68:71], v[8:11]\n\t"
      "v_add_f32 v34, v194, v198\n\t"
      "v_add_f32 v35, v195, v199\n\t"
      "v_mfma_f32_16x16x32_bf16 v[12:15], a[96:99], v[68:71], v[12:15]\n\t"
      "v_add_f32 v32, v32, v34\n\t"
      "v_add_f32 v33, v33, v35\n\t"
      "ds_read_b128 v[116:119], v255 offset:29696\n\t"
      "s_waitcnt lgkmcnt(11)\n\t"
      "v_mfma_f32_16x16x32_bf16 v[0:3], a[36:39], v[72:75], v[0:3]\n\t"
      "v_add_f32 v32, v32, v33\n\t"
      "v_mfma_f32_16x16x32_bf16 v[4:7], a[100:103], v[72:75], v[4:7]\n\t"
      "ds_read_b128 v[120:123], v255 offset:30720\n\t"
      "s_waitcnt lgkmcnt(11)\n\t"
      "v_mfma_f32_16x16x32_bf16 v[8:11], a[36:39], v[76:79], v[8:11]\n\t"
      "v_add_f32_dpp v32, v32, v32 quad_perm:[1,0,3,2] row_mask:0xf bank_mask:0xf\n\t"
      "v_mfma_f32_16x16x32_bf16 v[12:15], a[100:103], v[76:79], v[12:15]\n\t"
      "ds_read_b128 v[124:127], v255 offset:31744\n\t"
      "s_waitcnt lgkmcnt(11)\n\t"
      "v_mfma_f32_16x16x32_bf16 v[0:3], a[40:43], v[80:83], v[0:3]\n\t"
      "v_add_f32_dpp v32, v32, v32 quad_perm:[2,3,0,1] row_mask:0xf bank_mask:0xf\n\t"
      "v_mfma_f32_16x16x32_bf16 v[4:7], a[104:107], v[80:83], v[4:7]\n\t"
      "s_waitcnt lgkmcnt(10)\n\t"
      "v_mfma_f32_16x16x32_bf16 v[8:11], a[40:43], v[84:87], v[8:11]\n\t"
      "v_add_f32_dpp v32, v32, v32 row_ror:4 row_mask:0xf bank_mask:0xf\n\t"
      "v_mfma_f32_16x16x32_bf16 v[12:15], a[104:107], v[84:87], v[12:15]\n\t"
      "s_waitcnt lgkmcnt(9)\n\t"
      "v_mfma_f32_16x16x32_bf16 v[0:3], a[44:47], v[88:91], v[0:3]\n\t"
      "v_add_f32_dpp v32, v32, v32 row_ror:8 row_mask:0xf bank_mask:0xf\n\t"
      "v_mov_b32 v33, v32\n\t"
      "v_mfma_f32_16x16x32_bf16 v[4:7], a[108:111], v[88:91], v[4:7]\n\t"
      "s_waitcnt lgkmcnt(8)\n\t"
      "v_mfma_f32_16x16x32_bf16 v[8:11], a[44:47], v[92:95], v[8:11]\n\t"
      "v_permlane16_swap_b32 v32, v33\n\t"
      "v_add_f32 v32, v32, v33\n\t"
      "v_mfma_f32_16x16x32_bf16 v[12:15], a[108:111], v[92:95], v[12:15]\n\t"
      "v_mov_b32 v33, v32\n\t"
      "s_waitcnt lgkmcnt(7)\n\t"
      "v_mfma_f32_16x16x32_bf16 v[0:3], a[48:51], v[96:99], v[0:3]\n\t"
      "v_permlane32_swap_b32 v32, v33\n\t"
      "v_add_f32 v32, v32, v33\n\t"
      "v_mfma_f32_16x16x32_bf16 v[4:7], a[112:115], v[96:99], v[4:7]\n\t"
      "v_mul_f32 v204, 0x3b000000, v32\n\t"
      "v_sub_f32 v40, v192, v204\n\t"
      "s_waitcnt lgkmcnt(6)\n\t"
      "v_mfma_f32_16x16x32_bf16 v[8:11], a[48:51], v[100:103], v[8:11]\n\t"
      "v_sub_f32 v41, v193, v204\n\t"
      "v_sub_f32 v42, v194, v204\n\t"
      "v_mfma_f32_16x16x32_bf16 v[12:15], a[112:115], v[100:103], v[12:15]\n\t"
      "v_sub_f32 v43, v195, v204\n\t"
      "v_sub_f32 v44, v196, v204\n\t"
      "s_waitcnt lgkmcnt(5)\n\t"
      "v_mfma_f32_16x16x32_bf16 v[0:3], a[52:55], v[104:107], v[0:3]\n\t"
      "v_sub_f32 v45, v197, v204\n\t"
      "v_sub_f32 v46, v198, v204\n\t"
      "v_mfma_f32_16x16x32_bf16 v[4:7], a[116:119], v[104:107], v[4:7]\n\t"
      "v_sub_f32 v47, v199, v204\n\t"
      "v_mul_f32 v32, v40, v40\n\t"
      "s_waitcnt lgkmcnt(4)\n\t"
      "v_mfma_f32_16x16x32_bf16 v[8:11], a[52:55], v[108:111], v[8:11]\n\t"
      "v_mul_f32 v33, v41, v41\n\t"
      "v_fma_f32 v32, v42, v42, v32\n\t"
      "v_mfma_f32_16x16x32_bf16 v[12:15], a[116:119], v[108:111], v[12:15]\n\t"
      "v_fma_f32 v33, v43, v43, v33\n\t"
      "v_fma_f32 v32, v44, v44, v32\n\t"
      "s_waitcnt lgkmcnt(3)\n\t"
      "v_mfma_f32_16x16x32_bf16 v[0:3], a[56:59], v[112:115], v[0:3]\n\t"
      "v_fma_f32 v33, v45, v45, v33\n\t"
      "v_fma_f32 v32, v46, v46, v32\n\t"
      "v_mfma_f32_16x16x32_bf16 v[4:7], a[120:123], v[112:115], v[4:7]\n\t"
      "v_fma_f32 v33, v47, v47, v33\n\t"
      "v_add_f32 v32, v32, v33\n\t"
      "s_waitcnt lgkmcnt(2)\n\t"
      "v_mfma_f32_16x16x32_bf16 v[8:11], a[56:59], v[116:119], v[8:11]\n\t"
      "v_add_f32_dpp v32, v32, v32 quad_perm:[1,0,3,2] row_mask:0xf bank_mask:0xf\n\t"
      "v_mfma_f32_16x16x32_bf16 v[12:15], a[120:123], v[116:119], v[12:15]\n\t"
      "s_waitcnt lgkmcnt(1)\n\t"
      "v_mfma_f32_16x16x32_bf16 v[0:3], a[60:63], v[120:123], v[0:3]\n\t"
      "v_add_f32_dpp v32, v32, v32 quad_perm:[2,3,0,1] row_mask:0xf bank_mask:0xf\n\t"
      "v_mfma_f32_16x16x32_bf16 v[4:7], a[124:127], v[120:123], v[4:7]\n\t"
      "s_waitcnt lgkmcnt(0)\n\t"
      "v_mfma_f32_16x16x32_bf16 v[8:11], a[60:63], v[124:127], v[8:11]\n\t"
      "v_add_f32_dpp v32, v32, v32 row_ror:4 row_mask:0xf bank_mask:0xf\n\t"
      "v_mfma_f32_16x16x32_bf16 v[12:15], a[124:127], v[124:127], v[12:15]\n\t"
      "s_nop 0\n\t"
      "v_add_f32_dpp v32, v32, v32 row_ror:8 row_mask:0xf bank_mask:0xf\n\t"
      "v_mov_b32 v33, v32\n\t"
      "s_nop 1\n\t"
      "v_permlane16_swap_b32 v32, v33\n\t"
      "v_add_f32 v32, v32, v33\n\t"
      "v_mov_b32 v33, v32\n\t"
      "s_nop 1\n\t"
      "v_permlane32_swap_b32 v32, v33\n\t"
      "v_add_f32 v32, v32, v33\n\t"
      "v_mul_f32 v205, 0x3b000000, v32\n\t"
      "v_xor_b32 v254, 0x8000, v254\n\t"
      "v_xor_b32 v255, 0x8000, v255\n\t"
      "s_add_u32 s69, s53, 0x4000\n\t"
      "s_add_u32 s48, s48, 4\n\t"
      "s_waitcnt vmcnt(40)\n\t"
      "v_cvt_pk_bf16_f32 v0, v0, v1\n\t"
      "v_cvt_pk_bf16_f32 v1, v2, v3\n\t"
      "v_cvt_pk_bf16_f32 v2, v4, v5\n\t"
      "v_cvt_pk_bf16_f32 v3, v6, v7\n\t"
      "v_min_u32 v0, -2, v0\n\t"
      "v_min_u32 v1, -2, v1\n\t"
      "v_min_u32 v2, -2, v2\n\t"
      "v_min_u32 v3, -2, v3\n\t"
      "buffer_store_dwordx4 v[0:3], v238, s[36:39], s53 offen sc1\n\t"
      "v_cvt_pk_bf16_f32 v8, v8, v9\n\t"
      "v_cvt_pk_bf16_f32 v9, v10, v11\n\t"
      "v_cvt_pk_bf16_f32 v10, v12, v13\n\t"
      "v_cvt_pk_bf16_f32 v11, v14, v15\n\t"
      "v_min_u32 v8, -2, v8\n\t"
      "v_min_u32 v9, -2, v9\n\t"
      "v_min_u32 v10, -2, v10\n\t"
      "v_min_u32 v11, -2, v11\n\t"
      "buffer_store_dwordx4 v[8:11], v238, s[36:39], s69 offen sc1\n\t"
      "s_add_u32 s53, s53, 1048576\n\t"
      "s_nop 1\n\t"
      "s_branch WTOP%=\n\t"
      ".p2align 5\n\t"
      "WOL3%=:\n\t"
      "buffer_load_dwordx4 v[128:131], v252, s[36:39], s51 offen sc1\n\t"
      "s_waitcnt lgkmcnt(11)\n\t"
      "v_mfma_f32_16x16x32_bf16 v[0:3], a[0:3], v[64:67], 0\n\t"
      "v_mfma_f32_16x16x32_bf16 v[16:19], a[224:227], v[160:163], 0\n\t"
      "v_mfma_f32_16x16x32_bf16 v[4:7], a[64:67], v[64:67], 0\n\t"
      "v_mfma_f32_16x16x32_bf16 v[20:23], a[228:231], v[160:163], 0\n\t"
      "v_and_b32 v193, s74, v224\n\t"
      "v_lshlrev_b32 v192, 16, v224\n\t"
      "ds_read_b128 v[112:115], v255 offset:12288\n\t"
      "s_waitcnt lgkmcnt(11)\n\t"
      "v_mfma_f32_16x16x32_bf16 v[8:11], a[0:3], v[68:71], 0\n\t"
      "v_mfma_f32_16x16x32_bf16 v[16:19], a[224:227], v[164:167], v[16:19]\n\t"
      "v_mfma_f32_16x16x32_bf16 v[12:15], a[64:67], v[68:71], 0\n\t"
      "v_mfma_f32_16x16x32_bf16 v[20:23], a[228:231], v[164:167], v[20:23]\n\t"
      "v_and_b32 v195, s74, v225\n\t"
      "v_lshlrev_b32 v194, 16, v225\n\t"
      "ds_read_b128 v[116:119], v255 offset:13312\n\t"
      "s_waitcnt lgkmcnt(11)\n\t"
      "v_mfma_f32_16x16x32_bf16 v[0:3], a[4:7], v[72:75], v[0:3]\n\t"
      "v_mfma_f32_16x16x32_bf16 v[16:19], a[224:227], v[168:171], v[16:19]\n\t"
      "v_mfma_f32_16x16x32_bf16 v[4:7], a[68:71], v[72:75], v[4:7]\n\t"
      "v_mfma_f32_16x16x32_bf16 v[20:23], a[228:231], v[168:171], v[20:23]\n\t"
      "v_and_b32 v197, s74, v226\n\t"
      "v_lshlrev_b32 v196, 16, v226\n\t"
      "ds_read_b128 v[120:123], v255 offset:14336\n\t"
      "s_waitcnt lgkmcnt(11)\n\t"
      "v_mfma_f32_16x16x32_bf16 v[8:11], a[4:7], v[76:79], v[8:11]\n\t"
      "v_mfma_f32_16x16x32_bf16 v[16:19], a[224:227], v[172:175], v[16:19]\n\t"
      "v_mfma_f32_16x16x32_bf16 v[12:15], a[68:71], v[76:79], v[12:15]\n\t"
      "buffer_load_dwordx4 v[132:135], v252, s[36:39], s51 offen offset:512 sc1\n\t"
      "ds_read_b128 v[124:127], v255 offset:15360\n\t"
      "s_waitcnt lgkmcnt(11)\n\t"
      "v_mfma_f32_16x16x32_bf16 v[0:3], a[8:11], v[80:83], v[0:3]\n\t"
      "v_mfma_f32_16x16x32_bf16 v[20:23], a[228:231], v[172:175], v[20:23]\n\t"
      "v_and_b32 v199, s74, v227\n\t"
      "v_lshlrev_b32 v198, 16, v227\n\t"
      "v_mfma_f32_16x16x32_bf16 v[4:7], a[72:75], v[80:83], v[4:7]\n\t"
      "v_mfma_f32_16x16x32_bf16 v[16:19], a[224:227], v[176:179], v[16:19]\n\t"
      "ds_read_b128 v[64:67], v255 offset:16384\n\t"
      "s_waitcnt lgkmcnt(11)\n\t"
      "v_mfma_f32_16x16x32_bf16 v[8:11], a[8:11], v[84:87], v[8:11]\n\t"
      "v_mfma_f32_16x16x32_bf16 v[20:23], a[228:231], v[176:179], v[20:23]\n\t"
      "v_mfma_f32_16x16x32_bf16 v[12:15], a[72:75], v[84:87], v[12:15]\n\t"
      "v_mfma_f32_16x16x32_bf16 v[16:19], a[224:227], v[180:183], v[16:19]\n\t"
      "ds_read_b128 v[68:71], v255 offset:17408\n\t"
      "s_waitcnt lgkmcnt(11)\n\t"
      "v_mfma_f32_16x16x32_bf16 v[0:3], a[12:15], v[88:91], v[0:3]\n\t"
      "v_mfma_f32_16x16x32_bf16 v[20:23], a[228:231], v[180:183], v[20:23]\n\t"
      "v_mfma_f32_16x16x32_bf16 v[4:7], a[76:79], v[88:91], v[4:7]\n\t"
      "v_mfma_f32_16x16x32_bf16 v[16:19], a[224:227], v[184:187], v[16:19]\n\t"
      "ds_read_b128 v[72:75], v255 offset:18432\n\t"
      "s_waitcnt lgkmcnt(11)\n\t"
      "v_mfma_f32_16x16x32_bf16 v[8:11], a[12:15], v[92:95], v[8:11]\n\t"
      "v_mfma_f32_16x16x32_bf16 v[20:23], a[228:231], v[184:187], v[20:23]\n\t"
      "v_mfma_f32_16x16x32_bf16 v[12:15], a[76:79], v[92:95], v[12:15]\n\t"
      "buffer_load_dwordx4 v[136:139], v252, s[36:39], s51 offen offset:1024 sc1\n\t"
      "ds_read_b128 v[76:79], v255 offset:19456\n\t"
      "s_waitcnt lgkmcnt(11)\n\t"
      "v_mfma_f32_16x16x32_bf16 v[0:3], a[16:19], v[96:99], v[0:3]\n\t"
      "v_mfma_f32_16x16x32_bf16 v[16:19], a[224:227], v[188:191], v[16:19]\n\t"
      "v_mfma_f32_16x16x32_bf16 v[4:7], a[80:83], v[96:99], v[4:7]\n\t"
      "v_mfma_f32_16x16x32_bf16 v[20:23], a[228:231], v[188:191], v[20:23]\n\t"
      "ds_read_b128 v[80:83], v255 offset:20480\n\t"
      "s_waitcnt lgkmcnt(11)\n\t"
      "v_mfma_f32_16x16x32_bf16 v[8:11], a[16:19], v[100:103], v[8:11]\n\t"
      "v_cvt_pk_bf16_f32 v32, v16, v17\n\t"
      "v_cvt_pk_bf16_f32 v34, v18, v19\n\t"
      "v_mfma_f32_16x16x32_bf16 v[12:15], a[80:83], v[100:103], v[12:15]\n\t"
      "ds_read_b128 v[84:87], v255 offset:21504\n\t"
      "s_waitcnt lgkmcnt(11)\n\t"
      "v_mfma_f32_16x16x32_bf16 v[0:3], a[20:23], v[104:107], v[0:3]\n\t"
      "v_cvt_pk_bf16_f32 v36, v20, v21\n\t"
      "v_cvt_pk_bf16_f32 v38, v22, v23\n\t"
      "v_mfma_f32_16x16x32_bf16 v[4:7], a[84:87], v[104:107], v[4:7]\n\t"
      "v_and_b32 v33, s74, v32\n\t"
      "v_lshlrev_b32 v32, 16, v32\n\t"
      "ds_read_b128 v[88:91], v255 offset:22528\n\t"
      "s_waitcnt lgkmcnt(11)\n\t"
      "v_mfma_f32_16x16x32_bf16 v[8:11], a[20:23], v[108:111], v[8:11]\n\t"
      "v_and_b32 v35, s74, v34\n\t"
      "v_lshlrev_b32 v34, 16, v34\n\t"
      "v_mfma_f32_16x16x32_bf16 v[12:15], a[84:87], v[108:111], v[12:15]\n\t"
      "buffer_load_dwordx4 v[140:143], v252, s[36:39], s51 offen offset:1536 sc1\n\t"
      "ds_read_b128 v[92:95], v255 offset:23552\n\t"
      "s_waitcnt lgkmcnt(11)\n\t"
      "v_mfma_f32_16x16x32_bf16 v[0:3], a[24:27], v[112:115], v[0:3]\n\t"
      "v_and_b32 v37, s74, v36\n\t"
      "v_lshlrev_b32 v36, 16, v36\n\t"
      "v_mfma_f32_16x16x32_bf16 v[4:7], a[88:91], v[112:115], v[4:7]\n\t"
      "v_and_b32 v39, s74, v38\n\t"
      "v_lshlrev_b32 v38, 16, v38\n\t"
      "ds_read_b128 v[96:99], v255 offset:24576\n\t"
      "s_waitcnt lgkmcnt(11)\n\t"
      "v_mfma_f32_16x16x32_bf16 v[8:11], a[24:27], v[116:119], v[8:11]\n\t"
      "v_add_f32 v192, v192, v32\n\t"
      "v_add_f32 v193, v193, v33\n\t"
      "v_mfma_f32_16x16x32_bf16 v[12:15], a[88:91], v[116:119], v[12:15]\n\t"
      "v_add_f32 v194, v194, v34\n\t"
      "v_add_f32 v195, v195, v35\n\t"
      "ds_read_b128 v[100:103], v255 offset:25600\n\t"
      "s_waitcnt lgkmcnt(11)\n\t"
      "v_mfma_f32_16x16x32_bf16 v[0:3], a[28:31], v[120:123], v[0:3]\n\t"
      "v_add_f32 v196, v196, v36\n\t"
      "v_add_f32 v197, v197, v37\n\t"
      "v_mfma_f32_16x16x32_bf16 v[4:7], a[92:95], v[120:123], v[4:7]\n\t"
      "v_add_f32 v198, v198, v38\n\t"
      "v_add_f32 v199, v199, v39\n\t"
      "ds_read_b128 v[104:107], v255 offset:26624\n\t"
      "s_waitcnt lgkmcnt(11)\n\t"
      "v_mfma_f32_16x16x32_bf16 v[8:11], a[28:31], v[124:127], v[8:11]\n\t"
      "buffer_load_dwordx4 v[144:147], v253, s[36:39], s52 offen sc1\n\t"
      "v_mfma_f32_16x16x32_bf16 v[12:15], a[92:95], v[124:127], v[12:15]\n\t"
      "v_cvt_pk_bf16_f32 v200, v192, v193\n\t"
      "v_cvt_pk_bf16_f32 v201, v194, v195\n\t"
      "ds_read_b128 v[108:111], v255 offset:27648\n\t"
      "s_waitcnt lgkmcnt(11)\n\t"
      "v_mfma_f32_16x16x32_bf16 v[0:3], a[32:35], v[64:67], v[0:3]\n\t"
      "v_cvt_pk_bf16_f32 v202, v196, v197\n\t"
      "v_cvt_pk_bf16_f32 v203, v198, v199\n\t"
      "v_mfma_f32_16x16x32_bf16 v[4:7], a[96:99], v[64:67], v[4:7]\n\t"
      "v_and_b32 v193, s74, v200\n\t"
      "v_lshlrev_b32 v192, 16, v200\n\t"
      "ds_read_b128 v[112:115], v255 offset:28672\n\t"
      "s_waitcnt lgkmcnt(11)\n\t"
      "v_mfma_f32_16x16x32_bf16 v[8:11], a[32:35], v[68:71], v[8:11]\n\t"
      "v_and_b32 v195, s74, v201\n\t"
      "v_lshlrev_b32 v194, 16, v201\n\t"
      "v_mfma_f32_16x16x32_bf16 v[12:15], a[96:99], v[68:71], v[12:15]\n\t"
      "v_and_b32 v197, s74, v202\n\t"
      "v_lshlrev_b32 v196, 16, v202\n\t"
      "ds_read_b128 v[116:119], v255 offset:29696\n\t"
      "s_waitcnt lgkmcnt(11)\n\t"
      "v_mfma_f32_16x16x32_bf16 v[0:3], a[36:39], v[72:75], v[0:3]\n\t"
      "v_and_b32 v199, s74, v203\n\t"
      "v_lshlrev_b32 v198, 16, v203\n\t"
      "v_mfma_f32_16x16x32_bf16 v[4:7], a[100:103], v[72:75], v[4:7]\n\t"
      "v_add_f32 v32, v192, v196\n\t"
      "v_add_f32 v33, v193, v197\n\t"
      "ds_read_b128 v[120:123], v255 offset:30720\n\t"
      "s_waitcnt lgkmcnt(11)\n\t"
      "v_mfma_f32_16x16x32_bf16 v[8:11], a[36:39], v[76:79], v[8:11]\n\t"
      "buffer_load_dwordx4 v[148:151], v253, s[36:39], s52 offen offset:512 sc1\n\t"
      "v_mfma_f32_16x16x32_bf16 v[12:15], a[100:103], v[76:79], v[12:15]\n\t"
      "v_add_f32 v34, v194, v198\n\t"
      "v_add_f32 v35, v195, v199\n\t"
      "ds_read_b128 v[124:127], v255 offset:31744\n\t"
      "s_waitcnt lgkmcnt(11)\n\t"
      "v_mfma_f32_16x16x32_bf16 v[0:3], a[40:43], v[80:83], v[0:3]\n\t"
      "v_add_f32 v32, v32, v34\n\t"
      "v_add_f32 v33, v33, v35\n\t"
      "v_mfma_f32_16x16x32_bf16 v[4:7], a[104:107], v[80:83], v[4:7]\n\t"
      "v_add_f32 v32, v32, v33\n\t"
      "s_waitcnt lgkmcnt(10)\n\t"
      "v_mfma_f32_16x16x32_bf16 v[8:11], a[40:43], v[84:87], v[8:11]\n\t"
      "v_add_f32_dpp v32, v32, v32 quad_perm:[1,0,3,2] row_mask:0xf bank_mask:0xf\n\t"
      "v_mfma_f32_16x16x32_bf16 v[12:15], a[104:107], v[84:87], v[12:15]\n\t"
      "s_waitcnt lgkmcnt(9)\n\t"
      "v_mfma_f32_16x16x32_bf16 v[0:3], a[44:47], v[88:91], v[0:3]\n\t"
      "v_add_f32_dpp v32, v32, v32 quad_perm:[2,3,0,1] row_mask:0xf bank_mask:0xf\n\t"
      "v_mfma_f32_16x16x32_bf16 v[4:7], a[108:111], v[88:91], v[4:7]\n\t"
      "s_waitcnt lgkmcnt(8)\n\t"
      "v_mfma_f32_16x16x32_bf16 v[8:11], a[44:47], v[92:95], v[8:11]\n\t"
      "buffer_load_dwordx4 v[152:155], v253, s[36:39], s52 offen offset:1024 sc1\n\t"
      "v_mfma_f32_16x16x32_bf16 v[12:15], a[108:111], v[92:95], v[12:15]\n\t"
      "v_add_f32_dpp v32, v32, v32 row_ror:4 row_mask:0xf bank_mask:0xf\n\t"
      "s_waitcnt lgkmcnt(7)\n\t"
      "v_mfma_f32_16x16x32_bf16 v[0:3], a[48:51], v[96:99], v[0:3]\n\t"
      "v_add_f32_dpp v32, v32, v32 row_ror:8 row_mask:0xf bank_mask:0xf\n\t"
      "v_mov_b32 v33, v32\n\t"
      "v_mfma_f32_16x16x32_bf16 v[4:7], a[112:115], v[96:99], v[4:7]\n\t"
      "s_waitcnt lgkmcnt(6)\n\t"
      "v_mfma_f32_16x16x32_bf16 v[8:11], a[48:51], v[100:103], v[8:11]\n\t"
      "v_permlane16_swap_b32 v32, v33\n\t"
      "v_add_f32 v32, v32, v33\n\t"
      "v_mfma_f32_16x16x32_bf16 v[12:15], a[112:115], v[100:103], v[12:15]\n\t"
      "v_mov_b32 v33, v32\n\t"
      "s_waitcnt lgkmcnt(5)\n\t"
      "v_mfma_f32_16x16x32_bf16 v[0:3], a[52:55], v[104:107], v[0:3]\n\t"
      "v_permlane32_swap_b32 v32, v33\n\t"
      "v_add_f32 v32, v32, v33\n\t"
      "v_mfma_f32_16x16x32_bf16 v[4:7], a[116:119], v[104:107], v[4:7]\n\t"
      "v_mul_f32 v204, 0x3b000000, v32\n\t"
      "v_sub_f32 v40, v192, v204\n\t"
      "s_waitcnt lgkmcnt(4)\n\t"
      "v_mfma_f32_16x16x32_bf16 v[8:11], a[52:55], v[108:111], v[8:11]\n\t"
      "v_sub_f32 v41, v193, v204\n\t"
      "v_sub_f32 v42, v194, v204\n\t"
      "v_mfma_f32_16x16x32_bf16 v[12:15], a[116:119], v[108:111], v[12:15]\n\t"
      "buffer_load_dwordx4 v[156:159], v253, s[36:39], s52 offen offset:1536 sc1\n\t"
      "s_waitcnt lgkmcnt(3)\n\t"
      "v_mfma_f32_16x16x32_bf16 v[0:3], a[56:59], v[112:115], v[0:3]\n\t"
      "v_sub_f32 v43, v195, v204\n\t"
      "v_sub_f32 v44, v196, v204\n\t"
      "v_mfma_f32_16x16x32_bf16 v[4:7], a[120:123], v[112:115], v[4:7]\n\t"
      "v_sub_f32 v45, v197, v204\n\t"
      "v_sub_f32 v46, v198, v204\n\t"
      "s_waitcnt lgkmcnt(2)\n\t"
      "v_mfma_f32_16x16x32_bf16 v[8:11], a[56:59], v[116:119], v[8:11]\n\t"
      "v_sub_f32 v47, v199, v204\n\t"
      "v_mul_f32 v32, v40, v40\n\t"
      "v_mfma_f32_16x16x32_bf16 v[12:15], a[120:123], v[116:119], v[12:15]\n\t"
      "v_mul_f32 v33, v41, v41\n\t"
      "v_fma_f32 v32, v42, v42, v32\n\t"
      "s_waitcnt lgkmcnt(1)\n\t"
      "v_mfma_f32_16x16x32_bf16 v[0:3], a[60:63], v[120:123], v[0:3]\n\t"
      "v_fma_f32 v33, v43, v43, v33\n\t"
      "v_fma_f32 v32, v44, v44, v32\n\t"
      "v_mfma_f32_16x16x32_bf16 v[4:7], a[124:127], v[120:123], v[4:7]\n\t"
      "v_fma_f32 v33, v45, v45, v33\n\t"
      "v_fma_f32 v32, v46, v46, v32\n\t"
      "s_waitcnt lgkmcnt(0)\n\t"
      "v_mfma_f32_16x16x32_bf16 v[8:11], a[60:63], v[124:127], v[8:11]\n\t"
      "v_fma_f32 v33, v47, v47, v33\n\t"
      "v_add_f32 v32, v32, v33\n\t"
      "v_mfma_f32_16x16x32_bf16 v[12:15], a[124:127], v[124:127], v[12:15]\n\t"
      "s_nop 0\n\t"
      "v_add_f32_dpp v32, v32, v32 quad_perm:[1,0,3,2] row_mask:0xf bank_mask:0xf\n\t"
      "s_nop 1\n\t"
      "v_add_f32_dpp v32, v32, v32 quad_perm:[2,3,0,1] row_mask:0xf bank_mask:0xf\n\t"
      "s_nop 1\n\t"
      "v_add_f32_dpp v32, v32, v32 row_ror:4 row_mask:0xf bank_mask:0xf\n\t"
      "s_nop 1\n\t"
      "v_add_f32_dpp v32, v32, v32 row_ror:8 row_mask:0xf bank_mask:0xf\n\t"
      "v_mov_b32 v33, v32\n\t"
      "s_nop 1\n\t"
      "v_permlane16_swap_b32 v32, v33\n\t"
      "v_add_f32 v32, v32, v33\n\t"
      "v_mov_b32 v33, v32\n\t"
      "s_nop 1\n\t"
      "v_permlane32_swap_b32 v32, v33\n\t"
      "v_add_f32 v32, v32, v33\n\t"
      "v_mul_f32 v205, 0x3b000000, v32\n\t"
      "v_xor_b32 v254, 0x8000, v254\n\t"
      "v_xor_b32 v255, 0x8000, v255\n\t"
      "s_add_u32 s69, s53, 0x4000\n\t"
      "s_add_u32 s48, s48, 4\n\t"
      "s_waitcnt vmcnt(40)\n\t"
      "v_cvt_pk_bf16_f32 v0, v0, v1\n\t"
      "v_cvt_pk_bf16_f32 v1, v2, v3\n\t"
      "v_cvt_pk_bf16_f32 v2, v4, v5\n\t"
      "v_cvt_pk_bf16_f32 v3, v6, v7\n\t"
      "v_min_u32 v0, -2, v0\n\t"
      "v_min_u32 v1, -2, v1\n\t"
      "v_min_u32 v2, -2, v2\n\t"
      "v_min_u32 v3, -2, v3\n\t"
      "buffer_store_dwordx4 v[0:3], v238, s[36:39], s53 offen sc1\n\t"
      "v_cvt_pk_bf16_f32 v8, v8, v9\n\t"
      "v_cvt_pk_bf16_f32 v9, v10, v11\n\t"
      "v_cvt_pk_bf16_f32 v10, v12, v13\n\t"
      "v_cvt_pk_bf16_f32 v11, v14, v15\n\t"
      "v_min_u32 v8, -2, v8\n\t"
      "v_min_u32 v9, -2, v9\n\t"
      "v_min_u32 v10, -2, v10\n\t"
      "v_min_u32 v11, -2, v11\n\t"
      "buffer_store_dwordx4 v[8:11], v238, s[36:39], s69 offen sc1\n\t"
      "s_add_u32 s53, s53, 1048576\n\t"
      "s_nop 1\n\t"
      "s_branch WTOP%=\n\t"
      ".p2align 5\n\t"
      "WOM2%=:\n\t"
      "s_mov_b32 s65, 0\n\t"
      "s_add_u32 s24, s96, 8\n\t"
      "s_cmp_lg_u32 s96, 0\n\t"
      "s_cbranch_scc1 WOL5%=\n\t"
      "WOL4%=:\n\t"
      "s_waitcnt lgkmcnt(11)\n\t"
      "v_mfma_f32_16x16x32_bf16 v[0:3], a[0:3], v[64:67], 0\n\t"
      "v_cvt_pk_bf16_f32 v204, v204, v205\n\t"
      "v_and_b32 v205, s74, v204\n\t"
      "v_mfma_f32_16x16x32_bf16 v[4:7], a[64:67], v[64:67], 0\n\t"
      "v_lshlrev_b32 v204, 16, v204\n\t"
      "v_add_f32 v205, 0x3727c5ac, v205\n\t"
      "ds_read_b128 v[112:115], v255 offset:12288\n\t"
      "s_waitcnt lgkmcnt(11)\n\t"
      "v_mfma_f32_16x16x32_bf16 v[8:11], a[0:3], v[68:71], 0\n\t"
      "v_sub_f32 v40, v192, v204\n\t"
      "v_sub_f32 v41, v193, v204\n\t"
      "v_mfma_f32_16x16x32_bf16 v[12:15], a[64:67], v[68:71], 0\n\t"
      "v_sub_f32 v42, v194, v204\n\t"
      "v_sub_f32 v43, v195, v204\n\t"
      "ds_read_b128 v[116:119], v255 offset:13312\n\t"
      "s_waitcnt lgkmcnt(11)\n\t"
      "v_mfma_f32_16x16x32_bf16 v[0:3], a[4:7], v[72:75], v[0:3]\n\t"
      "v_sub_f32 v44, v196, v204\n\t"
      "v_sub_f32 v45, v197, v204\n\t"
      "v_mfma_f32_16x16x32_bf16 v[4:7], a[68:71], v[72:75], v[4:7]\n\t"
      "v_sub_f32 v46, v198, v204\n\t"
      "v_sub_f32 v47, v199, v204\n\t"
      "ds_read_b128 v[120:123], v255 offset:14336\n\t"
      "s_waitcnt lgkmcnt(11)\n\t"
      "v_mfma_f32_16x16x32_bf16 v[8:11], a[4:7], v[76:79], v[8:11]\n\t"
      "v_cvt_pk_bf16_f32 v205, v205, v205\n\t"
      "v_and_b32 v205, s74, v205\n\t"
      "v_mfma_f32_16x16x32_bf16 v[12:15], a[68:71], v[76:79], v[12:15]\n\t"
      "v_sqrt_f32 v205, v205\n\t"
      "ds_read_b128 v[124:127], v255 offset:15360\n\t"
      "s_waitcnt lgkmcnt(11)\n\t"
      "v_mfma_f32_16x16x32_bf16 v[0:3], a[8:11], v[80:83], v[0:3]\n\t"
      "v_cvt_pk_bf16_f32 v40, v40, v41\n\t"
      "v_cvt_pk_bf16_f32 v42, v42, v43\n\t"
      "v_mfma_f32_16x16x32_bf16 v[4:7], a[72:75], v[80:83], v[4:7]\n\t"
      "v_cvt_pk_bf16_f32 v44, v44, v45\n\t"
      "v_cvt_pk_bf16_f32 v46, v46, v47\n\t"
      "ds_read_b128 v[64:67], v255 offset:16384\n\t"
      "s_waitcnt lgkmcnt(11)\n\t"
      "v_mfma_f32_16x16x32_bf16 v[8:11], a[8:11], v[84:87], v[8:11]\n\t"
      "v_cvt_pk_bf16_f32 v205, v205, v205\n\t"
      "v_and_b32 v205, s74, v205\n\t"
      "v_mfma_f32_16x16x32_bf16 v[12:15], a[72:75], v[84:87], v[12:15]\n\t"
      "v_rcp_f32 v204, v205\n\t"
      "ds_read_b128 v[68:71], v255 offset:17408\n\t"
      "s_waitcnt lgkmcnt(11)\n\t"
      "v_mfma_f32_16x16x32_bf16 v[0:3], a[12:15], v[88:91], v[0:3]\n\t"
      "v_and_b32 v41, s74, v40\n\t"
      "v_lshlrev_b32 v40, 16, v40\n\t"
      "v_mfma_f32_16x16x32_bf16 v[4:7], a[76:79], v[88:91], v[4:7]\n\t"
      "v_and_b32 v43, s74, v42\n\t"
      "v_lshlrev_b32 v42, 16, v42\n\t"
      "ds_read_b128 v[72:75], v255 offset:18432\n\t"
      "s_waitcnt lgkmcnt(11)\n\t"
      "v_mfma_f32_16x16x32_bf16 v[8:11], a[12:15], v[92:95], v[8:11]\n\t"
      "v_and_b32 v45, s74, v44\n\t"
      "v_lshlrev_b32 v44, 16, v44\n\t"
      "v_mfma_f32_16x16x32_bf16 v[12:15], a[76:79], v[92:95], v[12:15]\n\t"
      "v_and_b32 v47, s74, v46\n\t"
      "v_lshlrev_b32 v46, 16, v46\n\t"
      "ds_read_b128 v[76:79], v255 offset:19456\n\t"
      "s_waitcnt lgkmcnt(11)\n\t"
      "v_mfma_f32_16x16x32_bf16 v[0:3], a[16:19], v[96:99], v[0:3]\n\t"
      "v_mul_f32 v40, v40, v204\n\t"
      "v_mul_f32 v41, v41, v204\n\t"
      "v_mfma_f32_16x16x32_bf16 v[4:7], a[80:83], v[96:99], v[4:7]\n\t"
      "v_mul_f32 v42, v42, v204\n\t"
      "v_mul_f32 v43, v43, v204\n\t"
      "ds_read_b128 v[80:83], v255 offset:20480\n\t"
      "s_waitcnt lgkmcnt(11)\n\t"
      "v_mfma_f32_16x16x32_bf16 v[8:11], a[16:19], v[100:103], v[8:11]\n\t"
      "v_mul_f32 v44, v44, v204\n\t"
      "v_mul_f32 v45, v45, v204\n\t"
      "v_mfma_f32_16x16x32_bf16 v[12:15], a[80:83], v[100:103], v[12:15]\n\t"
      "v_mul_f32 v46, v46, v204\n\t"
      "v_mul_f32 v47, v47, v204\n\t"
      "ds_read_b128 v[84:87], v255 offset:21504\n\t"
      "s_waitcnt lgkmcnt(11)\n\t"
      "v_mfma_f32_16x16x32_bf16 v[0:3], a[20:23], v[104:107], v[0:3]\n\t"
      "v_cvt_pk_bf16_f32 v52, v40, v41\n\t"
      "v_cvt_pk_bf16_f32 v53, v42, v43\n\t"
      "v_mfma_f32_16x16x32_bf16 v[4:7], a[84:87], v[104:107], v[4:7]\n\t"
      "v_cvt_pk_bf16_f32 v54, v44, v45\n\t"
      "v_cvt_pk_bf16_f32 v55, v46, v47\n\t"
      "ds_read_b128 v[88:91], v255 offset:22528\n\t"
      "s_waitcnt lgkmcnt(11)\n\t"
      "v_mfma_f32_16x16x32_bf16 v[8:11], a[20:23], v[108:111], v[8:11]\n\t"
      "v_and_b32 v33, s74, v52\n\t"
      "v_lshlrev_b32 v32, 16, v52\n\t"
      "v_mfma_f32_16x16x32_bf16 v[12:15], a[84:87], v[108:111], v[12:15]\n\t"
      "v_and_b32 v35, s74, v53\n\t"
      "v_lshlrev_b32 v34, 16, v53\n\t"
      "ds_read_b128 v[92:95], v255 offset:23552\n\t"
      "s_waitcnt lgkmcnt(11)\n\t"
      "v_mfma_f32_16x16x32_bf16 v[0:3], a[24:27], v[112:115], v[0:3]\n\t"
      "v_and_b32 v37, s74, v54\n\t"
      "v_lshlrev_b32 v36, 16, v54\n\t"
      "v_mfma_f32_16x16x32_bf16 v[4:7], a[88:91], v[112:115], v[4:7]\n\t"
      "v_and_b32 v39, s74, v55\n\t"
      "v_lshlrev_b32 v38, 16, v55\n\t"
      "ds_read_b128 v[96:99], v255 offset:24576\n\t"
      "s_waitcnt lgkmcnt(11)\n\t"
      "v_mfma_f32_16x16x32_bf16 v[8:11], a[24:27], v[116:119], v[8:11]\n\t"
      "v_mul_f32 v40, v32, v240\n\t"
      "v_mul_f32 v41, v33, v241\n\t"
      "v_mfma_f32_16x16x32_bf16 v[12:15], a[88:91], v[116:119], v[12:15]\n\t"
      "v_fma_f32 v40, v34, v242, v40\n\t"
      "v_fma_f32 v41, v35, v243, v41\n\t"
      "ds_read_b128 v[100:103], v255 offset:25600\n\t"
      "s_waitcnt lgkmcnt(11)\n\t"
      "v_mfma_f32_16x16x32_bf16 v[0:3], a[28:31], v[120:123], v[0:3]\n\t"
      "v_fma_f32 v40, v36, v244, v40\n\t"
      "v_fma_f32 v41, v37, v245, v41\n\t"
      "v_mfma_f32_16x16x32_bf16 v[4:7], a[92:95], v[120:123], v[4:7]\n\t"
      "v_fma_f32 v40, v38, v246, v40\n\t"
      "v_fma_f32 v41, v39, v247, v41\n\t"
      "ds_read_b128 v[104:107], v255 offset:26624\n\t"
      "s_waitcnt lgkmcnt(11)\n\t"
      "v_mfma_f32_16x16x32_bf16 v[8:11], a[28:31], v[124:127], v[8:11]\n\t"
      "v_add_f32 v40, v40, v41\n\t"
      "s_lshl_b32 s73, s49, 5\n\t"
      "s_add_u32 s73, s73, s60\n\t"
      "s_lshl_b32 s69, s73, 2\n\t"
      "s_add_u32 s69, s69, s57\n\t"
      "s_lshl_b32 s70, s73, 7\n\t"
      "s_add_u32 s70, s70, s57\n\t"
      "s_sub_u32 s70, s70, 212992\n\t"
      "s_cmp_ge_u32 s49, 56\n\t"
      "s_cselect_b32 s70, s70, s69\n\t"
      "v_add_f32_dpp v40, v40, v40 quad_perm:[1,0,3,2] row_mask:0xf bank_mask:0xf\n\t"
      "v_mfma_f32_16x16x32_bf16 v[12:15], a[92:95], v[124:127], v[12:15]\n\t"
      "ds_read_b128 v[108:111], v255 offset:27648\n\t"
      "s_waitcnt lgkmcnt(11)\n\t"
      "v_mfma_f32_16x16x32_bf16 v[0:3], a[32:35], v[64:67], v[0:3]\n\t"
      "v_add_f32_dpp v40, v40, v40 quad_perm:[2,3,0,1] row_mask:0xf bank_mask:0xf\n\t"
      "v_mfma_f32_16x16x32_bf16 v[4:7], a[96:99], v[64:67], v[4:7]\n\t"
      "ds_read_b128 v[112:115], v255 offset:28672\n\t"
      "s_waitcnt lgkmcnt(11)\n\t"
      "v_mfma_f32_16x16x32_bf16 v[8:11], a[32:35], v[68:71], v[8:11]\n\t"
      "v_add_f32_dpp v40, v40, v40 row_ror:4 row_mask:0xf bank_mask:0xf\n\t"
      "v_mfma_f32_16x16x32_bf16 v[12:15], a[96:99], v[68:71], v[12:15]\n\t"
      "ds_read_b128 v[116:119], v255 offset:29696\n\t"
      "s_waitcnt lgkmcnt(11)\n\t"
      "v_mfma_f32_16x16x32_bf16 v[0:3], a[36:39], v[72:75], v[0:3]\n\t"
      "v_add_f32_dpp v40, v40, v40 row_ror:8 row_mask:0xf bank_mask:0xf\n\t"
      "v_mov_b32 v41, v40\n\t"
      "v_mfma_f32_16x16x32_bf16 v[4:7], a[100:103], v[72:75], v[4:7]\n\t"
      "ds_read_b128 v[120:123], v255 offset:30720\n\t"
      "s_waitcnt lgkmcnt(11)\n\t"
      "v_mfma_f32_16x16x32_bf16 v[8:11], a[36:39], v[76:79], v[8:11]\n\t"
      "v_permlane16_swap_b32 v40, v41\n\t"
      "v_add_f32 v40, v40, v41\n\t"
      "v_mfma_f32_16x16x32_bf16 v[12:15], a[100:103], v[76:79], v[12:15]\n\t"
      "v_mov_b32 v41, v40\n\t"
      "ds_read_b128 v[124:127], v255 offset:31744\n\t"
      "s_waitcnt lgkmcnt(11)\n\t"
      "v_mfma_f32_16x16x32_bf16 v[0:3], a[40:43], v[80:83], v[0:3]\n\t"
      "v_permlane32_swap_b32 v40, v41\n\t"
      "v_add_f32 v40, v40, v41\n\t"
      "v_mfma_f32_16x16x32_bf16 v[4:7], a[104:107], v[80:83], v[4:7]\n\t"
      "v_cvt_pk_bf16_f32 v40, v40, v40\n\t"
      "v_and_b32 v40, s74, v40\n\t"
      "s_waitcnt lgkmcnt(10)\n\t"
      "v_mfma_f32_16x16x32_bf16 v[8:11], a[40:43], v[84:87], v[8:11]\n\t"
      "s_mov_b64 exec, 1\n\t"
      "buffer_store_dword v40, off, s[36:39], s70 sc1\n\t"
      "s_mov_b64 exec, -1\n\t"
      "v_add_f32 v192, v192, v32\n\t"
      "v_add_f32 v193, v193, v33\n\t"
      "v_mfma_f32_16x16x32_bf16 v[12:15], a[104:107], v[84:87], v[12:15]\n\t"
      "v_add_f32 v194, v194, v34\n\t"
      "v_add_f32 v195, v195, v35\n\t"
      "s_waitcnt lgkmcnt(9)\n\t"
      "v_mfma_f32_16x16x32_bf16 v[0:3], a[44:47], v[88:91], v[0:3]\n\t"
      "v_add_f32 v196, v196, v36\n\t"
      "v_add_f32 v197, v197, v37\n\t"
      "v_mfma_f32_16x16x32_bf16 v[4:7], a[108:111], v[88:91], v[4:7]\n\t"
      "v_add_f32 v198, v198, v38\n\t"
      "v_add_f32 v199, v199, v39\n\t"
      "s_waitcnt lgkmcnt(8)\n\t"
      "v_mfma_f32_16x16x32_bf16 v[8:11], a[44:47], v[92:95], v[8:11]\n\t"
      "v_readfirstlane_b32 s68, v40\n\t"
      "v_cvt_pk_bf16_f32 v56, v192, v193\n\t"
      "v_mfma_f32_16x16x32_bf16 v[12:15], a[108:111], v[92:95], v[12:15]\n\t"
      "v_cvt_pk_bf16_f32 v57, v194, v195\n\t"
      "v_cvt_pk_bf16_f32 v58, v196, v197\n\t"
      "s_waitcnt lgkmcnt(7)\n\t"
      "v_mfma_f32_16x16x32_bf16 v[0:3], a[48:51], v[96:99], v[0:3]\n\t"
      "v_cvt_pk_bf16_f32 v59, v198, v199\n\t"
      "s_add_u32 s67, s69, 8192\n\t"
      "s_lshl_b32 s73, s73, 10\n\t"
      "s_add_u32 s69, s73, s58\n\t"
      "s_add_u32 s70, s73, s59\n\t"
      "buffer_store_dwordx2 v[56:57], v249, s[44:47], s73 offen sc1\n\t"
      "v_mfma_f32_16x16x32_bf16 v[4:7], a[112:115], v[96:99], v[4:7]\n\t"
      "buffer_store_dwordx2 v[52:53], v249, s[36:39], s69 offen sc1\n\t"
      "buffer_store_dwordx2 v[200:201], v249, s[36:39], s70 offen sc1\n\t"
      "s_waitcnt lgkmcnt(6)\n\t"
      "v_mfma_f32_16x16x32_bf16 v[8:11], a[48:51], v[100:103], v[8:11]\n\t"
      "buffer_store_dwordx2 v[58:59], v249, s[44:47], s73 offen offset:512 sc1\n\t"
      "buffer_store_dwordx2 v[54:55], v249, s[36:39], s69 offen offset:512 sc1\n\t"
      "v_mfma_f32_16x16x32_bf16 v[12:15], a[112:115], v[100:103], v[12:15]\n\t"
      "buffer_store_dwordx2 v[202:203], v249, s[36:39], s70 offen offset:512 sc1\n\t"
      "s_mov_b32 s49, s89\n\t"
      "s_add_u32 s89, s89, 16\n\t"
      "s_lshr_b32 s69, s89, 4\n\t"
      "s_cmp_eq_u32 s69, 3\n\t"
      "s_cselect_b32 s89, s93, s89\n\t"
      "s_cmp_ge_u32 s49, 48\n\t"
      "s_cselect_b32 s60, s94, s60\n\t"
      "s_cselect_b32 s54, s95, s54\n\t"
      "s_waitcnt lgkmcnt(5)\n\t"
      "v_mfma_f32_16x16x32_bf16 v[0:3], a[52:55], v[104:107], v[0:3]\n\t"
      "v_mfma_f32_16x16x32_bf16 v[4:7], a[116:119], v[104:107], v[4:7]\n\t"
      "s_waitcnt lgkmcnt(4)\n\t"
      "v_mfma_f32_16x16x32_bf16 v[8:11], a[52:55], v[108:111], v[8:11]\n\t"
      "v_mfma_f32_16x16x32_bf16 v[12:15], a[116:119], v[108:111], v[12:15]\n\t"
      "s_waitcnt lgkmcnt(3)\n\t"
      "v_mfma_f32_16x16x32_bf16 v[0:3], a[56:59], v[112:115], v[0:3]\n\t"
      "v_mfma_f32_16x16x32_bf16 v[4:7], a[120:123], v[112:115], v[4:7]\n\t"
      "s_waitcnt lgkmcnt(2)\n\t"
      "v_mfma_f32_16x16x32_bf16 v[8:11], a[56:59], v[116:119], v[8:11]\n\t"
      "v_mfma_f32_16x16x32_bf16 v[12:15], a[120:123], v[116:119], v[12:15]\n\t"
      "s_waitcnt lgkmcnt(1)\n\t"
      "v_mfma_f32_16x16x32_bf16 v[0:3], a[60:63], v[120:123], v[0:3]\n\t"
      "v_mfma_f32_16x16x32_bf16 v[4:7], a[124:127], v[120:123], v[4:7]\n\t"
      "s_waitcnt lgkmcnt(0)\n\t"
      "v_mfma_f32_16x16x32_bf16 v[8:11], a[60:63], v[124:127], v[8:11]\n\t"
      "v_mfma_f32_16x16x32_bf16 v[12:15], a[124:127], v[124:127], v[12:15]\n\t"
      "v_xor_b32 v254, 0x8000, v254\n\t"
      "v_xor_b32 v255, 0x8000, v255\n\t"
      "s_add_u32 s69, s53, 0x4000\n\t"
      "s_add_u32 s48, s48, 4\n\t"
      "s_waitcnt vmcnt(40)\n\t"
      "v_cvt_pk_bf16_f32 v0, v0, v1\n\t"
      "v_cvt_pk_bf16_f32 v1, v2, v3\n\t"
      "v_cvt_pk_bf16_f32 v2, v4, v5\n\t"
      "v_cvt_pk_bf16_f32 v3, v6, v7\n\t"
      "v_min_u32 v0, -2, v0\n\t"
      "v_min_u32 v1, -2, v1\n\t"
      "v_min_u32 v2, -2, v2\n\t"
      "v_min_u32 v3, -2, v3\n\t"
      "buffer_store_dwordx4 v[0:3], v238, s[36:39], s53 offen sc1\n\t"
      "v_cvt_pk_bf16_f32 v8, v8, v9\n\t"
      "v_cvt_pk_bf16_f32 v9, v10, v11\n\t"
      "v_cvt_pk_bf16_f32 v10, v12, v13\n\t"
      "v_cvt_pk_bf16_f32 v11, v14, v15\n\t"
      "v_min_u32 v8, -2, v8\n\t"
      "v_min_u32 v9, -2, v9\n\t"
      "v_min_u32 v10, -2, v10\n\t"
      "v_min_u32 v11, -2, v11\n\t"
      "buffer_store_dwordx4 v[8:11], v238, s[36:39], s69 offen sc1\n\t"
      "s_add_u32 s53, s53, 1048576\n\t"
      "s_nop 1\n\t"
      "s_branch WTOP%=\n\t"
      ".p2align 5\n\t"
      "WOL5%=:\n\t"
      "buffer_load_dwordx4 v[128:131], v252, s[36:39], s51 offen sc1\n\t"
      "s_waitcnt lgkmcnt(11)\n\t"
      "v_mfma_f32_16x16x32_bf16 v[0:3], a[0:3], v[64:67], 0\n\t"
      "v_cvt_pk_bf16_f32 v204, v204, v205\n\t"
      "v_and_b32 v205, s74, v204\n\t"
      "v_mfma_f32_16x16x32_bf16 v[4:7], a[64:67], v[64:67], 0\n\t"
      "v_lshlrev_b32 v204, 16, v204\n\t"
      "v_add_f32 v205, 0x3727c5ac, v205\n\t"
      "ds_read_b128 v[112:115], v255 offset:12288\n\t"
      "s_waitcnt lgkmcnt(11)\n\t"
      "v_mfma_f32_16x16x32_bf16 v[8:11], a[0:3], v[68:71], 0\n\t"
      "v_sub_f32 v40, v192, v204\n\t"
      "v_sub_f32 v41, v193, v204\n\t"
      "v_mfma_f32_16x16x32_bf16 v[12:15], a[64:67], v[68:71], 0\n\t"
      "v_sub_f32 v42, v194, v204\n\t"
      "v_sub_f32 v43, v195, v204\n\t"
      "ds_read_b128 v[116:119], v255 offset:13312\n\t"
      "s_waitcnt lgkmcnt(11)\n\t"
      "v_mfma_f32_16x16x32_bf16 v[0:3], a[4:7], v[72:75], v[0:3]\n\t"
      "v_sub_f32 v44, v196, v204\n\t"
      "v_sub_f32 v45, v197, v204\n\t"
      "v_mfma_f32_16x16x32_bf16 v[4:7], a[68:71], v[72:75], v[4:7]\n\t"
      "v_sub_f32 v46, v198, v204\n\t"
      "v_sub_f32 v47, v199, v204\n\t"
      "ds_read_b128 v[120:123], v255 offset:14336\n\t"
      "s_waitcnt lgkmcnt(11)\n\t"
      "v_mfma_f32_16x16x32_bf16 v[8:11], a[4:7], v[76:79], v[8:11]\n\t"
      "v_cvt_pk_bf16_f32 v205, v205, v205\n\t"
      "v_and_b32 v205, s74, v205\n\t"
      "v_mfma_f32_16x16x32_bf16 v[12:15], a[68:71], v[76:79], v[12:15]\n\t"
      "buffer_load_dwordx4 v[132:135], v252, s[36:39], s51 offen offset:512 sc1\n\t"
      "ds_read_b128 v[124:127], v255 offset:15360\n\t"
      "s_waitcnt lgkmcnt(11)\n\t"
      "v_mfma_f32_16x16x32_bf16 v[0:3], a[8:11], v[80:83], v[0:3]\n\t"
      "v_sqrt_f32 v205, v205\n\t"
      "v_mfma_f32_16x16x32_bf16 v[4:7], a[72:75], v[80:83], v[4:7]\n\t"
      "v_cvt_pk_bf16_f32 v40, v40, v41\n\t"
      "v_cvt_pk_bf16_f32 v42, v42, v43\n\t"
      "ds_read_b128 v[64:67], v255 offset:16384\n\t"
      "s_waitcnt lgkmcnt(11)\n\t"
      "v_mfma_f32_16x16x32_bf16 v[8:11], a[8:11], v[84:87], v[8:11]\n\t"
      "v_cvt_pk_bf16_f32 v44, v44, v45\n\t"
      "v_cvt_pk_bf16_f32 v46, v46, v47\n\t"
      "v_mfma_f32_16x16x32_bf16 v[12:15], a[72:75], v[84:87], v[12:15]\n\t"
      "v_cvt_pk_bf16_f32 v205, v205, v205\n\t"
      "v_and_b32 v205, s74, v205\n\t"
      "ds_read_b128 v[68:71], v255 offset:17408\n\t"
      "s_waitcnt lgkmcnt(11)\n\t"
      "v_mfma_f32_16x16x32_bf16 v[0:3], a[12:15], v[88:91], v[0:3]\n\t"
      "v_rcp_f32 v204, v205\n\t"
      "v_mfma_f32_16x16x32_bf16 v[4:7], a[76:79], v[88:91], v[4:7]\n\t"
      "v_and_b32 v41, s74, v40\n\t"
      "v_lshlrev_b32 v40, 16, v40\n\t"
      "ds_read_b128 v[72:75], v255 offset:18432\n\t"
      "s_waitcnt lgkmcnt(11)\n\t"
      "v_mfma_f32_16x16x32_bf16 v[8:11], a[12:15], v[92:95], v[8:11]\n\t"
      "v_and_b32 v43, s74, v42\n\t"
      "v_lshlrev_b32 v42, 16, v42\n\t"
      "v_mfma_f32_16x16x32_bf16 v[12:15], a[76:79], v[92:95], v[12:15]\n\t"
      "buffer_load_dwordx4 v[136:139], v252, s[36:39], s51 offen offset:1024 sc1\n\t"
      "ds_read_b128 v[76:79], v255 offset:19456\n\t"
      "s_waitcnt lgkmcnt(11)\n\t"
      "v_mfma_f32_16x16x32_bf16 v[0:3], a[16:19], v[96:99], v[0:3]\n\t"
      "v_and_b32 v45, s74, v44\n\t"
      "v_lshlrev_b32 v44, 16, v44\n\t"
      "v_mfma_f32_16x16x32_bf16 v[4:7], a[80:83], v[96:99], v[4:7]\n\t"
      "v_and_b32 v47, s74, v46\n\t"
      "v_lshlrev_b32 v46, 16, v46\n\t"
      "ds_read_b128 v[80:83], v255 offset:20480\n\t"
      "s_waitcnt lgkmcnt(11)\n\t"
      "v_mfma_f32_16x16x32_bf16 v[8:11], a[16:19], v[100:103], v[8:11]\n\t"
      "v_mul_f32 v40, v40, v204\n\t"
      "v_mul_f32 v41, v41, v204\n\t"
      "v_mfma_f32_16x16x32_bf16 v[12:15], a[80:83], v[100:103], v[12:15]\n\t"
      "v_mul_f32 v42, v42, v204\n\t"
      "v_mul_f32 v43, v43, v204\n\t"
      "ds_read_b128 v[84:87], v255 offset:21504\n\t"
      "s_waitcnt lgkmcnt(11)\n\t"
      "v_mfma_f32_16x16x32_bf16 v[0:3], a[20:23], v[104:107], v[0:3]\n\t"
      "v_mul_f32 v44, v44, v204\n\t"
      "v_mul_f32 v45, v45, v204\n\t"
      "v_mfma_f32_16x16x32_bf16 v[4:7], a[84:87], v[104:107], v[4:7]\n\t"
      "v_mul_f32 v46, v46, v204\n\t"
      "v_mul_f32 v47, v47, v204\n\t"
      "ds_read_b128 v[88:91], v255 offset:22528\n\t"
      "s_waitcnt lgkmcnt(11)\n\t"
      "v_mfma_f32_16x16x32_bf16 v[8:11], a[20:23], v[108:111], v[8:11]\n\t"
      "v_cvt_pk_bf16_f32 v52, v40, v41\n\t"
      "v_cvt_pk_bf16_f32 v53, v42, v43\n\t"
      "v_mfma_f32_16x16x32_bf16 v[12:15], a[84:87], v[108:111], v[12:15]\n\t"
      "buffer_load_dwordx4 v[140:143], v252, s[36:39], s51 offen offset:1536 sc1\n\t"
      "ds_read_b128 v[92:95], v255 offset:23552\n\t"
      "s_waitcnt lgkmcnt(11)\n\t"
      "v_mfma_f32_16x16x32_bf16 v[0:3], a[24:27], v[112:115], v[0:3]\n\t"
      "v_cvt_pk_bf16_f32 v54, v44, v45\n\t"
      "v_cvt_pk_bf16_f32 v55, v46, v47\n\t"
      "v_mfma_f32_16x16x32_bf16 v[4:7], a[88:91], v[112:115], v[4:7]\n\t"
      "v_and_b32 v33, s74, v52\n\t"
      "v_lshlrev_b32 v32, 16, v52\n\t"
      "ds_read_b128 v[96:99], v255 offset:24576\n\t"
      "s_waitcnt lgkmcnt(11)\n\t"
      "v_mfma_f32_16x16x32_bf16 v[8:11], a[24:27], v[116:119], v[8:11]\n\t"
      "v_and_b32 v35, s74, v53\n\t"
      "v_lshlrev_b32 v34, 16, v53\n\t"
      "v_mfma_f32_16x16x32_bf16 v[12:15], a[88:91], v[116:119], v[12:15]\n\t"
      "v_and_b32 v37, s74, v54\n\t"
      "v_lshlrev_b32 v36, 16, v54\n\t"
      "ds_read_b128 v[100:103], v255 offset:25600\n\t"
      "s_waitcnt lgkmcnt(11)\n\t"
      "v_mfma_f32_16x16x32_bf16 v[0:3], a[28:31], v[120:123], v[0:3]\n\t"
      "v_and_b32 v39, s74, v55\n\t"
      "v_lshlrev_b32 v38, 16, v55\n\t"
      "v_mfma_f32_16x16x32_bf16 v[4:7], a[92:95], v[120:123], v[4:7]\n\t"
      "v_mul_f32 v40, v32, v240\n\t"
      "v_mul_f32 v41, v33, v241\n\t"
      "ds_read_b128 v[104:107], v255 offset:26624\n\t"
      "s_waitcnt lgkmcnt(11)\n\t"
      "v_mfma_f32_16x16x32_bf16 v[8:11], a[28:31], v[124:127], v[8:11]\n\t"
      "buffer_load_dwordx4 v[144:147], v253, s[36:39], s52 offen sc1\n\t"
      "v_mfma_f32_16x16x32_bf16 v[12:15], a[92:95], v[124:127], v[12:15]\n\t"
      "v_fma_f32 v40, v34, v242, v40\n\t"
      "v_fma_f32 v41, v35, v243, v41\n\t"
      "ds_read_b128 v[108:111], v255 offset:27648\n\t"
      "s_waitcnt lgkmcnt(11)\n\t"
      "v_mfma_f32_16x16x32_bf16 v[0:3], a[32:35], v[64:67], v[0:3]\n\t"
      "v_fma_f32 v40, v36, v244, v40\n\t"
      "v_fma_f32 v41, v37, v245, v41\n\t"
      "v_mfma_f32_16x16x32_bf16 v[4:7], a[96:99], v[64:67], v[4:7]\n\t"
      "v_fma_f32 v40, v38, v246, v40\n\t"
      "v_fma_f32 v41, v39, v247, v41\n\t"
      "ds_read_b128 v[112:115], v255 offset:28672\n\t"
      "s_waitcnt lgkmcnt(11)\n\t"
      "v_mfma_f32_16x16x32_bf16 v[8:11], a[32:35], v[68:71], v[8:11]\n\t"
      "v_add_f32 v40, v40, v41\n\t"
      "s_lshl_b32 s73, s49, 5\n\t"
      "s_add_u32 s73, s73, s60\n\t"
      "s_lshl_b32 s69, s73, 2\n\t"
      "s_add_u32 s69, s69, s57\n\t"
      "s_lshl_b32 s70, s73, 7\n\t"
      "s_add_u32 s70, s70, s57\n\t"
      "s_sub_u32 s70, s70, 212992\n\t"
      "s_cmp_ge_u32 s49, 56\n\t"
      "s_cselect_b32 s70, s70, s69\n\t"
      "v_add_f32_dpp v40, v40, v40 quad_perm:[1,0,3,2] row_mask:0xf bank_mask:0xf\n\t"
      "v_mfma_f32_16x16x32_bf16 v[12:15], a[96:99], v[68:71], v[12:15]\n\t"
      "ds_read_b128 v[116:119], v255 offset:29696\n\t"
      "s_waitcnt lgkmcnt(11)\n\t"
      "v_mfma_f32_16x16x32_bf16 v[0:3], a[36:39], v[72:75], v[0:3]\n\t"
      "v_add_f32_dpp v40, v40, v40 quad_perm:[2,3,0,1] row_mask:0xf bank_mask:0xf\n\t"
      "v_mfma_f32_16x16x32_bf16 v[4:7], a[100:103], v[72:75], v[4:7]\n\t"
      "ds_read_b128 v[120:123], v255 offset:30720\n\t"
      "s_waitcnt lgkmcnt(11)\n\t"
      "v_mfma_f32_16x16x32_bf16 v[8:11], a[36:39], v[76:79], v[8:11]\n\t"
      "buffer_load_dwordx4 v[148:151], v253, s[36:39], s52 offen offset:512 sc1\n\t"
      "v_mfma_f32_16x16x32_bf16 v[12:15], a[100:103], v[76:79], v[12:15]\n\t"
      "v_add_f32_dpp v40, v40, v40 row_ror:4 row_mask:0xf bank_mask:0xf\n\t"
      "ds_read_b128 v[124:127], v255 offset:31744\n\t"
      "s_waitcnt lgkmcnt(11)\n\t"
      "v_mfma_f32_16x16x32_bf16 v[0:3], a[40:43], v[80:83], v[0:3]\n\t"
      "v_add_f32_dpp v40, v40, v40 row_ror:8 row_mask:0xf bank_mask:0xf\n\t"
      "v_mov_b32 v41, v40\n\t"
      "v_mfma_f32_16x16x32_bf16 v[4:7], a[104:107], v[80:83], v[4:7]\n\t"
      "s_waitcnt lgkmcnt(10)\n\t"
      "v_mfma_f32_16x16x32_bf16 v[8:11], a[40:43], v[84:87], v[8:11]\n\t"
      "v_permlane16_swap_b32 v40, v41\n\t"
      "v_add_f32 v40, v40, v41\n\t"
      "v_mfma_f32_16x16x32_bf16 v[12:15], a[104:107], v[84:87], v[12:15]\n\t"
      "v_mov_b32 v41, v40\n\t"
      "s_waitcnt lgkmcnt(9)\n\t"
      "v_mfma_f32_16x16x32_bf16 v[0:3], a[44:47], v[88:91], v[0:3]\n\t"
      "v_permlane32_swap_b32 v40, v41\n\t"
      "v_add_f32 v40, v40, v41\n\t"
      "v_mfma_f32_16x16x32_bf16 v[4:7], a[108:111], v[88:91], v[4:7]\n\t"
      "v_cvt_pk_bf16_f32 v40, v40, v40\n\t"
      "v_and_b32 v40, s74, v40\n\t"
      "s_waitcnt lgkmcnt(8)\n\t"
      "v_mfma_f32_16x16x32_bf16 v[8:11], a[44:47], v[92:95], v[8:11]\n\t"
      "buffer_load_dwordx4 v[152:155], v253, s[36:39], s52 offen offset:1024 sc1\n\t"
      "v_mfma_f32_16x16x32_bf16 v[12:15], a[108:111], v[92:95], v[12:15]\n\t"
      "s_mov_b64 exec, 1\n\t"
      "buffer_store_dword v40, off, s[36:39], s70 sc1\n\t"
      "s_mov_b64 exec, -1\n\t"
      "v_add_f32 v192, v192, v32\n\t"
      "v_add_f32 v193, v193, v33\n\t"
      "s_waitcnt lgkmcnt(7)\n\t"
      "v_mfma_f32_16x16x32_bf16 v[0:3], a[48:51], v[96:99], v[0:3]\n\t"
      "v_add_f32 v194, v194, v34\n\t"
      "v_add_f32 v195, v195, v35\n\t"
      "v_mfma_f32_16x16x32_bf16 v[4:7], a[112:115], v[96:99], v[4:7]\n\t"
      "v_add_f32 v196, v196, v36\n\t"
      "v_add_f32 v197, v197, v37\n\t"
      "s_waitcnt lgkmcnt(6)\n\t"
      "v_mfma_f32_16x16x32_bf16 v[8:11], a[48:51], v[100:103], v[8:11]\n\t"
      "v_add_f32 v198, v198, v38\n\t"
      "v_add_f32 v199, v199, v39\n\t"
      "v_mfma_f32_16x16x32_bf16 v[12:15], a[112:115], v[100:103], v[12:15]\n\t"
      "v_readfirstlane_b32 s68, v40\n\t"
      "v_cvt_pk_bf16_f32 v56, v192, v193\n\t"
      "s_waitcnt lgkmcnt(5)\n\t"
      "v_mfma_f32_16x16x32_bf16 v[0:3], a[52:55], v[104:107], v[0:3]\n\t"
      "v_cvt_pk_bf16_f32 v57, v194, v195\n\t"
      "v_cvt_pk_bf16_f32 v58, v196, v197\n\t"
      "v_mfma_f32_16x16x32_bf16 v[4:7], a[116:119], v[104:107], v[4:7]\n\t"
      "v_cvt_pk_bf16_f32 v59, v198, v199\n\t"
      "s_add_u32 s67, s69, 8192\n\t"
      "s_lshl_b32 s73, s73, 10\n\t"
      "s_add_u32 s69, s73, s58\n\t"
      "s_add_u32 s70, s73, s59\n\t"
      "buffer_store_dwordx2 v[56:57], v249, s[44:47], s73 offen sc1\n\t"
      "s_waitcnt lgkmcnt(4)\n\t"
      "v_mfma_f32_16x16x32_bf16 v[8:11], a[52:55], v[108:111], v[8:11]\n\t"
      "buffer_store_dwordx2 v[52:53], v249, s[36:39], s69 offen sc1\n\t"
      "buffer_store_dwordx2 v[200:201], v249, s[36:39], s70 offen sc1\n\t"
      "v_mfma_f32_16x16x32_bf16 v[12:15], a[116:119], v[108:111], v[12:15]\n\t"
      "buffer_load_dwordx4 v[156:159], v253, s[36:39], s52 offen offset:1536 sc1\n\t"
      "s_waitcnt lgkmcnt(3)\n\t"
      "v_mfma_f32_16x16x32_bf16 v[0:3], a[56:59], v[112:115], v[0:3]\n\t"
      "buffer_store_dwordx2 v[58:59], v249, s[44:47], s73 offen offset:512 sc1\n\t"
      "buffer_store_dwordx2 v[54:55], v249, s[36:39], s69 offen offset:512 sc1\n\t"
      "v_mfma_f32_16x16x32_bf16 v[4:7], a[120:123], v[112:115], v[4:7]\n\t"
      "buffer_store_dwordx2 v[202:203], v249, s[36:39], s70 offen offset:512 sc1\n\t"
      "s_mov_b32 s49, s89\n\t"
      "s_add_u32 s89, s89, 16\n\t"
      "s_lshr_b32 s69, s89, 4\n\t"
      "s_cmp_eq_u32 s69, 3\n\t"
      "s_cselect_b32 s89, s93, s89\n\t"
      "s_cmp_ge_u32 s49, 48\n\t"
      "s_cselect_b32 s60, s94, s60\n\t"
      "s_cselect_b32 s54, s95, s54\n\t"
      "s_waitcnt lgkmcnt(2)\n\t"
      "v_mfma_f32_16x16x32_bf16 v[8:11], a[56:59], v[116:119], v[8:11]\n\t"
      "v_mfma_f32_16x16x32_bf16 v[12:15], a[120:123], v[116:119], v[12:15]\n\t"
      "s_waitcnt lgkmcnt(1)\n\t"
      "v_mfma_f32_16x16x32_bf16 v[0:3], a[60:63], v[120:123], v[0:3]\n\t"
      "v_mfma_f32_16x16x32_bf16 v[4:7], a[124:127], v[120:123], v[4:7]\n\t"
      "s_waitcnt lgkmcnt(0)\n\t"
      "v_mfma_f32_16x16x32_bf16 v[8:11], a[60:63], v[124:127], v[8:11]\n\t"
      "v_mfma_f32_16x16x32_bf16 v[12:15], a[124:127], v[124:127], v[12:15]\n\t"
      "v_xor_b32 v254, 0x8000, v254\n\t"
      "v_xor_b32 v255, 0x8000, v255\n\t"
      "s_add_u32 s69, s53, 0x4000\n\t"
      "s_add_u32 s48, s48, 4\n\t"
      "s_waitcnt vmcnt(40)\n\t"
      "v_cvt_pk_bf16_f32 v0, v0, v1\n\t"
      "v_cvt_pk_bf16_f32 v1, v2, v3\n\t"
      "v_cvt_pk_bf16_f32 v2, v4, v5\n\t"
      "v_cvt_pk_bf16_f32 v3, v6, v7\n\t"
      "v_min_u32 v0, -2, v0\n\t"
      "v_min_u32 v1, -2, v1\n\t"
      "v_min_u32 v2, -2, v2\n\t"
      "v_min_u32 v3, -2, v3\n\t"
      "buffer_store_dwordx4 v[0:3], v238, s[36:39], s53 offen sc1\n\t"
      "v_cvt_pk_bf16_f32 v8, v8, v9\n\t"
      "v_cvt_pk_bf16_f32 v9, v10, v11\n\t"
      "v_cvt_pk_bf16_f32 v10, v12, v13\n\t"
      "v_cvt_pk_bf16_f32 v11, v14, v15\n\t"
      "v_min_u32 v8, -2, v8\n\t"
      "v_min_u32 v9, -2, v9\n\t"
      "v_min_u32 v10, -2, v10\n\t"
      "v_min_u32 v11, -2, v11\n\t"
      "buffer_store_dwordx4 v[8:11], v238, s[36:39], s69 offen sc1\n\t"
      "s_add_u32 s53, s53, 1048576\n\t"
      "s_nop 1\n\t"
      "s_branch WTOP%=\n\t"
      ".p2align 5\n\t"
      "WKF%=:\n\t"
      "s_cmp_eq_u32 s48, 63\n\t"
      "s_cselect_b32 s69, 0, 46\n\t"
      "s_add_u32 s88, s88, s69\n\t"
      "s_add_u32 s88, s88, -16\n\t"
      "WKT%=:\n\t"
      "s_memrealtime s[90:91]\n\t"
      "s_waitcnt lgkmcnt(0)\n\t"
      "s_sub_u32 s69, s88, s90\n\t"
      "s_and_b32 s69, s69, 0xffff\n\t"
      "s_cmp_lt_u32 s69, 400\n\t"
      "s_cbranch_scc1 WKT%=\n\t"
      "WKL%=:\n\t"
      "buffer_load_dwordx4 v[128:131], v252, s[36:39], s51 offen sc1\n\t"
      "buffer_load_dwordx4 v[132:135], v252, s[36:39], s51 offen offset:512 sc1\n\t"
      "buffer_load_dwordx4 v[136:139], v252, s[36:39], s51 offen offset:1024 sc1\n\t"
      "buffer_load_dwordx4 v[140:143], v252, s[36:39], s51 offen offset:1536 sc1\n\t"
      "buffer_load_dwordx4 v[144:147], v253, s[36:39], s52 offen sc1\n\t"
      "buffer_load_dwordx4 v[148:151], v253, s[36:39], s52 offen offset:512 sc1\n\t"
      "buffer_load_dwordx4 v[152:155], v253, s[36:39], s52 offen offset:1024 sc1\n\t"
      "buffer_load_dwordx4 v[156:159], v253, s[36:39], s52 offen offset:1536 sc1\n\t"
      "v_lshlrev_b32 v6, 4, v248\n\t"
      "v_mov_b64 v[4:5], 0\n\t"
      "s_cmp_ge_u32 s49, s48\n\t"
      "s_cbranch_scc1 WKL1%=\n\t"
      "buffer_load_dword v4, v6, s[36:39], s55 offen sc1\n\t"
      "buffer_load_dword v5, v6, s[36:39], s55 offen offset:128 sc1\n\t"
      "WKL1%=:\n\t"
      "s_waitcnt vmcnt(0)\n\t"
      "v_min_u32 v4, v4, v5\n\t"
      "v_cmp_ge_u32 vcc, s49, v4\n\t"
      "s_nop 0\n\t"
      "s_cmp_eq_u64 vcc, 0\n\t"
      "s_cselect_b32 s66, 1, 0\n\t"
      "s_cmp_lt_u32 s48, 40\n\t"
      "s_cbranch_scc1 WKV%=\n\t"
      "WKC%=:\n\t"
      "v_max3_u32 v0, v128, v129, v130\n\t"
      "v_max3_u32 v0, v0, v131, v132\n\t"
      "v_max3_u32 v0, v0, v133, v134\n\t"
      "v_max3_u32 v0, v0, v135, v136\n\t"
      "v_max3_u32 v0, v0, v137, v138\n\t"
      "v_max3_u32 v0, v0, v139, v140\n\t"
      "v_max3_u32 v0, v0, v141, v142\n\t"
      "v_max3_u32 v0, v0, v143, v144\n\t"
      "v_max3_u32 v0, v0, v145, v146\n\t"
      "v_max3_u32 v0, v0, v147, v148\n\t"
      "v_max3_u32 v0, v0, v149, v150\n\t"
      "v_max3_u32 v0, v0, v151, v152\n\t"
      "v_max3_u32 v0, v0, v153, v154\n\t"
      "v_max3_u32 v0, v0, v155, v156\n\t"
      "v_max3_u32 v0, v0, v157, v158\n\t"
      "v_max_u32 v0, v0, v159\n\t"
      "v_cmp_eq_u32 vcc, -1, v0\n\t"
      "s_nop 0\n\t"
      "s_cbranch_vccz WKV%=\n\t"
      "s_sleep 2\n\t"
      "s_sub_u32 s72, s72, 1\n\t"
      "s_cmp_gt_i32 s72, 0\n\t"
      "s_cbranch_scc1 WKL%=\n\t"
      "WKV%=:\n\t"
      "s_cmp_lt_u32 s48, 60\n\t"
      "s_cbranch_scc1 WKV1%=\n\t"
      "s_cmp_eq_u32 s66, 0\n\t"
      "s_cbranch_scc1 WB0%=\n\t"
      "s_lshl_b32 s70, s49, 5\n\t"
      "s_add_u32 s70, s70, s60\n\t"
      "s_lshl_b32 s70, s70, 10\n\t"
      "buffer_load_dwordx2 v[224:225], v249, s[40:43], s70 offen \n\t"
      "buffer_load_dwordx2 v[226:227], v249, s[40:43], s70 offen offset:512 \n\t"
      "s_lshl_b32 s69, s49, 18\n\t"
      "s_add_u32 s69, s69, s54\n\t"
      "buffer_load_dwordx4 v[160:163], v248, s[36:39], s69 offen sc1\n\t"
      "s_add_u32 s69, s69, 0x8000\n\t"
      "buffer_load_dwordx4 v[164:167], v248, s[36:39], s69 offen sc1\n\t"
      "s_add_u32 s69, s69, 0x8000\n\t"
      "buffer_load_dwordx4 v[168:171], v248, s[36:39], s69 offen sc1\n\t"
      "s_add_u32 s69, s69, 0x8000\n\t"
      "buffer_load_dwordx4 v[172:175], v248, s[36:39], s69 offen sc1\n\t"
      "s_add_u32 s69, s69, 0x8000\n\t"
      "buffer_load_dwordx4 v[176:179], v248, s[36:39], s69 offen sc1\n\t"
      "s_add_u32 s69, s69, 0x8000\n\t"
      "buffer_load_dwordx4 v[180:183], v248, s[36:39], s69 offen sc1\n\t"
      "s_add_u32 s69, s69, 0x8000\n\t"
      "buffer_load_dwordx4 v[184:187], v248, s[36:39], s69 offen sc1\n\t"
      "s_add_u32 s69, s69, 0x8000\n\t"
      "buffer_load_dwordx4 v[188:191], v248, s[36:39], s69 offen sc1\n\t"
      "s_mov_b32 s65, 1\n\t"
      "WB0%=:\n\t"
      "v_mfma_f32_16x16x32_bf16 v[0:3], v[128:131], a[160:163], 0\n\t"
      "v_mfma_f32_16x16x32_bf16 v[4:7], v[128:131], a[164:167], 0\n\t"
      "v_mfma_f32_16x16x32_bf16 v[0:3], v[132:135], a[168:171], v[0:3]\n\t"
      "v_mfma_f32_16x16x32_bf16 v[4:7], v[132:135], a[172:175], v[4:7]\n\t"
      "v_mfma_f32_16x16x32_bf16 v[0:3], v[136:139], a[176:179], v[0:3]\n\t"
      "v_mfma_f32_16x16x32_bf16 v[4:7], v[136:139], a[180:183], v[4:7]\n\t"
      "v_mfma_f32_16x16x32_bf16 v[0:3], v[140:143], a[184:187], v[0:3]\n\t"
      "v_mfma_f32_16x16x32_bf16 v[4:7], v[140:143], a[188:191], v[4:7]\n\t"
      "s_add_u32 s51, s51, 262144\n\t"
      "s_add_u32 s52, s52, 262144\n\t"
      "s_nop 4\n\t"
      "v_max3_f32 v8, v0, v1, v2\n\t"
      "v_max3_f32 v8, v8, v3, v234\n\t"
      "v_max3_f32 v9, v4, v5, v6\n\t"
      "v_max3_f32 v9, v9, v7, v235\n\t"
      "s_nop 1\n\t"
      "v_permlane16_swap_b32 v8, v9\n\t"
      "v_max_f32 v8, v8, v9\n\t"
      "v_mov_b32 v9, v8\n\t"
      "s_nop 1\n\t"
      "v_permlane16_swap_b32 v8, v9\n\t"
      "v_pk_mul_f32 v[8:9], v[8:9], s[86:87]\n\t"
      "v_pk_fma_f32 v[10:11], v[234:235], s[86:87], v[8:9] neg_lo:[0,0,1] neg_hi:[0,0,1]\n\t"
      "v_pk_fma_f32 v[0:1], v[0:1], s[86:87], v[8:9] op_sel:[0,0,0] op_sel_hi:[1,1,0] neg_lo:[0,0,1] neg_hi:[0,0,1]\n\t"
      "v_pk_fma_f32 v[2:3], v[2:3], s[86:87], v[8:9] op_sel:[0,0,0] op_sel_hi:[1,1,0] neg_lo:[0,0,1] neg_hi:[0,0,1]\n\t"
      "v_pk_fma_f32 v[4:5], v[4:5], s[86:87], v[8:9] op_sel:[0,0,1] op_sel_hi:[1,1,1] neg_lo:[0,0,1] neg_hi:[0,0,1]\n\t"
      "v_pk_fma_f32 v[6:7], v[6:7], s[86:87], v[8:9] op_sel:[0,0,1] op_sel_hi:[1,1,1] neg_lo:[0,0,1] neg_hi:[0,0,1]\n\t"
      "v_exp_f32 v10, v10\n\t"
      "v_exp_f32 v11, v11\n\t"
      "v_exp_f32 v0, v0\n\t"
      "v_exp_f32 v1, v1\n\t"
      "v_exp_f32 v2, v2\n\t"
      "v_exp_f32 v3, v3\n\t"
      "v_exp_f32 v4, v4\n\t"
      "v_exp_f32 v5, v5\n\t"
      "v_exp_f32 v6, v6\n\t"
      "v_exp_f32 v7, v7\n\t"
      "v_pk_mul_f32 v[12:13], v[236:237], v[10:11]\n\t"
      "v_pk_add_f32 v[14:15], v[0:1], v[2:3]\n\t"
      "v_pk_add_f32 v[16:17], v[4:5], v[6:7]\n\t"
      "v_add_f32 v14, v14, v15\n\t"
      "v_add_f32 v15, v16, v17\n\t"
      "v_fma_f32 v12, v14, 0.5, v12\n\t"
      "v_fma_f32 v13, v15, 0.5, v13\n\t"
      "s_nop 1\n\t"
      "v_permlane16_swap_b32 v12, v13\n\t"
      "v_add_f32 v12, v12, v13\n\t"
      "v_mov_b32 v13, v12\n\t"
      "s_nop 1\n\t"
      "v_permlane32_swap_b32 v12, v13\n\t"
      "v_add_f32 v12, v12, v13\n\t"
      "v_mov_b32 v13, v12\n\t"
      "s_nop 1\n\t"
      "v_permlane16_swap_b32 v12, v13\n\t"
      "v_rcp_f32 v12, v12\n\t"
      "v_rcp_f32 v13, v13\n\t"
      "s_nop 0\n\t"
      "v_pk_mul_f32 v[10:11], v[10:11], v[12:13]\n\t"
      "v_pk_mul_f32 v[12:13], v[12:13], 0.5 op_sel_hi:[1,0]\n\t"
      "v_pk_mul_f32 v[0:1], v[0:1], v[12:13] op_sel:[0,0] op_sel_hi:[1,0]\n\t"
      "v_pk_mul_f32 v[2:3], v[2:3], v[12:13] op_sel:[0,0] op_sel_hi:[1,0]\n\t"
      "v_pk_mul_f32 v[4:5], v[4:5], v[12:13] op_sel:[0,1] op_sel_hi:[1,1]\n\t"
      "v_pk_mul_f32 v[6:7], v[6:7], v[12:13] op_sel:[0,1] op_sel_hi:[1,1]\n\t"
      "v_cvt_pk_bf16_f32 v16, v0, v1\n\t"
      "v_cvt_pk_bf16_f32 v17, v2, v3\n\t"
      "v_cvt_pk_bf16_f32 v18, v4, v5\n\t"
      "v_cvt_pk_bf16_f32 v19, v6, v7\n\t"
      "v_pk_mul_f32 v[64:65], v[64:65], v[10:11] op_sel:[0,0] op_sel_hi:[1,0]\n\t"
      "v_pk_mul_f32 v[66:67], v[66:67], v[10:11] op_sel:[0,0] op_sel_hi:[1,0]\n\t"
      "v_pk_mul_f32 v[68:69], v[68:69], v[10:11] op_sel:[0,0] op_sel_hi:[1,0]\n\t"
      "v_pk_mul_f32 v[70:71], v[70:71], v[10:11] op_sel:[0,0] op_sel_hi:[1,0]\n\t"
      "v_mfma_f32_16x16x16_bf16 v[64:67], v[144:145], v[16:17], v[64:67]\n\t"
      "v_pk_mul_f32 v[72:73], v[72:73], v[10:11] op_sel:[0,0] op_sel_hi:[1,0]\n\t"
      "v_pk_mul_f32 v[74:75], v[74:75], v[10:11] op_sel:[0,0] op_sel_hi:[1,0]\n\t"
      "v_mfma_f32_16x16x16_bf16 v[68:71], v[146:147], v[16:17], v[68:71]\n\t"
      "v_pk_mul_f32 v[76:77], v[76:77], v[10:11] op_sel:[0,0] op_sel_hi:[1,0]\n\t"
      "v_pk_mul_f32 v[78:79], v[78:79], v[10:11] op_sel:[0,0] op_sel_hi:[1,0]\n\t"
      "v_mfma_f32_16x16x16_bf16 v[72:75], v[148:149], v[16:17], v[72:75]\n\t"
      "v_pk_mul_f32 v[80:81], v[80:81], v[10:11] op_sel:[0,0] op_sel_hi:[1,0]\n\t"
      "v_pk_mul_f32 v[82:83], v[82:83], v[10:11] op_sel:[0,0] op_sel_hi:[1,0]\n\t"
      "v_mfma_f32_16x16x16_bf16 v[76:79], v[150:151], v[16:17], v[76:79]\n\t"
      "v_pk_mul_f32 v[84:85], v[84:85], v[10:11] op_sel:[0,0] op_sel_hi:[1,0]\n\t"
      "v_pk_mul_f32 v[86:87], v[86:87], v[10:11] op_sel:[0,0] op_sel_hi:[1,0]\n\t"
      "v_mfma_f32_16x16x16_bf16 v[80:83], v[152:153], v[16:17], v[80:83]\n\t"
      "v_pk_mul_f32 v[88:89], v[88:89], v[10:11] op_sel:[0,0] op_sel_hi:[1,0]\n\t"
      "v_pk_mul_f32 v[90:91], v[90:91], v[10:11] op_sel:[0,0] op_sel_hi:[1,0]\n\t"
      "v_mfma_f32_16x16x16_bf16 v[84:87], v[154:155], v[16:17], v[84:87]\n\t"
      "v_pk_mul_f32 v[92:93], v[92:93], v[10:11] op_sel:[0,0] op_sel_hi:[1,0]\n\t"
      "v_pk_mul_f32 v[94:95], v[94:95], v[10:11] op_sel:[0,0] op_sel_hi:[1,0]\n\t"
      "v_cvt_pk_bf16_f32 v24, v64, v65\n\t"
      "v_cvt_pk_bf16_f32 v25, v66, v67\n\t"
      "v_cvt_pk_bf16_f32 v26, v68, v69\n\t"
      "v_cvt_pk_bf16_f32 v27, v70, v71\n\t"
      "ds_write_b128 v254, v[24:27]\n\t"
      "v_mfma_f32_16x16x16_bf16 v[88:91], v[156:157], v[16:17], v[88:91]\n\t"
      "v_pk_mul_f32 v[96:97], v[96:97], v[10:11] op_sel:[0,1] op_sel_hi:[1,1]\n\t"
      "v_pk_mul_f32 v[98:99], v[98:99], v[10:11] op_sel:[0,1] op_sel_hi:[1,1]\n\t"
      "v_mfma_f32_16x16x16_bf16 v[92:95], v[158:159], v[16:17], v[92:95]\n\t"
      "v_pk_mul_f32 v[100:101], v[100:101], v[10:11] op_sel:[0,1] op_sel_hi:[1,1]\n\t"
      "v_pk_mul_f32 v[102:103], v[102:103], v[10:11] op_sel:[0,1] op_sel_hi:[1,1]\n\t"
      "v_cvt_pk_bf16_f32 v28, v72, v73\n\t"
      "v_cvt_pk_bf16_f32 v29, v74, v75\n\t"
      "v_cvt_pk_bf16_f32 v30, v76, v77\n\t"
      "v_cvt_pk_bf16_f32 v31, v78, v79\n\t"
      "ds_write_b128 v254, v[28:31] offset:2048\n\t"
      "v_mfma_f32_16x16x16_bf16 v[96:99], v[144:145], v[18:19], v[96:99]\n\t"
      "v_pk_mul_f32 v[104:105], v[104:105], v[10:11] op_sel:[0,1] op_sel_hi:[1,1]\n\t"
      "v_pk_mul_f32 v[106:107], v[106:107], v[10:11] op_sel:[0,1] op_sel_hi:[1,1]\n\t"
      "v_mfma_f32_16x16x16_bf16 v[100:103], v[146:147], v[18:19], v[100:103]\n\t"
      "v_pk_mul_f32 v[108:109], v[108:109], v[10:11] op_sel:[0,1] op_sel_hi:[1,1]\n\t"
      "v_pk_mul_f32 v[110:111], v[110:111], v[10:11] op_sel:[0,1] op_sel_hi:[1,1]\n\t"
      "v_cvt_pk_bf16_f32 v24, v80, v81\n\t"
      "v_cvt_pk_bf16_f32 v25, v82, v83\n\t"
      "v_cvt_pk_bf16_f32 v26, v84, v85\n\t"
      "v_cvt_pk_bf16_f32 v27, v86, v87\n\t"
      "ds_write_b128 v254, v[24:27] offset:4096\n\t"
      "v_mfma_f32_16x16x16_bf16 v[104:107], v[148:149], v[18:19], v[104:107]\n\t"
      "v_pk_mul_f32 v[112:113], v[112:113], v[10:11] op_sel:[0,1] op_sel_hi:[1,1]\n\t"
      "v_pk_mul_f32 v[114:115], v[114:115], v[10:11] op_sel:[0,1] op_sel_hi:[1,1]\n\t"
      "v_mfma_f32_16x16x16_bf16 v[108:111], v[150:151], v[18:19], v[108:111]\n\t"
      "v_pk_mul_f32 v[116:117], v[116:117], v[10:11] op_sel:[0,1] op_sel_hi:[1,1]\n\t"
      "v_pk_mul_f32 v[118:119], v[118:119], v[10:11] op_sel:[0,1] op_sel_hi:[1,1]\n\t"
      "v_cvt_pk_bf16_f32 v28, v88, v89\n\t"
      "v_cvt_pk_bf16_f32 v29, v90, v91\n\t"
      "v_cvt_pk_bf16_f32 v30, v92, v93\n\t"
      "v_cvt_pk_bf16_f32 v31, v94, v95\n\t"
      "ds_write_b128 v254, v[28:31] offset:6144\n\t"
      "v_mfma_f32_16x16x16_bf16 v[112:115], v[152:153], v[18:19], v[112:115]\n\t"
      "v_pk_mul_f32 v[120:121], v[120:121], v[10:11] op_sel:[0,1] op_sel_hi:[1,1]\n\t"
      "v_pk_mul_f32 v[122:123], v[122:123], v[10:11] op_sel:[0,1] op_sel_hi:[1,1]\n\t"
      "v_mfma_f32_16x16x16_bf16 v[116:119], v[154:155], v[18:19], v[116:119]\n\t"
      "v_pk_mul_f32 v[124:125], v[124:125], v[10:11] op_sel:[0,1] op_sel_hi:[1,1]\n\t"
      "v_pk_mul_f32 v[126:127], v[126:127], v[10:11] op_sel:[0,1] op_sel_hi:[1,1]\n\t"
      "v_cvt_pk_bf16_f32 v24, v96, v97\n\t"
      "v_cvt_pk_bf16_f32 v25, v98, v99\n\t"
      "v_cvt_pk_bf16_f32 v26, v100, v101\n\t"
      "v_cvt_pk_bf16_f32 v27, v102, v103\n\t"
      "ds_write_b128 v254, v[24:27] offset:1024\n\t"
      "v_mfma_f32_16x16x16_bf16 v[120:123], v[156:157], v[18:19], v[120:123]\n\t"
      "v_mfma_f32_16x16x16_bf16 v[124:127], v[158:159], v[18:19], v[124:127]\n\t"
      "v_cvt_pk_bf16_f32 v28, v104, v105\n\t"
      "v_cvt_pk_bf16_f32 v29, v106, v107\n\t"
      "v_cvt_pk_bf16_f32 v30, v108, v109\n\t"
      "v_cvt_pk_bf16_f32 v31, v110, v111\n\t"
      "ds_write_b128 v254, v[28:31] offset:3072\n\t"
      "v_cvt_pk_bf16_f32 v24, v112, v113\n\t"
      "v_cvt_pk_bf16_f32 v25, v114, v115\n\t"
      "v_cvt_pk_bf16_f32 v26, v116, v117\n\t"
      "v_cvt_pk_bf16_f32 v27, v118, v119\n\t"
      "ds_write_b128 v254, v[24:27] offset:5120\n\t"
      "v_cvt_pk_bf16_f32 v28, v120, v121\n\t"
      "v_cvt_pk_bf16_f32 v29, v122, v123\n\t"
      "v_cvt_pk_bf16_f32 v30, v124, v125\n\t"
      "v_cvt_pk_bf16_f32 v31, v126, v127\n\t"
      "ds_write_b128 v254, v[28:31] offset:7168\n\t"
      "s_waitcnt vmcnt(40) lgkmcnt(0)\n\t"
      "s_barrier\n\t"
      "WO%=:\n\t"
      "s_waitcnt vmcnt(16)\n\t"
      "ds_read_b128 v[64:67], v255\n\t"
      "ds_read_b128 v[68:71], v255 offset:1024\n\t"
      "ds_read_b128 v[72:75], v255 offset:2048\n\t"
      "ds_read_b128 v[76:79], v255 offset:3072\n\t"
      "ds_read_b128 v[80:83], v255 offset:4096\n\t"
      "ds_read_b128 v[84:87], v255 offset:5120\n\t"
      "ds_read_b128 v[88:91], v255 offset:6144\n\t"
      "ds_read_b128 v[92:95], v255 offset:7168\n\t"
      "ds_read_b128 v[96:99], v255 offset:8192\n\t"
      "ds_read_b128 v[100:103], v255 offset:9216\n\t"
      "ds_read_b128 v[104:107], v255 offset:10240\n\t"
      "ds_read_b128 v[108:111], v255 offset:11264\n\t"
      "s_cmp_ge_u32 s48, 32\n\t"
      "s_cbranch_scc1 WO3%=\n\t"
      "v_mov_b64 v[20:21], -1\n\t"
      "v_mov_b64 v[22:23], -1\n\t"
      "buffer_store_dwordx4 v[20:23], v238, s[36:39], s98 offen sc1\n\t"
      "s_bitcmp0_b32 s48, 2\n\t"
      "s_cselect_b32 s69, 1, 63\n\t"
      "s_lshl_b32 s69, s69, 14\n\t"
      "s_add_u32 s98, s98, s69\n\t"
      "WO3%=:\n\t"
      "v_lshlrev_b32 v16, 4, v248\n\t"
      "s_cmp_ge_u32 s48, 60\n\t"
      "s_cbranch_scc1 WO1%=\n\t"
      "buffer_load_dwordx4 v[228:231], off, s[36:39], s56 sc1\n\t"
      "s_mov_b32 s97, s96\n\t"
      "v_mov_b64 v[232:233], 0\n\t"
      "s_cmp_lg_u32 s65, 0\n\t"
      "s_cselect_b32 s70, s89, s49\n\t"
      "s_cmp_ge_u32 s70, s48\n\t"
      "s_cbranch_scc1 WO1%=\n\t"
      "buffer_load_dword v232, v16, s[36:39], s55 offen sc1\n\t"
      "buffer_load_dword v233, v16, s[36:39], s55 offen offset:128 sc1\n\t"
      "WO1%=:\n\t"
      "s_cmp_lt_u32 s48, 56\n\t"
      "s_cselect_b32 s69, 3145728, 0\n\t"
      "s_add_u32 s50, s50, s69\n\t"
      "s_mov_b32 s24, 0\n\t"
      "s_cmp_eq_u32 s65, 2\n\t"
      "s_cbranch_scc1 WOM2%=\n\t"
      "s_cmp_lt_u32 s48, 60\n\t"
      "s_cselect_b32 s69, s65, 0\n\t"
      "s_cmp_lg_u32 s69, 0\n\t"
      "s_cbranch_scc1 WOM1%=\n\t"
      "s_cmp_lg_u32 s96, 0\n\t"
      "s_cbranch_scc1 WOL1%=\n\t"
      "WOL0%=:\n\t"
      "s_waitcnt lgkmcnt(11)\n\t"
      "v_mfma_f32_16x16x32_bf16 v[0:3], a[0:3], v[64:67], 0\n\t"
      "v_mfma_f32_16x16x32_bf16 v[4:7], a[64:67], v[64:67], 0\n\t"
      "ds_read_b128 v[112:115], v255 offset:12288\n\t"
      "s_waitcnt lgkmcnt(11)\n\t"
      "v_mfma_f32_16x16x32_bf16 v[8:11], a[0:3], v[68:71], 0\n\t"
      "v_mfma_f32_16x16x32_bf16 v[12:15], a[64:67], v[68:71], 0\n\t"
      "ds_read_b128 v[116:119], v255 offset:13312\n\t"
      "s_waitcnt lgkmcnt(11)\n\t"
      "v_mfma_f32_16x16x32_bf16 v[0:3], a[4:7], v[72:75], v[0:3]\n\t"
      "v_mfma_f32_16x16x32_bf16 v[4:7], a[68:71], v[72:75], v[4:7]\n\t"
      "ds_read_b128 v[120:123], v255 offset:14336\n\t"
      "s_waitcnt lgkmcnt(11)\n\t"
      "v_mfma_f32_16x16x32_bf16 v[8:11], a[4:7], v[76:79], v[8:11]\n\t"
      "v_mfma_f32_16x16x32_bf16 v[12:15], a[68:71], v[76:79], v[12:15]\n\t"
      "ds_read_b128 v[124:127], v255 offset:15360\n\t"
      "s_waitcnt lgkmcnt(11)\n\t"
      "v_mfma_f32_16x16x32_bf16 v[0:3], a[8:11], v[80:83], v[0:3]\n\t"
      "v_mfma_f32_16x16x32_bf16 v[4:7], a[72:75], v[80:83], v[4:7]\n\t"
      "ds_read_b128 v[64:67], v255 offset:16384\n\t"
      "s_waitcnt lgkmcnt(11)\n\t"
      "v_mfma_f32_16x16x32_bf16 v[8:11], a[8:11], v[84:87], v[8:11]\n\t"
      "v_mfma_f32_16x16x32_bf16 v[12:15], a[72:75], v[84:87], v[12:15]\n\t"
      "ds_read_b128 v[68:71], v255 offset:17408\n\t"
      "s_waitcnt lgkmcnt(11)\n\t"
      "v_mfma_f32_16x16x32_bf16 v[0:3], a[12:15], v[88:91], v[0:3]\n\t"
      "v_mfma_f32_16x16x32_bf16 v[4:7], a[76:79], v[88:91], v[4:7]\n\t"
      "ds_read_b128 v[72:75], v255 offset:18432\n\t"
      "s_waitcnt lgkmcnt(11)\n\t"
      "v_mfma_f32_16x16x32_bf16 v[8:11], a[12:15], v[92:95], v[8:11]\n\t"
      "v_mfma_f32_16x16x32_bf16 v[12:15], a[76:79], v[92:95], v[12:15]\n\t"
      "ds_read_b128 v[76:79], v255 offset:19456\n\t"
      "s_waitcnt lgkmcnt(11)\n\t"
      "v_mfma_f32_16x16x32_bf16 v[0:3], a[16:19], v[96:99], v[0:3]\n\t"
      "v_mfma_f32_16x16x32_bf16 v[4:7], a[80:83], v[96:99], v[4:7]\n\t"
      "ds_read_b128 v[80:83], v255 offset:20480\n\t"
      "s_waitcnt lgkmcnt(11)\n\t"
      "v_mfma_f32_16x16x32_bf16 v[8:11], a[16:19], v[100:103], v[8:11]\n\t"
      "v_mfma_f32_16x16x32_bf16 v[12:15], a[80:83], v[100:103], v[12:15]\n\t"
      "ds_read_b128 v[84:87], v255 offset:21504\n\t"
      "s_waitcnt lgkmcnt(11)\n\t"
      "v_mfma_f32_16x16x32_bf16 v[0:3], a[20:23], v[104:107], v[0:3]\n\t"
      "v_mfma_f32_16x16x32_bf16 v[4:7], a[84:87], v[104:107], v[4:7]\n\t"
      "ds_read_b128 v[88:91], v255 offset:22528\n\t"
      "s_waitcnt lgkmcnt(11)\n\t"
      "v_mfma_f32_16x16x32_bf16 v[8:11], a[20:23], v[108:111], v[8:11]\n\t"
      "v_mfma_f32_16x16x32_bf16 v[12:15], a[84:87], v[108:111], v[12:15]\n\t"
      "ds_read_b128 v[92:95], v255 offset:23552\n\t"
      "s_waitcnt lgkmcnt(11)\n\t"
      "v_mfma_f32_16x16x32_bf16 v[0:3], a[24:27], v[112:115], v[0:3]\n\t"
      "v_mfma_f32_16x16x32_bf16 v[4:7], a[88:91], v[112:115], v[4:7]\n\t"
      "ds_read_b128 v[96:99], v255 offset:24576\n\t"
      "s_waitcnt lgkmcnt(11)\n\t"
      "v_mfma_f32_16x16x32_bf16 v[8:11], a[24:27], v[116:119], v[8:11]\n\t"
      "v_mfma_f32_16x16x32_bf16 v[12:15], a[88:91], v[116:119], v[12:15]\n\t"
      "ds_read_b128 v[100:103], v255 offset:25600\n\t"
      "s_waitcnt lgkmcnt(11)\n\t"
      "v_mfma_f32_16x16x32_bf16 v[0:3], a[28:31], v[120:123], v[0:3]\n\t"
      "v_mfma_f32_16x16x32_bf16 v[4:7], a[92:95], v[120:123], v[4:7]\n\t"
      "ds_read_b128 v[104:107], v255 offset:26624\n\t"
      "s_waitcnt lgkmcnt(11)\n\t"
      "v_mfma_f32_16x16x32_bf16 v[8:11], a[28:31], v[124:127], v[8:11]\n\t"
      "v_mfma_f32_16x16x32_bf16 v[12:15], a[92:95], v[124:127], v[12:15]\n\t"
      "ds_read_b128 v[108:111], v255 offset:27648\n\t"
      "s_waitcnt lgkmcnt(11)\n\t"
      "v_mfma_f32_16x16x32_bf16 v[0:3], a[32:35], v[64:67], v[0:3]\n\t"
      "v_mfma_f32_16x16x32_bf16 v[4:7], a[96:99], v[64:67], v[4:7]\n\t"
      "ds_read_b128 v[112:115], v255 offset:28672\n\t"
      "s_waitcnt lgkmcnt(11)\n\t"
      "v_mfma_f32_16x16x32_bf16 v[8:11], a[32:35], v[68:71], v[8:11]\n\t"
      "v_mfma_f32_16x16x32_bf16 v[12:15], a[96:99], v[68:71], v[12:15]\n\t"
      "ds_read_b128 v[116:119], v255 offset:29696\n\t"
      "s_waitcnt lgkmcnt(11)\n\t"
      "v_mfma_f32_16x16x32_bf16 v[0:3], a[36:39], v[72:75], v[0:3]\n\t"
      "v_mfma_f32_16x16x32_bf16 v[4:7], a[100:103], v[72:75], v[4:7]\n\t"
      "ds_read_b128 v[120:123], v255 offset:30720\n\t"
      "s_waitcnt lgkmcnt(11)\n\t"
      "v_mfma_f32_16x16x32_bf16 v[8:11], a[36:39], v[76:79], v[8:11]\n\t"
      "v_mfma_f32_16x16x32_bf16 v[12:15], a[100:103], v[76:79], v[12:15]\n\t"
      "ds_read_b128 v[124:127], v255 offset:31744\n\t"
      "s_waitcnt lgkmcnt(11)\n\t"
      "v_mfma_f32_16x16x32_bf16 v[0:3], a[40:43], v[80:83], v[0:3]\n\t"
      "v_mfma_f32_16x16x32_bf16 v[4:7], a[104:107], v[80:83], v[4:7]\n\t"
      "s_waitcnt lgkmcnt(10)\n\t"
      "v_mfma_f32_16x16x32_bf16 v[8:11], a[40:43], v[84:87], v[8:11]\n\t"
      "v_mfma_f32_16x16x32_bf16 v[12:15], a[104:107], v[84:87], v[12:15]\n\t"
      "s_waitcnt lgkmcnt(9)\n\t"
      "v_mfma_f32_16x16x32_bf16 v[0:3], a[44:47], v[88:91], v[0:3]\n\t"
      "v_mfma_f32_16x16x32_bf16 v[4:7], a[108:111], v[88:91], v[4:7]\n\t"
      "s_waitcnt lgkmcnt(8)\n\t"
      "v_mfma_f32_16x16x32_bf16 v[8:11], a[44:47], v[92:95], v[8:11]\n\t"
      "v_mfma_f32_16x16x32_bf16 v[12:15], a[108:111], v[92:95], v[12:15]\n\t"
      "s_waitcnt lgkmcnt(7)\n\t"
      "v_mfma_f32_16x16x32_bf16 v[0:3], a[48:51], v[96:99], v[0:3]\n\t"
      "v_mfma_f32_16x16x32_bf16 v[4:7], a[112:115], v[96:99], v[4:7]\n\t"
      "s_waitcnt lgkmcnt(6)\n\t"
      "v_mfma_f32_16x16x32_bf16 v[8:11], a[48:51], v[100:103], v[8:11]\n\t"
      "v_mfma_f32_16x16x32_bf16 v[12:15], a[112:115], v[100:103], v[12:15]\n\t"
      "s_waitcnt lgkmcnt(5)\n\t"
      "v_mfma_f32_16x16x32_bf16 v[0:3], a[52:55], v[104:107], v[0:3]\n\t"
      "v_mfma_f32_16x16x32_bf16 v[4:7], a[116:119], v[104:107], v[4:7]\n\t"
      "s_waitcnt lgkmcnt(4)\n\t"
      "v_mfma_f32_16x16x32_bf16 v[8:11], a[52:55], v[108:111], v[8:11]\n\t"
      "v_mfma_f32_16x16x32_bf16 v[12:15], a[116:119], v[108:111], v[12:15]\n\t"
      "s_waitcnt lgkmcnt(3)\n\t"
      "v_mfma_f32_16x16x32_bf16 v[0:3], a[56:59], v[112:115], v[0:3]\n\t"
      "v_mfma_f32_16x16x32_bf16 v[4:7], a[120:123], v[112:115], v[4:7]\n\t"
      "s_waitcnt lgkmcnt(2)\n\t"
      "v_mfma_f32_16x16x32_bf16 v[8:11], a[56:59], v[116:119], v[8:11]\n\t"
      "v_mfma_f32_16x16x32_bf16 v[12:15], a[120:123], v[116:119], v[12:15]\n\t"
      "s_waitcnt lgkmcnt(1)\n\t"
      "v_mfma_f32_16x16x32_bf16 v[0:3], a[60:63], v[120:123], v[0:3]\n\t"
      "v_mfma_f32_16x16x32_bf16 v[4:7], a[124:127], v[120:123], v[4:7]\n\t"
      "s_waitcnt lgkmcnt(0)\n\t"
      "v_mfma_f32_16x16x32_bf16 v[8:11], a[60:63], v[124:127], v[8:11]\n\t"
      "v_mfma_f32_16x16x32_bf16 v[12:15], a[124:127], v[124:127], v[12:15]\n\t"
      "v_xor_b32 v254, 0x8000, v254\n\t"
      "v_xor_b32 v255, 0x8000, v255\n\t"
      "s_add_u32 s69, s53, 0x4000\n\t"
      "s_add_u32 s48, s48, 4\n\t"
      "s_waitcnt vmcnt(40)\n\t"
      "v_cvt_pk_bf16_f32 v0, v0, v1\n\t"
      "v_cvt_pk_bf16_f32 v1, v2, v3\n\t"
      "v_cvt_pk_bf16_f32 v2, v4, v5\n\t"
      "v_cvt_pk_bf16_f32 v3, v6, v7\n\t"
      "v_min_u32 v0, -2, v0\n\t"
      "v_min_u32 v1, -2, v1\n\t"
      "v_min_u32 v2, -2, v2\n\t"
      "v_min_u32 v3, -2, v3\n\t"
      "buffer_store_dwordx4 v[0:3], v238, s[36:39], s53 offen sc1\n\t"
      "v_cvt_pk_bf16_f32 v8, v8, v9\n\t"
      "v_cvt_pk_bf16_f32 v9, v10, v11\n\t"
      "v_cvt_pk_bf16_f32 v10, v12, v13\n\t"
      "v_cvt_pk_bf16_f32 v11, v14, v15\n\t"
      "v_min_u32 v8, -2, v8\n\t"
      "v_min_u32 v9, -2, v9\n\t"
      "v_min_u32 v10, -2, v10\n\t"
      "v_min_u32 v11, -2, v11\n\t"
      "buffer_store_dwordx4 v[8:11], v238, s[36:39], s69 offen sc1\n\t"
      "s_add_u32 s53, s53, 1048576\n\t"
      "s_nop 1\n\t"
      "s_branch WTOP%=\n\t"
      ".p2align 5\n\t"
      "WEND%=:\n\t"
      : "+{v[0:15]}"(c0), "+{v[16:31]}"(c1), "+{v[240:255]}"(c2), "+{v238}"(c3), "+{v[32:39]}"(c4), "+s"(w), "+s"(wv), "+s"(A),
        "+{a[0:31]}"(wo.a), "+{a[32:63]}"(wo.b), "+{a[64:95]}"(wo.c), "+{a[96:127]}"(wo.d)
      :
      : "memory", "vcc", "scc", "v40", "v41", "v42", "v43", "v44", "v45", "v46", "v47", "v48", "v49", "v50", "v51", "v52", "v53", "v54", "v55", "v56", "v57", "v58", "v59", "v60", "v61", "v62", "v63", "v64", "v65", "v66", "v67", "v68", "v69", "v70", "v71", "v72", "v73", "v74", "v75", "v76", "v77", "v78", "v79", "v80", "v81", "v82", "v83", "v84", "v85", "v86", "v87", "v88", "v89", "v90", "v91", "v92", "v93", "v94", "v95", "v96", "v97", "v98", "v99", "v100", "v101", "v102", "v103", "v104", "v105", "v106", "v107", "v108", "v109", "v110", "v111", "v112", "v113", "v114", "v115", "v116", "v117", "v118", "v119", "v120", "v121", "v122", "v123", "v124", "v125", "v126", "v127", "v128", "v129", "v130", "v131", "v132", "v133", "v134", "v135", "v136", "v137", "v138", "v139", "v140", "v141", "v142", "v143", "v144", "v145", "v146", "v147", "v148", "v149", "v150", "v151", "v152", "v153", "v154", "v155", "v156", "v157", "v158", "v159", "v160", "v161", "v162", "v163", "v164", "v165", "v166", "v167", "v168", "v169", "v170", "v171", "v172", "v173", "v174", "v175", "v176", "v177", "v178", "v179", "v180", "v181", "v182", "v183", "v184", "v185", "v186", "v187", "v188", "v189", "v190", "v191", "v192", "v193", "v194", "v195", "v196", "v197", "v198", "v199", "v200", "v201", "v202", "v203", "v204", "v205", "v206", "v207", "v208", "v209", "v210", "v211", "v212", "v213", "v214", "v215", "v216", "v217", "v218", "v219", "v220", "v221", "v222", "v223", "v224", "v225", "v226", "v227", "v228", "v229", "v230", "v231", "v232", "v233", "v234", "v235", "v236", "v237", "v239", "a128", "a129", "a130", "a131", "a132", "a133", "a134", "a135", "a136", "a137", "a138", "a139", "a140", "a141", "a142", "a143", "a144", "a145", "a146", "a147", "a148", "a149", "a150", "a151", "a152", "a153", "a154", "a155", "a156", "a157", "a158", "a159", "a160", "a161", "a162", "a163", "a164", "a165", "a166", "a167", "a168", "a169", "a170", "a171", "a172", "a173", "a174", "a175", "a176", "a177", "a178", "a179", "a180", "a181", "a182", "a183", "a184", "a185", "a186", "a187", "a188", "a189", "a190", "a191", "a192", "a193", "a194", "a195", "a196", "a197", "a198", "a199", "a200", "a201", "a202", "a203", "a204", "a205", "a206", "a207", "a208", "a209", "a210", "a211", "a212", "a213", "a214", "a215", "a216", "a217", "a218", "a219", "a220", "a221", "a222", "a223", "a224", "a225", "a226", "a227", "a228", "a229", "a230", "a231", "a232", "a233", "a234", "a235", "a236", "a237", "a238", "a239", "a240", "a241", "a242", "a243", "a244", "a245", "a246", "a247", "a248", "a249", "a250", "a251", "a252", "a253", "a254", "a255", "s24", "s36", "s37", "s38", "s39", "s40", "s41", "s42", "s43", "s44", "s45", "s46", "s47", "s48", "s49", "s50", "s51", "s52", "s53", "s54", "s55", "s56", "s57", "s58", "s59", "s60", "s61", "s62", "s63", "s64", "s65", "s66", "s67", "s68", "s69", "s70", "s71", "s72", "s73", "s74", "s75", "s76", "s77", "s78", "s79", "s80", "s81", "s82", "s83", "s84", "s85", "s86", "s87", "s88", "s89", "s90", "s91", "s92", "s93", "s94", "s95", "s96", "s97", "s98", "s99");
}

__global__ void __launch_bounds__(256, 1) mega_kernel(MegaParams P) {
  __shared__ __attribute__((aligned(16))) char lds[LDS_BYTES];
  int b, wv;
  KArgs A = (KArgs)__builtin_amdgcn_kernarg_segment_ptr();
  u32x16 ka; u32x8 kb;
  asm volatile("s_load_dwordx16 s[36:51], %4, 0\n\t"
               "s_load_dwordx8 s[52:59], %4, 64\n\t"
               "s_getpc_b64 s[60:61]\n"
               "KPC:\n\t"
               "s_lshr_b32 %0, %5, 3\n\t"
               "s_and_b32 %0, %0, 7\n\t"
               "s_lshl_b32 %0, %0, 14\n\t"
               "v_lshl_add_u32 v254, %6, 6, %0\n\t"
               "v_min_u32 v254, KENDW-KPC, v254\n\t"
               "global_load_dword v255, v254, s[60:61]\n\t"
               "v_readfirstlane_b32 %1, %6\n\t"
               "s_lshr_b32 %0, %5, 1\n\t"
               "s_andn2_b32 %0, %0, 3\n\t"
               "s_and_b32 s60, %5, 3\n\t"
               "s_or_b32 %0, %0, s60\n\t"
               "s_lshr_b32 %1, %1, 6\n\t"
               "s_bitcmp1_b32 %5, 2\n\t"
               "s_cbranch_scc1 GSTART\n\t"
               "s_waitcnt lgkmcnt(0)"
               : "=&{s34}"(b), "=&{s33}"(wv), "=&{s[36:51]}"(ka), "=&{s[52:59]}"(kb), "+{s[0:1]}"(A)
               : "s"(blockIdx.x), "v"(threadIdx.x) : "v252", "v253", "v254", "v255", "s60", "s61", "s62", "s63", "scc");
  static_assert(XS == 0, "");
  if (b >= NREC) {
    int w = b, wv2 = wv;
    WoRegs wo;
    const unsigned tag = worker_role(w, wv2, A, wo);
    worker_tasks(lds, w, wv2, A, tag, wo);
    asm volatile(".p2align 6");
    if (w == 0 && wv2 == 0) {
      const int lane = fresh_lane();
#pragma unroll 1
      for (unsigned spins = 0; spins < (1u << 16); ++spins) {
        const u32x2 sel = top2_wave((const float*)(A->ws + O_RBUF) + S_, lane, spins == 0, spins < 8);
        if (spins && sel[1]) {
          if (lane < 8) __hip_atomic_store((gu32*)(A->ws + O_CNT + lane * 64 + 8), (unsigned)__builtin_amdgcn_s_memrealtime(), RLX_AGENT);
          if (lane < 8) __hip_atomic_store((gu64*)(A->ws + O_CNT + lane * 64), (u64)sel[0] | ((u64)sel[1] << 32), RLX_AGENT);
          break;
        }
        __builtin_amdgcn_s_sleep(SSLEEP);
        if (spins == 0 && SWAIT) {
          unsigned f = 0;
#pragma unroll 1
          for (unsigned n = 0; n < 4096; ++n) {
            f = __builtin_amdgcn_readfirstlane(__hip_atomic_load((gu32*)(A->ws + O_KVSTEP), RLX_AGENT));
            if ((f >> 16) >= 61) break;
            __builtin_amdgcn_s_sleep(4);
          }
          f += SWAIT - ((f >> 16) - 61) * 156;
#pragma unroll 1
          while (((f - (unsigned)__builtin_amdgcn_s_memrealtime()) & 0xffffu) < 400u) __builtin_amdgcn_s_sleep(1);
        }
      }
    }
    __syncthreads();
    if ((w & 2) == 0) mlp2_role(A, lds, (w & 1) * 32 + (w >> 2), wv2 * 64 + fresh_lane());
    asm volatile("KENDW:");
    __builtin_amdgcn_endpgm();
    __builtin_unreachable();
  } else {
    KArgs kp = A;
    rec_role(lds, kp, b, wv, ka, kb);
    asm volatile(".p2align 6");
    const int tid = wv * 64 + fresh_lane();
    __syncthreads();
    if ((b & 31) < M1_WGS / 4) mlp1_role(kp, lds, (b & 31) * 4 + (b >> 5), tid);
    else if (SW2 && b == M1_WGS / 4 + 1) {
      if (wv == 0) {
        const unsigned t0 = (unsigned)__builtin_amdgcn_s_memrealtime();
#pragma unroll 1
        for (unsigned spins = 0; spins < (1u << 16); ++spins) {
          const u32x2 sel = top2_wave((const float*)(kp->ws + O_RBUF) + S_, tid, spins == 0, false);
          if (spins && sel[1]) {
            if (tid < 32) __hip_atomic_store((gu64*)(kp->ws + O_SEL2 + tid * 128), (u64)sel[0] | ((u64)sel[1] << 32), RLX_AGENT);
            break;
          }
#pragma unroll 1
          while ((unsigned)__builtin_amdgcn_s_memrealtime() - t0 < SW2) __builtin_amdgcn_s_sleep(1);
        }
      }
    }
    else if (b == M1_WGS / 4) {
      if (tid == 64) {
        gu32* e = (gu32*)(kp->ws + O_EPOCH);
        __hip_atomic_store(e, mktag(__hip_atomic_load(e, RLX_AGENT)), RLX_AGENT);
      }
    }
    __builtin_amdgcn_endpgm();
    __builtin_unreachable();
  }
}

extern "C" void kernel_launch(void* const* d_in, const int* in_sizes, int n_in, void* d_out, int out_size, void* d_ws, size_t ws_size,
                              hipStream_t stream) {
  MegaParams P;
  P.x = (const u16*)d_in[0]; P.mem0 = (const u16*)d_in[1];
  P.Wq = (const u16*)d_in[2]; P.Wk = (const u16*)d_in[3]; P.Wv = (const u16*)d_in[4]; P.Wo = (const u16*)d_in[5];
  P.w_r = (const u16*)d_in[8]; P.wg = (const u16*)d_in[9]; P.wu = (const u16*)d_in[10]; P.wd = (const u16*)d_in[11];
  P.out = (u16*)d_out; P.ws = (char*)d_ws;
  mega_kernel<<<256, 256, 0, stream>>>(P);
}
```

```cpp
#include <hip/hip_runtime.h>
#include <hip/hip_bf16.h>
#define SWAIT 0
#define SW2 0
#define ASLEEP 32
#define ASALL 0
#define PSLEEP 8
#define FSLEEP 6
#define RDLY 250
#define RDL2 100
#define ADLY 130
#define CPF 1
#define TCH 0
#define SNEED 0
#define MPF 1

typedef unsigned short u16;
typedef unsigned long long u64;
typedef __attribute__((ext_vector_type(8))) short bf16x8;
typedef __attribute__((ext_vector_type(4))) float f32x4;
typedef __attribute__((ext_vector_type(4))) unsigned u32x4;
#define GAS __attribute__((address_space(1)))
typedef GAS u64 gu64;
typedef GAS unsigned gu32;
#define RLX_AGENT __ATOMIC_RELAXED, __HIP_MEMORY_SCOPE_AGENT

constexpr int S_ = 2048, DM = 512, NH = 32, DH = 128, MM = 8, SEG = 32, NSEG = 64;
constexpr int HD = NH * DH;
constexpr int QKVN = 3 * HD;
constexpr int FF = 1792;

__device__ __forceinline__ float bf2f(u16 v) { return __builtin_bit_cast(float, (unsigned)v << 16); }
__device__ __forceinline__ u16 f2bf(float f) { unsigned u = __builtin_bit_cast(unsigned, f); return (u16)((u + 0x7fffu + ((u >> 16) & 1u)) >> 16); }
__device__ __forceinline__ float rbf(float f) { return bf2f(f2bf(f)); }

typedef __attribute__((ext_vector_type(2))) float f32x2;
typedef __attribute__((ext_vector_type(2))) __bf16 bf16x2_t;
typedef __attribute__((ext_vector_type(2))) unsigned u32x2;
__device__ __forceinline__ unsigned pk2(float a, float b) { return __builtin_bit_cast(unsigned, __builtin_convertvector((f32x2){a, b}, bf16x2_t)); }
__device__ __forceinline__ unsigned f2u(float f) { return __builtin_bit_cast(unsigned, f); }
__device__ __forceinline__ float u2f(unsigned u) { return __builtin_bit_cast(float, u); }
__device__ __forceinline__ float lo2f(unsigned u) { return __builtin_bit_cast(float, u << 16); }
__device__ __forceinline__ float hi2f(unsigned u) { return __builtin_bit_cast(float, u & 0xffff0000u); }
__device__ __forceinline__ void unpack8(const u32x4 v, float (&f)[8]) {
#pragma unroll
  for (int i = 0; i < 4; ++i) { f[2 * i] = lo2f(v[i]); f[2 * i + 1] = hi2f(v[i]); }
}

constexpr unsigned O_EPOCH = 0;
constexpr unsigned O_TFLAG = 256;
constexpr unsigned O_MFLAG = O_TFLAG + 16 * 32 * 64;
constexpr unsigned O_LFLAG = O_MFLAG + 32 * 128;
constexpr unsigned O_WM = O_LFLAG + 128 * 128;
constexpr unsigned O_KVSTEP = O_WM + 128 * 128;
constexpr unsigned O_CNT = O_KVSTEP + 2048;
constexpr unsigned O_PFLAG = O_CNT + 2048;
constexpr unsigned O_SEL2 = O_PFLAG + 4 * 128 * 128;
constexpr unsigned O_ACT = O_SEL2 + 32 * 128;
constexpr unsigned O_RBUF = O_ACT + 3 * 112 * 128;
constexpr unsigned O_INIT_END = O_RBUF + 2 * S_ * 4 + 256 * 128;
constexpr unsigned O_STAMPS = 196608 + 65 * 32768;
constexpr unsigned O_XN = O_STAMPS + (4 << 20);
constexpr unsigned O_QKV = O_XN + S_ * DM * 2;
constexpr unsigned O_KVM = O_QKV + S_ * QKVN * 2;
constexpr unsigned O_VTM = O_KVM + NSEG * 8 * HD * 2;
constexpr unsigned O_PART = O_VTM + NSEG * 8 * HD * 2;
constexpr unsigned O_HBUF = O_PART + S_ * 8 * DM * 4;
constexpr unsigned O_LNH = O_HBUF + S_ * DM * 2;
constexpr unsigned O_MFRAG = O_LNH + S_ * DM * 2;
constexpr unsigned O_SLOTS = (O_MFRAG + 2 * DM * FF * 2 + 0x1fffff) & ~0x1fffffu;
constexpr unsigned WS_BYTES = O_SLOTS + 65 * 32768;
#define QO(i) (((i) & 1) * 16384 + ((i) >> 1) * 8192)
#define CAL(b) (O_SLOTS + 128 + ((b) >> 3) * 16384 + ((b) & 7) * 1024)
static_assert(O_INIT_END <= O_STAMPS && (O_INIT_END - O_KVSTEP) / 16 <= 128 * 128, "layout");
static_assert(O_TFLAG == 256 && O_MFLAG == 33024 && O_LFLAG == 37120 && O_STAMPS == 2326528 && O_XN == 6520832 && O_QKV == 8617984 && O_KVM == 58949632 && O_VTM == 63143936 && O_PART == 67338240 && O_MFRAG == 105086976 && O_SLOTS == 109051904 && O_CNT + 1024 == 72960 && O_INIT_END <= 262144 && O_WM == 53504, "layout");
constexpr int L_BS = 520, L_QN = 49152, L_TB = 98304, L_VT = 32768;
struct MegaParams {
  const u16* x; const u16* mem0; const u16* Wq; const u16* Wk; const u16* Wv; const u16* Wo;
  const u16* w_r; const u16* wg; const u16* wu; const u16* wd;
  u16* out;
  char* ws;
};
__device__ __forceinline__ unsigned mktag(unsigned e) { return e * 0x9e3779b1u + 0x7f4a7c15u; }

typedef __attribute__((ext_vector_type(4))) short s16x4;
#define LAS __attribute__((address_space(3)))
constexpr int LDS_BYTES = 163840;
#define SB() __builtin_amdgcn_sched_barrier(0)
#define LDS_BARRIER() asm volatile("s_waitcnt lgkmcnt(0)\n\ts_barrier" ::: "memory")
#define VM_DRAIN() asm volatile("s_waitcnt vmcnt(0)" ::: "memory")
__device__ __forceinline__ float xmax16_32(float m) {
  u32x2 p = __builtin_amdgcn_permlane16_swap(f2u(m), f2u(m), false, false);
  m = fmaxf(u2f(p[0]), u2f(p[1]));
  p = __builtin_amdgcn_permlane32_swap(f2u(m), f2u(m), false, false);
  return fmaxf(u2f(p[0]), u2f(p[1]));
}
__device__ __forceinline__ float xsum16_32(float m) {
  u32x2 p = __builtin_amdgcn_permlane16_swap(f2u(m), f2u(m), false, false);
  m = u2f(p[0]) + u2f(p[1]);
  p = __builtin_amdgcn_permlane32_swap(f2u(m), f2u(m), false, false);
  return u2f(p[0]) + u2f(p[1]);
}
__device__ __forceinline__ unsigned wave_max_u32(unsigned v) {
  v = max(v, (unsigned)__builtin_amdgcn_mov_dpp(v, 0xb1, 0xf, 0xf, true));
  v = max(v, (unsigned)__builtin_amdgcn_mov_dpp(v, 0x4e, 0xf, 0xf, true));
  v = max(v, (unsigned)__builtin_amdgcn_mov_dpp(v, 0x124, 0xf, 0xf, true));
  v = max(v, (unsigned)__builtin_amdgcn_mov_dpp(v, 0x128, 0xf, 0xf, true));
  u32x2 p = __builtin_amdgcn_permlane16_swap(v, v, false, false);
  v = max(p[0], p[1]);
  p = __builtin_amdgcn_permlane32_swap(v, v, false, false);
  return max(p[0], p[1]);
}
__device__ __forceinline__ u32x4 ld_sc1(__amdgpu_buffer_rsrc_t rs, unsigned off) { return __builtin_amdgcn_raw_buffer_load_b128(rs, off, 0, 16); }
__device__ __forceinline__ u32x2 ld8_sc1(__amdgpu_buffer_rsrc_t rs, unsigned off) { return __builtin_amdgcn_raw_buffer_load_b64(rs, off, 0, 16); }

#define XS 0
#define VBX __builtin_amdgcn_readfirstlane((int)(((blockIdx.x & 4) << 5) + (blockIdx.x >> 3) * 4 + (blockIdx.x & 3)))
constexpr int NREC = 128, NWORK = 128;
typedef __attribute__((ext_vector_type(8))) unsigned u32x8;
typedef __attribute__((ext_vector_type(16))) unsigned u32x16;
typedef __attribute__((ext_vector_type(32))) unsigned u32x32;
#define RSRC(p, n) __builtin_amdgcn_make_buffer_rsrc((void*)(p), 0, (n), 0x00020000)

__device__ __forceinline__ int fresh_lane() {
  int l;
  asm volatile("v_mbcnt_lo_u32_b32 %0, -1, 0\n\tv_mbcnt_hi_u32_b32 %0, -1, %0" : "=v"(l));
  return l;
}
typedef const __attribute__((address_space(4))) MegaParams* KArgs;
#define ARGS() KArgs A = (KArgs)__builtin_amdgcn_kernarg_segment_ptr(); asm volatile("" : "+s"(A))
#define MS(k)
__device__ __forceinline__ void rec_role(char* lds, KArgs& A, int& b, int& wv, const u32x16 ka, const u32x8 kb) {
#define HMAP 0
  const int h = HMAP == 1 ? (b & 3) * 8 + ((b >> 2) & 7) : (HMAP == 2 ? (b & 1) * 16 + (b & 2) / 2 + ((b >> 2) & 7) * 2 : b & 31), part = b >> 5;
  const int lane = fresh_lane(), l15 = lane & 15, l4 = lane >> 4;
  const int b1 = (l15 >> 1) & 1, mrow = 2 * (l15 >> 2) + (l15 & 1);
  const int r4 = l15 >> 2, c4 = l15 & 3;
#define KA(v, i) ((u64)v[i] | (u64)v[(i) + 1] << 32)
  const u64 wsa = KA(kb, 6), woa = KA(ka, 10), wva = KA(ka, 8), wqa = KA(ka, 4), wka = KA(ka, 6), m0a = KA(ka, 2);
  const unsigned awv = (unsigned)((8 * wv + l4) * 8192 + h * 256 + ((l15 ^ (l4 | (wv << 2))) << 4)), rot = 1 ? part : 0;
  const int sig = 1 ? (h >> 2) & 1 : 0;
  const int sig2 = 1 ? (h >> 3) & 1 : 0;
  unsigned tch;
  asm volatile(
      "s_lshl_b32 s91, %5, 4\n\t"
      "s_add_u32 m0, %5, 0\n\t"
      "s_xor_b32 s90, s91, 0\n\t"
      "buffer_load_dwordx4 %1, %3, s90 offen lds\n\t"
      "s_add_u32 m0, %5, 1024\n\t"
      "s_xor_b32 s90, s91, 16384\n\t"
      "buffer_load_dwordx4 %1, %3, s90 offen lds\n\t"
      "s_add_u32 m0, %5, 8192\n\t"
      "s_xor_b32 s90, s91, 32768\n\t"
      "buffer_load_dwordx4 %1, %3, s90 offen lds\n\t"
      "s_add_u32 m0, %5, 9216\n\t"
      "s_xor_b32 s90, s91, 49152\n\t"
      "buffer_load_dwordx4 %1, %3, s90 offen lds\n\t"
      "s_add_u32 m0, %5, 16384\n\t"
      "s_xor_b32 s90, s91, 65536\n\t"
      "buffer_load_dwordx4 %1, %3, s90 offen lds\n\t"
      "s_add_u32 m0, %5, 17408\n\t"
      "s_xor_b32 s90, s91, 81920\n\t"
      "buffer_load_dwordx4 %1, %3, s90 offen lds\n\t"
      "s_add_u32 m0, %5, 24576\n\t"
      "s_xor_b32 s90, s91, 98304\n\t"
      "buffer_load_dwordx4 %1, %3, s90 offen lds\n\t"
      "s_add_u32 m0, %5, 25600\n\t"
      "s_xor_b32 s90, s91, 114688\n\t"
      "buffer_load_dwordx4 %1, %3, s90 offen lds\n\t"
      : "=&{s87}"(tch)
      : "v"((unsigned)((h * DH + (lane >> 2)) * DM + 128 * part + 32 * wv + 8 * (2 * (lane & 1) + (((lane >> 1) & 1) ^ sig))) * 2), "v"(awv), "s"(RSRC(woa, DM * HD * 2)), "s"(RSRC(wva, DM * HD * 2)),
        "s"(wv * 2048), "s"(rot << 19), "s"(wva), "s"(wqa), "s"(wka), "s"(wsa), "s"(m0a)
      : "memory", "scc", "s90", "s91", "s92");
  SB();
  const unsigned i0 = 1024 + b * 512 + wv * 64 + lane, o1 = O_KVSTEP + ((wv >> 1) * 8192 + b * 64 + lane) * 16;
  const unsigned oz = wv == 2 ? CAL(b ^ 1) + (lane >> 5) * 8192 + ((lane >> 3) & 3) * 256 + (lane & 7) * 16 : ((wv & 1) && o1 < O_INIT_END ? o1 : 0xffffff00u);
  const int sl = lane ^ ((l4 & 1) << 1), su = ((l15 >> 3) * 16 + c4 * 4 + l4) ^ ((l15 >> 3) << 1);
  const int jt = wv ^ rot;
  u32x8 cc = {oz, (unsigned)lane, (unsigned)lane,
              wv == 2 || o1 < O_ACT ? 0u : ~0u,
              (unsigned)(QO(jt >> 1) + (((jt & 1) * 2 + (l4 >> 1)) * 8 + (r4 & 1) * 2 + (l15 >> 3)) * 256 + ((l15 & 1) ^ sig2) * 64 + (2 * b1 + (l4 & 1)) * 16),
              (unsigned)(h * 2048 + (2 * (part & 1) + (l4 >> 1)) * 512 + (2 * b1 + (l4 & 1)) * 128 + mrow * 16),
              (unsigned)(h * 2048 + part * 512 + (l4 >> 1) * 256 + (8 * wv + 4 * (l15 >> 3) + c4) * 16 + (r4 & 1) * 8 + (l4 & 1) * 4),
              (unsigned)((2 * (wv >> 1) + (l4 >> 1)) * 2048 + ((2 * (wv & 1) + (l4 & 1)) * 16 + l15) * 16)};
  u32x8 c3 = {(unsigned)lane, O_SLOTS + (i0 >> 3) * 256 + (i0 & 7) * 16,
              (unsigned)(wv * 2048 + (8 * l4 + r4) * 64 + c4 * 16),
              (unsigned)(wv * 2048 + (r4 >> 1) * 1024 + (l4 * 16 + 8 * (c4 >> 1) + 4 * (r4 & 1) + (c4 & 1)) * 16),
              (unsigned)(L_VT + (8 * r4 + c4) * 256 + (((4 * wv + l4) ^ l15) << 4)),
              awv,
              (unsigned)(L_VT + ((wv & 1) * 32 + 8 * l4 + r4) * 256 + (((4 * part + 2 * (wv >> 1) + (c4 >> 1)) ^ (r4 | (l4 << 2))) << 4) + (c4 & 1) * 8),
              (unsigned)(QO(part) + h * 256 + (lane & 7) * 16)};
  const int L = (h & 7) * 8 + (lane & 7);
  const u64 mp = 1 ? (u64)((const u16*)m0a + (4 * (l15 >> 3) + 2 * ((l15 & 1) ^ sig2) + (r4 & 1)) * DM + 64 * jt + 16 * l4 + 2 * b1) :
                (u64)((const u16*)m0a + (2 * ((L >> 2) & 3) + ((L >> 4) & 1)) * DM + 128 * part + 32 * (h >> 3) + 8 * (L >> 5) + 16 * (L & 1) + (L & 2));
  u32x8 c0 = {(unsigned)((8 * wv + l4) * 8192 + h * 256 + ((l15 ^ (2 * l4 + 8 * (wv & 1))) << 4)),
              (unsigned)(L_TB + (8 * l4 + r4) * 256 + (((4 * wv + (c4 >> 1)) ^ (2 * r4 + 8 * (l4 & 1))) << 4) + (c4 & 1) * 8),
              (unsigned)mp, (unsigned)(mp >> 32), (unsigned)(CAL(b) + wv * 256 + ((wv ^ (b >> 2)) & 1) * 8192) & (unsigned)((b - 32) >> 31) & (unsigned)((1 + 1 - wv) >> 31), (unsigned)(QO(part) + sig * 1024 + sig2 * 512),
              b1 ? 0xEEEEu : 0x4444u, (unsigned)((wv * 8 + (lane >> 5)) * 256 + (lane & 31) * 4)};
  u32x4 c1 = {(unsigned)(sl * 8),
                    (unsigned)(L_QN + ((wv >> 1) * 2 + (r4 & 1)) * L_BS + ((wv & 1) * 32 + su) * 8),
                    (unsigned)lane, (unsigned)lane * 16};
  const unsigned k0 = O_QKV + h * 24576, v0 = O_QKV + 8192 + SEG * QKVN * 2 + h * 24576 + wv * 2048;
  const unsigned role = wv < 2 ? wv : (wv == 3 ? 4 : (part < 2 ? 2 + part : 5));
  u32x16 sc = {(unsigned)lane, (unsigned)wv, role, O_SLOTS, k0, O_MFRAG + (min(h, 27) * 4 + part) * 32768 + wv * 4096, v0, rot << 19, O_KVM, O_VTM,
                     O_TFLAG + h * 64, O_KVSTEP + h * 64 + part * 4, O_STAMPS + (b * 4 + wv) * 65 * 64,
                     O_EPOCH, O_MFLAG + h * 128, (unsigned)wv * 2048};
  u32x16 s2 = {(unsigned)woa, (unsigned)(woa >> 32) & 0xffffu, DM * HD * 2, 0x00020000u, (unsigned)wva, (unsigned)(wva >> 32) & 0xffffu, O_WM, O_WM + b * 128,
               (unsigned)wsa, (unsigned)(wsa >> 32) & 0xffffu, WS_BYTES, 0x00020000u,
               (unsigned)wqa, (unsigned)(wqa >> 32) & 0xffffu, (unsigned)wka, (unsigned)(wka >> 32) & 0xffffu};
  asm volatile(
      "s_nop 1\n\t"
      "v_readfirstlane_b32 s40, v0\n\t"
      "v_readfirstlane_b32 s41, v1\n\t"
      "v_readfirstlane_b32 s42, v2\n\t"
      "v_readfirstlane_b32 s43, v3\n\t"
      "v_readfirstlane_b32 s44, v4\n\t"
      "v_readfirstlane_b32 s45, v5\n\t"
      "v_readfirstlane_b32 s46, v6\n\t"
      "v_readfirstlane_b32 s47, v7\n\t"
      "v_readfirstlane_b32 s48, v8\n\t"
      "v_readfirstlane_b32 s49, v9\n\t"
      "v_readfirstlane_b32 s50, v10\n\t"
      "v_readfirstlane_b32 s51, v11\n\t"
      "v_readfirstlane_b32 s52, v12\n\t"
      "v_readfirstlane_b32 s53, v13\n\t"
      "v_readfirstlane_b32 s54, v14\n\t"
      "v_readfirstlane_b32 s55, v15\n\t"
      "v_readfirstlane_b32 s88, v16\n\t"
      "v_readfirstlane_b32 s89, v17\n\t"
      "v_readfirstlane_b32 s90, v18\n\t"
      "v_readfirstlane_b32 s91, v19\n\t"
      "v_readfirstlane_b32 s92, v20\n\t"
      "v_readfirstlane_b32 s93, v21\n\t"
      "v_readfirstlane_b32 s94, v22\n\t"
      "v_readfirstlane_b32 s95, v23\n\t"
      "v_readfirstlane_b32 s36, v24\n\t"
      "v_readfirstlane_b32 s37, v25\n\t"
      "v_readfirstlane_b32 s38, v26\n\t"
      "v_readfirstlane_b32 s39, v27\n\t"
      "v_readfirstlane_b32 s96, v28\n\t"
      "v_readfirstlane_b32 s97, v29\n\t"
      "v_readfirstlane_b32 s98, v30\n\t"
      "v_readfirstlane_b32 s99, v31\n\t"
      "s_nop 4\n\t"
      "s_memtime s[80:81]\n\t"
      "s_memrealtime s[82:83]\n\t"
      "v_readfirstlane_b32 s86, v120\n\t"
      "v_add_u32 v100, s55, v127\n\t"
      "s_mov_b64 s[88:89], s[92:93]\n\t"
      "s_add_u32 m0, s55, 32768\n\t"
      "s_xor_b32 s61, s47, 0\n\t"
      "buffer_load_dwordx4 v105, s[88:91], s61 offen lds\n\t"
      "s_add_u32 m0, s55, 33792\n\t"
      "s_xor_b32 s61, s47, 32768\n\t"
      "buffer_load_dwordx4 v105, s[88:91], s61 offen lds\n\t"
      "s_add_u32 m0, s55, 40960\n\t"
      "s_xor_b32 s61, s47, 262144\n\t"
      "buffer_load_dwordx4 v105, s[88:91], s61 offen lds\n\t"
      "s_add_u32 m0, s55, 41984\n\t"
      "s_xor_b32 s61, s47, 294912\n\t"
      "buffer_load_dwordx4 v105, s[88:91], s61 offen lds\n\t"
      "s_add_u32 m0, s55, 49152\n\t"
      "s_xor_b32 s61, s47, 524288\n\t"
      "buffer_load_dwordx4 v105, s[88:91], s61 offen lds\n\t"
      "s_add_u32 m0, s55, 50176\n\t"
      "s_xor_b32 s61, s47, 557056\n\t"
      "buffer_load_dwordx4 v105, s[88:91], s61 offen lds\n\t"
      "s_add_u32 m0, s55, 57344\n\t"
      "s_xor_b32 s61, s47, 786432\n\t"
      "buffer_load_dwordx4 v105, s[88:91], s61 offen lds\n\t"
      "s_add_u32 m0, s55, 58368\n\t"
      "s_xor_b32 s61, s47, 819200\n\t"
      "buffer_load_dwordx4 v105, s[88:91], s61 offen lds\n\t"
      "buffer_load_dword v126, off, s[36:39], s53 sc1\n\t"
      "global_load_dword a172, v[118:119], off offset:0\n\t"
      "global_load_dword a173, v[118:119], off offset:8\n\t"
      "global_load_dword a174, v[118:119], off offset:16\n\t"
      "global_load_dword a175, v[118:119], off offset:24\n\t"
      "global_load_dword a236, v[118:119], off offset:512\n\t"
      "global_load_dword a237, v[118:119], off offset:520\n\t"
      "global_load_dword a238, v[118:119], off offset:528\n\t"
      "global_load_dword a239, v[118:119], off offset:536\n\t"
      "s_xor_b32 s61, s86, 0x2000\n\t"
      "s_buffer_load_dword s87, s[36:39], s61\n\t"
      "s_waitcnt vmcnt(17)\n\t"
      "s_waitcnt lgkmcnt(0)\n\t"
      "ds_read_b64_tr_b16 v[64:65], v102\n\t"
      "ds_read_b64_tr_b16 v[66:67], v102 offset:256\n\t"
      "ds_read_b64_tr_b16 v[68:69], v102 offset:8\n\t"
      "ds_read_b64_tr_b16 v[70:71], v102 offset:264\n\t"
      "ds_read_b64_tr_b16 v[72:73], v102 offset:8192\n\t"
      "ds_read_b64_tr_b16 v[74:75], v102 offset:8448\n\t"
      "ds_read_b64_tr_b16 v[76:77], v102 offset:8200\n\t"
      "ds_read_b64_tr_b16 v[78:79], v102 offset:8456\n\t"
      "ds_read_b64_tr_b16 v[80:81], v102 offset:16384\n\t"
      "ds_read_b64_tr_b16 v[82:83], v102 offset:16640\n\t"
      "ds_read_b64_tr_b16 v[84:85], v102 offset:16392\n\t"
      "ds_read_b64_tr_b16 v[86:87], v102 offset:16648\n\t"
      "s_waitcnt lgkmcnt(4)\n\t"
      "ds_read_b64_tr_b16 v[88:89], v102 offset:24576\n\t"
      "ds_read_b64_tr_b16 v[90:91], v102 offset:24832\n\t"
      "ds_read_b64_tr_b16 v[92:93], v102 offset:24584\n\t"
      "ds_read_b64_tr_b16 v[94:95], v102 offset:24840\n\t"
      "s_mov_b64 s[88:89], s[96:97]\n\t"
      "s_add_u32 m0, s55, 98304\n\t"
      "s_xor_b32 s61, s47, 0\n\t"
      "buffer_load_dwordx4 v116, s[88:91], s61 offen lds\n\t"
      "s_add_u32 m0, s55, 99328\n\t"
      "s_xor_b32 s61, s47, 32768\n\t"
      "buffer_load_dwordx4 v116, s[88:91], s61 offen lds\n\t"
      "s_add_u32 m0, s55, 106496\n\t"
      "s_xor_b32 s61, s47, 262144\n\t"
      "buffer_load_dwordx4 v116, s[88:91], s61 offen lds\n\t"
      "s_add_u32 m0, s55, 107520\n\t"
      "s_xor_b32 s61, s47, 294912\n\t"
      "buffer_load_dwordx4 v116, s[88:91], s61 offen lds\n\t"
      "s_waitcnt lgkmcnt(0)\n\t"
      "ds_write_b128 v103, v[64:67]\n\t"
      "ds_write_b128 v103, v[68:71] offset:32\n\t"
      "ds_write_b128 v103, v[72:75] offset:8192\n\t"
      "ds_write_b128 v103, v[76:79] offset:8224\n\t"
      "ds_write_b128 v103, v[80:83] offset:16384\n\t"
      "ds_write_b128 v103, v[84:87] offset:16416\n\t"
      "ds_write_b128 v103, v[88:91] offset:24576\n\t"
      "ds_write_b128 v103, v[92:95] offset:24608\n\t"
      "s_waitcnt lgkmcnt(4)\n\t"
      "ds_read_b128 v[32:35], v100\n\t"
      "ds_read_b128 v[36:39], v100 offset:1024\n\t"
      "ds_read_b128 v[40:43], v100 offset:8192\n\t"
      "ds_read_b128 v[44:47], v100 offset:9216\n\t"
      "ds_read_b128 v[48:51], v100 offset:16384\n\t"
      "ds_read_b128 v[52:55], v100 offset:17408\n\t"
      "ds_read_b128 v[56:59], v100 offset:24576\n\t"
      "ds_read_b128 v[60:63], v100 offset:25600\n\t"
      "v_xor_b32 v118, 32, v117\n\t"
      "v_xor_b32 v119, 64, v104\n\t"
      "v_xor_b32 v102, 128, v104\n\t"
      "v_xor_b32 v103, 192, v104\n\t"
      "s_mov_b64 s[88:89], s[92:93]\n\t"
      "s_add_u32 m0, s55, 65536\n\t"
      "s_xor_b32 s61, s47, 1048576\n\t"
      "buffer_load_dwordx4 v105, s[88:91], s61 offen lds\n\t"
      "s_add_u32 m0, s55, 66560\n\t"
      "s_xor_b32 s61, s47, 1081344\n\t"
      "buffer_load_dwordx4 v105, s[88:91], s61 offen lds\n\t"
      "s_mov_b64 s[88:89], s[96:97]\n\t"
      "s_add_u32 m0, s55, 114688\n\t"
      "s_xor_b32 s61, s47, 524288\n\t"
      "buffer_load_dwordx4 v116, s[88:91], s61 offen lds\n\t"
      "s_add_u32 m0, s55, 115712\n\t"
      "s_xor_b32 s61, s47, 557056\n\t"
      "buffer_load_dwordx4 v116, s[88:91], s61 offen lds\n\t"
      "s_add_u32 m0, s55, 122880\n\t"
      "s_xor_b32 s61, s47, 786432\n\t"
      "buffer_load_dwordx4 v116, s[88:91], s61 offen lds\n\t"
      "s_add_u32 m0, s55, 123904\n\t"
      "s_xor_b32 s61, s47, 819200\n\t"
      "buffer_load_dwordx4 v116, s[88:91], s61 offen lds\n\t"
      "s_waitcnt lgkmcnt(0)\n\t"
      "s_waitcnt vmcnt(25)\n\t"
      "s_barrier\n\t"
      "ds_read_b128 v[0:3], v104\n\t"
      "ds_read_b128 v[4:7], v119\n\t"
      "ds_read_b128 v[8:11], v102\n\t"
      "ds_read_b128 v[12:15], v103\n\t"
      "ds_read_b128 v[64:67], v104 offset:1024\n\t"
      "ds_read_b128 v[68:71], v119 offset:1024\n\t"
      "ds_read_b128 v[72:75], v102 offset:1024\n\t"
      "ds_read_b128 v[76:79], v103 offset:1024\n\t"
      "s_mov_b64 s[88:89], s[92:93]\n\t"
      "s_add_u32 m0, s55, 73728\n\t"
      "s_xor_b32 s61, s47, 1310720\n\t"
      "buffer_load_dwordx4 v105, s[88:91], s61 offen lds\n\t"
      "s_add_u32 m0, s55, 74752\n\t"
      "s_xor_b32 s61, s47, 1343488\n\t"
      "buffer_load_dwordx4 v105, s[88:91], s61 offen lds\n\t"
      "s_mov_b64 s[88:89], s[96:97]\n\t"
      "s_add_u32 m0, s55, 131072\n\t"
      "s_xor_b32 s61, s47, 1048576\n\t"
      "buffer_load_dwordx4 v116, s[88:91], s61 offen lds\n\t"
      "s_add_u32 m0, s55, 132096\n\t"
      "s_xor_b32 s61, s47, 1081344\n\t"
      "buffer_load_dwordx4 v116, s[88:91], s61 offen lds\n\t"
      "s_add_u32 m0, s55, 139264\n\t"
      "s_xor_b32 s61, s47, 1310720\n\t"
      "buffer_load_dwordx4 v116, s[88:91], s61 offen lds\n\t"
      "s_add_u32 m0, s55, 140288\n\t"
      "s_xor_b32 s61, s47, 1343488\n\t"
      "buffer_load_dwordx4 v116, s[88:91], s61 offen lds\n\t"
      "s_mov_b64 s[88:89], s[92:93]\n\t"
      "s_add_u32 m0, s55, 81920\n\t"
      "s_xor_b32 s61, s47, 1572864\n\t"
      "buffer_load_dwordx4 v105, s[88:91], s61 offen lds\n\t"
      "s_add_u32 m0, s55, 82944\n\t"
      "s_xor_b32 s61, s47, 1605632\n\t"
      "buffer_load_dwordx4 v105, s[88:91], s61 offen lds\n\t"
      "s_waitcnt vmcnt(31)\n\t"
      "s_barrier\n\t"
      "s_waitcnt lgkmcnt(7)\n\t"
      "v_mfma_f32_16x16x32_bf16 v[16:19], v[0:3], v[32:35], 0\n\t"
      "ds_read_b128 v[80:83], v104 offset:8192\n\t"
      "v_mfma_f32_16x16x32_bf16 v[20:23], v[0:3], v[36:39], 0\n\t"
      "s_mov_b64 s[88:89], s[96:97]\n\t"
      "s_add_u32 m0, s55, 147456\n\t"
      "s_xor_b32 s61, s47, 1572864\n\t"
      "buffer_load_dwordx4 v116, s[88:91], s61 offen lds\n\t"
      "ds_read_b64_tr_b16 a[176:177], v106\n\t"
      "s_waitcnt lgkmcnt(8)\n\t"
      "v_mfma_f32_16x16x32_bf16 v[16:19], v[4:7], v[40:43], v[16:19]\n\t"
      "ds_read_b128 v[84:87], v119 offset:8192\n\t"
      "v_mfma_f32_16x16x32_bf16 v[20:23], v[4:7], v[44:47], v[20:23]\n\t"
      "s_add_u32 m0, s55, 148480\n\t"
      "s_xor_b32 s61, s47, 1605632\n\t"
      "buffer_load_dwordx4 v116, s[88:91], s61 offen lds\n\t"
      "ds_read_b64_tr_b16 a[178:179], v106 offset:1024\n\t"
      "s_waitcnt lgkmcnt(9)\n\t"
      "v_mfma_f32_16x16x32_bf16 v[16:19], v[8:11], v[48:51], v[16:19]\n\t"
      "ds_read_b128 v[88:91], v102 offset:8192\n\t"
      "v_mfma_f32_16x16x32_bf16 v[20:23], v[8:11], v[52:55], v[20:23]\n\t"
      "s_add_u32 m0, s55, 155648\n\t"
      "s_xor_b32 s61, s47, 1835008\n\t"
      "buffer_load_dwordx4 v116, s[88:91], s61 offen lds\n\t"
      "s_add_u32 m0, s55, 156672\n\t"
      "s_xor_b32 s61, s47, 1867776\n\t"
      "buffer_load_dwordx4 v116, s[88:91], s61 offen lds\n\t"
      "s_waitcnt lgkmcnt(9)\n\t"
      "v_mfma_f32_16x16x32_bf16 v[16:19], v[12:15], v[56:59], v[16:19]\n\t"
      "ds_read_b128 v[92:95], v103 offset:8192\n\t"
      "v_mfma_f32_16x16x32_bf16 v[20:23], v[12:15], v[60:63], v[20:23]\n\t"
      "s_mov_b64 s[88:89], s[92:93]\n\t"
      "s_add_u32 m0, s55, 90112\n\t"
      "s_xor_b32 s61, s47, 1835008\n\t"
      "buffer_load_dwordx4 v105, s[88:91], s61 offen lds\n\t"
      "s_add_u32 m0, s55, 91136\n\t"
      "s_xor_b32 s61, s47, 1867776\n\t"
      "buffer_load_dwordx4 v105, s[88:91], s61 offen lds\n\t"
      "s_waitcnt lgkmcnt(9)\n\t"
      "v_mfma_f32_16x16x32_bf16 v[24:27], v[64:67], v[32:35], 0\n\t"
      "ds_read_b128 v[0:3], v104 offset:9216\n\t"
      "v_mfma_f32_16x16x32_bf16 v[28:31], v[64:67], v[36:39], 0\n\t"
      "s_waitcnt lgkmcnt(9)\n\t"
      "v_mfma_f32_16x16x32_bf16 v[24:27], v[68:71], v[40:43], v[24:27]\n\t"
      "ds_read_b128 v[4:7], v119 offset:9216\n\t"
      "v_cvt_pk_bf16_f32 v128, v16, v17\n\t"
      "v_mfma_f32_16x16x32_bf16 v[28:31], v[68:71], v[44:47], v[28:31]\n\t"
      "v_cvt_pk_bf16_f32 v129, v18, v19\n\t"
      "s_waitcnt lgkmcnt(9)\n\t"
      "v_mfma_f32_16x16x32_bf16 v[24:27], v[72:75], v[48:51], v[24:27]\n\t"
      "ds_read_b128 v[8:11], v102 offset:9216\n\t"
      "v_cvt_pk_bf16_f32 v192, v20, v21\n\t"
      "v_mfma_f32_16x16x32_bf16 v[28:31], v[72:75], v[52:55], v[28:31]\n\t"
      "v_cvt_pk_bf16_f32 v193, v22, v23\n\t"
      "s_waitcnt lgkmcnt(9)\n\t"
      "v_mfma_f32_16x16x32_bf16 v[24:27], v[76:79], v[56:59], v[24:27]\n\t"
      "ds_read_b128 v[12:15], v103 offset:9216\n\t"
      "v_mfma_f32_16x16x32_bf16 v[28:31], v[76:79], v[60:63], v[28:31]\n\t"
      "s_waitcnt vmcnt(35)\n\t"
      "s_waitcnt lgkmcnt(6)\n\t"
      "s_barrier\n\t"
      "v_mfma_f32_16x16x32_bf16 v[16:19], v[80:83], v[32:35], 0\n\t"
      "ds_read_b128 v[64:67], v104 offset:16384\n\t"
      "v_mfma_f32_16x16x32_bf16 v[20:23], v[80:83], v[36:39], 0\n\t"
      "s_add_u32 m0, s55, 32768\n\t"
      "s_xor_b32 s61, s47, 2097152\n\t"
      "buffer_load_dwordx4 v105, s[88:91], s61 offen lds\n\t"
      "s_add_u32 m0, s55, 33792\n\t"
      "s_xor_b32 s61, s47, 2129920\n\t"
      "buffer_load_dwordx4 v105, s[88:91], s61 offen lds\n\t"
      "v_mfma_f32_16x16x32_bf16 v[16:19], v[84:87], v[40:43], v[16:19]\n\t"
      "ds_read_b128 v[68:71], v119 offset:16384\n\t"
      "v_cvt_pk_bf16_f32 v130, v24, v25\n\t"
      "v_mfma_f32_16x16x32_bf16 v[20:23], v[84:87], v[44:47], v[20:23]\n\t"
      "v_cvt_pk_bf16_f32 v131, v26, v27\n\t"
      "s_waitcnt lgkmcnt(7)\n\t"
      "v_mfma_f32_16x16x32_bf16 v[16:19], v[88:91], v[48:51], v[16:19]\n\t"
      "ds_read_b128 v[72:75], v102 offset:16384\n\t"
      "v_cvt_pk_bf16_f32 v194, v28, v29\n\t"
      "v_mfma_f32_16x16x32_bf16 v[20:23], v[88:91], v[52:55], v[20:23]\n\t"
      "v_cvt_pk_bf16_f32 v195, v30, v31\n\t"
      "s_waitcnt lgkmcnt(7)\n\t"
      "v_mfma_f32_16x16x32_bf16 v[16:19], v[92:95], v[56:59], v[16:19]\n\t"
      "ds_read_b128 v[76:79], v103 offset:16384\n\t"
      "v_mfma_f32_16x16x32_bf16 v[20:23], v[92:95], v[60:63], v[20:23]\n\t"
      "v_mfma_f32_16x16x32_bf16 a[168:171], v[128:131], v[128:131], 0\n\t"
      "s_waitcnt lgkmcnt(7)\n\t"
      "v_mfma_f32_16x16x32_bf16 v[24:27], v[0:3], v[32:35], 0\n\t"
      "ds_read_b128 v[80:83], v104 offset:17408\n\t"
      "v_mfma_f32_16x16x32_bf16 v[28:31], v[0:3], v[36:39], 0\n\t"
      "s_waitcnt lgkmcnt(7)\n\t"
      "v_mfma_f32_16x16x32_bf16 v[24:27], v[4:7], v[40:43], v[24:27]\n\t"
      "ds_read_b128 v[84:87], v119 offset:17408\n\t"
      "v_cvt_pk_bf16_f32 v132, v16, v17\n\t"
      "v_mfma_f32_16x16x32_bf16 v[28:31], v[4:7], v[44:47], v[28:31]\n\t"
      "v_cvt_pk_bf16_f32 v133, v18, v19\n\t"
      "s_waitcnt lgkmcnt(7)\n\t"
      "v_mfma_f32_16x16x32_bf16 v[24:27], v[8:11], v[48:51], v[24:27]\n\t"
      "ds_read_b128 v[88:91], v102 offset:17408\n\t"
      "v_cvt_pk_bf16_f32 v196, v20, v21\n\t"
      "v_mfma_f32_16x16x32_bf16 v[28:31], v[8:11], v[52:55], v[28:31]\n\t"
      "v_cvt_pk_bf16_f32 v197, v22, v23\n\t"
      "s_waitcnt lgkmcnt(7)\n\t"
      "v_mfma_f32_16x16x32_bf16 v[24:27], v[12:15], v[56:59], v[24:27]\n\t"
      "ds_read_b128 v[92:95], v103 offset:17408\n\t"
      "v_mfma_f32_16x16x32_bf16 v[28:31], v[12:15], v[60:63], v[28:31]\n\t"
      "v_mfma_f32_16x16x32_bf16 a[232:235], v[192:195], v[192:195], 0\n\t"
      "s_waitcnt vmcnt(35)\n\t"
      "s_waitcnt vmcnt(22)\n\t"
      "s_barrier\n\t"
      "s_waitcnt lgkmcnt(7)\n\t"
      "v_mfma_f32_16x16x32_bf16 v[16:19], v[64:67], v[32:35], 0\n\t"
      "ds_read_b128 v[0:3], v104 offset:24576\n\t"
      "v_mfma_f32_16x16x32_bf16 v[20:23], v[64:67], v[36:39], 0\n\t"
      "s_add_u32 m0, s55, 40960\n\t"
      "s_xor_b32 s61, s47, 2359296\n\t"
      "buffer_load_dwordx4 v105, s[88:91], s61 offen lds\n\t"
      "ds_read_b64_tr_b16 a[180:181], v106 offset:16384\n\t"
      "s_waitcnt lgkmcnt(8)\n\t"
      "v_mfma_f32_16x16x32_bf16 v[16:19], v[68:71], v[40:43], v[16:19]\n\t"
      "ds_read_b128 v[4:7], v119 offset:24576\n\t"
      "v_cvt_pk_bf16_f32 v134, v24, v25\n\t"
      "v_mfma_f32_16x16x32_bf16 v[20:23], v[68:71], v[44:47], v[20:23]\n\t"
      "s_add_u32 m0, s55, 41984\n\t"
      "s_xor_b32 s61, s47, 2392064\n\t"
      "buffer_load_dwordx4 v105, s[88:91], s61 offen lds\n\t"
      "ds_read_b64_tr_b16 a[182:183], v106 offset:17408\n\t"
      "v_cvt_pk_bf16_f32 v135, v26, v27\n\t"
      "s_waitcnt lgkmcnt(9)\n\t"
      "v_mfma_f32_16x16x32_bf16 v[16:19], v[72:75], v[48:51], v[16:19]\n\t"
      "ds_read_b128 v[8:11], v102 offset:24576\n\t"
      "v_cvt_pk_bf16_f32 v198, v28, v29\n\t"
      "v_mfma_f32_16x16x32_bf16 v[20:23], v[72:75], v[52:55], v[20:23]\n\t"
      "ds_read_b64_tr_b16 a[0:1], v117\n\t"
      "ds_read_b64_tr_b16 a[2:3], v117 offset:1024\n\t"
      "v_cvt_pk_bf16_f32 v199, v30, v31\n\t"
      "s_waitcnt lgkmcnt(11)\n\t"
      "v_mfma_f32_16x16x32_bf16 v[16:19], v[76:79], v[56:59], v[16:19]\n\t"
      "ds_read_b128 v[12:15], v103 offset:24576\n\t"
      "v_mfma_f32_16x16x32_bf16 v[20:23], v[76:79], v[60:63], v[20:23]\n\t"
      "ds_read_b64_tr_b16 a[64:65], v118\n\t"
      "ds_read_b64_tr_b16 a[66:67], v118 offset:1024\n\t"
      "v_mfma_f32_16x16x32_bf16 a[168:171], v[132:135], v[132:135], a[168:171]\n\t"
      "s_waitcnt lgkmcnt(13)\n\t"
      "v_mfma_f32_16x16x32_bf16 v[24:27], v[80:83], v[32:35], 0\n\t"
      "ds_read_b128 v[64:67], v104 offset:25600\n\t"
      "v_mfma_f32_16x16x32_bf16 v[28:31], v[80:83], v[36:39], 0\n\t"
      "ds_read_b64_tr_b16 a[4:5], v117 offset:8192\n\t"
      "s_waitcnt lgkmcnt(12)\n\t"
      "ds_read_b64_tr_b16 a[6:7], v117 offset:9216\n\t"
      "v_mfma_f32_16x16x32_bf16 v[24:27], v[84:87], v[40:43], v[24:27]\n\t"
      "ds_read_b128 v[68:71], v119 offset:25600\n\t"
      "v_cvt_pk_bf16_f32 v136, v16, v17\n\t"
      "v_mfma_f32_16x16x32_bf16 v[28:31], v[84:87], v[44:47], v[28:31]\n\t"
      "ds_read_b64_tr_b16 a[68:69], v118 offset:8192\n\t"
      "s_waitcnt lgkmcnt(12)\n\t"
      "ds_read_b64_tr_b16 a[70:71], v118 offset:9216\n\t"
      "v_cvt_pk_bf16_f32 v137, v18, v19\n\t"
      "v_mfma_f32_16x16x32_bf16 v[24:27], v[88:91], v[48:51], v[24:27]\n\t"
      "ds_read_b128 v[72:75], v102 offset:25600\n\t"
      "v_cvt_pk_bf16_f32 v200, v20, v21\n\t"
      "v_mfma_f32_16x16x32_bf16 v[28:31], v[88:91], v[52:55], v[28:31]\n\t"
      "v_cvt_pk_bf16_f32 v201, v22, v23\n\t"
      "v_mfma_f32_16x16x32_bf16 v[24:27], v[92:95], v[56:59], v[24:27]\n\t"
      "ds_read_b128 v[76:79], v103 offset:25600\n\t"
      "v_mfma_f32_16x16x32_bf16 v[28:31], v[92:95], v[60:63], v[28:31]\n\t"
      "v_mfma_f32_16x16x32_bf16 a[232:235], v[196:199], v[196:199], a[232:235]\n\t"
      "s_waitcnt vmcnt(22)\n\t"
      "s_waitcnt vmcnt(18)\n\t"
      "s_waitcnt lgkmcnt(2)\n\t"
      "s_barrier\n\t"
      "v_mfma_f32_16x16x32_bf16 v[16:19], v[0:3], v[32:35], 0\n\t"
      "ds_read_b128 v[80:83], v104 offset:32768\n\t"
      "v_mfma_f32_16x16x32_bf16 v[20:23], v[0:3], v[36:39], 0\n\t"
      "s_add_u32 m0, s55, 49152\n\t"
      "s_xor_b32 s61, s47, 2621440\n\t"
      "buffer_load_dwordx4 v105, s[88:91], s61 offen lds\n\t"
      "ds_read_b64_tr_b16 a[8:9], v117 offset:16384\n\t"
      "v_mfma_f32_16x16x32_bf16 v[16:19], v[4:7], v[40:43], v[16:19]\n\t"
      "ds_read_b128 v[84:87], v119 offset:32768\n\t"
      "v_cvt_pk_bf16_f32 v138, v24, v25\n\t"
      "v_mfma_f32_16x16x32_bf16 v[20:23], v[4:7], v[44:47], v[20:23]\n\t"
      "s_add_u32 m0, s55, 50176\n\t"
      "s_xor_b32 s61, s47, 2654208\n\t"
      "buffer_load_dwordx4 v105, s[88:91], s61 offen lds\n\t"
      "ds_read_b64_tr_b16 a[10:11], v117 offset:17408\n\t"
      "v_cvt_pk_bf16_f32 v139, v26, v27\n\t"
      "v_mfma_f32_16x16x32_bf16 v[16:19], v[8:11], v[48:51], v[16:19]\n\t"
      "ds_read_b128 v[88:91], v102 offset:32768\n\t"
      "v_cvt_pk_bf16_f32 v202, v28, v29\n\t"
      "v_mfma_f32_16x16x32_bf16 v[20:23], v[8:11], v[52:55], v[20:23]\n\t"
      "s_mov_b64 s[88:89], s[96:97]\n\t"
      "s_add_u32 m0, s55, 98304\n\t"
      "s_xor_b32 s61, s47, 2097152\n\t"
      "buffer_load_dwordx4 v116, s[88:91], s61 offen lds\n\t"
      "ds_read_b64_tr_b16 a[72:73], v118 offset:16384\n\t"
      "v_cvt_pk_bf16_f32 v203, v30, v31\n\t"
      "v_mfma_f32_16x16x32_bf16 v[16:19], v[12:15], v[56:59], v[16:19]\n\t"
      "ds_read_b128 v[92:95], v103 offset:32768\n\t"
      "v_mfma_f32_16x16x32_bf16 v[20:23], v[12:15], v[60:63], v[20:23]\n\t"
      "s_add_u32 m0, s55, 99328\n\t"
      "s_xor_b32 s61, s47, 2129920\n\t"
      "buffer_load_dwordx4 v116, s[88:91], s61 offen lds\n\t"
      "ds_read_b64_tr_b16 a[74:75], v118 offset:17408\n\t"
      "v_mfma_f32_16x16x32_bf16 a[168:171], v[136:139], v[136:139], a[168:171]\n\t"
      "v_mfma_f32_16x16x32_bf16 v[24:27], v[64:67], v[32:35], 0\n\t"
      "ds_read_b128 v[0:3], v104 offset:33792\n\t"
      "v_mfma_f32_16x16x32_bf16 v[28:31], v[64:67], v[36:39], 0\n\t"
      "s_add_u32 m0, s55, 106496\n\t"
      "s_xor_b32 s61, s47, 2359296\n\t"
      "buffer_load_dwordx4 v116, s[88:91], s61 offen lds\n\t"
      "ds_read_b64_tr_b16 a[12:13], v117 offset:24576\n\t"
      "v_mfma_f32_16x16x32_bf16 v[24:27], v[68:71], v[40:43], v[24:27]\n\t"
      "ds_read_b128 v[4:7], v119 offset:33792\n\t"
      "v_cvt_pk_bf16_f32 v140, v16, v17\n\t"
      "v_mfma_f32_16x16x32_bf16 v[28:31], v[68:71], v[44:47], v[28:31]\n\t"
      "s_add_u32 m0, s55, 107520\n\t"
      "s_xor_b32 s61, s47, 2392064\n\t"
      "buffer_load_dwordx4 v116, s[88:91], s61 offen lds\n\t"
      "ds_read_b64_tr_b16 a[14:15], v117 offset:25600\n\t"
      "v_cvt_pk_bf16_f32 v141, v18, v19\n\t"
      "s_waitcnt lgkmcnt(13)\n\t"
      "v_mfma_f32_16x16x32_bf16 v[24:27], v[72:75], v[48:51], v[24:27]\n\t"
      "ds_read_b128 v[8:11], v102 offset:33792\n\t"
      "v_cvt_pk_bf16_f32 v204, v20, v21\n\t"
      "v_mfma_f32_16x16x32_bf16 v[28:31], v[72:75], v[52:55], v[28:31]\n\t"
      "ds_read_b64_tr_b16 a[76:77], v118 offset:24576\n\t"
      "s_waitcnt lgkmcnt(12)\n\t"
      "ds_read_b64_tr_b16 a[78:79], v118 offset:25600\n\t"
      "v_cvt_pk_bf16_f32 v205, v22, v23\n\t"
      "v_mfma_f32_16x16x32_bf16 v[24:27], v[76:79], v[56:59], v[24:27]\n\t"
      "ds_read_b128 v[12:15], v103 offset:33792\n\t"
      "v_mfma_f32_16x16x32_bf16 v[28:31], v[76:79], v[60:63], v[28:31]\n\t"
      "v_mfma_f32_16x16x32_bf16 a[232:235], v[200:203], v[200:203], a[232:235]\n\t"
      "s_waitcnt vmcnt(22)\n\t"
      "s_waitcnt vmcnt(18)\n\t"
      "s_waitcnt lgkmcnt(1)\n\t"
      "s_barrier\n\t"
      "v_mfma_f32_16x16x32_bf16 v[16:19], v[80:83], v[32:35], 0\n\t"
      "ds_read_b128 v[64:67], v104 offset:40960\n\t"
      "v_mfma_f32_16x16x32_bf16 v[20:23], v[80:83], v[36:39], 0\n\t"
      "s_mov_b64 s[88:89], s[92:93]\n\t"
      "s_add_u32 m0, s55, 57344\n\t"
      "s_xor_b32 s61, s47, 2883584\n\t"
      "buffer_load_dwordx4 v105, s[88:91], s61 offen lds\n\t"
      "ds_read_b64_tr_b16 a[184:185], v106 offset:32768\n\t"
      "v_mfma_f32_16x16x32_bf16 v[16:19], v[84:87], v[40:43], v[16:19]\n\t"
      "ds_read_b128 v[68:71], v119 offset:40960\n\t"
      "v_cvt_pk_bf16_f32 v142, v24, v25\n\t"
      "v_mfma_f32_16x16x32_bf16 v[20:23], v[84:87], v[44:47], v[20:23]\n\t"
      "s_add_u32 m0, s55, 58368\n\t"
      "s_xor_b32 s61, s47, 2916352\n\t"
      "buffer_load_dwordx4 v105, s[88:91], s61 offen lds\n\t"
      "ds_read_b64_tr_b16 a[186:187], v106 offset:33792\n\t"
      "v_cvt_pk_bf16_f32 v143, v26, v27\n\t"
      "v_mfma_f32_16x16x32_bf16 v[16:19], v[88:91], v[48:51], v[16:19]\n\t"
      "ds_read_b128 v[72:75], v102 offset:40960\n\t"
      "v_cvt_pk_bf16_f32 v206, v28, v29\n\t"
      "v_mfma_f32_16x16x32_bf16 v[20:23], v[88:91], v[52:55], v[20:23]\n\t"
      "s_mov_b64 s[88:89], s[96:97]\n\t"
      "s_add_u32 m0, s55, 114688\n\t"
      "s_xor_b32 s61, s47, 2621440\n\t"
      "buffer_load_dwordx4 v116, s[88:91], s61 offen lds\n\t"
      "ds_read_b64_tr_b16 a[16:17], v117 offset:32768\n\t"
      "v_cvt_pk_bf16_f32 v207, v30, v31\n\t"
      "v_mfma_f32_16x16x32_bf16 v[16:19], v[92:95], v[56:59], v[16:19]\n\t"
      "ds_read_b128 v[76:79], v103 offset:40960\n\t"
      "v_mfma_f32_16x16x32_bf16 v[20:23], v[92:95], v[60:63], v[20:23]\n\t"
      "s_add_u32 m0, s55, 115712\n\t"
      "s_xor_b32 s61, s47, 2654208\n\t"
      "buffer_load_dwordx4 v116, s[88:91], s61 offen lds\n\t"
      "ds_read_b64_tr_b16 a[18:19], v117 offset:33792\n\t"
      "v_mfma_f32_16x16x32_bf16 a[168:171], v[140:143], v[140:143], a[168:171]\n\t"
      "v_mfma_f32_16x16x32_bf16 v[24:27], v[0:3], v[32:35], 0\n\t"
      "ds_read_b128 v[80:83], v104 offset:41984\n\t"
      "v_mfma_f32_16x16x32_bf16 v[28:31], v[0:3], v[36:39], 0\n\t"
      "s_add_u32 m0, s55, 122880\n\t"
      "s_xor_b32 s61, s47, 2883584\n\t"
      "buffer_load_dwordx4 v116, s[88:91], s61 offen lds\n\t"
      "ds_read_b64_tr_b16 a[80:81], v118 offset:32768\n\t"
      "v_mfma_f32_16x16x32_bf16 v[24:27], v[4:7], v[40:43], v[24:27]\n\t"
      "ds_read_b128 v[84:87], v119 offset:41984\n\t"
      "v_cvt_pk_bf16_f32 v144, v16, v17\n\t"
      "v_mfma_f32_16x16x32_bf16 v[28:31], v[4:7], v[44:47], v[28:31]\n\t"
      "s_add_u32 m0, s55, 123904\n\t"
      "s_xor_b32 s61, s47, 2916352\n\t"
      "buffer_load_dwordx4 v116, s[88:91], s61 offen lds\n\t"
      "ds_read_b64_tr_b16 a[82:83], v118 offset:33792\n\t"
      "v_cvt_pk_bf16_f32 v145, v18, v19\n\t"
      "v_mfma_f32_16x16x32_bf16 v[24:27], v[8:11], v[48:51], v[24:27]\n\t"
      "ds_read_b128 v[88:91], v102 offset:41984\n\t"
      "v_cvt_pk_bf16_f32 v208, v20, v21\n\t"
      "v_mfma_f32_16x16x32_bf16 v[28:31], v[8:11], v[52:55], v[28:31]\n\t"
      "ds_read_b64_tr_b16 a[20:21], v117 offset:40960\n\t"
      "s_waitcnt lgkmcnt(12)\n\t"
      "ds_read_b64_tr_b16 a[22:23], v117 offset:41984\n\t"
      "v_cvt_pk_bf16_f32 v209, v22, v23\n\t"
      "v_mfma_f32_16x16x32_bf16 v[24:27], v[12:15], v[56:59], v[24:27]\n\t"
      "ds_read_b128 v[92:95], v103 offset:41984\n\t"
      "v_mfma_f32_16x16x32_bf16 v[28:31], v[12:15], v[60:63], v[28:31]\n\t"
      "ds_read_b64_tr_b16 a[84:85], v118 offset:40960\n\t"
      "s_waitcnt lgkmcnt(12)\n\t"
      "ds_read_b64_tr_b16 a[86:87], v118 offset:41984\n\t"
      "v_mfma_f32_16x16x32_bf16 a[232:235], v[204:207], v[204:207], a[232:235]\n\t"
      "s_waitcnt vmcnt(22)\n\t"
      "s_waitcnt vmcnt(18)\n\t"
      "s_waitcnt lgkmcnt(0)\n\t"
      "s_barrier\n\t"
      "v_mfma_f32_16x16x32_bf16 v[16:19], v[64:67], v[32:35], 0\n\t"
      "ds_read_b128 v[0:3], v104 offset:49152\n\t"
      "v_mfma_f32_16x16x32_bf16 v[20:23], v[64:67], v[36:39], 0\n\t"
      "s_mov_b64 s[88:89], s[92:93]\n\t"
      "s_add_u32 m0, s55, 65536\n\t"
      "s_xor_b32 s61, s47, 3145728\n\t"
      "buffer_load_dwordx4 v105, s[88:91], s61 offen lds\n\t"
      "ds_read_b64_tr_b16 a[24:25], v117 offset:49152\n\t"
      "v_mfma_f32_16x16x32_bf16 v[16:19], v[68:71], v[40:43], v[16:19]\n\t"
      "ds_read_b128 v[4:7], v119 offset:49152\n\t"
      "v_cvt_pk_bf16_f32 v146, v24, v25\n\t"
      "v_mfma_f32_16x16x32_bf16 v[20:23], v[68:71], v[44:47], v[20:23]\n\t"
      "s_add_u32 m0, s55, 66560\n\t"
      "s_xor_b32 s61, s47, 3178496\n\t"
      "buffer_load_dwordx4 v105, s[88:91], s61 offen lds\n\t"
      "ds_read_b64_tr_b16 a[26:27], v117 offset:50176\n\t"
      "v_cvt_pk_bf16_f32 v147, v26, v27\n\t"
      "v_mfma_f32_16x16x32_bf16 v[16:19], v[72:75], v[48:51], v[16:19]\n\t"
      "ds_read_b128 v[8:11], v102 offset:49152\n\t"
      "v_cvt_pk_bf16_f32 v210, v28, v29\n\t"
      "v_mfma_f32_16x16x32_bf16 v[20:23], v[72:75], v[52:55], v[20:23]\n\t"
      "s_mov_b64 s[88:89], s[96:97]\n\t"
      "s_add_u32 m0, s55, 131072\n\t"
      "s_xor_b32 s61, s47, 3145728\n\t"
      "buffer_load_dwordx4 v116, s[88:91], s61 offen lds\n\t"
      "ds_read_b64_tr_b16 a[88:89], v118 offset:49152\n\t"
      "v_cvt_pk_bf16_f32 v211, v30, v31\n\t"
      "v_mfma_f32_16x16x32_bf16 v[16:19], v[76:79], v[56:59], v[16:19]\n\t"
      "ds_read_b128 v[12:15], v103 offset:49152\n\t"
      "v_mfma_f32_16x16x32_bf16 v[20:23], v[76:79], v[60:63], v[20:23]\n\t"
      "s_add_u32 m0, s55, 132096\n\t"
      "s_xor_b32 s61, s47, 3178496\n\t"
      "buffer_load_dwordx4 v116, s[88:91], s61 offen lds\n\t"
      "ds_read_b64_tr_b16 a[90:91], v118 offset:50176\n\t"
      "v_mfma_f32_16x16x32_bf16 a[168:171], v[144:147], v[144:147], a[168:171]\n\t"
      "v_mfma_f32_16x16x32_bf16 v[24:27], v[80:83], v[32:35], 0\n\t"
      "ds_read_b128 v[64:67], v104 offset:50176\n\t"
      "v_mfma_f32_16x16x32_bf16 v[28:31], v[80:83], v[36:39], 0\n\t"
      "s_add_u32 m0, s55, 139264\n\t"
      "s_xor_b32 s61, s47, 3407872\n\t"
      "buffer_load_dwordx4 v116, s[88:91], s61 offen lds\n\t"
      "ds_read_b64_tr_b16 a[28:29], v117 offset:57344\n\t"
      "v_mfma_f32_16x16x32_bf16 v[24:27], v[84:87], v[40:43], v[24:27]\n\t"
      "ds_read_b128 v[68:71], v119 offset:50176\n\t"
      "v_cvt_pk_bf16_f32 v148, v16, v17\n\t"
      "v_mfma_f32_16x16x32_bf16 v[28:31], v[84:87], v[44:47], v[28:31]\n\t"
      "s_add_u32 m0, s55, 140288\n\t"
      "s_xor_b32 s61, s47, 3440640\n\t"
      "buffer_load_dwordx4 v116, s[88:91], s61 offen lds\n\t"
      "ds_read_b64_tr_b16 a[30:31], v117 offset:58368\n\t"
      "v_cvt_pk_bf16_f32 v149, v18, v19\n\t"
      "v_mfma_f32_16x16x32_bf16 v[24:27], v[88:91], v[48:51], v[24:27]\n\t"
      "ds_read_b128 v[72:75], v102 offset:50176\n\t"
      "v_cvt_pk_bf16_f32 v212, v20, v21\n\t"
      "v_mfma_f32_16x16x32_bf16 v[28:31], v[88:91], v[52:55], v[28:31]\n\t"
      "ds_read_b64_tr_b16 a[92:93], v118 offset:57344\n\t"
      "ds_read_b64_tr_b16 a[94:95], v118 offset:58368\n\t"
      "v_cvt_pk_bf16_f32 v213, v22, v23\n\t"
      "v_mfma_f32_16x16x32_bf16 v[24:27], v[92:95], v[56:59], v[24:27]\n\t"
      "s_waitcnt lgkmcnt(12)\n\t"
      "ds_read_b128 v[76:79], v103 offset:50176\n\t"
      "v_mfma_f32_16x16x32_bf16 v[28:31], v[92:95], v[60:63], v[28:31]\n\t"
      "v_mfma_f32_16x16x32_bf16 a[232:235], v[208:211], v[208:211], a[232:235]\n\t"
      "s_waitcnt vmcnt(22)\n\t"
      "s_waitcnt vmcnt(12)\n\t"
      "s_waitcnt lgkmcnt(1)\n\t"
      "s_barrier\n\t"
      "v_mfma_f32_16x16x32_bf16 v[16:19], v[0:3], v[32:35], 0\n\t"
      "ds_read_b128 v[80:83], v104 offset:57344\n\t"
      "v_mfma_f32_16x16x32_bf16 v[20:23], v[0:3], v[36:39], 0\n\t"
      "s_mov_b64 s[88:89], s[92:93]\n\t"
      "s_add_u32 m0, s55, 73728\n\t"
      "s_xor_b32 s61, s47, 3407872\n\t"
      "buffer_load_dwordx4 v105, s[88:91], s61 offen lds\n\t"
      "ds_read_b64_tr_b16 a[188:189], v106 offset:49152\n\t"
      "v_mfma_f32_16x16x32_bf16 v[16:19], v[4:7], v[40:43], v[16:19]\n\t"
      "ds_read_b128 v[84:87], v119 offset:57344\n\t"
      "v_cvt_pk_bf16_f32 v150, v24, v25\n\t"
      "v_mfma_f32_16x16x32_bf16 v[20:23], v[4:7], v[44:47], v[20:23]\n\t"
      "s_add_u32 m0, s55, 74752\n\t"
      "s_xor_b32 s61, s47, 3440640\n\t"
      "buffer_load_dwordx4 v105, s[88:91], s61 offen lds\n\t"
      "ds_read_b64_tr_b16 a[190:191], v106 offset:50176\n\t"
      "v_cvt_pk_bf16_f32 v151, v26, v27\n\t"
      "v_mfma_f32_16x16x32_bf16 v[16:19], v[8:11], v[48:51], v[16:19]\n\t"
      "ds_read_b128 v[88:91], v102 offset:57344\n\t"
      "v_cvt_pk_bf16_f32 v214, v28, v29\n\t"
      "v_mfma_f32_16x16x32_bf16 v[20:23], v[8:11], v[52:55], v[20:23]\n\t"
      "s_mov_b64 s[88:89], s[96:97]\n\t"
      "s_add_u32 m0, s55, 147456\n\t"
      "s_xor_b32 s61, s47, 3670016\n\t"
      "buffer_load_dwordx4 v116, s[88:91], s61 offen lds\n\t"
      "ds_read_b64_tr_b16 a[32:33], v117\n\t"
      "v_cvt_pk_bf16_f32 v215, v30, v31\n\t"
      "v_mfma_f32_16x16x32_bf16 v[16:19], v[12:15], v[56:59], v[16:19]\n\t"
      "ds_read_b128 v[92:95], v103 offset:57344\n\t"
      "v_mfma_f32_16x16x32_bf16 v[20:23], v[12:15], v[60:63], v[20:23]\n\t"
      "s_add_u32 m0, s55, 148480\n\t"
      "s_xor_b32 s61, s47, 3702784\n\t"
      "buffer_load_dwordx4 v116, s[88:91], s61 offen lds\n\t"
      "ds_read_b64_tr_b16 a[34:35], v117 offset:1024\n\t"
      "v_mfma_f32_16x16x32_bf16 a[168:171], v[148:151], v[148:151], a[168:171]\n\t"
      "v_readfirstlane_b32 s53, v126\n\t"
      "s_nop 0\n\t"
      "s_mul_i32 s53, s53, 0x9e3779b1\n\t"
      "s_add_u32 s53, s53, 0x7f4a7c15\n\t"
      "v_mov_b32 v96, 0x20000000\n\t"
      "v_mov_b32 v97, 0x20000000\n\t"
      "v_mov_b32 v98, 0x20000000\n\t"
      "v_mov_b32 v99, 0x20000000\n\t"
      "s_mov_b32 s61, 8192\n\t"
      "buffer_store_dwordx4 v[96:99], v101, s[36:39], 0 offen sc1\n\t"
      "buffer_store_dwordx4 v[96:99], v101, s[36:39], s61 offen sc1\n\t"
      "s_nop 1\n\t"
      "v_mov_b32 v96, v111\n\t"
      "v_mov_b32 v97, v111\n\t"
      "v_mov_b32 v98, v111\n\t"
      "v_mov_b32 v99, v111\n\t"
      "buffer_store_dwordx4 v[96:99], v108, s[36:39], 0 offen sc1\n\t"
      "v_mfma_f32_16x16x32_bf16 v[24:27], v[64:67], v[32:35], 0\n\t"
      "ds_read_b128 v[0:3], v104 offset:58368\n\t"
      "v_mfma_f32_16x16x32_bf16 v[28:31], v[64:67], v[36:39], 0\n\t"
      "s_add_u32 m0, s55, 155648\n\t"
      "s_xor_b32 s61, s47, 3932160\n\t"
      "buffer_load_dwordx4 v116, s[88:91], s61 offen lds\n\t"
      "ds_read_b64_tr_b16 a[96:97], v118\n\t"
      "v_mfma_f32_16x16x32_bf16 v[24:27], v[68:71], v[40:43], v[24:27]\n\t"
      "ds_read_b128 v[4:7], v119 offset:58368\n\t"
      "v_cvt_pk_bf16_f32 v152, v16, v17\n\t"
      "v_mfma_f32_16x16x32_bf16 v[28:31], v[68:71], v[44:47], v[28:31]\n\t"
      "s_add_u32 m0, s55, 156672\n\t"
      "s_xor_b32 s61, s47, 3964928\n\t"
      "buffer_load_dwordx4 v116, s[88:91], s61 offen lds\n\t"
      "ds_read_b64_tr_b16 a[98:99], v118 offset:1024\n\t"
      "v_cvt_pk_bf16_f32 v153, v18, v19\n\t"
      "v_mfma_f32_16x16x32_bf16 v[24:27], v[72:75], v[48:51], v[24:27]\n\t"
      "ds_read_b128 v[8:11], v102 offset:58368\n\t"
      "v_cvt_pk_bf16_f32 v216, v20, v21\n\t"
      "v_mfma_f32_16x16x32_bf16 v[28:31], v[72:75], v[52:55], v[28:31]\n\t"
      "ds_read_b64_tr_b16 a[36:37], v117 offset:8192\n\t"
      "s_waitcnt lgkmcnt(12)\n\t"
      "ds_read_b64_tr_b16 a[38:39], v117 offset:9216\n\t"
      "v_cvt_pk_bf16_f32 v217, v22, v23\n\t"
      "v_mfma_f32_16x16x32_bf16 v[24:27], v[76:79], v[56:59], v[24:27]\n\t"
      "ds_read_b128 v[12:15], v103 offset:58368\n\t"
      "v_mfma_f32_16x16x32_bf16 v[28:31], v[76:79], v[60:63], v[28:31]\n\t"
      "ds_read_b64_tr_b16 a[100:101], v118 offset:8192\n\t"
      "s_waitcnt lgkmcnt(12)\n\t"
      "ds_read_b64_tr_b16 a[102:103], v118 offset:9216\n\t"
      "v_mfma_f32_16x16x32_bf16 a[232:235], v[212:215], v[212:215], a[232:235]\n\t"
      "s_waitcnt vmcnt(15)\n\t"
      "s_waitcnt lgkmcnt(0)\n\t"
      "s_barrier\n\t"
      "v_mfma_f32_16x16x32_bf16 v[16:19], v[80:83], v[32:35], 0\n\t"
      "ds_read_b128 v[64:67], v104\n\t"
      "v_mfma_f32_16x16x32_bf16 v[20:23], v[80:83], v[36:39], 0\n\t"
      "s_mov_b64 s[88:89], s[92:93]\n\t"
      "s_add_u32 m0, s55, 81920\n\t"
      "s_xor_b32 s61, s47, 3670016\n\t"
      "buffer_load_dwordx4 v105, s[88:91], s61 offen lds\n\t"
      "ds_read_b64_tr_b16 a[40:41], v117 offset:16384\n\t"
      "v_mfma_f32_16x16x32_bf16 v[16:19], v[84:87], v[40:43], v[16:19]\n\t"
      "ds_read_b128 v[68:71], v119\n\t"
      "v_cvt_pk_bf16_f32 v154, v24, v25\n\t"
      "v_mfma_f32_16x16x32_bf16 v[20:23], v[84:87], v[44:47], v[20:23]\n\t"
      "s_add_u32 m0, s55, 82944\n\t"
      "s_xor_b32 s61, s47, 3702784\n\t"
      "buffer_load_dwordx4 v105, s[88:91], s61 offen lds\n\t"
      "ds_read_b64_tr_b16 a[42:43], v117 offset:17408\n\t"
      "v_cvt_pk_bf16_f32 v155, v26, v27\n\t"
      "v_mfma_f32_16x16x32_bf16 v[16:19], v[88:91], v[48:51], v[16:19]\n\t"
      "ds_read_b128 v[72:75], v102\n\t"
      "v_cvt_pk_bf16_f32 v218, v28, v29\n\t"
      "v_mfma_f32_16x16x32_bf16 v[20:23], v[88:91], v[52:55], v[20:23]\n\t"
      "s_mov_b64 s[88:89], s[98:99]\n\t"
      "s_add_u32 m0, s55, 98304\n\t"
      "s_xor_b32 s61, s47, 0\n\t"
      "buffer_load_dwordx4 v116, s[88:91], s61 offen lds\n\t"
      "ds_read_b64_tr_b16 a[104:105], v118 offset:16384\n\t"
      "v_cvt_pk_bf16_f32 v219, v30, v31\n\t"
      "v_mfma_f32_16x16x32_bf16 v[16:19], v[92:95], v[56:59], v[16:19]\n\t"
      "ds_read_b128 v[76:79], v103\n\t"
      "v_mfma_f32_16x16x32_bf16 v[20:23], v[92:95], v[60:63], v[20:23]\n\t"
      "s_add_u32 m0, s55, 99328\n\t"
      "s_xor_b32 s61, s47, 32768\n\t"
      "buffer_load_dwordx4 v116, s[88:91], s61 offen lds\n\t"
      "ds_read_b64_tr_b16 a[106:107], v118 offset:17408\n\t"
      "v_mfma_f32_16x16x32_bf16 a[168:171], v[152:155], v[152:155], a[168:171]\n\t"
      "v_mfma_f32_16x16x32_bf16 v[24:27], v[0:3], v[32:35], 0\n\t"
      "ds_read_b128 v[80:83], v104 offset:1024\n\t"
      "v_mfma_f32_16x16x32_bf16 v[28:31], v[0:3], v[36:39], 0\n\t"
      "s_add_u32 m0, s55, 106496\n\t"
      "s_xor_b32 s61, s47, 262144\n\t"
      "buffer_load_dwordx4 v116, s[88:91], s61 offen lds\n\t"
      "ds_read_b64_tr_b16 a[44:45], v117 offset:24576\n\t"
      "v_mfma_f32_16x16x32_bf16 v[24:27], v[4:7], v[40:43], v[24:27]\n\t"
      "ds_read_b128 v[84:87], v119 offset:1024\n\t"
      "v_cvt_pk_bf16_f32 v156, v16, v17\n\t"
      "v_mfma_f32_16x16x32_bf16 v[28:31], v[4:7], v[44:47], v[28:31]\n\t"
      "s_add_u32 m0, s55, 107520\n\t"
      "s_xor_b32 s61, s47, 294912\n\t"
      "buffer_load_dwordx4 v116, s[88:91], s61 offen lds\n\t"
      "ds_read_b64_tr_b16 a[46:47], v117 offset:25600\n\t"
      "v_cvt_pk_bf16_f32 v157, v18, v19\n\t"
      "v_mfma_f32_16x16x32_bf16 v[24:27], v[8:11], v[48:51], v[24:27]\n\t"
      "ds_read_b128 v[88:91], v102 offset:1024\n\t"
      "v_cvt_pk_bf16_f32 v220, v20, v21\n\t"
      "v_mfma_f32_16x16x32_bf16 v[28:31], v[8:11], v[52:55], v[28:31]\n\t"
      "ds_read_b64_tr_b16 a[108:109], v118 offset:24576\n\t"
      "ds_read_b64_tr_b16 a[110:111], v118 offset:25600\n\t"
      "v_cvt_pk_bf16_f32 v221, v22, v23\n\t"
      "v_mfma_f32_16x16x32_bf16 v[24:27], v[12:15], v[56:59], v[24:27]\n\t"
      "s_waitcnt lgkmcnt(12)\n\t"
      "ds_read_b128 v[92:95], v103 offset:1024\n\t"
      "v_mfma_f32_16x16x32_bf16 v[28:31], v[12:15], v[60:63], v[28:31]\n\t"
      "v_mfma_f32_16x16x32_bf16 a[232:235], v[216:219], v[216:219], a[232:235]\n\t"
      "s_waitcnt vmcnt(15)\n\t"
      "s_waitcnt lgkmcnt(1)\n\t"
      "s_barrier\n\t"
      "v_mfma_f32_16x16x32_bf16 v[16:19], v[64:67], v[32:35], 0\n\t"
      "ds_read_b128 v[0:3], v104 offset:8192\n\t"
      "v_mfma_f32_16x16x32_bf16 v[20:23], v[64:67], v[36:39], 0\n\t"
      "s_mov_b64 s[88:89], s[92:93]\n\t"
      "s_add_u32 m0, s55, 90112\n\t"
      "s_xor_b32 s61, s47, 3932160\n\t"
      "buffer_load_dwordx4 v105, s[88:91], s61 offen lds\n\t"
      "ds_read_b64_tr_b16 a[240:241], v106\n\t"
      "v_mfma_f32_16x16x32_bf16 v[16:19], v[68:71], v[40:43], v[16:19]\n\t"
      "ds_read_b128 v[4:7], v119 offset:8192\n\t"
      "v_cvt_pk_bf16_f32 v158, v24, v25\n\t"
      "v_mfma_f32_16x16x32_bf16 v[20:23], v[68:71], v[44:47], v[20:23]\n\t"
      "s_add_u32 m0, s55, 91136\n\t"
      "s_xor_b32 s61, s47, 3964928\n\t"
      "buffer_load_dwordx4 v105, s[88:91], s61 offen lds\n\t"
      "ds_read_b64_tr_b16 a[242:243], v106 offset:1024\n\t"
      "v_cvt_pk_bf16_f32 v159, v26, v27\n\t"
      "v_mfma_f32_16x16x32_bf16 v[16:19], v[72:75], v[48:51], v[16:19]\n\t"
      "ds_read_b128 v[8:11], v102 offset:8192\n\t"
      "v_cvt_pk_bf16_f32 v222, v28, v29\n\t"
      "v_mfma_f32_16x16x32_bf16 v[20:23], v[72:75], v[52:55], v[20:23]\n\t"
      "s_mov_b64 s[88:89], s[98:99]\n\t"
      "s_add_u32 m0, s55, 114688\n\t"
      "s_xor_b32 s61, s47, 524288\n\t"
      "buffer_load_dwordx4 v116, s[88:91], s61 offen lds\n\t"
      "ds_read_b64_tr_b16 a[48:49], v117 offset:32768\n\t"
      "v_cvt_pk_bf16_f32 v223, v30, v31\n\t"
      "v_mfma_f32_16x16x32_bf16 v[16:19], v[76:79], v[56:59], v[16:19]\n\t"
      "ds_read_b128 v[12:15], v103 offset:8192\n\t"
      "v_mfma_f32_16x16x32_bf16 v[20:23], v[76:79], v[60:63], v[20:23]\n\t"
      "s_add_u32 m0, s55, 115712\n\t"
      "s_xor_b32 s61, s47, 557056\n\t"
      "buffer_load_dwordx4 v116, s[88:91], s61 offen lds\n\t"
      "ds_read_b64_tr_b16 a[50:51], v117 offset:33792\n\t"
      "v_mfma_f32_16x16x32_bf16 a[168:171], v[156:159], v[156:159], a[168:171]\n\t"
      "v_mfma_f32_16x16x32_bf16 v[24:27], v[80:83], v[32:35], 0\n\t"
      "ds_read_b128 v[64:67], v104 offset:9216\n\t"
      "v_mfma_f32_16x16x32_bf16 v[28:31], v[80:83], v[36:39], 0\n\t"
      "s_add_u32 m0, s55, 122880\n\t"
      "s_xor_b32 s61, s47, 786432\n\t"
      "buffer_load_dwordx4 v116, s[88:91], s61 offen lds\n\t"
      "ds_read_b64_tr_b16 a[112:113], v118 offset:32768\n\t"
      "v_mfma_f32_16x16x32_bf16 v[24:27], v[84:87], v[40:43], v[24:27]\n\t"
      "ds_read_b128 v[68:71], v119 offset:9216\n\t"
      "v_cvt_pk_bf16_f32 v160, v16, v17\n\t"
      "v_mfma_f32_16x16x32_bf16 v[28:31], v[84:87], v[44:47], v[28:31]\n\t"
      "s_add_u32 m0, s55, 123904\n\t"
      "s_xor_b32 s61, s47, 819200\n\t"
      "buffer_load_dwordx4 v116, s[88:91], s61 offen lds\n\t"
      "ds_read_b64_tr_b16 a[114:115], v118 offset:33792\n\t"
      "v_cvt_pk_bf16_f32 v161, v18, v19\n\t"
      "v_mfma_f32_16x16x32_bf16 v[24:27], v[88:91], v[48:51], v[24:27]\n\t"
      "ds_read_b128 v[72:75], v102 offset:9216\n\t"
      "v_cvt_pk_bf16_f32 v224, v20, v21\n\t"
      "v_mfma_f32_16x16x32_bf16 v[28:31], v[88:91], v[52:55], v[28:31]\n\t"
      "ds_read_b64_tr_b16 a[52:53], v117 offset:40960\n\t"
      "s_waitcnt lgkmcnt(12)\n\t"
      "ds_read_b64_tr_b16 a[54:55], v117 offset:41984\n\t"
      "v_cvt_pk_bf16_f32 v225, v22, v23\n\t"
      "v_mfma_f32_16x16x32_bf16 v[24:27], v[92:95], v[56:59], v[24:27]\n\t"
      "ds_read_b128 v[76:79], v103 offset:9216\n\t"
      "v_mfma_f32_16x16x32_bf16 v[28:31], v[92:95], v[60:63], v[28:31]\n\t"
      "ds_read_b64_tr_b16 a[116:117], v118 offset:40960\n\t"
      "s_waitcnt lgkmcnt(12)\n\t"
      "ds_read_b64_tr_b16 a[118:119], v118 offset:41984\n\t"
      "v_mfma_f32_16x16x32_bf16 a[232:235], v[220:223], v[220:223], a[232:235]\n\t"
      "s_waitcnt vmcnt(12)\n\t"
      "s_waitcnt lgkmcnt(0)\n\t"
      "s_barrier\n\t"
      "v_mfma_f32_16x16x32_bf16 v[16:19], v[0:3], v[32:35], 0\n\t"
      "ds_read_b128 v[80:83], v104 offset:16384\n\t"
      "v_mfma_f32_16x16x32_bf16 v[20:23], v[0:3], v[36:39], 0\n\t"
      "s_add_u32 m0, s55, 131072\n\t"
      "s_xor_b32 s61, s47, 1048576\n\t"
      "buffer_load_dwordx4 v116, s[88:91], s61 offen lds\n\t"
      "ds_read_b64_tr_b16 a[56:57], v117 offset:49152\n\t"
      "v_mfma_f32_16x16x32_bf16 v[16:19], v[4:7], v[40:43], v[16:19]\n\t"
      "ds_read_b128 v[84:87], v119 offset:16384\n\t"
      "v_cvt_pk_bf16_f32 v162, v24, v25\n\t"
      "v_mfma_f32_16x16x32_bf16 v[20:23], v[4:7], v[44:47], v[20:23]\n\t"
      "s_add_u32 m0, s55, 132096\n\t"
      "s_xor_b32 s61, s47, 1081344\n\t"
      "buffer_load_dwordx4 v116, s[88:91], s61 offen lds\n\t"
      "ds_read_b64_tr_b16 a[58:59], v117 offset:50176\n\t"
      "v_cvt_pk_bf16_f32 v163, v26, v27\n\t"
      "v_mfma_f32_16x16x32_bf16 v[16:19], v[8:11], v[48:51], v[16:19]\n\t"
      "ds_read_b128 v[88:91], v102 offset:16384\n\t"
      "v_cvt_pk_bf16_f32 v226, v28, v29\n\t"
      "v_mfma_f32_16x16x32_bf16 v[20:23], v[8:11], v[52:55], v[20:23]\n\t"
      "s_add_u32 m0, s55, 139264\n\t"
      "s_xor_b32 s61, s47, 1310720\n\t"
      "buffer_load_dwordx4 v116, s[88:91], s61 offen lds\n\t"
      "ds_read_b64_tr_b16 a[120:121], v118 offset:49152\n\t"
      "v_cvt_pk_bf16_f32 v227, v30, v31\n\t"
      "v_mfma_f32_16x16x32_bf16 v[16:19], v[12:15], v[56:59], v[16:19]\n\t"
      "ds_read_b128 v[92:95], v103 offset:16384\n\t"
      "v_mfma_f32_16x16x32_bf16 v[20:23], v[12:15], v[60:63], v[20:23]\n\t"
      "s_add_u32 m0, s55, 140288\n\t"
      "s_xor_b32 s61, s47, 1343488\n\t"
      "buffer_load_dwordx4 v116, s[88:91], s61 offen lds\n\t"
      "ds_read_b64_tr_b16 a[122:123], v118 offset:50176\n\t"
      "v_mfma_f32_16x16x32_bf16 a[168:171], v[160:163], v[160:163], a[168:171]\n\t"
      "v_mfma_f32_16x16x32_bf16 v[24:27], v[64:67], v[32:35], 0\n\t"
      "ds_read_b128 v[0:3], v104 offset:17408\n\t"
      "v_mfma_f32_16x16x32_bf16 v[28:31], v[64:67], v[36:39], 0\n\t"
      "ds_read_b64_tr_b16 a[60:61], v117 offset:57344\n\t"
      "ds_read_b64_tr_b16 a[62:63], v117 offset:58368\n\t"
      "v_mfma_f32_16x16x32_bf16 v[24:27], v[68:71], v[40:43], v[24:27]\n\t"
      "ds_read_b128 v[4:7], v119 offset:17408\n\t"
      "v_cvt_pk_bf16_f32 v164, v16, v17\n\t"
      "v_mfma_f32_16x16x32_bf16 v[28:31], v[68:71], v[44:47], v[28:31]\n\t"
      "ds_read_b64_tr_b16 a[124:125], v118 offset:57344\n\t"
      "ds_read_b64_tr_b16 a[126:127], v118 offset:58368\n\t"
      "v_cvt_pk_bf16_f32 v165, v18, v19\n\t"
      "v_mfma_f32_16x16x32_bf16 v[24:27], v[72:75], v[48:51], v[24:27]\n\t"
      "ds_read_b128 v[8:11], v102 offset:17408\n\t"
      "v_cvt_pk_bf16_f32 v228, v20, v21\n\t"
      "v_mfma_f32_16x16x32_bf16 v[28:31], v[72:75], v[52:55], v[28:31]\n\t"
      "v_cvt_pk_bf16_f32 v229, v22, v23\n\t"
      "v_mfma_f32_16x16x32_bf16 v[24:27], v[76:79], v[56:59], v[24:27]\n\t"
      "s_waitcnt lgkmcnt(12)\n\t"
      "ds_read_b128 v[12:15], v103 offset:17408\n\t"
      "v_mfma_f32_16x16x32_bf16 v[28:31], v[76:79], v[60:63], v[28:31]\n\t"
      "v_mfma_f32_16x16x32_bf16 a[232:235], v[224:227], v[224:227], a[232:235]\n\t"
      "s_waitcnt vmcnt(10)\n\t"
      "s_waitcnt lgkmcnt(2)\n\t"
      "s_barrier\n\t"
      "v_mfma_f32_16x16x32_bf16 v[16:19], v[80:83], v[32:35], 0\n\t"
      "ds_read_b128 v[64:67], v104 offset:24576\n\t"
      "v_mfma_f32_16x16x32_bf16 v[20:23], v[80:83], v[36:39], 0\n\t"
      "s_add_u32 m0, s55, 147456\n\t"
      "s_xor_b32 s61, s47, 1572864\n\t"
      "buffer_load_dwordx4 v116, s[88:91], s61 offen lds\n\t"
      "ds_read_b64_tr_b16 a[244:245], v106 offset:16384\n\t"
      "v_mfma_f32_16x16x32_bf16 v[16:19], v[84:87], v[40:43], v[16:19]\n\t"
      "ds_read_b128 v[68:71], v119 offset:24576\n\t"
      "v_cvt_pk_bf16_f32 v166, v24, v25\n\t"
      "v_mfma_f32_16x16x32_bf16 v[20:23], v[84:87], v[44:47], v[20:23]\n\t"
      "s_add_u32 m0, s55, 148480\n\t"
      "s_xor_b32 s61, s47, 1605632\n\t"
      "buffer_load_dwordx4 v116, s[88:91], s61 offen lds\n\t"
      "ds_read_b64_tr_b16 a[246:247], v106 offset:17408\n\t"
      "v_cvt_pk_bf16_f32 v167, v26, v27\n\t"
      "v_mfma_f32_16x16x32_bf16 v[16:19], v[88:91], v[48:51], v[16:19]\n\t"
      "ds_read_b128 v[72:75], v102 offset:24576\n\t"
      "v_cvt_pk_bf16_f32 v230, v28, v29\n\t"
      "v_mfma_f32_16x16x32_bf16 v[20:23], v[88:91], v[52:55], v[20:23]\n\t"
      "s_add_u32 m0, s55, 155648\n\t"
      "s_xor_b32 s61, s47, 1835008\n\t"
      "buffer_load_dwordx4 v116, s[88:91], s61 offen lds\n\t"
      "ds_read_b64_tr_b16 a[128:129], v117\n\t"
      "v_cvt_pk_bf16_f32 v231, v30, v31\n\t"
      "v_mfma_f32_16x16x32_bf16 v[16:19], v[92:95], v[56:59], v[16:19]\n\t"
      "ds_read_b128 v[76:79], v103 offset:24576\n\t"
      "v_mfma_f32_16x16x32_bf16 v[20:23], v[92:95], v[60:63], v[20:23]\n\t"
      "s_add_u32 m0, s55, 156672\n\t"
      "s_xor_b32 s61, s47, 1867776\n\t"
      "buffer_load_dwordx4 v116, s[88:91], s61 offen lds\n\t"
      "ds_read_b64_tr_b16 a[130:131], v117 offset:1024\n\t"
      "v_mfma_f32_16x16x32_bf16 a[168:171], v[164:167], v[164:167], a[168:171]\n\t"
      "v_mfma_f32_16x16x32_bf16 v[24:27], v[0:3], v[32:35], 0\n\t"
      "ds_read_b128 v[80:83], v104 offset:25600\n\t"
      "v_mfma_f32_16x16x32_bf16 v[28:31], v[0:3], v[36:39], 0\n\t"
      "ds_read_b64_tr_b16 a[192:193], v118\n\t"
      "ds_read_b64_tr_b16 a[194:195], v118 offset:1024\n\t"
      "v_mfma_f32_16x16x32_bf16 v[24:27], v[4:7], v[40:43], v[24:27]\n\t"
      "ds_read_b128 v[84:87], v119 offset:25600\n\t"
      "v_cvt_pk_bf16_f32 v168, v16, v17\n\t"
      "v_mfma_f32_16x16x32_bf16 v[28:31], v[4:7], v[44:47], v[28:31]\n\t"
      "ds_read_b64_tr_b16 a[132:133], v117 offset:8192\n\t"
      "s_waitcnt lgkmcnt(12)\n\t"
      "ds_read_b64_tr_b16 a[134:135], v117 offset:9216\n\t"
      "v_cvt_pk_bf16_f32 v169, v18, v19\n\t"
      "v_mfma_f32_16x16x32_bf16 v[24:27], v[8:11], v[48:51], v[24:27]\n\t"
      "ds_read_b128 v[88:91], v102 offset:25600\n\t"
      "v_cvt_pk_bf16_f32 v232, v20, v21\n\t"
      "v_mfma_f32_16x16x32_bf16 v[28:31], v[8:11], v[52:55], v[28:31]\n\t"
      "ds_read_b64_tr_b16 a[196:197], v118 offset:8192\n\t"
      "s_waitcnt lgkmcnt(12)\n\t"
      "ds_read_b64_tr_b16 a[198:199], v118 offset:9216\n\t"
      "v_cvt_pk_bf16_f32 v233, v22, v23\n\t"
      "v_mfma_f32_16x16x32_bf16 v[24:27], v[12:15], v[56:59], v[24:27]\n\t"
      "ds_read_b128 v[92:95], v103 offset:25600\n\t"
      "v_mfma_f32_16x16x32_bf16 v[28:31], v[12:15], v[60:63], v[28:31]\n\t"
      "v_mfma_f32_16x16x32_bf16 a[232:235], v[228:231], v[228:231], a[232:235]\n\t"
      "s_waitcnt vmcnt(8)\n\t"
      "s_waitcnt lgkmcnt(1)\n\t"
      "s_barrier\n\t"
      "v_mfma_f32_16x16x32_bf16 v[16:19], v[64:67], v[32:35], 0\n\t"
      "ds_read_b128 v[0:3], v104 offset:32768\n\t"
      "v_mfma_f32_16x16x32_bf16 v[20:23], v[64:67], v[36:39], 0\n\t"
      "s_add_u32 m0, s55, 98304\n\t"
      "s_xor_b32 s61, s47, 2097152\n\t"
      "buffer_load_dwordx4 v116, s[88:91], s61 offen lds\n\t"
      "ds_read_b64_tr_b16 a[136:137], v117 offset:16384\n\t"
      "v_mfma_f32_16x16x32_bf16 v[16:19], v[68:71], v[40:43], v[16:19]\n\t"
      "ds_read_b128 v[4:7], v119 offset:32768\n\t"
      "v_cvt_pk_bf16_f32 v170, v24, v25\n\t"
      "v_mfma_f32_16x16x32_bf16 v[20:23], v[68:71], v[44:47], v[20:23]\n\t"
      "s_add_u32 m0, s55, 99328\n\t"
      "s_xor_b32 s61, s47, 2129920\n\t"
      "buffer_load_dwordx4 v116, s[88:91], s61 offen lds\n\t"
      "ds_read_b64_tr_b16 a[138:139], v117 offset:17408\n\t"
      "v_cvt_pk_bf16_f32 v171, v26, v27\n\t"
      "v_mfma_f32_16x16x32_bf16 v[16:19], v[72:75], v[48:51], v[16:19]\n\t"
      "ds_read_b128 v[8:11], v102 offset:32768\n\t"
      "v_cvt_pk_bf16_f32 v234, v28, v29\n\t"
      "v_mfma_f32_16x16x32_bf16 v[20:23], v[72:75], v[52:55], v[20:23]\n\t"
      "s_add_u32 m0, s55, 106496\n\t"
      "s_xor_b32 s61, s47, 2359296\n\t"
      "buffer_load_dwordx4 v116, s[88:91], s61 offen lds\n\t"
      "ds_read_b64_tr_b16 a[200:201], v118 offset:16384\n\t"
      "v_cvt_pk_bf16_f32 v235, v30, v31\n\t"
      "v_mfma_f32_16x16x32_bf16 v[16:19], v[76:79], v[56:59], v[16:19]\n\t"
      "ds_read_b128 v[12:15], v103 offset:32768\n\t"
      "v_mfma_f32_16x16x32_bf16 v[20:23], v[76:79], v[60:63], v[20:23]\n\t"
      "s_add_u32 m0, s55, 107520\n\t"
      "s_xor_b32 s61, s47, 2392064\n\t"
      "buffer_load_dwordx4 v116, s[88:91], s61 offen lds\n\t"
      "ds_read_b64_tr_b16 a[202:203], v118 offset:17408\n\t"
      "v_mfma_f32_16x16x32_bf16 a[168:171], v[168:171], v[168:171], a[168:171]\n\t"
      "v_mfma_f32_16x16x32_bf16 v[24:27], v[80:83], v[32:35], 0\n\t"
      "ds_read_b128 v[64:67], v104 offset:33792\n\t"
      "v_mfma_f32_16x16x32_bf16 v[28:31], v[80:83], v[36:39], 0\n\t"
      "ds_read_b64_tr_b16 a[140:141], v117 offset:24576\n\t"
      "ds_read_b64_tr_b16 a[142:143], v117 offset:25600\n\t"
      "v_mfma_f32_16x16x32_bf16 v[24:27], v[84:87], v[40:43], v[24:27]\n\t"
      "ds_read_b128 v[68:71], v119 offset:33792\n\t"
      "v_cvt_pk_bf16_f32 v172, v16, v17\n\t"
      "v_mfma_f32_16x16x32_bf16 v[28:31], v[84:87], v[44:47], v[28:31]\n\t"
      "ds_read_b64_tr_b16 a[204:205], v118 offset:24576\n\t"
      "ds_read_b64_tr_b16 a[206:207], v118 offset:25600\n\t"
      "v_cvt_pk_bf16_f32 v173, v18, v19\n\t"
      "v_mfma_f32_16x16x32_bf16 v[24:27], v[88:91], v[48:51], v[24:27]\n\t"
      "s_waitcnt lgkmcnt(12)\n\t"
      "ds_read_b128 v[72:75], v102 offset:33792\n\t"
      "v_cvt_pk_bf16_f32 v236, v20, v21\n\t"
      "v_mfma_f32_16x16x32_bf16 v[28:31], v[88:91], v[52:55], v[28:31]\n\t"
      "v_cvt_pk_bf16_f32 v237, v22, v23\n\t"
      "v_mfma_f32_16x16x32_bf16 v[24:27], v[92:95], v[56:59], v[24:27]\n\t"
      "ds_read_b128 v[76:79], v103 offset:33792\n\t"
      "v_mfma_f32_16x16x32_bf16 v[28:31], v[92:95], v[60:63], v[28:31]\n\t"
      "v_mfma_f32_16x16x32_bf16 a[232:235], v[232:235], v[232:235], a[232:235]\n\t"
      "s_waitcnt vmcnt(8)\n\t"
      "s_waitcnt lgkmcnt(2)\n\t"
      "s_barrier\n\t"
      "v_mfma_f32_16x16x32_bf16 v[16:19], v[0:3], v[32:35], 0\n\t"
      "ds_read_b128 v[80:83], v104 offset:40960\n\t"
      "v_mfma_f32_16x16x32_bf16 v[20:23], v[0:3], v[36:39], 0\n\t"
      "s_add_u32 m0, s55, 114688\n\t"
      "s_xor_b32 s61, s47, 2621440\n\t"
      "buffer_load_dwordx4 v116, s[88:91], s61 offen lds\n\t"
      "ds_read_b64_tr_b16 a[248:249], v106 offset:32768\n\t"
      "v_mfma_f32_16x16x32_bf16 v[16:19], v[4:7], v[40:43], v[16:19]\n\t"
      "ds_read_b128 v[84:87], v119 offset:40960\n\t"
      "v_cvt_pk_bf16_f32 v174, v24, v25\n\t"
      "v_mfma_f32_16x16x32_bf16 v[20:23], v[4:7], v[44:47], v[20:23]\n\t"
      "s_add_u32 m0, s55, 115712\n\t"
      "s_xor_b32 s61, s47, 2654208\n\t"
      "buffer_load_dwordx4 v116, s[88:91], s61 offen lds\n\t"
      "ds_read_b64_tr_b16 a[250:251], v106 offset:33792\n\t"
      "v_cvt_pk_bf16_f32 v175, v26, v27\n\t"
      "v_mfma_f32_16x16x32_bf16 v[16:19], v[8:11], v[48:51], v[16:19]\n\t"
      "ds_read_b128 v[88:91], v102 offset:40960\n\t"
      "v_cvt_pk_bf16_f32 v238, v28, v29\n\t"
      "v_mfma_f32_16x16x32_bf16 v[20:23], v[8:11], v[52:55], v[20:23]\n\t"
      "s_add_u32 m0, s55, 122880\n\t"
      "s_xor_b32 s61, s47, 2883584\n\t"
      "buffer_load_dwordx4 v116, s[88:91], s61 offen lds\n\t"
      "ds_read_b64_tr_b16 a[144:145], v117 offset:32768\n\t"
      "v_cvt_pk_bf16_f32 v239, v30, v31\n\t"
      "v_mfma_f32_16x16x32_bf16 v[16:19], v[12:15], v[56:59], v[16:19]\n\t"
      "ds_read_b128 v[92:95], v103 offset:40960\n\t"
      "v_mfma_f32_16x16x32_bf16 v[20:23], v[12:15], v[60:63], v[20:23]\n\t"
      "s_add_u32 m0, s55, 123904\n\t"
      "s_xor_b32 s61, s47, 2916352\n\t"
      "buffer_load_dwordx4 v116, s[88:91], s61 offen lds\n\t"
      "ds_read_b64_tr_b16 a[146:147], v117 offset:33792\n\t"
      "v_mfma_f32_16x16x32_bf16 a[168:171], v[172:175], v[172:175], a[168:171]\n\t"
      "v_mfma_f32_16x16x32_bf16 v[24:27], v[64:67], v[32:35], 0\n\t"
      "ds_read_b128 v[0:3], v104 offset:41984\n\t"
      "v_mfma_f32_16x16x32_bf16 v[28:31], v[64:67], v[36:39], 0\n\t"
      "ds_read_b64_tr_b16 a[208:209], v118 offset:32768\n\t"
      "ds_read_b64_tr_b16 a[210:211], v118 offset:33792\n\t"
      "v_mfma_f32_16x16x32_bf16 v[24:27], v[68:71], v[40:43], v[24:27]\n\t"
      "ds_read_b128 v[4:7], v119 offset:41984\n\t"
      "v_cvt_pk_bf16_f32 v176, v16, v17\n\t"
      "v_mfma_f32_16x16x32_bf16 v[28:31], v[68:71], v[44:47], v[28:31]\n\t"
      "ds_read_b64_tr_b16 a[148:149], v117 offset:40960\n\t"
      "s_waitcnt lgkmcnt(12)\n\t"
      "ds_read_b64_tr_b16 a[150:151], v117 offset:41984\n\t"
      "v_cvt_pk_bf16_f32 v177, v18, v19\n\t"
      "v_mfma_f32_16x16x32_bf16 v[24:27], v[72:75], v[48:51], v[24:27]\n\t"
      "ds_read_b128 v[8:11], v102 offset:41984\n\t"
      "v_cvt_pk_bf16_f32 v240, v20, v21\n\t"
      "v_mfma_f32_16x16x32_bf16 v[28:31], v[72:75], v[52:55], v[28:31]\n\t"
      "ds_read_b64_tr_b16 a[212:213], v118 offset:40960\n\t"
      "s_waitcnt lgkmcnt(12)\n\t"
      "ds_read_b64_tr_b16 a[214:215], v118 offset:41984\n\t"
      "v_cvt_pk_bf16_f32 v241, v22, v23\n\t"
      "v_mfma_f32_16x16x32_bf16 v[24:27], v[76:79], v[56:59], v[24:27]\n\t"
      "ds_read_b128 v[12:15], v103 offset:41984\n\t"
      "v_mfma_f32_16x16x32_bf16 v[28:31], v[76:79], v[60:63], v[28:31]\n\t"
      "v_mfma_f32_16x16x32_bf16 a[232:235], v[236:239], v[236:239], a[232:235]\n\t"
      "s_waitcnt vmcnt(8)\n\t"
      "s_waitcnt lgkmcnt(1)\n\t"
      "s_barrier\n\t"
      "v_mfma_f32_16x16x32_bf16 v[16:19], v[80:83], v[32:35], 0\n\t"
      "ds_read_b128 v[64:67], v104 offset:49152\n\t"
      "v_mfma_f32_16x16x32_bf16 v[20:23], v[80:83], v[36:39], 0\n\t"
      "s_add_u32 m0, s55, 131072\n\t"
      "s_xor_b32 s61, s47, 3145728\n\t"
      "buffer_load_dwordx4 v116, s[88:91], s61 offen lds\n\t"
      "ds_read_b64_tr_b16 a[152:153], v117 offset:49152\n\t"
      "v_mfma_f32_16x16x32_bf16 v[16:19], v[84:87], v[40:43], v[16:19]\n\t"
      "ds_read_b128 v[68:71], v119 offset:49152\n\t"
      "v_cvt_pk_bf16_f32 v178, v24, v25\n\t"
      "v_mfma_f32_16x16x32_bf16 v[20:23], v[84:87], v[44:47], v[20:23]\n\t"
      "s_add_u32 m0, s55, 132096\n\t"
      "s_xor_b32 s61, s47, 3178496\n\t"
      "buffer_load_dwordx4 v116, s[88:91], s61 offen lds\n\t"
      "ds_read_b64_tr_b16 a[154:155], v117 offset:50176\n\t"
      "v_cvt_pk_bf16_f32 v179, v26, v27\n\t"
      "v_mfma_f32_16x16x32_bf16 v[16:19], v[88:91], v[48:51], v[16:19]\n\t"
      "ds_read_b128 v[72:75], v102 offset:49152\n\t"
      "v_cvt_pk_bf16_f32 v242, v28, v29\n\t"
      "v_mfma_f32_16x16x32_bf16 v[20:23], v[88:91], v[52:55], v[20:23]\n\t"
      "s_add_u32 m0, s55, 139264\n\t"
      "s_xor_b32 s61, s47, 3407872\n\t"
      "buffer_load_dwordx4 v116, s[88:91], s61 offen lds\n\t"
      "ds_read_b64_tr_b16 a[216:217], v118 offset:49152\n\t"
      "v_cvt_pk_bf16_f32 v243, v30, v31\n\t"
      "v_mfma_f32_16x16x32_bf16 v[16:19], v[92:95], v[56:59], v[16:19]\n\t"
      "ds_read_b128 v[76:79], v103 offset:49152\n\t"
      "v_mfma_f32_16x16x32_bf16 v[20:23], v[92:95], v[60:63], v[20:23]\n\t"
      "s_add_u32 m0, s55, 140288\n\t"
      "s_xor_b32 s61, s47, 3440640\n\t"
      "buffer_load_dwordx4 v116, s[88:91], s61 offen lds\n\t"
      "ds_read_b64_tr_b16 a[218:219], v118 offset:50176\n\t"
      "v_mfma_f32_16x16x32_bf16 a[168:171], v[176:179], v[176:179], a[168:171]\n\t"
      "v_mfma_f32_16x16x32_bf16 v[24:27], v[0:3], v[32:35], 0\n\t"
      "ds_read_b128 v[80:83], v104 offset:50176\n\t"
      "v_mfma_f32_16x16x32_bf16 v[28:31], v[0:3], v[36:39], 0\n\t"
      "ds_read_b64_tr_b16 a[156:157], v117 offset:57344\n\t"
      "ds_read_b64_tr_b16 a[158:159], v117 offset:58368\n\t"
      "v_mfma_f32_16x16x32_bf16 v[24:27], v[4:7], v[40:43], v[24:27]\n\t"
      "ds_read_b128 v[84:87], v119 offset:50176\n\t"
      "v_cvt_pk_bf16_f32 v180, v16, v17\n\t"
      "v_mfma_f32_16x16x32_bf16 v[28:31], v[4:7], v[44:47], v[28:31]\n\t"
      "ds_read_b64_tr_b16 a[220:221], v118 offset:57344\n\t"
      "ds_read_b64_tr_b16 a[222:223], v118 offset:58368\n\t"
      "v_cvt_pk_bf16_f32 v181, v18, v19\n\t"
      "v_mfma_f32_16x16x32_bf16 v[24:27], v[8:11], v[48:51], v[24:27]\n\t"
      "s_waitcnt lgkmcnt(12)\n\t"
      "ds_read_b128 v[88:91], v102 offset:50176\n\t"
      "v_cvt_pk_bf16_f32 v244, v20, v21\n\t"
      "v_mfma_f32_16x16x32_bf16 v[28:31], v[8:11], v[52:55], v[28:31]\n\t"
      "v_cvt_pk_bf16_f32 v245, v22, v23\n\t"
      "v_mfma_f32_16x16x32_bf16 v[24:27], v[12:15], v[56:59], v[24:27]\n\t"
      "ds_read_b128 v[92:95], v103 offset:50176\n\t"
      "v_mfma_f32_16x16x32_bf16 v[28:31], v[12:15], v[60:63], v[28:31]\n\t"
      "v_mfma_f32_16x16x32_bf16 a[232:235], v[240:243], v[240:243], a[232:235]\n\t"
      "s_waitcnt vmcnt(8)\n\t"
      "s_waitcnt lgkmcnt(2)\n\t"
      "s_barrier\n\t"
      "v_mfma_f32_16x16x32_bf16 v[16:19], v[64:67], v[32:35], 0\n\t"
      "ds_read_b128 v[0:3], v104 offset:57344\n\t"
      "v_mfma_f32_16x16x32_bf16 v[20:23], v[64:67], v[36:39], 0\n\t"
      "s_add_u32 m0, s55, 147456\n\t"
      "s_xor_b32 s61, s47, 3670016\n\t"
      "buffer_load_dwordx4 v116, s[88:91], s61 offen lds\n\t"
      "ds_read_b64_tr_b16 a[252:253], v106 offset:49152\n\t"
      "v_mfma_f32_16x16x32_bf16 v[16:19], v[68:71], v[40:43], v[16:19]\n\t"
      "ds_read_b128 v[4:7], v119 offset:57344\n\t"
      "v_cvt_pk_bf16_f32 v182, v24, v25\n\t"
      "v_mfma_f32_16x16x32_bf16 v[20:23], v[68:71], v[44:47], v[20:23]\n\t"
      "s_add_u32 m0, s55, 148480\n\t"
      "s_xor_b32 s61, s47, 3702784\n\t"
      "buffer_load_dwordx4 v116, s[88:91], s61 offen lds\n\t"
      "ds_read_b64_tr_b16 a[254:255], v106 offset:50176\n\t"
      "v_cvt_pk_bf16_f32 v183, v26, v27\n\t"
      "v_mfma_f32_16x16x32_bf16 v[16:19], v[72:75], v[48:51], v[16:19]\n\t"
      "ds_read_b128 v[8:11], v102 offset:57344\n\t"
      "v_cvt_pk_bf16_f32 v246, v28, v29\n\t"
      "v_mfma_f32_16x16x32_bf16 v[20:23], v[72:75], v[52:55], v[20:23]\n\t"
      "s_add_u32 m0, s55, 155648\n\t"
      "s_xor_b32 s61, s47, 3932160\n\t"
      "buffer_load_dwordx4 v116, s[88:91], s61 offen lds\n\t"
      "ds_read_b64_tr_b16 a[160:161], v117\n\t"
      "v_cvt_pk_bf16_f32 v247, v30, v31\n\t"
      "v_mfma_f32_16x16x32_bf16 v[16:19], v[76:79], v[56:59], v[16:19]\n\t"
      "ds_read_b128 v[12:15], v103 offset:57344\n\t"
      "v_mfma_f32_16x16x32_bf16 v[20:23], v[76:79], v[60:63], v[20:23]\n\t"
      "s_add_u32 m0, s55, 156672\n\t"
      "s_xor_b32 s61, s47, 3964928\n\t"
      "buffer_load_dwordx4 v116, s[88:91], s61 offen lds\n\t"
      "ds_read_b64_tr_b16 a[162:163], v117 offset:1024\n\t"
      "v_mfma_f32_16x16x32_bf16 a[168:171], v[180:183], v[180:183], a[168:171]\n\t"
      "v_mfma_f32_16x16x32_bf16 v[24:27], v[80:83], v[32:35], 0\n\t"
      "ds_read_b128 v[64:67], v104 offset:58368\n\t"
      "v_mfma_f32_16x16x32_bf16 v[28:31], v[80:83], v[36:39], 0\n\t"
      "ds_read_b64_tr_b16 a[224:225], v118\n\t"
      "ds_read_b64_tr_b16 a[226:227], v118 offset:1024\n\t"
      "v_mfma_f32_16x16x32_bf16 v[24:27], v[84:87], v[40:43], v[24:27]\n\t"
      "ds_read_b128 v[68:71], v119 offset:58368\n\t"
      "v_cvt_pk_bf16_f32 v184, v16, v17\n\t"
      "v_mfma_f32_16x16x32_bf16 v[28:31], v[84:87], v[44:47], v[28:31]\n\t"
      "ds_read_b64_tr_b16 a[164:165], v117 offset:8192\n\t"
      "s_waitcnt lgkmcnt(12)\n\t"
      "ds_read_b64_tr_b16 a[166:167], v117 offset:9216\n\t"
      "v_cvt_pk_bf16_f32 v185, v18, v19\n\t"
      "v_mfma_f32_16x16x32_bf16 v[24:27], v[88:91], v[48:51], v[24:27]\n\t"
      "ds_read_b128 v[72:75], v102 offset:58368\n\t"
      "v_cvt_pk_bf16_f32 v248, v20, v21\n\t"
      "v_mfma_f32_16x16x32_bf16 v[28:31], v[88:91], v[52:55], v[28:31]\n\t"
      "ds_read_b64_tr_b16 a[228:229], v118 offset:8192\n\t"
      "s_waitcnt lgkmcnt(12)\n\t"
      "ds_read_b64_tr_b16 a[230:231], v118 offset:9216\n\t"
      "v_cvt_pk_bf16_f32 v249, v22, v23\n\t"
      "v_mfma_f32_16x16x32_bf16 v[24:27], v[92:95], v[56:59], v[24:27]\n\t"
      "ds_read_b128 v[76:79], v103 offset:58368\n\t"
      "v_mfma_f32_16x16x32_bf16 v[28:31], v[92:95], v[60:63], v[28:31]\n\t"
      "v_mfma_f32_16x16x32_bf16 a[232:235], v[244:247], v[244:247], a[232:235]\n\t"
      "v_mfma_f32_16x16x32_bf16 v[16:19], v[0:3], v[32:35], 0\n\t"
      "v_mfma_f32_16x16x32_bf16 v[20:23], v[0:3], v[36:39], 0\n\t"
      "v_mfma_f32_16x16x32_bf16 v[16:19], v[4:7], v[40:43], v[16:19]\n\t"
      "v_cvt_pk_bf16_f32 v186, v24, v25\n\t"
      "v_mfma_f32_16x16x32_bf16 v[20:23], v[4:7], v[44:47], v[20:23]\n\t"
      "v_cvt_pk_bf16_f32 v187, v26, v27\n\t"
      "s_waitcnt lgkmcnt(13)\n\t"
      "v_mfma_f32_16x16x32_bf16 v[16:19], v[8:11], v[48:51], v[16:19]\n\t"
      "v_cvt_pk_bf16_f32 v250, v28, v29\n\t"
      "v_mfma_f32_16x16x32_bf16 v[20:23], v[8:11], v[52:55], v[20:23]\n\t"
      "v_cvt_pk_bf16_f32 v251, v30, v31\n\t"
      "s_waitcnt lgkmcnt(11)\n\t"
      "v_mfma_f32_16x16x32_bf16 v[16:19], v[12:15], v[56:59], v[16:19]\n\t"
      "v_mfma_f32_16x16x32_bf16 v[20:23], v[12:15], v[60:63], v[20:23]\n\t"
      "v_mfma_f32_16x16x32_bf16 a[168:171], v[184:187], v[184:187], a[168:171]\n\t"
      "buffer_load_dwordx4 v[96:99], off, s[36:39], s54 sc1\n\t"
      "s_waitcnt lgkmcnt(9)\n\t"
      "v_mfma_f32_16x16x32_bf16 v[24:27], v[64:67], v[32:35], 0\n\t"
      "v_mfma_f32_16x16x32_bf16 v[28:31], v[64:67], v[36:39], 0\n\t"
      "s_waitcnt lgkmcnt(6)\n\t"
      "v_mfma_f32_16x16x32_bf16 v[24:27], v[68:71], v[40:43], v[24:27]\n\t"
      "v_cvt_pk_bf16_f32 v188, v16, v17\n\t"
      "v_mfma_f32_16x16x32_bf16 v[28:31], v[68:71], v[44:47], v[28:31]\n\t"
      "v_cvt_pk_bf16_f32 v189, v18, v19\n\t"
      "s_waitcnt lgkmcnt(3)\n\t"
      "v_mfma_f32_16x16x32_bf16 v[24:27], v[72:75], v[48:51], v[24:27]\n\t"
      "v_cvt_pk_bf16_f32 v252, v20, v21\n\t"
      "v_mfma_f32_16x16x32_bf16 v[28:31], v[72:75], v[52:55], v[28:31]\n\t"
      "v_cvt_pk_bf16_f32 v253, v22, v23\n\t"
      "s_waitcnt lgkmcnt(0)\n\t"
      "v_mfma_f32_16x16x32_bf16 v[24:27], v[76:79], v[56:59], v[24:27]\n\t"
      "v_mfma_f32_16x16x32_bf16 v[28:31], v[76:79], v[60:63], v[28:31]\n\t"
      "v_mfma_f32_16x16x32_bf16 a[232:235], v[248:251], v[248:251], a[232:235]\n\t"
      "v_lshl_add_u32 v8, s41, 10, v127\n\t"
      "v_add_u32 v8, 65536, v8\n\t"
      "v_mov_b32 v12, 0\n\t"
      "v_mov_b32 v9, s41\n\t"
      "v_lshlrev_b32 v9, 2, v9\n\t"
      "v_cvt_pk_bf16_f32 v190, v24, v25\n\t"
      "v_cvt_pk_bf16_f32 v191, v26, v27\n\t"
      "v_cvt_pk_bf16_f32 v254, v28, v29\n\t"
      "v_cvt_pk_bf16_f32 v255, v30, v31\n\t"
      "s_nop 1\n\t"
      "v_mfma_f32_16x16x32_bf16 a[168:171], v[188:191], v[188:191], a[168:171]\n\t"
      "v_mfma_f32_16x16x32_bf16 a[232:235], v[252:255], v[252:255], a[232:235]\n\t"
      "s_sub_u32 s64, s46, 786432\n\t"
      "s_mov_b32 s71, 98304\n\t"
      "s_mov_b32 s63, 0x10000\n\t"
      "s_nop 4\n\t"
      "v_accvgpr_read_b32 v0, a168\n\t"
      "v_accvgpr_read_b32 v1, a169\n\t"
      "v_accvgpr_read_b32 v2, a170\n\t"
      "v_accvgpr_read_b32 v3, a171\n\t"
      "v_accvgpr_read_b32 v4, a232\n\t"
      "v_accvgpr_read_b32 v5, a233\n\t"
      "v_accvgpr_read_b32 v6, a234\n\t"
      "v_accvgpr_read_b32 v7, a235\n\t"
      "v_and_b32 v0, 0x7fffffff, v0\n\t"
      "v_and_b32 v1, 0x7fffffff, v1\n\t"
      "v_and_b32 v2, 0x7fffffff, v2\n\t"
      "v_and_b32 v3, 0x7fffffff, v3\n\t"
      "v_and_b32 v4, 0x7fffffff, v4\n\t"
      "v_and_b32 v5, 0x7fffffff, v5\n\t"
      "v_and_b32 v6, 0x7fffffff, v6\n\t"
      "v_and_b32 v7, 0x7fffffff, v7\n\t"
      "v_max3_u32 v0, v0, v1, v2\n\t"
      "v_max3_u32 v3, v3, v4, v5\n\t"
      "v_max3_u32 v0, v0, v6, v7\n\t"
      "v_max_u32 v0, v0, v3\n\t"
      "s_nop 1\n\t"
      "v_max_u32_dpp v0, v0, v0 quad_perm:[1,0,3,2] row_mask:0xf bank_mask:0xf\n\t"
      "s_nop 1\n\t"
      "v_max_u32_dpp v0, v0, v0 quad_perm:[2,3,0,1] row_mask:0xf bank_mask:0xf\n\t"
      "s_nop 1\n\t"
      "v_max_u32_dpp v0, v0, v0 row_ror:4 row_mask:0xf bank_mask:0xf\n\t"
      "s_nop 1\n\t"
      "v_max_u32_dpp v0, v0, v0 row_ror:8 row_mask:0xf bank_mask:0xf\n\t"
      "v_mov_b32 v1, v0\n\t"
      "s_nop 1\n\t"
      "v_permlane16_swap_b32 v0, v1\n\t"
      "v_max_u32 v0, v0, v1\n\t"
      "v_mov_b32 v1, v0\n\t"
      "s_nop 1\n\t"
      "v_permlane32_swap_b32 v0, v1\n\t"
      "v_max_u32 v0, v0, v1\n\t"
      "ds_write_b32 v9, v0 offset:32768\n\t"
      "s_waitcnt lgkmcnt(0)\n\t"
      "s_barrier\n\t"
      "ds_read_b128 v[4:7], v12 offset:32768\n\t"
      "s_waitcnt lgkmcnt(0)\n\t"
      "v_max_u32 v4, v4, v5\n\t"
      "v_max3_u32 v4, v4, v6, v7\n\t"
      "v_mov_b32 v5, s53\n\t"
      "s_memrealtime s[92:93]\n\t"
      "v_add_u32 v4, 0x10000, v4\n\t"
      "s_mov_b64 exec, 1\n\t"
      "s_mov_b32 s61, 0xffff\n\t"
      "s_waitcnt lgkmcnt(0)\n\t"
      "v_mov_b32 v6, s92\n\t"
      "v_bfi_b32 v4, s61, v6, v4\n\t"
      "buffer_store_dwordx2 v[4:5], off, s[36:39], s95 sc1\n\t"
      "s_mov_b64 exec, -1\n\t"
      "s_waitcnt vmcnt(1)\n\t"
      "v_bitop3_b32 v96, v96, v97, s53 bitop3:0x7e\n\t"
      "v_bitop3_b32 v98, v98, v99, s53 bitop3:0x7e\n\t"
      "v_or_b32 v96, v96, v98\n\t"
      "s_add_u32 m0, s55, 98304\n\t"
      "v_readfirstlane_b32 s56, v96\n\t"
      "s_nop 1\n\t"
      "s_cmp_eq_u32 s56, 0\n\t"
      "s_cbranch_scc0 LF2%=\n\t"
      "buffer_load_dwordx4 v127, s[36:39], s64 offen sc1 lds\n\t"
      "buffer_load_dwordx4 v127, s[36:39], s64 offen offset:1024 sc1 lds\n\t"
      "LF2%=:\n\t"
      "buffer_load_dwordx4 v[96:99], off, s[36:39], s54 sc1\n\t"
      "ds_write_b128 v8, a[176:179]\n\t"
      "ds_write_b128 v8, a[180:183] offset:4096\n\t"
      "ds_write_b128 v8, a[184:187] offset:8192\n\t"
      "ds_write_b128 v8, a[188:191] offset:12288\n\t"
      "ds_write_b128 v8, a[240:243] offset:16384\n\t"
      "ds_write_b128 v8, a[244:247] offset:20480\n\t"
      "ds_write_b128 v8, a[248:251] offset:24576\n\t"
      "ds_write_b128 v8, a[252:255] offset:28672\n\t"
      "ds_write_b128 v100, v[112:115] offset:57344\n\t"
      "s_cbranch_scc1 LF3%=\n\t"
      "LF0%=:\n\t"
      "s_waitcnt vmcnt(0)\n\t"
      "v_bitop3_b32 v96, v96, v97, s53 bitop3:0x7e\n\t"
      "v_bitop3_b32 v98, v98, v99, s53 bitop3:0x7e\n\t"
      "v_or_b32 v96, v96, v98\n\t"
      "s_add_u32 m0, s55, 98304\n\t"
      "v_readfirstlane_b32 s56, v96\n\t"
      "s_nop 1\n\t"
      "s_cmp_eq_u32 s56, 0\n\t"
      "s_cbranch_scc1 LF1%=\n\t"
      "buffer_load_dwordx4 v[96:99], off, s[36:39], s54 sc1\n\t"
      "s_sub_u32 s63, s63, 1\n\t"
      "s_cmp_lg_u32 s63, 0\n\t"
      "s_cbranch_scc1 LF0%=\n\t"
      "LF1%=:\n\t"
      "buffer_load_dwordx4 v127, s[36:39], s64 offen sc1 lds\n\t"
      "buffer_load_dwordx4 v127, s[36:39], s64 offen offset:1024 sc1 lds\n\t"
      "LF3%=:\n\t"
      "v_lshlrev_b32 v52, 4, v127\n\t"
      "s_mov_b32 s62, 0xffff0000\n\t"
      "v_accvgpr_read_b32 v85, a172\n\t"
      "v_accvgpr_read_b32 v87, a173\n\t"
      "v_accvgpr_read_b32 v89, a174\n\t"
      "v_accvgpr_read_b32 v91, a175\n\t"
      "v_accvgpr_read_b32 v93, a236\n\t"
      "v_accvgpr_read_b32 v95, a237\n\t"
      "v_lshl_or_b32 v84, v85, 16, 1\n\t"
      "v_and_or_b32 v85, v85, s62, 1\n\t"
      "v_lshl_or_b32 v86, v87, 16, 1\n\t"
      "v_and_or_b32 v87, v87, s62, 1\n\t"
      "v_lshl_or_b32 v88, v89, 16, 1\n\t"
      "v_and_or_b32 v89, v89, s62, 1\n\t"
      "v_lshl_or_b32 v90, v91, 16, 1\n\t"
      "v_and_or_b32 v91, v91, s62, 1\n\t"
      "v_lshl_or_b32 v92, v93, 16, 1\n\t"
      "v_and_or_b32 v93, v93, s62, 1\n\t"
      "v_lshl_or_b32 v94, v95, 16, 1\n\t"
      "v_and_or_b32 v95, v95, s62, 1\n\t"
      "s_cmp_eq_u32 s86, 0\n\t"
      "s_cbranch_scc1 LC9%=\n\t"
      "s_waitcnt lgkmcnt(0)\n\t"
      "s_memtime s[96:97]\n\t"
      "s_waitcnt lgkmcnt(0)\n\t"
      "s_buffer_load_dword s87, s[36:39], s86\n\t"
      "s_waitcnt lgkmcnt(0)\n\t"
      "s_memtime s[98:99]\n\t"
      "s_waitcnt lgkmcnt(0)\n\t"
      "LC9%=:\n\t"
      "s_sub_u32 s61, s98, s96\n\t"
      "s_lshr_b32 s61, s61, 2\n\t"
      "s_min_u32 s61, s61, 511\n\t"
      "s_sub_u32 s62, 0, s61\n\t"
      "s_bitcmp1_b32 s86, 13\n\t"
      "s_cselect_b32 s61, s62, s61\n\t"
      "s_cmp_eq_u32 s86, 0\n\t"
      "s_cselect_b32 s65, 0, s61\n\t"
      "v_readfirstlane_b32 s97, v121\n\t"
      "v_mov_b32 v65, v115\n\t"
      "v_mov_b32 v64, 0\n\t"
      "v_mov_b32 v66, 0\n\t"
      "v_mov_b32 v67, 0\n\t"
      "v_mov_b32 v109, 0\n\t"
      "v_mov_b32 v110, 0\n\t"
      "v_mov_b32 v111, 0\n\t"
      "s_mov_b32 s40, -1\n\t"
      "s_add_u32 s45, s55, 32768\n\t"
      "v_mov_b32 v126, 0\n\t"
      "s_mov_b32 s56, 0\n\t"
      "s_mov_b32 s58, 1\n\t"
      "s_mov_b32 s59, 0x3e0293ee\n\t"
      "s_mov_b32 s96, 0x40820000\n\t"
      "s_mov_b32 s57, 1.0\n\t"
      "s_add_u32 s68, s43, 8192\n\t"
      "s_add_u32 s97, s97, s43\n\t"
      "s_add_u32 s97, s97, 0x8000\n\t"
      "s_mov_b32 s99, 0\n\t"
      "s_mov_b32 s84, 3\n\t"
      "s_mov_b32 s85, 0x80400000\n\t"
      "s_mov_b32 s26, 2460\n\t"
      "s_mov_b32 s25, 0\n\t"
      "s_memtime s[78:79]\n\t"
      "s_waitcnt lgkmcnt(0)\n\t"
      "s_sub_u32 s78, s78, 4000\n\t"
      "s_mov_b32 s88, s78\n\t"
      "s_mov_b32 s27, s78\n\t"
      "s_mov_b32 s24, s26\n\t"
      "s_mov_b32 s23, s80\n\t"
      "s_add_u32 s50, s50, 32\n\t"
      "s_mov_b32 s72, 0xcccccccc\n\t"
      "s_mov_b32 s73, 0xcccccccc\n\t"
      "s_waitcnt vmcnt(0) lgkmcnt(0)\n\t"
      "s_barrier\n\t"
      "s_mov_b32 m0, s45\n\t"
      "s_sub_u32 s61, 204800, s71\n\t"
      "buffer_load_dwordx4 v65, s[36:39], s44 offen sc1 lds\n\t"
      "buffer_load_dwordx4 v65, s[36:39], s44 offen offset:1024 sc1 lds\n\t"
      "s_add_u32 m0, s55, s61\n\t"
      "s_nop 0\n\t"
      "buffer_load_dwordx4 v127, s[36:39], s46 offen sc1 lds\n\t"
      "buffer_load_dwordx4 v127, s[36:39], s46 offen offset:1024 sc1 lds\n\t"
      "buffer_load_dwordx2 v[48:49], v52, s[36:39], s94 offen sc1\n\t"
      "buffer_load_dwordx2 v[50:51], v52, s[36:39], s94 offen offset:128 sc1\n\t"
      "s_mov_b32 s62, 0xffff0000\n\t"
      "v_accvgpr_read_b32 v97, a238\n\t"
      "v_accvgpr_read_b32 v99, a239\n\t"
      "v_lshl_or_b32 v96, v97, 16, 1\n\t"
      "v_and_or_b32 v97, v97, s62, 1\n\t"
      "v_lshl_or_b32 v98, v99, 16, 1\n\t"
      "v_and_or_b32 v99, v99, s62, 1\n\t"
      "ds_read_b64_tr_b16 a[168:169], v117 offset:16384\n\t"
      "ds_read_b64_tr_b16 a[170:171], v117 offset:17408\n\t"
      "ds_read_b64_tr_b16 a[232:233], v118 offset:16384\n\t"
      "ds_read_b64_tr_b16 a[234:235], v118 offset:17408\n\t"
      "ds_read_b64_tr_b16 a[172:173], v117 offset:24576\n\t"
      "ds_read_b64_tr_b16 a[174:175], v117 offset:25600\n\t"
      "ds_read_b64_tr_b16 a[236:237], v118 offset:24576\n\t"
      "ds_read_b64_tr_b16 a[238:239], v118 offset:25600\n\t"
      "ds_read_b64_tr_b16 a[176:177], v117 offset:32768\n\t"
      "ds_read_b64_tr_b16 a[178:179], v117 offset:33792\n\t"
      "ds_read_b64_tr_b16 a[240:241], v118 offset:32768\n\t"
      "ds_read_b64_tr_b16 a[242:243], v118 offset:33792\n\t"
      "ds_read_b64_tr_b16 a[180:181], v117 offset:40960\n\t"
      "ds_read_b64_tr_b16 a[182:183], v117 offset:41984\n\t"
      "ds_read_b64_tr_b16 a[244:245], v118 offset:40960\n\t"
      "s_waitcnt lgkmcnt(12)\n\t"
      "ds_read_b64_tr_b16 a[246:247], v118 offset:41984\n\t"
      "ds_read_b64_tr_b16 a[184:185], v117 offset:49152\n\t"
      "ds_read_b64_tr_b16 a[186:187], v117 offset:50176\n\t"
      "s_waitcnt lgkmcnt(12)\n\t"
      "ds_read_b64_tr_b16 a[248:249], v118 offset:49152\n\t"
      "ds_read_b64_tr_b16 a[250:251], v118 offset:50176\n\t"
      "ds_read_b64_tr_b16 a[188:189], v117 offset:57344\n\t"
      "s_waitcnt lgkmcnt(12)\n\t"
      "ds_read_b64_tr_b16 a[190:191], v117 offset:58368\n\t"
      "ds_read_b64_tr_b16 a[252:253], v118 offset:57344\n\t"
      "ds_read_b64_tr_b16 a[254:255], v118 offset:58368\n\t"
      "s_waitcnt lgkmcnt(6)\n\t"
      "s_cmp_eq_u32 s41, 1\n\t"
      "s_cbranch_scc1 LWIN_1%=\n\t"
      "s_cmp_eq_u32 s41, 2\n\t"
      "s_cbranch_scc1 LWIN_2%=\n\t"
      "s_cmp_eq_u32 s41, 3\n\t"
      "s_cbranch_scc1 LWIN_3%=\n\t"
      "s_branch LWIN_0%=\n\t"
      ".p2align 6\n\t.fill 16, 4, 0xbf800000\n\t"
      "LPOLL_0%=:\n\t"
      "buffer_load_dwordx4 v[84:87], v60, s[36:39], s43 offen sc1\n\t"
      "buffer_load_dwordx4 v[88:91], v60, s[36:39], s43 offen offset:1024 sc1\n\t"
      "s_waitcnt lgkmcnt(4)\n\t"
      "v_mfma_f32_16x16x32_bf16 v[32:35], v[0:3], v[8:11], v[32:35]\n\t"
      "v_mfma_f32_16x16x32_bf16 v[36:39], v[4:7], v[8:11], v[36:39]\n\t"
      "v_mfma_f32_16x16x32_bf16 v[40:43], v[0:3], v[12:15], v[40:43]\n\t"
      "v_mfma_f32_16x16x32_bf16 v[44:47], v[4:7], v[12:15], v[44:47]\n\t"
      "buffer_load_dwordx4 v[92:95], v60, s[36:39], s68 offen sc1\n\t"
      "buffer_load_dwordx4 v[96:99], v60, s[36:39], s68 offen offset:1024 sc1\n\t"
      "ds_read_b64 v[54:55], v124 offset:53312\n\t"
      "s_xor_b32 s69, s97, 1024\n\t"
      "s_xor_b32 s98, s97, 512\n\t"
      "s_xor_b32 s23, s97, 1536\n\t"
      "s_waitcnt lgkmcnt(1)\n\t"
      "v_mfma_f32_16x16x32_bf16 v[32:35], v[76:79], v[112:115], v[32:35]\n\t"
      "v_mfma_f32_16x16x32_bf16 v[36:39], v[80:83], v[112:115], v[36:39]\n\t"
      "v_mfma_f32_16x16x32_bf16 v[40:43], v[76:79], v[116:119], v[40:43]\n\t"
      "v_mfma_f32_16x16x32_bf16 v[44:47], v[80:83], v[116:119], v[44:47]\n\t"
      "s_waitcnt lgkmcnt(0)\n\t"
      "v_add_f32 v48, v48, v54\n\t"
      "v_add_f32_e64 v49, v49, v55\n\t"
      "v_cvt_pk_bf16_f32 v48, v48, v49\n\t"
      "buffer_store_dword v48, v62, s[36:39], s28 offen sc1\n\t"
      "s_mov_b64 exec, 1\n\t"
      "ds_write_b64 v109, v[110:111] offset:55392\n\t"
      "s_mov_b64 exec, -1\n\t"
      "s_nop 4\n\t"
      "v_cvt_pk_bf16_f32 v68, v32, v33\n\t"
      "v_cvt_pk_bf16_f32 v69, v34, v35\n\t"
      "v_cvt_pk_bf16_f32 v70, v36, v37\n\t"
      "v_cvt_pk_bf16_f32 v71, v38, v39\n\t"
      "v_cvt_pk_bf16_f32 v74, v40, v41\n\t"
      "v_cvt_pk_bf16_f32 v75, v42, v43\n\t"
      "v_cvt_pk_bf16_f32 v72, v44, v45\n\t"
      "v_cvt_pk_bf16_f32 v73, v46, v47\n\t"
      "v_mov_b64 v[32:33], 0\n\t"
      "v_mov_b64 v[34:35], 0\n\t"
      "v_mov_b64 v[36:37], 0\n\t"
      "v_mov_b64 v[38:39], 0\n\t"
      "v_mov_b64 v[40:41], 0\n\t"
      "v_mov_b64 v[42:43], 0\n\t"
      "v_mov_b64 v[44:45], 0\n\t"
      "v_mov_b64 v[46:47], 0\n\t"
      "v_mov_b64 v[48:49], 0\n\t"
      "v_mov_b64 v[50:51], 0\n\t"
      "v_mov_b64 v[52:53], 0\n\t"
      "v_mov_b64 v[54:55], 0\n\t"
      "LGO_0%=:\n\t"
      "s_nop 0\n\t"
      "s_nop 0\n\t"
      "s_nop 0\n\t"
      "s_nop 0\n\t"
      "s_waitcnt vmcnt(4)\n\t"
      "v_pk_fma_f32 v[6:7], v[86:87], s[56:57], v[126:127] op_sel:[0,1,0] op_sel_hi:[1,1,0]\n\t"
      "v_pk_fma_f32 v[4:5], v[84:85], s[56:57], v[126:127] op_sel:[0,1,0] op_sel_hi:[1,1,0]\n\t"
      "v_cvt_pk_bf16_f32 v0, v4, v5\n\t"
      "v_cvt_pk_bf16_f32 v1, v6, v7\n\t"
      "s_nop 0\n\t"
      "ds_write_b64 v127, v[0:1] offset:40960\n\t"
      "s_waitcnt vmcnt(3)\n\t"
      "v_pk_fma_f32 v[8:9], v[88:89], s[56:57], v[126:127] op_sel:[0,1,0] op_sel_hi:[1,1,0]\n\t"
      "v_pk_fma_f32 v[10:11], v[90:91], s[56:57], v[126:127] op_sel:[0,1,0] op_sel_hi:[1,1,0]\n\t"
      "v_cvt_pk_bf16_f32 v2, v8, v9\n\t"
      "v_cvt_pk_bf16_f32 v3, v10, v11\n\t"
      "ds_write_b64 v127, v[2:3] offset:40968\n\t"
      "v_bitop3_b32 v64, v84, v85, v86 bitop3:0x80\n\t"
      "v_mfma_f32_16x16x32_bf16 v[56:59], v[0:3], v[0:3], 0\n\t"
      "v_bitop3_b32 v64, v64, v87, v88 bitop3:0x80\n\t"
      "v_bitop3_b32 v64, v64, v89, v90 bitop3:0x80\n\t"
      "v_bitop3_b32 v64, v64, v91, s58 bitop3:0x80\n\t"
      "v_cmp_eq_u32_e64 vcc, 0, v64\n\t"
      "v_smfmac_f32_16x16x64_bf16 v[36:39], v[0:3], a[64:71], v122\n\t"
      "s_nop 0\n\t"
      "v_bitop3_b32 v120, v120, v121, s53 bitop3:0x7e\n\t"
      "v_smfmac_f32_16x16x64_bf16 v[44:47], v[0:3], a[192:199], v122\n\t"
      "s_cbranch_vccnz LR0_0%=\n\t"
      "LC0_0%=:\n\t"
      "s_waitcnt lgkmcnt(0)\n\t"
      "s_barrier\n\t"
      "ds_read_b128 v[4:7], v127 offset:41984\n\t"
      "v_smfmac_f32_16x16x64_bf16 v[52:55], v[0:3], v[192:199], v122\n\t"
      "ds_read_b128 v[8:11], v127 offset:43008\n\t"
      "v_smfmac_f32_16x16x64_bf16 v[48:51], v[0:3], v[128:135], v122\n\t"
      "ds_read_b128 v[12:15], v127 offset:44032\n\t"
      "v_smfmac_f32_16x16x64_bf16 v[32:35], v[0:3], a[0:7], v122\n\t"
      "ds_read_b128 v[84:87], v127 offset:36864\n\t"
      "v_smfmac_f32_16x16x64_bf16 v[40:43], v[0:3], a[128:135], v122\n\t"
      "s_waitcnt lgkmcnt(3)\n\t"
      "v_smfmac_f32_16x16x64_bf16 v[32:35], v[4:7], a[8:15], v122\n\t"
      "ds_read_b128 v[80:83], v127 offset:35840\n\t"
      "v_smfmac_f32_16x16x64_bf16 v[40:43], v[4:7], a[136:143], v122\n\t"
      "s_waitcnt vmcnt(2)\n\t"
      "v_fma_f32 v23, v95, s57, v126\n\t"
      "v_fma_f32 v20, v92, s57, v126\n\t"
      "v_smfmac_f32_16x16x64_bf16 v[36:39], v[4:7], a[72:79], v122\n\t"
      "v_fma_f32 v22, v94, s57, v126\n\t"
      "v_cvt_pk_bf16_f32 v17, v22, v23\n\t"
      "v_bitop3_b32 v64, v92, v93, v94 bitop3:0x80\n\t"
      "v_smfmac_f32_16x16x64_bf16 v[44:47], v[4:7], a[200:207], v122\n\t"
      "s_waitcnt lgkmcnt(2)\n\t"
      "v_fma_f32 v21, v93, s57, v126\n\t"
      "v_cvt_pk_bf16_f32 v16, v20, v21\n\t"
      "v_smfmac_f32_16x16x64_bf16 v[36:39], v[8:11], a[80:87], v122\n\t"
      "s_waitcnt vmcnt(1)\n\t"
      "ds_write_b64 v127, v[16:17] offset:45056\n\t"
      "v_fma_f32 v27, v99, s57, v126\n\t"
      "v_fma_f32 v26, v98, s57, v126\n\t"
      "v_cvt_pk_bf16_f32 v19, v26, v27\n\t"
      "v_fma_f32 v25, v97, s57, v126\n\t"
      "v_fma_f32 v24, v96, s57, v126\n\t"
      "v_cvt_pk_bf16_f32 v18, v24, v25\n\t"
      "v_smfmac_f32_16x16x64_bf16 v[44:47], v[8:11], a[208:215], v122\n\t"
      "ds_write_b64 v127, v[18:19] offset:45064\n\t"
      "v_smfmac_f32_16x16x64_bf16 v[40:43], v[8:11], a[144:151], v122\n\t"
      "ds_read_b128 v[88:91], v127 offset:39936\n\t"
      "v_bitop3_b32 v64, v64, v95, v96 bitop3:0x80\n\t"
      "v_bitop3_b32 v64, v64, v97, v98 bitop3:0x80\n\t"
      "v_smfmac_f32_16x16x64_bf16 v[40:43], v[12:15], a[152:159], v122\n\t"
      "v_bitop3_b32 v64, v64, v99, s60 bitop3:0x80\n\t"
      "v_smfmac_f32_16x16x64_bf16 v[36:39], v[12:15], a[88:95], v122\n\t"
      "v_cmp_eq_u32_e64 vcc, 0, v64\n\t"
      "v_smfmac_f32_16x16x64_bf16 v[44:47], v[12:15], a[216:223], v122\n\t"
      "s_waitcnt lgkmcnt(1)\n\t"
      "v_smfmac_f32_16x16x64_bf16 v[32:35], v[8:11], a[16:23], v122\n\t"
      "s_cbranch_vccnz LR1_0%=\n\t"
      "LC1_0%=:\n\t"
      "v_smfmac_f32_16x16x64_bf16 v[40:43], v[16:19], a[160:167], v122\n\t"
      "s_barrier\n\t"
      "v_mfma_f32_16x16x32_bf16 v[56:59], v[16:19], v[16:19], v[56:59]\n\t"
      "ds_read_b128 v[20:23], v127 offset:46080\n\t"
      "s_nop 0\n\t"
      "v_smfmac_f32_16x16x64_bf16 v[32:35], v[12:15], a[24:31], v122\n\t"
      "ds_read_b128 v[24:27], v127 offset:47104\n\t"
      "v_smfmac_f32_16x16x64_bf16 v[44:47], v[16:19], a[224:231], v122\n\t"
      "ds_read_b128 v[28:31], v127 offset:48128\n\t"
      "v_max3_f32 v56, v56, v57, v58\n\t"
      "v_smfmac_f32_16x16x64_bf16 v[36:39], v[16:19], a[96:103], v122\n\t"
      "ds_read_b128 v[76:79], v127 offset:32768\n\t"
      "v_max_f32_e64 v56, v56, v59\n\t"
      "v_smfmac_f32_16x16x64_bf16 v[32:35], v[16:19], a[32:39], v122\n\t"
      "s_waitcnt lgkmcnt(3)\n\t"
      "v_readfirstlane_b32 s56, v120\n\t"
      "v_smfmac_f32_16x16x64_bf16 v[32:35], v[20:23], a[40:47], v122\n\t"
      "s_waitcnt lgkmcnt(2)\n\t"
      "v_max_f32_dpp v56, v56, v56 quad_perm:[1,0,3,2] row_mask:0xf bank_mask:0xf\n\t"
      "v_smfmac_f32_16x16x64_bf16 v[32:35], v[24:27], a[48:55], v122\n\t"
      "s_waitcnt lgkmcnt(1)\n\t"
      "v_max_f32_dpp v56, v56, v56 quad_perm:[2,3,0,1] row_mask:0xf bank_mask:0xf\n\t"
      "v_smfmac_f32_16x16x64_bf16 v[32:35], v[28:31], a[56:63], v122\n\t"
      "s_mov_b32 exec_lo, 0x100001\n\t"
      "s_mov_b32 exec_hi, 0x10000100\n\t"
      "v_smfmac_f32_16x16x64_bf16 v[36:39], v[20:23], a[104:111], v122\n\t"
      "ds_max_i32 v109, v56 offset:55392\n\t"
      "v_smfmac_f32_16x16x64_bf16 v[40:43], v[20:23], a[168:175], v122\n\t"
      "s_mov_b64 exec, -1\n\t"
      "v_add_f32 v33, v33, v35\n\t"
      "v_smfmac_f32_16x16x64_bf16 v[36:39], v[24:27], a[112:119], v122\n\t"
      "ds_read_b128 v[60:63], v127 offset:37888\n\t"
      "v_mul_f32_e64 v33, s59, v33\n\t"
      "v_smfmac_f32_16x16x64_bf16 v[36:39], v[28:31], a[120:127], v122\n\t"
      "v_add_f32_e64 v32, v32, v34\n\t"
      "v_smfmac_f32_16x16x64_bf16 v[44:47], v[20:23], a[232:239], v122\n\t"
      "ds_read_b128 v[56:59], v127 offset:38912\n\t"
      "v_mul_f32_e64 v32, s59, v32\n\t"
      "v_smfmac_f32_16x16x64_bf16 v[44:47], v[24:27], a[240:247], v122\n\t"
      "v_add_f32 v36, v36, v38\n\t"
      "v_add_f32 v37, v37, v39\n\t"
      "v_mul_f32_e64 v37, s59, v37\n\t"
      "v_smfmac_f32_16x16x64_bf16 v[40:43], v[24:27], a[176:183], v122\n\t"
      "ds_read_b128 v[92:95], v127 offset:34816\n\t"
      "v_cvt_pk_bf16_f32 v112, v32, v33\n\t"
      "v_smfmac_f32_16x16x64_bf16 v[44:47], v[28:31], a[248:255], v122\n\t"
      "ds_read_b128 v[96:99], v127 offset:33792\n\t"
      "v_mul_f32_e64 v36, s59, v36\n\t"
      "v_cvt_pk_bf16_f32 v113, v36, v37\n\t"
      "v_smfmac_f32_16x16x64_bf16 v[40:43], v[28:31], a[184:191], v122\n\t"
      "ds_write_b64 v125, v[112:113]\n\t"
      "v_smfmac_f32_16x16x64_bf16 v[48:51], v[4:7], v[136:143], v122\n\t"
      "s_nop 0\n\t"
      "v_add_f32 v45, v45, v47\n\t"
      "v_add_f32_e64 v44, v44, v46\n\t"
      "v_cvt_pk_bf16_f32 v121, v44, v45\n\t"
      "v_add_f32 v41, v41, v43\n\t"
      "v_add_f32 v40, v40, v42\n\t"
      "v_cvt_pk_bf16_f32 v120, v40, v41\n\t"
      "v_smfmac_f32_16x16x64_bf16 v[48:51], v[8:11], v[144:151], v122\n\t"
      "s_mov_b32 s58, 0x10000000\n\t"
      "ds_write_b64 v125, v[120:121] offset:2080\n\t"
      "v_smfmac_f32_16x16x64_bf16 v[48:51], v[12:15], v[152:159], v122\n\t"
      "s_waitcnt lgkmcnt(1)\n\t"
      "s_barrier\n\t"
      "ds_read_b64_tr_b16 v[36:37], v124 offset:50192\n\t"
      "v_smfmac_f32_16x16x64_bf16 v[48:51], v[16:19], v[160:167], v122\n\t"
      "ds_read_b32 v108, v109 offset:55392\n\t"
      "ds_read_b64_tr_b16 v[32:33], v124 offset:49152\n\t"
      "v_smfmac_f32_16x16x64_bf16 v[52:55], v[4:7], v[200:207], v122\n\t"
      "ds_read_b64_tr_b16 v[34:35], v124 offset:49672\n\t"
      "s_waitcnt vmcnt(0) lgkmcnt(4)\n\t"
      "ds_read_b64_tr_b16 v[38:39], v124 offset:50712\n\t"
      "v_smfmac_f32_16x16x64_bf16 v[48:51], v[20:23], v[168:175], v122\n\t"
      "s_barrier\n\t"
      "ds_read_b64_tr_b16 v[46:47], v124 offset:52792\n\t"
      "v_smfmac_f32_16x16x64_bf16 v[48:51], v[24:27], v[176:183], v122\n\t"
      "s_waitcnt lgkmcnt(4)\n\t"
      "ds_read_b64_tr_b16 v[44:45], v124 offset:52272\n\t"
      "v_readfirstlane_b32 s61, v108\n\t"
      "v_smfmac_f32_16x16x64_bf16 v[48:51], v[28:31], v[184:191], v122\n\t"
      "s_waitcnt lgkmcnt(3)\n\t"
      "ds_read_b64_tr_b16 v[42:43], v124 offset:51752\n\t"
      "ds_read_b64_tr_b16 v[40:41], v124 offset:51232\n\t"
      "s_waitcnt lgkmcnt(4)\n\t"
      "v_mfma_f32_16x16x32_bf16 v[112:115], v[76:79], v[32:35], 0\n\t"
      "s_lshr_b32 s61, s61, 23\n\t"
      "s_add_u32 s61, s61, s67\n\t"
      "v_mov_b32_dpp v102, v34 quad_perm:[2,3,0,1] row_mask:0xf bank_mask:0xf\n\t"
      "v_mov_b32_dpp v106, v38 quad_perm:[2,3,0,1] row_mask:0xf bank_mask:0xf\n\t"
      "s_max_u32 s61, s61, s66\n\t"
      "v_mfma_f32_16x16x32_bf16 v[116:119], v[80:83], v[32:35], 0\n\t"
      "s_lshr_b32 s61, s61, 1\n\t"
      "v_add_f32_e64 v48, v48, v50\n\t"
      "v_mov_b32_dpp v100, v32 quad_perm:[2,3,0,1] row_mask:0xf bank_mask:0xf\n\t"
      "v_mfma_f32_16x16x32_bf16 v[116:119], v[88:91], v[36:39], v[116:119]\n\t"
      "s_min_u32 s61, s61, 200\n\t"
      "v_mov_b32_dpp v107, v39 quad_perm:[2,3,0,1] row_mask:0xf bank_mask:0xf\n\t"
      "s_waitcnt lgkmcnt(0)\n\t"
      "v_mfma_f32_16x16x32_bf16 v[76:79], v[40:43], v[32:35], 0\n\t"
      "buffer_load_dwordx4 v127, s[36:39], s46 offen sc1 lds\n\t"
      "s_lshl_b32 s61, s61, 23\n\t"
      "v_mfma_f32_16x16x32_bf16 v[76:79], v[44:47], v[36:39], v[76:79]\n\t"
      "s_add_u32 s65, s61, 33554432\n\t"
      "v_mov_b32_dpp v104, v36 quad_perm:[2,3,0,1] row_mask:0xf bank_mask:0xf\n\t"
      "v_mov_b32_dpp v103, v35 quad_perm:[2,3,0,1] row_mask:0xf bank_mask:0xf\n\t"
      "v_mov_b32_dpp v101, v33 quad_perm:[2,3,0,1] row_mask:0xf bank_mask:0xf\n\t"
      "v_mfma_f32_16x16x32_bf16 v[112:115], v[84:87], v[36:39], v[112:115]\n\t"
      "s_pack_hh_b32_b16 s75, s65, s65\n\t"
      "v_add_f32 v49, v49, v51\n\t"
      "v_cvt_pk_bf16_f32 v64, v48, v49\n\t"
      "v_cndmask_b32_e64 v120, v76, v78, s[72:73]\n\t"
      "v_cndmask_b32_e64 v121, v77, v79, s[72:73]\n\t"
      "v_mov_b32_dpp v105, v37 quad_perm:[2,3,0,1] row_mask:0xf bank_mask:0xf\n\t"
      "v_mfma_f32_16x16x32_bf16 v[112:115], v[92:95], v[100:103], v[112:115]\n\t"
      "buffer_load_dwordx4 v127, s[36:39], s46 offen offset:1024 sc1 lds\n\t"
      "v_mfma_f32_16x16x32_bf16 v[116:119], v[96:99], v[100:103], v[116:119]\n\t"
      "s_pack_hh_b32_b16 s74, s65, s65\n\t"
      "v_add_f32_dpp v121, v121, v121 quad_perm:[2,3,0,1] row_mask:0xf bank_mask:0xf\n\t"
      "v_add_f32_dpp v120, v120, v120 quad_perm:[2,3,0,1] row_mask:0xf bank_mask:0xf\n\t"
      "v_mfma_f32_16x16x32_bf16 v[112:115], v[56:59], v[104:107], v[112:115]\n\t"
      "v_exp_f32 v81, v121\n\t"
      "v_mfma_f32_16x16x32_bf16 v[116:119], v[60:63], v[104:107], v[116:119]\n\t"
      "s_mov_b32 m0, s45\n\t"
      "v_mov_b64 v[90:91], s[74:75]\n\t"
      "v_exp_f32 v80, v120\n\t"
      "v_mov_b64 v[88:89], s[74:75]\n\t"
      "v_cvt_pk_bf16_f32 v108, v80, v81\n\t"
      "v_smfmac_f32_16x16x64_bf16 v[52:55], v[8:11], v[208:215], v122\n\t"
      "buffer_load_dwordx4 v65, s[36:39], s44 offen offset:1024 sc1 lds\n\t"
      "v_mfma_f32_16x16x32_bf16 v[84:87], v[108:111], v[64:67], 0\n\t"
      "s_add_u32 s57, s61, 310378496\n\t"
      "v_cndmask_b32_e64 v112, v112, v116, s[72:73]\n\t"
      "v_exp_f32_e64 v56, v112\n\t"
      "v_mfma_f32_16x16x32_bf16 v[48:51], v[108:111], v[88:91], 0\n\t"
      "s_add_u32 s43, s43, 0x8000\n\t"
      "v_cndmask_b32_e64 v113, v113, v117, s[72:73]\n\t"
      "v_cndmask_b32_e64 v114, v114, v118, s[72:73]\n\t"
      "v_exp_f32_e64 v58, v114\n\t"
      "v_cndmask_b32_e64 v115, v115, v119, s[72:73]\n\t"
      "v_exp_f32 v57, v113\n\t"
      "v_exp_f32 v59, v115\n\t"
      "v_cvt_pk_bf16_f32 v96, v56, v57\n\t"
      "v_cvt_pk_bf16_f32 v97, v58, v59\n\t"
      "v_max3_f32 v82, |v112|, |v113|, |v114|\n\t"
      "v_mov_b32_dpp v98, v96 quad_perm:[2,3,0,1] row_mask:0xf bank_mask:0xf\n\t"
      "v_mov_b32_dpp v99, v97 quad_perm:[2,3,0,1] row_mask:0xf bank_mask:0xf\n\t"
      "v_smfmac_f32_16x16x64_bf16 v[52:55], v[12:15], v[216:223], v122\n\t"
      "v_max3_f32 v83, |v115|, |v120|, |v121|\n\t"
      "v_mfma_f32_16x16x32_bf16 v[48:51], v[96:99], v[88:91], v[48:51]\n\t"
      "v_max_f32_e64 v82, v82, v83\n\t"
      "v_cmp_nge_f32 vcc, 0x42000000, v82\n\t"
      "v_mfma_f32_16x16x32_bf16 v[84:87], v[96:99], v[68:71], v[84:87]\n\t"
      "buffer_load_dwordx4 v65, s[36:39], s44 offen sc1 lds\n\t"
      "v_smfmac_f32_16x16x64_bf16 v[52:55], v[16:19], v[224:231], v122\n\t"
      "s_add_u32 s68, s68, 0x8000\n\t"
      "s_cbranch_vccnz LMX_0%=\n\t"
      "v_rcp_f32 v49, v49\n\t"
      "v_rcp_f32_e64 v48, v48\n\t"
      "v_smfmac_f32_16x16x64_bf16 v[52:55], v[20:23], v[232:239], v122\n\t"
      "v_fma_f32 v85, v85, v49, s96\n\t"
      "v_fma_f32 v84, v84, v48, s96\n\t"
      "v_smfmac_f32_16x16x64_bf16 v[52:55], v[24:27], v[240:247], v122\n\t"
      "buffer_atomic_add v84, v123, s[36:39], s97 offen\n\t"
      "buffer_atomic_add v85, v123, s[36:39], s98 offen\n\t"
      "v_smfmac_f32_16x16x64_bf16 v[52:55], v[28:31], v[248:255], v122\n\t"
      "v_mfma_f32_16x16x32_bf16 v[92:95], v[96:99], v[72:75], 0\n\t"
      "s_nop 3\n\t"
      "v_add_f32 v53, v53, v55\n\t"
      "v_add_f32_e64 v52, v52, v54\n\t"
      "v_cvt_pk_bf16_f32 v66, v52, v53\n\t"
      "s_add_u32 s62, s61, 0x83400000\n\t"
      "v_mov_b32_e64 v126, s62\n\t"
      "v_mfma_f32_16x16x32_bf16 v[92:95], v[108:111], v[66:69], v[92:95]\n\t"
      "s_add_u32 s97, s97, 0x8000\n\t"
      "s_nop 6\n\t"
      "v_fma_f32 v95, v95, v49, s96\n\t"
      "buffer_atomic_add v95, v123, s[36:39], s23 offen\n\t"
      "v_fma_f32 v94, v94, v48, s96\n\t"
      "buffer_atomic_add v94, v123, s[36:39], s69 offen\n\t"
      "LWIN_0%=:\n\t"
      "v_add_u32 v100, s55, v127\n\t"
      "ds_read_b128 v[60:63], v100 offset:57344\n\t"
      "s_cmp_eq_u32 s56, 0\n\t"
      "s_cbranch_scc0 LSLOW_0%=\n\t"
      "LWIN1_0%=:\n\t"
      "buffer_load_dwordx2 v[120:121], off, s[36:39], s50 sc1\n\t"
      "s_mov_b32 s64, s50\n\t"
      "s_bitcmp1_b64 s[84:85], s40\n\t"
      "s_cbranch_scc1 LEARLY_0%=\n\t"
      "LROLE_0%=:\n\t"
      "v_add_u32 v52, 65536, v127\n\t"
      "ds_read_b128 v[88:91], v52\n\t"
      "ds_read_b128 v[92:95], v52 offset:1024\n\t"
      "ds_read_b128 v[32:35], v52 offset:4096\n\t"
      "ds_read_b128 v[36:39], v52 offset:5120\n\t"
      "ds_read_b128 v[40:43], v52 offset:8192\n\t"
      "ds_read_b128 v[44:47], v52 offset:9216\n\t"
      "v_mov_b64 v[48:49], 0\n\t"
      "v_mov_b64 v[50:51], 0\n\t"
      "v_add_u32 v104, s71, v100\n\t"
      "ds_read_b128 v[76:79], v104\n\t"
      "ds_read_b128 v[80:83], v104 offset:1024\n\t"
      "ds_read_b128 v[112:115], v100\n\t"
      "ds_read_b128 v[116:119], v100 offset:1024\n\t"
      "v_xor_b32 v101, 2048, v104\n\t"
      "v_xor_b32 v102, 4096, v104\n\t"
      "v_xor_b32 v103, 6144, v104\n\t"
      "s_waitcnt lgkmcnt(8)\n\t"
      "v_smfmac_f32_16x16x64_bf16 v[48:51], v[0:3], v[88:95], v122\n\t"
      "ds_read_b128 v[88:91], v52 offset:12288\n\t"
      "ds_read_b128 v[92:95], v52 offset:13312\n\t"
      "s_add_u32 m0, s55, s71\n\t"
      "s_sub_u32 s71, 204800, s71\n\t"
      "s_waitcnt lgkmcnt(8)\n\t"
      "v_smfmac_f32_16x16x64_bf16 v[48:51], v[4:7], v[32:39], v122\n\t"
      "ds_read_b128 v[16:19], v101\n\t"
      "ds_read_b128 v[20:23], v101 offset:1024\n\t"
      "s_add_u32 s40, s40, 1\n\t"
      "s_cmp_lt_u32 s40, 63\n\t"
      "s_waitcnt lgkmcnt(8)\n\t"
      "v_smfmac_f32_16x16x64_bf16 v[48:51], v[8:11], v[40:47], v122\n\t"
      "ds_read_b128 v[24:27], v100 offset:8192\n\t"
      "ds_read_b128 v[28:31], v100 offset:9216\n\t"
      "s_cselect_b32 s61, 786432, 0\n\t"
      "s_cselect_b32 s60, s58, 0\n\t"
      "s_waitcnt lgkmcnt(4)\n\t"
      "v_smfmac_f32_16x16x64_bf16 v[48:51], v[12:15], v[88:95], v122\n\t"
      "s_add_u32 s44, s44, s61\n\t"
      "s_cmp_lt_u32 s40, 62\n\t"
      "ds_read_b128 v[0:3], v102\n\t"
      "ds_read_b128 v[4:7], v102 offset:1024\n\t"
      "ds_read_b128 v[8:11], v100 offset:16384\n\t"
      "ds_read_b128 v[12:15], v100 offset:17408\n\t"
      "s_cselect_b32 s61, 786432, 0\n\t"
      "s_add_u32 s46, s46, s61\n\t"
      "s_add_u32 s61, s40, 3\n\t"
      "s_and_b32 s62, s61, 3\n\t"
      "s_cselect_b32 s62, 16, 2000\n\t"
      "s_cmp_lt_u32 s61, 64\n\t"
      "s_cselect_b32 s62, s62, 0\n\t"
      "s_add_u32 s50, s50, s62\n\t"
      "s_mov_b32 s28, s49\n\t"
      "s_add_u32 s49, s49, 65536\n\t"
      "s_cmp_eq_u32 s40, 64\n\t"
      "v_add_f32 v48, v48, v50\n\t"
      "v_add_f32 v49, v49, v51\n\t"
      "s_cbranch_scc1 LFIN_0%=\n\t"
      "v_mfma_f32_16x16x32_bf16 v[32:35], v[76:79], v[112:115], 0\n\t"
      "s_mov_b32 s63, 0x2000\n\t"
      "v_mfma_f32_16x16x32_bf16 v[36:39], v[80:83], v[112:115], 0\n\t"
      "v_mfma_f32_16x16x32_bf16 v[40:43], v[76:79], v[116:119], 0\n\t"
      "v_mfma_f32_16x16x32_bf16 v[44:47], v[80:83], v[116:119], 0\n\t"
      "ds_read_b128 v[76:79], v103\n\t"
      "ds_read_b128 v[80:83], v103 offset:1024\n\t"
      "ds_read_b128 v[112:115], v100 offset:24576\n\t"
      "ds_read_b128 v[116:119], v100 offset:25600\n\t"
      "s_waitcnt lgkmcnt(8)\n\t"
      "v_mfma_f32_16x16x32_bf16 v[32:35], v[16:19], v[24:27], v[32:35]\n\t"
      "v_mfma_f32_16x16x32_bf16 v[36:39], v[20:23], v[24:27], v[36:39]\n\t"
      "v_mfma_f32_16x16x32_bf16 v[40:43], v[16:19], v[28:31], v[40:43]\n\t"
      "v_mfma_f32_16x16x32_bf16 v[44:47], v[20:23], v[28:31], v[44:47]\n\t"
      "LGATE_0%=:\n\t"
      "LBAR_0%=:\n\t"
      "s_cmp_gt_u32 s40, 1\n\t"
      "s_barrier\n\t"
      "s_cbranch_scc1 LPOLL_0%=\n\t"
      "s_branch LFIRST_0%=\n\t"
      ".p2align 6\n\t.fill 16, 4, 0xbf800000\n\t"
      "LPOLL_1%=:\n\t"
      "buffer_load_dwordx4 v[84:87], v60, s[36:39], s43 offen sc1\n\t"
      "buffer_load_dwordx4 v[88:91], v60, s[36:39], s43 offen offset:1024 sc1\n\t"
      "s_waitcnt lgkmcnt(4)\n\t"
      "v_mfma_f32_16x16x32_bf16 v[32:35], v[0:3], v[8:11], v[32:35]\n\t"
      "v_mfma_f32_16x16x32_bf16 v[36:39], v[4:7], v[8:11], v[36:39]\n\t"
      "v_mfma_f32_16x16x32_bf16 v[40:43], v[0:3], v[12:15], v[40:43]\n\t"
      "v_mfma_f32_16x16x32_bf16 v[44:47], v[4:7], v[12:15], v[44:47]\n\t"
      "buffer_load_dwordx4 v[92:95], v60, s[36:39], s68 offen sc1\n\t"
      "buffer_load_dwordx4 v[96:99], v60, s[36:39], s68 offen offset:1024 sc1\n\t"
      "ds_read_b64 v[54:55], v124 offset:53832\n\t"
      "s_xor_b32 s69, s97, 1024\n\t"
      "s_xor_b32 s98, s97, 512\n\t"
      "s_xor_b32 s23, s97, 1536\n\t"
      "s_waitcnt lgkmcnt(1)\n\t"
      "v_mfma_f32_16x16x32_bf16 v[32:35], v[76:79], v[112:115], v[32:35]\n\t"
      "v_mfma_f32_16x16x32_bf16 v[36:39], v[80:83], v[112:115], v[36:39]\n\t"
      "v_mfma_f32_16x16x32_bf16 v[40:43], v[76:79], v[116:119], v[40:43]\n\t"
      "v_mfma_f32_16x16x32_bf16 v[44:47], v[80:83], v[116:119], v[44:47]\n\t"
      "s_waitcnt lgkmcnt(0)\n\t"
      "v_add_f32 v48, v48, v54\n\t"
      "v_add_f32_e64 v49, v49, v55\n\t"
      "v_cvt_pk_bf16_f32 v48, v48, v49\n\t"
      "buffer_store_dword v48, v62, s[36:39], s28 offen sc1\n\t"
      "s_mov_b64 exec, 1\n\t"
      "ds_write_b64 v109, v[110:111] offset:55392\n\t"
      "s_mov_b64 exec, -1\n\t"
      "s_nop 4\n\t"
      "v_cvt_pk_bf16_f32 v68, v32, v33\n\t"
      "v_cvt_pk_bf16_f32 v69, v34, v35\n\t"
      "v_cvt_pk_bf16_f32 v70, v36, v37\n\t"
      "v_cvt_pk_bf16_f32 v71, v38, v39\n\t"
      "v_cvt_pk_bf16_f32 v74, v40, v41\n\t"
      "v_cvt_pk_bf16_f32 v75, v42, v43\n\t"
      "v_cvt_pk_bf16_f32 v72, v44, v45\n\t"
      "v_cvt_pk_bf16_f32 v73, v46, v47\n\t"
      "v_mov_b64 v[32:33], 0\n\t"
      "v_mov_b64 v[34:35], 0\n\t"
      "v_mov_b64 v[36:37], 0\n\t"
      "v_mov_b64 v[38:39], 0\n\t"
      "v_mov_b64 v[40:41], 0\n\t"
      "v_mov_b64 v[42:43], 0\n\t"
      "v_mov_b64 v[44:45], 0\n\t"
      "v_mov_b64 v[46:47], 0\n\t"
      "v_mov_b64 v[48:49], 0\n\t"
      "v_mov_b64 v[50:51], 0\n\t"
      "v_mov_b64 v[52:53], 0\n\t"
      "v_mov_b64 v[54:55], 0\n\t"
      "LGO_1%=:\n\t"
      "s_nop 0\n\t"
      "s_nop 0\n\t"
      "s_nop 0\n\t"
      "s_nop 0\n\t"
      "s_waitcnt vmcnt(4)\n\t"
      "v_pk_fma_f32 v[2:3], v[86:87], s[56:57], v[126:127] op_sel:[0,1,0] op_sel_hi:[1,1,0]\n\t"
      "v_pk_fma_f32 v[0:1], v[84:85], s[56:57], v[126:127] op_sel:[0,1,0] op_sel_hi:[1,1,0]\n\t"
      "v_cvt_pk_bf16_f32 v4, v0, v1\n\t"
      "v_cvt_pk_bf16_f32 v5, v2, v3\n\t"
      "s_nop 0\n\t"
      "ds_write_b64 v127, v[4:5] offset:41984\n\t"
      "s_waitcnt vmcnt(3)\n\t"
      "v_pk_fma_f32 v[12:13], v[88:89], s[56:57], v[126:127] op_sel:[0,1,0] op_sel_hi:[1,1,0]\n\t"
      "v_pk_fma_f32 v[14:15], v[90:91], s[56:57], v[126:127] op_sel:[0,1,0] op_sel_hi:[1,1,0]\n\t"
      "v_cvt_pk_bf16_f32 v6, v12, v13\n\t"
      "v_cvt_pk_bf16_f32 v7, v14, v15\n\t"
      "ds_write_b64 v127, v[6:7] offset:41992\n\t"
      "v_bitop3_b32 v64, v84, v85, v86 bitop3:0x80\n\t"
      "v_mfma_f32_16x16x32_bf16 v[56:59], v[4:7], v[4:7], 0\n\t"
      "v_bitop3_b32 v64, v64, v87, v88 bitop3:0x80\n\t"
      "v_bitop3_b32 v64, v64, v89, v90 bitop3:0x80\n\t"
      "v_bitop3_b32 v64, v64, v91, s58 bitop3:0x80\n\t"
      "v_cmp_eq_u32_e64 vcc, 0, v64\n\t"
      "v_smfmac_f32_16x16x64_bf16 v[36:39], v[4:7], a[72:79], v122\n\t"
      "s_nop 0\n\t"
      "v_bitop3_b32 v120, v120, v121, s53 bitop3:0x7e\n\t"
      "v_smfmac_f32_16x16x64_bf16 v[44:47], v[4:7], a[200:207], v122\n\t"
      "s_cbranch_vccnz LR0_1%=\n\t"
      "LC0_1%=:\n\t"
      "s_waitcnt lgkmcnt(0)\n\t"
      "s_barrier\n\t"
      "ds_read_b128 v[0:3], v127 offset:40960\n\t"
      "v_smfmac_f32_16x16x64_bf16 v[52:55], v[4:7], v[200:207], v122\n\t"
      "ds_read_b128 v[12:15], v127 offset:44032\n\t"
      "v_smfmac_f32_16x16x64_bf16 v[48:51], v[4:7], v[136:143], v122\n\t"
      "ds_read_b128 v[8:11], v127 offset:43008\n\t"
      "v_smfmac_f32_16x16x64_bf16 v[32:35], v[4:7], a[8:15], v122\n\t"
      "ds_read_b128 v[84:87], v127 offset:36864\n\t"
      "v_smfmac_f32_16x16x64_bf16 v[40:43], v[4:7], a[136:143], v122\n\t"
      "s_waitcnt lgkmcnt(3)\n\t"
      "v_smfmac_f32_16x16x64_bf16 v[32:35], v[0:3], a[0:7], v122\n\t"
      "ds_read_b128 v[80:83], v127 offset:35840\n\t"
      "v_smfmac_f32_16x16x64_bf16 v[40:43], v[0:3], a[128:135], v122\n\t"
      "s_waitcnt vmcnt(2)\n\t"
      "v_fma_f32 v19, v95, s57, v126\n\t"
      "v_fma_f32 v16, v92, s57, v126\n\t"
      "v_smfmac_f32_16x16x64_bf16 v[36:39], v[0:3], a[64:71], v122\n\t"
      "v_fma_f32 v18, v94, s57, v126\n\t"
      "v_cvt_pk_bf16_f32 v21, v18, v19\n\t"
      "v_bitop3_b32 v64, v92, v93, v94 bitop3:0x80\n\t"
      "v_smfmac_f32_16x16x64_bf16 v[44:47], v[0:3], a[192:199], v122\n\t"
      "s_waitcnt lgkmcnt(2)\n\t"
      "v_fma_f32 v17, v93, s57, v126\n\t"
      "v_cvt_pk_bf16_f32 v20, v16, v17\n\t"
      "v_smfmac_f32_16x16x64_bf16 v[36:39], v[12:15], a[88:95], v122\n\t"
      "s_waitcnt vmcnt(1)\n\t"
      "ds_write_b64 v127, v[20:21] offset:46080\n\t"
      "v_fma_f32 v31, v99, s57, v126\n\t"
      "v_fma_f32 v30, v98, s57, v126\n\t"
      "v_cvt_pk_bf16_f32 v23, v30, v31\n\t"
      "v_fma_f32 v29, v97, s57, v126\n\t"
      "v_fma_f32 v28, v96, s57, v126\n\t"
      "v_cvt_pk_bf16_f32 v22, v28, v29\n\t"
      "v_smfmac_f32_16x16x64_bf16 v[44:47], v[12:15], a[216:223], v122\n\t"
      "ds_write_b64 v127, v[22:23] offset:46088\n\t"
      "v_smfmac_f32_16x16x64_bf16 v[40:43], v[12:15], a[152:159], v122\n\t"
      "ds_read_b128 v[88:91], v127 offset:39936\n\t"
      "v_bitop3_b32 v64, v64, v95, v96 bitop3:0x80\n\t"
      "v_bitop3_b32 v64, v64, v97, v98 bitop3:0x80\n\t"
      "v_smfmac_f32_16x16x64_bf16 v[40:43], v[8:11], a[144:151], v122\n\t"
      "v_bitop3_b32 v64, v64, v99, s60 bitop3:0x80\n\t"
      "v_smfmac_f32_16x16x64_bf16 v[36:39], v[8:11], a[80:87], v122\n\t"
      "v_cmp_eq_u32_e64 vcc, 0, v64\n\t"
      "v_smfmac_f32_16x16x64_bf16 v[44:47], v[8:11], a[208:215], v122\n\t"
      "s_waitcnt lgkmcnt(1)\n\t"
      "v_smfmac_f32_16x16x64_bf16 v[32:35], v[12:15], a[24:31], v122\n\t"
      "s_cbranch_vccnz LR1_1%=\n\t"
      "LC1_1%=:\n\t"
      "v_smfmac_f32_16x16x64_bf16 v[40:43], v[20:23], a[168:175], v122\n\t"
      "s_barrier\n\t"
      "v_mfma_f32_16x16x32_bf16 v[56:59], v[20:23], v[20:23], v[56:59]\n\t"
      "ds_read_b128 v[16:19], v127 offset:45056\n\t"
      "s_nop 0\n\t"
      "v_smfmac_f32_16x16x64_bf16 v[32:35], v[8:11], a[16:23], v122\n\t"
      "ds_read_b128 v[28:31], v127 offset:48128\n\t"
      "v_smfmac_f32_16x16x64_bf16 v[44:47], v[20:23], a[232:239], v122\n\t"
      "ds_read_b128 v[24:27], v127 offset:47104\n\t"
      "v_max3_f32 v56, v56, v57, v58\n\t"
      "v_smfmac_f32_16x16x64_bf16 v[36:39], v[20:23], a[104:111], v122\n\t"
      "ds_read_b128 v[76:79], v127 offset:32768\n\t"
      "v_max_f32_e64 v56, v56, v59\n\t"
      "v_smfmac_f32_16x16x64_bf16 v[32:35], v[20:23], a[40:47], v122\n\t"
      "s_waitcnt lgkmcnt(3)\n\t"
      "v_readfirstlane_b32 s56, v120\n\t"
      "v_smfmac_f32_16x16x64_bf16 v[32:35], v[16:19], a[32:39], v122\n\t"
      "s_waitcnt lgkmcnt(2)\n\t"
      "v_max_f32_dpp v56, v56, v56 quad_perm:[1,0,3,2] row_mask:0xf bank_mask:0xf\n\t"
      "v_smfmac_f32_16x16x64_bf16 v[32:35], v[28:31], a[56:63], v122\n\t"
      "s_waitcnt lgkmcnt(1)\n\t"
      "v_max_f32_dpp v56, v56, v56 quad_perm:[2,3,0,1] row_mask:0xf bank_mask:0xf\n\t"
      "v_smfmac_f32_16x16x64_bf16 v[32:35], v[24:27], a[48:55], v122\n\t"
      "s_mov_b32 exec_lo, 0x100001\n\t"
      "s_mov_b32 exec_hi, 0x10000100\n\t"
      "v_smfmac_f32_16x16x64_bf16 v[36:39], v[16:19], a[96:103], v122\n\t"
      "ds_max_i32 v109, v56 offset:55392\n\t"
      "v_smfmac_f32_16x16x64_bf16 v[40:43], v[16:19], a[160:167], v122\n\t"
      "s_mov_b64 exec, -1\n\t"
      "v_add_f32 v33, v33, v35\n\t"
      "v_smfmac_f32_16x16x64_bf16 v[36:39], v[28:31], a[120:127], v122\n\t"
      "ds_read_b128 v[60:63], v127 offset:37888\n\t"
      "v_mul_f32_e64 v33, s59, v33\n\t"
      "v_smfmac_f32_16x16x64_bf16 v[36:39], v[24:27], a[112:119], v122\n\t"
      "v_add_f32_e64 v32, v32, v34\n\t"
      "v_smfmac_f32_16x16x64_bf16 v[44:47], v[16:19], a[224:231], v122\n\t"
      "ds_read_b128 v[56:59], v127 offset:38912\n\t"
      "v_mul_f32_e64 v32, s59, v32\n\t"
      "v_smfmac_f32_16x16x64_bf16 v[44:47], v[28:31], a[248:255], v122\n\t"
      "v_add_f32 v36, v36, v38\n\t"
      "v_add_f32 v37, v37, v39\n\t"
      "v_mul_f32_e64 v37, s59, v37\n\t"
      "v_smfmac_f32_16x16x64_bf16 v[40:43], v[28:31], a[184:191], v122\n\t"
      "ds_read_b128 v[92:95], v127 offset:34816\n\t"
      "v_cvt_pk_bf16_f32 v112, v32, v33\n\t"
      "v_smfmac_f32_16x16x64_bf16 v[44:47], v[24:27], a[240:247], v122\n\t"
      "ds_read_b128 v[96:99], v127 offset:33792\n\t"
      "v_mul_f32_e64 v36, s59, v36\n\t"
      "v_cvt_pk_bf16_f32 v113, v36, v37\n\t"
      "v_smfmac_f32_16x16x64_bf16 v[40:43], v[24:27], a[176:183], v122\n\t"
      "ds_write_b64 v125, v[112:113]\n\t"
      "v_smfmac_f32_16x16x64_bf16 v[48:51], v[0:3], v[128:135], v122\n\t"
      "s_nop 0\n\t"
      "v_add_f32 v45, v45, v47\n\t"
      "v_add_f32_e64 v44, v44, v46\n\t"
      "v_cvt_pk_bf16_f32 v121, v44, v45\n\t"
      "v_add_f32 v41, v41, v43\n\t"
      "v_add_f32 v40, v40, v42\n\t"
      "v_cvt_pk_bf16_f32 v120, v40, v41\n\t"
      "v_smfmac_f32_16x16x64_bf16 v[48:51], v[12:15], v[152:159], v122\n\t"
      "s_mov_b32 s58, 0x10000000\n\t"
      "ds_write_b64 v125, v[120:121] offset:2080\n\t"
      "v_smfmac_f32_16x16x64_bf16 v[48:51], v[8:11], v[144:151], v122\n\t"
      "s_waitcnt lgkmcnt(1)\n\t"
      "s_barrier\n\t"
      "ds_read_b64_tr_b16 v[36:37], v124 offset:50192\n\t"
      "v_smfmac_f32_16x16x64_bf16 v[48:51], v[20:23], v[168:175], v122\n\t"
      "ds_read_b32 v108, v109 offset:55392\n\t"
      "ds_read_b64_tr_b16 v[32:33], v124 offset:49152\n\t"
      "v_smfmac_f32_16x16x64_bf16 v[52:55], v[0:3], v[192:199], v122\n\t"
      "ds_read_b64_tr_b16 v[34:35], v124 offset:49672\n\t"
      "s_waitcnt vmcnt(0) lgkmcnt(4)\n\t"
      "ds_read_b64_tr_b16 v[38:39], v124 offset:50712\n\t"
      "v_smfmac_f32_16x16x64_bf16 v[48:51], v[16:19], v[160:167], v122\n\t"
      "s_barrier\n\t"
      "ds_read_b64_tr_b16 v[46:47], v124 offset:52792\n\t"
      "v_smfmac_f32_16x16x64_bf16 v[48:51], v[28:31], v[184:191], v122\n\t"
      "s_waitcnt lgkmcnt(4)\n\t"
      "ds_read_b64_tr_b16 v[44:45], v124 offset:52272\n\t"
      "v_readfirstlane_b32 s61, v108\n\t"
      "v_smfmac_f32_16x16x64_bf16 v[48:51], v[24:27], v[176:183], v122\n\t"
      "s_waitcnt lgkmcnt(3)\n\t"
      "ds_read_b64_tr_b16 v[42:43], v124 offset:51752\n\t"
      "ds_read_b64_tr_b16 v[40:41], v124 offset:51232\n\t"
      "s_waitcnt lgkmcnt(4)\n\t"
      "v_mfma_f32_16x16x32_bf16 v[112:115], v[76:79], v[32:35], 0\n\t"
      "s_lshr_b32 s61, s61, 23\n\t"
      "s_add_u32 s61, s61, s67\n\t"
      "v_mov_b32_dpp v102, v34 quad_perm:[2,3,0,1] row_mask:0xf bank_mask:0xf\n\t"
      "v_mov_b32_dpp v106, v38 quad_perm:[2,3,0,1] row_mask:0xf bank_mask:0xf\n\t"
      "s_max_u32 s61, s61, s66\n\t"
      "v_mfma_f32_16x16x32_bf16 v[116:119], v[80:83], v[32:35], 0\n\t"
      "s_lshr_b32 s61, s61, 1\n\t"
      "v_add_f32_e64 v48, v48, v50\n\t"
      "v_mov_b32_dpp v100, v32 quad_perm:[2,3,0,1] row_mask:0xf bank_mask:0xf\n\t"
      "v_mfma_f32_16x16x32_bf16 v[116:119], v[88:91], v[36:39], v[116:119]\n\t"
      "s_min_u32 s61, s61, 200\n\t"
      "v_mov_b32_dpp v107, v39 quad_perm:[2,3,0,1] row_mask:0xf bank_mask:0xf\n\t"
      "s_waitcnt lgkmcnt(0)\n\t"
      "v_mfma_f32_16x16x32_bf16 v[76:79], v[40:43], v[32:35], 0\n\t"
      "buffer_load_dwordx4 v127, s[36:39], s46 offen sc1 lds\n\t"
      "s_lshl_b32 s61, s61, 23\n\t"
      "v_mfma_f32_16x16x32_bf16 v[76:79], v[44:47], v[36:39], v[76:79]\n\t"
      "s_add_u32 s65, s61, 33554432\n\t"
      "v_mov_b32_dpp v104, v36 quad_perm:[2,3,0,1] row_mask:0xf bank_mask:0xf\n\t"
      "v_mov_b32_dpp v103, v35 quad_perm:[2,3,0,1] row_mask:0xf bank_mask:0xf\n\t"
      "v_mov_b32_dpp v101, v33 quad_perm:[2,3,0,1] row_mask:0xf bank_mask:0xf\n\t"
      "v_mfma_f32_16x16x32_bf16 v[112:115], v[84:87], v[36:39], v[112:115]\n\t"
      "s_pack_hh_b32_b16 s75, s65, s65\n\t"
      "v_add_f32 v49, v49, v51\n\t"
      "v_cvt_pk_bf16_f32 v64, v48, v49\n\t"
      "v_cndmask_b32_e64 v120, v76, v78, s[72:73]\n\t"
      "v_cndmask_b32_e64 v121, v77, v79, s[72:73]\n\t"
      "v_mov_b32_dpp v105, v37 quad_perm:[2,3,0,1] row_mask:0xf bank_mask:0xf\n\t"
      "v_mfma_f32_16x16x32_bf16 v[112:115], v[92:95], v[100:103], v[112:115]\n\t"
      "buffer_load_dwordx4 v127, s[36:39], s46 offen offset:1024 sc1 lds\n\t"
      "v_mfma_f32_16x16x32_bf16 v[116:119], v[96:99], v[100:103], v[116:119]\n\t"
      "s_pack_hh_b32_b16 s74, s65, s65\n\t"
      "v_add_f32_dpp v121, v121, v121 quad_perm:[2,3,0,1] row_mask:0xf bank_mask:0xf\n\t"
      "v_add_f32_dpp v120, v120, v120 quad_perm:[2,3,0,1] row_mask:0xf bank_mask:0xf\n\t"
      "v_mfma_f32_16x16x32_bf16 v[112:115], v[56:59], v[104:107], v[112:115]\n\t"
      "v_exp_f32 v81, v121\n\t"
      "v_mfma_f32_16x16x32_bf16 v[116:119], v[60:63], v[104:107], v[116:119]\n\t"
      "s_mov_b32 m0, s45\n\t"
      "v_mov_b64 v[90:91], s[74:75]\n\t"
      "v_exp_f32 v80, v120\n\t"
      "v_mov_b64 v[88:89], s[74:75]\n\t"
      "v_cvt_pk_bf16_f32 v108, v80, v81\n\t"
      "v_smfmac_f32_16x16x64_bf16 v[52:55], v[12:15], v[216:223], v122\n\t"
      "buffer_load_dwordx4 v65, s[36:39], s44 offen offset:1024 sc1 lds\n\t"
      "v_mfma_f32_16x16x32_bf16 v[84:87], v[108:111], v[64:67], 0\n\t"
      "s_add_u32 s57, s61, 310378496\n\t"
      "v_cndmask_b32_e64 v112, v112, v116, s[72:73]\n\t"
      "v_exp_f32_e64 v56, v112\n\t"
      "v_mfma_f32_16x16x32_bf16 v[48:51], v[108:111], v[88:91], 0\n\t"
      "s_add_u32 s43, s43, 0x8000\n\t"
      "v_cndmask_b32_e64 v113, v113, v117, s[72:73]\n\t"
      "v_cndmask_b32_e64 v114, v114, v118, s[72:73]\n\t"
      "v_exp_f32_e64 v58, v114\n\t"
      "v_cndmask_b32_e64 v115, v115, v119, s[72:73]\n\t"
      "v_exp_f32 v57, v113\n\t"
      "v_exp_f32 v59, v115\n\t"
      "v_cvt_pk_bf16_f32 v96, v56, v57\n\t"
      "v_cvt_pk_bf16_f32 v97, v58, v59\n\t"
      "v_max3_f32 v82, |v112|, |v113|, |v114|\n\t"
      "v_mov_b32_dpp v98, v96 quad_perm:[2,3,0,1] row_mask:0xf bank_mask:0xf\n\t"
      "v_mov_b32_dpp v99, v97 quad_perm:[2,3,0,1] row_mask:0xf bank_mask:0xf\n\t"
      "v_smfmac_f32_16x16x64_bf16 v[52:55], v[8:11], v[208:215], v122\n\t"
      "v_max3_f32 v83, |v115|, |v120|, |v121|\n\t"
      "v_mfma_f32_16x16x32_bf16 v[48:51], v[96:99], v[88:91], v[48:51]\n\t"
      "v_max_f32_e64 v82, v82, v83\n\t"
      "v_cmp_nge_f32 vcc, 0x42000000, v82\n\t"
      "v_mfma_f32_16x16x32_bf16 v[84:87], v[96:99], v[68:71], v[84:87]\n\t"
      "buffer_load_dwordx4 v65, s[36:39], s44 offen sc1 lds\n\t"
      "v_smfmac_f32_16x16x64_bf16 v[52:55], v[20:23], v[232:239], v122\n\t"
      "s_add_u32 s68, s68, 0x8000\n\t"
      "s_cbranch_vccnz LMX_1%=\n\t"
      "v_rcp_f32 v49, v49\n\t"
      "v_rcp_f32_e64 v48, v48\n\t"
      "v_smfmac_f32_16x16x64_bf16 v[52:55], v[16:19], v[224:231], v122\n\t"
      "v_fma_f32 v85, v85, v49, s96\n\t"
      "v_fma_f32 v84, v84, v48, s96\n\t"
      "v_smfmac_f32_16x16x64_bf16 v[52:55], v[28:31], v[248:255], v122\n\t"
      "buffer_atomic_add v84, v123, s[36:39], s97 offen\n\t"
      "buffer_atomic_add v85, v123, s[36:39], s98 offen\n\t"
      "v_smfmac_f32_16x16x64_bf16 v[52:55], v[24:27], v[240:247], v122\n\t"
      "v_mfma_f32_16x16x32_bf16 v[92:95], v[96:99], v[72:75], 0\n\t"
      "s_nop 3\n\t"
      "v_add_f32 v53, v53, v55\n\t"
      "v_add_f32_e64 v52, v52, v54\n\t"
      "v_cvt_pk_bf16_f32 v66, v52, v53\n\t"
      "s_add_u32 s62, s61, 0x83400000\n\t"
      "v_mov_b32_e64 v126, s62\n\t"
      "v_mfma_f32_16x16x32_bf16 v[92:95], v[108:111], v[66:69], v[92:95]\n\t"
      "s_add_u32 s97, s97, 0x8000\n\t"
      "s_nop 6\n\t"
      "v_fma_f32 v95, v95, v49, s96\n\t"
      "buffer_atomic_add v95, v123, s[36:39], s23 offen\n\t"
      "v_fma_f32 v94, v94, v48, s96\n\t"
      "buffer_atomic_add v94, v123, s[36:39], s69 offen\n\t"
      "LWIN_1%=:\n\t"
      "v_add_u32 v100, s55, v127\n\t"
      "ds_read_b128 v[60:63], v100 offset:57344\n\t"
      "s_cmp_eq_u32 s56, 0\n\t"
      "s_cbranch_scc0 LSLOW_1%=\n\t"
      "LWIN1_1%=:\n\t"
      "buffer_load_dwordx2 v[120:121], off, s[36:39], s50 sc1\n\t"
      "s_mov_b32 s64, s50\n\t"
      "s_bitcmp1_b64 s[84:85], s40\n\t"
      "s_cbranch_scc1 LEARLY_1%=\n\t"
      "LROLE_1%=:\n\t"
      "v_add_u32 v52, 67584, v127\n\t"
      "ds_read_b128 v[88:91], v52\n\t"
      "ds_read_b128 v[92:95], v52 offset:1024\n\t"
      "ds_read_b128 v[32:35], v52 offset:4096\n\t"
      "ds_read_b128 v[36:39], v52 offset:5120\n\t"
      "ds_read_b128 v[40:43], v52 offset:8192\n\t"
      "ds_read_b128 v[44:47], v52 offset:9216\n\t"
      "v_mov_b64 v[48:49], 0\n\t"
      "v_mov_b64 v[50:51], 0\n\t"
      "v_add_u32 v104, s71, v100\n\t"
      "ds_read_b128 v[76:79], v104\n\t"
      "ds_read_b128 v[80:83], v104 offset:1024\n\t"
      "ds_read_b128 v[112:115], v100\n\t"
      "ds_read_b128 v[116:119], v100 offset:1024\n\t"
      "v_xor_b32 v101, 2048, v104\n\t"
      "v_xor_b32 v102, 4096, v104\n\t"
      "v_xor_b32 v103, 6144, v104\n\t"
      "s_waitcnt lgkmcnt(8)\n\t"
      "v_smfmac_f32_16x16x64_bf16 v[48:51], v[0:3], v[88:95], v122\n\t"
      "ds_read_b128 v[88:91], v52 offset:12288\n\t"
      "ds_read_b128 v[92:95], v52 offset:13312\n\t"
      "s_add_u32 m0, s55, s71\n\t"
      "s_sub_u32 s71, 204800, s71\n\t"
      "s_waitcnt lgkmcnt(8)\n\t"
      "v_smfmac_f32_16x16x64_bf16 v[48:51], v[4:7], v[32:39], v122\n\t"
      "ds_read_b128 v[16:19], v101\n\t"
      "ds_read_b128 v[20:23], v101 offset:1024\n\t"
      "s_add_u32 s40, s40, 1\n\t"
      "s_cmp_lt_u32 s40, 63\n\t"
      "s_waitcnt lgkmcnt(8)\n\t"
      "v_smfmac_f32_16x16x64_bf16 v[48:51], v[8:11], v[40:47], v122\n\t"
      "ds_read_b128 v[24:27], v100 offset:8192\n\t"
      "ds_read_b128 v[28:31], v100 offset:9216\n\t"
      "s_cselect_b32 s61, 786432, 0\n\t"
      "s_cselect_b32 s60, s58, 0\n\t"
      "s_waitcnt lgkmcnt(4)\n\t"
      "v_smfmac_f32_16x16x64_bf16 v[48:51], v[12:15], v[88:95], v122\n\t"
      "s_add_u32 s44, s44, s61\n\t"
      "s_cmp_lt_u32 s40, 62\n\t"
      "ds_read_b128 v[0:3], v102\n\t"
      "ds_read_b128 v[4:7], v102 offset:1024\n\t"
      "ds_read_b128 v[8:11], v100 offset:16384\n\t"
      "ds_read_b128 v[12:15], v100 offset:17408\n\t"
      "s_cselect_b32 s61, 786432, 0\n\t"
      "s_add_u32 s46, s46, s61\n\t"
      "s_add_u32 s61, s40, 3\n\t"
      "s_and_b32 s62, s61, 3\n\t"
      "s_cselect_b32 s62, 16, 2000\n\t"
      "s_cmp_lt_u32 s61, 64\n\t"
      "s_cselect_b32 s62, s62, 0\n\t"
      "s_add_u32 s50, s50, s62\n\t"
      "s_mov_b32 s28, s49\n\t"
      "s_add_u32 s49, s49, 65536\n\t"
      "s_cmp_eq_u32 s40, 64\n\t"
      "v_add_f32 v48, v48, v50\n\t"
      "v_add_f32 v49, v49, v51\n\t"
      "s_cbranch_scc1 LFIN_1%=\n\t"
      "v_mfma_f32_16x16x32_bf16 v[32:35], v[76:79], v[112:115], 0\n\t"
      "s_mov_b32 s63, 0x2000\n\t"
      "v_mfma_f32_16x16x32_bf16 v[36:39], v[80:83], v[112:115], 0\n\t"
      "v_mfma_f32_16x16x32_bf16 v[40:43], v[76:79], v[116:119], 0\n\t"
      "v_mfma_f32_16x16x32_bf16 v[44:47], v[80:83], v[116:119], 0\n\t"
      "ds_read_b128 v[76:79], v103\n\t"
      "ds_read_b128 v[80:83], v103 offset:1024\n\t"
      "ds_read_b128 v[112:115], v100 offset:24576\n\t"
      "ds_read_b128 v[116:119], v100 offset:25600\n\t"
      "s_waitcnt lgkmcnt(8)\n\t"
      "v_mfma_f32_16x16x32_bf16 v[32:35], v[16:19], v[24:27], v[32:35]\n\t"
      "v_mfma_f32_16x16x32_bf16 v[36:39], v[20:23], v[24:27], v[36:39]\n\t"
      "v_mfma_f32_16x16x32_bf16 v[40:43], v[16:19], v[28:31], v[40:43]\n\t"
      "v_mfma_f32_16x16x32_bf16 v[44:47], v[20:23], v[28:31], v[44:47]\n\t"
      "LGATE_1%=:\n\t"
      "LBAR_1%=:\n\t"
      "s_cmp_gt_u32 s40, 1\n\t"
      "s_barrier\n\t"
      "s_cbranch_scc1 LPOLL_1%=\n\t"
      "s_branch LFIRST_1%=\n\t"
      ".p2align 6\n\t.fill 16, 4, 0xbf800000\n\t"
      "LPOLL_2%=:\n\t"
      "buffer_load_dwordx4 v[84:87], v60, s[36:39], s43 offen sc1\n\t"
      "buffer_load_dwordx4 v[88:91], v60, s[36:39], s43 offen offset:1024 sc1\n\t"
      "s_waitcnt lgkmcnt(8)\n\t"
      "v_mfma_f32_16x16x32_bf16 v[32:35], v[0:3], v[8:11], v[32:35]\n\t"
      "v_mfma_f32_16x16x32_bf16 v[36:39], v[4:7], v[8:11], v[36:39]\n\t"
      "v_mfma_f32_16x16x32_bf16 v[40:43], v[0:3], v[12:15], v[40:43]\n\t"
      "v_mfma_f32_16x16x32_bf16 v[44:47], v[4:7], v[12:15], v[44:47]\n\t"
      "buffer_load_dwordx4 v[92:95], v60, s[36:39], s68 offen sc1\n\t"
      "buffer_load_dwordx4 v[96:99], v60, s[36:39], s68 offen offset:1024 sc1\n\t"
      "v_mov_b64 v[48:49], 0\n\t"
      "v_mov_b64 v[50:51], 0\n\t"
      "v_mov_b64 v[52:53], 0\n\t"
      "v_mov_b64 v[54:55], 0\n\t"
      "s_xor_b32 s69, s97, 1024\n\t"
      "s_xor_b32 s98, s97, 512\n\t"
      "s_xor_b32 s23, s97, 1536\n\t"
      "s_waitcnt lgkmcnt(4)\n\t"
      "v_mfma_f32_16x16x32_bf16 v[32:35], v[16:19], v[24:27], v[32:35]\n\t"
      "v_mfma_f32_16x16x32_bf16 v[36:39], v[20:23], v[24:27], v[36:39]\n\t"
      "v_mfma_f32_16x16x32_bf16 v[40:43], v[16:19], v[28:31], v[40:43]\n\t"
      "v_mfma_f32_16x16x32_bf16 v[44:47], v[20:23], v[28:31], v[44:47]\n\t"
      "s_waitcnt lgkmcnt(0)\n\t"
      "v_mfma_f32_16x16x32_bf16 v[32:35], v[76:79], v[112:115], v[32:35]\n\t"
      "v_mfma_f32_16x16x32_bf16 v[36:39], v[80:83], v[112:115], v[36:39]\n\t"
      "v_mfma_f32_16x16x32_bf16 v[40:43], v[76:79], v[116:119], v[40:43]\n\t"
      "v_mfma_f32_16x16x32_bf16 v[44:47], v[80:83], v[116:119], v[44:47]\n\t"
      "s_mov_b64 exec, 1\n\t"
      "ds_write_b64 v109, v[110:111] offset:55392\n\t"
      "s_mov_b64 exec, -1\n\t"
      "s_nop 4\n\t"
      "v_cvt_pk_bf16_f32 v68, v32, v33\n\t"
      "v_cvt_pk_bf16_f32 v69, v34, v35\n\t"
      "v_cvt_pk_bf16_f32 v70, v36, v37\n\t"
      "v_cvt_pk_bf16_f32 v71, v38, v39\n\t"
      "v_cvt_pk_bf16_f32 v74, v40, v41\n\t"
      "v_cvt_pk_bf16_f32 v75, v42, v43\n\t"
      "v_cvt_pk_bf16_f32 v72, v44, v45\n\t"
      "v_cvt_pk_bf16_f32 v73, v46, v47\n\t"
      "v_mov_b64 v[32:33], 0\n\t"
      "v_mov_b64 v[34:35], 0\n\t"
      "v_mov_b64 v[36:37], 0\n\t"
      "v_mov_b64 v[38:39], 0\n\t"
      "v_mov_b64 v[40:41], 0\n\t"
      "v_mov_b64 v[42:43], 0\n\t"
      "v_mov_b64 v[44:45], 0\n\t"
      "v_mov_b64 v[46:47], 0\n\t"
      "LGO_2%=:\n\t"
      "s_nop 0\n\t"
      "s_nop 0\n\t"
      "s_nop 0\n\t"
      "s_nop 0\n\t"
      "s_nop 0\n\t"
      "s_waitcnt vmcnt(3)\n\t"
      "v_pk_fma_f32 v[14:15], v[86:87], s[56:57], v[126:127] op_sel:[0,1,0] op_sel_hi:[1,1,0]\n\t"
      "v_pk_fma_f32 v[12:13], v[84:85], s[56:57], v[126:127] op_sel:[0,1,0] op_sel_hi:[1,1,0]\n\t"
      "v_cvt_pk_bf16_f32 v8, v12, v13\n\t"
      "v_cvt_pk_bf16_f32 v9, v14, v15\n\t"
      "s_nop 0\n\t"
      "ds_write_b64 v127, v[8:9] offset:43008\n\t"
      "s_waitcnt vmcnt(2)\n\t"
      "v_pk_fma_f32 v[0:1], v[88:89], s[56:57], v[126:127] op_sel:[0,1,0] op_sel_hi:[1,1,0]\n\t"
      "v_pk_fma_f32 v[2:3], v[90:91], s[56:57], v[126:127] op_sel:[0,1,0] op_sel_hi:[1,1,0]\n\t"
      "v_cvt_pk_bf16_f32 v10, v0, v1\n\t"
      "v_cvt_pk_bf16_f32 v11, v2, v3\n\t"
      "ds_write_b64 v127, v[10:11] offset:43016\n\t"
      "v_bitop3_b32 v64, v84, v85, v86 bitop3:0x80\n\t"
      "v_mfma_f32_16x16x32_bf16 v[56:59], v[8:11], v[8:11], 0\n\t"
      "v_bitop3_b32 v64, v64, v87, v88 bitop3:0x80\n\t"
      "v_bitop3_b32 v64, v64, v89, v90 bitop3:0x80\n\t"
      "v_bitop3_b32 v64, v64, v91, s58 bitop3:0x80\n\t"
      "v_cmp_eq_u32_e64 vcc, 0, v64\n\t"
      "v_smfmac_f32_16x16x64_bf16 v[36:39], v[8:11], a[80:87], v122\n\t"
      "s_nop 0\n\t"
      "v_bitop3_b32 v120, v120, v121, s53 bitop3:0x7e\n\t"
      "v_smfmac_f32_16x16x64_bf16 v[44:47], v[8:11], a[208:215], v122\n\t"
      "s_cbranch_vccnz LR0_2%=\n\t"
      "LC0_2%=:\n\t"
      "s_waitcnt lgkmcnt(0)\n\t"
      "s_barrier\n\t"
      "ds_read_b128 v[12:15], v127 offset:44032\n\t"
      "v_smfmac_f32_16x16x64_bf16 v[52:55], v[8:11], v[208:215], v122\n\t"
      "ds_read_b128 v[0:3], v127 offset:40960\n\t"
      "v_smfmac_f32_16x16x64_bf16 v[48:51], v[8:11], v[144:151], v122\n\t"
      "ds_read_b128 v[4:7], v127 offset:41984\n\t"
      "v_smfmac_f32_16x16x64_bf16 v[32:35], v[8:11], a[16:23], v122\n\t"
      "ds_read_b128 v[84:87], v127 offset:36864\n\t"
      "v_smfmac_f32_16x16x64_bf16 v[40:43], v[8:11], a[144:151], v122\n\t"
      "s_waitcnt lgkmcnt(3)\n\t"
      "v_smfmac_f32_16x16x64_bf16 v[32:35], v[12:15], a[24:31], v122\n\t"
      "ds_read_b128 v[80:83], v127 offset:35840\n\t"
      "v_smfmac_f32_16x16x64_bf16 v[40:43], v[12:15], a[152:159], v122\n\t"
      "s_waitcnt vmcnt(1)\n\t"
      "v_fma_f32 v31, v95, s57, v126\n\t"
      "v_fma_f32 v28, v92, s57, v126\n\t"
      "v_smfmac_f32_16x16x64_bf16 v[36:39], v[12:15], a[88:95], v122\n\t"
      "v_fma_f32 v30, v94, s57, v126\n\t"
      "v_cvt_pk_bf16_f32 v25, v30, v31\n\t"
      "v_bitop3_b32 v64, v92, v93, v94 bitop3:0x80\n\t"
      "v_smfmac_f32_16x16x64_bf16 v[44:47], v[12:15], a[216:223], v122\n\t"
      "s_waitcnt lgkmcnt(2)\n\t"
      "v_fma_f32 v29, v93, s57, v126\n\t"
      "v_cvt_pk_bf16_f32 v24, v28, v29\n\t"
      "v_smfmac_f32_16x16x64_bf16 v[36:39], v[0:3], a[64:71], v122\n\t"
      "s_waitcnt vmcnt(0)\n\t"
      "ds_write_b64 v127, v[24:25] offset:47104\n\t"
      "v_fma_f32 v19, v99, s57, v126\n\t"
      "v_fma_f32 v18, v98, s57, v126\n\t"
      "v_cvt_pk_bf16_f32 v27, v18, v19\n\t"
      "v_fma_f32 v17, v97, s57, v126\n\t"
      "v_fma_f32 v16, v96, s57, v126\n\t"
      "v_cvt_pk_bf16_f32 v26, v16, v17\n\t"
      "v_smfmac_f32_16x16x64_bf16 v[44:47], v[0:3], a[192:199], v122\n\t"
      "ds_write_b64 v127, v[26:27] offset:47112\n\t"
      "v_smfmac_f32_16x16x64_bf16 v[40:43], v[0:3], a[128:135], v122\n\t"
      "ds_read_b128 v[88:91], v127 offset:39936\n\t"
      "v_bitop3_b32 v64, v64, v95, v96 bitop3:0x80\n\t"
      "v_bitop3_b32 v64, v64, v97, v98 bitop3:0x80\n\t"
      "v_smfmac_f32_16x16x64_bf16 v[40:43], v[4:7], a[136:143], v122\n\t"
      "v_bitop3_b32 v64, v64, v99, s60 bitop3:0x80\n\t"
      "v_smfmac_f32_16x16x64_bf16 v[36:39], v[4:7], a[72:79], v122\n\t"
      "v_cmp_eq_u32_e64 vcc, 0, v64\n\t"
      "v_smfmac_f32_16x16x64_bf16 v[44:47], v[4:7], a[200:207], v122\n\t"
      "s_waitcnt lgkmcnt(1)\n\t"
      "v_smfmac_f32_16x16x64_bf16 v[32:35], v[0:3], a[0:7], v122\n\t"
      "s_cbranch_vccnz LR1_2%=\n\t"
      "LC1_2%=:\n\t"
      "v_smfmac_f32_16x16x64_bf16 v[40:43], v[24:27], a[176:183], v122\n\t"
      "s_barrier\n\t"
      "v_mfma_f32_16x16x32_bf16 v[56:59], v[24:27], v[24:27], v[56:59]\n\t"
      "ds_read_b128 v[28:31], v127 offset:48128\n\t"
      "s_nop 0\n\t"
      "v_smfmac_f32_16x16x64_bf16 v[32:35], v[4:7], a[8:15], v122\n\t"
      "ds_read_b128 v[16:19], v127 offset:45056\n\t"
      "v_smfmac_f32_16x16x64_bf16 v[44:47], v[24:27], a[240:247], v122\n\t"
      "ds_read_b128 v[20:23], v127 offset:46080\n\t"
      "v_max3_f32 v56, v56, v57, v58\n\t"
      "v_smfmac_f32_16x16x64_bf16 v[36:39], v[24:27], a[112:119], v122\n\t"
      "ds_read_b128 v[76:79], v127 offset:32768\n\t"
      "v_max_f32_e64 v56, v56, v59\n\t"
      "v_smfmac_f32_16x16x64_bf16 v[32:35], v[24:27], a[48:55], v122\n\t"
      "s_waitcnt lgkmcnt(3)\n\t"
      "v_readfirstlane_b32 s56, v120\n\t"
      "v_smfmac_f32_16x16x64_bf16 v[32:35], v[28:31], a[56:63], v122\n\t"
      "s_waitcnt lgkmcnt(2)\n\t"
      "v_max_f32_dpp v56, v56, v56 quad_perm:[1,0,3,2] row_mask:0xf bank_mask:0xf\n\t"
      "v_smfmac_f32_16x16x64_bf16 v[32:35], v[16:19], a[32:39], v122\n\t"
      "s_waitcnt lgkmcnt(1)\n\t"
      "v_max_f32_dpp v56, v56, v56 quad_perm:[2,3,0,1] row_mask:0xf bank_mask:0xf\n\t"
      "v_smfmac_f32_16x16x64_bf16 v[32:35], v[20:23], a[40:47], v122\n\t"
      "s_mov_b32 exec_lo, 0x100001\n\t"
      "s_mov_b32 exec_hi, 0x10000100\n\t"
      "v_smfmac_f32_16x16x64_bf16 v[36:39], v[28:31], a[120:127], v122\n\t"
      "ds_max_i32 v109, v56 offset:55392\n\t"
      "v_smfmac_f32_16x16x64_bf16 v[40:43], v[28:31], a[184:191], v122\n\t"
      "s_mov_b64 exec, -1\n\t"
      "v_add_f32 v33, v33, v35\n\t"
      "v_smfmac_f32_16x16x64_bf16 v[36:39], v[16:19], a[96:103], v122\n\t"
      "ds_read_b128 v[60:63], v127 offset:37888\n\t"
      "v_mul_f32_e64 v33, s59, v33\n\t"
      "v_smfmac_f32_16x16x64_bf16 v[36:39], v[20:23], a[104:111], v122\n\t"
      "v_add_f32_e64 v32, v32, v34\n\t"
      "v_smfmac_f32_16x16x64_bf16 v[44:47], v[28:31], a[248:255], v122\n\t"
      "ds_read_b128 v[56:59], v127 offset:38912\n\t"
      "v_mul_f32_e64 v32, s59, v32\n\t"
      "v_smfmac_f32_16x16x64_bf16 v[44:47], v[16:19], a[224:231], v122\n\t"
      "v_add_f32 v36, v36, v38\n\t"
      "v_add_f32 v37, v37, v39\n\t"
      "v_mul_f32_e64 v37, s59, v37\n\t"
      "v_smfmac_f32_16x16x64_bf16 v[40:43], v[16:19], a[160:167], v122\n\t"
      "ds_read_b128 v[92:95], v127 offset:34816\n\t"
      "v_cvt_pk_bf16_f32 v112, v32, v33\n\t"
      "v_smfmac_f32_16x16x64_bf16 v[44:47], v[20:23], a[232:239], v122\n\t"
      "ds_read_b128 v[96:99], v127 offset:33792\n\t"
      "v_mul_f32_e64 v36, s59, v36\n\t"
      "v_cvt_pk_bf16_f32 v113, v36, v37\n\t"
      "v_smfmac_f32_16x16x64_bf16 v[40:43], v[20:23], a[168:175], v122\n\t"
      "ds_write_b64 v125, v[112:113]\n\t"
      "v_smfmac_f32_16x16x64_bf16 v[48:51], v[12:15], v[152:159], v122\n\t"
      "s_nop 0\n\t"
      "v_add_f32 v45, v45, v47\n\t"
      "v_add_f32_e64 v44, v44, v46\n\t"
      "v_cvt_pk_bf16_f32 v121, v44, v45\n\t"
      "v_add_f32 v41, v41, v43\n\t"
      "v_add_f32 v40, v40, v42\n\t"
      "v_cvt_pk_bf16_f32 v120, v40, v41\n\t"
      "v_smfmac_f32_16x16x64_bf16 v[48:51], v[0:3], v[128:135], v122\n\t"
      "s_mov_b32 s58, 0x10000000\n\t"
      "ds_write_b64 v125, v[120:121] offset:2080\n\t"
      "v_smfmac_f32_16x16x64_bf16 v[48:51], v[4:7], v[136:143], v122\n\t"
      "s_waitcnt lgkmcnt(1)\n\t"
      "s_barrier\n\t"
      "ds_read_b64_tr_b16 v[36:37], v124 offset:50192\n\t"
      "v_smfmac_f32_16x16x64_bf16 v[48:51], v[24:27], v[176:183], v122\n\t"
      "ds_read_b32 v108, v109 offset:55392\n\t"
      "ds_read_b64_tr_b16 v[32:33], v124 offset:49152\n\t"
      "v_smfmac_f32_16x16x64_bf16 v[52:55], v[12:15], v[216:223], v122\n\t"
      "ds_read_b64_tr_b16 v[34:35], v124 offset:49672\n\t"
      "s_waitcnt lgkmcnt(4)\n\t"
      "ds_read_b64_tr_b16 v[38:39], v124 offset:50712\n\t"
      "v_smfmac_f32_16x16x64_bf16 v[48:51], v[28:31], v[184:191], v122\n\t"
      "s_barrier\n\t"
      "ds_read_b64_tr_b16 v[46:47], v124 offset:52792\n\t"
      "v_smfmac_f32_16x16x64_bf16 v[48:51], v[16:19], v[160:167], v122\n\t"
      "s_waitcnt lgkmcnt(4)\n\t"
      "ds_read_b64_tr_b16 v[44:45], v124 offset:52272\n\t"
      "v_readfirstlane_b32 s61, v108\n\t"
      "v_smfmac_f32_16x16x64_bf16 v[48:51], v[20:23], v[168:175], v122\n\t"
      "s_waitcnt lgkmcnt(3)\n\t"
      "ds_read_b64_tr_b16 v[42:43], v124 offset:51752\n\t"
      "ds_read_b64_tr_b16 v[40:41], v124 offset:51232\n\t"
      "s_waitcnt lgkmcnt(4)\n\t"
      "v_mfma_f32_16x16x32_bf16 v[112:115], v[76:79], v[32:35], 0\n\t"
      "s_lshr_b32 s61, s61, 23\n\t"
      "s_add_u32 s61, s61, s67\n\t"
      "v_mov_b32_dpp v102, v34 quad_perm:[2,3,0,1] row_mask:0xf bank_mask:0xf\n\t"
      "v_mov_b32_dpp v106, v38 quad_perm:[2,3,0,1] row_mask:0xf bank_mask:0xf\n\t"
      "s_max_u32 s61, s61, s66\n\t"
      "v_mfma_f32_16x16x32_bf16 v[116:119], v[80:83], v[32:35], 0\n\t"
      "s_lshr_b32 s61, s61, 1\n\t"
      "v_add_f32_e64 v48, v48, v50\n\t"
      "v_mov_b32_dpp v100, v32 quad_perm:[2,3,0,1] row_mask:0xf bank_mask:0xf\n\t"
      "v_mfma_f32_16x16x32_bf16 v[116:119], v[88:91], v[36:39], v[116:119]\n\t"
      "s_min_u32 s61, s61, 200\n\t"
      "v_mov_b32_dpp v107, v39 quad_perm:[2,3,0,1] row_mask:0xf bank_mask:0xf\n\t"
      "s_waitcnt lgkmcnt(0)\n\t"
      "v_mfma_f32_16x16x32_bf16 v[76:79], v[40:43], v[32:35], 0\n\t"
      "buffer_load_dwordx4 v127, s[36:39], s46 offen sc1 lds\n\t"
      "s_lshl_b32 s61, s61, 23\n\t"
      "v_mfma_f32_16x16x32_bf16 v[76:79], v[44:47], v[36:39], v[76:79]\n\t"
      "s_add_u32 s65, s61, 33554432\n\t"
      "v_mov_b32_dpp v104, v36 quad_perm:[2,3,0,1] row_mask:0xf bank_mask:0xf\n\t"
      "v_mov_b32_dpp v103, v35 quad_perm:[2,3,0,1] row_mask:0xf bank_mask:0xf\n\t"
      "v_mov_b32_dpp v101, v33 quad_perm:[2,3,0,1] row_mask:0xf bank_mask:0xf\n\t"
      "v_mfma_f32_16x16x32_bf16 v[112:115], v[84:87], v[36:39], v[112:115]\n\t"
      "s_pack_hh_b32_b16 s75, s65, s65\n\t"
      "v_add_f32 v49, v49, v51\n\t"
      "v_cvt_pk_bf16_f32 v64, v48, v49\n\t"
      "v_cndmask_b32_e64 v120, v76, v78, s[72:73]\n\t"
      "v_cndmask_b32_e64 v121, v77, v79, s[72:73]\n\t"
      "v_mov_b32_dpp v105, v37 quad_perm:[2,3,0,1] row_mask:0xf bank_mask:0xf\n\t"
      "v_mfma_f32_16x16x32_bf16 v[112:115], v[92:95], v[100:103], v[112:115]\n\t"
      "buffer_load_dwordx4 v127, s[36:39], s46 offen offset:1024 sc1 lds\n\t"
      "v_mfma_f32_16x16x32_bf16 v[116:119], v[96:99], v[100:103], v[116:119]\n\t"
      "s_pack_hh_b32_b16 s74, s65, s65\n\t"
      "v_add_f32_dpp v121, v121, v121 quad_perm:[2,3,0,1] row_mask:0xf bank_mask:0xf\n\t"
      "v_add_f32_dpp v120, v120, v120 quad_perm:[2,3,0,1] row_mask:0xf bank_mask:0xf\n\t"
      "v_mfma_f32_16x16x32_bf16 v[112:115], v[56:59], v[104:107], v[112:115]\n\t"
      "v_exp_f32 v81, v121\n\t"
      "v_mfma_f32_16x16x32_bf16 v[116:119], v[60:63], v[104:107], v[116:119]\n\t"
      "s_mov_b32 m0, s45\n\t"
      "v_mov_b64 v[90:91], s[74:75]\n\t"
      "v_exp_f32 v80, v120\n\t"
      "v_mov_b64 v[88:89], s[74:75]\n\t"
      "v_cvt_pk_bf16_f32 v108, v80, v81\n\t"
      "v_smfmac_f32_16x16x64_bf16 v[52:55], v[0:3], v[192:199], v122\n\t"
      "buffer_load_dwordx4 v65, s[36:39], s44 offen offset:1024 sc1 lds\n\t"
      "v_mfma_f32_16x16x32_bf16 v[84:87], v[108:111], v[64:67], 0\n\t"
      "s_add_u32 s57, s61, 310378496\n\t"
      "v_cndmask_b32_e64 v112, v112, v116, s[72:73]\n\t"
      "v_exp_f32_e64 v56, v112\n\t"
      "v_mfma_f32_16x16x32_bf16 v[48:51], v[108:111], v[88:91], 0\n\t"
      "s_add_u32 s43, s43, 0x8000\n\t"
      "v_cndmask_b32_e64 v113, v113, v117, s[72:73]\n\t"
      "v_cndmask_b32_e64 v114, v114, v118, s[72:73]\n\t"
      "v_exp_f32_e64 v58, v114\n\t"
      "v_cndmask_b32_e64 v115, v115, v119, s[72:73]\n\t"
      "v_exp_f32 v57, v113\n\t"
      "v_exp_f32 v59, v115\n\t"
      "v_cvt_pk_bf16_f32 v96, v56, v57\n\t"
      "v_cvt_pk_bf16_f32 v97, v58, v59\n\t"
      "v_max3_f32 v82, |v112|, |v113|, |v114|\n\t"
      "v_mov_b32_dpp v98, v96 quad_perm:[2,3,0,1] row_mask:0xf bank_mask:0xf\n\t"
      "v_mov_b32_dpp v99, v97 quad_perm:[2,3,0,1] row_mask:0xf bank_mask:0xf\n\t"
      "v_smfmac_f32_16x16x64_bf16 v[52:55], v[4:7], v[200:207], v122\n\t"
      "v_max3_f32 v83, |v115|, |v120|, |v121|\n\t"
      "v_mfma_f32_16x16x32_bf16 v[48:51], v[96:99], v[88:91], v[48:51]\n\t"
      "v_max_f32_e64 v82, v82, v83\n\t"
      "v_cmp_nge_f32 vcc, 0x42000000, v82\n\t"
      "v_mfma_f32_16x16x32_bf16 v[84:87], v[96:99], v[68:71], v[84:87]\n\t"
      "buffer_load_dwordx4 v65, s[36:39], s44 offen sc1 lds\n\t"
      "v_smfmac_f32_16x16x64_bf16 v[52:55], v[24:27], v[240:247], v122\n\t"
      "s_add_u32 s68, s68, 0x8000\n\t"
      "s_cbranch_vccnz LMX_2%=\n\t"
      "v_rcp_f32 v49, v49\n\t"
      "v_rcp_f32_e64 v48, v48\n\t"
      "v_smfmac_f32_16x16x64_bf16 v[52:55], v[28:31], v[248:255], v122\n\t"
      "v_fma_f32 v85, v85, v49, s96\n\t"
      "v_fma_f32 v84, v84, v48, s96\n\t"
      "v_smfmac_f32_16x16x64_bf16 v[52:55], v[16:19], v[224:231], v122\n\t"
      "buffer_atomic_add v84, v123, s[36:39], s97 offen\n\t"
      "buffer_atomic_add v85, v123, s[36:39], s98 offen\n\t"
      "v_smfmac_f32_16x16x64_bf16 v[52:55], v[20:23], v[232:239], v122\n\t"
      "v_mfma_f32_16x16x32_bf16 v[92:95], v[96:99], v[72:75], 0\n\t"
      "s_nop 3\n\t"
      "v_add_f32 v53, v53, v55\n\t"
      "v_add_f32_e64 v52, v52, v54\n\t"
      "v_cvt_pk_bf16_f32 v66, v52, v53\n\t"
      "s_add_u32 s62, s61, 0x83400000\n\t"
      "v_mov_b32_e64 v126, s62\n\t"
      "v_mfma_f32_16x16x32_bf16 v[92:95], v[108:111], v[66:69], v[92:95]\n\t"
      "s_add_u32 s97, s97, 0x8000\n\t"
      "s_nop 6\n\t"
      "v_fma_f32 v95, v95, v49, s96\n\t"
      "buffer_atomic_add v95, v123, s[36:39], s23 offen\n\t"
      "v_fma_f32 v94, v94, v48, s96\n\t"
      "buffer_atomic_add v94, v123, s[36:39], s69 offen\n\t"
      "LWIN_2%=:\n\t"
      "v_add_u32 v100, s55, v127\n\t"
      "ds_read_b128 v[60:63], v100 offset:57344\n\t"
      "s_cmp_eq_u32 s56, 0\n\t"
      "s_cbranch_scc0 LSLOW_2%=\n\t"
      "LWIN1_2%=:\n\t"
      "buffer_load_dwordx2 v[120:121], off, s[36:39], s50 sc1\n\t"
      "s_mov_b32 s64, s50\n\t"
      "s_bitcmp1_b64 s[84:85], s40\n\t"
      "s_cbranch_scc1 LEARLY_2%=\n\t"
      "LROLE_2%=:\n\t"
      "v_add_u32 v52, 65536, v127\n\t"
      "ds_read_b128 v[88:91], v52 offset:16384\n\t"
      "ds_read_b128 v[92:95], v52 offset:17408\n\t"
      "ds_read_b128 v[32:35], v52 offset:20480\n\t"
      "ds_read_b128 v[36:39], v52 offset:21504\n\t"
      "s_cmp_eq_u32 s42, 3\n\t"
      "s_waitcnt lgkmcnt(4)\n\t"
      "s_cbranch_scc1 LWK1_2%=\n\t"
      "s_cmp_eq_u32 s42, 2\n\t"
      "s_cbranch_scc0 LW4_2%=\n\t"
      "buffer_store_dwordx4 v[40:43], v61, s[36:39], s48 offen sc1\n\t"
      "LW4_2%=:\n\t"
      "s_add_u32 s48, s48, 65536\n\t"
      "s_nop 0\n\t"
      "ds_read_b128 v[40:43], v52 offset:24576\n\t"
      "ds_read_b128 v[44:47], v52 offset:25600\n\t"
      "v_mov_b64 v[48:49], 0\n\t"
      "v_mov_b64 v[50:51], 0\n\t"
      "s_memrealtime s[92:93]\n\t"
      "s_waitcnt lgkmcnt(0)\n\t"
      "s_sub_u32 s61, s92, s95\n\t"
      "s_mov_b32 s95, s92\n\t"
      "s_min_u32 s61, s61, 190\n\t"
      "s_add_u32 s61, s61, s92\n\t"
      "s_pack_ll_b32_b16 s61, s61, s40\n\t"
      "v_mov_b32 v63, s61\n\t"
      "s_mov_b64 exec, 1\n\t"
      "buffer_store_dword v63, off, s[36:39], s51 sc1\n\t"
      "s_mov_b64 exec, -1\n\t"
      "v_add_u32 v104, s71, v100\n\t"
      "ds_read_b128 v[76:79], v104\n\t"
      "ds_read_b128 v[80:83], v104 offset:1024\n\t"
      "ds_read_b128 v[112:115], v100\n\t"
      "ds_read_b128 v[116:119], v100 offset:1024\n\t"
      "v_xor_b32 v101, 2048, v104\n\t"
      "v_xor_b32 v102, 4096, v104\n\t"
      "v_xor_b32 v103, 6144, v104\n\t"
      "v_smfmac_f32_16x16x64_bf16 v[48:51], v[16:19], v[88:95], v122\n\t"
      "ds_read_b128 v[88:91], v52 offset:28672\n\t"
      "ds_read_b128 v[92:95], v52 offset:29696\n\t"
      "s_add_u32 m0, s55, s71\n\t"
      "s_sub_u32 s71, 204800, s71\n\t"
      "v_smfmac_f32_16x16x64_bf16 v[48:51], v[20:23], v[32:39], v122\n\t"
      "ds_read_b128 v[0:3], v101\n\t"
      "ds_read_b128 v[4:7], v101 offset:1024\n\t"
      "s_add_u32 s40, s40, 1\n\t"
      "s_cmp_lt_u32 s40, 63\n\t"
      "v_smfmac_f32_16x16x64_bf16 v[48:51], v[24:27], v[40:47], v122\n\t"
      "ds_read_b128 v[8:11], v100 offset:8192\n\t"
      "ds_read_b128 v[12:15], v100 offset:9216\n\t"
      "s_cselect_b32 s61, 786432, 0\n\t"
      "s_cselect_b32 s60, s58, 0\n\t"
      "s_waitcnt lgkmcnt(4)\n\t"
      "v_smfmac_f32_16x16x64_bf16 v[48:51], v[28:31], v[88:95], v122\n\t"
      "s_add_u32 s44, s44, s61\n\t"
      "s_cmp_lt_u32 s40, 62\n\t"
      "ds_read_b128 v[16:19], v102\n\t"
      "ds_read_b128 v[20:23], v102 offset:1024\n\t"
      "ds_read_b128 v[24:27], v100 offset:16384\n\t"
      "ds_read_b128 v[28:31], v100 offset:17408\n\t"
      "s_cselect_b32 s61, 786432, 0\n\t"
      "s_add_u32 s46, s46, s61\n\t"
      "s_add_u32 s61, s40, 3\n\t"
      "s_and_b32 s62, s61, 3\n\t"
      "s_cselect_b32 s62, 16, 2000\n\t"
      "s_cmp_lt_u32 s61, 64\n\t"
      "s_cselect_b32 s62, s62, 0\n\t"
      "s_add_u32 s50, s50, s62\n\t"
      "s_cmp_eq_u32 s40, 64\n\t"
      "v_add_f32 v48, v48, v50\n\t"
      "v_add_f32 v49, v49, v51\n\t"
      "ds_write_b64 v124, v[48:49] offset:53312\n\t"
      "s_cbranch_scc1 LFIN_2%=\n\t"
      "v_mfma_f32_16x16x32_bf16 v[32:35], v[76:79], v[112:115], 0\n\t"
      "s_mov_b32 s63, 0x2000\n\t"
      "v_mfma_f32_16x16x32_bf16 v[36:39], v[80:83], v[112:115], 0\n\t"
      "v_mfma_f32_16x16x32_bf16 v[40:43], v[76:79], v[116:119], 0\n\t"
      "v_mfma_f32_16x16x32_bf16 v[44:47], v[80:83], v[116:119], 0\n\t"
      "ds_read_b128 v[76:79], v103\n\t"
      "ds_read_b128 v[80:83], v103 offset:1024\n\t"
      "ds_read_b128 v[112:115], v100 offset:24576\n\t"
      "ds_read_b128 v[116:119], v100 offset:25600\n\t"
      "LGATE_2%=:\n\t"
      "LBAR_2%=:\n\t"
      "s_cmp_gt_u32 s40, 1\n\t"
      "s_waitcnt lgkmcnt(0)\n\t"
      "s_barrier\n\t"
      "s_cbranch_scc1 LPOLL_2%=\n\t"
      "s_branch LFIRST_2%=\n\t"
      ".p2align 6\n\t.fill 16, 4, 0xbf800000\n\t"
      "LPOLL_3%=:\n\t"
      "s_memtime s[78:79]\n\t"
      "buffer_load_dwordx4 v[84:87], v60, s[36:39], s43 offen sc1\n\t"
      "buffer_load_dwordx4 v[88:91], v60, s[36:39], s43 offen offset:1024 sc1\n\t"
      "s_waitcnt lgkmcnt(8)\n\t"
      "v_mfma_f32_16x16x32_bf16 v[32:35], v[0:3], v[8:11], v[32:35]\n\t"
      "v_mfma_f32_16x16x32_bf16 v[36:39], v[4:7], v[8:11], v[36:39]\n\t"
      "v_mfma_f32_16x16x32_bf16 v[40:43], v[0:3], v[12:15], v[40:43]\n\t"
      "v_mfma_f32_16x16x32_bf16 v[44:47], v[4:7], v[12:15], v[44:47]\n\t"
      "buffer_load_dwordx4 v[92:95], v60, s[36:39], s68 offen sc1\n\t"
      "buffer_load_dwordx4 v[96:99], v60, s[36:39], s68 offen offset:1024 sc1\n\t"
      "v_mov_b64 v[48:49], 0\n\t"
      "v_mov_b64 v[50:51], 0\n\t"
      "v_mov_b64 v[52:53], 0\n\t"
      "v_mov_b64 v[54:55], 0\n\t"
      "s_xor_b32 s69, s97, 1024\n\t"
      "s_xor_b32 s98, s97, 512\n\t"
      "s_xor_b32 s23, s97, 1536\n\t"
      "s_waitcnt lgkmcnt(4)\n\t"
      "v_mfma_f32_16x16x32_bf16 v[32:35], v[16:19], v[24:27], v[32:35]\n\t"
      "v_mfma_f32_16x16x32_bf16 v[36:39], v[20:23], v[24:27], v[36:39]\n\t"
      "v_mfma_f32_16x16x32_bf16 v[40:43], v[16:19], v[28:31], v[40:43]\n\t"
      "v_mfma_f32_16x16x32_bf16 v[44:47], v[20:23], v[28:31], v[44:47]\n\t"
      "s_waitcnt lgkmcnt(0)\n\t"
      "v_mfma_f32_16x16x32_bf16 v[32:35], v[76:79], v[112:115], v[32:35]\n\t"
      "v_mfma_f32_16x16x32_bf16 v[36:39], v[80:83], v[112:115], v[36:39]\n\t"
      "v_mfma_f32_16x16x32_bf16 v[40:43], v[76:79], v[116:119], v[40:43]\n\t"
      "v_mfma_f32_16x16x32_bf16 v[44:47], v[80:83], v[116:119], v[44:47]\n\t"
      "s_mov_b64 exec, 1\n\t"
      "ds_write_b64 v109, v[110:111] offset:55392\n\t"
      "s_mov_b64 exec, -1\n\t"
      "s_nop 4\n\t"
      "v_cvt_pk_bf16_f32 v68, v32, v33\n\t"
      "v_cvt_pk_bf16_f32 v69, v34, v35\n\t"
      "v_cvt_pk_bf16_f32 v70, v36, v37\n\t"
      "v_cvt_pk_bf16_f32 v71, v38, v39\n\t"
      "v_cvt_pk_bf16_f32 v74, v40, v41\n\t"
      "v_cvt_pk_bf16_f32 v75, v42, v43\n\t"
      "v_cvt_pk_bf16_f32 v72, v44, v45\n\t"
      "v_cvt_pk_bf16_f32 v73, v46, v47\n\t"
      "v_mov_b64 v[32:33], 0\n\t"
      "v_mov_b64 v[34:35], 0\n\t"
      "v_mov_b64 v[36:37], 0\n\t"
      "v_mov_b64 v[38:39], 0\n\t"
      "v_mov_b64 v[40:41], 0\n\t"
      "v_mov_b64 v[42:43], 0\n\t"
      "v_mov_b64 v[44:45], 0\n\t"
      "v_mov_b64 v[46:47], 0\n\t"
      "LGO_3%=:\n\t"
      "s_nop 0\n\t"
      "s_waitcnt vmcnt(3)\n\t"
      "s_memtime s[88:89]\n\t"
      "v_pk_fma_f32 v[10:11], v[86:87], s[56:57], v[126:127] op_sel:[0,1,0] op_sel_hi:[1,1,0]\n\t"
      "v_pk_fma_f32 v[8:9], v[84:85], s[56:57], v[126:127] op_sel:[0,1,0] op_sel_hi:[1,1,0]\n\t"
      "v_cvt_pk_bf16_f32 v12, v8, v9\n\t"
      "v_cvt_pk_bf16_f32 v13, v10, v11\n\t"
      "s_nop 0\n\t"
      "ds_write_b64 v127, v[12:13] offset:44032\n\t"
      "s_waitcnt vmcnt(2)\n\t"
      "v_pk_fma_f32 v[4:5], v[88:89], s[56:57], v[126:127] op_sel:[0,1,0] op_sel_hi:[1,1,0]\n\t"
      "v_pk_fma_f32 v[6:7], v[90:91], s[56:57], v[126:127] op_sel:[0,1,0] op_sel_hi:[1,1,0]\n\t"
      "v_cvt_pk_bf16_f32 v14, v4, v5\n\t"
      "v_cvt_pk_bf16_f32 v15, v6, v7\n\t"
      "ds_write_b64 v127, v[14:15] offset:44040\n\t"
      "v_bitop3_b32 v64, v84, v85, v86 bitop3:0x80\n\t"
      "v_mfma_f32_16x16x32_bf16 v[56:59], v[12:15], v[12:15], 0\n\t"
      "v_bitop3_b32 v64, v64, v87, v88 bitop3:0x80\n\t"
      "v_bitop3_b32 v64, v64, v89, v90 bitop3:0x80\n\t"
      "v_bitop3_b32 v64, v64, v91, s58 bitop3:0x80\n\t"
      "v_cmp_eq_u32_e64 vcc, 0, v64\n\t"
      "v_smfmac_f32_16x16x64_bf16 v[36:39], v[12:15], a[88:95], v122\n\t"
      "s_nop 0\n\t"
      "v_bitop3_b32 v120, v120, v121, s53 bitop3:0x7e\n\t"
      "v_smfmac_f32_16x16x64_bf16 v[44:47], v[12:15], a[216:223], v122\n\t"
      "s_cbranch_vccnz LR0_3%=\n\t"
      "LC0_3%=:\n\t"
      "s_waitcnt lgkmcnt(0)\n\t"
      "s_barrier\n\t"
      "ds_read_b128 v[8:11], v127 offset:43008\n\t"
      "v_smfmac_f32_16x16x64_bf16 v[52:55], v[12:15], v[216:223], v122\n\t"
      "ds_read_b128 v[4:7], v127 offset:41984\n\t"
      "v_smfmac_f32_16x16x64_bf16 v[48:51], v[12:15], v[152:159], v122\n\t"
      "ds_read_b128 v[0:3], v127 offset:40960\n\t"
      "v_smfmac_f32_16x16x64_bf16 v[32:35], v[12:15], a[24:31], v122\n\t"
      "ds_read_b128 v[84:87], v127 offset:36864\n\t"
      "v_smfmac_f32_16x16x64_bf16 v[40:43], v[12:15], a[152:159], v122\n\t"
      "s_waitcnt lgkmcnt(3)\n\t"
      "v_smfmac_f32_16x16x64_bf16 v[32:35], v[8:11], a[16:23], v122\n\t"
      "ds_read_b128 v[80:83], v127 offset:35840\n\t"
      "v_smfmac_f32_16x16x64_bf16 v[40:43], v[8:11], a[144:151], v122\n\t"
      "s_waitcnt vmcnt(1)\n\t"
      "v_fma_f32 v27, v95, s57, v126\n\t"
      "v_fma_f32 v24, v92, s57, v126\n\t"
      "v_smfmac_f32_16x16x64_bf16 v[36:39], v[8:11], a[80:87], v122\n\t"
      "v_fma_f32 v26, v94, s57, v126\n\t"
      "v_cvt_pk_bf16_f32 v29, v26, v27\n\t"
      "v_bitop3_b32 v64, v92, v93, v94 bitop3:0x80\n\t"
      "v_smfmac_f32_16x16x64_bf16 v[44:47], v[8:11], a[208:215], v122\n\t"
      "s_waitcnt lgkmcnt(2)\n\t"
      "v_fma_f32 v25, v93, s57, v126\n\t"
      "v_cvt_pk_bf16_f32 v28, v24, v25\n\t"
      "v_smfmac_f32_16x16x64_bf16 v[36:39], v[4:7], a[72:79], v122\n\t"
      "s_waitcnt vmcnt(0)\n\t"
      "ds_write_b64 v127, v[28:29] offset:48128\n\t"
      "v_fma_f32 v23, v99, s57, v126\n\t"
      "v_fma_f32 v22, v98, s57, v126\n\t"
      "v_cvt_pk_bf16_f32 v31, v22, v23\n\t"
      "v_fma_f32 v21, v97, s57, v126\n\t"
      "v_fma_f32 v20, v96, s57, v126\n\t"
      "v_cvt_pk_bf16_f32 v30, v20, v21\n\t"
      "v_smfmac_f32_16x16x64_bf16 v[44:47], v[4:7], a[200:207], v122\n\t"
      "ds_write_b64 v127, v[30:31] offset:48136\n\t"
      "v_smfmac_f32_16x16x64_bf16 v[40:43], v[4:7], a[136:143], v122\n\t"
      "ds_read_b128 v[88:91], v127 offset:39936\n\t"
      "v_bitop3_b32 v64, v64, v95, v96 bitop3:0x80\n\t"
      "v_bitop3_b32 v64, v64, v97, v98 bitop3:0x80\n\t"
      "v_smfmac_f32_16x16x64_bf16 v[40:43], v[0:3], a[128:135], v122\n\t"
      "v_bitop3_b32 v64, v64, v99, s60 bitop3:0x80\n\t"
      "v_smfmac_f32_16x16x64_bf16 v[36:39], v[0:3], a[64:71], v122\n\t"
      "v_cmp_eq_u32_e64 vcc, 0, v64\n\t"
      "v_smfmac_f32_16x16x64_bf16 v[44:47], v[0:3], a[192:199], v122\n\t"
      "s_waitcnt lgkmcnt(1)\n\t"
      "v_smfmac_f32_16x16x64_bf16 v[32:35], v[4:7], a[8:15], v122\n\t"
      "s_cbranch_vccnz LR1_3%=\n\t"
      "LC1_3%=:\n\t"
      "v_smfmac_f32_16x16x64_bf16 v[40:43], v[28:31], a[184:191], v122\n\t"
      "s_barrier\n\t"
      "v_mfma_f32_16x16x32_bf16 v[56:59], v[28:31], v[28:31], v[56:59]\n\t"
      "ds_read_b128 v[24:27], v127 offset:47104\n\t"
      "s_nop 0\n\t"
      "v_smfmac_f32_16x16x64_bf16 v[32:35], v[0:3], a[0:7], v122\n\t"
      "ds_read_b128 v[20:23], v127 offset:46080\n\t"
      "v_smfmac_f32_16x16x64_bf16 v[44:47], v[28:31], a[248:255], v122\n\t"
      "ds_read_b128 v[16:19], v127 offset:45056\n\t"
      "v_max3_f32 v56, v56, v57, v58\n\t"
      "v_smfmac_f32_16x16x64_bf16 v[36:39], v[28:31], a[120:127], v122\n\t"
      "ds_read_b128 v[76:79], v127 offset:32768\n\t"
      "v_max_f32_e64 v56, v56, v59\n\t"
      "v_smfmac_f32_16x16x64_bf16 v[32:35], v[28:31], a[56:63], v122\n\t"
      "s_waitcnt lgkmcnt(3)\n\t"
      "v_readfirstlane_b32 s56, v120\n\t"
      "v_smfmac_f32_16x16x64_bf16 v[32:35], v[24:27], a[48:55], v122\n\t"
      "s_waitcnt lgkmcnt(2)\n\t"
      "v_max_f32_dpp v56, v56, v56 quad_perm:[1,0,3,2] row_mask:0xf bank_mask:0xf\n\t"
      "v_smfmac_f32_16x16x64_bf16 v[32:35], v[20:23], a[40:47], v122\n\t"
      "s_waitcnt lgkmcnt(1)\n\t"
      "v_max_f32_dpp v56, v56, v56 quad_perm:[2,3,0,1] row_mask:0xf bank_mask:0xf\n\t"
      "v_smfmac_f32_16x16x64_bf16 v[32:35], v[16:19], a[32:39], v122\n\t"
      "s_mov_b32 exec_lo, 0x100001\n\t"
      "s_mov_b32 exec_hi, 0x10000100\n\t"
      "v_smfmac_f32_16x16x64_bf16 v[36:39], v[24:27], a[112:119], v122\n\t"
      "ds_max_i32 v109, v56 offset:55392\n\t"
      "v_smfmac_f32_16x16x64_bf16 v[40:43], v[24:27], a[176:183], v122\n\t"
      "s_mov_b64 exec, -1\n\t"
      "v_add_f32 v33, v33, v35\n\t"
      "v_smfmac_f32_16x16x64_bf16 v[36:39], v[20:23], a[104:111], v122\n\t"
      "ds_read_b128 v[60:63], v127 offset:37888\n\t"
      "v_mul_f32_e64 v33, s59, v33\n\t"
      "v_smfmac_f32_16x16x64_bf16 v[36:39], v[16:19], a[96:103], v122\n\t"
      "v_add_f32_e64 v32, v32, v34\n\t"
      "v_smfmac_f32_16x16x64_bf16 v[44:47], v[24:27], a[240:247], v122\n\t"
      "ds_read_b128 v[56:59], v127 offset:38912\n\t"
      "v_mul_f32_e64 v32, s59, v32\n\t"
      "v_smfmac_f32_16x16x64_bf16 v[44:47], v[20:23], a[232:239], v122\n\t"
      "v_add_f32 v36, v36, v38\n\t"
      "v_add_f32 v37, v37, v39\n\t"
      "v_mul_f32_e64 v37, s59, v37\n\t"
      "v_smfmac_f32_16x16x64_bf16 v[40:43], v[20:23], a[168:175], v122\n\t"
      "ds_read_b128 v[92:95], v127 offset:34816\n\t"
      "v_cvt_pk_bf16_f32 v112, v32, v33\n\t"
      "v_smfmac_f32_16x16x64_bf16 v[44:47], v[16:19], a[224:231], v122\n\t"
      "ds_read_b128 v[96:99], v127 offset:33792\n\t"
      "v_mul_f32_e64 v36, s59, v36\n\t"
      "v_cvt_pk_bf16_f32 v113, v36, v37\n\t"
      "v_smfmac_f32_16x16x64_bf16 v[40:43], v[16:19], a[160:167], v122\n\t"
      "ds_write_b64 v125, v[112:113]\n\t"
      "v_smfmac_f32_16x16x64_bf16 v[48:51], v[8:11], v[144:151], v122\n\t"
      "s_nop 0\n\t"
      "v_add_f32 v45, v45, v47\n\t"
      "v_add_f32_e64 v44, v44, v46\n\t"
      "v_cvt_pk_bf16_f32 v121, v44, v45\n\t"
      "v_add_f32 v41, v41, v43\n\t"
      "v_add_f32 v40, v40, v42\n\t"
      "v_cvt_pk_bf16_f32 v120, v40, v41\n\t"
      "v_smfmac_f32_16x16x64_bf16 v[48:51], v[4:7], v[136:143], v122\n\t"
      "s_mov_b32 s58, 0x10000000\n\t"
      "ds_write_b64 v125, v[120:121] offset:2080\n\t"
      "v_smfmac_f32_16x16x64_bf16 v[48:51], v[0:3], v[128:135], v122\n\t"
      "s_waitcnt lgkmcnt(1)\n\t"
      "s_barrier\n\t"
      "ds_read_b64_tr_b16 v[36:37], v124 offset:50192\n\t"
      "v_smfmac_f32_16x16x64_bf16 v[48:51], v[28:31], v[184:191], v122\n\t"
      "ds_read_b32 v108, v109 offset:55392\n\t"
      "ds_read_b64_tr_b16 v[32:33], v124 offset:49152\n\t"
      "v_smfmac_f32_16x16x64_bf16 v[52:55], v[8:11], v[208:215], v122\n\t"
      "ds_read_b64_tr_b16 v[34:35], v124 offset:49672\n\t"
      "s_waitcnt lgkmcnt(4)\n\t"
      "ds_read_b64_tr_b16 v[38:39], v124 offset:50712\n\t"
      "v_smfmac_f32_16x16x64_bf16 v[48:51], v[24:27], v[176:183], v122\n\t"
      "s_barrier\n\t"
      "ds_read_b64_tr_b16 v[46:47], v124 offset:52792\n\t"
      "v_smfmac_f32_16x16x64_bf16 v[48:51], v[20:23], v[168:175], v122\n\t"
      "s_waitcnt lgkmcnt(4)\n\t"
      "ds_read_b64_tr_b16 v[44:45], v124 offset:52272\n\t"
      "v_readfirstlane_b32 s61, v108\n\t"
      "v_smfmac_f32_16x16x64_bf16 v[48:51], v[16:19], v[160:167], v122\n\t"
      "s_waitcnt lgkmcnt(3)\n\t"
      "ds_read_b64_tr_b16 v[42:43], v124 offset:51752\n\t"
      "ds_read_b64_tr_b16 v[40:41], v124 offset:51232\n\t"
      "s_waitcnt lgkmcnt(4)\n\t"
      "v_mfma_f32_16x16x32_bf16 v[112:115], v[76:79], v[32:35], 0\n\t"
      "s_lshr_b32 s61, s61, 23\n\t"
      "s_add_u32 s61, s61, s67\n\t"
      "v_mov_b32_dpp v102, v34 quad_perm:[2,3,0,1] row_mask:0xf bank_mask:0xf\n\t"
      "v_mov_b32_dpp v106, v38 quad_perm:[2,3,0,1] row_mask:0xf bank_mask:0xf\n\t"
      "s_max_u32 s61, s61, s66\n\t"
      "v_mfma_f32_16x16x32_bf16 v[116:119], v[80:83], v[32:35], 0\n\t"
      "s_lshr_b32 s61, s61, 1\n\t"
      "v_add_f32_e64 v48, v48, v50\n\t"
      "v_mov_b32_dpp v100, v32 quad_perm:[2,3,0,1] row_mask:0xf bank_mask:0xf\n\t"
      "v_mfma_f32_16x16x32_bf16 v[116:119], v[88:91], v[36:39], v[116:119]\n\t"
      "s_min_u32 s61, s61, 200\n\t"
      "v_mov_b32_dpp v107, v39 quad_perm:[2,3,0,1] row_mask:0xf bank_mask:0xf\n\t"
      "s_waitcnt lgkmcnt(0)\n\t"
      "v_mfma_f32_16x16x32_bf16 v[76:79], v[40:43], v[32:35], 0\n\t"
      "buffer_load_dwordx4 v127, s[36:39], s46 offen sc1 lds\n\t"
      "s_lshl_b32 s61, s61, 23\n\t"
      "v_mfma_f32_16x16x32_bf16 v[76:79], v[44:47], v[36:39], v[76:79]\n\t"
      "s_add_u32 s65, s61, 33554432\n\t"
      "v_mov_b32_dpp v104, v36 quad_perm:[2,3,0,1] row_mask:0xf bank_mask:0xf\n\t"
      "v_mov_b32_dpp v103, v35 quad_perm:[2,3,0,1] row_mask:0xf bank_mask:0xf\n\t"
      "v_mov_b32_dpp v101, v33 quad_perm:[2,3,0,1] row_mask:0xf bank_mask:0xf\n\t"
      "v_mfma_f32_16x16x32_bf16 v[112:115], v[84:87], v[36:39], v[112:115]\n\t"
      "s_pack_hh_b32_b16 s75, s65, s65\n\t"
      "v_add_f32 v49, v49, v51\n\t"
      "v_cvt_pk_bf16_f32 v64, v48, v49\n\t"
      "v_cndmask_b32_e64 v120, v76, v78, s[72:73]\n\t"
      "v_cndmask_b32_e64 v121, v77, v79, s[72:73]\n\t"
      "v_mov_b32_dpp v105, v37 quad_perm:[2,3,0,1] row_mask:0xf bank_mask:0xf\n\t"
      "v_mfma_f32_16x16x32_bf16 v[112:115], v[92:95], v[100:103], v[112:115]\n\t"
      "buffer_load_dwordx4 v127, s[36:39], s46 offen offset:1024 sc1 lds\n\t"
      "v_mfma_f32_16x16x32_bf16 v[116:119], v[96:99], v[100:103], v[116:119]\n\t"
      "s_pack_hh_b32_b16 s74, s65, s65\n\t"
      "v_add_f32_dpp v121, v121, v121 quad_perm:[2,3,0,1] row_mask:0xf bank_mask:0xf\n\t"
      "v_add_f32_dpp v120, v120, v120 quad_perm:[2,3,0,1] row_mask:0xf bank_mask:0xf\n\t"
      "v_mfma_f32_16x16x32_bf16 v[112:115], v[56:59], v[104:107], v[112:115]\n\t"
      "v_exp_f32 v81, v121\n\t"
      "v_mfma_f32_16x16x32_bf16 v[116:119], v[60:63], v[104:107], v[116:119]\n\t"
      "s_mov_b32 m0, s45\n\t"
      "v_mov_b64 v[90:91], s[74:75]\n\t"
      "v_exp_f32 v80, v120\n\t"
      "v_mov_b64 v[88:89], s[74:75]\n\t"
      "v_cvt_pk_bf16_f32 v108, v80, v81\n\t"
      "v_smfmac_f32_16x16x64_bf16 v[52:55], v[4:7], v[200:207], v122\n\t"
      "buffer_load_dwordx4 v65, s[36:39], s44 offen offset:1024 sc1 lds\n\t"
      "v_mfma_f32_16x16x32_bf16 v[84:87], v[108:111], v[64:67], 0\n\t"
      "s_add_u32 s57, s61, 310378496\n\t"
      "v_cndmask_b32_e64 v112, v112, v116, s[72:73]\n\t"
      "v_exp_f32_e64 v56, v112\n\t"
      "v_mfma_f32_16x16x32_bf16 v[48:51], v[108:111], v[88:91], 0\n\t"
      "s_add_u32 s43, s43, 0x8000\n\t"
      "v_cndmask_b32_e64 v113, v113, v117, s[72:73]\n\t"
      "v_cndmask_b32_e64 v114, v114, v118, s[72:73]\n\t"
      "v_exp_f32_e64 v58, v114\n\t"
      "v_cndmask_b32_e64 v115, v115, v119, s[72:73]\n\t"
      "v_exp_f32 v57, v113\n\t"
      "v_exp_f32 v59, v115\n\t"
      "v_cvt_pk_bf16_f32 v96, v56, v57\n\t"
      "v_cvt_pk_bf16_f32 v97, v58, v59\n\t"
      "v_max3_f32 v82, |v112|, |v113|, |v114|\n\t"
      "v_mov_b32_dpp v98, v96 quad_perm:[2,3,0,1] row_mask:0xf bank_mask:0xf\n\t"
      "v_mov_b32_dpp v99, v97 quad_perm:[2,3,0,1] row_mask:0xf bank_mask:0xf\n\t"
      "v_smfmac_f32_16x16x64_bf16 v[52:55], v[0:3], v[192:199], v122\n\t"
      "v_max3_f32 v83, |v115|, |v120|, |v121|\n\t"
      "v_mfma_f32_16x16x32_bf16 v[48:51], v[96:99], v[88:91], v[48:51]\n\t"
      "v_max_f32_e64 v82, v82, v83\n\t"
      "v_cmp_nge_f32 vcc, 0x42000000, v82\n\t"
      "v_mfma_f32_16x16x32_bf16 v[84:87], v[96:99], v[68:71], v[84:87]\n\t"
      "buffer_load_dwordx4 v65, s[36:39], s44 offen sc1 lds\n\t"
      "v_smfmac_f32_16x16x64_bf16 v[52:55], v[28:31], v[248:255], v122\n\t"
      "s_add_u32 s68, s68, 0x8000\n\t"
      "s_cbranch_vccnz LMX_3%=\n\t"
      "v_rcp_f32 v49, v49\n\t"
      "v_rcp_f32_e64 v48, v48\n\t"
      "v_smfmac_f32_16x16x64_bf16 v[52:55], v[24:27], v[240:247], v122\n\t"
      "v_fma_f32 v85, v85, v49, s96\n\t"
      "v_fma_f32 v84, v84, v48, s96\n\t"
      "v_smfmac_f32_16x16x64_bf16 v[52:55], v[20:23], v[232:239], v122\n\t"
      "buffer_atomic_add v84, v123, s[36:39], s97 offen\n\t"
      "buffer_atomic_add v85, v123, s[36:39], s98 offen\n\t"
      "v_smfmac_f32_16x16x64_bf16 v[52:55], v[16:19], v[224:231], v122\n\t"
      "v_mfma_f32_16x16x32_bf16 v[92:95], v[96:99], v[72:75], 0\n\t"
      "s_nop 3\n\t"
      "v_add_f32 v53, v53, v55\n\t"
      "v_add_f32_e64 v52, v52, v54\n\t"
      "v_cvt_pk_bf16_f32 v66, v52, v53\n\t"
      "s_add_u32 s62, s61, 0x83400000\n\t"
      "v_mov_b32_e64 v126, s62\n\t"
      "v_mfma_f32_16x16x32_bf16 v[92:95], v[108:111], v[66:69], v[92:95]\n\t"
      "s_add_u32 s97, s97, 0x8000\n\t"
      "s_nop 6\n\t"
      "v_fma_f32 v95, v95, v49, s96\n\t"
      "buffer_atomic_add v95, v123, s[36:39], s23 offen\n\t"
      "v_fma_f32 v94, v94, v48, s96\n\t"
      "buffer_atomic_add v94, v123, s[36:39], s69 offen\n\t"
      "LWIN_3%=:\n\t"
      "s_memtime s[76:77]\n\t"
      "s_memrealtime s[80:81]\n\t"
      "v_add_u32 v100, s55, v127\n\t"
      "ds_read_b128 v[60:63], v100 offset:57344\n\t"
      "s_cmp_eq_u32 s56, 0\n\t"
      "s_cbranch_scc0 LSLOW_3%=\n\t"
      "LWIN1_3%=:\n\t"
      "buffer_load_dwordx2 v[120:121], off, s[36:39], s50 sc1\n\t"
      "s_mov_b32 s64, s50\n\t"
      "s_bitcmp1_b64 s[84:85], s40\n\t"
      "s_cbranch_scc1 LEARLY_3%=\n\t"
      "LROLE_3%=:\n\t"
      "v_add_u32 v52, 67584, v127\n\t"
      "ds_read_b128 v[88:91], v52 offset:16384\n\t"
      "ds_read_b128 v[92:95], v52 offset:17408\n\t"
      "ds_read_b128 v[32:35], v52 offset:20480\n\t"
      "ds_read_b128 v[36:39], v52 offset:21504\n\t"
      "ds_read_b128 v[40:43], v52 offset:24576\n\t"
      "ds_read_b128 v[44:47], v52 offset:25600\n\t"
      "v_mov_b64 v[48:49], 0\n\t"
      "v_mov_b64 v[50:51], 0\n\t"
      "v_add_u32 v104, s71, v100\n\t"
      "ds_read_b128 v[76:79], v104\n\t"
      "ds_read_b128 v[80:83], v104 offset:1024\n\t"
      "ds_read_b128 v[112:115], v100\n\t"
      "ds_read_b128 v[116:119], v100 offset:1024\n\t"
      "v_xor_b32 v101, 2048, v104\n\t"
      "v_xor_b32 v102, 4096, v104\n\t"
      "v_xor_b32 v103, 6144, v104\n\t"
      "s_waitcnt lgkmcnt(8)\n\t"
      "v_smfmac_f32_16x16x64_bf16 v[48:51], v[16:19], v[88:95], v122\n\t"
      "ds_read_b128 v[88:91], v52 offset:28672\n\t"
      "ds_read_b128 v[92:95], v52 offset:29696\n\t"
      "s_add_u32 m0, s55, s71\n\t"
      "s_sub_u32 s71, 204800, s71\n\t"
      "s_waitcnt lgkmcnt(8)\n\t"
      "v_smfmac_f32_16x16x64_bf16 v[48:51], v[20:23], v[32:39], v122\n\t"
      "ds_read_b128 v[0:3], v101\n\t"
      "ds_read_b128 v[4:7], v101 offset:1024\n\t"
      "s_add_u32 s40, s40, 1\n\t"
      "s_cmp_lt_u32 s40, 63\n\t"
      "s_waitcnt lgkmcnt(8)\n\t"
      "v_smfmac_f32_16x16x64_bf16 v[48:51], v[24:27], v[40:47], v122\n\t"
      "ds_read_b128 v[8:11], v100 offset:8192\n\t"
      "ds_read_b128 v[12:15], v100 offset:9216\n\t"
      "s_cselect_b32 s61, 786432, 0\n\t"
      "s_cselect_b32 s60, s58, 0\n\t"
      "s_waitcnt lgkmcnt(4)\n\t"
      "v_smfmac_f32_16x16x64_bf16 v[48:51], v[28:31], v[88:95], v122\n\t"
      "s_add_u32 s44, s44, s61\n\t"
      "s_cmp_lt_u32 s40, 62\n\t"
      "ds_read_b128 v[16:19], v102\n\t"
      "ds_read_b128 v[20:23], v102 offset:1024\n\t"
      "ds_read_b128 v[24:27], v100 offset:16384\n\t"
      "ds_read_b128 v[28:31], v100 offset:17408\n\t"
      "s_cselect_b32 s61, 786432, 0\n\t"
      "s_add_u32 s46, s46, s61\n\t"
      "s_add_u32 s61, s40, 3\n\t"
      "s_and_b32 s62, s61, 3\n\t"
      "s_cselect_b32 s62, 16, 2000\n\t"
      "s_cmp_lt_u32 s61, 64\n\t"
      "s_cselect_b32 s62, s62, 0\n\t"
      "s_add_u32 s50, s50, s62\n\t"
      "s_cmp_eq_u32 s40, 64\n\t"
      "v_add_f32 v48, v48, v50\n\t"
      "v_add_f32 v49, v49, v51\n\t"
      "ds_write_b64 v124, v[48:49] offset:53832\n\t"
      "s_cbranch_scc1 LFIN_3%=\n\t"
      "v_mfma_f32_16x16x32_bf16 v[32:35], v[76:79], v[112:115], 0\n\t"
      "s_sub_u32 s61, s88, s78\n\t"
      "s_sub_i32 s61, s61, 560\n\t"
      "s_lshr_b32 s62, s25, 3\n\t"
      "v_mfma_f32_16x16x32_bf16 v[36:39], v[80:83], v[112:115], 0\n\t"
      "s_sub_u32 s25, s25, s62\n\t"
      "s_sub_i32 s25, s25, 24\n\t"
      "s_max_i32 s25, s25, 0\n\t"
      "v_mfma_f32_16x16x32_bf16 v[40:43], v[76:79], v[116:119], 0\n\t"
      "s_sub_i32 s62, s61, 96\n\t"
      "s_max_i32 s62, s62, 0\n\t"
      "s_min_i32 s62, s62, 320\n\t"
      "v_mfma_f32_16x16x32_bf16 v[44:47], v[80:83], v[116:119], 0\n\t"
      "s_lshr_b32 s62, s62, 2\n\t"
      "s_add_u32 s25, s25, s62\n\t"
      "s_min_i32 s61, s61, 96\n\t"
      "ds_read_b128 v[76:79], v103\n\t"
      "ds_read_b128 v[80:83], v103 offset:1024\n\t"
      "ds_read_b128 v[112:115], v100 offset:24576\n\t"
      "ds_read_b128 v[116:119], v100 offset:25600\n\t"
      "s_max_i32 s61, s61, -96\n\t"
      "s_ashr_i32 s61, s61, 3\n\t"
      "s_add_u32 s26, s26, s61\n\t"
      "s_max_u32 s26, s26, 1500\n\t"
      "s_min_u32 s26, s26, 2300\n\t"
      "s_sub_u32 s62, s88, s27\n\t"
      "s_mov_b32 s27, s88\n\t"
      "s_sub_i32 s62, s62, s24\n\t"
      "s_cmp_gt_i32 s62, 1150\n\t"
      "s_cselect_b32 s62, 200, 0\n\t"
      "s_max_u32 s25, s25, s62\n\t"
      "s_sub_u32 s61, s76, s28\n\t"
      "s_sub_u32 s62, s80, s82\n\t"
      "s_mov_b32 s28, s76\n\t"
      "s_mov_b32 s82, s80\n\t"
      "s_lshl_b32 s61, s61, 8\n\t"
      "s_mul_i32 s62, s62, s83\n\t"
      "s_sub_i32 s61, s61, s62\n\t"
      "s_ashr_i32 s61, s61, 10\n\t"
      "s_min_i32 s61, s61, 100\n\t"
      "s_max_i32 s61, s61, -100\n\t"
      "s_add_u32 s83, s83, s61\n\t"
      "s_mul_i32 s24, s26, s83\n\t"
      "s_lshr_b32 s24, s24, 12\n\t"
      "s_add_u32 s24, s24, s25\n\t"
      "s_mov_b32 s63, 0x2000\n\t"
      "LGATE_3%=:\n\t"
      "s_memtime s[92:93]\n\t"
      "s_waitcnt lgkmcnt(0)\n\t"
      "s_sub_u32 s61, s92, s88\n\t"
      "s_sub_i32 s61, s24, s61\n\t"
      "s_sub_i32 s61, s61, 70\n\t"
      "s_min_i32 s61, s61, 3000\n\t"
      "s_getpc_b64 s[92:93]\n\t"
      "LDPC_3%=:\n\t"
      "s_max_i32 s61, s61, 0\n\t"
      "s_andn2_b32 s62, 28, s61\n\t"
      "s_add_u32 s62, s62, LDSL_3%=-LDPC_3%=\n\t"
      "s_add_u32 s92, s92, s62\n\t"
      "s_addc_u32 s93, s93, 0\n\t"
      "LDLY_3%=:\n\t"
      "s_sub_i32 s61, s61, 32\n\t"
      "s_cmp_ge_i32 s61, 0\n\t"
      "s_cbranch_scc1 LDLY_3%=\n\t"
      "s_setpc_b64 s[92:93]\n\t"
      "LDSL_3%=:\n\t"
      "s_nop 0\n\t"
      "s_nop 0\n\t"
      "s_nop 0\n\t"
      "s_nop 0\n\t"
      "s_nop 0\n\t"
      "s_nop 0\n\t"
      "s_nop 0\n\t"
      "LBAR_3%=:\n\t"
      "s_cmp_gt_u32 s40, 1\n\t"
      "s_barrier\n\t"
      "s_cbranch_scc1 LPOLL_3%=\n\t"
      "s_branch LFIRST_3%=\n\t"
      "LUTOK_0%=:\n\t"
      "v_add_u32 v104, s71, v100\n\t"
      "ds_read_b128 v[76:79], v104\n\t"
      "ds_read_b128 v[80:83], v104 offset:1024\n\t"
      "ds_read_b128 v[112:115], v100\n\t"
      "ds_read_b128 v[116:119], v100 offset:1024\n\t"
      "v_xor_b32 v101, 2048, v104\n\t"
      "v_xor_b32 v102, 4096, v104\n\t"
      "v_xor_b32 v103, 6144, v104\n\t"
      "ds_read_b128 v[16:19], v101\n\t"
      "ds_read_b128 v[20:23], v101 offset:1024\n\t"
      "ds_read_b128 v[24:27], v100 offset:8192\n\t"
      "ds_read_b128 v[28:31], v100 offset:9216\n\t"
      "ds_read_b128 v[0:3], v102\n\t"
      "ds_read_b128 v[4:7], v102 offset:1024\n\t"
      "ds_read_b128 v[8:11], v100 offset:16384\n\t"
      "ds_read_b128 v[12:15], v100 offset:17408\n\t"
      "s_mov_b32 s28, s52\n\t"
      "s_add_u32 m0, s55, s71\n\t"
      "s_sub_u32 s71, 204800, s71\n\t"
      "s_add_u32 s40, s40, 1\n\t"
      "s_cmp_lt_u32 s40, 63\n\t"
      "s_cselect_b32 s61, 786432, 0\n\t"
      "s_cselect_b32 s60, s58, 0\n\t"
      "s_add_u32 s44, s44, s61\n\t"
      "s_cmp_lt_u32 s40, 62\n\t"
      "s_cselect_b32 s61, 786432, 0\n\t"
      "s_add_u32 s46, s46, s61\n\t"
      "s_add_u32 s61, s40, 3\n\t"
      "s_and_b32 s62, s61, 3\n\t"
      "s_cselect_b32 s62, 16, 2000\n\t"
      "s_cmp_lt_u32 s61, 64\n\t"
      "s_cselect_b32 s62, s62, 0\n\t"
      "s_add_u32 s50, s50, s62\n\t"
      "s_mov_b32 s63, 0x2000\n\t"
      "s_waitcnt lgkmcnt(8)\n\t"
      "v_mfma_f32_16x16x32_bf16 v[32:35], v[76:79], v[112:115], 0\n\t"
      "v_mfma_f32_16x16x32_bf16 v[36:39], v[80:83], v[112:115], 0\n\t"
      "v_mfma_f32_16x16x32_bf16 v[40:43], v[76:79], v[116:119], 0\n\t"
      "v_mfma_f32_16x16x32_bf16 v[44:47], v[80:83], v[116:119], 0\n\t"
      "ds_read_b128 v[76:79], v103\n\t"
      "ds_read_b128 v[80:83], v103 offset:1024\n\t"
      "ds_read_b128 v[112:115], v100 offset:24576\n\t"
      "ds_read_b128 v[116:119], v100 offset:25600\n\t"
      "s_waitcnt lgkmcnt(8)\n\t"
      "v_mfma_f32_16x16x32_bf16 v[32:35], v[16:19], v[24:27], v[32:35]\n\t"
      "v_mfma_f32_16x16x32_bf16 v[36:39], v[20:23], v[24:27], v[36:39]\n\t"
      "v_mfma_f32_16x16x32_bf16 v[40:43], v[16:19], v[28:31], v[40:43]\n\t"
      "v_mfma_f32_16x16x32_bf16 v[44:47], v[20:23], v[28:31], v[44:47]\n\t"
      "s_branch LGATE_0%=\n\t"
      "LFIRST_0%=:\n\t"
      "s_cmp_eq_u32 s40, 0\n\t"
      "s_cbranch_scc1 LWM0_0%=\n\t"
      "s_barrier\n\t"
      "ds_read_b32 v99, v109 offset:55392\n\t"
      "s_waitcnt lgkmcnt(0)\n\t"
      "v_readfirstlane_b32 s61, v99\n\t"
      "s_nop 1\n\t"
      "s_cmp_gt_i32 s61, 0\n\t"
      "s_cselect_b32 s99, 0x2000, 0\n\t"
      "s_xor_b32 s97, s97, s99\n\t"
      "s_barrier\n\t"
      "s_mov_b32 s63, 0x2000\n\t"
      "s_branch LPOLL_0%=\n\t"
      "LWM0_0%=:\n\t"
      "s_mov_b32 s63, 0x10000\n\t"
      "LWM_0%=:\n\t"
      "s_waitcnt vmcnt(0) lgkmcnt(0)\n\t"
      "v_bitop3_b32 v49, v49, v51, s53 bitop3:0x7e\n\t"
      "v_max_u32 v48, v48, v50\n\t"
      "v_cmp_ne_u32 vcc, 0, v49\n\t"
      "s_nop 1\n\t"
      "v_max_u32_dpp v48, v48, v48 quad_perm:[1,0,3,2] row_mask:0xf bank_mask:0xf\n\t"
      "s_nop 1\n\t"
      "v_max_u32_dpp v48, v48, v48 quad_perm:[2,3,0,1] row_mask:0xf bank_mask:0xf\n\t"
      "s_nop 1\n\t"
      "v_max_u32_dpp v48, v48, v48 row_ror:4 row_mask:0xf bank_mask:0xf\n\t"
      "s_nop 1\n\t"
      "v_max_u32_dpp v48, v48, v48 row_ror:8 row_mask:0xf bank_mask:0xf\n\t"
      "v_mov_b32 v49, v48\n\t"
      "s_nop 1\n\t"
      "v_permlane16_swap_b32 v48, v49\n\t"
      "v_max_u32 v48, v48, v49\n\t"
      "v_mov_b32 v49, v48\n\t"
      "s_nop 1\n\t"
      "v_permlane32_swap_b32 v48, v49\n\t"
      "v_max_u32 v48, v48, v49\n\t"
      "v_mul_f32 v49, 0x40a00000, v48\n\t"
      "s_nop 0\n\t"
      "v_readfirstlane_b32 s67, v49\n\t"
      "v_mul_f32 v49, 0x43030000, v49\n\t"
      "s_nop 0\n\t"
      "v_readfirstlane_b32 s66, v49\n\t"
      "s_cbranch_vccz LWM1_0%=\n\t"
      "s_sleep 4\n\t"
      "buffer_load_dwordx2 v[48:49], v52, s[36:39], s94 offen sc1\n\t"
      "buffer_load_dwordx2 v[50:51], v52, s[36:39], s94 offen offset:128 sc1\n\t"
      "s_sub_u32 s63, s63, 1\n\t"
      "s_cmp_lg_u32 s63, 0\n\t"
      "s_cbranch_scc1 LWM_0%=\n\t"
      "s_waitcnt vmcnt(0)\n\t"
      "LWM1_0%=:\n\t"
      "buffer_load_dwordx2 v[120:121], off, s[36:39], s64 sc1\n\t"
      "s_lshr_b32 s67, s67, 23\n\t"
      "s_add_u32 s67, s67, 5\n\t"
      "s_lshr_b32 s66, s66, 23\n\t"
      "s_add_u32 s66, s66, 129\n\t"
      "s_max_u32 s66, s66, 80\n\t"
      "s_mov_b32 s63, 0x2000\n\t"
      "LPOL0_0%=:\n\t"
      "s_waitcnt lgkmcnt(4)\n\t"
      "v_mfma_f32_16x16x32_bf16 v[32:35], v[0:3], v[8:11], v[32:35]\n\t"
      "v_mfma_f32_16x16x32_bf16 v[36:39], v[4:7], v[8:11], v[36:39]\n\t"
      "v_mfma_f32_16x16x32_bf16 v[40:43], v[0:3], v[12:15], v[40:43]\n\t"
      "v_mfma_f32_16x16x32_bf16 v[44:47], v[4:7], v[12:15], v[44:47]\n\t"
      "v_mov_b64 v[48:49], 0\n\t"
      "v_mov_b64 v[50:51], 0\n\t"
      "v_mov_b64 v[52:53], 0\n\t"
      "v_mov_b64 v[54:55], 0\n\t"
      "s_xor_b32 s69, s97, 1024\n\t"
      "s_xor_b32 s98, s97, 512\n\t"
      "s_xor_b32 s23, s97, 1536\n\t"
      "s_waitcnt lgkmcnt(0)\n\t"
      "v_mfma_f32_16x16x32_bf16 v[32:35], v[76:79], v[112:115], v[32:35]\n\t"
      "v_mfma_f32_16x16x32_bf16 v[36:39], v[80:83], v[112:115], v[36:39]\n\t"
      "v_mfma_f32_16x16x32_bf16 v[40:43], v[76:79], v[116:119], v[40:43]\n\t"
      "v_mfma_f32_16x16x32_bf16 v[44:47], v[80:83], v[116:119], v[44:47]\n\t"
      "s_mov_b64 exec, 1\n\t"
      "ds_write_b64 v109, v[110:111] offset:55392\n\t"
      "s_mov_b64 exec, -1\n\t"
      "s_nop 4\n\t"
      "v_cvt_pk_bf16_f32 v68, v32, v33\n\t"
      "v_cvt_pk_bf16_f32 v69, v34, v35\n\t"
      "v_cvt_pk_bf16_f32 v70, v36, v37\n\t"
      "v_cvt_pk_bf16_f32 v71, v38, v39\n\t"
      "v_cvt_pk_bf16_f32 v74, v40, v41\n\t"
      "v_cvt_pk_bf16_f32 v75, v42, v43\n\t"
      "v_cvt_pk_bf16_f32 v72, v44, v45\n\t"
      "v_cvt_pk_bf16_f32 v73, v46, v47\n\t"
      "v_mov_b64 v[32:33], 0\n\t"
      "v_mov_b64 v[34:35], 0\n\t"
      "v_mov_b64 v[36:37], 0\n\t"
      "v_mov_b64 v[38:39], 0\n\t"
      "v_mov_b64 v[40:41], 0\n\t"
      "v_mov_b64 v[42:43], 0\n\t"
      "v_mov_b64 v[44:45], 0\n\t"
      "v_mov_b64 v[46:47], 0\n\t"
      "s_waitcnt vmcnt(0)\n\t"
      "s_branch LGO_0%=\n\t"
      "LEARLY_0%=:\n\t"
      "s_cmp_eq_u32 s40, 54\n\t"
      "s_cbranch_scc1 LPW_0%=\n\t"
      "s_cmp_lt_i32 s40, 0\n\t"
      "s_waitcnt lgkmcnt(0)\n\t"
      "s_cbranch_scc1 LUTOK_0%=\n\t"
      "s_xor_b32 s43, s43, s99\n\t"
      "s_xor_b32 s68, s68, s99\n\t"
      "s_branch LROLE_0%=\n\t"
      "LPW_0%=:\n\t"
      "s_and_b32 s61, s51, 0x70c\n\t"
      "s_cmp_lg_u32 s61, 0x100\n\t"
      "s_cbranch_scc1 LROLE_0%=\n\t"
      "s_getpc_b64 s[92:93]\n\t"
      "LPX_0%=:\n\t"
      "s_add_u32 s92, s92, LCOLD%=-LPX_0%=+0\n\t"
      "s_addc_u32 s93, s93, (LCOLD%=-LPX_0%=+0)>>32\n\t"
      "v_lshlrev_b32 v99, 2, v127\n\t"
      "global_load_dword v98, v99, s[92:93]\n\t"
      "v_add_u32 v99, 4096, v99\n\t"
      "global_load_dword v98, v99, s[92:93]\n\t"
      "v_add_u32 v99, 4096, v99\n\t"
      "s_branch LROLE_0%=\n\t"
      "LSLOW_0%=:\n\t"
      "s_mov_b32 s63, 0x10000\n\t"
      "LSLOW1_0%=:\n\t"
      "buffer_load_dwordx2 v[92:93], off, s[36:39], s64 sc1\n\t"
      "s_waitcnt vmcnt(0)\n\t"
      "v_bitop3_b32 v92, v92, v93, s53 bitop3:0x7e\n\t"
      "s_nop 0\n\t"
      "v_readfirstlane_b32 s56, v92\n\t"
      "s_nop 1\n\t"
      "s_cmp_eq_u32 s56, 0\n\t"
      "s_cbranch_scc1 LSLOW2_0%=\n\t"
      "s_sleep 8\n\t"
      "s_sub_u32 s63, s63, 1\n\t"
      "s_cmp_lg_u32 s63, 0\n\t"
      "s_cbranch_scc1 LSLOW1_0%=\n\t"
      "LSLOW2_0%=:\n\t"
      "s_mov_b32 m0, s45\n\t"
      "s_sub_u32 s61, 204800, s71\n\t"
      "buffer_load_dwordx4 v65, s[36:39], s44 offen sc1 lds\n\t"
      "buffer_load_dwordx4 v65, s[36:39], s44 offen offset:1024 sc1 lds\n\t"
      "s_add_u32 m0, s55, s61\n\t"
      "s_nop 0\n\t"
      "buffer_load_dwordx4 v127, s[36:39], s46 offen sc1 lds\n\t"
      "buffer_load_dwordx4 v127, s[36:39], s46 offen offset:1024 sc1 lds\n\t"
      "s_branch LWIN1_0%=\n\t"
      "LMX_0%=:\n\t"
      "s_waitcnt lgkmcnt(0)\n\t"
      "s_nop 7\n\t"
      "v_smfmac_f32_16x16x64_bf16 v[52:55], v[20:23], v[232:239], v122\n\t"
      "v_smfmac_f32_16x16x64_bf16 v[52:55], v[24:27], v[240:247], v122\n\t"
      "v_smfmac_f32_16x16x64_bf16 v[52:55], v[28:31], v[248:255], v122\n\t"
      "s_nop 7\n\t"
      "v_add_f32 v53, v53, v55\n\t"
      "v_add_f32_e64 v52, v52, v54\n\t"
      "v_cvt_pk_bf16_f32 v66, v52, v53\n\t"
      "s_add_u32 s62, s61, 0x83400000\n\t"
      "v_mov_b32_e64 v126, s62\n\t"
      "v_max3_f32 v82, v112, v113, v114\n\t"
      "v_max_f32 v82, v82, v115\n\t"
      "s_nop 1\n\t"
      "v_max_f32_dpp v82, v82, v82 quad_perm:[2,3,0,1] row_mask:0xf bank_mask:0xf\n\t"
      "v_max3_f32 v83, v82, v120, v121\n\t"
      "v_max3_f32 v60, v82, v120, v121\n\t"
      "s_nop 1\n\t"
      "v_permlane16_swap_b32 v83, v60\n\t"
      "v_max_f32 v61, v83, v60\n\t"
      "v_max_f32 v60, v83, v60\n\t"
      "s_nop 1\n\t"
      "v_permlane32_swap_b32 v61, v60\n\t"
      "v_max_f32 v83, v61, v60\n\t"
      "v_sub_f32 v112, v112, v83\n\t"
      "v_sub_f32 v113, v113, v83\n\t"
      "v_sub_f32 v114, v114, v83\n\t"
      "v_sub_f32 v115, v115, v83\n\t"
      "v_sub_f32 v120, v120, v83\n\t"
      "v_sub_f32 v121, v121, v83\n\t"
      "v_exp_f32_e64 v56, v112\n\t"
      "v_exp_f32 v57, v113\n\t"
      "v_exp_f32_e64 v58, v114\n\t"
      "v_exp_f32 v59, v115\n\t"
      "v_exp_f32 v80, v120\n\t"
      "v_exp_f32 v81, v121\n\t"
      "v_cvt_pk_bf16_f32 v96, v56, v57\n\t"
      "v_cvt_pk_bf16_f32 v97, v58, v59\n\t"
      "v_cvt_pk_bf16_f32 v108, v80, v81\n\t"
      "v_mov_b32_dpp v98, v96 quad_perm:[2,3,0,1] row_mask:0xf bank_mask:0xf\n\t"
      "v_mov_b32_dpp v99, v97 quad_perm:[2,3,0,1] row_mask:0xf bank_mask:0xf\n\t"
      "v_mfma_f32_16x16x32_bf16 v[48:51], v[108:111], v[88:91], 0\n\t"
      "s_nop 0\n\t"
      "v_mfma_f32_16x16x32_bf16 v[48:51], v[96:99], v[88:91], v[48:51]\n\t"
      "v_mfma_f32_16x16x32_bf16 v[84:87], v[108:111], v[64:67], 0\n\t"
      "v_mfma_f32_16x16x32_bf16 v[84:87], v[96:99], v[68:71], v[84:87]\n\t"
      "v_mfma_f32_16x16x32_bf16 v[92:95], v[96:99], v[72:75], 0\n\t"
      "v_mfma_f32_16x16x32_bf16 v[92:95], v[108:111], v[66:69], v[92:95]\n\t"
      "s_nop 3\n\t"
      "v_rcp_f32_e64 v48, v48\n\t"
      "v_rcp_f32 v49, v49\n\t"
      "v_fma_f32 v84, v84, v48, s96\n\t"
      "buffer_atomic_add v84, v123, s[36:39], s97 offen\n\t"
      "v_fma_f32 v85, v85, v49, s96\n\t"
      "buffer_atomic_add v85, v123, s[36:39], s98 offen\n\t"
      "v_fma_f32 v94, v94, v48, s96\n\t"
      "buffer_atomic_add v94, v123, s[36:39], s69 offen\n\t"
      "v_fma_f32 v95, v95, v49, s96\n\t"
      "buffer_atomic_add v95, v123, s[36:39], s23 offen\n\t"
      "s_add_u32 s97, s97, 0x8000\n\t"
      "s_branch LWIN_0%=\n\t"
      "LR0_0%=:\n\t"
      "buffer_load_dwordx4 v[84:87], v60, s[36:39], s43 offen sc1\n\t"
      "buffer_load_dwordx4 v[88:91], v60, s[36:39], s43 offen offset:1024 sc1\n\t"
      "s_waitcnt vmcnt(2)\n\t"
      "buffer_load_dwordx4 v[92:95], v60, s[36:39], s68 offen sc1\n\t"
      "buffer_load_dwordx4 v[96:99], v60, s[36:39], s68 offen offset:1024 sc1\n\t"
      "s_sub_u32 s63, s63, 1\n\t"
      "s_mov_b64 exec, 1\n\t"
      "buffer_store_dword v109, off, s[36:39], s52 sc1\n\t"
      "s_mov_b64 exec, -1\n\t"
      "s_waitcnt vmcnt(3) lgkmcnt(0)\n\t"
      "v_bitop3_b32 v64, v84, v85, v86 bitop3:0x80\n\t"
      "v_bitop3_b32 v64, v64, v87, v88 bitop3:0x80\n\t"
      "v_bitop3_b32 v64, v64, v89, v90 bitop3:0x80\n\t"
      "v_bitop3_b32 v64, v64, v91, s58 bitop3:0x80\n\t"
      "v_cmp_eq_u32 vcc, 0, v64\n\t"
      "v_pk_fma_f32 v[4:5], v[84:85], s[56:57], v[126:127] op_sel:[0,1,0] op_sel_hi:[1,1,0]\n\t"
      "v_pk_fma_f32 v[6:7], v[86:87], s[56:57], v[126:127] op_sel:[0,1,0] op_sel_hi:[1,1,0]\n\t"
      "v_cvt_pk_bf16_f32 v0, v4, v5\n\t"
      "v_cvt_pk_bf16_f32 v1, v6, v7\n\t"
      "ds_write_b64 v127, v[0:1] offset:40960\n\t"
      "v_pk_fma_f32 v[8:9], v[88:89], s[56:57], v[126:127] op_sel:[0,1,0] op_sel_hi:[1,1,0]\n\t"
      "v_pk_fma_f32 v[10:11], v[90:91], s[56:57], v[126:127] op_sel:[0,1,0] op_sel_hi:[1,1,0]\n\t"
      "v_cvt_pk_bf16_f32 v2, v8, v9\n\t"
      "v_cvt_pk_bf16_f32 v3, v10, v11\n\t"
      "ds_write_b64 v127, v[2:3] offset:40968\n\t"
      "s_cmp_le_i32 s63, 0\n\t"
      "s_waitcnt lgkmcnt(0)\n\t"
      "s_cbranch_scc1 LRZ_0%=\n\t"
      "s_cbranch_vccnz LR0_0%=\n\t"
      "LRZ_0%=:\n\t"
      "v_mov_b64 v[32:33], 0\n\t"
      "v_mov_b64 v[34:35], 0\n\t"
      "v_mov_b64 v[36:37], 0\n\t"
      "v_mov_b64 v[38:39], 0\n\t"
      "v_mov_b64 v[40:41], 0\n\t"
      "v_mov_b64 v[42:43], 0\n\t"
      "v_mov_b64 v[44:45], 0\n\t"
      "v_mov_b64 v[46:47], 0\n\t"
      "v_mov_b64 v[48:49], 0\n\t"
      "v_mov_b64 v[50:51], 0\n\t"
      "v_mov_b64 v[52:53], 0\n\t"
      "v_mov_b64 v[54:55], 0\n\t"
      "v_mfma_f32_16x16x32_bf16 v[56:59], v[0:3], v[0:3], 0\n\t"
      "v_smfmac_f32_16x16x64_bf16 v[36:39], v[0:3], a[64:71], v122\n\t"
      "v_smfmac_f32_16x16x64_bf16 v[44:47], v[0:3], a[192:199], v122\n\t"
      "s_branch LC0_0%=\n\t"
      "LUTOK_1%=:\n\t"
      "v_add_u32 v104, s71, v100\n\t"
      "ds_read_b128 v[76:79], v104\n\t"
      "ds_read_b128 v[80:83], v104 offset:1024\n\t"
      "ds_read_b128 v[112:115], v100\n\t"
      "ds_read_b128 v[116:119], v100 offset:1024\n\t"
      "v_xor_b32 v101, 2048, v104\n\t"
      "v_xor_b32 v102, 4096, v104\n\t"
      "v_xor_b32 v103, 6144, v104\n\t"
      "ds_read_b128 v[16:19], v101\n\t"
      "ds_read_b128 v[20:23], v101 offset:1024\n\t"
      "ds_read_b128 v[24:27], v100 offset:8192\n\t"
      "ds_read_b128 v[28:31], v100 offset:9216\n\t"
      "ds_read_b128 v[0:3], v102\n\t"
      "ds_read_b128 v[4:7], v102 offset:1024\n\t"
      "ds_read_b128 v[8:11], v100 offset:16384\n\t"
      "ds_read_b128 v[12:15], v100 offset:17408\n\t"
      "s_mov_b32 s28, s52\n\t"
      "s_add_u32 m0, s55, s71\n\t"
      "s_sub_u32 s71, 204800, s71\n\t"
      "s_add_u32 s40, s40, 1\n\t"
      "s_cmp_lt_u32 s40, 63\n\t"
      "s_cselect_b32 s61, 786432, 0\n\t"
      "s_cselect_b32 s60, s58, 0\n\t"
      "s_add_u32 s44, s44, s61\n\t"
      "s_cmp_lt_u32 s40, 62\n\t"
      "s_cselect_b32 s61, 786432, 0\n\t"
      "s_add_u32 s46, s46, s61\n\t"
      "s_add_u32 s61, s40, 3\n\t"
      "s_and_b32 s62, s61, 3\n\t"
      "s_cselect_b32 s62, 16, 2000\n\t"
      "s_cmp_lt_u32 s61, 64\n\t"
      "s_cselect_b32 s62, s62, 0\n\t"
      "s_add_u32 s50, s50, s62\n\t"
      "s_mov_b32 s63, 0x2000\n\t"
      "s_waitcnt lgkmcnt(8)\n\t"
      "v_mfma_f32_16x16x32_bf16 v[32:35], v[76:79], v[112:115], 0\n\t"
      "v_mfma_f32_16x16x32_bf16 v[36:39], v[80:83], v[112:115], 0\n\t"
      "v_mfma_f32_16x16x32_bf16 v[40:43], v[76:79], v[116:119], 0\n\t"
      "v_mfma_f32_16x16x32_bf16 v[44:47], v[80:83], v[116:119], 0\n\t"
      "ds_read_b128 v[76:79], v103\n\t"
      "ds_read_b128 v[80:83], v103 offset:1024\n\t"
      "ds_read_b128 v[112:115], v100 offset:24576\n\t"
      "ds_read_b128 v[116:119], v100 offset:25600\n\t"
      "s_waitcnt lgkmcnt(8)\n\t"
      "v_mfma_f32_16x16x32_bf16 v[32:35], v[16:19], v[24:27], v[32:35]\n\t"
      "v_mfma_f32_16x16x32_bf16 v[36:39], v[20:23], v[24:27], v[36:39]\n\t"
      "v_mfma_f32_16x16x32_bf16 v[40:43], v[16:19], v[28:31], v[40:43]\n\t"
      "v_mfma_f32_16x16x32_bf16 v[44:47], v[20:23], v[28:31], v[44:47]\n\t"
      "s_branch LGATE_1%=\n\t"
      "LFIRST_1%=:\n\t"
      "s_cmp_eq_u32 s40, 0\n\t"
      "s_cbranch_scc1 LWM0_1%=\n\t"
      "s_barrier\n\t"
      "ds_read_b32 v99, v109 offset:55392\n\t"
      "s_waitcnt lgkmcnt(0)\n\t"
      "v_readfirstlane_b32 s61, v99\n\t"
      "s_nop 1\n\t"
      "s_cmp_gt_i32 s61, 0\n\t"
      "s_cselect_b32 s99, 0x2000, 0\n\t"
      "s_xor_b32 s97, s97, s99\n\t"
      "s_barrier\n\t"
      "s_mov_b32 s63, 0x2000\n\t"
      "s_branch LPOLL_1%=\n\t"
      "LWM0_1%=:\n\t"
      "s_mov_b32 s63, 0x10000\n\t"
      "LWM_1%=:\n\t"
      "s_waitcnt vmcnt(0) lgkmcnt(0)\n\t"
      "v_bitop3_b32 v49, v49, v51, s53 bitop3:0x7e\n\t"
      "v_max_u32 v48, v48, v50\n\t"
      "v_cmp_ne_u32 vcc, 0, v49\n\t"
      "s_nop 1\n\t"
      "v_max_u32_dpp v48, v48, v48 quad_perm:[1,0,3,2] row_mask:0xf bank_mask:0xf\n\t"
      "s_nop 1\n\t"
      "v_max_u32_dpp v48, v48, v48 quad_perm:[2,3,0,1] row_mask:0xf bank_mask:0xf\n\t"
      "s_nop 1\n\t"
      "v_max_u32_dpp v48, v48, v48 row_ror:4 row_mask:0xf bank_mask:0xf\n\t"
      "s_nop 1\n\t"
      "v_max_u32_dpp v48, v48, v48 row_ror:8 row_mask:0xf bank_mask:0xf\n\t"
      "v_mov_b32 v49, v48\n\t"
      "s_nop 1\n\t"
      "v_permlane16_swap_b32 v48, v49\n\t"
      "v_max_u32 v48, v48, v49\n\t"
      "v_mov_b32 v49, v48\n\t"
      "s_nop 1\n\t"
      "v_permlane32_swap_b32 v48, v49\n\t"
      "v_max_u32 v48, v48, v49\n\t"
      "v_mul_f32 v49, 0x40a00000, v48\n\t"
      "s_nop 0\n\t"
      "v_readfirstlane_b32 s67, v49\n\t"
      "v_mul_f32 v49, 0x43030000, v49\n\t"
      "s_nop 0\n\t"
      "v_readfirstlane_b32 s66, v49\n\t"
      "s_cbranch_vccz LWM1_1%=\n\t"
      "s_sleep 4\n\t"
      "buffer_load_dwordx2 v[48:49], v52, s[36:39], s94 offen sc1\n\t"
      "buffer_load_dwordx2 v[50:51], v52, s[36:39], s94 offen offset:128 sc1\n\t"
      "s_sub_u32 s63, s63, 1\n\t"
      "s_cmp_lg_u32 s63, 0\n\t"
      "s_cbranch_scc1 LWM_1%=\n\t"
      "s_waitcnt vmcnt(0)\n\t"
      "LWM1_1%=:\n\t"
      "buffer_load_dwordx2 v[120:121], off, s[36:39], s64 sc1\n\t"
      "s_lshr_b32 s67, s67, 23\n\t"
      "s_add_u32 s67, s67, 5\n\t"
      "s_lshr_b32 s66, s66, 23\n\t"
      "s_add_u32 s66, s66, 129\n\t"
      "s_max_u32 s66, s66, 80\n\t"
      "s_mov_b32 s63, 0x2000\n\t"
      "LPOL0_1%=:\n\t"
      "s_waitcnt lgkmcnt(4)\n\t"
      "v_mfma_f32_16x16x32_bf16 v[32:35], v[0:3], v[8:11], v[32:35]\n\t"
      "v_mfma_f32_16x16x32_bf16 v[36:39], v[4:7], v[8:11], v[36:39]\n\t"
      "v_mfma_f32_16x16x32_bf16 v[40:43], v[0:3], v[12:15], v[40:43]\n\t"
      "v_mfma_f32_16x16x32_bf16 v[44:47], v[4:7], v[12:15], v[44:47]\n\t"
      "v_mov_b64 v[48:49], 0\n\t"
      "v_mov_b64 v[50:51], 0\n\t"
      "v_mov_b64 v[52:53], 0\n\t"
      "v_mov_b64 v[54:55], 0\n\t"
      "s_xor_b32 s69, s97, 1024\n\t"
      "s_xor_b32 s98, s97, 512\n\t"
      "s_xor_b32 s23, s97, 1536\n\t"
      "s_waitcnt lgkmcnt(0)\n\t"
      "v_mfma_f32_16x16x32_bf16 v[32:35], v[76:79], v[112:115], v[32:35]\n\t"
      "v_mfma_f32_16x16x32_bf16 v[36:39], v[80:83], v[112:115], v[36:39]\n\t"
      "v_mfma_f32_16x16x32_bf16 v[40:43], v[76:79], v[116:119], v[40:43]\n\t"
      "v_mfma_f32_16x16x32_bf16 v[44:47], v[80:83], v[116:119], v[44:47]\n\t"
      "s_mov_b64 exec, 1\n\t"
      "ds_write_b64 v109, v[110:111] offset:55392\n\t"
      "s_mov_b64 exec, -1\n\t"
      "s_nop 4\n\t"
      "v_cvt_pk_bf16_f32 v68, v32, v33\n\t"
      "v_cvt_pk_bf16_f32 v69, v34, v35\n\t"
      "v_cvt_pk_bf16_f32 v70, v36, v37\n\t"
      "v_cvt_pk_bf16_f32 v71, v38, v39\n\t"
      "v_cvt_pk_bf16_f32 v74, v40, v41\n\t"
      "v_cvt_pk_bf16_f32 v75, v42, v43\n\t"
      "v_cvt_pk_bf16_f32 v72, v44, v45\n\t"
      "v_cvt_pk_bf16_f32 v73, v46, v47\n\t"
      "v_mov_b64 v[32:33], 0\n\t"
      "v_mov_b64 v[34:35], 0\n\t"
      "v_mov_b64 v[36:37], 0\n\t"
      "v_mov_b64 v[38:39], 0\n\t"
      "v_mov_b64 v[40:41], 0\n\t"
      "v_mov_b64 v[42:43], 0\n\t"
      "v_mov_b64 v[44:45], 0\n\t"
      "v_mov_b64 v[46:47], 0\n\t"
      "s_waitcnt vmcnt(0)\n\t"
      "s_branch LGO_1%=\n\t"
      "LEARLY_1%=:\n\t"
      "s_cmp_eq_u32 s40, 54\n\t"
      "s_cbranch_scc1 LROLE_1%=\n\t"
      "s_cmp_lt_i32 s40, 0\n\t"
      "s_waitcnt lgkmcnt(0)\n\t"
      "s_cbranch_scc1 LUTOK_1%=\n\t"
      "s_xor_b32 s43, s43, s99\n\t"
      "s_xor_b32 s68, s68, s99\n\t"
      "s_branch LROLE_1%=\n\t"
      "LSLOW_1%=:\n\t"
      "s_mov_b32 s63, 0x10000\n\t"
      "LSLOW1_1%=:\n\t"
      "buffer_load_dwordx2 v[92:93], off, s[36:39], s64 sc1\n\t"
      "s_waitcnt vmcnt(0)\n\t"
      "v_bitop3_b32 v92, v92, v93, s53 bitop3:0x7e\n\t"
      "s_nop 0\n\t"
      "v_readfirstlane_b32 s56, v92\n\t"
      "s_nop 1\n\t"
      "s_cmp_eq_u32 s56, 0\n\t"
      "s_cbranch_scc1 LSLOW2_1%=\n\t"
      "s_sleep 8\n\t"
      "s_sub_u32 s63, s63, 1\n\t"
      "s_cmp_lg_u32 s63, 0\n\t"
      "s_cbranch_scc1 LSLOW1_1%=\n\t"
      "LSLOW2_1%=:\n\t"
      "s_mov_b32 m0, s45\n\t"
      "s_sub_u32 s61, 204800, s71\n\t"
      "buffer_load_dwordx4 v65, s[36:39], s44 offen sc1 lds\n\t"
      "buffer_load_dwordx4 v65, s[36:39], s44 offen offset:1024 sc1 lds\n\t"
      "s_add_u32 m0, s55, s61\n\t"
      "s_nop 0\n\t"
      "buffer_load_dwordx4 v127, s[36:39], s46 offen sc1 lds\n\t"
      "buffer_load_dwordx4 v127, s[36:39], s46 offen offset:1024 sc1 lds\n\t"
      "s_branch LWIN1_1%=\n\t"
      "LMX_1%=:\n\t"
      "s_waitcnt lgkmcnt(0)\n\t"
      "s_nop 7\n\t"
      "v_smfmac_f32_16x16x64_bf16 v[52:55], v[16:19], v[224:231], v122\n\t"
      "v_smfmac_f32_16x16x64_bf16 v[52:55], v[28:31], v[248:255], v122\n\t"
      "v_smfmac_f32_16x16x64_bf16 v[52:55], v[24:27], v[240:247], v122\n\t"
      "s_nop 7\n\t"
      "v_add_f32 v53, v53, v55\n\t"
      "v_add_f32_e64 v52, v52, v54\n\t"
      "v_cvt_pk_bf16_f32 v66, v52, v53\n\t"
      "s_add_u32 s62, s61, 0x83400000\n\t"
      "v_mov_b32_e64 v126, s62\n\t"
      "v_max3_f32 v82, v112, v113, v114\n\t"
      "v_max_f32 v82, v82, v115\n\t"
      "s_nop 1\n\t"
      "v_max_f32_dpp v82, v82, v82 quad_perm:[2,3,0,1] row_mask:0xf bank_mask:0xf\n\t"
      "v_max3_f32 v83, v82, v120, v121\n\t"
      "v_max3_f32 v60, v82, v120, v121\n\t"
      "s_nop 1\n\t"
      "v_permlane16_swap_b32 v83, v60\n\t"
      "v_max_f32 v61, v83, v60\n\t"
      "v_max_f32 v60, v83, v60\n\t"
      "s_nop 1\n\t"
      "v_permlane32_swap_b32 v61, v60\n\t"
      "v_max_f32 v83, v61, v60\n\t"
      "v_sub_f32 v112, v112, v83\n\t"
      "v_sub_f32 v113, v113, v83\n\t"
      "v_sub_f32 v114, v114, v83\n\t"
      "v_sub_f32 v115, v115, v83\n\t"
      "v_sub_f32 v120, v120, v83\n\t"
      "v_sub_f32 v121, v121, v83\n\t"
      "v_exp_f32_e64 v56, v112\n\t"
      "v_exp_f32 v57, v113\n\t"
      "v_exp_f32_e64 v58, v114\n\t"
      "v_exp_f32 v59, v115\n\t"
      "v_exp_f32 v80, v120\n\t"
      "v_exp_f32 v81, v121\n\t"
      "v_cvt_pk_bf16_f32 v96, v56, v57\n\t"
      "v_cvt_pk_bf16_f32 v97, v58, v59\n\t"
      "v_cvt_pk_bf16_f32 v108, v80, v81\n\t"
      "v_mov_b32_dpp v98, v96 quad_perm:[2,3,0,1] row_mask:0xf bank_mask:0xf\n\t"
      "v_mov_b32_dpp v99, v97 quad_perm:[2,3,0,1] row_mask:0xf bank_mask:0xf\n\t"
      "v_mfma_f32_16x16x32_bf16 v[48:51], v[108:111], v[88:91], 0\n\t"
      "s_nop 0\n\t"
      "v_mfma_f32_16x16x32_bf16 v[48:51], v[96:99], v[88:91], v[48:51]\n\t"
      "v_mfma_f32_16x16x32_bf16 v[84:87], v[108:111], v[64:67], 0\n\t"
      "v_mfma_f32_16x16x32_bf16 v[84:87], v[96:99], v[68:71], v[84:87]\n\t"
      "v_mfma_f32_16x16x32_bf16 v[92:95], v[96:99], v[72:75], 0\n\t"
      "v_mfma_f32_16x16x32_bf16 v[92:95], v[108:111], v[66:69], v[92:95]\n\t"
      "s_nop 3\n\t"
      "v_rcp_f32_e64 v48, v48\n\t"
      "v_rcp_f32 v49, v49\n\t"
      "v_fma_f32 v84, v84, v48, s96\n\t"
      "buffer_atomic_add v84, v123, s[36:39], s97 offen\n\t"
      "v_fma_f32 v85, v85, v49, s96\n\t"
      "buffer_atomic_add v85, v123, s[36:39], s98 offen\n\t"
      "v_fma_f32 v94, v94, v48, s96\n\t"
      "buffer_atomic_add v94, v123, s[36:39], s69 offen\n\t"
      "v_fma_f32 v95, v95, v49, s96\n\t"
      "buffer_atomic_add v95, v123, s[36:39], s23 offen\n\t"
      "s_add_u32 s97, s97, 0x8000\n\t"
      "s_branch LWIN_1%=\n\t"
      "LR0_1%=:\n\t"
      "buffer_load_dwordx4 v[84:87], v60, s[36:39], s43 offen sc1\n\t"
      "buffer_load_dwordx4 v[88:91], v60, s[36:39], s43 offen offset:1024 sc1\n\t"
      "s_waitcnt vmcnt(2)\n\t"
      "buffer_load_dwordx4 v[92:95], v60, s[36:39], s68 offen sc1\n\t"
      "buffer_load_dwordx4 v[96:99], v60, s[36:39], s68 offen offset:1024 sc1\n\t"
      "s_sub_u32 s63, s63, 1\n\t"
      "s_mov_b64 exec, 1\n\t"
      "buffer_store_dword v109, off, s[36:39], s52 sc1\n\t"
      "s_mov_b64 exec, -1\n\t"
      "s_waitcnt vmcnt(3) lgkmcnt(0)\n\t"
      "v_bitop3_b32 v64, v84, v85, v86 bitop3:0x80\n\t"
      "v_bitop3_b32 v64, v64, v87, v88 bitop3:0x80\n\t"
      "v_bitop3_b32 v64, v64, v89, v90 bitop3:0x80\n\t"
      "v_bitop3_b32 v64, v64, v91, s58 bitop3:0x80\n\t"
      "v_cmp_eq_u32 vcc, 0, v64\n\t"
      "v_pk_fma_f32 v[0:1], v[84:85], s[56:57], v[126:127] op_sel:[0,1,0] op_sel_hi:[1,1,0]\n\t"
      "v_pk_fma_f32 v[2:3], v[86:87], s[56:57], v[126:127] op_sel:[0,1,0] op_sel_hi:[1,1,0]\n\t"
      "v_cvt_pk_bf16_f32 v4, v0, v1\n\t"
      "v_cvt_pk_bf16_f32 v5, v2, v3\n\t"
      "ds_write_b64 v127, v[4:5] offset:41984\n\t"
      "v_pk_fma_f32 v[12:13], v[88:89], s[56:57], v[126:127] op_sel:[0,1,0] op_sel_hi:[1,1,0]\n\t"
      "v_pk_fma_f32 v[14:15], v[90:91], s[56:57], v[126:127] op_sel:[0,1,0] op_sel_hi:[1,1,0]\n\t"
      "v_cvt_pk_bf16_f32 v6, v12, v13\n\t"
      "v_cvt_pk_bf16_f32 v7, v14, v15\n\t"
      "ds_write_b64 v127, v[6:7] offset:41992\n\t"
      "s_cmp_le_i32 s63, 0\n\t"
      "s_waitcnt lgkmcnt(0)\n\t"
      "s_cbranch_scc1 LRZ_1%=\n\t"
      "s_cbranch_vccnz LR0_1%=\n\t"
      "LRZ_1%=:\n\t"
      "v_mov_b64 v[32:33], 0\n\t"
      "v_mov_b64 v[34:35], 0\n\t"
      "v_mov_b64 v[36:37], 0\n\t"
      "v_mov_b64 v[38:39], 0\n\t"
      "v_mov_b64 v[40:41], 0\n\t"
      "v_mov_b64 v[42:43], 0\n\t"
      "v_mov_b64 v[44:45], 0\n\t"
      "v_mov_b64 v[46:47], 0\n\t"
      "v_mov_b64 v[48:49], 0\n\t"
      "v_mov_b64 v[50:51], 0\n\t"
      "v_mov_b64 v[52:53], 0\n\t"
      "v_mov_b64 v[54:55], 0\n\t"
      "v_mfma_f32_16x16x32_bf16 v[56:59], v[4:7], v[4:7], 0\n\t"
      "v_smfmac_f32_16x16x64_bf16 v[36:39], v[4:7], a[72:79], v122\n\t"
      "v_smfmac_f32_16x16x64_bf16 v[44:47], v[4:7], a[200:207], v122\n\t"
      "s_branch LC0_1%=\n\t"
      "LUTOK_2%=:\n\t"
      "v_add_u32 v104, s71, v100\n\t"
      "ds_read_b128 v[76:79], v104\n\t"
      "ds_read_b128 v[80:83], v104 offset:1024\n\t"
      "ds_read_b128 v[112:115], v100\n\t"
      "ds_read_b128 v[116:119], v100 offset:1024\n\t"
      "v_xor_b32 v101, 2048, v104\n\t"
      "v_xor_b32 v102, 4096, v104\n\t"
      "v_xor_b32 v103, 6144, v104\n\t"
      "ds_read_b128 v[0:3], v101\n\t"
      "ds_read_b128 v[4:7], v101 offset:1024\n\t"
      "ds_read_b128 v[8:11], v100 offset:8192\n\t"
      "ds_read_b128 v[12:15], v100 offset:9216\n\t"
      "ds_read_b128 v[16:19], v102\n\t"
      "ds_read_b128 v[20:23], v102 offset:1024\n\t"
      "ds_read_b128 v[24:27], v100 offset:16384\n\t"
      "ds_read_b128 v[28:31], v100 offset:17408\n\t"
      "s_add_u32 m0, s55, s71\n\t"
      "s_sub_u32 s71, 204800, s71\n\t"
      "s_add_u32 s40, s40, 1\n\t"
      "s_cmp_lt_u32 s40, 63\n\t"
      "s_cselect_b32 s61, 786432, 0\n\t"
      "s_cselect_b32 s60, s58, 0\n\t"
      "s_add_u32 s44, s44, s61\n\t"
      "s_cmp_lt_u32 s40, 62\n\t"
      "s_cselect_b32 s61, 786432, 0\n\t"
      "s_add_u32 s46, s46, s61\n\t"
      "s_add_u32 s61, s40, 3\n\t"
      "s_and_b32 s62, s61, 3\n\t"
      "s_cselect_b32 s62, 16, 2000\n\t"
      "s_cmp_lt_u32 s61, 64\n\t"
      "s_cselect_b32 s62, s62, 0\n\t"
      "s_add_u32 s50, s50, s62\n\t"
      "s_mov_b32 s63, 0x2000\n\t"
      "s_waitcnt lgkmcnt(8)\n\t"
      "v_mfma_f32_16x16x32_bf16 v[32:35], v[76:79], v[112:115], 0\n\t"
      "v_mfma_f32_16x16x32_bf16 v[36:39], v[80:83], v[112:115], 0\n\t"
      "v_mfma_f32_16x16x32_bf16 v[40:43], v[76:79], v[116:119], 0\n\t"
      "v_mfma_f32_16x16x32_bf16 v[44:47], v[80:83], v[116:119], 0\n\t"
      "ds_read_b128 v[76:79], v103\n\t"
      "ds_read_b128 v[80:83], v103 offset:1024\n\t"
      "ds_read_b128 v[112:115], v100 offset:24576\n\t"
      "ds_read_b128 v[116:119], v100 offset:25600\n\t"
      "s_branch LGATE_2%=\n\t"
      "LFIRST_2%=:\n\t"
      "s_cmp_eq_u32 s40, 0\n\t"
      "s_cbranch_scc1 LWM0_2%=\n\t"
      "s_barrier\n\t"
      "ds_read_b32 v99, v109 offset:55392\n\t"
      "s_waitcnt lgkmcnt(0)\n\t"
      "v_readfirstlane_b32 s61, v99\n\t"
      "s_nop 1\n\t"
      "s_cmp_gt_i32 s61, 0\n\t"
      "s_cselect_b32 s99, 0x2000, 0\n\t"
      "s_xor_b32 s97, s97, s99\n\t"
      "s_barrier\n\t"
      "s_mov_b32 s63, 0x2000\n\t"
      "s_branch LPOLL_2%=\n\t"
      "LWM0_2%=:\n\t"
      "s_mov_b32 s63, 0x10000\n\t"
      "LWM_2%=:\n\t"
      "s_waitcnt vmcnt(0) lgkmcnt(0)\n\t"
      "v_bitop3_b32 v49, v49, v51, s53 bitop3:0x7e\n\t"
      "v_max_u32 v48, v48, v50\n\t"
      "v_cmp_ne_u32 vcc, 0, v49\n\t"
      "s_nop 1\n\t"
      "v_max_u32_dpp v48, v48, v48 quad_perm:[1,0,3,2] row_mask:0xf bank_mask:0xf\n\t"
      "s_nop 1\n\t"
      "v_max_u32_dpp v48, v48, v48 quad_perm:[2,3,0,1] row_mask:0xf bank_mask:0xf\n\t"
      "s_nop 1\n\t"
      "v_max_u32_dpp v48, v48, v48 row_ror:4 row_mask:0xf bank_mask:0xf\n\t"
      "s_nop 1\n\t"
      "v_max_u32_dpp v48, v48, v48 row_ror:8 row_mask:0xf bank_mask:0xf\n\t"
      "v_mov_b32 v49, v48\n\t"
      "s_nop 1\n\t"
      "v_permlane16_swap_b32 v48, v49\n\t"
      "v_max_u32 v48, v48, v49\n\t"
      "v_mov_b32 v49, v48\n\t"
      "s_nop 1\n\t"
      "v_permlane32_swap_b32 v48, v49\n\t"
      "v_max_u32 v48, v48, v49\n\t"
      "v_mul_f32 v49, 0x40a00000, v48\n\t"
      "s_nop 0\n\t"
      "v_readfirstlane_b32 s67, v49\n\t"
      "v_mul_f32 v49, 0x43030000, v49\n\t"
      "s_nop 0\n\t"
      "v_readfirstlane_b32 s66, v49\n\t"
      "s_cbranch_vccz LWM1_2%=\n\t"
      "s_sleep 4\n\t"
      "buffer_load_dwordx2 v[48:49], v52, s[36:39], s94 offen sc1\n\t"
      "buffer_load_dwordx2 v[50:51], v52, s[36:39], s94 offen offset:128 sc1\n\t"
      "s_sub_u32 s63, s63, 1\n\t"
      "s_cmp_lg_u32 s63, 0\n\t"
      "s_cbranch_scc1 LWM_2%=\n\t"
      "s_waitcnt vmcnt(0)\n\t"
      "LWM1_2%=:\n\t"
      "buffer_load_dwordx2 v[120:121], off, s[36:39], s64 sc1\n\t"
      "s_lshr_b32 s67, s67, 23\n\t"
      "s_add_u32 s67, s67, 5\n\t"
      "s_lshr_b32 s66, s66, 23\n\t"
      "s_add_u32 s66, s66, 129\n\t"
      "s_max_u32 s66, s66, 80\n\t"
      "s_mov_b32 s63, 0x2000\n\t"
      "LPOL0_2%=:\n\t"
      "s_waitcnt lgkmcnt(8)\n\t"
      "v_mfma_f32_16x16x32_bf16 v[32:35], v[0:3], v[8:11], v[32:35]\n\t"
      "v_mfma_f32_16x16x32_bf16 v[36:39], v[4:7], v[8:11], v[36:39]\n\t"
      "v_mfma_f32_16x16x32_bf16 v[40:43], v[0:3], v[12:15], v[40:43]\n\t"
      "v_mfma_f32_16x16x32_bf16 v[44:47], v[4:7], v[12:15], v[44:47]\n\t"
      "v_mov_b64 v[48:49], 0\n\t"
      "v_mov_b64 v[50:51], 0\n\t"
      "v_mov_b64 v[52:53], 0\n\t"
      "v_mov_b64 v[54:55], 0\n\t"
      "s_xor_b32 s69, s97, 1024\n\t"
      "s_xor_b32 s98, s97, 512\n\t"
      "s_xor_b32 s23, s97, 1536\n\t"
      "s_waitcnt lgkmcnt(4)\n\t"
      "v_mfma_f32_16x16x32_bf16 v[32:35], v[16:19], v[24:27], v[32:35]\n\t"
      "v_mfma_f32_16x16x32_bf16 v[36:39], v[20:23], v[24:27], v[36:39]\n\t"
      "v_mfma_f32_16x16x32_bf16 v[40:43], v[16:19], v[28:31], v[40:43]\n\t"
      "v_mfma_f32_16x16x32_bf16 v[44:47], v[20:23], v[28:31], v[44:47]\n\t"
      "s_waitcnt lgkmcnt(0)\n\t"
      "v_mfma_f32_16x16x32_bf16 v[32:35], v[76:79], v[112:115], v[32:35]\n\t"
      "v_mfma_f32_16x16x32_bf16 v[36:39], v[80:83], v[112:115], v[36:39]\n\t"
      "v_mfma_f32_16x16x32_bf16 v[40:43], v[76:79], v[116:119], v[40:43]\n\t"
      "v_mfma_f32_16x16x32_bf16 v[44:47], v[80:83], v[116:119], v[44:47]\n\t"
      "s_mov_b64 exec, 1\n\t"
      "ds_write_b64 v109, v[110:111] offset:55392\n\t"
      "s_mov_b64 exec, -1\n\t"
      "s_nop 4\n\t"
      "v_cvt_pk_bf16_f32 v68, v32, v33\n\t"
      "v_cvt_pk_bf16_f32 v69, v34, v35\n\t"
      "v_cvt_pk_bf16_f32 v70, v36, v37\n\t"
      "v_cvt_pk_bf16_f32 v71, v38, v39\n\t"
      "v_cvt_pk_bf16_f32 v74, v40, v41\n\t"
      "v_cvt_pk_bf16_f32 v75, v42, v43\n\t"
      "v_cvt_pk_bf16_f32 v72, v44, v45\n\t"
      "v_cvt_pk_bf16_f32 v73, v46, v47\n\t"
      "v_mov_b64 v[32:33], 0\n\t"
      "v_mov_b64 v[34:35], 0\n\t"
      "v_mov_b64 v[36:37], 0\n\t"
      "v_mov_b64 v[38:39], 0\n\t"
      "v_mov_b64 v[40:41], 0\n\t"
      "v_mov_b64 v[42:43], 0\n\t"
      "v_mov_b64 v[44:45], 0\n\t"
      "v_mov_b64 v[46:47], 0\n\t"
      "s_waitcnt vmcnt(0)\n\t"
      "s_branch LGO_2%=\n\t"
      "LEARLY_2%=:\n\t"
      "s_cmp_eq_u32 s40, 54\n\t"
      "s_cbranch_scc1 LROLE_2%=\n\t"
      "s_cmp_lt_i32 s40, 0\n\t"
      "s_waitcnt lgkmcnt(0)\n\t"
      "s_cbranch_scc1 LUTOK_2%=\n\t"
      "s_xor_b32 s43, s43, s99\n\t"
      "s_xor_b32 s68, s68, s99\n\t"
      "s_branch LROLE_2%=\n\t"
      "LSLOW_2%=:\n\t"
      "s_mov_b32 s63, 0x10000\n\t"
      "LSLOW1_2%=:\n\t"
      "buffer_load_dwordx2 v[92:93], off, s[36:39], s64 sc1\n\t"
      "s_waitcnt vmcnt(0)\n\t"
      "v_bitop3_b32 v92, v92, v93, s53 bitop3:0x7e\n\t"
      "s_nop 0\n\t"
      "v_readfirstlane_b32 s56, v92\n\t"
      "s_nop 1\n\t"
      "s_cmp_eq_u32 s56, 0\n\t"
      "s_cbranch_scc1 LSLOW2_2%=\n\t"
      "s_sleep 8\n\t"
      "s_sub_u32 s63, s63, 1\n\t"
      "s_cmp_lg_u32 s63, 0\n\t"
      "s_cbranch_scc1 LSLOW1_2%=\n\t"
      "LSLOW2_2%=:\n\t"
      "s_mov_b32 m0, s45\n\t"
      "s_sub_u32 s61, 204800, s71\n\t"
      "buffer_load_dwordx4 v65, s[36:39], s44 offen sc1 lds\n\t"
      "buffer_load_dwordx4 v65, s[36:39], s44 offen offset:1024 sc1 lds\n\t"
      "s_add_u32 m0, s55, s61\n\t"
      "s_nop 0\n\t"
      "buffer_load_dwordx4 v127, s[36:39], s46 offen sc1 lds\n\t"
      "buffer_load_dwordx4 v127, s[36:39], s46 offen offset:1024 sc1 lds\n\t"
      "s_branch LWIN1_2%=\n\t"
      "LMX_2%=:\n\t"
      "s_waitcnt lgkmcnt(0)\n\t"
      "s_nop 7\n\t"
      "v_smfmac_f32_16x16x64_bf16 v[52:55], v[28:31], v[248:255], v122\n\t"
      "v_smfmac_f32_16x16x64_bf16 v[52:55], v[16:19], v[224:231], v122\n\t"
      "v_smfmac_f32_16x16x64_bf16 v[52:55], v[20:23], v[232:239], v122\n\t"
      "s_nop 7\n\t"
      "v_add_f32 v53, v53, v55\n\t"
      "v_add_f32_e64 v52, v52, v54\n\t"
      "v_cvt_pk_bf16_f32 v66, v52, v53\n\t"
      "s_add_u32 s62, s61, 0x83400000\n\t"
      "v_mov_b32_e64 v126, s62\n\t"
      "v_max3_f32 v82, v112, v113, v114\n\t"
      "v_max_f32 v82, v82, v115\n\t"
      "s_nop 1\n\t"
      "v_max_f32_dpp v82, v82, v82 quad_perm:[2,3,0,1] row_mask:0xf bank_mask:0xf\n\t"
      "v_max3_f32 v83, v82, v120, v121\n\t"
      "v_max3_f32 v60, v82, v120, v121\n\t"
      "s_nop 1\n\t"
      "v_permlane16_swap_b32 v83, v60\n\t"
      "v_max_f32 v61, v83, v60\n\t"
      "v_max_f32 v60, v83, v60\n\t"
      "s_nop 1\n\t"
      "v_permlane32_swap_b32 v61, v60\n\t"
      "v_max_f32 v83, v61, v60\n\t"
      "v_sub_f32 v112, v112, v83\n\t"
      "v_sub_f32 v113, v113, v83\n\t"
      "v_sub_f32 v114, v114, v83\n\t"
      "v_sub_f32 v115, v115, v83\n\t"
      "v_sub_f32 v120, v120, v83\n\t"
      "v_sub_f32 v121, v121, v83\n\t"
      "v_exp_f32_e64 v56, v112\n\t"
      "v_exp_f32 v57, v113\n\t"
      "v_exp_f32_e64 v58, v114\n\t"
      "v_exp_f32 v59, v115\n\t"
      "v_exp_f32 v80, v120\n\t"
      "v_exp_f32 v81, v121\n\t"
      "v_cvt_pk_bf16_f32 v96, v56, v57\n\t"
      "v_cvt_pk_bf16_f32 v97, v58, v59\n\t"
      "v_cvt_pk_bf16_f32 v108, v80, v81\n\t"
      "v_mov_b32_dpp v98, v96 quad_perm:[2,3,0,1] row_mask:0xf bank_mask:0xf\n\t"
      "v_mov_b32_dpp v99, v97 quad_perm:[2,3,0,1] row_mask:0xf bank_mask:0xf\n\t"
      "v_mfma_f32_16x16x32_bf16 v[48:51], v[108:111], v[88:91], 0\n\t"
      "s_nop 0\n\t"
      "v_mfma_f32_16x16x32_bf16 v[48:51], v[96:99], v[88:91], v[48:51]\n\t"
      "v_mfma_f32_16x16x32_bf16 v[84:87], v[108:111], v[64:67], 0\n\t"
      "v_mfma_f32_16x16x32_bf16 v[84:87], v[96:99], v[68:71], v[84:87]\n\t"
      "v_mfma_f32_16x16x32_bf16 v[92:95], v[96:99], v[72:75], 0\n\t"
      "v_mfma_f32_16x16x32_bf16 v[92:95], v[108:111], v[66:69], v[92:95]\n\t"
      "s_nop 3\n\t"
      "v_rcp_f32_e64 v48, v48\n\t"
      "v_rcp_f32 v49, v49\n\t"
      "v_fma_f32 v84, v84, v48, s96\n\t"
      "buffer_atomic_add v84, v123, s[36:39], s97 offen\n\t"
      "v_fma_f32 v85, v85, v49, s96\n\t"
      "buffer_atomic_add v85, v123, s[36:39], s98 offen\n\t"
      "v_fma_f32 v94, v94, v48, s96\n\t"
      "buffer_atomic_add v94, v123, s[36:39], s69 offen\n\t"
      "v_fma_f32 v95, v95, v49, s96\n\t"
      "buffer_atomic_add v95, v123, s[36:39], s23 offen\n\t"
      "s_add_u32 s97, s97, 0x8000\n\t"
      "s_branch LWIN_2%=\n\t"
      "LR0_2%=:\n\t"
      "buffer_load_dwordx4 v[84:87], v60, s[36:39], s43 offen sc1\n\t"
      "buffer_load_dwordx4 v[88:91], v60, s[36:39], s43 offen offset:1024 sc1\n\t"
      "s_waitcnt vmcnt(2)\n\t"
      "buffer_load_dwordx4 v[92:95], v60, s[36:39], s68 offen sc1\n\t"
      "buffer_load_dwordx4 v[96:99], v60, s[36:39], s68 offen offset:1024 sc1\n\t"
      "s_sub_u32 s63, s63, 1\n\t"
      "s_waitcnt vmcnt(2) lgkmcnt(0)\n\t"
      "v_bitop3_b32 v64, v84, v85, v86 bitop3:0x80\n\t"
      "v_bitop3_b32 v64, v64, v87, v88 bitop3:0x80\n\t"
      "v_bitop3_b32 v64, v64, v89, v90 bitop3:0x80\n\t"
      "v_bitop3_b32 v64, v64, v91, s58 bitop3:0x80\n\t"
      "v_cmp_eq_u32 vcc, 0, v64\n\t"
      "v_pk_fma_f32 v[12:13], v[84:85], s[56:57], v[126:127] op_sel:[0,1,0] op_sel_hi:[1,1,0]\n\t"
      "v_pk_fma_f32 v[14:15], v[86:87], s[56:57], v[126:127] op_sel:[0,1,0] op_sel_hi:[1,1,0]\n\t"
      "v_cvt_pk_bf16_f32 v8, v12, v13\n\t"
      "v_cvt_pk_bf16_f32 v9, v14, v15\n\t"
      "ds_write_b64 v127, v[8:9] offset:43008\n\t"
      "v_pk_fma_f32 v[0:1], v[88:89], s[56:57], v[126:127] op_sel:[0,1,0] op_sel_hi:[1,1,0]\n\t"
      "v_pk_fma_f32 v[2:3], v[90:91], s[56:57], v[126:127] op_sel:[0,1,0] op_sel_hi:[1,1,0]\n\t"
      "v_cvt_pk_bf16_f32 v10, v0, v1\n\t"
      "v_cvt_pk_bf16_f32 v11, v2, v3\n\t"
      "ds_write_b64 v127, v[10:11] offset:43016\n\t"
      "s_cmp_le_i32 s63, 0\n\t"
      "s_waitcnt lgkmcnt(0)\n\t"
      "s_cbranch_scc1 LRZ_2%=\n\t"
      "s_cbranch_vccnz LR0_2%=\n\t"
      "LRZ_2%=:\n\t"
      "v_mov_b64 v[32:33], 0\n\t"
      "v_mov_b64 v[34:35], 0\n\t"
      "v_mov_b64 v[36:37], 0\n\t"
      "v_mov_b64 v[38:39], 0\n\t"
      "v_mov_b64 v[40:41], 0\n\t"
      "v_mov_b64 v[42:43], 0\n\t"
      "v_mov_b64 v[44:45], 0\n\t"
      "v_mov_b64 v[46:47], 0\n\t"
      "v_mov_b64 v[48:49], 0\n\t"
      "v_mov_b64 v[50:51], 0\n\t"
      "v_mov_b64 v[52:53], 0\n\t"
      "v_mov_b64 v[54:55], 0\n\t"
      "v_mfma_f32_16x16x32_bf16 v[56:59], v[8:11], v[8:11], 0\n\t"
      "v_smfmac_f32_16x16x64_bf16 v[36:39], v[8:11], a[80:87], v122\n\t"
      "v_smfmac_f32_16x16x64_bf16 v[44:47], v[8:11], a[208:215], v122\n\t"
      "s_branch LC0_2%=\n\t"
      "LUTOK_3%=:\n\t"
      "v_add_u32 v104, s71, v100\n\t"
      "ds_read_b128 v[76:79], v104\n\t"
      "ds_read_b128 v[80:83], v104 offset:1024\n\t"
      "ds_read_b128 v[112:115], v100\n\t"
      "ds_read_b128 v[116:119], v100 offset:1024\n\t"
      "v_xor_b32 v101, 2048, v104\n\t"
      "v_xor_b32 v102, 4096, v104\n\t"
      "v_xor_b32 v103, 6144, v104\n\t"
      "ds_read_b128 v[0:3], v101\n\t"
      "ds_read_b128 v[4:7], v101 offset:1024\n\t"
      "ds_read_b128 v[8:11], v100 offset:8192\n\t"
      "ds_read_b128 v[12:15], v100 offset:9216\n\t"
      "ds_read_b128 v[16:19], v102\n\t"
      "ds_read_b128 v[20:23], v102 offset:1024\n\t"
      "ds_read_b128 v[24:27], v100 offset:16384\n\t"
      "ds_read_b128 v[28:31], v100 offset:17408\n\t"
      "s_sub_u32 s61, s88, s78\n\t"
      "s_sub_i32 s61, s61, 560\n\t"
      "s_lshr_b32 s62, s25, 3\n\t"
      "s_sub_u32 s25, s25, s62\n\t"
      "s_sub_i32 s25, s25, 24\n\t"
      "s_max_i32 s25, s25, 0\n\t"
      "s_sub_i32 s62, s61, 96\n\t"
      "s_max_i32 s62, s62, 0\n\t"
      "s_min_i32 s62, s62, 320\n\t"
      "s_lshr_b32 s62, s62, 2\n\t"
      "s_add_u32 s25, s25, s62\n\t"
      "s_min_i32 s61, s61, 96\n\t"
      "s_max_i32 s61, s61, -96\n\t"
      "s_ashr_i32 s61, s61, 3\n\t"
      "s_add_u32 s26, s26, s61\n\t"
      "s_max_u32 s26, s26, 1500\n\t"
      "s_min_u32 s26, s26, 2300\n\t"
      "s_sub_u32 s62, s88, s27\n\t"
      "s_mov_b32 s27, s88\n\t"
      "s_sub_i32 s62, s62, s24\n\t"
      "s_cmp_gt_i32 s62, 1150\n\t"
      "s_cselect_b32 s62, 200, 0\n\t"
      "s_max_u32 s25, s25, s62\n\t"
      "s_sub_u32 s61, s76, s28\n\t"
      "s_sub_u32 s62, s80, s82\n\t"
      "s_mov_b32 s28, s76\n\t"
      "s_mov_b32 s82, s80\n\t"
      "s_lshl_b32 s61, s61, 8\n\t"
      "s_mul_i32 s62, s62, s83\n\t"
      "s_sub_i32 s61, s61, s62\n\t"
      "s_ashr_i32 s61, s61, 10\n\t"
      "s_min_i32 s61, s61, 100\n\t"
      "s_max_i32 s61, s61, -100\n\t"
      "s_add_u32 s83, s83, s61\n\t"
      "s_mul_i32 s24, s26, s83\n\t"
      "s_lshr_b32 s24, s24, 12\n\t"
      "s_add_u32 s24, s24, s25\n\t"
      "s_add_u32 m0, s55, s71\n\t"
      "s_sub_u32 s71, 204800, s71\n\t"
      "s_add_u32 s40, s40, 1\n\t"
      "s_cmp_lt_u32 s40, 63\n\t"
      "s_cselect_b32 s61, 786432, 0\n\t"
      "s_cselect_b32 s60, s58, 0\n\t"
      "s_add_u32 s44, s44, s61\n\t"
      "s_cmp_lt_u32 s40, 62\n\t"
      "s_cselect_b32 s61, 786432, 0\n\t"
      "s_add_u32 s46, s46, s61\n\t"
      "s_add_u32 s61, s40, 3\n\t"
      "s_and_b32 s62, s61, 3\n\t"
      "s_cselect_b32 s62, 16, 2000\n\t"
      "s_cmp_lt_u32 s61, 64\n\t"
      "s_cselect_b32 s62, s62, 0\n\t"
      "s_add_u32 s50, s50, s62\n\t"
      "s_mov_b32 s63, 0x2000\n\t"
      "s_waitcnt lgkmcnt(8)\n\t"
      "v_mfma_f32_16x16x32_bf16 v[32:35], v[76:79], v[112:115], 0\n\t"
      "v_mfma_f32_16x16x32_bf16 v[36:39], v[80:83], v[112:115], 0\n\t"
      "v_mfma_f32_16x16x32_bf16 v[40:43], v[76:79], v[116:119], 0\n\t"
      "v_mfma_f32_16x16x32_bf16 v[44:47], v[80:83], v[116:119], 0\n\t"
      "ds_read_b128 v[76:79], v103\n\t"
      "ds_read_b128 v[80:83], v103 offset:1024\n\t"
      "ds_read_b128 v[112:115], v100 offset:24576\n\t"
      "ds_read_b128 v[116:119], v100 offset:25600\n\t"
      "s_branch LGATE_3%=\n\t"
      "LFIRST_3%=:\n\t"
      "s_cmp_eq_u32 s40, 0\n\t"
      "s_cbranch_scc1 LWM0_3%=\n\t"
      "s_mov_b32 s63, 0x10000\n\t"
      "LST0_3%=:\n\t"
      "s_waitcnt vmcnt(0)\n\t"
      "v_bitop3_b32 v97, v57, v85, s53 bitop3:0x7e\n\t"
      "v_cmp_ne_u32 vcc, 0, v97\n\t"
      "v_add_u32 v99, v58, v86\n\t"
      "v_subrev_u32 v97, s87, v56\n\t"
      "v_subrev_u32 v98, s87, v84\n\t"
      "v_max_i32 v97, v97, v98\n\t"
      "s_cbranch_vccz LST3_3%=\n\t"
      "buffer_load_dwordx4 v[56:59], v96, s[36:39], s86 offen sc1\n\t"
      "buffer_load_dwordx4 v[84:87], v96, s[36:39], s86 offen offset:128 sc1\n\t"
      "s_sub_u32 s63, s63, 1\n\t"
      "s_cmp_eq_u32 s63, 0\n\t"
      "s_cbranch_scc0 LST0_3%=\n\t"
      "s_waitcnt vmcnt(0)\n\t"
      "LST3_3%=:\n\t"
      "s_nop 1\n\t"
      "v_add_u32_dpp v99, v99, v99 quad_perm:[1,0,3,2] row_mask:0xf bank_mask:0xf\n\t"
      "s_nop 1\n\t"
      "v_add_u32_dpp v99, v99, v99 quad_perm:[2,3,0,1] row_mask:0xf bank_mask:0xf\n\t"
      "s_nop 1\n\t"
      "v_add_u32_dpp v99, v99, v99 row_ror:4 row_mask:0xf bank_mask:0xf\n\t"
      "s_nop 1\n\t"
      "v_add_u32_dpp v99, v99, v99 row_ror:8 row_mask:0xf bank_mask:0xf\n\t"
      "v_mov_b32 v98, v99\n\t"
      "s_nop 1\n\t"
      "v_permlane16_swap_b32 v99, v98\n\t"
      "v_add_u32 v99, v99, v98\n\t"
      "v_mov_b32 v98, v99\n\t"
      "s_nop 1\n\t"
      "v_permlane32_swap_b32 v99, v98\n\t"
      "v_add_u32 v99, v99, v98\n\t"
      "ds_write_b32 v109, v99 offset:55392\n\t"
      "s_nop 1\n\t"
      "v_max_i32_dpp v97, v97, v97 quad_perm:[1,0,3,2] row_mask:0xf bank_mask:0xf\n\t"
      "s_nop 1\n\t"
      "v_max_i32_dpp v97, v97, v97 quad_perm:[2,3,0,1] row_mask:0xf bank_mask:0xf\n\t"
      "s_nop 1\n\t"
      "v_max_i32_dpp v97, v97, v97 row_ror:4 row_mask:0xf bank_mask:0xf\n\t"
      "s_nop 1\n\t"
      "v_max_i32_dpp v97, v97, v97 row_ror:8 row_mask:0xf bank_mask:0xf\n\t"
      "v_mov_b32 v98, v97\n\t"
      "s_nop 1\n\t"
      "v_permlane16_swap_b32 v97, v98\n\t"
      "v_max_i32 v97, v97, v98\n\t"
      "v_mov_b32 v98, v97\n\t"
      "s_nop 1\n\t"
      "v_permlane32_swap_b32 v97, v98\n\t"
      "v_max_i32 v97, v97, v98\n\t"
      "v_max_u32 v99, v59, v87\n\t"
      "s_nop 1\n\t"
      "v_max_u32_dpp v99, v99, v99 quad_perm:[1,0,3,2] row_mask:0xf bank_mask:0xf\n\t"
      "s_nop 1\n\t"
      "v_max_u32_dpp v99, v99, v99 quad_perm:[2,3,0,1] row_mask:0xf bank_mask:0xf\n\t"
      "s_nop 1\n\t"
      "v_max_u32_dpp v99, v99, v99 row_ror:4 row_mask:0xf bank_mask:0xf\n\t"
      "s_nop 1\n\t"
      "v_max_u32_dpp v99, v99, v99 row_ror:8 row_mask:0xf bank_mask:0xf\n\t"
      "v_mov_b32 v98, v99\n\t"
      "s_nop 1\n\t"
      "v_permlane16_swap_b32 v99, v98\n\t"
      "v_max_u32 v99, v99, v98\n\t"
      "v_mov_b32 v98, v99\n\t"
      "s_nop 1\n\t"
      "v_permlane32_swap_b32 v99, v98\n\t"
      "v_max_u32 v99, v99, v98\n\t"
      "v_rcp_f32 v99, v99\n\t"
      "s_nop 0\n\t"
      "v_mul_f32 v99, 0x41800000, v99\n\t"
      "s_mov_b32 s25, 0\n\t"
      "v_mul_f32 v99, 0x4519c000, v99\n\t"
      "v_cvt_u32_f32 v99, v99\n\t"
      "s_nop 0\n\t"
      "v_readfirstlane_b32 s26, v99\n\t"
      "s_waitcnt lgkmcnt(0)\n\t"
      "s_barrier\n\t"
      "ds_read_b32 v99, v109 offset:55392\n\t"
      "v_readfirstlane_b32 s62, v97\n\t"
      "s_waitcnt lgkmcnt(0)\n\t"
      "v_readfirstlane_b32 s61, v99\n\t"
      "s_nop 1\n\t"
      "s_cmp_gt_i32 s61, 0\n\t"
      "s_cselect_b32 s99, 0x2000, 0\n\t"
      "s_xor_b32 s97, s97, s99\n\t"
      "s_min_u32 s62, s62, 500\n\t"
      "s_add_u32 s62, s62, 195\n\t"
      "LST1_3%=:\n\t"
      "s_memrealtime s[92:93]\n\t"
      "s_waitcnt lgkmcnt(0)\n\t"
      "s_sub_u32 s61, s92, s87\n\t"
      "s_cmp_lt_u32 s61, s62\n\t"
      "s_cbranch_scc1 LST1_3%=\n\t"
      "s_barrier\n\t"
      "s_mov_b32 s63, 0x2000\n\t"
      "s_branch LPOLL_3%=\n\t"
      "LWM0_3%=:\n\t"
      "s_mov_b32 s63, 0x10000\n\t"
      "LWM_3%=:\n\t"
      "s_waitcnt vmcnt(0) lgkmcnt(0)\n\t"
      "v_bitop3_b32 v49, v49, v51, s53 bitop3:0x7e\n\t"
      "v_max_u32 v48, v48, v50\n\t"
      "v_cmp_ne_u32 vcc, 0, v49\n\t"
      "s_nop 1\n\t"
      "v_max_u32_dpp v48, v48, v48 quad_perm:[1,0,3,2] row_mask:0xf bank_mask:0xf\n\t"
      "s_nop 1\n\t"
      "v_max_u32_dpp v48, v48, v48 quad_perm:[2,3,0,1] row_mask:0xf bank_mask:0xf\n\t"
      "s_nop 1\n\t"
      "v_max_u32_dpp v48, v48, v48 row_ror:4 row_mask:0xf bank_mask:0xf\n\t"
      "s_nop 1\n\t"
      "v_max_u32_dpp v48, v48, v48 row_ror:8 row_mask:0xf bank_mask:0xf\n\t"
      "v_mov_b32 v49, v48\n\t"
      "s_nop 1\n\t"
      "v_permlane16_swap_b32 v48, v49\n\t"
      "v_max_u32 v48, v48, v49\n\t"
      "v_mov_b32 v49, v48\n\t"
      "s_nop 1\n\t"
      "v_permlane32_swap_b32 v48, v49\n\t"
      "v_max_u32 v48, v48, v49\n\t"
      "v_mul_f32 v49, 0x40a00000, v48\n\t"
      "s_nop 0\n\t"
      "v_readfirstlane_b32 s67, v49\n\t"
      "v_mul_f32 v49, 0x43030000, v49\n\t"
      "s_nop 0\n\t"
      "v_readfirstlane_b32 s66, v49\n\t"
      "s_cbranch_vccz LWM1_3%=\n\t"
      "s_sleep 4\n\t"
      "buffer_load_dwordx2 v[48:49], v52, s[36:39], s94 offen sc1\n\t"
      "buffer_load_dwordx2 v[50:51], v52, s[36:39], s94 offen offset:128 sc1\n\t"
      "s_sub_u32 s63, s63, 1\n\t"
      "s_cmp_lg_u32 s63, 0\n\t"
      "s_cbranch_scc1 LWM_3%=\n\t"
      "s_waitcnt vmcnt(0)\n\t"
      "LWM1_3%=:\n\t"
      "buffer_load_dwordx2 v[120:121], off, s[36:39], s64 sc1\n\t"
      "s_lshr_b32 s67, s67, 23\n\t"
      "s_add_u32 s67, s67, 5\n\t"
      "s_lshr_b32 s66, s66, 23\n\t"
      "s_add_u32 s66, s66, 129\n\t"
      "s_max_u32 s66, s66, 80\n\t"
      "s_mov_b32 s63, 0x2000\n\t"
      "LPOL0_3%=:\n\t"
      "s_memtime s[78:79]\n\t"
      "s_waitcnt lgkmcnt(8)\n\t"
      "v_mfma_f32_16x16x32_bf16 v[32:35], v[0:3], v[8:11], v[32:35]\n\t"
      "v_mfma_f32_16x16x32_bf16 v[36:39], v[4:7], v[8:11], v[36:39]\n\t"
      "v_mfma_f32_16x16x32_bf16 v[40:43], v[0:3], v[12:15], v[40:43]\n\t"
      "v_mfma_f32_16x16x32_bf16 v[44:47], v[4:7], v[12:15], v[44:47]\n\t"
      "v_mov_b64 v[48:49], 0\n\t"
      "v_mov_b64 v[50:51], 0\n\t"
      "v_mov_b64 v[52:53], 0\n\t"
      "v_mov_b64 v[54:55], 0\n\t"
      "s_xor_b32 s69, s97, 1024\n\t"
      "s_xor_b32 s98, s97, 512\n\t"
      "s_xor_b32 s23, s97, 1536\n\t"
      "s_waitcnt lgkmcnt(4)\n\t"
      "v_mfma_f32_16x16x32_bf16 v[32:35], v[16:19], v[24:27], v[32:35]\n\t"
      "v_mfma_f32_16x16x32_bf16 v[36:39], v[20:23], v[24:27], v[36:39]\n\t"
      "v_mfma_f32_16x16x32_bf16 v[40:43], v[16:19], v[28:31], v[40:43]\n\t"
      "v_mfma_f32_16x16x32_bf16 v[44:47], v[20:23], v[28:31], v[44:47]\n\t"
      "s_waitcnt lgkmcnt(0)\n\t"
      "v_mfma_f32_16x16x32_bf16 v[32:35], v[76:79], v[112:115], v[32:35]\n\t"
      "v_mfma_f32_16x16x32_bf16 v[36:39], v[80:83], v[112:115], v[36:39]\n\t"
      "v_mfma_f32_16x16x32_bf16 v[40:43], v[76:79], v[116:119], v[40:43]\n\t"
      "v_mfma_f32_16x16x32_bf16 v[44:47], v[80:83], v[116:119], v[44:47]\n\t"
      "s_mov_b64 exec, 1\n\t"
      "ds_write_b64 v109, v[110:111] offset:55392\n\t"
      "s_mov_b64 exec, -1\n\t"
      "s_nop 4\n\t"
      "v_cvt_pk_bf16_f32 v68, v32, v33\n\t"
      "v_cvt_pk_bf16_f32 v69, v34, v35\n\t"
      "v_cvt_pk_bf16_f32 v70, v36, v37\n\t"
      "v_cvt_pk_bf16_f32 v71, v38, v39\n\t"
      "v_cvt_pk_bf16_f32 v74, v40, v41\n\t"
      "v_cvt_pk_bf16_f32 v75, v42, v43\n\t"
      "v_cvt_pk_bf16_f32 v72, v44, v45\n\t"
      "v_cvt_pk_bf16_f32 v73, v46, v47\n\t"
      "v_mov_b64 v[32:33], 0\n\t"
      "v_mov_b64 v[34:35], 0\n\t"
      "v_mov_b64 v[36:37], 0\n\t"
      "v_mov_b64 v[38:39], 0\n\t"
      "v_mov_b64 v[40:41], 0\n\t"
      "v_mov_b64 v[42:43], 0\n\t"
      "v_mov_b64 v[44:45], 0\n\t"
      "v_mov_b64 v[46:47], 0\n\t"
      "s_waitcnt vmcnt(0)\n\t"
      "s_branch LGO_3%=\n\t"
      "LEARLY_3%=:\n\t"
      "s_cmp_eq_u32 s40, 54\n\t"
      "s_cbranch_scc1 LROLE_3%=\n\t"
      "s_cmp_lt_i32 s40, 0\n\t"
      "s_waitcnt lgkmcnt(0)\n\t"
      "s_cbranch_scc1 LSTART_3%=\n\t"
      "s_xor_b32 s43, s43, s99\n\t"
      "s_xor_b32 s68, s68, s99\n\t"
      "s_cmp_lg_u32 s40, 0\n\t"
      "s_cbranch_scc1 LROLE_3%=\n\t"
      "v_lshlrev_b32 v96, 4, v127\n\t"
      "buffer_load_dwordx4 v[56:59], v96, s[36:39], s86 offen sc1\n\t"
      "buffer_load_dwordx4 v[84:87], v96, s[36:39], s86 offen offset:128 sc1\n\t"
      "s_branch LROLE_3%=\n\t"
      "LSTART_3%=:\n\t"
      "s_memrealtime s[92:93]\n\t"
      "s_memtime s[90:91]\n\t"
      "s_mov_b64 exec, 1\n\t"
      "v_mov_b32 v57, s53\n\t"
      "v_mov_b32 v58, s65\n\t"
      "s_add_u32 s86, s94, 208640\n\t"
      "s_add_u32 s61, s95, 208640\n\t"
      "s_waitcnt lgkmcnt(0)\n\t"
      "v_mov_b32 v56, s92\n\t"
      "s_mov_b32 s87, s92\n\t"
      "s_sub_u32 s80, s90, s23\n\t"
      "s_sub_u32 s82, s92, s82\n\t"
      "v_cvt_f32_u32 v59, s82\n\t"
      "v_cvt_f32_u32 v63, s80\n\t"
      "v_rcp_f32 v59, v59\n\t"
      "s_nop 0\n\t"
      "v_mul_f32 v59, v59, v63\n\t"
      "v_mul_f32 v63, 0x43800000, v59\n\t"
      "v_cvt_u32_f32 v63, v63\n\t"
      "s_nop 0\n\t"
      "v_readfirstlane_b32 s83, v63\n\t"
      "s_mov_b32 s76, s90\n\t"
      "s_mov_b32 s80, s92\n\t"
      "s_sub_u32 s28, s90, 2100\n\t"
      "s_sub_u32 s82, s92, 100\n\t"
      "s_mov_b32 s26, 1800\n\t"
      "buffer_store_dwordx4 v[56:59], off, s[36:39], s61 sc1\n\t"
      "s_mov_b64 exec, -1\n\t"
      "s_branch LUTOK_3%=\n\t"
      "LSLOW_3%=:\n\t"
      "s_mov_b32 s63, 0x10000\n\t"
      "LSLOW1_3%=:\n\t"
      "buffer_load_dwordx2 v[92:93], off, s[36:39], s64 sc1\n\t"
      "s_waitcnt vmcnt(0)\n\t"
      "v_bitop3_b32 v92, v92, v93, s53 bitop3:0x7e\n\t"
      "s_nop 0\n\t"
      "v_readfirstlane_b32 s56, v92\n\t"
      "s_nop 1\n\t"
      "s_cmp_eq_u32 s56, 0\n\t"
      "s_cbranch_scc1 LSLOW2_3%=\n\t"
      "s_sleep 8\n\t"
      "s_sub_u32 s63, s63, 1\n\t"
      "s_cmp_lg_u32 s63, 0\n\t"
      "s_cbranch_scc1 LSLOW1_3%=\n\t"
      "LSLOW2_3%=:\n\t"
      "s_mov_b32 m0, s45\n\t"
      "s_sub_u32 s61, 204800, s71\n\t"
      "buffer_load_dwordx4 v65, s[36:39], s44 offen sc1 lds\n\t"
      "buffer_load_dwordx4 v65, s[36:39], s44 offen offset:1024 sc1 lds\n\t"
      "s_add_u32 m0, s55, s61\n\t"
      "s_nop 0\n\t"
      "buffer_load_dwordx4 v127, s[36:39], s46 offen sc1 lds\n\t"
      "buffer_load_dwordx4 v127, s[36:39], s46 offen offset:1024 sc1 lds\n\t"
      "s_branch LWIN1_3%=\n\t"
      "LMX_3%=:\n\t"
      "s_waitcnt lgkmcnt(0)\n\t"
      "s_nop 7\n\t"
      "v_smfmac_f32_16x16x64_bf16 v[52:55], v[24:27], v[240:247], v122\n\t"
      "v_smfmac_f32_16x16x64_bf16 v[52:55], v[20:23], v[232:239], v122\n\t"
      "v_smfmac_f32_16x16x64_bf16 v[52:55], v[16:19], v[224:231], v122\n\t"
      "s_nop 7\n\t"
      "v_add_f32 v53, v53, v55\n\t"
      "v_add_f32_e64 v52, v52, v54\n\t"
      "v_cvt_pk_bf16_f32 v66, v52, v53\n\t"
      "s_add_u32 s62, s61, 0x83400000\n\t"
      "v_mov_b32_e64 v126, s62\n\t"
      "v_max3_f32 v82, v112, v113, v114\n\t"
      "v_max_f32 v82, v82, v115\n\t"
      "s_nop 1\n\t"
      "v_max_f32_dpp v82, v82, v82 quad_perm:[2,3,0,1] row_mask:0xf bank_mask:0xf\n\t"
      "v_max3_f32 v83, v82, v120, v121\n\t"
      "v_max3_f32 v60, v82, v120, v121\n\t"
      "s_nop 1\n\t"
      "v_permlane16_swap_b32 v83, v60\n\t"
      "v_max_f32 v61, v83, v60\n\t"
      "v_max_f32 v60, v83, v60\n\t"
      "s_nop 1\n\t"
      "v_permlane32_swap_b32 v61, v60\n\t"
      "v_max_f32 v83, v61, v60\n\t"
      "v_sub_f32 v112, v112, v83\n\t"
      "v_sub_f32 v113, v113, v83\n\t"
      "v_sub_f32 v114, v114, v83\n\t"
      "v_sub_f32 v115, v115, v83\n\t"
      "v_sub_f32 v120, v120, v83\n\t"
      "v_sub_f32 v121, v121, v83\n\t"
      "v_exp_f32_e64 v56, v112\n\t"
      "v_exp_f32 v57, v113\n\t"
      "v_exp_f32_e64 v58, v114\n\t"
      "v_exp_f32 v59, v115\n\t"
      "v_exp_f32 v80, v120\n\t"
      "v_exp_f32 v81, v121\n\t"
      "v_cvt_pk_bf16_f32 v96, v56, v57\n\t"
      "v_cvt_pk_bf16_f32 v97, v58, v59\n\t"
      "v_cvt_pk_bf16_f32 v108, v80, v81\n\t"
      "v_mov_b32_dpp v98, v96 quad_perm:[2,3,0,1] row_mask:0xf bank_mask:0xf\n\t"
      "v_mov_b32_dpp v99, v97 quad_perm:[2,3,0,1] row_mask:0xf bank_mask:0xf\n\t"
      "v_mfma_f32_16x16x32_bf16 v[48:51], v[108:111], v[88:91], 0\n\t"
      "s_nop 0\n\t"
      "v_mfma_f32_16x16x32_bf16 v[48:51], v[96:99], v[88:91], v[48:51]\n\t"
      "v_mfma_f32_16x16x32_bf16 v[84:87], v[108:111], v[64:67], 0\n\t"
      "v_mfma_f32_16x16x32_bf16 v[84:87], v[96:99], v[68:71], v[84:87]\n\t"
      "v_mfma_f32_16x16x32_bf16 v[92:95], v[96:99], v[72:75], 0\n\t"
      "v_mfma_f32_16x16x32_bf16 v[92:95], v[108:111], v[66:69], v[92:95]\n\t"
      "s_nop 3\n\t"
      "v_rcp_f32_e64 v48, v48\n\t"
      "v_rcp_f32 v49, v49\n\t"
      "v_fma_f32 v84, v84, v48, s96\n\t"
      "buffer_atomic_add v84, v123, s[36:39], s97 offen\n\t"
      "v_fma_f32 v85, v85, v49, s96\n\t"
      "buffer_atomic_add v85, v123, s[36:39], s98 offen\n\t"
      "v_fma_f32 v94, v94, v48, s96\n\t"
      "buffer_atomic_add v94, v123, s[36:39], s69 offen\n\t"
      "v_fma_f32 v95, v95, v49, s96\n\t"
      "buffer_atomic_add v95, v123, s[36:39], s23 offen\n\t"
      "s_add_u32 s97, s97, 0x8000\n\t"
      "s_branch LWIN_3%=\n\t"
      "LR0_3%=:\n\t"
      "buffer_load_dwordx4 v[84:87], v60, s[36:39], s43 offen sc1\n\t"
      "buffer_load_dwordx4 v[88:91], v60, s[36:39], s43 offen offset:1024 sc1\n\t"
      "s_waitcnt vmcnt(2)\n\t"
      "buffer_load_dwordx4 v[92:95], v60, s[36:39], s68 offen sc1\n\t"
      "buffer_load_dwordx4 v[96:99], v60, s[36:39], s68 offen offset:1024 sc1\n\t"
      "s_sub_u32 s63, s63, 1\n\t"
      "s_waitcnt vmcnt(2) lgkmcnt(0)\n\t"
      "v_bitop3_b32 v64, v84, v85, v86 bitop3:0x80\n\t"
      "v_bitop3_b32 v64, v64, v87, v88 bitop3:0x80\n\t"
      "v_bitop3_b32 v64, v64, v89, v90 bitop3:0x80\n\t"
      "v_bitop3_b32 v64, v64, v91, s58 bitop3:0x80\n\t"
      "v_cmp_eq_u32 vcc, 0, v64\n\t"
      "v_pk_fma_f32 v[8:9], v[84:85], s[56:57], v[126:127] op_sel:[0,1,0] op_sel_hi:[1,1,0]\n\t"
      "v_pk_fma_f32 v[10:11], v[86:87], s[56:57], v[126:127] op_sel:[0,1,0] op_sel_hi:[1,1,0]\n\t"
      "v_cvt_pk_bf16_f32 v12, v8, v9\n\t"
      "v_cvt_pk_bf16_f32 v13, v10, v11\n\t"
      "ds_write_b64 v127, v[12:13] offset:44032\n\t"
      "v_pk_fma_f32 v[4:5], v[88:89], s[56:57], v[126:127] op_sel:[0,1,0] op_sel_hi:[1,1,0]\n\t"
      "v_pk_fma_f32 v[6:7], v[90:91], s[56:57], v[126:127] op_sel:[0,1,0] op_sel_hi:[1,1,0]\n\t"
      "v_cvt_pk_bf16_f32 v14, v4, v5\n\t"
      "v_cvt_pk_bf16_f32 v15, v6, v7\n\t"
      "ds_write_b64 v127, v[14:15] offset:44040\n\t"
      "s_memtime s[88:89]\n\t"
      "s_cmp_le_i32 s63, 0\n\t"
      "s_waitcnt lgkmcnt(0)\n\t"
      "s_cbranch_scc1 LRZ_3%=\n\t"
      "s_cbranch_vccnz LR0_3%=\n\t"
      "LRZ_3%=:\n\t"
      "s_sub_u32 s88, s88, 0\n\t"
      "v_mov_b64 v[32:33], 0\n\t"
      "v_mov_b64 v[34:35], 0\n\t"
      "v_mov_b64 v[36:37], 0\n\t"
      "v_mov_b64 v[38:39], 0\n\t"
      "v_mov_b64 v[40:41], 0\n\t"
      "v_mov_b64 v[42:43], 0\n\t"
      "v_mov_b64 v[44:45], 0\n\t"
      "v_mov_b64 v[46:47], 0\n\t"
      "v_mov_b64 v[48:49], 0\n\t"
      "v_mov_b64 v[50:51], 0\n\t"
      "v_mov_b64 v[52:53], 0\n\t"
      "v_mov_b64 v[54:55], 0\n\t"
      "v_mfma_f32_16x16x32_bf16 v[56:59], v[12:15], v[12:15], 0\n\t"
      "v_smfmac_f32_16x16x64_bf16 v[36:39], v[12:15], a[88:95], v122\n\t"
      "v_smfmac_f32_16x16x64_bf16 v[44:47], v[12:15], a[216:223], v122\n\t"
      "s_branch LC0_3%=\n\t"
      "LCOLD%=:\n\t"
      "LR1_0%=:\n\t"
      "v_bitop3_b32 v64, v92, v93, v94 bitop3:0x80\n\t"
      "v_bitop3_b32 v64, v64, v95, v96 bitop3:0x80\n\t"
      "v_bitop3_b32 v64, v64, v97, v98 bitop3:0x80\n\t"
      "v_bitop3_b32 v64, v64, v99, s58 bitop3:0x80\n\t"
      "v_cmp_eq_u32 vcc, 0, v64\n\t"
      "s_nop 0\n\t"
      "s_cbranch_vccz LRX_0%=\n\t"
      "LRY_0%=:\n\t"
      "buffer_load_dwordx4 v[92:95], v60, s[36:39], s68 offen sc1\n\t"
      "buffer_load_dwordx4 v[96:99], v60, s[36:39], s68 offen offset:1024 sc1\n\t"
      "s_sub_u32 s63, s63, 1\n\t"
      "s_waitcnt vmcnt(0) lgkmcnt(0)\n\t"
      "v_bitop3_b32 v64, v92, v93, v94 bitop3:0x80\n\t"
      "v_bitop3_b32 v64, v64, v95, v96 bitop3:0x80\n\t"
      "v_bitop3_b32 v64, v64, v97, v98 bitop3:0x80\n\t"
      "v_bitop3_b32 v64, v64, v99, s58 bitop3:0x80\n\t"
      "v_cmp_eq_u32 vcc, 0, v64\n\t"
      "v_pk_fma_f32 v[20:21], v[92:93], s[56:57], v[126:127] op_sel:[0,1,0] op_sel_hi:[1,1,0]\n\t"
      "v_pk_fma_f32 v[22:23], v[94:95], s[56:57], v[126:127] op_sel:[0,1,0] op_sel_hi:[1,1,0]\n\t"
      "v_cvt_pk_bf16_f32 v16, v20, v21\n\t"
      "v_cvt_pk_bf16_f32 v17, v22, v23\n\t"
      "ds_write_b64 v127, v[16:17] offset:45056\n\t"
      "v_pk_fma_f32 v[24:25], v[96:97], s[56:57], v[126:127] op_sel:[0,1,0] op_sel_hi:[1,1,0]\n\t"
      "v_pk_fma_f32 v[26:27], v[98:99], s[56:57], v[126:127] op_sel:[0,1,0] op_sel_hi:[1,1,0]\n\t"
      "v_cvt_pk_bf16_f32 v18, v24, v25\n\t"
      "v_cvt_pk_bf16_f32 v19, v26, v27\n\t"
      "ds_write_b64 v127, v[18:19] offset:45064\n\t"
      "s_cmp_le_i32 s63, 0\n\t"
      "s_waitcnt lgkmcnt(0)\n\t"
      "s_cbranch_scc1 LRX_0%=\n\t"
      "s_cbranch_vccnz LRY_0%=\n\t"
      "LRX_0%=:\n\t"
      "s_cmp_eq_u32 s40, 63\n\t"
      "s_cbranch_scc1 LLAST_0%=\n\t"
      "s_branch LC1_0%=\n\t"
      "LLAST_0%=:\n\t"
      "s_waitcnt vmcnt(0) lgkmcnt(0)\n\t"
      "s_barrier\n\t"
      "ds_read_b128 v[20:23], v127 offset:46080\n\t"
      "ds_read_b128 v[24:27], v127 offset:47104\n\t"
      "ds_read_b128 v[28:31], v127 offset:48128\n\t"
      "v_smfmac_f32_16x16x64_bf16 v[40:43], v[16:19], a[160:167], v122\n\t"
      "v_smfmac_f32_16x16x64_bf16 v[44:47], v[16:19], a[224:231], v122\n\t"
      "s_waitcnt lgkmcnt(2)\n\t"
      "v_smfmac_f32_16x16x64_bf16 v[40:43], v[20:23], a[168:175], v122\n\t"
      "v_smfmac_f32_16x16x64_bf16 v[44:47], v[20:23], a[232:239], v122\n\t"
      "s_waitcnt lgkmcnt(1)\n\t"
      "v_smfmac_f32_16x16x64_bf16 v[44:47], v[24:27], a[240:247], v122\n\t"
      "v_smfmac_f32_16x16x64_bf16 v[40:43], v[24:27], a[176:183], v122\n\t"
      "s_waitcnt lgkmcnt(0)\n\t"
      "v_smfmac_f32_16x16x64_bf16 v[44:47], v[28:31], a[248:255], v122\n\t"
      "v_smfmac_f32_16x16x64_bf16 v[40:43], v[28:31], a[184:191], v122\n\t"
      "s_nop 7\n\t"
      "v_add_f32 v40, v40, v42\n\t"
      "v_add_f32 v41, v41, v43\n\t"
      "v_cvt_pk_bf16_f32 v120, v40, v41\n\t"
      "v_add_f32 v44, v44, v46\n\t"
      "v_add_f32 v45, v45, v47\n\t"
      "v_cvt_pk_bf16_f32 v121, v44, v45\n\t"
      "ds_write_b64 v125, v[120:121] offset:2080\n\t"
      "s_waitcnt lgkmcnt(0)\n\t"
      "s_barrier\n\t"
      "ds_read_b64_tr_b16 v[40:41], v124 offset:51232\n\t"
      "ds_read_b64_tr_b16 v[42:43], v124 offset:51752\n\t"
      "ds_read_b64_tr_b16 v[44:45], v124 offset:52272\n\t"
      "ds_read_b64_tr_b16 v[46:47], v124 offset:52792\n\t"
      "v_add_u32 v100, s55, v127\n\t"
      "ds_read_b128 v[60:63], v100 offset:57344\n\t"
      "s_waitcnt lgkmcnt(0)\n\t"
      "s_branch LROLE_0%=\n\t"
      "LFIN_0%=:\n\t"
      "s_waitcnt lgkmcnt(0)\n\t"
      "s_barrier\n\t"
      "ds_read_b64 v[54:55], v124 offset:53312\n\t"
      "s_waitcnt lgkmcnt(0)\n\t"
      "v_add_f32 v48, v48, v54\n\t"
      "v_add_f32 v49, v49, v55\n\t"
      "v_cvt_pk_bf16_f32 v48, v48, v49\n\t"
      "buffer_store_dword v48, v62, s[36:39], s28 offen sc1\n\t"
      "s_branch LEND%=\n\t"
      "LR1_1%=:\n\t"
      "v_bitop3_b32 v64, v92, v93, v94 bitop3:0x80\n\t"
      "v_bitop3_b32 v64, v64, v95, v96 bitop3:0x80\n\t"
      "v_bitop3_b32 v64, v64, v97, v98 bitop3:0x80\n\t"
      "v_bitop3_b32 v64, v64, v99, s58 bitop3:0x80\n\t"
      "v_cmp_eq_u32 vcc, 0, v64\n\t"
      "s_nop 0\n\t"
      "s_cbranch_vccz LRX_1%=\n\t"
      "LRY_1%=:\n\t"
      "buffer_load_dwordx4 v[92:95], v60, s[36:39], s68 offen sc1\n\t"
      "buffer_load_dwordx4 v[96:99], v60, s[36:39], s68 offen offset:1024 sc1\n\t"
      "s_sub_u32 s63, s63, 1\n\t"
      "s_waitcnt vmcnt(0) lgkmcnt(0)\n\t"
      "v_bitop3_b32 v64, v92, v93, v94 bitop3:0x80\n\t"
      "v_bitop3_b32 v64, v64, v95, v96 bitop3:0x80\n\t"
      "v_bitop3_b32 v64, v64, v97, v98 bitop3:0x80\n\t"
      "v_bitop3_b32 v64, v64, v99, s58 bitop3:0x80\n\t"
      "v_cmp_eq_u32 vcc, 0, v64\n\t"
      "v_pk_fma_f32 v[16:17], v[92:93], s[56:57], v[126:127] op_sel:[0,1,0] op_sel_hi:[1,1,0]\n\t"
      "v_pk_fma_f32 v[18:19], v[94:95], s[56:57], v[126:127] op_sel:[0,1,0] op_sel_hi:[1,1,0]\n\t"
      "v_cvt_pk_bf16_f32 v20, v16, v17\n\t"
      "v_cvt_pk_bf16_f32 v21, v18, v19\n\t"
      "ds_write_b64 v127, v[20:21] offset:46080\n\t"
      "v_pk_fma_f32 v[28:29], v[96:97], s[56:57], v[126:127] op_sel:[0,1,0] op_sel_hi:[1,1,0]\n\t"
      "v_pk_fma_f32 v[30:31], v[98:99], s[56:57], v[126:127] op_sel:[0,1,0] op_sel_hi:[1,1,0]\n\t"
      "v_cvt_pk_bf16_f32 v22, v28, v29\n\t"
      "v_cvt_pk_bf16_f32 v23, v30, v31\n\t"
      "ds_write_b64 v127, v[22:23] offset:46088\n\t"
      "s_cmp_le_i32 s63, 0\n\t"
      "s_waitcnt lgkmcnt(0)\n\t"
      "s_cbranch_scc1 LRX_1%=\n\t"
      "s_cbranch_vccnz LRY_1%=\n\t"
      "LRX_1%=:\n\t"
      "s_cmp_eq_u32 s40, 63\n\t"
      "s_cbranch_scc1 LLAST_1%=\n\t"
      "s_branch LC1_1%=\n\t"
      "LLAST_1%=:\n\t"
      "s_waitcnt vmcnt(0) lgkmcnt(0)\n\t"
      "s_barrier\n\t"
      "ds_read_b128 v[16:19], v127 offset:45056\n\t"
      "ds_read_b128 v[28:31], v127 offset:48128\n\t"
      "ds_read_b128 v[24:27], v127 offset:47104\n\t"
      "v_smfmac_f32_16x16x64_bf16 v[40:43], v[20:23], a[168:175], v122\n\t"
      "v_smfmac_f32_16x16x64_bf16 v[44:47], v[20:23], a[232:239], v122\n\t"
      "s_waitcnt lgkmcnt(2)\n\t"
      "v_smfmac_f32_16x16x64_bf16 v[40:43], v[16:19], a[160:167], v122\n\t"
      "v_smfmac_f32_16x16x64_bf16 v[44:47], v[16:19], a[224:231], v122\n\t"
      "s_waitcnt lgkmcnt(1)\n\t"
      "v_smfmac_f32_16x16x64_bf16 v[44:47], v[28:31], a[248:255], v122\n\t"
      "v_smfmac_f32_16x16x64_bf16 v[40:43], v[28:31], a[184:191], v122\n\t"
      "s_waitcnt lgkmcnt(0)\n\t"
      "v_smfmac_f32_16x16x64_bf16 v[44:47], v[24:27], a[240:247], v122\n\t"
      "v_smfmac_f32_16x16x64_bf16 v[40:43], v[24:27], a[176:183], v122\n\t"
      "s_nop 7\n\t"
      "v_add_f32 v40, v40, v42\n\t"
      "v_add_f32 v41, v41, v43\n\t"
      "v_cvt_pk_bf16_f32 v120, v40, v41\n\t"
      "v_add_f32 v44, v44, v46\n\t"
      "v_add_f32 v45, v45, v47\n\t"
      "v_cvt_pk_bf16_f32 v121, v44, v45\n\t"
      "ds_write_b64 v125, v[120:121] offset:2080\n\t"
      "s_waitcnt lgkmcnt(0)\n\t"
      "s_barrier\n\t"
      "ds_read_b64_tr_b16 v[40:41], v124 offset:51232\n\t"
      "ds_read_b64_tr_b16 v[42:43], v124 offset:51752\n\t"
      "ds_read_b64_tr_b16 v[44:45], v124 offset:52272\n\t"
      "ds_read_b64_tr_b16 v[46:47], v124 offset:52792\n\t"
      "v_add_u32 v100, s55, v127\n\t"
      "ds_read_b128 v[60:63], v100 offset:57344\n\t"
      "s_waitcnt lgkmcnt(0)\n\t"
      "s_branch LROLE_1%=\n\t"
      "LFIN_1%=:\n\t"
      "s_waitcnt lgkmcnt(0)\n\t"
      "s_barrier\n\t"
      "ds_read_b64 v[54:55], v124 offset:53832\n\t"
      "s_waitcnt lgkmcnt(0)\n\t"
      "v_add_f32 v48, v48, v54\n\t"
      "v_add_f32 v49, v49, v55\n\t"
      "v_cvt_pk_bf16_f32 v48, v48, v49\n\t"
      "buffer_store_dword v48, v62, s[36:39], s28 offen sc1\n\t"
      "s_branch LEND%=\n\t"
      "LWK1_2%=:\n\t"
      "buffer_store_dwordx4 v[44:47], v61, s[36:39], s48 offen sc1\n\t"
      "s_branch LW4_2%=\n\t"
      "LR1_2%=:\n\t"
      "v_bitop3_b32 v64, v92, v93, v94 bitop3:0x80\n\t"
      "v_bitop3_b32 v64, v64, v95, v96 bitop3:0x80\n\t"
      "v_bitop3_b32 v64, v64, v97, v98 bitop3:0x80\n\t"
      "v_bitop3_b32 v64, v64, v99, s58 bitop3:0x80\n\t"
      "v_cmp_eq_u32 vcc, 0, v64\n\t"
      "s_nop 0\n\t"
      "s_cbranch_vccz LRX_2%=\n\t"
      "LRY_2%=:\n\t"
      "buffer_load_dwordx4 v[92:95], v60, s[36:39], s68 offen sc1\n\t"
      "buffer_load_dwordx4 v[96:99], v60, s[36:39], s68 offen offset:1024 sc1\n\t"
      "s_sub_u32 s63, s63, 1\n\t"
      "s_waitcnt vmcnt(0) lgkmcnt(0)\n\t"
      "v_bitop3_b32 v64, v92, v93, v94 bitop3:0x80\n\t"
      "v_bitop3_b32 v64, v64, v95, v96 bitop3:0x80\n\t"
      "v_bitop3_b32 v64, v64, v97, v98 bitop3:0x80\n\t"
      "v_bitop3_b32 v64, v64, v99, s58 bitop3:0x80\n\t"
      "v_cmp_eq_u32 vcc, 0, v64\n\t"
      "v_pk_fma_f32 v[28:29], v[92:93], s[56:57], v[126:127] op_sel:[0,1,0] op_sel_hi:[1,1,0]\n\t"
      "v_pk_fma_f32 v[30:31], v[94:95], s[56:57], v[126:127] op_sel:[0,1,0] op_sel_hi:[1,1,0]\n\t"
      "v_cvt_pk_bf16_f32 v24, v28, v29\n\t"
      "v_cvt_pk_bf16_f32 v25, v30, v31\n\t"
      "ds_write_b64 v127, v[24:25] offset:47104\n\t"
      "v_pk_fma_f32 v[16:17], v[96:97], s[56:57], v[126:127] op_sel:[0,1,0] op_sel_hi:[1,1,0]\n\t"
      "v_pk_fma_f32 v[18:19], v[98:99], s[56:57], v[126:127] op_sel:[0,1,0] op_sel_hi:[1,1,0]\n\t"
      "v_cvt_pk_bf16_f32 v26, v16, v17\n\t"
      "v_cvt_pk_bf16_f32 v27, v18, v19\n\t"
      "ds_write_b64 v127, v[26:27] offset:47112\n\t"
      "s_cmp_le_i32 s63, 0\n\t"
      "s_waitcnt lgkmcnt(0)\n\t"
      "s_cbranch_scc1 LRX_2%=\n\t"
      "s_cbranch_vccnz LRY_2%=\n\t"
      "LRX_2%=:\n\t"
      "s_cmp_eq_u32 s40, 63\n\t"
      "s_cbranch_scc1 LLAST_2%=\n\t"
      "s_branch LC1_2%=\n\t"
      "LLAST_2%=:\n\t"
      "s_waitcnt vmcnt(0) lgkmcnt(0)\n\t"
      "s_barrier\n\t"
      "ds_read_b128 v[28:31], v127 offset:48128\n\t"
      "ds_read_b128 v[16:19], v127 offset:45056\n\t"
      "ds_read_b128 v[20:23], v127 offset:46080\n\t"
      "v_smfmac_f32_16x16x64_bf16 v[40:43], v[24:27], a[176:183], v122\n\t"
      "v_smfmac_f32_16x16x64_bf16 v[44:47], v[24:27], a[240:247], v122\n\t"
      "s_waitcnt lgkmcnt(2)\n\t"
      "v_smfmac_f32_16x16x64_bf16 v[40:43], v[28:31], a[184:191], v122\n\t"
      "v_smfmac_f32_16x16x64_bf16 v[44:47], v[28:31], a[248:255], v122\n\t"
      "s_waitcnt lgkmcnt(1)\n\t"
      "v_smfmac_f32_16x16x64_bf16 v[44:47], v[16:19], a[224:231], v122\n\t"
      "v_smfmac_f32_16x16x64_bf16 v[40:43], v[16:19], a[160:167], v122\n\t"
      "s_waitcnt lgkmcnt(0)\n\t"
      "v_smfmac_f32_16x16x64_bf16 v[44:47], v[20:23], a[232:239], v122\n\t"
      "v_smfmac_f32_16x16x64_bf16 v[40:43], v[20:23], a[168:175], v122\n\t"
      "s_nop 7\n\t"
      "v_add_f32 v40, v40, v42\n\t"
      "v_add_f32 v41, v41, v43\n\t"
      "v_cvt_pk_bf16_f32 v120, v40, v41\n\t"
      "v_add_f32 v44, v44, v46\n\t"
      "v_add_f32 v45, v45, v47\n\t"
      "v_cvt_pk_bf16_f32 v121, v44, v45\n\t"
      "ds_write_b64 v125, v[120:121] offset:2080\n\t"
      "s_waitcnt lgkmcnt(0)\n\t"
      "s_barrier\n\t"
      "ds_read_b64_tr_b16 v[40:41], v124 offset:51232\n\t"
      "ds_read_b64_tr_b16 v[42:43], v124 offset:51752\n\t"
      "ds_read_b64_tr_b16 v[44:45], v124 offset:52272\n\t"
      "ds_read_b64_tr_b16 v[46:47], v124 offset:52792\n\t"
      "v_add_u32 v100, s55, v127\n\t"
      "ds_read_b128 v[60:63], v100 offset:57344\n\t"
      "s_waitcnt lgkmcnt(0)\n\t"
      "s_branch LROLE_2%=\n\t"
      "LFIN_2%=:\n\t"
      "s_waitcnt lgkmcnt(0)\n\t"
      "s_barrier\n\t"
      "s_branch LEND%=\n\t"
      "LR1_3%=:\n\t"
      "v_bitop3_b32 v64, v92, v93, v94 bitop3:0x80\n\t"
      "v_bitop3_b32 v64, v64, v95, v96 bitop3:0x80\n\t"
      "v_bitop3_b32 v64, v64, v97, v98 bitop3:0x80\n\t"
      "v_bitop3_b32 v64, v64, v99, s58 bitop3:0x80\n\t"
      "v_cmp_eq_u32 vcc, 0, v64\n\t"
      "s_nop 0\n\t"
      "s_cbranch_vccz LRX_3%=\n\t"
      "LRY_3%=:\n\t"
      "buffer_load_dwordx4 v[92:95], v60, s[36:39], s68 offen sc1\n\t"
      "buffer_load_dwordx4 v[96:99], v60, s[36:39], s68 offen offset:1024 sc1\n\t"
      "s_sub_u32 s63, s63, 1\n\t"
      "s_waitcnt vmcnt(0) lgkmcnt(0)\n\t"
      "v_bitop3_b32 v64, v92, v93, v94 bitop3:0x80\n\t"
      "v_bitop3_b32 v64, v64, v95, v96 bitop3:0x80\n\t"
      "v_bitop3_b32 v64, v64, v97, v98 bitop3:0x80\n\t"
      "v_bitop3_b32 v64, v64, v99, s58 bitop3:0x80\n\t"
      "v_cmp_eq_u32 vcc, 0, v64\n\t"
      "v_pk_fma_f32 v[24:25], v[92:93], s[56:57], v[126:127] op_sel:[0,1,0] op_sel_hi:[1,1,0]\n\t"
      "v_pk_fma_f32 v[26:27], v[94:95], s[56:57], v[126:127] op_sel:[0,1,0] op_sel_hi:[1,1,0]\n\t"
      "v_cvt_pk_bf16_f32 v28, v24, v25\n\t"
      "v_cvt_pk_bf16_f32 v29, v26, v27\n\t"
      "ds_write_b64 v127, v[28:29] offset:48128\n\t"
      "v_pk_fma_f32 v[20:21], v[96:97], s[56:57], v[126:127] op_sel:[0,1,0] op_sel_hi:[1,1,0]\n\t"
      "v_pk_fma_f32 v[22:23], v[98:99], s[56:57], v[126:127] op_sel:[0,1,0] op_sel_hi:[1,1,0]\n\t"
      "v_cvt_pk_bf16_f32 v30, v20, v21\n\t"
      "v_cvt_pk_bf16_f32 v31, v22, v23\n\t"
      "ds_write_b64 v127, v[30:31] offset:48136\n\t"
      "s_memtime s[88:89]\n\t"
      "s_cmp_le_i32 s63, 0\n\t"
      "s_waitcnt lgkmcnt(0)\n\t"
      "s_cbranch_scc1 LRX_3%=\n\t"
      "s_cbranch_vccnz LRY_3%=\n\t"
      "s_sub_u32 s88, s88, 336\n\t"
      "LRX_3%=:\n\t"
      "s_cmp_eq_u32 s40, 63\n\t"
      "s_cbranch_scc1 LLAST_3%=\n\t"
      "s_branch LC1_3%=\n\t"
      "LLAST_3%=:\n\t"
      "s_waitcnt vmcnt(0) lgkmcnt(0)\n\t"
      "s_barrier\n\t"
      "ds_read_b128 v[24:27], v127 offset:47104\n\t"
      "ds_read_b128 v[20:23], v127 offset:46080\n\t"
      "ds_read_b128 v[16:19], v127 offset:45056\n\t"
      "v_smfmac_f32_16x16x64_bf16 v[40:43], v[28:31], a[184:191], v122\n\t"
      "v_smfmac_f32_16x16x64_bf16 v[44:47], v[28:31], a[248:255], v122\n\t"
      "s_waitcnt lgkmcnt(2)\n\t"
      "v_smfmac_f32_16x16x64_bf16 v[40:43], v[24:27], a[176:183], v122\n\t"
      "v_smfmac_f32_16x16x64_bf16 v[44:47], v[24:27], a[240:247], v122\n\t"
      "s_waitcnt lgkmcnt(1)\n\t"
      "v_smfmac_f32_16x16x64_bf16 v[44:47], v[20:23], a[232:239], v122\n\t"
      "v_smfmac_f32_16x16x64_bf16 v[40:43], v[20:23], a[168:175], v122\n\t"
      "s_waitcnt lgkmcnt(0)\n\t"
      "v_smfmac_f32_16x16x64_bf16 v[44:47], v[16:19], a[224:231], v122\n\t"
      "v_smfmac_f32_16x16x64_bf16 v[40:43], v[16:19], a[160:167], v122\n\t"
      "s_nop 7\n\t"
      "v_add_f32 v40, v40, v42\n\t"
      "v_add_f32 v41, v41, v43\n\t"
      "v_cvt_pk_bf16_f32 v120, v40, v41\n\t"
      "v_add_f32 v44, v44, v46\n\t"
      "v_add_f32 v45, v45, v47\n\t"
      "v_cvt_pk_bf16_f32 v121, v44, v45\n\t"
      "ds_write_b64 v125, v[120:121] offset:2080\n\t"
      "s_waitcnt lgkmcnt(0)\n\t"
      "s_barrier\n\t"
      "ds_read_b64_tr_b16 v[40:41], v124 offset:51232\n\t"
      "ds_read_b64_tr_b16 v[42:43], v124 offset:51752\n\t"
      "ds_read_b64_tr_b16 v[44:45], v124 offset:52272\n\t"
      "ds_read_b64_tr_b16 v[46:47], v124 offset:52792\n\t"
      "v_add_u32 v100, s55, v127\n\t"
      "ds_read_b128 v[60:63], v100 offset:57344\n\t"
      "s_waitcnt lgkmcnt(0)\n\t"
      "s_branch LROLE_3%=\n\t"
      "LFIN_3%=:\n\t"
      "s_waitcnt lgkmcnt(0)\n\t"
      "s_barrier\n\t"
      "s_branch LEND%=\n\t"
      "LEND%=:\n\t"
      "s_waitcnt vmcnt(2) lgkmcnt(0)\n\t"
      "s_cmp_lg_u32 s42, 9\n\t"
      "s_cbranch_scc1 LOUT%=\n\t"
      "s_waitcnt vmcnt(0)\n\t"
      "LOUT%=:\n\t"
      : "+{v[116:123]}"(c0), "+{v[124:127]}"(c1), "+{v[0:15]}"(sc), "+{v[16:31]}"(s2), "+{v[100:107]}"(c3), "+{v[108:115]}"(cc), "+s"(A), "+s"(b), "+s"(wv), "+{s87}"(tch)
      :
      : "memory", "vcc", "scc", "v32", "v33", "v34", "v35", "v36", "v37", "v38", "v39", "v40", "v41", "v42", "v43", "v44", "v45", "v46", "v47", "v48", "v49", "v50", "v51", "v52", "v53", "v54", "v55", "v56", "v57", "v58", "v59", "v60", "v61", "v62", "v63", "v64", "v65", "v66", "v67", "v68", "v69", "v70", "v71", "v72", "v73", "v74", "v75", "v76", "v77", "v78", "v79", "v80", "v81", "v82", "v83", "v84", "v85", "v86", "v87", "v88", "v89", "v90", "v91", "v92", "v93", "v94", "v95", "v96", "v97", "v98", "v99", "v128", "v129", "v130", "v131", "v132", "v133", "v134", "v135", "v136", "v137", "v138", "v139", "v140", "v141", "v142", "v143", "v144", "v145", "v146", "v147", "v148", "v149", "v150", "v151", "v152", "v153", "v154", "v155", "v156", "v157", "v158", "v159", "v160", "v161", "v162", "v163", "v164", "v165", "v166", "v167", "v168", "v169", "v170", "v171", "v172", "v173", "v174", "v175", "v176", "v177", "v178", "v179", "v180", "v181", "v182", "v183", "v184", "v185", "v186", "v187", "v188", "v189", "v190", "v191", "v192", "v193", "v194", "v195", "v196", "v197", "v198", "v199", "v200", "v201", "v202", "v203", "v204", "v205", "v206", "v207", "v208", "v209", "v210", "v211", "v212", "v213", "v214", "v215", "v216", "v217", "v218", "v219", "v220", "v221", "v222", "v223", "v224", "v225", "v226", "v227", "v228", "v229", "v230", "v231", "v232", "v233", "v234", "v235", "v236", "v237", "v238", "v239", "v240", "v241", "v242", "v243", "v244", "v245", "v246", "v247", "v248", "v249", "v250", "v251", "v252", "v253", "v254", "v255", "a0", "a1", "a2", "a3", "a4", "a5", "a6", "a7", "a8", "a9", "a10", "a11", "a12", "a13", "a14", "a15", "a16", "a17", "a18", "a19", "a20", "a21", "a22", "a23", "a24", "a25", "a26", "a27", "a28", "a29", "a30", "a31", "a32", "a33", "a34", "a35", "a36", "a37", "a38", "a39", "a40", "a41", "a42", "a43", "a44", "a45", "a46", "a47", "a48", "a49", "a50", "a51", "a52", "a53", "a54", "a55", "a56", "a57", "a58", "a59", "a60", "a61", "a62", "a63", "a64", "a65", "a66", "a67", "a68", "a69", "a70", "a71", "a72", "a73", "a74", "a75", "a76", "a77", "a78", "a79", "a80", "a81", "a82", "a83", "a84", "a85", "a86", "a87", "a88", "a89", "a90", "a91", "a92", "a93", "a94", "a95", "a96", "a97", "a98", "a99", "a100", "a101", "a102", "a103", "a104", "a105", "a106", "a107", "a108", "a109", "a110", "a111", "a112", "a113", "a114", "a115", "a116", "a117", "a118", "a119", "a120", "a121", "a122", "a123", "a124", "a125", "a126", "a127", "a128", "a129", "a130", "a131", "a132", "a133", "a134", "a135", "a136", "a137", "a138", "a139", "a140", "a141", "a142", "a143", "a144", "a145", "a146", "a147", "a148", "a149", "a150", "a151", "a152", "a153", "a154", "a155", "a156", "a157", "a158", "a159", "a160", "a161", "a162", "a163", "a164", "a165", "a166", "a167", "a168", "a169", "a170", "a171", "a172", "a173", "a174", "a175", "a176", "a177", "a178", "a179", "a180", "a181", "a182", "a183", "a184", "a185", "a186", "a187", "a188", "a189", "a190", "a191", "a192", "a193", "a194", "a195", "a196", "a197", "a198", "a199", "a200", "a201", "a202", "a203", "a204", "a205", "a206", "a207", "a208", "a209", "a210", "a211", "a212", "a213", "a214", "a215", "a216", "a217", "a218", "a219", "a220", "a221", "a222", "a223", "a224", "a225", "a226", "a227", "a228", "a229", "a230", "a231", "a232", "a233", "a234", "a235", "a236", "a237", "a238", "a239", "a240", "a241", "a242", "a243", "a244", "a245", "a246", "a247", "a248", "a249", "a250", "a251", "a252", "a253", "a254", "a255", "s22", "s23", "s24", "s25", "s26", "s27", "s28", "s29", "s30", "s31", "s36", "s37", "s38", "s39", "s40", "s41", "s42", "s43", "s44", "s45", "s46", "s47", "s48", "s49", "s50", "s51", "s52", "s53", "s54", "s55", "s56", "s57", "s58", "s59", "s60", "s61", "s62", "s63", "s64", "s65", "s66", "s67", "s68", "s69", "s70", "s71", "s72", "s73", "s74", "s75", "s76", "s77", "s78", "s79", "s80", "s81", "s82", "s83", "s84", "s85", "s86", "s88", "s89", "s90", "s91", "s92", "s93", "s94", "s95", "s96", "s97", "s98", "s99");
}

#define SSLEEP 1
#define TSLEEP 4
__device__ __forceinline__ unsigned mkkey(unsigned w, int idx) {
  unsigned u = w >> 16;
  u = (u & 0x7fffu) == 0 ? 0u : u;
  unsigned o = (u & 0x8000u) ? (~u & 0xffffu) : (u | 0x8000u);
  o = (u & 0x7fffu) > 0x7f80u ? 0u : o;
  return (o << 16) | (0xffffu - (unsigned)idx);
}
template <bool spread>
__device__ __forceinline__ u32x2 top2(const float* rbuf, int tid, int need, unsigned* sk) {
  unsigned key[9];
  const __amdgpu_buffer_rsrc_t rrs = __builtin_amdgcn_make_buffer_rsrc((void*)rbuf, 0, spread ? 2 * S_ * 4 + 256 * 128 : S_ * 4, 0x00020000);
  const int t8 = spread && tid >= 224 ? 0 : tid * 8;
#pragma unroll 1
  for (unsigned spins = 0; spins < (1u << 20); ++spins) {
    asm volatile("" ::: "memory");
    const u32x4 a = ld_sc1(rrs, t8 * 4), b = ld_sc1(rrs, t8 * 4 + 16);
    key[8] = spread ? __builtin_amdgcn_raw_buffer_load_b32(rrs, 2 * S_ * 4 + tid * 128, 0, 16) : a[0];
    unsigned mx = key[8];
#pragma unroll
    for (int k = 0; k < 4; ++k) { key[k] = a[k]; key[4 + k] = b[k]; mx = max(mx, max(a[k], b[k])); }
    if (mx != ~0u || tid * 8 >= need) break;
    __builtin_amdgcn_s_sleep(TSLEEP);
  }
  unsigned m = 0;
#pragma unroll
  for (int k = 0; k < 9; ++k) {
    key[k] = mkkey(key[k], k < 8 ? t8 + k : (spread ? S_ - 256 + tid : t8));
    m = max(m, key[k]);
  }
  m = wave_max_u32(m);
  __syncthreads();
  if ((tid & 63) == 0) sk[tid >> 6] = m;
  __syncthreads();
  const unsigned k0 = max(max(sk[0], sk[1]), max(sk[2], sk[3]));
  m = 0;
#pragma unroll
  for (int k = 0; k < 9; ++k) m = max(m, key[k] == k0 ? 0u : key[k]);
  m = wave_max_u32(m);
  if ((tid & 63) == 0) sk[4 + (tid >> 6)] = m;
  __syncthreads();
  const unsigned k1 = max(max(sk[4], sk[5]), max(sk[6], sk[7]));
  return (u32x2){(unsigned)__builtin_amdgcn_readfirstlane(k0), (unsigned)__builtin_amdgcn_readfirstlane(k1)};
}
__device__ __forceinline__ u32x2 top2_wave(const float* rbuf, int lane, bool dry, bool strict) {
  const __amdgpu_buffer_rsrc_t rrs = __builtin_amdgcn_make_buffer_rsrc((void*)rbuf, 0, S_ * 4, 0x00020000);
  asm volatile("" ::: "memory");
  u32x4 a[8];
#pragma unroll
  for (int i = 0; i < 8; ++i) a[i] = (u32x4){0u, 0u, 0u, 0u};
  if (!dry) {
#pragma unroll
    for (int i = 0; i < 8; ++i) a[i] = ld_sc1(rrs, (i * 64 + lane) * 16);
  }
  if (SNEED) {
    unsigned mx = 0;
#pragma unroll
    for (int i = 0; i < 8; ++i) {
      const unsigned x = max(max(a[i][0], a[i][1]), max(a[i][2], a[i][3]));
      mx = max(mx, i * 8 + (lane >> 3) < SNEED ? x : 0u);
    }
    if (strict && __builtin_amdgcn_ballot_w64(mx == ~0u)) return (u32x2){0u, 0u};
  }
  unsigned m1 = 0, m2 = 0;
#pragma unroll
  for (int i = 0; i < 32; ++i) {
    const unsigned w = a[i >> 2][i & 3], o = w ^ ((unsigned)((int)w >> 31) | 0x80000000u);
    const unsigned k = (o & 0xffff0000u) | ((0xffffu - (unsigned)(((i >> 2) * 64 + lane) * 4 + (i & 3))) & ~w);
    m2 = max(min(m1, k), m2);
    m1 = max(m1, k);
  }
  unsigned k[2];
  k[0] = wave_max_u32(m1);
  k[1] = wave_max_u32(m1 == k[0] ? m2 : m1);
  if (!k[1]) return (u32x2){0u, 0u};
#pragma unroll
  for (int i = 0; i < 2; ++i) k[i] = mkkey((k[i] >> 31) ? k[i] & 0x7fffffffu : ~k[i], 0xffff - (k[i] & 0xffff));
  return (u32x2){max(k[0], k[1]), min(k[0], k[1])};
}
__device__ __forceinline__ bool other_top2a(const float* rbuf, int tid, unsigned k0, unsigned k1) {
  const __amdgpu_buffer_rsrc_t rrs = __builtin_amdgcn_make_buffer_rsrc((void*)rbuf, 0, S_ * 4, 0x00020000);
  const int t8 = tid >= 224 ? 0 : tid * 8;
  bool bad = false;
#pragma unroll 1
  for (unsigned spins = 0; spins < (1u << 20); ++spins) {
    asm volatile("" ::: "memory");
    const u32x4 a = ld_sc1(rrs, t8 * 4), b = ld_sc1(rrs, t8 * 4 + 16);
    unsigned mx = 0;
    bad = false;
#pragma unroll
    for (int k = 0; k < 4; ++k) {
      const unsigned ka = mkkey(a[k], t8 + k), kb = mkkey(b[k], t8 + 4 + k);
      bad |= (ka > k1 && ka != k0) || (kb > k1 && kb != k0);
      mx = max(mx, max(a[k], b[k]));
    }
    if (__builtin_amdgcn_ballot_w64(mx == ~0u) == 0) break;
    __builtin_amdgcn_s_sleep(TSLEEP);
  }
  return __builtin_amdgcn_ballot_w64(bad) != 0;
}
__device__ __forceinline__ bool other_top2b(const float* rbuf, int tid, unsigned k0, unsigned k1, bool bad, unsigned* sk, unsigned& tmp, unsigned f) {
  if (RDLY && (f >> 16) == 63) {
    const unsigned t = f + RDLY - (3 - (tid >> 6)) * RDL2;
#pragma unroll 1
    while (((t - (unsigned)__builtin_amdgcn_s_memrealtime()) & 0xffffu) < 400u) __builtin_amdgcn_s_sleep(1);
  }
  const __amdgpu_buffer_rsrc_t rrs = __builtin_amdgcn_make_buffer_rsrc((void*)rbuf, 0, 2 * S_ * 4 + 256 * 128, 0x00020000);
  unsigned w = RDLY ? __builtin_amdgcn_raw_buffer_load_b32(rrs, 2 * S_ * 4 + tid * 128, 0, 16) : ~0u, n;
  tmp = 0;
  if (!RDLY || __builtin_amdgcn_ballot_w64(w == ~0u)) {
  asm volatile("buffer_load_dword %1, %3, %4, 0 offen sc1\n\ts_sleep %5\n\t"
               "buffer_load_dword %1, %3, %4, 0 offen sc1\n\ts_sleep %5\n\t"
               "s_mov_b32 %2, 0x100000\n"
               "TP%=:\n\t"
               "buffer_load_dword %1, %3, %4, 0 offen sc1\n\t"
               "s_sub_u32 %2, %2, 1\n\t"
               "s_waitcnt vmcnt(2)\n\t"
               "v_mov_b32 %0, %1\n\t"
               "v_cmp_eq_u32 vcc, -1, %0\n\t"
               "s_cbranch_scc1 TQ%=\n\t"
               "s_cbranch_vccnz TP%=\n"
               "TQ%=:"
               : "=&v"(w), "=&v"(tmp), "=&s"(n) : "v"(2 * S_ * 4 + tid * 128), "s"(rrs), "n"(PSLEEP) : "memory", "vcc", "scc");
  }
  const unsigned k = mkkey(w, S_ - 256 + tid);
  bad |= k > k1 && k != k0;
  const unsigned any = __builtin_amdgcn_ballot_w64(bad) != 0;
  if ((tid & 63) == 0) sk[tid >> 6] = any;
  LDS_BARRIER();
  const u32x4 v = *(const u32x4*)sk;
  return __builtin_amdgcn_readfirstlane(v[0] | v[1] | v[2] | v[3]) != 0;
}

constexpr int M1_WGS = FF / 16;
constexpr int L_WD = 131072;
__device__ __forceinline__ void wait_set(gu32* f) {
#pragma unroll 1
  for (unsigned spins = 0; spins < (1u << 18); ++spins) {
    if (__hip_atomic_load(f, RLX_AGENT) != ~0u) return;
    __builtin_amdgcn_s_sleep(4);
  }
}
__device__ __forceinline__ u32x2 wait_selt(char* ws, int b, unsigned& tm) {
  const __amdgpu_buffer_rsrc_t rs = __builtin_amdgcn_make_buffer_rsrc((void*)(ws + O_CNT), 0, 1024, 0x00020000);
  u32x4 v;
#pragma unroll 1
  for (unsigned spins = 0; spins < (1u << 18); ++spins) {
    asm volatile("" ::: "memory");
    v = ld_sc1(rs, (b & 7) * 64);
    if (v[1]) break;
    __builtin_amdgcn_s_sleep(FSLEEP);
  }
  tm = __builtin_amdgcn_readfirstlane(v[2]);
  return (u32x2){(unsigned)__builtin_amdgcn_readfirstlane(v[0]), (unsigned)__builtin_amdgcn_readfirstlane(v[1])};
}
__device__ __forceinline__ u32x2 wait_sel(gu32* f, u64 v = 0) {
#pragma unroll 1
  for (unsigned spins = 0; spins < (1u << 18) && !v; ++spins) {
    v = __hip_atomic_load((gu64*)f, RLX_AGENT);
    if (v) break;
    __builtin_amdgcn_s_sleep(FSLEEP);
  }
  return (u32x2){(unsigned)__builtin_amdgcn_readfirstlane((unsigned)v), (unsigned)__builtin_amdgcn_readfirstlane((unsigned)(v >> 32))};
}
__device__ __forceinline__ unsigned code_ahead(int tid) {
  unsigned d = 0;
  if (MPF) { u64 pc; asm volatile("s_getpc_b64 %1\n\tglobal_load_dword %0, %2, %1" : "=v"(d), "=&s"(pc) : "v"((tid & 127) * 64) : "memory"); }
  return d;
}
__device__ __forceinline__ bf16x8 as_frag(u32x4 v) { return __builtin_bit_cast(bf16x8, v); }
__device__ __forceinline__ u32x4 tr_frag(char* p0, char* p1) {
  const s16x4 a = __builtin_amdgcn_ds_read_tr16_b64_v4i16((LAS s16x4*)p0), b = __builtin_amdgcn_ds_read_tr16_b64_v4i16((LAS s16x4*)p1);
  const u32x2 x = __builtin_bit_cast(u32x2, a), y = __builtin_bit_cast(u32x2, b);
  return (u32x4){x[0], x[1], y[0], y[1]};
}
__device__ __forceinline__ void mlp1_role(KArgs P, char* lds, int b, int tid) {
  float* part = (float*)lds;
  unsigned* si = (unsigned*)(lds + 4096);
  const int lane = tid & 63, wv = tid >> 6, l15 = lane & 15, l4 = lane >> 4;
  const __amdgpu_buffer_rsrc_t lrs = __builtin_amdgcn_make_buffer_rsrc((void*)(P->ws + O_LNH), 0, S_ * DM * 2, 0x00020000);
  const __amdgpu_buffer_rsrc_t mrs = __builtin_amdgcn_make_buffer_rsrc((void*)(P->ws + O_MFRAG), 0, 2 * DM * FF * 2, 0x00020000);
  u32x4 aw[2][4];
  MS(0);
  unsigned ca = code_ahead(tid);
  const u64 s0 = __hip_atomic_load((gu64*)(P->ws + O_CNT + (b & 7) * 64), RLX_AGENT);
  const unsigned fl = __hip_atomic_load((gu32*)(P->ws + O_KVSTEP + (b >> 2) * 64 + (b & 3) * 4), RLX_AGENT);
#pragma unroll
  for (int i = 0; i < 8; ++i) aw[i >> 2][i & 3] = ld_sc1(mrs, (unsigned)(b * 32768 + (i >> 2) * 16384 + (wv * 4 + (i & 3)) * 1024 + lane * 16));
  u32x2 sel = wait_sel((gu32*)(P->ws + O_CNT) + (b & 7) * 16, s0);
  asm volatile("" :: "v"(ca));
  MS(1);
  int round = 0;
#pragma unroll 1
  for (;;) {
    const int idx = 0xffff - (((l15 & 1) ? sel[1] : sel[0]) & 0xffff);
    if (round == 2) wait_set((gu32*)(P->ws + O_RBUF) + S_ + idx);
    u32x4 bx[4];
#pragma unroll
    for (int i = 0; i < 4; ++i) bx[i] = ld_sc1(lrs, (unsigned)(idx * 1024 + (wv * 4 + i) * 64 + l4 * 16));
    f32x4 ag = {0.f, 0.f, 0.f, 0.f}, au = {0.f, 0.f, 0.f, 0.f};
#pragma unroll
    for (int i = 0; i < 4; ++i) {
      ag = __builtin_amdgcn_mfma_f32_16x16x32_bf16(as_frag(aw[0][i]), as_frag(bx[i]), ag, 0, 0, 0);
      au = __builtin_amdgcn_mfma_f32_16x16x32_bf16(as_frag(aw[1][i]), as_frag(bx[i]), au, 0, 0, 0);
    }
    if (l15 < 2) {
      *(f32x4*)(part + wv * 64 + l15 * 16 + l4 * 4) = ag;
      *(f32x4*)(part + wv * 64 + 32 + l15 * 16 + l4 * 4) = au;
    }
    LDS_BARRIER();
    MS(2);
    if (tid < 16) {
      float a[2];
#pragma unroll
      for (int e = 0; e < 2; ++e) {
        const float* q = part + tid * 2 + e;
        const float g = rbf((q[0] + q[64]) + (q[128] + q[192])), u = rbf((q[32] + q[96]) + (q[160] + q[224]));
        a[e] = rbf(g / (1.0f + __expf(-g))) * u;
      }
      unsigned av = pk2(a[0], a[1]);
      av = av == ~0u ? 0x7fc07fc0u : av;
      __hip_atomic_store((gu32*)(P->ws + O_ACT) + (round * M1_WGS + b) * 32 + tid, av, RLX_AGENT);
    }
    MS(4);
    if (round == 2) break;
    if (SW2 && !round) {
      const u32x2 s2 = wait_sel((gu32*)(P->ws + O_SEL2) + (b & 31) * 32);
      if (s2[0] != sel[0] || s2[1] != sel[1]) { sel = s2; round = 1; __syncthreads(); continue; }
    }
    unsigned tmp;
    const bool again = other_top2b((const float*)(P->ws + O_RBUF), tid, sel[0], sel[1], other_top2a((const float*)(P->ws + O_RBUF), tid, sel[0], sel[1]), si, tmp, __builtin_amdgcn_readfirstlane(fl));
    MS(5);
    asm volatile("s_waitcnt vmcnt(0)" :: "v"(tmp) : "memory");
    MS(6);
    if (!again) break;
    sel = top2<true>((const float*)(P->ws + O_RBUF), tid, S_, si + 4);
    round = 2;
  }
}
__device__ __forceinline__ void mlp2_role(KArgs P, char* lds, int b, int tid) {
  float* part = (float*)lds;
  unsigned* si = (unsigned*)(lds + 4096);
  const int lane = tid & 63, wv = tid >> 6, l15 = lane & 15, l4 = lane >> 4;
  const __amdgpu_buffer_rsrc_t hrs = __builtin_amdgcn_make_buffer_rsrc((void*)(P->ws + O_HBUF), 0, S_ * DM * 2, 0x00020000);
  const __amdgpu_buffer_rsrc_t lrs = __builtin_amdgcn_make_buffer_rsrc((void*)(P->ws + O_LNH), 0, S_ * DM * 2, 0x00020000);
  const __amdgpu_buffer_rsrc_t ors = __builtin_amdgcn_make_buffer_rsrc((void*)P->out, 0, S_ * DM * 2, 0x00020000);
  const __amdgpu_buffer_rsrc_t ars = __builtin_amdgcn_make_buffer_rsrc((void*)(P->ws + O_ACT), 0, 3 * M1_WGS * 128, 0x00020000);
  u32x4 aw[7];
  const int c0 = b * 8;
  MS(0);
  unsigned ca = code_ahead(tid);
  {
    char* p = lds + L_WD + (((l15 >> 1) & 1) * 896 + wv * 224 + l4 * 8 + (l15 >> 2)) * 16 + (l15 & 1) * 8;
#pragma unroll
    for (int i = 0; i < 7; ++i) aw[i] = tr_frag(p + i * 512, p + i * 512 + 64);
  }
  unsigned tsel;
  u32x2 sel = wait_selt(P->ws, b, tsel);
  asm volatile("" :: "v"(ca));
  const unsigned fl = __hip_atomic_load((gu32*)(P->ws + O_KVSTEP + (c0 >> 4) * 64), RLX_AGENT);
  MS(1);
  int round = 0;
#pragma unroll 1
  for (;;) {
    const int idx = 0xffff - (((tid & 8) ? sel[1] : sel[0]) & 0xffff), col = c0 + (tid & 7);
    if (round == 2) wait_set((gu32*)(P->ws + O_RBUF) + S_ + idx);
    const float rr = __hip_atomic_load((const GAS float*)(P->ws + O_RBUF) + S_ + idx, RLX_AGENT);
    const float lv = bf2f(__builtin_amdgcn_raw_buffer_load_b16(lrs, (unsigned)(idx * DM + col) * 2, 0, 16));
    const float hv = bf2f(__builtin_amdgcn_raw_buffer_load_b16(hrs, (unsigned)(idx * DM + col) * 2, 0, 16));
    const float sg = rbf(1.0f / (1.0f + __expf(-rr)));
    const bool bad = other_top2a((const float*)(P->ws + O_RBUF), tid, sel[0], sel[1]);
    if (ADLY && !round) {
#pragma unroll 1
      while (tsel + ADLY - (unsigned)__builtin_amdgcn_s_memrealtime() < 400u) __builtin_amdgcn_s_sleep(1);
    } else if (!round && (ASALL || c0 < 256)) __builtin_amdgcn_s_sleep(ASLEEP);
    u32x4 av[7];
    u64 v2 = 0;
#pragma unroll 1
    for (unsigned spins = 0; spins < (1u << 20); ++spins) {
      asm volatile("" ::: "memory");
      unsigned mx = 0;
      if (SW2) v2 = __hip_atomic_load((gu64*)(P->ws + O_SEL2 + (b & 31) * 128), RLX_AGENT);
#pragma unroll
      for (int i = 0; i < 7; ++i) {
        av[i] = ld_sc1(ars, (unsigned)((round * M1_WGS + ((l15 >> 1) & 1) * 56 + (wv * 7 + i) * 2 + (l4 >> 1)) * 128 + (l15 & 1) * 32 + (l4 & 1) * 16));
        mx = max(max(mx, max(av[i][0], av[i][1])), max(av[i][2], av[i][3]));
      }
      if (__builtin_amdgcn_ballot_w64(mx == ~0u) == 0) break;
      __builtin_amdgcn_s_sleep(4);
    }
    MS(2);
    if (SW2 && !round) {
      const u32x2 s2 = wait_sel((gu32*)(P->ws + O_SEL2) + (b & 31) * 32, v2);
      if (s2[0] != sel[0] || s2[1] != sel[1]) { sel = s2; round = 1; continue; }
    }
    f32x4 a0 = {0.f, 0.f, 0.f, 0.f}, a1 = {0.f, 0.f, 0.f, 0.f};
#pragma unroll
    for (int i = 0; i < 7; ++i) {
      if (i & 1) a1 = __builtin_amdgcn_mfma_f32_16x16x32_bf16(as_frag(aw[i]), as_frag(av[i]), a1, 0, 0, 0);
      else a0 = __builtin_amdgcn_mfma_f32_16x16x32_bf16(as_frag(aw[i]), as_frag(av[i]), a0, 0, 0, 0);
    }
    if ((l15 >> 1) == (l4 >> 1)) *(f32x4*)(part + wv * 32 + l15 * 8 + (l4 & 1) * 4) = a0 + a1;
    LDS_BARRIER();
    const float* q = part + (tid & 15);
    const float y = rbf(((q[0] + q[16]) + (q[32] + q[48])) + ((q[64] + q[80]) + (q[96] + q[112])));
    const float upd = rbf(y * sg);
    const float bb = rbf(lv + upd);
    const u16 res = f2bf(hv + bb);
    MS(4);
    if (tid < 16) __builtin_amdgcn_raw_buffer_store_b16(res, ors, (unsigned)(idx * DM + col) * 2, 0, 16);
    if (round == 2) break;
    unsigned tmp;
    const bool again = other_top2b((const float*)(P->ws + O_RBUF), tid, sel[0], sel[1], bad, si, tmp, __builtin_amdgcn_readfirstlane(fl));
    MS(5);
    asm volatile("s_waitcnt vmcnt(0)" :: "v"(tmp) : "memory");
    MS(6);
    if (!again) break;
    if (tid < 16) __builtin_amdgcn_raw_buffer_store_b16(f2bf(hv + lv), ors, (unsigned)(idx * DM + col) * 2, 0, 16);
    VM_DRAIN();
    sel = top2<true>((const float*)(P->ws + O_RBUF), tid, S_, si + 4);
    round = 2;
  }
}

struct WoRegs { u32x32 a, b, c, d; };
__device__ __forceinline__ unsigned worker_role(int& w, int& wv, KArgs& A, WoRegs& wo) {
  unsigned tag;
  asm volatile(
      "GSTART:\n\t"
      "v_mbcnt_lo_u32_b32 v96, -1, 0\n\t"
      "v_mbcnt_hi_u32_b32 v96, -1, v96\n\t"
      "s_and_b32 s82, %0, 31\n\t"
      "s_mov_b32 s66, 4194304\n\t"
      "s_mov_b32 s67, 0x20000\n\t"
      "s_mov_b32 s62, 111181824\n\t"
      "s_mov_b32 s63, 0x20000\n\t"
      "v_and_b32 v104, 3, v96\n\t"
      "v_lshrrev_b32 v105, 2, v96\n\t"
      "s_lshl_b32 s78, %0, 14\n\t"
      "s_lshl_b32 s79, %1, 12\n\t"
      "s_add_u32 s79, s79, s78\n\t"
      "v_lshlrev_b32 v106, 10, v104\n\t"
      "v_lshl_add_u32 v106, v105, 4, v106\n\t"
      "v_lshlrev_b32 v99, 13, v105\n\t"
      "v_lshl_add_u32 v99, v104, 4, v99\n\t"
      "s_lshl_b32 s80, s82, 8\n\t"
      "s_lshl_b32 s72, %1, 6\n\t"
      "s_add_u32 s80, s80, s72\n\t"
      "v_add_u32 v99, s80, v99\n\t"
      "s_bitcmp1_b32 %0, 5\n\t"
      "s_waitcnt lgkmcnt(0)\n\t"
      "s_mov_b64 s[64:65], s[36:37]\n\t"
      "s_mov_b64 s[60:61], s[58:59]\n\t"
      "buffer_load_dwordx4 v[64:67], v106, s[64:67], s79 offen \n\t"
      "buffer_load_dwordx4 v[68:71], v106, s[64:67], s79 offen offset:256 \n\t"
      "buffer_load_dwordx4 v[72:75], v106, s[64:67], s79 offen offset:512 \n\t"
      "buffer_load_dwordx4 v[76:79], v106, s[64:67], s79 offen offset:768 \n\t"
      "s_cselect_b64 s[38:39], s[44:45], s[42:43]\n\t"
      "s_mov_b32 s78, 0\n\t"
      "s_load_dword s87, s[38:39], s78\n\t"
      "s_mov_b32 s78, 4194300\n\t"
      "s_load_dword s87, s[38:39], s78\n\t"
      "s_mov_b32 s78, 128\n\t"
      "s_load_dword s87, s[60:61], s78\n\t"
      "s_mov_b32 s78, 6520704\n\t"
      "s_load_dword s87, s[60:61], s78\n\t"
      "v_mov_b32 v118, v106\n\t"
      "s_mov_b64 s[64:65], s[36:37]\n\t"
      "s_lshr_b32 s78, %0, 6\n\t"
      "s_lshl_b32 s78, s78, 15\n\t"
      "s_lshl_b32 s79, %1, 12\n\t"
      "s_add_u32 s79, s79, s78\n\t"
      "buffer_load_dwordx4 v[128:131], v118, s[64:67], s79 offen \n\t"
      "buffer_load_dwordx4 v[132:135], v118, s[64:67], s79 offen offset:256 \n\t"
      "buffer_load_dwordx4 v[136:139], v118, s[64:67], s79 offen offset:512 \n\t"
      "buffer_load_dwordx4 v[140:143], v118, s[64:67], s79 offen offset:768 \n\t"
      "s_add_u32 s79, s79, 16384\n\t"
      "buffer_load_dwordx4 v[144:147], v118, s[64:67], s79 offen \n\t"
      "buffer_load_dwordx4 v[148:151], v118, s[64:67], s79 offen offset:256 \n\t"
      "buffer_load_dwordx4 v[152:155], v118, s[64:67], s79 offen offset:512 \n\t"
      "buffer_load_dwordx4 v[156:159], v118, s[64:67], s79 offen offset:768 \n\t"
      "s_mov_b64 s[64:65], s[38:39]\n\t"
      "s_mov_b64 s[64:65], s[38:39]\n\t"
      "buffer_load_dword v107, off, s[60:63], 0 sc1\n\t"
      "v_lshrrev_b32 v101, 4, v96\n\t"
      "v_and_b32 v102, 15, v96\n\t"
      "v_lshlrev_b32 v103, 2, v101\n\t"
      "v_xor_b32 v102, v102, v103\n\t"
      "v_lshlrev_b32 v108, 13, v101\n\t"
      "v_lshl_add_u32 v108, v102, 4, v108\n\t"
      "s_lshl_b32 s78, s82, 8\n\t"
      "s_lshl_b32 s79, %1, 16\n\t"
      "s_add_u32 s78, s78, s79\n\t"
      "v_add_u32 v108, s78, v108\n\t"
      "s_lshl_b32 s76, %1, 11\n\t"
      "s_mov_b32 s77, 0\n\t"
      "s_add_u32 m0, s76, 0\n\t"
      "s_add_u32 s68, s77, 0\n\t"
      "buffer_load_dwordx4 v108, s[64:67], s68 offen lds\n\t"
      "s_add_u32 m0, s76, 1024\n\t"
      "s_add_u32 s68, s77, 32768\n\t"
      "s_add_u32 s76, s76, 8192\n\t"
      "s_add_u32 s77, s77, 0x40000\n\t"
      "buffer_load_dwordx4 v108, s[64:67], s68 offen lds\n\t"
      "s_mov_b32 s84, 0xffff0000\n\t"
      "s_mov_b32 s86, 0\n\t"
      "s_lshl_b32 s85, %1, 13\n\t"
      "v_lshlrev_b32 v97, 4, v96\n\t"
      "v_lshrrev_b32 v101, 4, v96\n\t"
      "v_and_b32 v102, 3, v105\n\t"
      "v_lshlrev_b32 v100, 9, v101\n\t"
      "v_lshl_add_u32 v100, v102, 6, v100\n\t"
      "v_lshl_add_u32 v100, v104, 4, v100\n\t"
      "v_lshlrev_b32 v106, 10, v101\n\t"
      "v_lshl_add_u32 v106, v102, 8, v106\n\t"
      "v_lshl_add_u32 v106, v104, 4, v106\n\t"
      "s_lshl_b32 s74, %0, 14\n\t"
      "s_add_u32 s74, s74, s72\n\t"
      "s_add_u32 s74, s74, 6520832\n\t"
      "s_lshr_b32 s78, %0, 6\n\t"
      "v_min_u32 v104, 31, v96\n\t"
      "v_lshl_add_u32 v104, v104, 1, s78\n\t"
      "s_lshr_b32 s79, %0, 5\n\t"
      "s_and_b32 s83, s79, 1\n\t"
      "s_add_u32 s80, s85, 6520832\n\t"
      "v_lshl_add_u32 v101, v104, 15, s80\n\t"
      "s_mul_i32 s80, s82, 24576\n\t"
      "s_lshl_b32 s72, %1, 11\n\t"
      "s_add_u32 s80, s80, s72\n\t"
      "s_add_u32 s80, s80, 8617984\n\t"
      "s_lshl_b32 s72, s83, 13\n\t"
      "s_add_u32 s80, s80, s72\n\t"
      "v_mov_b32 v102, s80\n\t"
      "s_mov_b32 s72, 786432\n\t"
      "v_mad_u32_u24 v102, v104, s72, v102\n\t"
      "s_lshl_b32 s80, s82, 6\n\t"
      "s_lshl_b32 s72, s83, 2\n\t"
      "s_add_u32 s80, s80, s72\n\t"
      "s_add_u32 s80, s80, 256\n\t"
      "v_lshrrev_b32 v105, 2, v104\n\t"
      "v_lshl_add_u32 v103, v105, 11, s80\n\t"
      "v_and_b32 v104, 3, v104\n\t"
      "v_lshl_add_u32 v103, v104, 4, v103\n\t"
      "s_lshl_b32 s80, s82, 7\n\t"
      "s_lshl_b32 s79, s79, 2\n\t"
      "s_add_u32 s80, s80, s79\n\t"
      "s_add_u32 s80, s80, 33024\n\t"
      "v_writelane_b32 v103, s80, 0\n\t"
      "v_writelane_b32 v103, 0, 63\n\t"
      "s_cmp_eq_u32 %1, 0\n\t"
      "s_cselect_b64 s[90:91], 1, 0\n\t"
      "v_lshrrev_b32 v104, 4, v96\n\t"
      "v_bfe_u32 v105, v96, 2, 2\n\t"
      "v_and_b32 v110, 3, v96\n\t"
      "s_lshl_b32 s78, %1, 2\n\t"
      "v_lshl_add_u32 v109, v104, 3, v105\n\t"
      "v_lshlrev_b32 v109, 8, v109\n\t"
      "v_or_b32 v110, s78, v110\n\t"
      "v_lshlrev_b32 v105, 2, v105\n\t"
      "v_xor_b32 v110, v110, v105\n\t"
      "v_lshl_add_u32 v109, v110, 4, v109\n\t"
      "s_lshl_b32 s78, %1, 6\n\t"
      "s_add_u32 s78, s78, 131072\n\t"
      "v_add_u32 v110, s78, v106\n\t"
      "s_bfe_u32 s80, %0, 0x10005\n\t"
      "s_and_b32 s79, %1, 1\n\t"
      "v_bfe_u32 v105, v96, 4, 1\n\t"
      "v_lshlrev_b32 v105, 1, v105\n\t"
      "v_and_b32 v116, 7, v96\n\t"
      "v_xor_b32 v116, v116, v105\n\t"
      "s_lshl_b32 s78, s79, 2\n\t"
      "v_xor_b32 v116, s78, v116\n\t"
      "v_lshlrev_b32 v116, 4, v116\n\t"
      "v_lshrrev_b32 v105, 3, v96\n\t"
      "v_lshl_add_u32 v116, v105, 13, v116\n\t"
      "s_lshl_b32 s78, s82, 8\n\t"
      "s_lshl_b32 s72, s80, 7\n\t"
      "s_add_u32 s78, s78, s72\n\t"
      "s_lshl_b32 s72, %1, 16\n\t"
      "s_add_u32 s78, s78, s72\n\t"
      "v_add_u32 v116, s78, v116\n\t"
      "v_bfe_u32 v105, v96, 2, 2\n\t"
      "v_lshl_add_u32 v117, v104, 3, v105\n\t"
      "v_lshlrev_b32 v117, 7, v117\n\t"
      "v_bfe_u32 v105, v96, 3, 2\n\t"
      "v_xor_b32 v105, %1, v105\n\t"
      "v_lshl_add_u32 v117, v105, 5, v117\n\t"
      "v_and_b32 v105, 3, v96\n\t"
      "v_lshl_add_u32 v117, v105, 3, v117\n\t"
      "v_and_b32 v111, 15, v96\n\t"
      "v_lshlrev_b32 v111, 4, v111\n\t"
      "v_and_b32 v105, 1, v104\n\t"
      "v_lshl_add_u32 v111, v105, 10, v111\n\t"
      "v_lshrrev_b32 v105, 1, v104\n\t"
      "v_lshl_add_u32 v111, v105, 8, v111\n\t"
      "s_lshl_b32 s78, s79, 9\n\t"
      "s_add_u32 s78, s78, 16384\n\t"
      "s_lshl_b32 s80, s80, 12\n\t"
      "s_sub_u32 s78, s78, s80\n\t"
      "s_add_u32 s80, %1, 1\n\t"
      "s_lshr_b32 s80, s80, 1\n\t"
      "s_lshl_b32 s80, s80, 11\n\t"
      "s_sub_u32 s78, s78, s80\n\t"
      "v_add_u32 v111, s78, v111\n\t"
      "s_mov_b32 s69, 0\n\t"
      "s_add_u32 m0, s76, 0\n\t"
      "s_add_u32 s68, s77, 0\n\t"
      "buffer_load_dwordx4 v108, s[64:67], s68 offen lds\n\t"
      "s_add_u32 m0, s76, 1024\n\t"
      "s_add_u32 s68, s77, 32768\n\t"
      "s_add_u32 s76, s76, 8192\n\t"
      "s_add_u32 s77, s77, 0x40000\n\t"
      "buffer_load_dwordx4 v108, s[64:67], s68 offen lds\n\t"
      "s_add_u32 m0, s76, 0\n\t"
      "s_add_u32 s68, s77, 0\n\t"
      "buffer_load_dwordx4 v108, s[64:67], s68 offen lds\n\t"
      "s_add_u32 m0, s76, 1024\n\t"
      "s_add_u32 s68, s77, 32768\n\t"
      "s_add_u32 s76, s76, 8192\n\t"
      "s_add_u32 s77, s77, 0x40000\n\t"
      "buffer_load_dwordx4 v108, s[64:67], s68 offen lds\n\t"
      "s_add_u32 m0, s76, 0\n\t"
      "s_add_u32 s68, s77, 0\n\t"
      "buffer_load_dwordx4 v108, s[64:67], s68 offen lds\n\t"
      "s_add_u32 m0, s76, 1024\n\t"
      "s_add_u32 s68, s77, 32768\n\t"
      "s_add_u32 s76, s76, 8192\n\t"
      "s_add_u32 s77, s77, 0x40000\n\t"
      "buffer_load_dwordx4 v108, s[64:67], s68 offen lds\n\t"
      "s_mov_b64 s[88:89], 0\n\t"
      "s_waitcnt vmcnt(8)\n\t"
      "GLN%=:\n\t"
      "v_and_b32 v1, s84, v64\n\t"
      "v_lshlrev_b32 v0, 16, v64\n\t"
      "v_and_b32 v3, s84, v65\n\t"
      "v_lshlrev_b32 v2, 16, v65\n\t"
      "v_and_b32 v5, s84, v66\n\t"
      "v_lshlrev_b32 v4, 16, v66\n\t"
      "v_and_b32 v7, s84, v67\n\t"
      "v_lshlrev_b32 v6, 16, v67\n\t"
      "v_and_b32 v9, s84, v68\n\t"
      "v_lshlrev_b32 v8, 16, v68\n\t"
      "v_and_b32 v11, s84, v69\n\t"
      "v_lshlrev_b32 v10, 16, v69\n\t"
      "v_and_b32 v13, s84, v70\n\t"
      "v_lshlrev_b32 v12, 16, v70\n\t"
      "v_and_b32 v15, s84, v71\n\t"
      "v_lshlrev_b32 v14, 16, v71\n\t"
      "s_mov_b64 exec, s[88:89]\n\t"
      "s_add_u32 m0, s76, 0\n\t"
      "s_add_u32 s68, s77, 0\n\t"
      "buffer_load_dwordx4 v108, s[64:67], s68 offen lds\n\t"
      "s_mov_b64 exec, -1\n\t"
      "v_and_b32 v17, s84, v72\n\t"
      "v_lshlrev_b32 v16, 16, v72\n\t"
      "v_and_b32 v19, s84, v73\n\t"
      "v_lshlrev_b32 v18, 16, v73\n\t"
      "v_and_b32 v21, s84, v74\n\t"
      "v_lshlrev_b32 v20, 16, v74\n\t"
      "v_and_b32 v23, s84, v75\n\t"
      "v_lshlrev_b32 v22, 16, v75\n\t"
      "v_and_b32 v25, s84, v76\n\t"
      "v_lshlrev_b32 v24, 16, v76\n\t"
      "v_and_b32 v27, s84, v77\n\t"
      "v_lshlrev_b32 v26, 16, v77\n\t"
      "v_and_b32 v29, s84, v78\n\t"
      "v_lshlrev_b32 v28, 16, v78\n\t"
      "v_and_b32 v31, s84, v79\n\t"
      "v_lshlrev_b32 v30, 16, v79\n\t"
      "s_mov_b64 exec, s[88:89]\n\t"
      "s_add_u32 m0, s76, 1024\n\t"
      "s_add_u32 s68, s77, 32768\n\t"
      "s_add_u32 s76, s76, 8192\n\t"
      "s_add_u32 s77, s77, 0x40000\n\t"
      "buffer_load_dwordx4 v108, s[64:67], s68 offen lds\n\t"
      "s_mov_b64 exec, -1\n\t"
      "v_pk_add_f32 v[32:33], v[0:1], v[2:3]\n\t"
      "v_pk_add_f32 v[34:35], v[4:5], v[6:7]\n\t"
      "v_pk_add_f32 v[36:37], v[8:9], v[10:11]\n\t"
      "v_pk_add_f32 v[38:39], v[12:13], v[14:15]\n\t"
      "v_pk_add_f32 v[40:41], v[16:17], v[18:19]\n\t"
      "v_pk_add_f32 v[42:43], v[20:21], v[22:23]\n\t"
      "v_pk_add_f32 v[44:45], v[24:25], v[26:27]\n\t"
      "v_pk_add_f32 v[46:47], v[28:29], v[30:31]\n\t"
      "v_pk_add_f32 v[32:33], v[32:33], v[40:41]\n\t"
      "v_pk_add_f32 v[34:35], v[34:35], v[42:43]\n\t"
      "v_pk_add_f32 v[36:37], v[36:37], v[44:45]\n\t"
      "v_pk_add_f32 v[38:39], v[38:39], v[46:47]\n\t"
      "v_pk_add_f32 v[32:33], v[32:33], v[36:37]\n\t"
      "v_pk_add_f32 v[34:35], v[34:35], v[38:39]\n\t"
      "v_pk_add_f32 v[32:33], v[32:33], v[34:35]\n\t"
      "v_add_f32 v32, v32, v33\n\t"
      "s_mov_b64 exec, s[88:89]\n\t"
      "s_add_u32 m0, s76, 0\n\t"
      "s_add_u32 s68, s77, 0\n\t"
      "buffer_load_dwordx4 v108, s[64:67], s68 offen lds\n\t"
      "s_mov_b64 exec, -1\n\t"
      "v_add_f32_dpp v32, v32, v32 row_ror:4 row_mask:0xf bank_mask:0xf\n\t"
      "s_nop 1\n\t"
      "v_add_f32_dpp v32, v32, v32 row_ror:8 row_mask:0xf bank_mask:0xf\n\t"
      "v_mov_b32 v33, v32\n\t"
      "s_nop 1\n\t"
      "v_permlane16_swap_b32 v32, v33\n\t"
      "v_add_f32 v32, v32, v33\n\t"
      "v_mov_b32 v33, v32\n\t"
      "s_nop 1\n\t"
      "v_permlane32_swap_b32 v32, v33\n\t"
      "v_add_f32 v32, v32, v33\n\t"
      "v_mul_f32 v80, 0x3b000000, v32\n\t"
      "v_pk_add_f32 v[32:33], v[0:1], v[80:81] op_sel_hi:[1,0] neg_lo:[0,1] neg_hi:[0,1]\n\t"
      "v_pk_add_f32 v[34:35], v[2:3], v[80:81] op_sel_hi:[1,0] neg_lo:[0,1] neg_hi:[0,1]\n\t"
      "v_pk_add_f32 v[36:37], v[4:5], v[80:81] op_sel_hi:[1,0] neg_lo:[0,1] neg_hi:[0,1]\n\t"
      "v_pk_add_f32 v[38:39], v[6:7], v[80:81] op_sel_hi:[1,0] neg_lo:[0,1] neg_hi:[0,1]\n\t"
      "v_pk_add_f32 v[40:41], v[8:9], v[80:81] op_sel_hi:[1,0] neg_lo:[0,1] neg_hi:[0,1]\n\t"
      "v_pk_add_f32 v[42:43], v[10:11], v[80:81] op_sel_hi:[1,0] neg_lo:[0,1] neg_hi:[0,1]\n\t"
      "v_pk_add_f32 v[44:45], v[12:13], v[80:81] op_sel_hi:[1,0] neg_lo:[0,1] neg_hi:[0,1]\n\t"
      "s_mov_b64 exec, s[88:89]\n\t"
      "s_add_u32 m0, s76, 1024\n\t"
      "s_add_u32 s68, s77, 32768\n\t"
      "s_add_u32 s76, s76, 8192\n\t"
      "s_add_u32 s77, s77, 0x40000\n\t"
      "buffer_load_dwordx4 v108, s[64:67], s68 offen lds\n\t"
      "s_mov_b64 exec, -1\n\t"
      "v_pk_add_f32 v[46:47], v[14:15], v[80:81] op_sel_hi:[1,0] neg_lo:[0,1] neg_hi:[0,1]\n\t"
      "v_pk_add_f32 v[48:49], v[16:17], v[80:81] op_sel_hi:[1,0] neg_lo:[0,1] neg_hi:[0,1]\n\t"
      "v_pk_add_f32 v[50:51], v[18:19], v[80:81] op_sel_hi:[1,0] neg_lo:[0,1] neg_hi:[0,1]\n\t"
      "v_pk_add_f32 v[52:53], v[20:21], v[80:81] op_sel_hi:[1,0] neg_lo:[0,1] neg_hi:[0,1]\n\t"
      "v_pk_add_f32 v[54:55], v[22:23], v[80:81] op_sel_hi:[1,0] neg_lo:[0,1] neg_hi:[0,1]\n\t"
      "v_pk_add_f32 v[56:57], v[24:25], v[80:81] op_sel_hi:[1,0] neg_lo:[0,1] neg_hi:[0,1]\n\t"
      "v_pk_add_f32 v[58:59], v[26:27], v[80:81] op_sel_hi:[1,0] neg_lo:[0,1] neg_hi:[0,1]\n\t"
      "v_pk_add_f32 v[60:61], v[28:29], v[80:81] op_sel_hi:[1,0] neg_lo:[0,1] neg_hi:[0,1]\n\t"
      "v_pk_add_f32 v[62:63], v[30:31], v[80:81] op_sel_hi:[1,0] neg_lo:[0,1] neg_hi:[0,1]\n\t"
      "v_pk_mul_f32 v[64:65], v[32:33], v[32:33]\n\t"
      "v_pk_mul_f32 v[66:67], v[34:35], v[34:35]\n\t"
      "v_pk_mul_f32 v[68:69], v[36:37], v[36:37]\n\t"
      "v_pk_mul_f32 v[70:71], v[38:39], v[38:39]\n\t"
      "v_pk_fma_f32 v[64:65], v[40:41], v[40:41], v[64:65]\n\t"
      "v_pk_fma_f32 v[66:67], v[42:43], v[42:43], v[66:67]\n\t"
      "v_pk_fma_f32 v[68:69], v[44:45], v[44:45], v[68:69]\n\t"
      "s_mov_b64 exec, s[88:89]\n\t"
      "s_add_u32 m0, s76, 0\n\t"
      "s_add_u32 s68, s77, 0\n\t"
      "buffer_load_dwordx4 v108, s[64:67], s68 offen lds\n\t"
      "s_mov_b64 exec, -1\n\t"
      "v_pk_fma_f32 v[70:71], v[46:47], v[46:47], v[70:71]\n\t"
      "v_pk_fma_f32 v[64:65], v[48:49], v[48:49], v[64:65]\n\t"
      "v_pk_fma_f32 v[66:67], v[50:51], v[50:51], v[66:67]\n\t"
      "v_pk_fma_f32 v[68:69], v[52:53], v[52:53], v[68:69]\n\t"
      "v_pk_fma_f32 v[70:71], v[54:55], v[54:55], v[70:71]\n\t"
      "v_pk_fma_f32 v[64:65], v[56:57], v[56:57], v[64:65]\n\t"
      "v_pk_fma_f32 v[66:67], v[58:59], v[58:59], v[66:67]\n\t"
      "v_pk_fma_f32 v[68:69], v[60:61], v[60:61], v[68:69]\n\t"
      "v_pk_fma_f32 v[70:71], v[62:63], v[62:63], v[70:71]\n\t"
      "v_pk_add_f32 v[64:65], v[64:65], v[66:67]\n\t"
      "v_pk_add_f32 v[68:69], v[68:69], v[70:71]\n\t"
      "v_pk_add_f32 v[64:65], v[64:65], v[68:69]\n\t"
      "v_add_f32 v64, v64, v65\n\t"
      "s_nop 1\n\t"
      "v_add_f32_dpp v64, v64, v64 row_ror:4 row_mask:0xf bank_mask:0xf\n\t"
      "s_nop 1\n\t"
      "v_add_f32_dpp v64, v64, v64 row_ror:8 row_mask:0xf bank_mask:0xf\n\t"
      "v_mov_b32 v65, v64\n\t"
      "s_mov_b64 exec, s[88:89]\n\t"
      "s_add_u32 m0, s76, 1024\n\t"
      "s_add_u32 s68, s77, 32768\n\t"
      "s_add_u32 s76, s76, 8192\n\t"
      "s_add_u32 s77, s77, 0x40000\n\t"
      "buffer_load_dwordx4 v108, s[64:67], s68 offen lds\n\t"
      "s_mov_b64 exec, -1\n\t"
      "v_permlane16_swap_b32 v64, v65\n\t"
      "v_add_f32 v64, v64, v65\n\t"
      "v_mov_b32 v65, v64\n\t"
      "s_nop 1\n\t"
      "v_permlane32_swap_b32 v64, v65\n\t"
      "v_add_f32 v64, v64, v65\n\t"
      "v_mul_f32 v81, 0x3b000000, v64\n\t"
      "v_cvt_pk_bf16_f32 v80, v80, v81\n\t"
      "v_and_b32 v81, s84, v80\n\t"
      "v_lshlrev_b32 v80, 16, v80\n\t"
      "v_add_f32 v81, 0x3727c5ac, v81\n\t"
      "v_pk_add_f32 v[32:33], v[0:1], v[80:81] op_sel_hi:[1,0] neg_lo:[0,1] neg_hi:[0,1]\n\t"
      "v_pk_add_f32 v[34:35], v[2:3], v[80:81] op_sel_hi:[1,0] neg_lo:[0,1] neg_hi:[0,1]\n\t"
      "v_pk_add_f32 v[36:37], v[4:5], v[80:81] op_sel_hi:[1,0] neg_lo:[0,1] neg_hi:[0,1]\n\t"
      "v_pk_add_f32 v[38:39], v[6:7], v[80:81] op_sel_hi:[1,0] neg_lo:[0,1] neg_hi:[0,1]\n\t"
      "v_pk_add_f32 v[40:41], v[8:9], v[80:81] op_sel_hi:[1,0] neg_lo:[0,1] neg_hi:[0,1]\n\t"
      "v_pk_add_f32 v[42:43], v[10:11], v[80:81] op_sel_hi:[1,0] neg_lo:[0,1] neg_hi:[0,1]\n\t"
      "s_mov_b64 exec, s[88:89]\n\t"
      "s_add_u32 m0, s76, 0\n\t"
      "s_add_u32 s68, s77, 0\n\t"
      "buffer_load_dwordx4 v108, s[64:67], s68 offen lds\n\t"
      "s_mov_b64 exec, -1\n\t"
      "v_pk_add_f32 v[44:45], v[12:13], v[80:81] op_sel_hi:[1,0] neg_lo:[0,1] neg_hi:[0,1]\n\t"
      "v_pk_add_f32 v[46:47], v[14:15], v[80:81] op_sel_hi:[1,0] neg_lo:[0,1] neg_hi:[0,1]\n\t"
      "v_pk_add_f32 v[48:49], v[16:17], v[80:81] op_sel_hi:[1,0] neg_lo:[0,1] neg_hi:[0,1]\n\t"
      "v_pk_add_f32 v[50:51], v[18:19], v[80:81] op_sel_hi:[1,0] neg_lo:[0,1] neg_hi:[0,1]\n\t"
      "v_pk_add_f32 v[52:53], v[20:21], v[80:81] op_sel_hi:[1,0] neg_lo:[0,1] neg_hi:[0,1]\n\t"
      "v_pk_add_f32 v[54:55], v[22:23], v[80:81] op_sel_hi:[1,0] neg_lo:[0,1] neg_hi:[0,1]\n\t"
      "v_pk_add_f32 v[56:57], v[24:25], v[80:81] op_sel_hi:[1,0] neg_lo:[0,1] neg_hi:[0,1]\n\t"
      "v_pk_add_f32 v[58:59], v[26:27], v[80:81] op_sel_hi:[1,0] neg_lo:[0,1] neg_hi:[0,1]\n\t"
      "v_pk_add_f32 v[60:61], v[28:29], v[80:81] op_sel_hi:[1,0] neg_lo:[0,1] neg_hi:[0,1]\n\t"
      "v_pk_add_f32 v[62:63], v[30:31], v[80:81] op_sel_hi:[1,0] neg_lo:[0,1] neg_hi:[0,1]\n\t"
      "v_cvt_pk_bf16_f32 v81, v81, v81\n\t"
      "v_and_b32 v81, s84, v81\n\t"
      "v_sqrt_f32 v81, v81\n\t"
      "v_cvt_pk_bf16_f32 v32, v32, v33\n\t"
      "v_cvt_pk_bf16_f32 v34, v34, v35\n\t"
      "v_cvt_pk_bf16_f32 v36, v36, v37\n\t"
      "s_mov_b64 exec, s[88:89]\n\t"
      "s_add_u32 m0, s76, 1024\n\t"
      "s_add_u32 s68, s77, 32768\n\t"
      "s_add_u32 s76, s76, 8192\n\t"
      "s_add_u32 s77, s77, 0x40000\n\t"
      "buffer_load_dwordx4 v108, s[64:67], s68 offen lds\n\t"
      "s_mov_b64 exec, -1\n\t"
      "v_cvt_pk_bf16_f32 v38, v38, v39\n\t"
      "v_cvt_pk_bf16_f32 v40, v40, v41\n\t"
      "v_cvt_pk_bf16_f32 v42, v42, v43\n\t"
      "v_cvt_pk_bf16_f32 v44, v44, v45\n\t"
      "v_cvt_pk_bf16_f32 v46, v46, v47\n\t"
      "v_cvt_pk_bf16_f32 v48, v48, v49\n\t"
      "v_cvt_pk_bf16_f32 v50, v50, v51\n\t"
      "v_cvt_pk_bf16_f32 v52, v52, v53\n\t"
      "v_cvt_pk_bf16_f32 v54, v54, v55\n\t"
      "v_cvt_pk_bf16_f32 v56, v56, v57\n\t"
      "v_cvt_pk_bf16_f32 v58, v58, v59\n\t"
      "v_cvt_pk_bf16_f32 v60, v60, v61\n\t"
      "v_cvt_pk_bf16_f32 v62, v62, v63\n\t"
      "v_cvt_pk_bf16_f32 v81, v81, v81\n\t"
      "v_and_b32 v81, s84, v81\n\t"
      "v_rcp_f32 v80, v81\n\t"
      "s_mov_b64 exec, s[88:89]\n\t"
      "s_add_u32 m0, s76, 0\n\t"
      "s_add_u32 s68, s77, 0\n\t"
      "buffer_load_dwordx4 v108, s[64:67], s68 offen lds\n\t"
      "s_mov_b64 exec, -1\n\t"
      "v_and_b32 v33, s84, v32\n\t"
      "v_lshlrev_b32 v32, 16, v32\n\t"
      "v_and_b32 v35, s84, v34\n\t"
      "v_lshlrev_b32 v34, 16, v34\n\t"
      "v_and_b32 v37, s84, v36\n\t"
      "v_lshlrev_b32 v36, 16, v36\n\t"
      "v_and_b32 v39, s84, v38\n\t"
      "v_lshlrev_b32 v38, 16, v38\n\t"
      "v_pk_mul_f32 v[32:33], v[32:33], v[80:81] op_sel_hi:[1,0]\n\t"
      "v_pk_mul_f32 v[34:35], v[34:35], v[80:81] op_sel_hi:[1,0]\n\t"
      "v_pk_mul_f32 v[36:37], v[36:37], v[80:81] op_sel_hi:[1,0]\n\t"
      "v_pk_mul_f32 v[38:39], v[38:39], v[80:81] op_sel_hi:[1,0]\n\t"
      "v_cvt_pk_bf16_f32 v64, v32, v33\n\t"
      "v_cvt_pk_bf16_f32 v65, v34, v35\n\t"
      "v_cvt_pk_bf16_f32 v66, v36, v37\n\t"
      "v_cvt_pk_bf16_f32 v67, v38, v39\n\t"
      "s_mov_b64 exec, s[88:89]\n\t"
      "s_add_u32 m0, s76, 1024\n\t"
      "s_add_u32 s68, s77, 32768\n\t"
      "s_add_u32 s76, s76, 8192\n\t"
      "s_add_u32 s77, s77, 0x40000\n\t"
      "buffer_load_dwordx4 v108, s[64:67], s68 offen lds\n\t"
      "s_mov_b64 exec, -1\n\t"
      "v_and_b32 v41, s84, v40\n\t"
      "v_lshlrev_b32 v40, 16, v40\n\t"
      "v_and_b32 v43, s84, v42\n\t"
      "v_lshlrev_b32 v42, 16, v42\n\t"
      "v_and_b32 v45, s84, v44\n\t"
      "v_lshlrev_b32 v44, 16, v44\n\t"
      "v_and_b32 v47, s84, v46\n\t"
      "v_lshlrev_b32 v46, 16, v46\n\t"
      "v_pk_mul_f32 v[40:41], v[40:41], v[80:81] op_sel_hi:[1,0]\n\t"
      "v_pk_mul_f32 v[42:43], v[42:43], v[80:81] op_sel_hi:[1,0]\n\t"
      "v_pk_mul_f32 v[44:45], v[44:45], v[80:81] op_sel_hi:[1,0]\n\t"
      "v_pk_mul_f32 v[46:47], v[46:47], v[80:81] op_sel_hi:[1,0]\n\t"
      "v_cvt_pk_bf16_f32 v68, v40, v41\n\t"
      "v_cvt_pk_bf16_f32 v69, v42, v43\n\t"
      "v_cvt_pk_bf16_f32 v70, v44, v45\n\t"
      "v_cvt_pk_bf16_f32 v71, v46, v47\n\t"
      "s_mov_b64 exec, s[88:89]\n\t"
      "s_add_u32 m0, s76, 0\n\t"
      "s_add_u32 s68, s77, 0\n\t"
      "buffer_load_dwordx4 v108, s[64:67], s68 offen lds\n\t"
      "s_mov_b64 exec, -1\n\t"
      "v_and_b32 v49, s84, v48\n\t"
      "v_lshlrev_b32 v48, 16, v48\n\t"
      "v_and_b32 v51, s84, v50\n\t"
      "v_lshlrev_b32 v50, 16, v50\n\t"
      "v_and_b32 v53, s84, v52\n\t"
      "v_lshlrev_b32 v52, 16, v52\n\t"
      "v_and_b32 v55, s84, v54\n\t"
      "v_lshlrev_b32 v54, 16, v54\n\t"
      "v_pk_mul_f32 v[48:49], v[48:49], v[80:81] op_sel_hi:[1,0]\n\t"
      "v_pk_mul_f32 v[50:51], v[50:51], v[80:81] op_sel_hi:[1,0]\n\t"
      "v_pk_mul_f32 v[52:53], v[52:53], v[80:81] op_sel_hi:[1,0]\n\t"
      "v_pk_mul_f32 v[54:55], v[54:55], v[80:81] op_sel_hi:[1,0]\n\t"
      "v_cvt_pk_bf16_f32 v72, v48, v49\n\t"
      "v_cvt_pk_bf16_f32 v73, v50, v51\n\t"
      "v_cvt_pk_bf16_f32 v74, v52, v53\n\t"
      "v_cvt_pk_bf16_f32 v75, v54, v55\n\t"
      "s_mov_b64 exec, s[88:89]\n\t"
      "s_add_u32 m0, s76, 1024\n\t"
      "s_add_u32 s68, s77, 32768\n\t"
      "s_add_u32 s76, s76, 8192\n\t"
      "s_add_u32 s77, s77, 0x40000\n\t"
      "buffer_load_dwordx4 v108, s[64:67], s68 offen lds\n\t"
      "s_mov_b64 exec, -1\n\t"
      "v_and_b32 v57, s84, v56\n\t"
      "v_lshlrev_b32 v56, 16, v56\n\t"
      "v_and_b32 v59, s84, v58\n\t"
      "v_lshlrev_b32 v58, 16, v58\n\t"
      "v_and_b32 v61, s84, v60\n\t"
      "v_lshlrev_b32 v60, 16, v60\n\t"
      "v_and_b32 v63, s84, v62\n\t"
      "v_lshlrev_b32 v62, 16, v62\n\t"
      "v_pk_mul_f32 v[56:57], v[56:57], v[80:81] op_sel_hi:[1,0]\n\t"
      "v_pk_mul_f32 v[58:59], v[58:59], v[80:81] op_sel_hi:[1,0]\n\t"
      "v_pk_mul_f32 v[60:61], v[60:61], v[80:81] op_sel_hi:[1,0]\n\t"
      "v_pk_mul_f32 v[62:63], v[62:63], v[80:81] op_sel_hi:[1,0]\n\t"
      "v_cvt_pk_bf16_f32 v76, v56, v57\n\t"
      "v_cvt_pk_bf16_f32 v77, v58, v59\n\t"
      "v_cvt_pk_bf16_f32 v78, v60, v61\n\t"
      "v_cvt_pk_bf16_f32 v79, v62, v63\n\t"
      "s_cmp_lg_u32 s69, 0\n\t"
      "s_cbranch_scc1 GLN1%=\n\t"
      "buffer_store_dwordx4 v[64:67], v106, s[60:63], s74 offen sc1\n\t"
      "s_add_u32 s74, s74, 4096\n\t"
      "buffer_store_dwordx4 v[68:71], v106, s[60:63], s74 offen sc1\n\t"
      "s_add_u32 s74, s74, 4096\n\t"
      "buffer_store_dwordx4 v[72:75], v106, s[60:63], s74 offen sc1\n\t"
      "s_add_u32 s74, s74, 4096\n\t"
      "buffer_store_dwordx4 v[76:79], v106, s[60:63], s74 offen sc1\n\t"
      "s_add_u32 s74, s74, 4096\n\t"
      "s_sub_u32 s76, s76, 49152\n\t"
      "s_sub_u32 s77, s77, 1572864\n\t"
      "s_mov_b64 s[88:89], -1\n\t"
      "v_readfirstlane_b32 %3, v107\n\t"
      "v_mov_b64 v[64:65], v[128:129]\n\t"
      "v_mov_b64 v[66:67], v[130:131]\n\t"
      "v_mov_b64 v[68:69], v[132:133]\n\t"
      "v_mov_b64 v[70:71], v[134:135]\n\t"
      "v_mov_b64 v[72:73], v[136:137]\n\t"
      "v_mov_b64 v[74:75], v[138:139]\n\t"
      "v_mov_b64 v[76:77], v[140:141]\n\t"
      "v_mov_b64 v[78:79], v[142:143]\n\t"
      "s_mul_i32 %3, %3, 0x9e3779b1\n\t"
      "s_add_u32 %3, %3, 0x7f4a7c15\n\t"
      "s_mov_b32 s69, 1\n\t"
      "s_branch GLN%=\n\t"
      "GLN1%=:\n\t"
      "ds_write_b128 v110, v[64:67]\n\t"
      "ds_write_b128 v110, v[68:71] offset:4096\n\t"
      "ds_write_b128 v110, v[72:75] offset:8192\n\t"
      "ds_write_b128 v110, v[76:79] offset:12288\n\t"
      "s_cmp_eq_u32 s69, 2\n\t"
      "s_cbranch_scc1 GLN2%=\n\t"
      "v_mov_b32 v107, %3\n\t"
      "v_add_u32 v110, 0x4000, v110\n\t"
      "s_lshl_b32 s79, %0, 7\n\t"
      "s_add_u32 s79, s79, 37120\n\t"
      "s_waitcnt vmcnt(12) lgkmcnt(0)\n\t"
      "s_barrier\n\t"
      "s_mov_b64 exec, s[90:91]\n\t"
      "buffer_store_dword v107, off, s[60:63], s79 sc1\n\t"
      "s_mov_b64 exec, -1\n\t"
      "v_mov_b64 v[64:65], v[144:145]\n\t"
      "v_mov_b64 v[66:67], v[146:147]\n\t"
      "v_mov_b64 v[68:69], v[148:149]\n\t"
      "v_mov_b64 v[70:71], v[150:151]\n\t"
      "v_mov_b64 v[72:73], v[152:153]\n\t"
      "v_mov_b64 v[74:75], v[154:155]\n\t"
      "v_mov_b64 v[76:77], v[156:157]\n\t"
      "v_mov_b64 v[78:79], v[158:159]\n\t"
      "s_mov_b32 s69, 2\n\t"
      "s_branch GLN%=\n\t"
      "GLN2%=:\n\t"
      "s_waitcnt lgkmcnt(10)\n\t"
      "ds_read_b64_tr_b16 a[0:1], v109\n\t"
      "ds_read_b64_tr_b16 a[2:3], v109 offset:1024\n\t"
      "ds_read_b64_tr_b16 a[4:5], v109 offset:8\n\t"
      "ds_read_b64_tr_b16 a[6:7], v109 offset:1032\n\t"
      "s_waitcnt lgkmcnt(10)\n\t"
      "ds_read_b64_tr_b16 a[8:9], v109 offset:8192\n\t"
      "ds_read_b64_tr_b16 a[10:11], v109 offset:9216\n\t"
      "ds_read_b64_tr_b16 a[12:13], v109 offset:8200\n\t"
      "ds_read_b64_tr_b16 a[14:15], v109 offset:9224\n\t"
      "s_waitcnt lgkmcnt(10)\n\t"
      "ds_read_b64_tr_b16 a[16:17], v109 offset:16384\n\t"
      "ds_read_b64_tr_b16 a[18:19], v109 offset:17408\n\t"
      "ds_read_b64_tr_b16 a[20:21], v109 offset:16392\n\t"
      "ds_read_b64_tr_b16 a[22:23], v109 offset:17416\n\t"
      "s_waitcnt lgkmcnt(10)\n\t"
      "ds_read_b64_tr_b16 a[24:25], v109 offset:24576\n\t"
      "ds_read_b64_tr_b16 a[26:27], v109 offset:25600\n\t"
      "ds_read_b64_tr_b16 a[28:29], v109 offset:24584\n\t"
      "ds_read_b64_tr_b16 a[30:31], v109 offset:25608\n\t"
      "v_lshlrev_b32 v106, 8, v96\n\t"
      "s_mov_b32 s79, 37120\n\t"
      "s_mov_b32 s81, 0x40000\n\t"
      "v_add_u32 v98, 131072, v97\n\t"
      "v_readlane_b32 s73, v102, 0\n\t"
      "v_readlane_b32 s75, v103, 0\n\t"
      "s_mov_b64 s[64:65], s[40:41]\n\t"
      "s_lshl_b32 s74, %1, 10\n\t"
      "v_readlane_b32 s72, v101, 1\n\t"
      "s_add_u32 s76, s85, 0\n\t"
      "s_waitcnt vmcnt(12)\n\t"
      "s_barrier\n\t"
      "s_waitcnt lgkmcnt(10)\n\t"
      "ds_read_b64_tr_b16 a[32:33], v109 offset:32768\n\t"
      "ds_read_b64_tr_b16 a[34:35], v109 offset:33792\n\t"
      "ds_read_b64_tr_b16 a[36:37], v109 offset:32776\n\t"
      "ds_read_b64_tr_b16 a[38:39], v109 offset:33800\n\t"
      "s_waitcnt lgkmcnt(10)\n\t"
      "ds_read_b64_tr_b16 a[40:41], v109 offset:40960\n\t"
      "ds_read_b64_tr_b16 a[42:43], v109 offset:41984\n\t"
      "ds_read_b64_tr_b16 a[44:45], v109 offset:40968\n\t"
      "ds_read_b64_tr_b16 a[46:47], v109 offset:41992\n\t"
      "s_waitcnt lgkmcnt(10)\n\t"
      "ds_read_b64_tr_b16 a[48:49], v109 offset:49152\n\t"
      "ds_read_b64_tr_b16 a[50:51], v109 offset:50176\n\t"
      "ds_read_b64_tr_b16 a[52:53], v109 offset:49160\n\t"
      "ds_read_b64_tr_b16 a[54:55], v109 offset:50184\n\t"
      "s_waitcnt lgkmcnt(10)\n\t"
      "ds_read_b64_tr_b16 a[56:57], v109 offset:57344\n\t"
      "ds_read_b64_tr_b16 a[58:59], v109 offset:58368\n\t"
      "ds_read_b64_tr_b16 a[60:61], v109 offset:57352\n\t"
      "ds_read_b64_tr_b16 a[62:63], v109 offset:58376\n\t"
      "v_add_u32 v109, 0x10000, v109\n\t"
      "s_waitcnt lgkmcnt(10)\n\t"
      "ds_read_b64_tr_b16 a[64:65], v109\n\t"
      "ds_read_b64_tr_b16 a[66:67], v109 offset:1024\n\t"
      "ds_read_b64_tr_b16 a[68:69], v109 offset:8\n\t"
      "ds_read_b64_tr_b16 a[70:71], v109 offset:1032\n\t"
      "s_waitcnt lgkmcnt(10)\n\t"
      "ds_read_b64_tr_b16 a[72:73], v109 offset:8192\n\t"
      "ds_read_b64_tr_b16 a[74:75], v109 offset:9216\n\t"
      "ds_read_b64_tr_b16 a[76:77], v109 offset:8200\n\t"
      "ds_read_b64_tr_b16 a[78:79], v109 offset:9224\n\t"
      "s_waitcnt vmcnt(0)\n\t"
      "s_barrier\n\t"
      "buffer_load_dword v128, v106, s[60:63], s79 offen sc1\n\t"
      "buffer_load_dword v129, v106, s[60:63], s79 offen offset:128 sc1\n\t"
      "s_waitcnt lgkmcnt(10)\n\t"
      "ds_read_b64_tr_b16 a[80:81], v109 offset:16384\n\t"
      "ds_read_b64_tr_b16 a[82:83], v109 offset:17408\n\t"
      "ds_read_b64_tr_b16 a[84:85], v109 offset:16392\n\t"
      "ds_read_b64_tr_b16 a[86:87], v109 offset:17416\n\t"
      "s_waitcnt lgkmcnt(10)\n\t"
      "ds_read_b64_tr_b16 a[88:89], v109 offset:24576\n\t"
      "ds_read_b64_tr_b16 a[90:91], v109 offset:25600\n\t"
      "ds_read_b64_tr_b16 a[92:93], v109 offset:24584\n\t"
      "ds_read_b64_tr_b16 a[94:95], v109 offset:25608\n\t"
      "s_waitcnt lgkmcnt(10)\n\t"
      "ds_read_b64_tr_b16 a[96:97], v109 offset:32768\n\t"
      "ds_read_b64_tr_b16 a[98:99], v109 offset:33792\n\t"
      "ds_read_b64_tr_b16 a[100:101], v109 offset:32776\n\t"
      "ds_read_b64_tr_b16 a[102:103], v109 offset:33800\n\t"
      "s_waitcnt lgkmcnt(10)\n\t"
      "ds_read_b64_tr_b16 a[104:105], v109 offset:40960\n\t"
      "ds_read_b64_tr_b16 a[106:107], v109 offset:41984\n\t"
      "ds_read_b64_tr_b16 a[108:109], v109 offset:40968\n\t"
      "ds_read_b64_tr_b16 a[110:111], v109 offset:41992\n\t"
      "s_waitcnt lgkmcnt(10)\n\t"
      "ds_read_b64_tr_b16 a[112:113], v109 offset:49152\n\t"
      "ds_read_b64_tr_b16 a[114:115], v109 offset:50176\n\t"
      "ds_read_b64_tr_b16 a[116:117], v109 offset:49160\n\t"
      "ds_read_b64_tr_b16 a[118:119], v109 offset:50184\n\t"
      "s_waitcnt lgkmcnt(10)\n\t"
      "ds_read_b64_tr_b16 a[120:121], v109 offset:57344\n\t"
      "ds_read_b64_tr_b16 a[122:123], v109 offset:58368\n\t"
      "ds_read_b64_tr_b16 a[124:125], v109 offset:57352\n\t"
      "ds_read_b64_tr_b16 a[126:127], v109 offset:58376\n\t"
      "s_mov_b64 s[88:89], 0\n\t"
      "s_mov_b64 s[38:39], 0\n\t"
      "s_waitcnt lgkmcnt(4)\n\t"
      "s_mov_b32 s71, 0\n\t"
      "GKV%=:\n\t"
      "ds_read_b128 v[0:3], v98\n\t"
      "ds_read_b128 v[4:7], v98 offset:1024\n\t"
      "ds_read_b128 v[8:11], v98 offset:2048\n\t"
      "ds_read_b128 v[12:15], v98 offset:3072\n\t"
      "ds_read_b128 v[16:19], v98 offset:4096\n\t"
      "ds_read_b128 v[20:23], v98 offset:5120\n\t"
      "ds_read_b128 v[24:27], v98 offset:6144\n\t"
      "ds_read_b128 v[28:31], v98 offset:7168\n\t"
      "ds_read_b128 v[32:35], v98 offset:8192\n\t"
      "ds_read_b128 v[36:39], v98 offset:9216\n\t"
      "s_waitcnt lgkmcnt(9)\n\t"
      "v_mfma_f32_16x16x32_bf16 v[64:67], a[0:3], v[0:3], 0\n\t"
      "ds_read_b128 v[40:43], v98 offset:10240\n\t"
      "v_mfma_f32_16x16x32_bf16 v[68:71], a[4:7], v[0:3], 0\n\t"
      "s_waitcnt lgkmcnt(9)\n\t"
      "v_mfma_f32_16x16x32_bf16 v[64:67], a[8:11], v[4:7], v[64:67]\n\t"
      "ds_read_b128 v[44:47], v98 offset:11264\n\t"
      "v_mfma_f32_16x16x32_bf16 v[68:71], a[12:15], v[4:7], v[68:71]\n\t"
      "s_waitcnt lgkmcnt(9)\n\t"
      "v_mfma_f32_16x16x32_bf16 v[64:67], a[16:19], v[8:11], v[64:67]\n\t"
      "ds_read_b128 v[48:51], v98 offset:12288\n\t"
      "v_mfma_f32_16x16x32_bf16 v[68:71], a[20:23], v[8:11], v[68:71]\n\t"
      "s_waitcnt lgkmcnt(9)\n\t"
      "v_mfma_f32_16x16x32_bf16 v[64:67], a[24:27], v[12:15], v[64:67]\n\t"
      "ds_read_b128 v[52:55], v98 offset:13312\n\t"
      "v_mfma_f32_16x16x32_bf16 v[68:71], a[28:31], v[12:15], v[68:71]\n\t"
      "s_waitcnt lgkmcnt(9)\n\t"
      "v_mfma_f32_16x16x32_bf16 v[64:67], a[32:35], v[16:19], v[64:67]\n\t"
      "ds_read_b128 v[56:59], v98 offset:14336\n\t"
      "v_mfma_f32_16x16x32_bf16 v[68:71], a[36:39], v[16:19], v[68:71]\n\t"
      "s_waitcnt lgkmcnt(9)\n\t"
      "v_mfma_f32_16x16x32_bf16 v[64:67], a[40:43], v[20:23], v[64:67]\n\t"
      "ds_read_b128 v[60:63], v98 offset:15360\n\t"
      "v_mfma_f32_16x16x32_bf16 v[68:71], a[44:47], v[20:23], v[68:71]\n\t"
      "s_waitcnt lgkmcnt(9)\n\t"
      "v_mfma_f32_16x16x32_bf16 v[64:67], a[48:51], v[24:27], v[64:67]\n\t"
      "ds_read_b128 v[0:3], v98 offset:16384\n\t"
      "v_mfma_f32_16x16x32_bf16 v[68:71], a[52:55], v[24:27], v[68:71]\n\t"
      "s_waitcnt lgkmcnt(9)\n\t"
      "v_mfma_f32_16x16x32_bf16 v[64:67], a[56:59], v[28:31], v[64:67]\n\t"
      "ds_read_b128 v[4:7], v98 offset:17408\n\t"
      "v_mfma_f32_16x16x32_bf16 v[68:71], a[60:63], v[28:31], v[68:71]\n\t"
      "s_waitcnt lgkmcnt(9)\n\t"
      "v_mfma_f32_16x16x32_bf16 v[64:67], a[64:67], v[32:35], v[64:67]\n\t"
      "ds_read_b128 v[8:11], v98 offset:18432\n\t"
      "v_mfma_f32_16x16x32_bf16 v[68:71], a[68:71], v[32:35], v[68:71]\n\t"
      "s_waitcnt lgkmcnt(9)\n\t"
      "v_mfma_f32_16x16x32_bf16 v[64:67], a[72:75], v[36:39], v[64:67]\n\t"
      "ds_read_b128 v[12:15], v98 offset:19456\n\t"
      "v_mfma_f32_16x16x32_bf16 v[68:71], a[76:79], v[36:39], v[68:71]\n\t"
      "s_waitcnt lgkmcnt(9)\n\t"
      "v_mfma_f32_16x16x32_bf16 v[64:67], a[80:83], v[40:43], v[64:67]\n\t"
      "ds_read_b128 v[16:19], v98 offset:20480\n\t"
      "v_mfma_f32_16x16x32_bf16 v[68:71], a[84:87], v[40:43], v[68:71]\n\t"
      "s_waitcnt lgkmcnt(9)\n\t"
      "v_mfma_f32_16x16x32_bf16 v[64:67], a[88:91], v[44:47], v[64:67]\n\t"
      "ds_read_b128 v[20:23], v98 offset:21504\n\t"
      "v_mfma_f32_16x16x32_bf16 v[68:71], a[92:95], v[44:47], v[68:71]\n\t"
      "s_waitcnt lgkmcnt(9)\n\t"
      "v_mfma_f32_16x16x32_bf16 v[64:67], a[96:99], v[48:51], v[64:67]\n\t"
      "ds_read_b128 v[24:27], v98 offset:22528\n\t"
      "v_mfma_f32_16x16x32_bf16 v[68:71], a[100:103], v[48:51], v[68:71]\n\t"
      "s_cmp_lg_u32 s71, 0\n\t"
      "s_barrier\n\t"
      "s_cbranch_scc1 GKV0%=\n\t"
      "s_waitcnt vmcnt(0)\n\t"
      "v_bitop3_b32 v128, v128, v129, %3 bitop3:0x7e\n\t"
      "v_cmp_ne_u32 vcc, 0, v128\n\t"
      "s_nop 0\n\t"
      "s_cmp_eq_u64 vcc, 0\n\t"
      "s_cselect_b64 s[36:37], -1, 0\n\t"
      "GKV0%=:\n\t"
      "s_waitcnt lgkmcnt(9)\n\t"
      "v_mfma_f32_16x16x32_bf16 v[64:67], a[104:107], v[52:55], v[64:67]\n\t"
      "ds_read_b128 v[28:31], v98 offset:23552\n\t"
      "v_mfma_f32_16x16x32_bf16 v[68:71], a[108:111], v[52:55], v[68:71]\n\t"
      "s_add_u32 m0, s76, 0\n\t"
      "s_mov_b64 exec, s[36:37]\n\t"
      "buffer_load_dwordx4 v97, s[60:63], s72 offen sc1 lds\n\t"
      "s_mov_b64 exec, -1\n\t"
      "s_waitcnt lgkmcnt(9)\n\t"
      "v_mfma_f32_16x16x32_bf16 v[64:67], a[112:115], v[56:59], v[64:67]\n\t"
      "ds_read_b128 v[32:35], v98 offset:24576\n\t"
      "v_mfma_f32_16x16x32_bf16 v[68:71], a[116:119], v[56:59], v[68:71]\n\t"
      "s_waitcnt lgkmcnt(9)\n\t"
      "v_mfma_f32_16x16x32_bf16 v[64:67], a[120:123], v[60:63], v[64:67]\n\t"
      "ds_read_b128 v[36:39], v98 offset:25600\n\t"
      "v_mfma_f32_16x16x32_bf16 v[68:71], a[124:127], v[60:63], v[68:71]\n\t"
      "s_add_u32 m0, s76, 0\n\t"
      "s_mov_b64 exec, s[36:37]\n\t"
      "buffer_load_dwordx4 v97, s[60:63], s72 offen offset:1024 sc1 lds\n\t"
      "s_mov_b64 exec, -1\n\t"
      "s_waitcnt lgkmcnt(9)\n\t"
      "v_mfma_f32_16x16x32_bf16 v[72:75], a[0:3], v[0:3], 0\n\t"
      "ds_read_b128 v[40:43], v98 offset:26624\n\t"
      "v_mfma_f32_16x16x32_bf16 v[76:79], a[4:7], v[0:3], 0\n\t"
      "s_waitcnt lgkmcnt(9)\n\t"
      "v_mfma_f32_16x16x32_bf16 v[72:75], a[8:11], v[4:7], v[72:75]\n\t"
      "ds_read_b128 v[44:47], v98 offset:27648\n\t"
      "v_mfma_f32_16x16x32_bf16 v[76:79], a[12:15], v[4:7], v[76:79]\n\t"
      "s_add_u32 m0, s76, 0\n\t"
      "s_mov_b64 exec, s[36:37]\n\t"
      "buffer_load_dwordx4 v97, s[60:63], s72 offen offset:2048 sc1 lds\n\t"
      "s_mov_b64 exec, -1\n\t"
      "s_cmp_eq_u32 s71, 1\n\t"
      "s_cbranch_scc0 GKV2%=\n\t"
      "s_waitcnt vmcnt(3)\n\t"
      "s_barrier\n\t"
      "s_mov_b64 exec, s[38:39]\n\t"
      "buffer_store_dword v107, off, s[60:63], s75 sc1\n\t"
      "s_mov_b64 exec, -1\n\t"
      "GKV2%=:\n\t"
      "s_waitcnt lgkmcnt(9)\n\t"
      "v_mfma_f32_16x16x32_bf16 v[72:75], a[16:19], v[8:11], v[72:75]\n\t"
      "ds_read_b128 v[48:51], v98 offset:28672\n\t"
      "v_mfma_f32_16x16x32_bf16 v[76:79], a[20:23], v[8:11], v[76:79]\n\t"
      "s_add_u32 m0, s74, 65536\n\t"
      "s_mov_b32 s78, 0\n\t"
      "s_mov_b64 exec, s[88:89]\n\t"
      "buffer_load_dwordx4 v116, s[64:67], s78 offen lds\n\t"
      "s_mov_b64 exec, -1\n\t"
      "s_waitcnt lgkmcnt(9)\n\t"
      "v_mfma_f32_16x16x32_bf16 v[72:75], a[24:27], v[12:15], v[72:75]\n\t"
      "ds_read_b128 v[52:55], v98 offset:29696\n\t"
      "v_mfma_f32_16x16x32_bf16 v[76:79], a[28:31], v[12:15], v[76:79]\n\t"
      "s_add_u32 m0, s76, 0\n\t"
      "s_mov_b64 exec, s[36:37]\n\t"
      "buffer_load_dwordx4 v97, s[60:63], s72 offen offset:3072 sc1 lds\n\t"
      "s_mov_b64 exec, -1\n\t"
      "s_waitcnt lgkmcnt(9)\n\t"
      "v_mfma_f32_16x16x32_bf16 v[72:75], a[32:35], v[16:19], v[72:75]\n\t"
      "ds_read_b128 v[56:59], v98 offset:30720\n\t"
      "v_mfma_f32_16x16x32_bf16 v[76:79], a[36:39], v[16:19], v[76:79]\n\t"
      "s_add_u32 m0, s74, 69632\n\t"
      "s_mov_b32 s78, 262144\n\t"
      "s_mov_b64 exec, s[88:89]\n\t"
      "buffer_load_dwordx4 v116, s[64:67], s78 offen lds\n\t"
      "s_mov_b64 exec, -1\n\t"
      "s_waitcnt lgkmcnt(9)\n\t"
      "v_mfma_f32_16x16x32_bf16 v[72:75], a[40:43], v[20:23], v[72:75]\n\t"
      "ds_read_b128 v[60:63], v98 offset:31744\n\t"
      "v_mfma_f32_16x16x32_bf16 v[76:79], a[44:47], v[20:23], v[76:79]\n\t"
      "s_add_u32 m0, s76, 4096\n\t"
      "s_add_u32 s72, s72, 4096\n\t"
      "s_mov_b64 exec, s[36:37]\n\t"
      "buffer_load_dwordx4 v97, s[60:63], s72 offen sc1 lds\n\t"
      "s_mov_b64 exec, -1\n\t"
      "v_cvt_pk_bf16_f32 v80, v64, v65\n\t"
      "v_cvt_pk_bf16_f32 v81, v66, v67\n\t"
      "v_cvt_pk_bf16_f32 v82, v68, v69\n\t"
      "v_cvt_pk_bf16_f32 v83, v70, v71\n\t"
      "buffer_store_dwordx4 v[80:83], v97, s[60:63], s73 offen sc1\n\t"
      "s_waitcnt lgkmcnt(9)\n\t"
      "v_mfma_f32_16x16x32_bf16 v[72:75], a[48:51], v[24:27], v[72:75]\n\t"
      "v_mfma_f32_16x16x32_bf16 v[76:79], a[52:55], v[24:27], v[76:79]\n\t"
      "s_add_u32 m0, s74, 73728\n\t"
      "s_mov_b32 s78, 524288\n\t"
      "s_mov_b64 exec, s[88:89]\n\t"
      "buffer_load_dwordx4 v116, s[64:67], s78 offen lds\n\t"
      "s_mov_b64 exec, -1\n\t"
      "s_waitcnt lgkmcnt(8)\n\t"
      "v_mfma_f32_16x16x32_bf16 v[72:75], a[56:59], v[28:31], v[72:75]\n\t"
      "v_mfma_f32_16x16x32_bf16 v[76:79], a[60:63], v[28:31], v[76:79]\n\t"
      "s_add_u32 m0, s76, 4096\n\t"
      "s_mov_b64 exec, s[36:37]\n\t"
      "buffer_load_dwordx4 v97, s[60:63], s72 offen offset:1024 sc1 lds\n\t"
      "s_mov_b64 exec, -1\n\t"
      "s_waitcnt lgkmcnt(7)\n\t"
      "v_mfma_f32_16x16x32_bf16 v[72:75], a[64:67], v[32:35], v[72:75]\n\t"
      "v_mfma_f32_16x16x32_bf16 v[76:79], a[68:71], v[32:35], v[76:79]\n\t"
      "s_add_u32 m0, s74, 77824\n\t"
      "s_mov_b32 s78, 786432\n\t"
      "s_mov_b64 exec, s[88:89]\n\t"
      "buffer_load_dwordx4 v116, s[64:67], s78 offen lds\n\t"
      "s_mov_b64 exec, -1\n\t"
      "s_waitcnt lgkmcnt(6)\n\t"
      "v_mfma_f32_16x16x32_bf16 v[72:75], a[72:75], v[36:39], v[72:75]\n\t"
      "v_mfma_f32_16x16x32_bf16 v[76:79], a[76:79], v[36:39], v[76:79]\n\t"
      "s_add_u32 m0, s76, 4096\n\t"
      "s_mov_b64 exec, s[36:37]\n\t"
      "buffer_load_dwordx4 v97, s[60:63], s72 offen offset:2048 sc1 lds\n\t"
      "s_mov_b64 exec, -1\n\t"
      "s_waitcnt lgkmcnt(5)\n\t"
      "v_mfma_f32_16x16x32_bf16 v[72:75], a[80:83], v[40:43], v[72:75]\n\t"
      "v_mfma_f32_16x16x32_bf16 v[76:79], a[84:87], v[40:43], v[76:79]\n\t"
      "s_add_u32 m0, s74, 81920\n\t"
      "s_mov_b32 s78, 1048576\n\t"
      "s_mov_b64 exec, s[88:89]\n\t"
      "buffer_load_dwordx4 v116, s[64:67], s78 offen lds\n\t"
      "s_mov_b64 exec, -1\n\t"
      "s_waitcnt lgkmcnt(4)\n\t"
      "v_mfma_f32_16x16x32_bf16 v[72:75], a[88:91], v[44:47], v[72:75]\n\t"
      "v_mfma_f32_16x16x32_bf16 v[76:79], a[92:95], v[44:47], v[76:79]\n\t"
      "s_add_u32 m0, s76, 4096\n\t"
      "s_mov_b64 exec, s[36:37]\n\t"
      "buffer_load_dwordx4 v97, s[60:63], s72 offen offset:3072 sc1 lds\n\t"
      "s_mov_b64 exec, -1\n\t"
      "s_waitcnt lgkmcnt(3)\n\t"
      "v_mfma_f32_16x16x32_bf16 v[72:75], a[96:99], v[48:51], v[72:75]\n\t"
      "v_mfma_f32_16x16x32_bf16 v[76:79], a[100:103], v[48:51], v[76:79]\n\t"
      "s_add_u32 m0, s74, 86016\n\t"
      "s_mov_b32 s78, 1310720\n\t"
      "s_mov_b64 exec, s[88:89]\n\t"
      "buffer_load_dwordx4 v116, s[64:67], s78 offen lds\n\t"
      "s_mov_b64 exec, -1\n\t"
      "s_waitcnt lgkmcnt(2)\n\t"
      "v_mfma_f32_16x16x32_bf16 v[72:75], a[104:107], v[52:55], v[72:75]\n\t"
      "v_mfma_f32_16x16x32_bf16 v[76:79], a[108:111], v[52:55], v[76:79]\n\t"
      "s_waitcnt lgkmcnt(1)\n\t"
      "v_mfma_f32_16x16x32_bf16 v[72:75], a[112:115], v[56:59], v[72:75]\n\t"
      "v_mfma_f32_16x16x32_bf16 v[76:79], a[116:119], v[56:59], v[76:79]\n\t"
      "s_add_u32 m0, s74, 90112\n\t"
      "s_mov_b32 s78, 1572864\n\t"
      "s_mov_b64 exec, s[88:89]\n\t"
      "buffer_load_dwordx4 v116, s[64:67], s78 offen lds\n\t"
      "s_mov_b64 exec, -1\n\t"
      "s_waitcnt lgkmcnt(0)\n\t"
      "v_mfma_f32_16x16x32_bf16 v[72:75], a[120:123], v[60:63], v[72:75]\n\t"
      "v_mfma_f32_16x16x32_bf16 v[76:79], a[124:127], v[60:63], v[76:79]\n\t"
      "s_nop 6\n\t"
      "v_cvt_pk_bf16_f32 v84, v72, v73\n\t"
      "v_cvt_pk_bf16_f32 v85, v74, v75\n\t"
      "v_cvt_pk_bf16_f32 v86, v76, v77\n\t"
      "v_cvt_pk_bf16_f32 v87, v78, v79\n\t"
      "buffer_store_dwordx4 v[84:87], v97, s[60:63], s73 offen offset:1024 sc1\n\t"
      "s_cmp_eq_u32 s71, 2\n\t"
      "s_cbranch_scc1 GKV3%=\n\t"
      "s_cmp_lg_u32 s71, 0\n\t"
      "s_cbranch_scc1 GKV1%=\n\t"
      "s_cmp_lg_u64 s[36:37], 0\n\t"
      "s_cbranch_scc1 GL2%=\n\t"
      "GL0%=:\n\t"
      "buffer_load_dword v128, v106, s[60:63], s79 offen sc1\n\t"
      "buffer_load_dword v129, v106, s[60:63], s79 offen offset:128 sc1\n\t"
      "s_sub_u32 s81, s81, 1\n\t"
      "s_waitcnt vmcnt(0)\n\t"
      "v_bitop3_b32 v128, v128, v129, %3 bitop3:0x7e\n\t"
      "v_cmp_ne_u32 vcc, 0, v128\n\t"
      "s_cmp_eq_u32 s81, 0\n\t"
      "s_cbranch_scc1 GL1%=\n\t"
      "s_cbranch_vccnz GL0%=\n\t"
      "GL1%=:\n\t"
      "v_readlane_b32 s72, v101, 1\n\t"
      "s_add_u32 s76, s85, 0\n\t"
      "s_add_u32 m0, s76, 0\n\t"
      "s_nop 2\n\t"
      "buffer_load_dwordx4 v97, s[60:63], s72 offen sc1 lds\n\t"
      "buffer_load_dwordx4 v97, s[60:63], s72 offen offset:1024 sc1 lds\n\t"
      "buffer_load_dwordx4 v97, s[60:63], s72 offen offset:2048 sc1 lds\n\t"
      "buffer_load_dwordx4 v97, s[60:63], s72 offen offset:3072 sc1 lds\n\t"
      "s_add_u32 m0, s76, 4096\n\t"
      "s_add_u32 s72, s72, 4096\n\t"
      "buffer_load_dwordx4 v97, s[60:63], s72 offen sc1 lds\n\t"
      "buffer_load_dwordx4 v97, s[60:63], s72 offen offset:1024 sc1 lds\n\t"
      "buffer_load_dwordx4 v97, s[60:63], s72 offen offset:2048 sc1 lds\n\t"
      "buffer_load_dwordx4 v97, s[60:63], s72 offen offset:3072 sc1 lds\n\t"
      "s_waitcnt vmcnt(0)\n\t"
      "GL2%=:\n\t"
      "s_mov_b32 s77, s73\n\t"
      "v_readlane_b32 s73, v102, 1\n\t"
      "v_mov_b32 v98, v97\n\t"
      "s_mov_b64 s[88:89], -1\n\t"
      "s_mov_b64 s[38:39], s[90:91]\n\t"
      "s_mov_b64 s[36:37], -1\n\t"
      "s_mov_b32 s71, 1\n\t"
      "v_readlane_b32 s72, v101, 2\n\t"
      "s_add_u32 s76, s85, 32768\n\t"
      "s_waitcnt vmcnt(1)\n\t"
      "s_barrier\n\t"
      "s_branch GKV%=\n\t"
      "GKV1%=:\n\t"
      "v_readlane_b32 s75, v103, 1\n\t"
      "v_add_u32 v117, 0x10000, v117\n\t"
      "v_add_u32 v98, 131072, v97\n\t"
      "s_mov_b32 s68, 0\n\t"
      "s_add_u32 m0, s74, 94208\n\t"
      "s_mov_b32 s78, 1835008\n\t"
      "buffer_load_dwordx4 v116, s[64:67], s78 offen lds\n\t"
      "s_add_u32 m0, s74, 98304\n\t"
      "s_mov_b32 s78, 2097152\n\t"
      "buffer_load_dwordx4 v116, s[64:67], s78 offen lds\n\t"
      "s_add_u32 m0, s74, 102400\n\t"
      "s_mov_b32 s78, 2359296\n\t"
      "buffer_load_dwordx4 v116, s[64:67], s78 offen lds\n\t"
      "s_add_u32 m0, s74, 106496\n\t"
      "s_mov_b32 s78, 2621440\n\t"
      "buffer_load_dwordx4 v116, s[64:67], s78 offen lds\n\t"
      "s_add_u32 m0, s74, 110592\n\t"
      "s_mov_b32 s78, 2883584\n\t"
      "buffer_load_dwordx4 v116, s[64:67], s78 offen lds\n\t"
      "s_add_u32 m0, s74, 114688\n\t"
      "s_mov_b32 s78, 3145728\n\t"
      "buffer_load_dwordx4 v116, s[64:67], s78 offen lds\n\t"
      "s_add_u32 m0, s74, 118784\n\t"
      "s_mov_b32 s78, 3407872\n\t"
      "buffer_load_dwordx4 v116, s[64:67], s78 offen lds\n\t"
      "s_add_u32 m0, s74, 122880\n\t"
      "s_mov_b32 s78, 3670016\n\t"
      "buffer_load_dwordx4 v116, s[64:67], s78 offen lds\n\t"
      "s_add_u32 m0, s74, 126976\n\t"
      "s_mov_b32 s78, 3932160\n\t"
      "buffer_load_dwordx4 v116, s[64:67], s78 offen lds\n\t"
      "v_add_u32 v98, 0x8000, v97\n\t"
      "v_readlane_b32 s73, v102, 2\n\t"
      "s_mov_b64 s[88:89], 0\n\t"
      "s_mov_b64 s[36:37], 0\n\t"
      "s_mov_b32 s71, 2\n\t"
      "s_waitcnt vmcnt(9)\n\t"
      "s_barrier\n\t"
      "s_mov_b64 exec, s[90:91]\n\t"
      "buffer_store_dword v107, off, s[60:63], s75 sc1\n\t"
      "s_mov_b64 exec, -1\n\t"
      "s_branch GKV%=\n\t"
      "GKV3%=:\n\t"
      "v_readlane_b32 s75, v103, 2\n\t"
      "v_add_u32 v98, 131072, v97\n\t"
      "s_waitcnt vmcnt(0)\n\t"
      "s_barrier\n\t"
      "s_mov_b64 exec, s[90:91]\n\t"
      "s_nop 0\n\t"
      "buffer_store_dword v107, off, s[60:63], s75 sc1\n\t"
      "s_mov_b64 exec, -1\n\t"
      "s_waitcnt lgkmcnt(12)\n\t"
      "ds_read_b64_tr_b16 a[128:129], v117\n\t"
      "ds_read_b64_tr_b16 a[130:131], v117 offset:512\n\t"
      "s_waitcnt lgkmcnt(12)\n\t"
      "ds_read_b64_tr_b16 a[132:133], v117 offset:4096\n\t"
      "ds_read_b64_tr_b16 a[134:135], v117 offset:4608\n\t"
      "s_waitcnt lgkmcnt(12)\n\t"
      "ds_read_b64_tr_b16 a[136:137], v117 offset:8192\n\t"
      "ds_read_b64_tr_b16 a[138:139], v117 offset:8704\n\t"
      "s_waitcnt lgkmcnt(12)\n\t"
      "ds_read_b64_tr_b16 a[140:141], v117 offset:12288\n\t"
      "ds_read_b64_tr_b16 a[142:143], v117 offset:12800\n\t"
      "s_waitcnt lgkmcnt(12)\n\t"
      "ds_read_b64_tr_b16 a[144:145], v117 offset:16384\n\t"
      "ds_read_b64_tr_b16 a[146:147], v117 offset:16896\n\t"
      "s_waitcnt lgkmcnt(12)\n\t"
      "ds_read_b64_tr_b16 a[148:149], v117 offset:20480\n\t"
      "ds_read_b64_tr_b16 a[150:151], v117 offset:20992\n\t"
      "s_waitcnt lgkmcnt(12)\n\t"
      "ds_read_b64_tr_b16 a[152:153], v117 offset:24576\n\t"
      "ds_read_b64_tr_b16 a[154:155], v117 offset:25088\n\t"
      "s_waitcnt lgkmcnt(12)\n\t"
      "ds_read_b64_tr_b16 a[156:157], v117 offset:28672\n\t"
      "ds_read_b64_tr_b16 a[158:159], v117 offset:29184\n\t"
      "s_waitcnt lgkmcnt(12)\n\t"
      "ds_read_b64_tr_b16 a[160:161], v117 offset:32768\n\t"
      "ds_read_b64_tr_b16 a[162:163], v117 offset:33280\n\t"
      "s_waitcnt lgkmcnt(12)\n\t"
      "ds_read_b64_tr_b16 a[164:165], v117 offset:36864\n\t"
      "ds_read_b64_tr_b16 a[166:167], v117 offset:37376\n\t"
      "s_waitcnt lgkmcnt(12)\n\t"
      "ds_read_b64_tr_b16 a[168:169], v117 offset:40960\n\t"
      "ds_read_b64_tr_b16 a[170:171], v117 offset:41472\n\t"
      "s_waitcnt lgkmcnt(12)\n\t"
      "ds_read_b64_tr_b16 a[172:173], v117 offset:45056\n\t"
      "ds_read_b64_tr_b16 a[174:175], v117 offset:45568\n\t"
      "s_waitcnt lgkmcnt(12)\n\t"
      "ds_read_b64_tr_b16 a[176:177], v117 offset:49152\n\t"
      "ds_read_b64_tr_b16 a[178:179], v117 offset:49664\n\t"
      "s_waitcnt lgkmcnt(12)\n\t"
      "ds_read_b64_tr_b16 a[180:181], v117 offset:53248\n\t"
      "ds_read_b64_tr_b16 a[182:183], v117 offset:53760\n\t"
      "s_waitcnt lgkmcnt(12)\n\t"
      "ds_read_b64_tr_b16 a[184:185], v117 offset:57344\n\t"
      "ds_read_b64_tr_b16 a[186:187], v117 offset:57856\n\t"
      "s_waitcnt lgkmcnt(12)\n\t"
      "ds_read_b64_tr_b16 a[188:189], v117 offset:61440\n\t"
      "ds_read_b64_tr_b16 a[190:191], v117 offset:61952\n\t"
      "s_waitcnt lgkmcnt(4)\n\t"
      "GQ%=:\n\t"
      "ds_read_b128 v[0:3], v98\n\t"
      "ds_read_b128 v[4:7], v98 offset:1024\n\t"
      "ds_read_b128 v[8:11], v98 offset:2048\n\t"
      "ds_read_b128 v[12:15], v98 offset:3072\n\t"
      "ds_read_b128 v[16:19], v98 offset:4096\n\t"
      "ds_read_b128 v[20:23], v98 offset:5120\n\t"
      "ds_read_b128 v[24:27], v98 offset:6144\n\t"
      "ds_read_b128 v[28:31], v98 offset:7168\n\t"
      "ds_read_b128 v[32:35], v98 offset:8192\n\t"
      "ds_read_b128 v[36:39], v98 offset:9216\n\t"
      "s_waitcnt lgkmcnt(9)\n\t"
      "v_mfma_f32_16x16x32_bf16 v[88:91], a[128:131], v[0:3], 0\n\t"
      "ds_read_b128 v[40:43], v98 offset:10240\n\t"
      "s_waitcnt lgkmcnt(9)\n\t"
      "v_mfma_f32_16x16x32_bf16 v[88:91], a[132:135], v[4:7], v[88:91]\n\t"
      "ds_read_b128 v[44:47], v98 offset:11264\n\t"
      "s_waitcnt lgkmcnt(9)\n\t"
      "v_mfma_f32_16x16x32_bf16 v[88:91], a[136:139], v[8:11], v[88:91]\n\t"
      "ds_read_b128 v[48:51], v98 offset:12288\n\t"
      "s_waitcnt lgkmcnt(9)\n\t"
      "v_mfma_f32_16x16x32_bf16 v[88:91], a[140:143], v[12:15], v[88:91]\n\t"
      "ds_read_b128 v[52:55], v98 offset:13312\n\t"
      "s_waitcnt lgkmcnt(9)\n\t"
      "v_mfma_f32_16x16x32_bf16 v[88:91], a[144:147], v[16:19], v[88:91]\n\t"
      "ds_read_b128 v[56:59], v98 offset:14336\n\t"
      "s_waitcnt lgkmcnt(9)\n\t"
      "v_mfma_f32_16x16x32_bf16 v[88:91], a[148:151], v[20:23], v[88:91]\n\t"
      "ds_read_b128 v[60:63], v98 offset:15360\n\t"
      "s_waitcnt lgkmcnt(9)\n\t"
      "v_mfma_f32_16x16x32_bf16 v[88:91], a[152:155], v[24:27], v[88:91]\n\t"
      "ds_read_b128 v[0:3], v98 offset:16384\n\t"
      "s_waitcnt lgkmcnt(9)\n\t"
      "v_mfma_f32_16x16x32_bf16 v[88:91], a[156:159], v[28:31], v[88:91]\n\t"
      "ds_read_b128 v[4:7], v98 offset:17408\n\t"
      "s_waitcnt lgkmcnt(9)\n\t"
      "v_mfma_f32_16x16x32_bf16 v[88:91], a[160:163], v[32:35], v[88:91]\n\t"
      "ds_read_b128 v[8:11], v98 offset:18432\n\t"
      "s_waitcnt lgkmcnt(9)\n\t"
      "v_mfma_f32_16x16x32_bf16 v[88:91], a[164:167], v[36:39], v[88:91]\n\t"
      "ds_read_b128 v[12:15], v98 offset:19456\n\t"
      "s_waitcnt lgkmcnt(9)\n\t"
      "v_mfma_f32_16x16x32_bf16 v[88:91], a[168:171], v[40:43], v[88:91]\n\t"
      "ds_read_b128 v[16:19], v98 offset:20480\n\t"
      "s_waitcnt lgkmcnt(9)\n\t"
      "v_mfma_f32_16x16x32_bf16 v[88:91], a[172:175], v[44:47], v[88:91]\n\t"
      "ds_read_b128 v[20:23], v98 offset:21504\n\t"
      "s_waitcnt lgkmcnt(9)\n\t"
      "v_mfma_f32_16x16x32_bf16 v[88:91], a[176:179], v[48:51], v[88:91]\n\t"
      "ds_read_b128 v[24:27], v98 offset:22528\n\t"
      "s_waitcnt lgkmcnt(9)\n\t"
      "v_mfma_f32_16x16x32_bf16 v[88:91], a[180:183], v[52:55], v[88:91]\n\t"
      "ds_read_b128 v[28:31], v98 offset:23552\n\t"
      "s_waitcnt lgkmcnt(9)\n\t"
      "v_mfma_f32_16x16x32_bf16 v[88:91], a[184:187], v[56:59], v[88:91]\n\t"
      "ds_read_b128 v[32:35], v98 offset:24576\n\t"
      "s_waitcnt lgkmcnt(9)\n\t"
      "v_mfma_f32_16x16x32_bf16 v[88:91], a[188:191], v[60:63], v[88:91]\n\t"
      "ds_read_b128 v[36:39], v98 offset:25600\n\t"
      "s_waitcnt lgkmcnt(9)\n\t"
      "v_mfma_f32_16x16x32_bf16 v[92:95], a[128:131], v[0:3], 0\n\t"
      "ds_read_b128 v[40:43], v98 offset:26624\n\t"
      "s_waitcnt lgkmcnt(9)\n\t"
      "v_mfma_f32_16x16x32_bf16 v[92:95], a[132:135], v[4:7], v[92:95]\n\t"
      "ds_read_b128 v[44:47], v98 offset:27648\n\t"
      "s_waitcnt lgkmcnt(9)\n\t"
      "v_mfma_f32_16x16x32_bf16 v[92:95], a[136:139], v[8:11], v[92:95]\n\t"
      "ds_read_b128 v[48:51], v98 offset:28672\n\t"
      "s_waitcnt lgkmcnt(9)\n\t"
      "v_mfma_f32_16x16x32_bf16 v[92:95], a[140:143], v[12:15], v[92:95]\n\t"
      "ds_read_b128 v[52:55], v98 offset:29696\n\t"
      "s_waitcnt lgkmcnt(9)\n\t"
      "v_mfma_f32_16x16x32_bf16 v[92:95], a[144:147], v[16:19], v[92:95]\n\t"
      "ds_read_b128 v[56:59], v98 offset:30720\n\t"
      "s_waitcnt lgkmcnt(9)\n\t"
      "v_mfma_f32_16x16x32_bf16 v[92:95], a[148:151], v[20:23], v[92:95]\n\t"
      "ds_read_b128 v[60:63], v98 offset:31744\n\t"
      "s_waitcnt lgkmcnt(9)\n\t"
      "v_mfma_f32_16x16x32_bf16 v[92:95], a[152:155], v[24:27], v[92:95]\n\t"
      "s_waitcnt lgkmcnt(8)\n\t"
      "v_mfma_f32_16x16x32_bf16 v[92:95], a[156:159], v[28:31], v[92:95]\n\t"
      "s_waitcnt lgkmcnt(7)\n\t"
      "v_mfma_f32_16x16x32_bf16 v[92:95], a[160:163], v[32:35], v[92:95]\n\t"
      "s_waitcnt lgkmcnt(6)\n\t"
      "v_mfma_f32_16x16x32_bf16 v[92:95], a[164:167], v[36:39], v[92:95]\n\t"
      "s_waitcnt lgkmcnt(5)\n\t"
      "v_mfma_f32_16x16x32_bf16 v[92:95], a[168:171], v[40:43], v[92:95]\n\t"
      "s_waitcnt lgkmcnt(4)\n\t"
      "v_mfma_f32_16x16x32_bf16 v[92:95], a[172:175], v[44:47], v[92:95]\n\t"
      "s_waitcnt lgkmcnt(3)\n\t"
      "v_mfma_f32_16x16x32_bf16 v[92:95], a[176:179], v[48:51], v[92:95]\n\t"
      "s_waitcnt lgkmcnt(2)\n\t"
      "v_mfma_f32_16x16x32_bf16 v[92:95], a[180:183], v[52:55], v[92:95]\n\t"
      "s_waitcnt lgkmcnt(1)\n\t"
      "v_mfma_f32_16x16x32_bf16 v[92:95], a[184:187], v[56:59], v[92:95]\n\t"
      "s_waitcnt lgkmcnt(0)\n\t"
      "v_mfma_f32_16x16x32_bf16 v[92:95], a[188:191], v[60:63], v[92:95]\n\t"
      "v_cvt_pk_bf16_f32 v112, v88, v89\n\t"
      "v_cvt_pk_bf16_f32 v113, v90, v91\n\t"
      "s_cmp_eq_u32 s68, 3\n\t"
      "s_waitcnt vmcnt(17)\n\t"
      "s_barrier\n\t"
      "s_cbranch_scc1 GQ1%=\n\t"
      "s_nop 3\n\t"
      "v_cvt_pk_bf16_f32 v114, v92, v93\n\t"
      "v_cvt_pk_bf16_f32 v115, v94, v95\n\t"
      "s_nop 0\n\t"
      "v_permlane16_swap_b32 v112, v114\n\t"
      "v_permlane16_swap_b32 v113, v115\n\t"
      "buffer_store_dwordx4 v[112:115], v111, s[60:63], s77 offen sc1\n\t"
      "s_cmp_lg_u32 s68, 0\n\t"
      "s_cbranch_scc1 GQ2%=\n\t"
      "v_readlane_b32 s72, v101, 3\n\t"
      "s_add_u32 s76, s85, 65536\n\t"
      "s_add_u32 m0, s76, 0\n\t"
      "s_nop 2\n\t"
      "buffer_load_dwordx4 v97, s[60:63], s72 offen sc1 lds\n\t"
      "buffer_load_dwordx4 v97, s[60:63], s72 offen offset:1024 sc1 lds\n\t"
      "buffer_load_dwordx4 v97, s[60:63], s72 offen offset:2048 sc1 lds\n\t"
      "buffer_load_dwordx4 v97, s[60:63], s72 offen offset:3072 sc1 lds\n\t"
      "s_add_u32 m0, s76, 4096\n\t"
      "s_add_u32 s72, s72, 4096\n\t"
      "buffer_load_dwordx4 v97, s[60:63], s72 offen sc1 lds\n\t"
      "buffer_load_dwordx4 v97, s[60:63], s72 offen offset:1024 sc1 lds\n\t"
      "buffer_load_dwordx4 v97, s[60:63], s72 offen offset:2048 sc1 lds\n\t"
      "buffer_load_dwordx4 v97, s[60:63], s72 offen offset:3072 sc1 lds\n\t"
      "v_readlane_b32 s72, v101, 4\n\t"
      "s_add_u32 s76, s85, 98304\n\t"
      "s_add_u32 m0, s76, 0\n\t"
      "s_nop 2\n\t"
      "buffer_load_dwordx4 v97, s[60:63], s72 offen sc1 lds\n\t"
      "buffer_load_dwordx4 v97, s[60:63], s72 offen offset:1024 sc1 lds\n\t"
      "buffer_load_dwordx4 v97, s[60:63], s72 offen offset:2048 sc1 lds\n\t"
      "buffer_load_dwordx4 v97, s[60:63], s72 offen offset:3072 sc1 lds\n\t"
      "s_add_u32 m0, s76, 4096\n\t"
      "s_add_u32 s72, s72, 4096\n\t"
      "buffer_load_dwordx4 v97, s[60:63], s72 offen sc1 lds\n\t"
      "buffer_load_dwordx4 v97, s[60:63], s72 offen offset:1024 sc1 lds\n\t"
      "buffer_load_dwordx4 v97, s[60:63], s72 offen offset:2048 sc1 lds\n\t"
      "buffer_load_dwordx4 v97, s[60:63], s72 offen offset:3072 sc1 lds\n\t"
      "v_mov_b32 v98, v97\n\t"
      "v_readlane_b32 s77, v102, 1\n\t"
      "s_mov_b32 s68, 1\n\t"
      "s_branch GQ%=\n\t"
      "GQ2%=:\n\t"
      "v_readlane_b32 s72, v101, 5\n\t"
      "s_add_u32 s76, s85, 0\n\t"
      "s_add_u32 m0, s76, 0\n\t"
      "s_nop 2\n\t"
      "buffer_load_dwordx4 v97, s[60:63], s72 offen sc1 lds\n\t"
      "buffer_load_dwordx4 v97, s[60:63], s72 offen offset:1024 sc1 lds\n\t"
      "buffer_load_dwordx4 v97, s[60:63], s72 offen offset:2048 sc1 lds\n\t"
      "buffer_load_dwordx4 v97, s[60:63], s72 offen offset:3072 sc1 lds\n\t"
      "s_add_u32 m0, s76, 4096\n\t"
      "s_add_u32 s72, s72, 4096\n\t"
      "buffer_load_dwordx4 v97, s[60:63], s72 offen sc1 lds\n\t"
      "buffer_load_dwordx4 v97, s[60:63], s72 offen offset:1024 sc1 lds\n\t"
      "buffer_load_dwordx4 v97, s[60:63], s72 offen offset:2048 sc1 lds\n\t"
      "buffer_load_dwordx4 v97, s[60:63], s72 offen offset:3072 sc1 lds\n\t"
      "v_add_u32 v98, 0x8000, v97\n\t"
      "s_mov_b32 s68, 3\n\t"
      "s_branch GQ%=\n\t"
      "GQ1%=:\n\t"
      "s_mov_b32 s69, 6\n\t"
      "s_mov_b32 s70, 63\n\t"
      "s_waitcnt vmcnt(9)\n\t"
      "s_mov_b32 s77, 0x10000\n\t"
      "s_mov_b32 s71, 5\n\t"
      "s_getpc_b64 s[88:89]\n\t"
      "GPC%=:\n\t"
      "s_add_u32 s88, s88, GJOBS%=-GPC%=\n\t"
      "s_addc_u32 s89, s89, 0\n\t"
      "v_add_u32 v98, s77, v97\n\t"
      "s_add_u32 s76, s85, 0x8000\n\t"
      "ds_read_b128 v[0:3], v98\n\t"
      "ds_read_b128 v[4:7], v98 offset:1024\n\t"
      "ds_read_b128 v[8:11], v98 offset:2048\n\t"
      "ds_read_b128 v[12:15], v98 offset:3072\n\t"
      "ds_read_b128 v[16:19], v98 offset:4096\n\t"
      "ds_read_b128 v[20:23], v98 offset:5120\n\t"
      "ds_read_b128 v[24:27], v98 offset:6144\n\t"
      "ds_read_b128 v[28:31], v98 offset:7168\n\t"
      "ds_read_b128 v[32:35], v98 offset:8192\n\t"
      "ds_read_b128 v[36:39], v98 offset:9216\n\t"
      ".p2align 6\n\t"
      "GU%=:\n\t"
      "s_cmp_eq_u32 s68, s71\n\t"
      "s_cbranch_scc1 GCALL%=\n\t"
      "GBACK%=:\n\t"
      "s_waitcnt lgkmcnt(9)\n\t"
      "v_mfma_f32_16x16x32_bf16 v[64:67], a[0:3], v[0:3], 0\n\t"
      "ds_read_b128 v[40:43], v98 offset:10240\n\t"
      "v_mfma_f32_16x16x32_bf16 v[68:71], a[4:7], v[0:3], 0\n\t"
      "v_readlane_b32 s72, v101, s69\n\t"
      "v_mfma_f32_16x16x32_bf16 v[88:91], a[128:131], v[0:3], 0\n\t"
      "s_waitcnt lgkmcnt(9)\n\t"
      "v_mfma_f32_16x16x32_bf16 v[64:67], a[8:11], v[4:7], v[64:67]\n\t"
      "ds_read_b128 v[44:47], v98 offset:11264\n\t"
      "v_mfma_f32_16x16x32_bf16 v[68:71], a[12:15], v[4:7], v[68:71]\n\t"
      "v_readlane_b32 s75, v103, s70\n\t"
      "v_mfma_f32_16x16x32_bf16 v[88:91], a[132:135], v[4:7], v[88:91]\n\t"
      "s_waitcnt lgkmcnt(9)\n\t"
      "v_mfma_f32_16x16x32_bf16 v[64:67], a[16:19], v[8:11], v[64:67]\n\t"
      "ds_read_b128 v[48:51], v98 offset:12288\n\t"
      "v_mfma_f32_16x16x32_bf16 v[68:71], a[20:23], v[8:11], v[68:71]\n\t"
      "v_cvt_pk_bf16_f32 v84, v72, v73\n\t"
      "v_cvt_pk_bf16_f32 v85, v74, v75\n\t"
      "v_mfma_f32_16x16x32_bf16 v[88:91], a[136:139], v[8:11], v[88:91]\n\t"
      "v_cvt_pk_bf16_f32 v114, v92, v93\n\t"
      "v_cvt_pk_bf16_f32 v115, v94, v95\n\t"
      "s_waitcnt lgkmcnt(9)\n\t"
      "v_mfma_f32_16x16x32_bf16 v[64:67], a[24:27], v[12:15], v[64:67]\n\t"
      "ds_read_b128 v[52:55], v98 offset:13312\n\t"
      "v_mfma_f32_16x16x32_bf16 v[68:71], a[28:31], v[12:15], v[68:71]\n\t"
      "v_cvt_pk_bf16_f32 v86, v76, v77\n\t"
      "v_cvt_pk_bf16_f32 v87, v78, v79\n\t"
      "v_mfma_f32_16x16x32_bf16 v[88:91], a[140:143], v[12:15], v[88:91]\n\t"
      "v_permlane16_swap_b32 v112, v114\n\t"
      "v_permlane16_swap_b32 v113, v115\n\t"
      "s_waitcnt lgkmcnt(9)\n\t"
      "v_mfma_f32_16x16x32_bf16 v[64:67], a[32:35], v[16:19], v[64:67]\n\t"
      "ds_read_b128 v[56:59], v98 offset:14336\n\t"
      "v_mfma_f32_16x16x32_bf16 v[68:71], a[36:39], v[16:19], v[68:71]\n\t"
      "buffer_store_dwordx4 v[84:87], v97, s[60:63], s73 offen offset:1024 sc1\n\t"
      "buffer_store_dwordx4 v[112:115], v111, s[60:63], s73 offen sc1\n\t"
      "v_mfma_f32_16x16x32_bf16 v[88:91], a[144:147], v[16:19], v[88:91]\n\t"
      "s_add_u32 m0, s76, 0\n\t"
      "buffer_load_dwordx4 v97, s[60:63], s72 offen sc1 lds\n\t"
      "s_waitcnt lgkmcnt(9)\n\t"
      "v_mfma_f32_16x16x32_bf16 v[64:67], a[40:43], v[20:23], v[64:67]\n\t"
      "ds_read_b128 v[60:63], v98 offset:15360\n\t"
      "v_mfma_f32_16x16x32_bf16 v[68:71], a[44:47], v[20:23], v[68:71]\n\t"
      "v_mfma_f32_16x16x32_bf16 v[88:91], a[148:151], v[20:23], v[88:91]\n\t"
      "s_waitcnt lgkmcnt(9)\n\t"
      "v_mfma_f32_16x16x32_bf16 v[64:67], a[48:51], v[24:27], v[64:67]\n\t"
      "ds_read_b128 v[0:3], v98 offset:16384\n\t"
      "v_mfma_f32_16x16x32_bf16 v[68:71], a[52:55], v[24:27], v[68:71]\n\t"
      "v_mfma_f32_16x16x32_bf16 v[88:91], a[152:155], v[24:27], v[88:91]\n\t"
      "s_waitcnt lgkmcnt(9)\n\t"
      "v_mfma_f32_16x16x32_bf16 v[64:67], a[56:59], v[28:31], v[64:67]\n\t"
      "ds_read_b128 v[4:7], v98 offset:17408\n\t"
      "v_mfma_f32_16x16x32_bf16 v[68:71], a[60:63], v[28:31], v[68:71]\n\t"
      "buffer_load_dwordx4 v97, s[60:63], s72 offen offset:1024 sc1 lds\n\t"
      "v_mfma_f32_16x16x32_bf16 v[88:91], a[156:159], v[28:31], v[88:91]\n\t"
      "s_waitcnt lgkmcnt(9)\n\t"
      "v_mfma_f32_16x16x32_bf16 v[64:67], a[64:67], v[32:35], v[64:67]\n\t"
      "ds_read_b128 v[8:11], v98 offset:18432\n\t"
      "v_mfma_f32_16x16x32_bf16 v[68:71], a[68:71], v[32:35], v[68:71]\n\t"
      "v_mfma_f32_16x16x32_bf16 v[88:91], a[160:163], v[32:35], v[88:91]\n\t"
      "s_waitcnt lgkmcnt(9)\n\t"
      "v_mfma_f32_16x16x32_bf16 v[64:67], a[72:75], v[36:39], v[64:67]\n\t"
      "ds_read_b128 v[12:15], v98 offset:19456\n\t"
      "v_mfma_f32_16x16x32_bf16 v[68:71], a[76:79], v[36:39], v[68:71]\n\t"
      "v_readlane_b32 s73, v102, s68\n\t"
      "v_mfma_f32_16x16x32_bf16 v[88:91], a[164:167], v[36:39], v[88:91]\n\t"
      "s_waitcnt lgkmcnt(9)\n\t"
      "v_mfma_f32_16x16x32_bf16 v[64:67], a[80:83], v[40:43], v[64:67]\n\t"
      "ds_read_b128 v[16:19], v98 offset:20480\n\t"
      "v_mfma_f32_16x16x32_bf16 v[68:71], a[84:87], v[40:43], v[68:71]\n\t"
      "buffer_load_dwordx4 v97, s[60:63], s72 offen offset:2048 sc1 lds\n\t"
      "v_mfma_f32_16x16x32_bf16 v[88:91], a[168:171], v[40:43], v[88:91]\n\t"
      "s_waitcnt lgkmcnt(9)\n\t"
      "v_mfma_f32_16x16x32_bf16 v[64:67], a[88:91], v[44:47], v[64:67]\n\t"
      "ds_read_b128 v[20:23], v98 offset:21504\n\t"
      "v_mfma_f32_16x16x32_bf16 v[68:71], a[92:95], v[44:47], v[68:71]\n\t"
      "v_mfma_f32_16x16x32_bf16 v[88:91], a[172:175], v[44:47], v[88:91]\n\t"
      "s_waitcnt lgkmcnt(9)\n\t"
      "v_mfma_f32_16x16x32_bf16 v[64:67], a[96:99], v[48:51], v[64:67]\n\t"
      "ds_read_b128 v[24:27], v98 offset:22528\n\t"
      "v_mfma_f32_16x16x32_bf16 v[68:71], a[100:103], v[48:51], v[68:71]\n\t"
      "v_mfma_f32_16x16x32_bf16 v[88:91], a[176:179], v[48:51], v[88:91]\n\t"
      "s_waitcnt lgkmcnt(9)\n\t"
      "v_mfma_f32_16x16x32_bf16 v[64:67], a[104:107], v[52:55], v[64:67]\n\t"
      "ds_read_b128 v[28:31], v98 offset:23552\n\t"
      "v_mfma_f32_16x16x32_bf16 v[68:71], a[108:111], v[52:55], v[68:71]\n\t"
      "buffer_load_dwordx4 v97, s[60:63], s72 offen offset:3072 sc1 lds\n\t"
      "v_mfma_f32_16x16x32_bf16 v[88:91], a[180:183], v[52:55], v[88:91]\n\t"
      "s_waitcnt lgkmcnt(9)\n\t"
      "v_mfma_f32_16x16x32_bf16 v[64:67], a[112:115], v[56:59], v[64:67]\n\t"
      "ds_read_b128 v[32:35], v98 offset:24576\n\t"
      "v_mfma_f32_16x16x32_bf16 v[68:71], a[116:119], v[56:59], v[68:71]\n\t"
      "v_mfma_f32_16x16x32_bf16 v[88:91], a[184:187], v[56:59], v[88:91]\n\t"
      "s_waitcnt lgkmcnt(9)\n\t"
      "v_mfma_f32_16x16x32_bf16 v[64:67], a[120:123], v[60:63], v[64:67]\n\t"
      "ds_read_b128 v[36:39], v98 offset:25600\n\t"
      "v_mfma_f32_16x16x32_bf16 v[68:71], a[124:127], v[60:63], v[68:71]\n\t"
      "s_add_u32 s68, s68, 1\n\t"
      "v_mfma_f32_16x16x32_bf16 v[88:91], a[188:191], v[60:63], v[88:91]\n\t"
      "s_waitcnt lgkmcnt(9)\n\t"
      "v_mfma_f32_16x16x32_bf16 v[72:75], a[0:3], v[0:3], 0\n\t"
      "ds_read_b128 v[40:43], v98 offset:26624\n\t"
      "v_mfma_f32_16x16x32_bf16 v[76:79], a[4:7], v[0:3], 0\n\t"
      "s_add_u32 m0, s76, 4096\n\t"
      "s_add_u32 s72, s72, 4096\n\t"
      "buffer_load_dwordx4 v97, s[60:63], s72 offen sc1 lds\n\t"
      "v_mfma_f32_16x16x32_bf16 v[92:95], a[128:131], v[0:3], 0\n\t"
      "s_add_u32 s69, s69, 1\n\t"
      "s_add_u32 s70, s70, 1\n\t"
      "s_waitcnt lgkmcnt(9)\n\t"
      "v_mfma_f32_16x16x32_bf16 v[72:75], a[8:11], v[4:7], v[72:75]\n\t"
      "ds_read_b128 v[44:47], v98 offset:27648\n\t"
      "v_mfma_f32_16x16x32_bf16 v[76:79], a[12:15], v[4:7], v[76:79]\n\t"
      "s_cmp_lt_u32 s68, 7\n\t"
      "s_cselect_b32 s70, 1, 3\n\t"
      "v_mfma_f32_16x16x32_bf16 v[92:95], a[132:135], v[4:7], v[92:95]\n\t"
      "s_sub_u32 s70, s68, s70\n\t"
      "s_add_u32 s76, s77, s85\n\t"
      "s_add_u32 s77, s77, 0x8000\n\t"
      "s_and_b32 s77, s77, 0x1ffff\n\t"
      "s_waitcnt lgkmcnt(9)\n\t"
      "v_mfma_f32_16x16x32_bf16 v[72:75], a[16:19], v[8:11], v[72:75]\n\t"
      "ds_read_b128 v[48:51], v98 offset:28672\n\t"
      "v_mfma_f32_16x16x32_bf16 v[76:79], a[20:23], v[8:11], v[76:79]\n\t"
      "v_cvt_pk_bf16_f32 v80, v64, v65\n\t"
      "v_cvt_pk_bf16_f32 v81, v66, v67\n\t"
      "v_mfma_f32_16x16x32_bf16 v[92:95], a[136:139], v[8:11], v[92:95]\n\t"
      "v_cvt_pk_bf16_f32 v112, v88, v89\n\t"
      "s_waitcnt lgkmcnt(9)\n\t"
      "v_mfma_f32_16x16x32_bf16 v[72:75], a[24:27], v[12:15], v[72:75]\n\t"
      "ds_read_b128 v[52:55], v98 offset:29696\n\t"
      "v_mfma_f32_16x16x32_bf16 v[76:79], a[28:31], v[12:15], v[76:79]\n\t"
      "buffer_load_dwordx4 v97, s[60:63], s72 offen offset:1024 sc1 lds\n\t"
      "v_cvt_pk_bf16_f32 v82, v68, v69\n\t"
      "v_mfma_f32_16x16x32_bf16 v[92:95], a[140:143], v[12:15], v[92:95]\n\t"
      "v_cvt_pk_bf16_f32 v83, v70, v71\n\t"
      "v_cvt_pk_bf16_f32 v113, v90, v91\n\t"
      "s_waitcnt lgkmcnt(9)\n\t"
      "v_mfma_f32_16x16x32_bf16 v[72:75], a[32:35], v[16:19], v[72:75]\n\t"
      "ds_read_b128 v[56:59], v98 offset:30720\n\t"
      "v_mfma_f32_16x16x32_bf16 v[76:79], a[36:39], v[16:19], v[76:79]\n\t"
      "buffer_store_dwordx4 v[80:83], v97, s[60:63], s73 offen sc1\n\t"
      "v_mfma_f32_16x16x32_bf16 v[92:95], a[144:147], v[16:19], v[92:95]\n\t"
      "s_waitcnt lgkmcnt(9)\n\t"
      "v_mfma_f32_16x16x32_bf16 v[72:75], a[40:43], v[20:23], v[72:75]\n\t"
      "ds_read_b128 v[60:63], v98 offset:31744\n\t"
      "v_mfma_f32_16x16x32_bf16 v[76:79], a[44:47], v[20:23], v[76:79]\n\t"
      "v_mfma_f32_16x16x32_bf16 v[92:95], a[148:151], v[20:23], v[92:95]\n\t"
      "s_waitcnt lgkmcnt(9)\n\t"
      "v_mfma_f32_16x16x32_bf16 v[72:75], a[48:51], v[24:27], v[72:75]\n\t"
      "v_mfma_f32_16x16x32_bf16 v[76:79], a[52:55], v[24:27], v[76:79]\n\t"
      "buffer_load_dwordx4 v97, s[60:63], s72 offen offset:2048 sc1 lds\n\t"
      "v_add_u32 v98, s77, v97\n\t"
      "v_mfma_f32_16x16x32_bf16 v[92:95], a[152:155], v[24:27], v[92:95]\n\t"
      "s_cmp_lg_u32 s75, 0\n\t"
      "s_waitcnt lgkmcnt(8)\n\t"
      "v_mfma_f32_16x16x32_bf16 v[72:75], a[56:59], v[28:31], v[72:75]\n\t"
      "v_mfma_f32_16x16x32_bf16 v[76:79], a[60:63], v[28:31], v[76:79]\n\t"
      "s_cselect_b64 vcc, s[90:91], 0\n\t"
      "v_mfma_f32_16x16x32_bf16 v[92:95], a[156:159], v[28:31], v[92:95]\n\t"
      "s_waitcnt lgkmcnt(7)\n\t"
      "v_mfma_f32_16x16x32_bf16 v[72:75], a[64:67], v[32:35], v[72:75]\n\t"
      "v_mfma_f32_16x16x32_bf16 v[76:79], a[68:71], v[32:35], v[76:79]\n\t"
      "buffer_load_dwordx4 v97, s[60:63], s72 offen offset:3072 sc1 lds\n\t"
      "v_mfma_f32_16x16x32_bf16 v[92:95], a[160:163], v[32:35], v[92:95]\n\t"
      "s_cmp_gt_u32 s68, 7\n\t"
      "s_waitcnt lgkmcnt(6)\n\t"
      "v_mfma_f32_16x16x32_bf16 v[72:75], a[72:75], v[36:39], v[72:75]\n\t"
      "v_mfma_f32_16x16x32_bf16 v[76:79], a[76:79], v[36:39], v[76:79]\n\t"
      "v_mfma_f32_16x16x32_bf16 v[92:95], a[164:167], v[36:39], v[92:95]\n\t"
      "s_waitcnt vmcnt(22) lgkmcnt(0)\n\t"
      "s_cbranch_scc1 GEF%=\n\t"
      "s_waitcnt vmcnt(9)\n\t"
      "GEF%=:\n\t"
      "s_barrier\n\t"
      "v_mfma_f32_16x16x32_bf16 v[72:75], a[80:83], v[40:43], v[72:75]\n\t"
      "ds_read_b128 v[0:3], v98\n\t"
      "v_mfma_f32_16x16x32_bf16 v[76:79], a[84:87], v[40:43], v[76:79]\n\t"
      "ds_read_b128 v[4:7], v98 offset:1024\n\t"
      "v_mfma_f32_16x16x32_bf16 v[92:95], a[168:171], v[40:43], v[92:95]\n\t"
      "v_mfma_f32_16x16x32_bf16 v[72:75], a[88:91], v[44:47], v[72:75]\n\t"
      "ds_read_b128 v[8:11], v98 offset:2048\n\t"
      "v_mfma_f32_16x16x32_bf16 v[76:79], a[92:95], v[44:47], v[76:79]\n\t"
      "ds_read_b128 v[12:15], v98 offset:3072\n\t"
      "v_mfma_f32_16x16x32_bf16 v[92:95], a[172:175], v[44:47], v[92:95]\n\t"
      "v_mfma_f32_16x16x32_bf16 v[72:75], a[96:99], v[48:51], v[72:75]\n\t"
      "ds_read_b128 v[16:19], v98 offset:4096\n\t"
      "v_mfma_f32_16x16x32_bf16 v[76:79], a[100:103], v[48:51], v[76:79]\n\t"
      "ds_read_b128 v[20:23], v98 offset:5120\n\t"
      "v_mfma_f32_16x16x32_bf16 v[92:95], a[176:179], v[48:51], v[92:95]\n\t"
      "v_mfma_f32_16x16x32_bf16 v[72:75], a[104:107], v[52:55], v[72:75]\n\t"
      "ds_read_b128 v[24:27], v98 offset:6144\n\t"
      "v_mfma_f32_16x16x32_bf16 v[76:79], a[108:111], v[52:55], v[76:79]\n\t"
      "ds_read_b128 v[28:31], v98 offset:7168\n\t"
      "v_mfma_f32_16x16x32_bf16 v[92:95], a[180:183], v[52:55], v[92:95]\n\t"
      "v_mfma_f32_16x16x32_bf16 v[72:75], a[112:115], v[56:59], v[72:75]\n\t"
      "ds_read_b128 v[32:35], v98 offset:8192\n\t"
      "v_mfma_f32_16x16x32_bf16 v[76:79], a[116:119], v[56:59], v[76:79]\n\t"
      "ds_read_b128 v[36:39], v98 offset:9216\n\t"
      "v_mfma_f32_16x16x32_bf16 v[92:95], a[184:187], v[56:59], v[92:95]\n\t"
      "s_cbranch_vccnz GFLAG%=\n\t"
      "GFB%=:\n\t"
      "v_mfma_f32_16x16x32_bf16 v[72:75], a[120:123], v[60:63], v[72:75]\n\t"
      "v_mfma_f32_16x16x32_bf16 v[76:79], a[124:127], v[60:63], v[76:79]\n\t"
      "v_mfma_f32_16x16x32_bf16 v[92:95], a[188:191], v[60:63], v[92:95]\n\t"
      "s_branch GU%=\n\t"
      "GFLAG%=:\n\t"
      "s_mov_b64 exec, 1\n\t"
      "buffer_store_dword v107, off, s[60:63], s75 sc1\n\t"
      "s_mov_b64 exec, -1\n\t"
      "s_branch GFB%=\n\t"
      "GCALL%=:\n\t"
      "s_setpc_b64 s[88:89]\n\t"
      "GJOBS%=:\n\t"
      "s_nop 7\n\t"
      "v_readlane_b32 s75, v103, 3\n\t"
      "s_waitcnt vmcnt(9)\n\t"
      "s_barrier\n\t"
      "s_mov_b64 exec, s[90:91]\n\t"
      "s_nop 1\n\t"
      "buffer_store_dword v107, off, s[60:63], s75 sc1\n\t"
      "s_mov_b64 exec, -1\n\t"
      "s_mov_b32 s71, 6\n\t"
      "s_getpc_b64 s[88:89]\n\t"
      "s_add_u32 s88, s88, 12\n\t"
      "s_addc_u32 s89, s89, 0\n\t"
      "s_branch GBACK%=\n\t"
      "s_bitcmp0_b32 %0, 1\n\t"
      "s_cselect_b64 s[36:37], -1, 0\n\t"
      "s_cmp_lt_u32 s82, 28\n\t"
      "s_cselect_b64 s[38:39], -1, 0\n\t"
      "s_min_u32 s74, s82, 27\n\t"
      "v_lshrrev_b32 v104, 3, v96\n\t"
      "v_and_b32 v105, 7, v96\n\t"
      "s_mov_b32 s78, 3584\n\t"
      "v_mul_u32_u24 v104, s78, v104\n\t"
      "v_lshl_add_u32 v104, v105, 4, v104\n\t"
      "s_lshr_b32 s78, %0, 5\n\t"
      "s_lshl_b32 s78, s78, 2\n\t"
      "s_add_u32 s78, s78, %1\n\t"
      "s_mul_i32 s78, s78, 114688\n\t"
      "s_lshl_b32 s80, s74, 7\n\t"
      "s_add_u32 s80, s80, s78\n\t"
      "s_mov_b64 s[64:65], s[50:51]\n\t"
      "s_add_u32 s78, s80, 0\n\t"
      "buffer_load_dwordx4 v[128:131], v104, s[64:67], s78 offen \n\t"
      "s_add_u32 s78, s80, 28672\n\t"
      "buffer_load_dwordx4 v[132:135], v104, s[64:67], s78 offen \n\t"
      "s_add_u32 s78, s80, 57344\n\t"
      "buffer_load_dwordx4 v[136:139], v104, s[64:67], s78 offen \n\t"
      "s_add_u32 s71, s71, 1\n\t"
      "s_getpc_b64 s[88:89]\n\t"
      "s_add_u32 s88, s88, 12\n\t"
      "s_addc_u32 s89, s89, 0\n\t"
      "s_branch GBACK%=\n\t"
      "s_add_u32 s78, s80, 86016\n\t"
      "buffer_load_dwordx4 v[140:143], v104, s[64:67], s78 offen \n\t"
      "s_mov_b64 s[64:65], s[52:53]\n\t"
      "s_add_u32 s78, s80, 0\n\t"
      "buffer_load_dwordx4 v[144:147], v104, s[64:67], s78 offen \n\t"
      "s_add_u32 s78, s80, 28672\n\t"
      "buffer_load_dwordx4 v[148:151], v104, s[64:67], s78 offen \n\t"
      "s_add_u32 s71, s71, 1\n\t"
      "s_getpc_b64 s[88:89]\n\t"
      "s_add_u32 s88, s88, 12\n\t"
      "s_addc_u32 s89, s89, 0\n\t"
      "s_branch GBACK%=\n\t"
      "s_add_u32 s78, s80, 57344\n\t"
      "buffer_load_dwordx4 v[152:155], v104, s[64:67], s78 offen \n\t"
      "s_add_u32 s78, s80, 86016\n\t"
      "buffer_load_dwordx4 v[156:159], v104, s[64:67], s78 offen \n\t"
      "v_mov_b32 v120, -1\n\t"
      "v_mov_b32 v121, -1\n\t"
      "v_mov_b32 v122, -1\n\t"
      "v_mov_b32 v123, -1\n\t"
      "s_lshr_b32 s78, %0, 5\n\t"
      "s_lshl_b32 s78, s78, 2\n\t"
      "s_add_u32 s78, s78, %1\n\t"
      "s_lshr_b32 s79, s78, 1\n\t"
      "s_lshl_b32 s79, s79, 16\n\t"
      "s_lshl_b32 s80, s82, 11\n\t"
      "s_add_u32 s79, s79, s80\n\t"
      "s_and_b32 s78, s78, 1\n\t"
      "s_lshl_b32 s78, s78, 10\n\t"
      "s_add_u32 s79, s79, s78\n\t"
      "s_add_u32 s79, s79, 66813952\n\t"
      "buffer_store_dwordx4 v[120:123], v97, s[60:63], s79 offen sc1\n\t"
      "s_add_u32 s71, s71, 1\n\t"
      "s_getpc_b64 s[88:89]\n\t"
      "s_add_u32 s88, s88, 12\n\t"
      "s_addc_u32 s89, s89, 0\n\t"
      "s_branch GBACK%=\n\t"
      "s_sub_u32 s79, s79, 4194304\n\t"
      "buffer_store_dwordx4 v[120:123], v97, s[60:63], s79 offen sc1\n\t"
      "s_add_u32 s79, s79, 3670016\n\t"
      "buffer_store_dwordx4 v[120:123], v97, s[60:63], s79 offen sc1\n\t"
      "s_sub_u32 s79, s79, 4194304\n\t"
      "buffer_store_dwordx4 v[120:123], v97, s[60:63], s79 offen sc1\n\t"
      "s_add_u32 s71, s71, 1\n\t"
      "s_getpc_b64 s[88:89]\n\t"
      "s_add_u32 s88, s88, 12\n\t"
      "s_addc_u32 s89, s89, 0\n\t"
      "s_branch GBACK%=\n\t"
      "s_add_u32 s79, s79, 3670016\n\t"
      "buffer_store_dwordx4 v[120:123], v97, s[60:63], s79 offen sc1\n\t"
      "s_sub_u32 s79, s79, 4194304\n\t"
      "buffer_store_dwordx4 v[120:123], v97, s[60:63], s79 offen sc1\n\t"
      "s_mov_b64 s[64:65], s[54:55]\n\t"
      "v_lshrrev_b32 v104, 5, v96\n\t"
      "v_and_b32 v105, 31, v96\n\t"
      "v_lshlrev_b32 v104, 10, v104\n\t"
      "v_lshl_add_u32 v104, v105, 4, v104\n\t"
      "s_andn2_b32 s78, %0, 3\n\t"
      "s_add_u32 s78, s78, %1\n\t"
      "s_mul_i32 s78, s78, 14336\n\t"
      "s_and_b32 s79, %0, 1\n\t"
      "s_lshl_b32 s79, s79, 9\n\t"
      "s_add_u32 s78, s78, s79\n\t"
      "s_mov_b64 exec, s[36:37]\n\t"
      "buffer_load_dwordx4 v[160:163], v104, s[64:67], s78 offen \n\t"
      "s_mov_b64 exec, -1\n\t"
      "s_add_u32 s71, s71, 1\n\t"
      "s_getpc_b64 s[88:89]\n\t"
      "s_add_u32 s88, s88, 12\n\t"
      "s_addc_u32 s89, s89, 0\n\t"
      "s_branch GBACK%=\n\t"
      "s_mov_b64 exec, s[36:37]\n\t"
      "buffer_load_dwordx4 v[164:167], v104, s[64:67], s78 offen offset:2048 \n\t"
      "s_add_u32 s78, s78, 4096\n\t"
      "buffer_load_dwordx4 v[168:171], v104, s[64:67], s78 offen \n\t"
      "buffer_load_dwordx4 v[172:175], v104, s[64:67], s78 offen offset:2048 \n\t"
      "s_mov_b64 exec, -1\n\t"
      "s_add_u32 s71, s71, 1\n\t"
      "s_getpc_b64 s[88:89]\n\t"
      "s_add_u32 s88, s88, 12\n\t"
      "s_addc_u32 s89, s89, 0\n\t"
      "s_branch GBACK%=\n\t"
      "s_mov_b64 exec, s[36:37]\n\t"
      "s_add_u32 s78, s78, 4096\n\t"
      "buffer_load_dwordx4 v[176:179], v104, s[64:67], s78 offen \n\t"
      "buffer_load_dwordx4 v[180:183], v104, s[64:67], s78 offen offset:2048 \n\t"
      "s_add_u32 s78, s78, 4096\n\t"
      "buffer_load_dwordx4 v[184:187], v104, s[64:67], s78 offen \n\t"
      "s_mov_b64 exec, -1\n\t"
      "s_add_u32 s71, s71, 1\n\t"
      "s_getpc_b64 s[88:89]\n\t"
      "s_add_u32 s88, s88, 12\n\t"
      "s_addc_u32 s89, s89, 0\n\t"
      "s_branch GBACK%=\n\t"
      "s_mov_b64 exec, s[36:37]\n\t"
      "s_mov_b64 exec, -1\n\t"
      "v_and_b32 v106, 3, v96\n\t"
      "v_mul_u32_u24 v106, 24, v106\n\t"
      "v_lshlrev_b32 v104, 1, v100\n\t"
      "v_sub_u32 v106, v104, v106\n\t"
      "v_add_u32 v104, s76, v97\n\t"
      "v_add_u32 v105, s76, v106\n\t"
      "s_waitcnt vmcnt(30) lgkmcnt(0)\n\t"
      "ds_write_b128 v104, v[128:131]\n\t"
      "ds_write_b128 v104, v[132:135] offset:1024\n\t"
      "ds_write_b128 v104, v[136:139] offset:2048\n\t"
      "ds_write_b128 v104, v[140:143] offset:3072\n\t"
      "ds_write_b128 v104, v[144:147] offset:4096\n\t"
      "ds_write_b128 v104, v[148:151] offset:5120\n\t"
      "ds_write_b128 v104, v[152:155] offset:6144\n\t"
      "ds_write_b128 v104, v[156:159] offset:7168\n\t"
      "s_lshr_b32 s78, %0, 5\n\t"
      "s_lshl_b32 s78, s78, 2\n\t"
      "s_add_u32 s78, s78, %1\n\t"
      "s_lshl_b32 s78, s78, 10\n\t"
      "s_lshl_b32 s79, s74, 17\n\t"
      "s_add_u32 s78, s78, s79\n\t"
      "s_add_u32 s78, s78, 105086976\n\t"
      "s_waitcnt lgkmcnt(6)\n\t"
      "ds_read_b64_tr_b16 v[128:129], v105\n\t"
      "ds_read_b64_tr_b16 v[130:131], v105 offset:512\n\t"
      "ds_read_b64_tr_b16 v[132:133], v105 offset:4096\n\t"
      "ds_read_b64_tr_b16 v[134:135], v105 offset:4608\n\t"
      "ds_read_b64_tr_b16 v[136:137], v105 offset:32\n\t"
      "ds_read_b64_tr_b16 v[138:139], v105 offset:544\n\t"
      "ds_read_b64_tr_b16 v[140:141], v105 offset:4128\n\t"
      "ds_read_b64_tr_b16 v[142:143], v105 offset:4640\n\t"
      "s_waitcnt lgkmcnt(6)\n\t"
      "ds_read_b64_tr_b16 v[144:145], v105 offset:64\n\t"
      "ds_read_b64_tr_b16 v[146:147], v105 offset:576\n\t"
      "ds_read_b64_tr_b16 v[148:149], v105 offset:4160\n\t"
      "ds_read_b64_tr_b16 v[150:151], v105 offset:4672\n\t"
      "ds_read_b64_tr_b16 v[152:153], v105 offset:96\n\t"
      "ds_read_b64_tr_b16 v[154:155], v105 offset:608\n\t"
      "ds_read_b64_tr_b16 v[156:157], v105 offset:4192\n\t"
      "ds_read_b64_tr_b16 v[158:159], v105 offset:4704\n\t"
      "s_waitcnt lgkmcnt(0)\n\t"
      "s_mov_b64 exec, s[38:39]\n\t"
      "buffer_store_dwordx4 v[128:131], v97, s[60:63], s78 offen sc1\n\t"
      "s_add_u32 s78, s78, 16384\n\t"
      "buffer_store_dwordx4 v[132:135], v97, s[60:63], s78 offen sc1\n\t"
      "s_add_u32 s78, s78, 16384\n\t"
      "buffer_store_dwordx4 v[136:139], v97, s[60:63], s78 offen sc1\n\t"
      "s_mov_b64 exec, -1\n\t"
      "s_add_u32 s71, s71, 1\n\t"
      "s_getpc_b64 s[88:89]\n\t"
      "s_add_u32 s88, s88, 12\n\t"
      "s_addc_u32 s89, s89, 0\n\t"
      "s_branch GBACK%=\n\t"
      "s_mov_b64 exec, s[38:39]\n\t"
      "s_add_u32 s78, s78, 16384\n\t"
      "buffer_store_dwordx4 v[140:143], v97, s[60:63], s78 offen sc1\n\t"
      "s_add_u32 s78, s78, 16384\n\t"
      "buffer_store_dwordx4 v[144:147], v97, s[60:63], s78 offen sc1\n\t"
      "s_add_u32 s78, s78, 16384\n\t"
      "buffer_store_dwordx4 v[148:151], v97, s[60:63], s78 offen sc1\n\t"
      "s_mov_b64 exec, -1\n\t"
      "s_add_u32 s71, s71, 1\n\t"
      "s_getpc_b64 s[88:89]\n\t"
      "s_add_u32 s88, s88, 12\n\t"
      "s_addc_u32 s89, s89, 0\n\t"
      "s_branch GBACK%=\n\t"
      "s_mov_b64 exec, s[38:39]\n\t"
      "s_add_u32 s78, s78, 16384\n\t"
      "buffer_store_dwordx4 v[152:155], v97, s[60:63], s78 offen sc1\n\t"
      "s_add_u32 s78, s78, 16384\n\t"
      "buffer_store_dwordx4 v[156:159], v97, s[60:63], s78 offen sc1\n\t"
      "s_add_u32 s78, s78, 16384\n\t"
      "s_mov_b64 exec, -1\n\t"
      "v_lshlrev_b32 v104, 10, v96\n\t"
      "s_and_b32 s78, %0, 1\n\t"
      "s_lshl_b32 s78, s78, 5\n\t"
      "s_lshr_b32 s79, %0, 2\n\t"
      "s_add_u32 s78, s78, s79\n\t"
      "s_lshl_b32 s78, s78, 4\n\t"
      "s_lshl_b32 s79, %1, 16\n\t"
      "s_add_u32 s78, s78, s79\n\t"
      "s_lshl_b32 s79, %1, 10\n\t"
      "s_mov_b64 exec, s[36:37]\n\t"
      "s_add_u32 m0, s79, 131072\n\t"
      "s_nop 0\n\t"
      "buffer_load_dwordx4 v104, s[64:67], s78 offen lds\n\t"
      "s_mov_b64 exec, -1\n\t"
      "s_add_u32 s71, s71, 1\n\t"
      "s_getpc_b64 s[88:89]\n\t"
      "s_add_u32 s88, s88, 12\n\t"
      "s_addc_u32 s89, s89, 0\n\t"
      "s_branch GBACK%=\n\t"
      "s_mov_b64 exec, s[36:37]\n\t"
      "s_add_u32 s78, s78, 0x40000\n\t"
      "s_add_u32 m0, s79, 135168\n\t"
      "s_nop 0\n\t"
      "buffer_load_dwordx4 v104, s[64:67], s78 offen lds\n\t"
      "s_add_u32 s78, s78, 0x40000\n\t"
      "s_add_u32 m0, s79, 139264\n\t"
      "s_nop 0\n\t"
      "buffer_load_dwordx4 v104, s[64:67], s78 offen lds\n\t"
      "s_add_u32 s78, s78, 0x40000\n\t"
      "s_add_u32 m0, s79, 143360\n\t"
      "s_nop 0\n\t"
      "buffer_load_dwordx4 v104, s[64:67], s78 offen lds\n\t"
      "s_mov_b64 exec, -1\n\t"
      "s_add_u32 s71, s71, 1\n\t"
      "s_getpc_b64 s[88:89]\n\t"
      "s_add_u32 s88, s88, 12\n\t"
      "s_addc_u32 s89, s89, 0\n\t"
      "s_branch GBACK%=\n\t"
      "s_mov_b64 exec, s[36:37]\n\t"
      "s_add_u32 s78, s78, 0x40000\n\t"
      "s_add_u32 m0, s79, 147456\n\t"
      "s_nop 0\n\t"
      "buffer_load_dwordx4 v104, s[64:67], s78 offen lds\n\t"
      "s_add_u32 s78, s78, 0x40000\n\t"
      "s_add_u32 m0, s79, 151552\n\t"
      "s_nop 0\n\t"
      "buffer_load_dwordx4 v104, s[64:67], s78 offen lds\n\t"
      "s_add_u32 s78, s78, 0x40000\n\t"
      "s_add_u32 m0, s79, 155648\n\t"
      "s_nop 0\n\t"
      "buffer_load_dwordx4 v104, s[64:67], s78 offen lds\n\t"
      "s_mov_b64 exec, -1\n\t"
      "s_add_u32 s71, s71, 1\n\t"
      "s_getpc_b64 s[88:89]\n\t"
      "s_add_u32 s88, s88, 12\n\t"
      "s_addc_u32 s89, s89, 0\n\t"
      "s_branch GBACK%=\n\t"
      "s_mov_b64 exec, s[36:37]\n\t"
      "s_add_u32 s78, s78, 0x40000\n\t"
      "s_mov_b64 exec, -1\n\t"
      "s_mov_b64 s[64:65], s[46:47]\n\t"
      "v_lshrrev_b32 v99, 2, v96\n\t"
      "v_and_b32 v106, 3, v96\n\t"
      "v_lshlrev_b32 v99, 10, v99\n\t"
      "v_lshl_add_u32 v99, v106, 4, v99\n\t"
      "s_lshr_b32 s78, %0, 4\n\t"
      "s_lshl_b32 s78, s78, 19\n\t"
      "s_bfe_u32 s79, %0, 0x20002\n\t"
      "s_lshl_b32 s79, s79, 8\n\t"
      "s_add_u32 s78, s78, s79\n\t"
      "s_lshl_b32 s79, %1, 6\n\t"
      "s_add_u32 s78, s78, s79\n\t"
      "v_add_u32 v99, s78, v99\n\t"
      "s_mov_b32 s78, 262144\n\t"
      "buffer_load_dwordx4 v[192:195], v99, s[64:67], s78 offen \n\t"
      "s_mov_b32 s78, 278528\n\t"
      "buffer_load_dwordx4 v[196:199], v99, s[64:67], s78 offen \n\t"
      "s_mov_b32 s78, 294912\n\t"
      "buffer_load_dwordx4 v[200:203], v99, s[64:67], s78 offen \n\t"
      "s_add_u32 s71, s71, 1\n\t"
      "s_getpc_b64 s[88:89]\n\t"
      "s_add_u32 s88, s88, 12\n\t"
      "s_addc_u32 s89, s89, 0\n\t"
      "s_branch GBACK%=\n\t"
      "s_mov_b32 s78, 311296\n\t"
      "buffer_load_dwordx4 v[204:207], v99, s[64:67], s78 offen \n\t"
      "s_mov_b32 s78, 327680\n\t"
      "buffer_load_dwordx4 v[208:211], v99, s[64:67], s78 offen \n\t"
      "s_mov_b32 s78, 344064\n\t"
      "buffer_load_dwordx4 v[212:215], v99, s[64:67], s78 offen \n\t"
      "s_add_u32 s71, s71, 1\n\t"
      "s_getpc_b64 s[88:89]\n\t"
      "s_add_u32 s88, s88, 12\n\t"
      "s_addc_u32 s89, s89, 0\n\t"
      "s_branch GBACK%=\n\t"
      "s_mov_b32 s78, 360448\n\t"
      "buffer_load_dwordx4 v[216:219], v99, s[64:67], s78 offen \n\t"
      "s_mov_b32 s78, 376832\n\t"
      "buffer_load_dwordx4 v[220:223], v99, s[64:67], s78 offen \n\t"
      "s_mov_b32 s78, 393216\n\t"
      "buffer_load_dwordx4 v[224:227], v99, s[64:67], s78 offen \n\t"
      "s_add_u32 s71, s71, 1\n\t"
      "s_getpc_b64 s[88:89]\n\t"
      "s_add_u32 s88, s88, 12\n\t"
      "s_addc_u32 s89, s89, 0\n\t"
      "s_branch GBACK%=\n\t"
      "s_mov_b32 s78, 409600\n\t"
      "buffer_load_dwordx4 v[228:231], v99, s[64:67], s78 offen \n\t"
      "s_mov_b32 s78, 425984\n\t"
      "buffer_load_dwordx4 v[232:235], v99, s[64:67], s78 offen \n\t"
      "s_mov_b32 s78, 442368\n\t"
      "buffer_load_dwordx4 v[236:239], v99, s[64:67], s78 offen \n\t"
      "s_add_u32 s71, s71, 1\n\t"
      "s_getpc_b64 s[88:89]\n\t"
      "s_add_u32 s88, s88, 12\n\t"
      "s_addc_u32 s89, s89, 0\n\t"
      "s_branch GBACK%=\n\t"
      "s_mov_b32 s78, 458752\n\t"
      "buffer_load_dwordx4 v[240:243], v99, s[64:67], s78 offen \n\t"
      "s_mov_b32 s78, 475136\n\t"
      "buffer_load_dwordx4 v[244:247], v99, s[64:67], s78 offen \n\t"
      "s_mov_b32 s78, 491520\n\t"
      "buffer_load_dwordx4 v[248:251], v99, s[64:67], s78 offen \n\t"
      "s_add_u32 s71, s71, 1\n\t"
      "s_getpc_b64 s[88:89]\n\t"
      "s_add_u32 s88, s88, 12\n\t"
      "s_addc_u32 s89, s89, 0\n\t"
      "s_branch GBACK%=\n\t"
      "s_mov_b32 s78, 507904\n\t"
      "buffer_load_dwordx4 v[252:255], v99, s[64:67], s78 offen \n\t"
      "s_mov_b32 s78, 0\n\t"
      "buffer_load_dwordx4 v[128:131], v99, s[64:67], s78 offen \n\t"
      "s_mov_b32 s78, 16384\n\t"
      "buffer_load_dwordx4 v[132:135], v99, s[64:67], s78 offen \n\t"
      "s_add_u32 s71, s71, 1\n\t"
      "s_getpc_b64 s[88:89]\n\t"
      "s_add_u32 s88, s88, 12\n\t"
      "s_addc_u32 s89, s89, 0\n\t"
      "s_branch GBACK%=\n\t"
      "s_mov_b32 s78, 32768\n\t"
      "buffer_load_dwordx4 v[136:139], v99, s[64:67], s78 offen \n\t"
      "s_mov_b32 s78, 49152\n\t"
      "buffer_load_dwordx4 v[140:143], v99, s[64:67], s78 offen \n\t"
      "s_mov_b32 s78, 65536\n\t"
      "buffer_load_dwordx4 v[144:147], v99, s[64:67], s78 offen \n\t"
      "s_add_u32 s71, s71, 1\n\t"
      "s_getpc_b64 s[88:89]\n\t"
      "s_add_u32 s88, s88, 12\n\t"
      "s_addc_u32 s89, s89, 0\n\t"
      "s_branch GBACK%=\n\t"
      "s_mov_b32 s78, 81920\n\t"
      "buffer_load_dwordx4 v[148:151], v99, s[64:67], s78 offen \n\t"
      "s_mov_b32 s78, 98304\n\t"
      "buffer_load_dwordx4 v[152:155], v99, s[64:67], s78 offen \n\t"
      "s_mov_b32 s78, 114688\n\t"
      "buffer_load_dwordx4 v[156:159], v99, s[64:67], s78 offen \n\t"
      "s_add_u32 s71, s71, 1\n\t"
      "s_getpc_b64 s[88:89]\n\t"
      "s_add_u32 s88, s88, 12\n\t"
      "s_addc_u32 s89, s89, 0\n\t"
      "s_branch GBACK%=\n\t"
      "s_mov_b32 s78, 131072\n\t"
      "buffer_load_dwordx4 v[160:163], v99, s[64:67], s78 offen \n\t"
      "s_mov_b32 s78, 147456\n\t"
      "buffer_load_dwordx4 v[164:167], v99, s[64:67], s78 offen \n\t"
      "s_mov_b32 s78, 163840\n\t"
      "buffer_load_dwordx4 v[168:171], v99, s[64:67], s78 offen \n\t"
      "s_add_u32 s71, s71, 1\n\t"
      "s_getpc_b64 s[88:89]\n\t"
      "s_add_u32 s88, s88, 12\n\t"
      "s_addc_u32 s89, s89, 0\n\t"
      "s_branch GBACK%=\n\t"
      "s_mov_b32 s78, 180224\n\t"
      "buffer_load_dwordx4 v[172:175], v99, s[64:67], s78 offen \n\t"
      "s_mov_b32 s78, 196608\n\t"
      "buffer_load_dwordx4 v[176:179], v99, s[64:67], s78 offen \n\t"
      "s_mov_b32 s78, 212992\n\t"
      "buffer_load_dwordx4 v[180:183], v99, s[64:67], s78 offen \n\t"
      "s_add_u32 s71, s71, 1\n\t"
      "s_getpc_b64 s[88:89]\n\t"
      "s_add_u32 s88, s88, 12\n\t"
      "s_addc_u32 s89, s89, 0\n\t"
      "s_branch GBACK%=\n\t"
      "s_mov_b32 s78, 229376\n\t"
      "buffer_load_dwordx4 v[184:187], v99, s[64:67], s78 offen \n\t"
      "s_mov_b32 s78, 245760\n\t"
      "buffer_load_dwordx4 v[188:191], v99, s[64:67], s78 offen \n\t"
      "s_mov_b32 s71, 32\n\t"
      "s_getpc_b64 s[88:89]\n\t"
      "s_add_u32 s88, s88, 12\n\t"
      "s_addc_u32 s89, s89, 0\n\t"
      "s_branch GBACK%=\n\t"
      "s_nop 7\n\t"
      "v_add_u32 v104, s76, v97\n\t"
      "v_add_u32 v105, s76, v100\n\t"
      "s_sub_u32 s68, s68, 1\n\t"
      "s_waitcnt vmcnt(11) lgkmcnt(0)\n\t"
      "v_cvt_pk_bf16_f32 v84, v72, v73\n\t"
      "v_cvt_pk_bf16_f32 v85, v74, v75\n\t"
      "v_cvt_pk_bf16_f32 v86, v76, v77\n\t"
      "v_cvt_pk_bf16_f32 v87, v78, v79\n\t"
      "buffer_store_dwordx4 v[84:87], v97, s[60:63], s73 offen offset:1024 sc1\n\t"
      "v_cvt_pk_bf16_f32 v114, v92, v93\n\t"
      "v_cvt_pk_bf16_f32 v115, v94, v95\n\t"
      "s_nop 0\n\t"
      "v_permlane16_swap_b32 v112, v114\n\t"
      "v_permlane16_swap_b32 v113, v115\n\t"
      "buffer_store_dwordx4 v[112:115], v111, s[60:63], s73 offen sc1\n\t"
      "v_readlane_b32 s75, v103, s68\n\t"
      "s_waitcnt lgkmcnt(6)\n\t"
      "ds_write_b128 v104, v[128:131]\n\t"
      "ds_write_b128 v104, v[132:135] offset:1024\n\t"
      "ds_read_b64_tr_b16 a[0:1], v105\n\t"
      "ds_read_b64_tr_b16 a[2:3], v105 offset:256\n\t"
      "ds_read_b64_tr_b16 a[64:65], v105 offset:8\n\t"
      "ds_read_b64_tr_b16 a[66:67], v105 offset:264\n\t"
      "s_waitcnt lgkmcnt(6)\n\t"
      "ds_write_b128 v104, v[136:139] offset:2048\n\t"
      "ds_write_b128 v104, v[140:143] offset:3072\n\t"
      "ds_read_b64_tr_b16 a[4:5], v105 offset:2048\n\t"
      "ds_read_b64_tr_b16 a[6:7], v105 offset:2304\n\t"
      "ds_read_b64_tr_b16 a[68:69], v105 offset:2056\n\t"
      "ds_read_b64_tr_b16 a[70:71], v105 offset:2312\n\t"
      "s_waitcnt lgkmcnt(6)\n\t"
      "ds_write_b128 v104, v[144:147] offset:4096\n\t"
      "ds_write_b128 v104, v[148:151] offset:5120\n\t"
      "ds_read_b64_tr_b16 a[8:9], v105 offset:4096\n\t"
      "ds_read_b64_tr_b16 a[10:11], v105 offset:4352\n\t"
      "ds_read_b64_tr_b16 a[72:73], v105 offset:4104\n\t"
      "ds_read_b64_tr_b16 a[74:75], v105 offset:4360\n\t"
      "s_waitcnt lgkmcnt(6)\n\t"
      "ds_write_b128 v104, v[152:155] offset:6144\n\t"
      "ds_write_b128 v104, v[156:159] offset:7168\n\t"
      "ds_read_b64_tr_b16 a[12:13], v105 offset:6144\n\t"
      "ds_read_b64_tr_b16 a[14:15], v105 offset:6400\n\t"
      "ds_read_b64_tr_b16 a[76:77], v105 offset:6152\n\t"
      "ds_read_b64_tr_b16 a[78:79], v105 offset:6408\n\t"
      "s_waitcnt lgkmcnt(6)\n\t"
      "ds_write_b128 v104, v[160:163]\n\t"
      "ds_write_b128 v104, v[164:167] offset:1024\n\t"
      "ds_read_b64_tr_b16 a[16:17], v105\n\t"
      "ds_read_b64_tr_b16 a[18:19], v105 offset:256\n\t"
      "ds_read_b64_tr_b16 a[80:81], v105 offset:8\n\t"
      "ds_read_b64_tr_b16 a[82:83], v105 offset:264\n\t"
      "s_waitcnt lgkmcnt(6)\n\t"
      "ds_write_b128 v104, v[168:171] offset:2048\n\t"
      "ds_write_b128 v104, v[172:175] offset:3072\n\t"
      "ds_read_b64_tr_b16 a[20:21], v105 offset:2048\n\t"
      "ds_read_b64_tr_b16 a[22:23], v105 offset:2304\n\t"
      "ds_read_b64_tr_b16 a[84:85], v105 offset:2056\n\t"
      "ds_read_b64_tr_b16 a[86:87], v105 offset:2312\n\t"
      "s_waitcnt lgkmcnt(6)\n\t"
      "ds_write_b128 v104, v[176:179] offset:4096\n\t"
      "ds_write_b128 v104, v[180:183] offset:5120\n\t"
      "ds_read_b64_tr_b16 a[24:25], v105 offset:4096\n\t"
      "ds_read_b64_tr_b16 a[26:27], v105 offset:4352\n\t"
      "ds_read_b64_tr_b16 a[88:89], v105 offset:4104\n\t"
      "ds_read_b64_tr_b16 a[90:91], v105 offset:4360\n\t"
      "s_waitcnt lgkmcnt(6)\n\t"
      "ds_write_b128 v104, v[184:187] offset:6144\n\t"
      "ds_write_b128 v104, v[188:191] offset:7168\n\t"
      "ds_read_b64_tr_b16 a[28:29], v105 offset:6144\n\t"
      "ds_read_b64_tr_b16 a[30:31], v105 offset:6400\n\t"
      "ds_read_b64_tr_b16 a[92:93], v105 offset:6152\n\t"
      "ds_read_b64_tr_b16 a[94:95], v105 offset:6408\n\t"
      "s_waitcnt lgkmcnt(6)\n\t"
      "ds_write_b128 v104, v[192:195]\n\t"
      "ds_write_b128 v104, v[196:199] offset:1024\n\t"
      "ds_read_b64_tr_b16 a[32:33], v105\n\t"
      "ds_read_b64_tr_b16 a[34:35], v105 offset:256\n\t"
      "ds_read_b64_tr_b16 a[96:97], v105 offset:8\n\t"
      "ds_read_b64_tr_b16 a[98:99], v105 offset:264\n\t"
      "s_waitcnt lgkmcnt(6)\n\t"
      "ds_write_b128 v104, v[200:203] offset:2048\n\t"
      "ds_write_b128 v104, v[204:207] offset:3072\n\t"
      "ds_read_b64_tr_b16 a[36:37], v105 offset:2048\n\t"
      "ds_read_b64_tr_b16 a[38:39], v105 offset:2304\n\t"
      "ds_read_b64_tr_b16 a[100:101], v105 offset:2056\n\t"
      "ds_read_b64_tr_b16 a[102:103], v105 offset:2312\n\t"
      "s_waitcnt lgkmcnt(6)\n\t"
      "ds_write_b128 v104, v[208:211] offset:4096\n\t"
      "ds_write_b128 v104, v[212:215] offset:5120\n\t"
      "ds_read_b64_tr_b16 a[40:41], v105 offset:4096\n\t"
      "ds_read_b64_tr_b16 a[42:43], v105 offset:4352\n\t"
      "ds_read_b64_tr_b16 a[104:105], v105 offset:4104\n\t"
      "ds_read_b64_tr_b16 a[106:107], v105 offset:4360\n\t"
      "s_waitcnt lgkmcnt(6)\n\t"
      "ds_write_b128 v104, v[216:219] offset:6144\n\t"
      "ds_write_b128 v104, v[220:223] offset:7168\n\t"
      "ds_read_b64_tr_b16 a[44:45], v105 offset:6144\n\t"
      "ds_read_b64_tr_b16 a[46:47], v105 offset:6400\n\t"
      "ds_read_b64_tr_b16 a[108:109], v105 offset:6152\n\t"
      "ds_read_b64_tr_b16 a[110:111], v105 offset:6408\n\t"
      "s_waitcnt lgkmcnt(6)\n\t"
      "ds_write_b128 v104, v[224:227]\n\t"
      "ds_write_b128 v104, v[228:231] offset:1024\n\t"
      "ds_read_b64_tr_b16 a[48:49], v105\n\t"
      "ds_read_b64_tr_b16 a[50:51], v105 offset:256\n\t"
      "ds_read_b64_tr_b16 a[112:113], v105 offset:8\n\t"
      "ds_read_b64_tr_b16 a[114:115], v105 offset:264\n\t"
      "s_waitcnt lgkmcnt(6)\n\t"
      "ds_write_b128 v104, v[232:235] offset:2048\n\t"
      "ds_write_b128 v104, v[236:239] offset:3072\n\t"
      "ds_read_b64_tr_b16 a[52:53], v105 offset:2048\n\t"
      "ds_read_b64_tr_b16 a[54:55], v105 offset:2304\n\t"
      "ds_read_b64_tr_b16 a[116:117], v105 offset:2056\n\t"
      "ds_read_b64_tr_b16 a[118:119], v105 offset:2312\n\t"
      "s_waitcnt lgkmcnt(6)\n\t"
      "ds_write_b128 v104, v[240:243] offset:4096\n\t"
      "ds_write_b128 v104, v[244:247] offset:5120\n\t"
      "ds_read_b64_tr_b16 a[56:57], v105 offset:4096\n\t"
      "ds_read_b64_tr_b16 a[58:59], v105 offset:4352\n\t"
      "ds_read_b64_tr_b16 a[120:121], v105 offset:4104\n\t"
      "ds_read_b64_tr_b16 a[122:123], v105 offset:4360\n\t"
      "s_waitcnt lgkmcnt(6)\n\t"
      "ds_write_b128 v104, v[248:251] offset:6144\n\t"
      "ds_write_b128 v104, v[252:255] offset:7168\n\t"
      "ds_read_b64_tr_b16 a[60:61], v105 offset:6144\n\t"
      "ds_read_b64_tr_b16 a[62:63], v105 offset:6400\n\t"
      "ds_read_b64_tr_b16 a[124:125], v105 offset:6152\n\t"
      "ds_read_b64_tr_b16 a[126:127], v105 offset:6408\n\t"
      "s_waitcnt vmcnt(0) lgkmcnt(0)\n\t"
      "s_barrier\n\t"
      "s_mov_b64 exec, s[90:91]\n\t"
      "buffer_store_dword v107, off, s[60:63], s75 sc1\n\t"
      "s_sub_u32 s75, s75, 32\n\t"
      "buffer_store_dword v107, off, s[60:63], s75 sc1\n\t"
      "s_sub_u32 s75, s75, 2016\n\t"
      "buffer_store_dword v107, off, s[60:63], s75 sc1\n\t"
      "s_mov_b64 exec, -1\n\t"
      : "+{s34}"(w), "+{s33}"(wv), "+{s[0:1]}"(A), "=&s"(tag), "={a[0:31]}"(wo.a), "={a[32:63]}"(wo.b), "={a[64:95]}"(wo.c), "={a[96:127]}"(wo.d)
      :
      : "memory", "vcc", "scc", "v0", "v1", "v2", "v3", "v4", "v5", "v6", "v7", "v8", "v9", "v10", "v11", "v12", "v13", "v14", "v15", "v16", "v17", "v18", "v19", "v20", "v21", "v22", "v23", "v24", "v25", "v26", "v27", "v28", "v29", "v30", "v31", "v32", "v33", "v34", "v35", "v36", "v37", "v38", "v39", "v40", "v41", "v42", "v43", "v44", "v45", "v46", "v47", "v48", "v49", "v50", "v51", "v52", "v53", "v54", "v55", "v56", "v57", "v58", "v59", "v60", "v61", "v62", "v63", "v64", "v65", "v66", "v67", "v68", "v69", "v70", "v71", "v72", "v73", "v74", "v75", "v76", "v77", "v78", "v79", "v80", "v81", "v82", "v83", "v84", "v85", "v86", "v87", "v88", "v89", "v90", "v91", "v92", "v93", "v94", "v95", "v96", "v97", "v98", "v99", "v100", "v101", "v102", "v103", "v104", "v105", "v106", "v107", "v108", "v109", "v110", "v111", "v112", "v113", "v114", "v115", "v116", "v117", "v118", "v119", "v120", "v121", "v122", "v123", "v124", "v125", "v126", "v127", "v128", "v129", "v130", "v131", "v132", "v133", "v134", "v135", "v136", "v137", "v138", "v139", "v140", "v141", "v142", "v143", "v144", "v145", "v146", "v147", "v148", "v149", "v150", "v151", "v152", "v153", "v154", "v155", "v156", "v157", "v158", "v159", "v160", "v161", "v162", "v163", "v164", "v165", "v166", "v167", "v168", "v169", "v170", "v171", "v172", "v173", "v174", "v175", "v176", "v177", "v178", "v179", "v180", "v181", "v182", "v183", "v184", "v185", "v186", "v187", "v188", "v189", "v190", "v191", "v192", "v193", "v194", "v195", "v196", "v197", "v198", "v199", "v200", "v201", "v202", "v203", "v204", "v205", "v206", "v207", "v208", "v209", "v210", "v211", "v212", "v213", "v214", "v215", "v216", "v217", "v218", "v219", "v220", "v221", "v222", "v223", "v224", "v225", "v226", "v227", "v228", "v229", "v230", "v231", "v232", "v233", "v234", "v235", "v236", "v237", "v238", "v239", "v240", "v241", "v242", "v243", "v244", "v245", "v246", "v247", "v248", "v249", "v250", "v251", "v252", "v253", "v254", "v255", "a128", "a129", "a130", "a131", "a132", "a133", "a134", "a135", "a136", "a137", "a138", "a139", "a140", "a141", "a142", "a143", "a144", "a145", "a146", "a147", "a148", "a149", "a150", "a151", "a152", "a153", "a154", "a155", "a156", "a157", "a158", "a159", "a160", "a161", "a162", "a163", "a164", "a165", "a166", "a167", "a168", "a169", "a170", "a171", "a172", "a173", "a174", "a175", "a176", "a177", "a178", "a179", "a180", "a181", "a182", "a183", "a184", "a185", "a186", "a187", "a188", "a189", "a190", "a191", "a192", "a193", "a194", "a195", "a196", "a197", "a198", "a199", "a200", "a201", "a202", "a203", "a204", "a205", "a206", "a207", "a208", "a209", "a210", "a211", "a212", "a213", "a214", "a215", "a216", "a217", "a218", "a219", "a220", "a221", "a222", "a223", "a224", "a225", "a226", "a227", "a228", "a229", "a230", "a231", "a232", "a233", "a234", "a235", "a236", "a237", "a238", "a239", "a240", "a241", "a242", "a243", "a244", "a245", "a246", "a247", "a248", "a249", "a250", "a251", "a252", "a253", "a254", "a255", "s36", "s37", "s38", "s39", "s40", "s41", "s42", "s43", "s44", "s45", "s46", "s47", "s48", "s49", "s50", "s51", "s52", "s53", "s54", "s55", "s56", "s57", "s58", "s59", "s60", "s61", "s62", "s63", "s64", "s65", "s66", "s67", "s68", "s69", "s70", "s71", "s72", "s73", "s74", "s75", "s76", "s77", "s78", "s79", "s80", "s81", "s82", "s83", "s84", "s85", "s86", "s87", "s88", "s89", "s90", "s91", "s92", "s93", "s94", "s95");
  return tag;
}

__device__ __forceinline__ void worker_tasks(char* lds, int& w, int& wv, KArgs& A, unsigned tag, WoRegs& wo) {
  asm volatile("" : "+s"(w), "+s"(wv), "+s"(A));
  const int lane = fresh_lane(), l15 = lane & 15, l4 = lane >> 4;
  const int team = w & 3, j = w >> 2, g = j >> 2, c = j & 3, h = g * 4 + wv;
  const u64 wsa = (u64)A->ws, xa = (u64)A->x, oa = (u64)A->out;
  u32x16 c0 = {(unsigned)wsa, (unsigned)(wsa >> 32) & 0xffffu, WS_BYTES, 0x00020000u, (unsigned)xa, (unsigned)(xa >> 32) & 0xffffu, S_ * DM * 2, 0x00020000u,
               (unsigned)oa, (unsigned)(oa >> 32) & 0xffffu, S_ * DM * 2, 0x00020000u, O_STAMPS + (310000 + j * 4 + wv) * 8, (unsigned)(l15 - l4 * 4),
               (unsigned)(team + 4 * wv + 48), (unsigned)j};
  const int r0 = 0 ? h : j;
  u32x16 c1 = {(unsigned)team, (unsigned)(team + 4 * (0 ? c : wv)), O_QKV + (team * 32 + h) * 24576, O_KVM + (team * 32 + h) * 2048, O_VTM + (team * 32 + h) * 2048,
               O_PART + (team * 8 + g) * 32768, O_PART + r0 * 1024, O_PFLAG + team * 16384, O_KVSTEP + h * 64, O_RBUF, O_LNH, O_HBUF, (unsigned)r0,
               (unsigned)wv * 8192, tag, O_PFLAG + team * 16384 + (j * 4 + wv) * 128};
  u32x8 c4 = {(unsigned)lane, (unsigned)lane, (unsigned)lane, (unsigned)lane, O_TFLAG + (6 * 32 + h) * 64 + 32 + (l15 & 1) * 16, O_TFLAG + (15 * 32 + h) * 64 + 32, O_WM + lane * 256 + 4, (unsigned)lane};
  const int rc = 8 * (l15 + 16 * (l4 >> 1)) + 4 * (l4 & 1);
  const u32x2 w0 = *(const u32x2*)(A->w_r + rc), w1 = *(const u32x2*)(A->w_r + 256 + rc);
  u32x16 c2 = {w0[0] << 16, w0[0] & 0xffff0000u, w0[1] << 16, w0[1] & 0xffff0000u, w1[0] << 16, w1[0] & 0xffff0000u, w1[1] << 16, w1[1] & 0xffff0000u,
               (unsigned)lane * 16, (unsigned)rc * 2, (unsigned)(lane ^ ((l4 & 1) * 8)) * 16,
               (unsigned)(wv * 8192 + (l15 & 3) * 256 + (((l4 * 4 + (l15 >> 2)) ^ ((l15 & 1) * 8)) << 4)),
               (unsigned)(l4 * 128 + (l15 & 7) * 16), (unsigned)(lane & 31) * 16,
               (unsigned)(65536 + wv * 8192 + lane * 16), (unsigned)(65536 + lane * 16)};
  unsigned c3 = (unsigned)(l15 * DM + c * 128 + wv * 32 + l4 * 8) * 2;
  asm volatile(
      ".p2align 5\n\t"
      "s_nop 1\n\t"
      "v_readfirstlane_b32 s36, v0\n\t"
      "v_readfirstlane_b32 s37, v1\n\t"
      "v_readfirstlane_b32 s38, v2\n\t"
      "v_readfirstlane_b32 s39, v3\n\t"
      "v_readfirstlane_b32 s40, v4\n\t"
      "v_readfirstlane_b32 s41, v5\n\t"
      "v_readfirstlane_b32 s42, v6\n\t"
      "v_readfirstlane_b32 s43, v7\n\t"
      "v_readfirstlane_b32 s44, v8\n\t"
      "v_readfirstlane_b32 s45, v9\n\t"
      "v_readfirstlane_b32 s46, v10\n\t"
      "v_readfirstlane_b32 s47, v11\n\t"
      "v_readfirstlane_b32 s48, v16\n\t"
      "v_readfirstlane_b32 s49, v17\n\t"
      "v_readfirstlane_b32 s50, v18\n\t"
      "v_readfirstlane_b32 s51, v19\n\t"
      "v_readfirstlane_b32 s52, v20\n\t"
      "v_readfirstlane_b32 s53, v21\n\t"
      "v_readfirstlane_b32 s54, v22\n\t"
      "v_readfirstlane_b32 s55, v23\n\t"
      "v_readfirstlane_b32 s56, v24\n\t"
      "v_readfirstlane_b32 s57, v25\n\t"
      "v_readfirstlane_b32 s58, v26\n\t"
      "v_readfirstlane_b32 s59, v27\n\t"
      "v_readfirstlane_b32 s60, v28\n\t"
      "v_readfirstlane_b32 s61, v29\n\t"
      "v_readfirstlane_b32 s62, v30\n\t"
      "v_readfirstlane_b32 s63, v31\n\t"
      "v_readfirstlane_b32 s75, v12\n\t"
      "v_readfirstlane_b32 s93, v14\n\t"
      "v_readfirstlane_b32 s94, v15\n\t"
      "v_cmp_ge_i32 s[76:77], v13, 0\n\t"
      "v_cmp_ge_i32 s[78:79], v13, 1\n\t"
      "v_cmp_ge_i32 s[80:81], v13, 2\n\t"
      "v_cmp_ge_i32 s[82:83], v13, 3\n\t"
      "s_nop 4\n\t"
      "v_readfirstlane_b32 s99, v37\n\t"
      "s_add_u32 s98, s53, 12582912\n\t"
      "v_lshrrev_b32 v40, 4, v248\n\t"
      "v_and_b32 v41, 15, v40\n\t"
      "v_lshrrev_b32 v40, 1, v40\n\t"
      "v_and_b32 v40, 24, v40\n\t"
      "v_sub_u32 v41, v41, v40\n\t"
      "s_mov_b32 s69, 0x3f80\n\t"
      "s_mov_b32 s70, 0x3f800000\n\t"
      "v_mov_b32 v42, 0\n\t"
      "v_cmp_eq_i32 vcc, 0, v41\n\t"
      "v_mov_b32 v43, s69\n\t"
      "s_nop 0\n\t"
      "v_cndmask_b32 v42, v42, v43\n\t"
      "v_cmp_eq_i32 vcc, 1, v41\n\t"
      "v_mov_b32 v43, s70\n\t"
      "s_nop 0\n\t"
      "v_cndmask_b32 v42, v42, v43\n\t"
      "v_accvgpr_write_b32 a224, v42\n\t"
      "v_mov_b32 v42, 0\n\t"
      "v_cmp_eq_i32 vcc, 2, v41\n\t"
      "v_mov_b32 v43, s69\n\t"
      "s_nop 0\n\t"
      "v_cndmask_b32 v42, v42, v43\n\t"
      "v_cmp_eq_i32 vcc, 3, v41\n\t"
      "v_mov_b32 v43, s70\n\t"
      "s_nop 0\n\t"
      "v_cndmask_b32 v42, v42, v43\n\t"
      "v_accvgpr_write_b32 a225, v42\n\t"
      "v_mov_b32 v42, 0\n\t"
      "v_cmp_eq_i32 vcc, 4, v41\n\t"
      "v_mov_b32 v43, s69\n\t"
      "s_nop 0\n\t"
      "v_cndmask_b32 v42, v42, v43\n\t"
      "v_cmp_eq_i32 vcc, 5, v41\n\t"
      "v_mov_b32 v43, s70\n\t"
      "s_nop 0\n\t"
      "v_cndmask_b32 v42, v42, v43\n\t"
      "v_accvgpr_write_b32 a226, v42\n\t"
      "v_mov_b32 v42, 0\n\t"
      "v_cmp_eq_i32 vcc, 6, v41\n\t"
      "v_mov_b32 v43, s69\n\t"
      "s_nop 0\n\t"
      "v_cndmask_b32 v42, v42, v43\n\t"
      "v_cmp_eq_i32 vcc, 7, v41\n\t"
      "v_mov_b32 v43, s70\n\t"
      "s_nop 0\n\t"
      "v_cndmask_b32 v42, v42, v43\n\t"
      "v_accvgpr_write_b32 a227, v42\n\t"
      "v_mov_b32 v42, 0\n\t"
      "v_cmp_eq_i32 vcc, -16, v41\n\t"
      "v_mov_b32 v43, s69\n\t"
      "s_nop 0\n\t"
      "v_cndmask_b32 v42, v42, v43\n\t"
      "v_cmp_eq_i32 vcc, -15, v41\n\t"
      "v_mov_b32 v43, s70\n\t"
      "s_nop 0\n\t"
      "v_cndmask_b32 v42, v42, v43\n\t"
      "v_accvgpr_write_b32 a228, v42\n\t"
      "v_mov_b32 v42, 0\n\t"
      "v_cmp_eq_i32 vcc, -14, v41\n\t"
      "v_mov_b32 v43, s69\n\t"
      "s_nop 0\n\t"
      "v_cndmask_b32 v42, v42, v43\n\t"
      "v_cmp_eq_i32 vcc, -13, v41\n\t"
      "v_mov_b32 v43, s70\n\t"
      "s_nop 0\n\t"
      "v_cndmask_b32 v42, v42, v43\n\t"
      "v_accvgpr_write_b32 a229, v42\n\t"
      "v_mov_b32 v42, 0\n\t"
      "v_cmp_eq_i32 vcc, -12, v41\n\t"
      "v_mov_b32 v43, s69\n\t"
      "s_nop 0\n\t"
      "v_cndmask_b32 v42, v42, v43\n\t"
      "v_cmp_eq_i32 vcc, -11, v41\n\t"
      "v_mov_b32 v43, s70\n\t"
      "s_nop 0\n\t"
      "v_cndmask_b32 v42, v42, v43\n\t"
      "v_accvgpr_write_b32 a230, v42\n\t"
      "v_mov_b32 v42, 0\n\t"
      "v_cmp_eq_i32 vcc, -10, v41\n\t"
      "v_mov_b32 v43, s69\n\t"
      "s_nop 0\n\t"
      "v_cndmask_b32 v42, v42, v43\n\t"
      "v_cmp_eq_i32 vcc, -9, v41\n\t"
      "v_mov_b32 v43, s70\n\t"
      "s_nop 0\n\t"
      "v_cndmask_b32 v42, v42, v43\n\t"
      "v_accvgpr_write_b32 a231, v42\n\t"
      "s_mov_b32 s72, 0x40000\n\t"
      "WE0%=:\n\t"
      "buffer_load_dwordx4 v[40:43], v36, s[36:39], 0 offen sc1\n\t"
      "buffer_load_dword v48, v38, s[36:39], 0 offen sc1\n\t"
      "buffer_load_dword v49, v38, s[36:39], 0 offen offset:128 sc1\n\t"
      "s_waitcnt vmcnt(0)\n\t"
      "v_bitop3_b32 v40, v40, v41, s62 bitop3:0x7e\n\t"
      "v_bitop3_b32 v48, v48, v49, s62 bitop3:0x7e\n\t"
      "v_or_b32 v40, v40, v48\n\t"
      "v_cmp_ne_u32 vcc, 0, v40\n\t"
      "s_nop 0\n\t"
      "s_cbranch_vccz WE1%=\n\t"
      "s_sleep 8\n\t"
      "s_sub_u32 s72, s72, 1\n\t"
      "s_cmp_lg_u32 s72, 0\n\t"
      "s_cbranch_scc1 WE0%=\n\t"
      "WE1%=:\n\t"
      "buffer_load_dwordx4 a[128:131], v248, s[36:39], s50 offen  sc1\n\t"
      "buffer_load_dwordx4 a[132:135], v248, s[36:39], s50 offen offset:1024  sc1\n\t"
      "buffer_load_dwordx4 a[136:139], v248, s[36:39], s50 offen offset:2048  sc1\n\t"
      "buffer_load_dwordx4 a[140:143], v248, s[36:39], s50 offen offset:3072  sc1\n\t"
      "s_add_u32 s69, s50, 4096\n\t"
      "buffer_load_dwordx4 a[144:147], v248, s[36:39], s69 offen  sc1\n\t"
      "buffer_load_dwordx4 a[148:151], v248, s[36:39], s69 offen offset:1024  sc1\n\t"
      "buffer_load_dwordx4 a[152:155], v248, s[36:39], s69 offen offset:2048  sc1\n\t"
      "buffer_load_dwordx4 a[156:159], v248, s[36:39], s69 offen offset:3072  sc1\n\t"
      "s_add_u32 s69, s50, 8192\n\t"
      "s_add_u32 m0, s61, 0\n\t"
      "s_nop 0\n\t"
      "buffer_load_dwordx4 v250, s[36:39], s69 offen sc1 lds\n\t"
      "buffer_load_dwordx4 v250, s[36:39], s69 offen offset:1024 sc1 lds\n\t"
      "buffer_load_dwordx4 v250, s[36:39], s69 offen offset:2048 sc1 lds\n\t"
      "buffer_load_dwordx4 v250, s[36:39], s69 offen offset:3072 sc1 lds\n\t"
      "s_add_u32 s69, s50, 12288\n\t"
      "s_add_u32 m0, s61, 4096\n\t"
      "s_nop 0\n\t"
      "buffer_load_dwordx4 v250, s[36:39], s69 offen sc1 lds\n\t"
      "buffer_load_dwordx4 v250, s[36:39], s69 offen offset:1024 sc1 lds\n\t"
      "buffer_load_dwordx4 v250, s[36:39], s69 offen offset:2048 sc1 lds\n\t"
      "buffer_load_dwordx4 v250, s[36:39], s69 offen offset:3072 sc1 lds\n\t"
      "s_add_u32 s69, s50, 16384\n\t"
      "buffer_load_dwordx4 a[160:163], v248, s[36:39], s69 offen  sc1\n\t"
      "buffer_load_dwordx4 a[164:167], v248, s[36:39], s69 offen offset:1024  sc1\n\t"
      "buffer_load_dwordx4 a[168:171], v248, s[36:39], s69 offen offset:2048  sc1\n\t"
      "buffer_load_dwordx4 a[172:175], v248, s[36:39], s69 offen offset:3072  sc1\n\t"
      "s_add_u32 s69, s50, 20480\n\t"
      "buffer_load_dwordx4 a[176:179], v248, s[36:39], s69 offen  sc1\n\t"
      "buffer_load_dwordx4 a[180:183], v248, s[36:39], s69 offen offset:1024  sc1\n\t"
      "buffer_load_dwordx4 a[184:187], v248, s[36:39], s69 offen offset:2048  sc1\n\t"
      "buffer_load_dwordx4 a[188:191], v248, s[36:39], s69 offen offset:3072  sc1\n\t"
      "s_add_u32 s50, s50, 3145728\n\t"
      "s_mov_b32 s64, 0\n\t"
      "s_mov_b32 s65, 0\n\t"
      "s_mov_b32 s66, 0\n\t"
      "s_mov_b32 s96, 0\n\t"
      "s_mov_b32 s97, 0\n\t"
      "s_add_u32 s89, s49, 16\n\t"
      "s_sub_u32 s95, s94, s60\n\t"
      "s_lshl_b32 s95, s95, 10\n\t"
      "s_add_u32 s95, s95, s54\n\t"
      "s_mov_b32 s67, -1\n\t"
      "s_mov_b32 s74, 0xffff0000\n\t"
      "s_mov_b32 s84, 0x3db504f3\n\t"
      "s_mov_b32 s86, 0x3e0293ee\n\t"
      "s_mov_b32 s85, 0x3db504f3\n\t"
      "s_mov_b32 s87, 0x3e0293ee\n\t"
      "v_mov_b32 v239, 0xff61b1e6\n\t"
      "s_waitcnt vmcnt(0) lgkmcnt(0)\n\t"
      "s_branch WA%=\n\t"
      ".p2align 5\n\t"
      "WTOP%=:\n\t"
      "s_cmp_ge_u32 s48, 64\n\t"
      "s_cbranch_scc1 WFIN%=\n\t"
      "s_lshr_b32 s69, s48, 2\n\t"
      "s_cmp_eq_u32 s69, 5\n\t"
      "s_cbranch_scc1 WH2%=\n\t"
      "WT0%=:\n\t"
      "s_waitcnt vmcnt(17)\n\t"
      "s_cmp_eq_u32 s24, 9\n\t"
      "s_cbranch_scc1 WT1%=\n\t"
      "s_waitcnt vmcnt(10)\n\t"
      "s_cmp_lg_u32 s97, 0\n\t"
      "s_cbranch_scc1 WT1%=\n\t"
      "s_waitcnt vmcnt(9)\n\t"
      "s_cmp_eq_u32 s24, 8\n\t"
      "s_cbranch_scc1 WT1%=\n\t"
      "s_waitcnt vmcnt(2)\n\t"
      "WT1%=:\n\t"
      "v_min_u32 v228, v228, v229\n\t"
      "v_min3_u32 v228, v228, v230, v231\n\t"
      "v_min_u32 v232, v232, v233\n\t"
      "s_cmp_lg_u32 s65, 0\n\t"
      "s_cselect_b32 s70, s89, s49\n\t"
      "v_readfirstlane_b32 s88, v228\n\t"
      "s_memrealtime s[90:91]\n\t"
      "v_cmp_ge_u32 vcc, s70, v232\n\t"
      "s_lshr_b32 s69, s88, 16\n\t"
      "s_cmp_gt_u32 s69, s48\n\t"
      "s_cselect_b32 s64, 1, 0\n\t"
      "s_cmp_eq_u64 vcc, 0\n\t"
      "s_cselect_b32 s66, 1, 0\n\t"
      "s_cmp_eq_u32 s65, 2\n\t"
      "s_cselect_b32 s66, 0, s66\n\t"
      "s_add_u32 s70, s48, 4\n\t"
      "s_cmp_gt_u32 s69, s70\n\t"
      "s_cselect_b32 s96, 1, 0\n\t"
      "s_or_b32 s64, s64, s97\n\t"
      "s_cmp_lg_u32 s97, 0\n\t"
      "s_cbranch_scc1 WA%=\n\t"
      "s_cmp_lg_u32 s64, 0\n\t"
      "s_cbranch_scc1 WT2%=\n\t"
      "s_sub_u32 s69, s48, s69\n\t"
      "s_mul_i32 s69, s69, 170\n\t"
      "s_waitcnt lgkmcnt(0)\n\t"
      "s_sub_u32 s70, s90, s88\n\t"
      "s_add_u32 s70, s70, 200\n\t"
      "s_and_b32 s70, s70, 0xffff\n\t"
      "s_add_u32 s69, s69, 60\n\t"
      "s_cmp_ge_u32 s70, s69\n\t"
      "s_cselect_b32 s69, s48, 0\n\t"
      "s_cmp_ge_u32 s69, 40\n\t"
      "s_cselect_b32 s64, 2, 0\n\t"
      "s_cmp_eq_u32 s64, 0\n\t"
      "s_cbranch_scc1 WA%=\n\t"
      "WT2%=:\n\t"
      "buffer_load_dwordx4 v[128:131], v252, s[36:39], s51 offen sc1\n\t"
      "buffer_load_dwordx4 v[132:135], v252, s[36:39], s51 offen offset:512 sc1\n\t"
      "buffer_load_dwordx4 v[136:139], v252, s[36:39], s51 offen offset:1024 sc1\n\t"
      "buffer_load_dwordx4 v[140:143], v252, s[36:39], s51 offen offset:1536 sc1\n\t"
      "buffer_load_dwordx4 v[144:147], v253, s[36:39], s52 offen sc1\n\t"
      "buffer_load_dwordx4 v[148:151], v253, s[36:39], s52 offen offset:512 sc1\n\t"
      "buffer_load_dwordx4 v[152:155], v253, s[36:39], s52 offen offset:1024 sc1\n\t"
      "buffer_load_dwordx4 v[156:159], v253, s[36:39], s52 offen offset:1536 sc1\n\t"
      "WA%=:\n\t"
      "v_mfma_f32_16x16x32_bf16 v[0:3], a[128:131], a[160:163], 0\n\t"
      "ds_read_b64_tr_b16 a[192:193], v251\n\t"
      "ds_read_b64_tr_b16 a[194:195], v251 offset:1024\n\t"
      "v_mfma_f32_16x16x32_bf16 v[4:7], a[128:131], a[164:167], 0\n\t"
      "ds_read_b64_tr_b16 a[196:197], v251 offset:8\n\t"
      "ds_read_b64_tr_b16 a[198:199], v251 offset:1032\n\t"
      "v_mfma_f32_16x16x32_bf16 v[8:11], a[132:135], a[164:167], 0\n\t"
      "ds_read_b64_tr_b16 a[200:201], v251 offset:2048\n\t"
      "ds_read_b64_tr_b16 a[202:203], v251 offset:3072\n\t"
      "v_mfma_f32_16x16x32_bf16 v[0:3], a[136:139], a[168:171], v[0:3]\n\t"
      "ds_read_b64_tr_b16 a[204:205], v251 offset:2056\n\t"
      "ds_read_b64_tr_b16 a[206:207], v251 offset:3080\n\t"
      "v_mfma_f32_16x16x32_bf16 v[4:7], a[136:139], a[172:175], v[4:7]\n\t"
      "v_mfma_f32_16x16x32_bf16 v[8:11], a[140:143], a[172:175], v[8:11]\n\t"
      "s_waitcnt lgkmcnt(0)\n\t"
      "ds_read_b64_tr_b16 a[208:209], v251 offset:4096\n\t"
      "ds_read_b64_tr_b16 a[210:211], v251 offset:5120\n\t"
      "ds_read_b64_tr_b16 a[212:213], v251 offset:4104\n\t"
      "ds_read_b64_tr_b16 a[214:215], v251 offset:5128\n\t"
      "ds_read_b64_tr_b16 a[216:217], v251 offset:6144\n\t"
      "ds_read_b64_tr_b16 a[218:219], v251 offset:7168\n\t"
      "ds_read_b64_tr_b16 a[220:221], v251 offset:6152\n\t"
      "ds_read_b64_tr_b16 a[222:223], v251 offset:7176\n\t"
      "v_mfma_f32_16x16x32_bf16 v[0:3], a[144:147], a[176:179], v[0:3]\n\t"
      "v_mfma_f32_16x16x32_bf16 v[4:7], a[144:147], a[180:183], v[4:7]\n\t"
      "v_mfma_f32_16x16x32_bf16 v[8:11], a[148:151], a[180:183], v[8:11]\n\t"
      "v_mfma_f32_16x16x32_bf16 v[0:3], a[152:155], a[184:187], v[0:3]\n\t"
      "v_mfma_f32_16x16x32_bf16 v[4:7], a[152:155], a[188:191], v[4:7]\n\t"
      "v_mfma_f32_16x16x32_bf16 v[8:11], a[156:159], a[188:191], v[8:11]\n\t"
      "s_waitcnt lgkmcnt(0)\n\t"
      "buffer_load_dwordx4 a[128:131], v248, s[36:39], s50 offen  sc1\n\t"
      "s_nop 3\n\t"
      "v_cndmask_b32_e64 v0, v239, v0, s[76:77]\n\t"
      "s_nop 0\n\t"
      "v_cndmask_b32_e64 v8, v239, v8, s[76:77]\n\t"
      "v_cndmask_b32_e64 v1, v239, v1, s[78:79]\n\t"
      "v_cndmask_b32_e64 v9, v239, v9, s[78:79]\n\t"
      "v_cndmask_b32_e64 v2, v239, v2, s[80:81]\n\t"
      "v_cndmask_b32_e64 v10, v239, v10, s[80:81]\n\t"
      "v_cndmask_b32_e64 v3, v239, v3, s[82:83]\n\t"
      "v_cndmask_b32_e64 v11, v239, v11, s[82:83]\n\t"
      "v_max3_f32 v234, v0, v1, v2\n\t"
      "v_max3_f32 v235, v4, v5, v6\n\t"
      "v_max_f32 v234, v234, v3\n\t"
      "v_max3_f32 v235, v235, v7, v8\n\t"
      "v_max3_f32 v235, v235, v9, v10\n\t"
      "v_max_f32 v235, v235, v11\n\t"
      "s_nop 1\n\t"
      "v_permlane16_swap_b32 v234, v235\n\t"
      "v_max_f32 v234, v234, v235\n\t"
      "buffer_load_dwordx4 a[132:135], v248, s[36:39], s50 offen offset:1024  sc1\n\t"
      "v_mov_b32 v235, v234\n\t"
      "s_nop 1\n\t"
      "v_permlane32_swap_b32 v234, v235\n\t"
      "v_max_f32 v234, v234, v235\n\t"
      "v_mov_b32 v235, v234\n\t"
      "s_nop 1\n\t"
      "v_permlane16_swap_b32 v234, v235\n\t"
      "v_pk_mul_f32 v[12:13], v[234:235], s[86:87]\n\t"
      "v_pk_fma_f32 v[0:1], v[0:1], s[86:87], v[12:13] op_sel:[0,0,0] op_sel_hi:[1,1,0] neg_lo:[0,0,1] neg_hi:[0,0,1]\n\t"
      "v_pk_fma_f32 v[2:3], v[2:3], s[86:87], v[12:13] op_sel:[0,0,0] op_sel_hi:[1,1,0] neg_lo:[0,0,1] neg_hi:[0,0,1]\n\t"
      "v_pk_fma_f32 v[4:5], v[4:5], s[86:87], v[12:13] op_sel:[0,0,1] op_sel_hi:[1,1,1] neg_lo:[0,0,1] neg_hi:[0,0,1]\n\t"
      "v_pk_fma_f32 v[6:7], v[6:7], s[86:87], v[12:13] op_sel:[0,0,1] op_sel_hi:[1,1,1] neg_lo:[0,0,1] neg_hi:[0,0,1]\n\t"
      "v_pk_fma_f32 v[8:9], v[8:9], s[86:87], v[12:13] op_sel:[0,0,1] op_sel_hi:[1,1,1] neg_lo:[0,0,1] neg_hi:[0,0,1]\n\t"
      "v_pk_fma_f32 v[10:11], v[10:11], s[86:87], v[12:13] op_sel:[0,0,1] op_sel_hi:[1,1,1] neg_lo:[0,0,1] neg_hi:[0,0,1]\n\t"
      "v_exp_f32 v0, v0\n\t"
      "v_exp_f32 v1, v1\n\t"
      "buffer_load_dwordx4 a[136:139], v248, s[36:39], s50 offen offset:2048  sc1\n\t"
      "v_exp_f32 v2, v2\n\t"
      "v_exp_f32 v3, v3\n\t"
      "v_exp_f32 v4, v4\n\t"
      "v_exp_f32 v5, v5\n\t"
      "v_exp_f32 v6, v6\n\t"
      "v_exp_f32 v7, v7\n\t"
      "v_exp_f32 v8, v8\n\t"
      "v_exp_f32 v9, v9\n\t"
      "buffer_load_dwordx4 a[140:143], v248, s[36:39], s50 offen offset:3072  sc1\n\t"
      "v_exp_f32 v10, v10\n\t"
      "v_exp_f32 v11, v11\n\t"
      "v_mov_b64 v[18:19], 0\n\t"
      "v_cvt_pk_bf16_f32 v16, v0, v1\n\t"
      "v_cvt_pk_bf16_f32 v17, v2, v3\n\t"
      "v_cvt_pk_bf16_f32 v20, v4, v5\n\t"
      "v_cvt_pk_bf16_f32 v21, v6, v7\n\t"
      "v_cvt_pk_bf16_f32 v22, v8, v9\n\t"
      "v_cvt_pk_bf16_f32 v23, v10, v11\n\t"
      "v_mfma_f32_16x16x32_bf16 v[64:67], a[192:195], v[16:19], 0\n\t"
      "v_pk_add_f32 v[0:1], v[0:1], v[2:3]\n\t"
      "s_add_u32 s69, s50, 4096\n\t"
      "buffer_load_dwordx4 a[144:147], v248, s[36:39], s69 offen  sc1\n\t"
      "v_mfma_f32_16x16x32_bf16 v[96:99], a[192:195], v[20:23], 0\n\t"
      "v_pk_add_f32 v[4:5], v[4:5], v[6:7]\n\t"
      "v_mfma_f32_16x16x32_bf16 v[68:71], a[196:199], v[16:19], 0\n\t"
      "v_pk_add_f32 v[8:9], v[8:9], v[10:11]\n\t"
      "v_mfma_f32_16x16x32_bf16 v[100:103], a[196:199], v[20:23], 0\n\t"
      "v_add_f32 v236, v0, v1\n\t"
      "v_mfma_f32_16x16x32_bf16 v[72:75], a[200:203], v[16:19], 0\n\t"
      "buffer_load_dwordx4 a[148:151], v248, s[36:39], s69 offen offset:1024  sc1\n\t"
      "v_pk_add_f32 v[4:5], v[4:5], v[8:9]\n\t"
      "v_mfma_f32_16x16x32_bf16 v[104:107], a[200:203], v[20:23], 0\n\t"
      "v_add_f32 v237, v4, v5\n\t"
      "v_mfma_f32_16x16x32_bf16 v[76:79], a[204:207], v[16:19], 0\n\t"
      "v_mfma_f32_16x16x32_bf16 v[108:111], a[204:207], v[20:23], 0\n\t"
      "buffer_load_dwordx4 a[152:155], v248, s[36:39], s69 offen offset:2048  sc1\n\t"
      "v_mfma_f32_16x16x32_bf16 v[80:83], a[208:211], v[16:19], 0\n\t"
      "v_mfma_f32_16x16x32_bf16 v[112:115], a[208:211], v[20:23], 0\n\t"
      "v_mfma_f32_16x16x32_bf16 v[84:87], a[212:215], v[16:19], 0\n\t"
      "v_mfma_f32_16x16x32_bf16 v[116:119], a[212:215], v[20:23], 0\n\t"
      "buffer_load_dwordx4 a[156:159], v248, s[36:39], s69 offen offset:3072  sc1\n\t"
      "v_mfma_f32_16x16x32_bf16 v[88:91], a[216:219], v[16:19], 0\n\t"
      "v_mfma_f32_16x16x32_bf16 v[120:123], a[216:219], v[20:23], 0\n\t"
      "v_mfma_f32_16x16x32_bf16 v[92:95], a[220:223], v[16:19], 0\n\t"
      "v_mfma_f32_16x16x32_bf16 v[124:127], a[220:223], v[20:23], 0\n\t"
      "s_waitcnt vmcnt(8)\n\t"
      "s_cmp_lt_u32 s48, 4\n\t"
      "s_cbranch_scc1 WGO%=\n\t"
      "v_mov_b32 v0, s48\n\t"
      "s_mov_b64 exec, 1\n\t"
      "buffer_store_dword v0, off, s[36:39], s63 sc1\n\t"
      "s_mov_b64 exec, -1\n\t"
      "s_cmp_lt_i32 s67, 0\n\t"
      "s_cbranch_scc1 WPB1%=\n\t"
      "v_mov_b32 v8, s68\n\t"
      "s_mov_b64 exec, 1\n\t"
      "buffer_store_dword v8, off, s[36:39], s67 sc1\n\t"
      "s_mov_b64 exec, -1\n\t"
      "s_mov_b32 s67, -1\n\t"
      "WPB1%=:\n\t"
      "WGO%=:\n\t"
      "s_cmp_eq_u32 s64, 1\n\t"
      "s_cbranch_scc1 WKV%=\n\t"
      "s_cmp_lg_u32 s65, 0\n\t"
      "s_cbranch_scc1 WR2%=\n\t"
      "WGO2%=:\n\t"
      "s_cmp_lt_u32 s48, 40\n\t"
      "s_cselect_b32 s71, 1, -1\n\t"
      "s_add_u32 s71, s71, s48\n\t"
      "s_mov_b32 s72, 0x40000\n\t"
      "WLOOP%=:\n\t"
      "s_cmp_ge_u32 s49, 48\n\t"
      "s_cselect_b32 s69, s49, 99\n\t"
      "s_cmp_lt_u32 s69, s48\n\t"
      "s_cbranch_scc1 WRW%=\n\t"
      "s_cmp_eq_u32 s64, 2\n\t"
      "s_cbranch_scc1 WKC%=\n\t"
      "v_lshlrev_b32 v6, 4, v248\n\t"
      "v_mov_b64 v[4:5], 0\n\t"
      "buffer_load_dwordx4 v[0:3], off, s[36:39], s56 sc1\n\t"
      "s_cmp_ge_u32 s49, s48\n\t"
      "s_cbranch_scc1 WL1%=\n\t"
      "buffer_load_dword v4, v6, s[36:39], s55 offen sc1\n\t"
      "buffer_load_dword v5, v6, s[36:39], s55 offen offset:128 sc1\n\t"
      "WL1%=:\n\t"
      "s_waitcnt vmcnt(0)\n\t"
      "s_cmp_lt_i32 s67, 0\n\t"
      "s_cbranch_scc1 WL2%=\n\t"
      "v_mov_b32 v8, s68\n\t"
      "s_mov_b64 exec, 1\n\t"
      "buffer_store_dword v8, off, s[36:39], s67 sc1\n\t"
      "s_mov_b64 exec, -1\n\t"
      "s_mov_b32 s67, -1\n\t"
      "WL2%=:\n\t"
      "v_min_u32 v0, v0, v1\n\t"
      "v_min3_u32 v0, v0, v2, v3\n\t"
      "v_min_u32 v4, v4, v5\n\t"
      "v_readfirstlane_b32 s88, v0\n\t"
      "v_cmp_ge_u32 vcc, s49, v4\n\t"
      "s_lshr_b32 s69, s88, 16\n\t"
      "s_max_u32 s70, s71, s48\n\t"
      "s_cmp_ge_u32 s69, s70\n\t"
      "s_cbranch_scc1 WKL%=\n\t"
      "s_cmp_ge_u32 s69, s71\n\t"
      "s_cbranch_scc1 WKF%=\n\t"
      "s_cbranch_vccz WRW%=\n\t"
      "s_sleep 6\n\t"
      "s_cmp_lt_u32 s49, s48\n\t"
      "s_cbranch_scc1 WKM%=\n\t"
      "s_sub_u32 s69, s71, s69\n\t"
      "s_min_u32 s69, s69, 9\n\t"
      "WKN%=:\n\t"
      "s_sub_u32 s69, s69, 1\n\t"
      "s_cmp_eq_u32 s69, 0\n\t"
      "s_cbranch_scc1 WKM%=\n\t"
      "s_sleep 48\n\t"
      "s_branch WKN%=\n\t"
      "WKM%=:\n\t"
      "s_sub_u32 s72, s72, 1\n\t"
      "s_cmp_gt_i32 s72, 0\n\t"
      "s_cbranch_scc1 WLOOP%=\n\t"
      "s_branch WKL%=\n\t"
      ".p2align 5\n\t"
      "WFIN%=:\n\t"
      "s_mov_b32 s64, 0\n\t"
      "s_mov_b32 s72, 0x40000\n\t"
      "s_waitcnt vmcnt(2)\n\t"
      "WFIN1%=:\n\t"
      "s_cmp_lt_i32 s67, 0\n\t"
      "s_cbranch_scc1 WF1%=\n\t"
      "v_mov_b32 v8, s68\n\t"
      "s_mov_b64 exec, 1\n\t"
      "buffer_store_dword v8, off, s[36:39], s67 sc1\n\t"
      "s_mov_b64 exec, -1\n\t"
      "s_mov_b32 s67, -1\n\t"
      "WF1%=:\n\t"
      "s_cmp_lg_u32 s65, 0\n\t"
      "s_cbranch_scc1 WRC%=\n\t"
      "s_cmp_ge_u32 s49, 64\n\t"
      "s_cbranch_scc1 WEND%=\n\t"
      "s_cmp_ge_u32 s49, 48\n\t"
      "s_cbranch_scc1 WRS%=\n\t"
      "v_lshlrev_b32 v6, 4, v248\n\t"
      "WRF%=:\n\t"
      "buffer_load_dword v4, v6, s[36:39], s55 offen sc1\n\t"
      "buffer_load_dword v5, v6, s[36:39], s55 offen offset:128 sc1\n\t"
      "s_waitcnt vmcnt(0)\n\t"
      "v_min_u32 v4, v4, v5\n\t"
      "v_cmp_ge_u32 vcc, s49, v4\n\t"
      "s_nop 0\n\t"
      "s_cbranch_vccz WRW%=\n\t"
      "s_sleep 8\n\t"
      "s_sub_u32 s72, s72, 1\n\t"
      "s_cmp_gt_i32 s72, 0\n\t"
      "s_cbranch_scc1 WRF%=\n\t"
      "WRS%=:\n\t"
      "s_waitcnt vmcnt(0)\n\t"
      "WRW%=:\n\t"
      "s_lshl_b32 s70, s49, 5\n\t"
      "s_add_u32 s70, s70, s60\n\t"
      "s_lshl_b32 s70, s70, 10\n\t"
      "buffer_load_dwordx2 v[224:225], v249, s[40:43], s70 offen \n\t"
      "buffer_load_dwordx2 v[226:227], v249, s[40:43], s70 offen offset:512 \n\t"
      "WRL%=:\n\t"
      "s_lshl_b32 s69, s49, 18\n\t"
      "s_add_u32 s69, s69, s54\n\t"
      "buffer_load_dwordx4 v[160:163], v248, s[36:39], s69 offen sc1\n\t"
      "s_add_u32 s69, s69, 0x8000\n\t"
      "buffer_load_dwordx4 v[164:167], v248, s[36:39], s69 offen sc1\n\t"
      "s_add_u32 s69, s69, 0x8000\n\t"
      "buffer_load_dwordx4 v[168:171], v248, s[36:39], s69 offen sc1\n\t"
      "s_add_u32 s69, s69, 0x8000\n\t"
      "buffer_load_dwordx4 v[172:175], v248, s[36:39], s69 offen sc1\n\t"
      "s_add_u32 s69, s69, 0x8000\n\t"
      "buffer_load_dwordx4 v[176:179], v248, s[36:39], s69 offen sc1\n\t"
      "s_add_u32 s69, s69, 0x8000\n\t"
      "buffer_load_dwordx4 v[180:183], v248, s[36:39], s69 offen sc1\n\t"
      "s_add_u32 s69, s69, 0x8000\n\t"
      "buffer_load_dwordx4 v[184:187], v248, s[36:39], s69 offen sc1\n\t"
      "s_add_u32 s69, s69, 0x8000\n\t"
      "buffer_load_dwordx4 v[188:191], v248, s[36:39], s69 offen sc1\n\t"
      "s_waitcnt vmcnt(0)\n\t"
      "s_cmp_lt_i32 s67, 0\n\t"
      "s_cbranch_scc1 WF2%=\n\t"
      "v_mov_b32 v8, s68\n\t"
      "s_mov_b64 exec, 1\n\t"
      "buffer_store_dword v8, off, s[36:39], s67 sc1\n\t"
      "s_mov_b64 exec, -1\n\t"
      "s_mov_b32 s67, -1\n\t"
      "WF2%=:\n\t"
      "WRC%=:\n\t"
      "v_mfma_f32_16x16x32_bf16 v[16:19], a[224:227], v[160:163], 0\n\t"
      "v_mfma_f32_16x16x32_bf16 v[20:23], a[228:231], v[160:163], 0\n\t"
      "v_and_b32 v193, s74, v224\n\t"
      "v_lshlrev_b32 v192, 16, v224\n\t"
      "v_mfma_f32_16x16x32_bf16 v[16:19], a[224:227], v[164:167], v[16:19]\n\t"
      "v_mfma_f32_16x16x32_bf16 v[20:23], a[228:231], v[164:167], v[20:23]\n\t"
      "v_and_b32 v195, s74, v225\n\t"
      "v_lshlrev_b32 v194, 16, v225\n\t"
      "v_mfma_f32_16x16x32_bf16 v[16:19], a[224:227], v[168:171], v[16:19]\n\t"
      "v_mfma_f32_16x16x32_bf16 v[20:23], a[228:231], v[168:171], v[20:23]\n\t"
      "v_and_b32 v197, s74, v226\n\t"
      "v_lshlrev_b32 v196, 16, v226\n\t"
      "v_mfma_f32_16x16x32_bf16 v[16:19], a[224:227], v[172:175], v[16:19]\n\t"
      "v_mfma_f32_16x16x32_bf16 v[20:23], a[228:231], v[172:175], v[20:23]\n\t"
      "v_and_b32 v199, s74, v227\n\t"
      "v_lshlrev_b32 v198, 16, v227\n\t"
      "v_mfma_f32_16x16x32_bf16 v[16:19], a[224:227], v[176:179], v[16:19]\n\t"
      "v_mfma_f32_16x16x32_bf16 v[20:23], a[228:231], v[176:179], v[20:23]\n\t"
      "v_mfma_f32_16x16x32_bf16 v[16:19], a[224:227], v[180:183], v[16:19]\n\t"
      "v_mfma_f32_16x16x32_bf16 v[20:23], a[228:231], v[180:183], v[20:23]\n\t"
      "v_mfma_f32_16x16x32_bf16 v[16:19], a[224:227], v[184:187], v[16:19]\n\t"
      "v_mfma_f32_16x16x32_bf16 v[20:23], a[228:231], v[184:187], v[20:23]\n\t"
      "v_mfma_f32_16x16x32_bf16 v[16:19], a[224:227], v[188:191], v[16:19]\n\t"
      "v_mfma_f32_16x16x32_bf16 v[20:23], a[228:231], v[188:191], v[20:23]\n\t"
      "s_nop 6\n\t"
      "v_cvt_pk_bf16_f32 v32, v16, v17\n\t"
      "v_cvt_pk_bf16_f32 v34, v18, v19\n\t"
      "v_cvt_pk_bf16_f32 v36, v20, v21\n\t"
      "v_cvt_pk_bf16_f32 v38, v22, v23\n\t"
      "v_and_b32 v33, s74, v32\n\t"
      "v_lshlrev_b32 v32, 16, v32\n\t"
      "v_and_b32 v35, s74, v34\n\t"
      "v_lshlrev_b32 v34, 16, v34\n\t"
      "v_and_b32 v37, s74, v36\n\t"
      "v_lshlrev_b32 v36, 16, v36\n\t"
      "v_and_b32 v39, s74, v38\n\t"
      "v_lshlrev_b32 v38, 16, v38\n\t"
      "v_pk_add_f32 v[192:193], v[192:193], v[32:33]\n\t"
      "v_pk_add_f32 v[194:195], v[194:195], v[34:35]\n\t"
      "v_pk_add_f32 v[196:197], v[196:197], v[36:37]\n\t"
      "v_pk_add_f32 v[198:199], v[198:199], v[38:39]\n\t"
      "v_cvt_pk_bf16_f32 v200, v192, v193\n\t"
      "v_cvt_pk_bf16_f32 v201, v194, v195\n\t"
      "v_cvt_pk_bf16_f32 v202, v196, v197\n\t"
      "v_cvt_pk_bf16_f32 v203, v198, v199\n\t"
      "v_and_b32 v193, s74, v200\n\t"
      "v_lshlrev_b32 v192, 16, v200\n\t"
      "v_and_b32 v195, s74, v201\n\t"
      "v_lshlrev_b32 v194, 16, v201\n\t"
      "v_and_b32 v197, s74, v202\n\t"
      "v_lshlrev_b32 v196, 16, v202\n\t"
      "v_and_b32 v199, s74, v203\n\t"
      "v_lshlrev_b32 v198, 16, v203\n\t"
      "v_pk_add_f32 v[32:33], v[192:193], v[196:197]\n\t"
      "v_pk_add_f32 v[34:35], v[194:195], v[198:199]\n\t"
      "v_pk_add_f32 v[32:33], v[32:33], v[34:35]\n\t"
      "v_add_f32 v32, v32, v33\n\t"
      "s_nop 1\n\t"
      "v_add_f32_dpp v32, v32, v32 quad_perm:[1,0,3,2] row_mask:0xf bank_mask:0xf\n\t"
      "s_nop 1\n\t"
      "v_add_f32_dpp v32, v32, v32 quad_perm:[2,3,0,1] row_mask:0xf bank_mask:0xf\n\t"
      "s_nop 1\n\t"
      "v_add_f32_dpp v32, v32, v32 row_ror:4 row_mask:0xf bank_mask:0xf\n\t"
      "s_nop 1\n\t"
      "v_add_f32_dpp v32, v32, v32 row_ror:8 row_mask:0xf bank_mask:0xf\n\t"
      "v_mov_b32 v33, v32\n\t"
      "s_nop 1\n\t"
      "v_permlane16_swap_b32 v32, v33\n\t"
      "v_add_f32 v32, v32, v33\n\t"
      "v_mov_b32 v33, v32\n\t"
      "s_nop 1\n\t"
      "v_permlane32_swap_b32 v32, v33\n\t"
      "v_add_f32 v32, v32, v33\n\t"
      "v_cmp_u_f32 vcc, v32, v32\n\t"
      "v_mul_f32 v204, 0x3b000000, v32\n\t"
      "s_cbranch_vccnz WRN%=\n\t"
      "WRD%=:\n\t"
      "v_pk_add_f32 v[40:41], v[192:193], v[204:205] op_sel_hi:[1,0] neg_lo:[0,1] neg_hi:[0,1]\n\t"
      "v_pk_add_f32 v[42:43], v[194:195], v[204:205] op_sel_hi:[1,0] neg_lo:[0,1] neg_hi:[0,1]\n\t"
      "v_pk_add_f32 v[44:45], v[196:197], v[204:205] op_sel_hi:[1,0] neg_lo:[0,1] neg_hi:[0,1]\n\t"
      "v_pk_add_f32 v[46:47], v[198:199], v[204:205] op_sel_hi:[1,0] neg_lo:[0,1] neg_hi:[0,1]\n\t"
      "v_pk_mul_f32 v[32:33], v[40:41], v[40:41]\n\t"
      "v_pk_fma_f32 v[32:33], v[42:43], v[42:43], v[32:33]\n\t"
      "v_pk_fma_f32 v[32:33], v[44:45], v[44:45], v[32:33]\n\t"
      "v_pk_fma_f32 v[32:33], v[46:47], v[46:47], v[32:33]\n\t"
      "v_add_f32 v32, v32, v33\n\t"
      "s_nop 1\n\t"
      "v_add_f32_dpp v32, v32, v32 quad_perm:[1,0,3,2] row_mask:0xf bank_mask:0xf\n\t"
      "s_nop 1\n\t"
      "v_add_f32_dpp v32, v32, v32 quad_perm:[2,3,0,1] row_mask:0xf bank_mask:0xf\n\t"
      "s_nop 1\n\t"
      "v_add_f32_dpp v32, v32, v32 row_ror:4 row_mask:0xf bank_mask:0xf\n\t"
      "s_nop 1\n\t"
      "v_add_f32_dpp v32, v32, v32 row_ror:8 row_mask:0xf bank_mask:0xf\n\t"
      "v_mov_b32 v33, v32\n\t"
      "s_nop 1\n\t"
      "v_permlane16_swap_b32 v32, v33\n\t"
      "v_add_f32 v32, v32, v33\n\t"
      "v_mov_b32 v33, v32\n\t"
      "s_nop 1\n\t"
      "v_permlane32_swap_b32 v32, v33\n\t"
      "v_add_f32 v32, v32, v33\n\t"
      "v_mul_f32 v205, 0x3b000000, v32\n\t"
      "v_cvt_pk_bf16_f32 v204, v204, v205\n\t"
      "v_and_b32 v205, s74, v204\n\t"
      "v_lshlrev_b32 v204, 16, v204\n\t"
      "v_add_f32 v205, 0x3727c5ac, v205\n\t"
      "v_pk_add_f32 v[40:41], v[192:193], v[204:205] op_sel_hi:[1,0] neg_lo:[0,1] neg_hi:[0,1]\n\t"
      "v_pk_add_f32 v[42:43], v[194:195], v[204:205] op_sel_hi:[1,0] neg_lo:[0,1] neg_hi:[0,1]\n\t"
      "v_pk_add_f32 v[44:45], v[196:197], v[204:205] op_sel_hi:[1,0] neg_lo:[0,1] neg_hi:[0,1]\n\t"
      "v_pk_add_f32 v[46:47], v[198:199], v[204:205] op_sel_hi:[1,0] neg_lo:[0,1] neg_hi:[0,1]\n\t"
      "v_cvt_pk_bf16_f32 v205, v205, v205\n\t"
      "v_and_b32 v205, s74, v205\n\t"
      "v_sqrt_f32 v205, v205\n\t"
      "v_cvt_pk_bf16_f32 v40, v40, v41\n\t"
      "v_cvt_pk_bf16_f32 v42, v42, v43\n\t"
      "v_cvt_pk_bf16_f32 v44, v44, v45\n\t"
      "v_cvt_pk_bf16_f32 v46, v46, v47\n\t"
      "v_cvt_pk_bf16_f32 v205, v205, v205\n\t"
      "v_and_b32 v205, s74, v205\n\t"
      "v_rcp_f32 v204, v205\n\t"
      "v_and_b32 v41, s74, v40\n\t"
      "v_lshlrev_b32 v40, 16, v40\n\t"
      "v_and_b32 v43, s74, v42\n\t"
      "v_lshlrev_b32 v42, 16, v42\n\t"
      "v_and_b32 v45, s74, v44\n\t"
      "v_lshlrev_b32 v44, 16, v44\n\t"
      "v_and_b32 v47, s74, v46\n\t"
      "v_lshlrev_b32 v46, 16, v46\n\t"
      "v_pk_mul_f32 v[40:41], v[40:41], v[204:205] op_sel_hi:[1,0]\n\t"
      "v_pk_mul_f32 v[42:43], v[42:43], v[204:205] op_sel_hi:[1,0]\n\t"
      "v_pk_mul_f32 v[44:45], v[44:45], v[204:205] op_sel_hi:[1,0]\n\t"
      "v_pk_mul_f32 v[46:47], v[46:47], v[204:205] op_sel_hi:[1,0]\n\t"
      "v_cvt_pk_bf16_f32 v52, v40, v41\n\t"
      "v_cvt_pk_bf16_f32 v53, v42, v43\n\t"
      "v_cvt_pk_bf16_f32 v54, v44, v45\n\t"
      "v_cvt_pk_bf16_f32 v55, v46, v47\n\t"
      "v_and_b32 v33, s74, v52\n\t"
      "v_lshlrev_b32 v32, 16, v52\n\t"
      "v_and_b32 v35, s74, v53\n\t"
      "v_lshlrev_b32 v34, 16, v53\n\t"
      "v_and_b32 v37, s74, v54\n\t"
      "v_lshlrev_b32 v36, 16, v54\n\t"
      "v_and_b32 v39, s74, v55\n\t"
      "v_lshlrev_b32 v38, 16, v55\n\t"
      "v_pk_mul_f32 v[40:41], v[32:33], v[240:241]\n\t"
      "v_pk_fma_f32 v[40:41], v[34:35], v[242:243], v[40:41]\n\t"
      "v_pk_fma_f32 v[40:41], v[36:37], v[244:245], v[40:41]\n\t"
      "v_pk_fma_f32 v[40:41], v[38:39], v[246:247], v[40:41]\n\t"
      "v_add_f32 v40, v40, v41\n\t"
      "s_lshl_b32 s73, s49, 5\n\t"
      "s_add_u32 s73, s73, s60\n\t"
      "s_lshl_b32 s69, s73, 2\n\t"
      "s_add_u32 s69, s69, s57\n\t"
      "s_lshl_b32 s70, s73, 7\n\t"
      "s_add_u32 s70, s70, s57\n\t"
      "s_sub_u32 s70, s70, 212992\n\t"
      "s_cmp_ge_u32 s49, 56\n\t"
      "s_cselect_b32 s70, s70, s69\n\t"
      "v_add_f32_dpp v40, v40, v40 quad_perm:[1,0,3,2] row_mask:0xf bank_mask:0xf\n\t"
      "s_nop 1\n\t"
      "v_add_f32_dpp v40, v40, v40 quad_perm:[2,3,0,1] row_mask:0xf bank_mask:0xf\n\t"
      "s_nop 1\n\t"
      "v_add_f32_dpp v40, v40, v40 row_ror:4 row_mask:0xf bank_mask:0xf\n\t"
      "s_nop 1\n\t"
      "v_add_f32_dpp v40, v40, v40 row_ror:8 row_mask:0xf bank_mask:0xf\n\t"
      "v_mov_b32 v41, v40\n\t"
      "s_nop 1\n\t"
      "v_permlane16_swap_b32 v40, v41\n\t"
      "v_add_f32 v40, v40, v41\n\t"
      "v_mov_b32 v41, v40\n\t"
      "s_nop 1\n\t"
      "v_permlane32_swap_b32 v40, v41\n\t"
      "v_add_f32 v40, v40, v41\n\t"
      "v_cvt_pk_bf16_f32 v40, v40, v40\n\t"
      "v_and_b32 v40, s74, v40\n\t"
      "s_mov_b64 exec, 1\n\t"
      "buffer_store_dword v40, off, s[36:39], s70 sc1\n\t"
      "s_mov_b64 exec, -1\n\t"
      "v_pk_add_f32 v[192:193], v[192:193], v[32:33]\n\t"
      "v_pk_add_f32 v[194:195], v[194:195], v[34:35]\n\t"
      "v_pk_add_f32 v[196:197], v[196:197], v[36:37]\n\t"
      "v_pk_add_f32 v[198:199], v[198:199], v[38:39]\n\t"
      "v_readfirstlane_b32 s68, v40\n\t"
      "v_cvt_pk_bf16_f32 v56, v192, v193\n\t"
      "v_cvt_pk_bf16_f32 v57, v194, v195\n\t"
      "v_cvt_pk_bf16_f32 v58, v196, v197\n\t"
      "v_cvt_pk_bf16_f32 v59, v198, v199\n\t"
      "s_add_u32 s67, s69, 8192\n\t"
      "s_lshl_b32 s73, s73, 10\n\t"
      "s_add_u32 s69, s73, s58\n\t"
      "s_add_u32 s70, s73, s59\n\t"
      "buffer_store_dwordx2 v[56:57], v249, s[44:47], s73 offen sc1\n\t"
      "buffer_store_dwordx2 v[52:53], v249, s[36:39], s69 offen sc1\n\t"
      "buffer_store_dwordx2 v[200:201], v249, s[36:39], s70 offen sc1\n\t"
      "buffer_store_dwordx2 v[58:59], v249, s[44:47], s73 offen offset:512 sc1\n\t"
      "buffer_store_dwordx2 v[54:55], v249, s[36:39], s69 offen offset:512 sc1\n\t"
      "buffer_store_dwordx2 v[202:203], v249, s[36:39], s70 offen offset:512 sc1\n\t"
      "s_mov_b32 s49, s89\n\t"
      "s_add_u32 s89, s89, 16\n\t"
      "s_lshr_b32 s69, s89, 4\n\t"
      "s_cmp_eq_u32 s69, 3\n\t"
      "s_cselect_b32 s89, s93, s89\n\t"
      "s_cmp_ge_u32 s49, 48\n\t"
      "s_cselect_b32 s60, s94, s60\n\t"
      "s_cselect_b32 s54, s95, s54\n\t"
      "s_cmp_ge_u32 s48, 64\n\t"
      "s_cbranch_scc1 WRX%=\n\t"
      "s_cmp_lg_u32 s65, 0\n\t"
      "s_mov_b32 s65, 0\n\t"
      "s_cbranch_scc1 WGO%=\n\t"
      "s_mov_b32 s66, 0\n\t"
      "s_branch WLOOP%=\n\t"
      ".p2align 5\n\t"
      "WRX%=:\n\t"
      "s_mov_b32 s65, 0\n\t"
      "s_waitcnt vmcnt(0)\n\t"
      "s_branch WFIN1%=\n\t"
      ".p2align 5\n\t"
      "WRN%=:\n\t"
      "v_max3_u32 v40, v160, v161, v162\n\t"
      "v_max3_u32 v40, v40, v163, v164\n\t"
      "v_max3_u32 v40, v40, v165, v166\n\t"
      "v_max3_u32 v40, v40, v167, v168\n\t"
      "v_max3_u32 v40, v40, v169, v170\n\t"
      "v_max3_u32 v40, v40, v171, v172\n\t"
      "v_max3_u32 v40, v40, v173, v174\n\t"
      "v_max3_u32 v40, v40, v175, v176\n\t"
      "v_max3_u32 v40, v40, v177, v178\n\t"
      "v_max3_u32 v40, v40, v179, v180\n\t"
      "v_max3_u32 v40, v40, v181, v182\n\t"
      "v_max3_u32 v40, v40, v183, v184\n\t"
      "v_max3_u32 v40, v40, v185, v186\n\t"
      "v_max3_u32 v40, v40, v187, v188\n\t"
      "v_max3_u32 v40, v40, v189, v190\n\t"
      "v_max_u32 v40, v40, v191\n\t"
      "v_cmp_eq_u32 vcc, -1, v40\n\t"
      "s_nop 0\n\t"
      "s_cbranch_vccz WRD%=\n\t"
      "s_cmp_lg_u32 s65, 0\n\t"
      "s_cbranch_scc1 WRD%=\n\t"
      "s_sub_u32 s72, s72, 1\n\t"
      "s_cmp_gt_i32 s72, 0\n\t"
      "s_cbranch_scc1 WRL%=\n\t"
      "s_branch WRD%=\n\t"
      ".p2align 5\n\t"
      "WH2%=:\n\t"
      "s_mov_b32 s72, 0x40000\n\t"
      "WH3%=:\n\t"
      "buffer_load_dwordx2 v[0:1], off, s[36:39], s99 sc1\n\t"
      "buffer_load_dwordx2 v[2:3], off, s[36:39], s99 offset:16 sc1\n\t"
      "s_sub_u32 s72, s72, 1\n\t"
      "s_waitcnt vmcnt(0)\n\t"
      "v_bitop3_b32 v0, v0, v1, s62 bitop3:0x7e\n\t"
      "v_bitop3_b32 v2, v2, v3, s62 bitop3:0x7e\n\t"
      "v_or_b32 v0, v0, v2\n\t"
      "v_cmp_ne_u32 vcc, 0, v0\n\t"
      "s_cmp_eq_u32 s72, 0\n\t"
      "s_cbranch_scc1 WT0%=\n\t"
      "s_cbranch_vccnz WH3%=\n\t"
      "s_branch WT0%=\n\t"
      ".p2align 5\n\t"
      "WKV1%=:\n\t"
      "s_cmp_eq_u32 s66, 0\n\t"
      "s_cbranch_scc1 WB1%=\n\t"
      "WB2%=:\n\t"
      "s_mov_b32 s65, 1\n\t"
      "v_mfma_f32_16x16x32_bf16 v[0:3], v[128:131], a[160:163], 0\n\t"
      "v_mfma_f32_16x16x32_bf16 v[4:7], v[128:131], a[164:167], 0\n\t"
      "v_mfma_f32_16x16x32_bf16 v[0:3], v[132:135], a[168:171], v[0:3]\n\t"
      "v_mfma_f32_16x16x32_bf16 v[4:7], v[132:135], a[172:175], v[4:7]\n\t"
      "v_mfma_f32_16x16x32_bf16 v[0:3], v[136:139], a[176:179], v[0:3]\n\t"
      "v_mfma_f32_16x16x32_bf16 v[4:7], v[136:139], a[180:183], v[4:7]\n\t"
      "v_mfma_f32_16x16x32_bf16 v[0:3], v[140:143], a[184:187], v[0:3]\n\t"
      "v_mfma_f32_16x16x32_bf16 v[4:7], v[140:143], a[188:191], v[4:7]\n\t"
      "s_add_u32 s51, s51, 262144\n\t"
      "s_add_u32 s52, s52, 262144\n\t"
      "s_lshl_b32 s70, s49, 5\n\t"
      "s_add_u32 s70, s70, s60\n\t"
      "s_lshl_b32 s70, s70, 10\n\t"
      "buffer_load_dwordx2 v[224:225], v249, s[40:43], s70 offen \n\t"
      "s_nop 0\n\t"
      "v_max3_f32 v8, v0, v1, v2\n\t"
      "v_max3_f32 v8, v8, v3, v234\n\t"
      "v_max3_f32 v9, v4, v5, v6\n\t"
      "v_max3_f32 v9, v9, v7, v235\n\t"
      "s_nop 1\n\t"
      "v_permlane16_swap_b32 v8, v9\n\t"
      "v_max_f32 v8, v8, v9\n\t"
      "buffer_load_dwordx2 v[226:227], v249, s[40:43], s70 offen offset:512 \n\t"
      "v_mov_b32 v9, v8\n\t"
      "s_nop 1\n\t"
      "v_permlane16_swap_b32 v8, v9\n\t"
      "v_pk_mul_f32 v[8:9], v[8:9], s[86:87]\n\t"
      "v_pk_fma_f32 v[10:11], v[234:235], s[86:87], v[8:9] neg_lo:[0,0,1] neg_hi:[0,0,1]\n\t"
      "v_pk_fma_f32 v[0:1], v[0:1], s[86:87], v[8:9] op_sel:[0,0,0] op_sel_hi:[1,1,0] neg_lo:[0,0,1] neg_hi:[0,0,1]\n\t"
      "v_pk_fma_f32 v[2:3], v[2:3], s[86:87], v[8:9] op_sel:[0,0,0] op_sel_hi:[1,1,0] neg_lo:[0,0,1] neg_hi:[0,0,1]\n\t"
      "s_lshl_b32 s69, s49, 18\n\t"
      "s_add_u32 s69, s69, s54\n\t"
      "buffer_load_dwordx4 v[160:163], v248, s[36:39], s69 offen sc1\n\t"
      "v_pk_fma_f32 v[4:5], v[4:5], s[86:87], v[8:9] op_sel:[0,0,1] op_sel_hi:[1,1,1] neg_lo:[0,0,1] neg_hi:[0,0,1]\n\t"
      "v_pk_fma_f32 v[6:7], v[6:7], s[86:87], v[8:9] op_sel:[0,0,1] op_sel_hi:[1,1,1] neg_lo:[0,0,1] neg_hi:[0,0,1]\n\t"
      "v_exp_f32 v10, v10\n\t"
      "v_exp_f32 v11, v11\n\t"
      "s_add_u32 s69, s69, 0x8000\n\t"
      "buffer_load_dwordx4 v[164:167], v248, s[36:39], s69 offen sc1\n\t"
      "v_exp_f32 v0, v0\n\t"
      "v_exp_f32 v1, v1\n\t"
      "v_exp_f32 v2, v2\n\t"
      "v_exp_f32 v3, v3\n\t"
      "s_add_u32 s69, s69, 0x8000\n\t"
      "buffer_load_dwordx4 v[168:171], v248, s[36:39], s69 offen sc1\n\t"
      "v_exp_f32 v4, v4\n\t"
      "v_exp_f32 v5, v5\n\t"
      "v_exp_f32 v6, v6\n\t"
      "s_add_u32 s69, s69, 0x8000\n\t"
      "buffer_load_dwordx4 v[172:175], v248, s[36:39], s69 offen sc1\n\t"
      "v_exp_f32 v7, v7\n\t"
      "v_pk_mul_f32 v[12:13], v[236:237], v[10:11]\n\t"
      "v_pk_add_f32 v[14:15], v[0:1], v[2:3]\n\t"
      "v_pk_add_f32 v[16:17], v[4:5], v[6:7]\n\t"
      "s_add_u32 s69, s69, 0x8000\n\t"
      "buffer_load_dwordx4 v[176:179], v248, s[36:39], s69 offen sc1\n\t"
      "v_add_f32 v14, v14, v15\n\t"
      "v_add_f32 v15, v16, v17\n\t"
      "v_fma_f32 v12, v14, 0.5, v12\n\t"
      "v_fma_f32 v13, v15, 0.5, v13\n\t"
      "s_nop 1\n\t"
      "v_permlane16_swap_b32 v12, v13\n\t"
      "v_add_f32 v12, v12, v13\n\t"
      "s_add_u32 s69, s69, 0x8000\n\t"
      "buffer_load_dwordx4 v[180:183], v248, s[36:39], s69 offen sc1\n\t"
      "v_mov_b32 v13, v12\n\t"
      "s_nop 1\n\t"
      "v_permlane32_swap_b32 v12, v13\n\t"
      "v_add_f32 v12, v12, v13\n\t"
      "v_mov_b32 v13, v12\n\t"
      "s_nop 1\n\t"
      "v_permlane16_swap_b32 v12, v13\n\t"
      "v_rcp_f32 v12, v12\n\t"
      "s_add_u32 s69, s69, 0x8000\n\t"
      "buffer_load_dwordx4 v[184:187], v248, s[36:39], s69 offen sc1\n\t"
      "v_rcp_f32 v13, v13\n\t"
      "s_nop 0\n\t"
      "v_pk_mul_f32 v[10:11], v[10:11], v[12:13]\n\t"
      "v_pk_mul_f32 v[12:13], v[12:13], 0.5 op_sel_hi:[1,0]\n\t"
      "v_pk_mul_f32 v[0:1], v[0:1], v[12:13] op_sel:[0,0] op_sel_hi:[1,0]\n\t"
      "s_add_u32 s69, s69, 0x8000\n\t"
      "buffer_load_dwordx4 v[188:191], v248, s[36:39], s69 offen sc1\n\t"
      "v_pk_mul_f32 v[2:3], v[2:3], v[12:13] op_sel:[0,0] op_sel_hi:[1,0]\n\t"
      "v_pk_mul_f32 v[4:5], v[4:5], v[12:13] op_sel:[0,1] op_sel_hi:[1,1]\n\t"
      "v_pk_mul_f32 v[6:7], v[6:7], v[12:13] op_sel:[0,1] op_sel_hi:[1,1]\n\t"
      "v_cvt_pk_bf16_f32 v16, v0, v1\n\t"
      "v_cvt_pk_bf16_f32 v17, v2, v3\n\t"
      "v_cvt_pk_bf16_f32 v18, v4, v5\n\t"
      "s_add_u32 s69, s50, 8192\n\t"
      "s_add_u32 m0, s61, 0\n\t"
      "s_nop 0\n\t"
      "buffer_load_dwordx4 v250, s[36:39], s69 offen sc1 lds\n\t"
      "v_cvt_pk_bf16_f32 v19, v6, v7\n\t"
      "v_pk_mul_f32 v[64:65], v[64:65], v[10:11] op_sel:[0,0] op_sel_hi:[1,0]\n\t"
      "v_pk_mul_f32 v[66:67], v[66:67], v[10:11] op_sel:[0,0] op_sel_hi:[1,0]\n\t"
      "v_pk_mul_f32 v[68:69], v[68:69], v[10:11] op_sel:[0,0] op_sel_hi:[1,0]\n\t"
      "v_pk_mul_f32 v[70:71], v[70:71], v[10:11] op_sel:[0,0] op_sel_hi:[1,0]\n\t"
      "v_mfma_f32_16x16x16_bf16 v[64:67], v[144:145], v[16:17], v[64:67]\n\t"
      "buffer_load_dwordx4 v250, s[36:39], s69 offen offset:1024 sc1 lds\n\t"
      "v_pk_mul_f32 v[72:73], v[72:73], v[10:11] op_sel:[0,0] op_sel_hi:[1,0]\n\t"
      "v_pk_mul_f32 v[74:75], v[74:75], v[10:11] op_sel:[0,0] op_sel_hi:[1,0]\n\t"
      "v_mfma_f32_16x16x16_bf16 v[68:71], v[146:147], v[16:17], v[68:71]\n\t"
      "v_pk_mul_f32 v[76:77], v[76:77], v[10:11] op_sel:[0,0] op_sel_hi:[1,0]\n\t"
      "buffer_load_dwordx4 v250, s[36:39], s69 offen offset:2048 sc1 lds\n\t"
      "v_pk_mul_f32 v[78:79], v[78:79], v[10:11] op_sel:[0,0] op_sel_hi:[1,0]\n\t"
      "v_mfma_f32_16x16x16_bf16 v[72:75], v[148:149], v[16:17], v[72:75]\n\t"
      "v_pk_mul_f32 v[80:81], v[80:81], v[10:11] op_sel:[0,0] op_sel_hi:[1,0]\n\t"
      "v_pk_mul_f32 v[82:83], v[82:83], v[10:11] op_sel:[0,0] op_sel_hi:[1,0]\n\t"
      "v_mfma_f32_16x16x16_bf16 v[76:79], v[150:151], v[16:17], v[76:79]\n\t"
      "buffer_load_dwordx4 v250, s[36:39], s69 offen offset:3072 sc1 lds\n\t"
      "v_pk_mul_f32 v[84:85], v[84:85], v[10:11] op_sel:[0,0] op_sel_hi:[1,0]\n\t"
      "v_pk_mul_f32 v[86:87], v[86:87], v[10:11] op_sel:[0,0] op_sel_hi:[1,0]\n\t"
      "v_mfma_f32_16x16x16_bf16 v[80:83], v[152:153], v[16:17], v[80:83]\n\t"
      "v_pk_mul_f32 v[88:89], v[88:89], v[10:11] op_sel:[0,0] op_sel_hi:[1,0]\n\t"
      "s_add_u32 s69, s50, 12288\n\t"
      "s_add_u32 m0, s61, 4096\n\t"
      "s_nop 0\n\t"
      "buffer_load_dwordx4 v250, s[36:39], s69 offen sc1 lds\n\t"
      "v_pk_mul_f32 v[90:91], v[90:91], v[10:11] op_sel:[0,0] op_sel_hi:[1,0]\n\t"
      "v_mfma_f32_16x16x16_bf16 v[84:87], v[154:155], v[16:17], v[84:87]\n\t"
      "v_pk_mul_f32 v[92:93], v[92:93], v[10:11] op_sel:[0,0] op_sel_hi:[1,0]\n\t"
      "v_pk_mul_f32 v[94:95], v[94:95], v[10:11] op_sel:[0,0] op_sel_hi:[1,0]\n\t"
      "v_cvt_pk_bf16_f32 v24, v64, v65\n\t"
      "buffer_load_dwordx4 v250, s[36:39], s69 offen offset:1024 sc1 lds\n\t"
      "v_cvt_pk_bf16_f32 v25, v66, v67\n\t"
      "v_cvt_pk_bf16_f32 v26, v68, v69\n\t"
      "v_cvt_pk_bf16_f32 v27, v70, v71\n\t"
      "ds_write_b128 v254, v[24:27]\n\t"
      "v_mfma_f32_16x16x16_bf16 v[88:91], v[156:157], v[16:17], v[88:91]\n\t"
      "buffer_load_dwordx4 v250, s[36:39], s69 offen offset:2048 sc1 lds\n\t"
      "v_pk_mul_f32 v[96:97], v[96:97], v[10:11] op_sel:[0,1] op_sel_hi:[1,1]\n\t"
      "v_pk_mul_f32 v[98:99], v[98:99], v[10:11] op_sel:[0,1] op_sel_hi:[1,1]\n\t"
      "v_mfma_f32_16x16x16_bf16 v[92:95], v[158:159], v[16:17], v[92:95]\n\t"
      "v_pk_mul_f32 v[100:101], v[100:101], v[10:11] op_sel:[0,1] op_sel_hi:[1,1]\n\t"
      "v_pk_mul_f32 v[102:103], v[102:103], v[10:11] op_sel:[0,1] op_sel_hi:[1,1]\n\t"
      "buffer_load_dwordx4 v250, s[36:39], s69 offen offset:3072 sc1 lds\n\t"
      "v_cvt_pk_bf16_f32 v28, v72, v73\n\t"
      "v_cvt_pk_bf16_f32 v29, v74, v75\n\t"
      "v_cvt_pk_bf16_f32 v30, v76, v77\n\t"
      "v_cvt_pk_bf16_f32 v31, v78, v79\n\t"
      "ds_write_b128 v254, v[28:31] offset:2048\n\t"
      "v_mfma_f32_16x16x16_bf16 v[96:99], v[144:145], v[18:19], v[96:99]\n\t"
      "s_add_u32 s69, s50, 16384\n\t"
      "buffer_load_dwordx4 a[160:163], v248, s[36:39], s69 offen  sc1\n\t"
      "v_pk_mul_f32 v[104:105], v[104:105], v[10:11] op_sel:[0,1] op_sel_hi:[1,1]\n\t"
      "v_pk_mul_f32 v[106:107], v[106:107], v[10:11] op_sel:[0,1] op_sel_hi:[1,1]\n\t"
      "v_mfma_f32_16x16x16_bf16 v[100:103], v[146:147], v[18:19], v[100:103]\n\t"
      "v_pk_mul_f32 v[108:109], v[108:109], v[10:11] op_sel:[0,1] op_sel_hi:[1,1]\n\t"
      "buffer_load_dwordx4 a[164:167], v248, s[36:39], s69 offen offset:1024  sc1\n\t"
      "v_pk_mul_f32 v[110:111], v[110:111], v[10:11] op_sel:[0,1] op_sel_hi:[1,1]\n\t"
      "v_cvt_pk_bf16_f32 v24, v80, v81\n\t"
      "v_cvt_pk_bf16_f32 v25, v82, v83\n\t"
      "v_cvt_pk_bf16_f32 v26, v84, v85\n\t"
      "v_cvt_pk_bf16_f32 v27, v86, v87\n\t"
      "ds_write_b128 v254, v[24:27] offset:4096\n\t"
      "buffer_load_dwordx4 a[168:171], v248, s[36:39], s69 offen offset:2048  sc1\n\t"
      "v_mfma_f32_16x16x16_bf16 v[104:107], v[148:149], v[18:19], v[104:107]\n\t"
      "v_pk_mul_f32 v[112:113], v[112:113], v[10:11] op_sel:[0,1] op_sel_hi:[1,1]\n\t"
      "v_pk_mul_f32 v[114:115], v[114:115], v[10:11] op_sel:[0,1] op_sel_hi:[1,1]\n\t"
      "v_mfma_f32_16x16x16_bf16 v[108:111], v[150:151], v[18:19], v[108:111]\n\t"
      "buffer_load_dwordx4 a[172:175], v248, s[36:39], s69 offen offset:3072  sc1\n\t"
      "v_pk_mul_f32 v[116:117], v[116:117], v[10:11] op_sel:[0,1] op_sel_hi:[1,1]\n\t"
      "v_pk_mul_f32 v[118:119], v[118:119], v[10:11] op_sel:[0,1] op_sel_hi:[1,1]\n\t"
      "v_cvt_pk_bf16_f32 v28, v88, v89\n\t"
      "v_cvt_pk_bf16_f32 v29, v90, v91\n\t"
      "v_cvt_pk_bf16_f32 v30, v92, v93\n\t"
      "v_cvt_pk_bf16_f32 v31, v94, v95\n\t"
      "s_add_u32 s69, s50, 20480\n\t"
      "buffer_load_dwordx4 a[176:179], v248, s[36:39], s69 offen  sc1\n\t"
      "ds_write_b128 v254, v[28:31] offset:6144\n\t"
      "v_mfma_f32_16x16x16_bf16 v[112:115], v[152:153], v[18:19], v[112:115]\n\t"
      "v_pk_mul_f32 v[120:121], v[120:121], v[10:11] op_sel:[0,1] op_sel_hi:[1,1]\n\t"
      "v_pk_mul_f32 v[122:123], v[122:123], v[10:11] op_sel:[0,1] op_sel_hi:[1,1]\n\t"
      "v_mfma_f32_16x16x16_bf16 v[116:119], v[154:155], v[18:19], v[116:119]\n\t"
      "buffer_load_dwordx4 a[180:183], v248, s[36:39], s69 offen offset:1024  sc1\n\t"
      "v_pk_mul_f32 v[124:125], v[124:125], v[10:11] op_sel:[0,1] op_sel_hi:[1,1]\n\t"
      "v_pk_mul_f32 v[126:127], v[126:127], v[10:11] op_sel:[0,1] op_sel_hi:[1,1]\n\t"
      "v_cvt_pk_bf16_f32 v24, v96, v97\n\t"
      "v_cvt_pk_bf16_f32 v25, v98, v99\n\t"
      "v_cvt_pk_bf16_f32 v26, v100, v101\n\t"
      "buffer_load_dwordx4 a[184:187], v248, s[36:39], s69 offen offset:2048  sc1\n\t"
      "v_cvt_pk_bf16_f32 v27, v102, v103\n\t"
      "ds_write_b128 v254, v[24:27] offset:1024\n\t"
      "v_mfma_f32_16x16x16_bf16 v[120:123], v[156:157], v[18:19], v[120:123]\n\t"
      "v_mfma_f32_16x16x16_bf16 v[124:127], v[158:159], v[18:19], v[124:127]\n\t"
      "buffer_load_dwordx4 a[188:191], v248, s[36:39], s69 offen offset:3072  sc1\n\t"
      "v_cvt_pk_bf16_f32 v28, v104, v105\n\t"
      "v_cvt_pk_bf16_f32 v29, v106, v107\n\t"
      "v_cvt_pk_bf16_f32 v30, v108, v109\n\t"
      "v_cvt_pk_bf16_f32 v31, v110, v111\n\t"
      "ds_write_b128 v254, v[28:31] offset:3072\n\t"
      "v_cvt_pk_bf16_f32 v24, v112, v113\n\t"
      "v_cvt_pk_bf16_f32 v25, v114, v115\n\t"
      "v_cvt_pk_bf16_f32 v26, v116, v117\n\t"
      "v_cvt_pk_bf16_f32 v27, v118, v119\n\t"
      "ds_write_b128 v254, v[24:27] offset:5120\n\t"
      "v_cvt_pk_bf16_f32 v28, v120, v121\n\t"
      "v_cvt_pk_bf16_f32 v29, v122, v123\n\t"
      "v_cvt_pk_bf16_f32 v30, v124, v125\n\t"
      "v_cvt_pk_bf16_f32 v31, v126, v127\n\t"
      "ds_write_b128 v254, v[28:31] offset:7168\n\t"
      "s_waitcnt vmcnt(40) lgkmcnt(0)\n\t"
      "s_barrier\n\t"
      "s_branch WO%=\n\t"
      ".p2align 5\n\t"
      "WB1%=:\n\t"
      "v_mfma_f32_16x16x32_bf16 v[0:3], v[128:131], a[160:163], 0\n\t"
      "v_mfma_f32_16x16x32_bf16 v[4:7], v[128:131], a[164:167], 0\n\t"
      "v_mfma_f32_16x16x32_bf16 v[0:3], v[132:135], a[168:171], v[0:3]\n\t"
      "v_mfma_f32_16x16x32_bf16 v[4:7], v[132:135], a[172:175], v[4:7]\n\t"
      "v_mfma_f32_16x16x32_bf16 v[0:3], v[136:139], a[176:179], v[0:3]\n\t"
      "v_mfma_f32_16x16x32_bf16 v[4:7], v[136:139], a[180:183], v[4:7]\n\t"
      "v_mfma_f32_16x16x32_bf16 v[0:3], v[140:143], a[184:187], v[0:3]\n\t"
      "v_mfma_f32_16x16x32_bf16 v[4:7], v[140:143], a[188:191], v[4:7]\n\t"
      "s_add_u32 s51, s51, 262144\n\t"
      "s_add_u32 s52, s52, 262144\n\t"
      "s_add_u32 s69, s50, 8192\n\t"
      "s_add_u32 m0, s61, 0\n\t"
      "s_nop 0\n\t"
      "buffer_load_dwordx4 v250, s[36:39], s69 offen sc1 lds\n\t"
      "s_nop 0\n\t"
      "v_max3_f32 v8, v0, v1, v2\n\t"
      "v_max3_f32 v8, v8, v3, v234\n\t"
      "v_max3_f32 v9, v4, v5, v6\n\t"
      "v_max3_f32 v9, v9, v7, v235\n\t"
      "s_nop 1\n\t"
      "v_permlane16_swap_b32 v8, v9\n\t"
      "v_max_f32 v8, v8, v9\n\t"
      "v_mov_b32 v9, v8\n\t"
      "s_nop 1\n\t"
      "v_permlane16_swap_b32 v8, v9\n\t"
      "v_pk_mul_f32 v[8:9], v[8:9], s[86:87]\n\t"
      "v_pk_fma_f32 v[10:11], v[234:235], s[86:87], v[8:9] neg_lo:[0,0,1] neg_hi:[0,0,1]\n\t"
      "buffer_load_dwordx4 v250, s[36:39], s69 offen offset:1024 sc1 lds\n\t"
      "v_pk_fma_f32 v[0:1], v[0:1], s[86:87], v[8:9] op_sel:[0,0,0] op_sel_hi:[1,1,0] neg_lo:[0,0,1] neg_hi:[0,0,1]\n\t"
      "v_pk_fma_f32 v[2:3], v[2:3], s[86:87], v[8:9] op_sel:[0,0,0] op_sel_hi:[1,1,0] neg_lo:[0,0,1] neg_hi:[0,0,1]\n\t"
      "v_pk_fma_f32 v[4:5], v[4:5], s[86:87], v[8:9] op_sel:[0,0,1] op_sel_hi:[1,1,1] neg_lo:[0,0,1] neg_hi:[0,0,1]\n\t"
      "v_pk_fma_f32 v[6:7], v[6:7], s[86:87], v[8:9] op_sel:[0,0,1] op_sel_hi:[1,1,1] neg_lo:[0,0,1] neg_hi:[0,0,1]\n\t"
      "v_exp_f32 v10, v10\n\t"
      "v_exp_f32 v11, v11\n\t"
      "v_exp_f32 v0, v0\n\t"
      "buffer_load_dwordx4 v250, s[36:39], s69 offen offset:2048 sc1 lds\n\t"
      "v_exp_f32 v1, v1\n\t"
      "v_exp_f32 v2, v2\n\t"
      "v_exp_f32 v3, v3\n\t"
      "v_exp_f32 v4, v4\n\t"
      "v_exp_f32 v5, v5\n\t"
      "buffer_load_dwordx4 v250, s[36:39], s69 offen offset:3072 sc1 lds\n\t"
      "v_exp_f32 v6, v6\n\t"
      "v_exp_f32 v7, v7\n\t"
      "v_pk_mul_f32 v[12:13], v[236:237], v[10:11]\n\t"
      "v_pk_add_f32 v[14:15], v[0:1], v[2:3]\n\t"
      "v_pk_add_f32 v[16:17], v[4:5], v[6:7]\n\t"
      "v_add_f32 v14, v14, v15\n\t"
      "v_add_f32 v15, v16, v17\n\t"
      "v_fma_f32 v12, v14, 0.5, v12\n\t"
      "s_add_u32 s69, s50, 12288\n\t"
      "s_add_u32 m0, s61, 4096\n\t"
      "s_nop 0\n\t"
      "buffer_load_dwordx4 v250, s[36:39], s69 offen sc1 lds\n\t"
      "v_fma_f32 v13, v15, 0.5, v13\n\t"
      "s_nop 1\n\t"
      "v_permlane16_swap_b32 v12, v13\n\t"
      "v_add_f32 v12, v12, v13\n\t"
      "v_mov_b32 v13, v12\n\t"
      "s_nop 1\n\t"
      "v_permlane32_swap_b32 v12, v13\n\t"
      "v_add_f32 v12, v12, v13\n\t"
      "v_mov_b32 v13, v12\n\t"
      "s_nop 1\n\t"
      "v_permlane16_swap_b32 v12, v13\n\t"
      "v_rcp_f32 v12, v12\n\t"
      "buffer_load_dwordx4 v250, s[36:39], s69 offen offset:1024 sc1 lds\n\t"
      "v_rcp_f32 v13, v13\n\t"
      "s_nop 0\n\t"
      "v_pk_mul_f32 v[10:11], v[10:11], v[12:13]\n\t"
      "v_pk_mul_f32 v[12:13], v[12:13], 0.5 op_sel_hi:[1,0]\n\t"
      "v_pk_mul_f32 v[0:1], v[0:1], v[12:13] op_sel:[0,0] op_sel_hi:[1,0]\n\t"
      "v_pk_mul_f32 v[2:3], v[2:3], v[12:13] op_sel:[0,0] op_sel_hi:[1,0]\n\t"
      "v_pk_mul_f32 v[4:5], v[4:5], v[12:13] op_sel:[0,1] op_sel_hi:[1,1]\n\t"
      "v_pk_mul_f32 v[6:7], v[6:7], v[12:13] op_sel:[0,1] op_sel_hi:[1,1]\n\t"
      "v_cvt_pk_bf16_f32 v16, v0, v1\n\t"
      "buffer_load_dwordx4 v250, s[36:39], s69 offen offset:2048 sc1 lds\n\t"
      "v_cvt_pk_bf16_f32 v17, v2, v3\n\t"
      "v_cvt_pk_bf16_f32 v18, v4, v5\n\t"
      "v_cvt_pk_bf16_f32 v19, v6, v7\n\t"
      "v_pk_mul_f32 v[64:65], v[64:65], v[10:11] op_sel:[0,0] op_sel_hi:[1,0]\n\t"
      "v_pk_mul_f32 v[66:67], v[66:67], v[10:11] op_sel:[0,0] op_sel_hi:[1,0]\n\t"
      "v_pk_mul_f32 v[68:69], v[68:69], v[10:11] op_sel:[0,0] op_sel_hi:[1,0]\n\t"
      "v_pk_mul_f32 v[70:71], v[70:71], v[10:11] op_sel:[0,0] op_sel_hi:[1,0]\n\t"
      "v_mfma_f32_16x16x16_bf16 v[64:67], v[144:145], v[16:17], v[64:67]\n\t"
      "v_pk_mul_f32 v[72:73], v[72:73], v[10:11] op_sel:[0,0] op_sel_hi:[1,0]\n\t"
      "buffer_load_dwordx4 v250, s[36:39], s69 offen offset:3072 sc1 lds\n\t"
      "v_pk_mul_f32 v[74:75], v[74:75], v[10:11] op_sel:[0,0] op_sel_hi:[1,0]\n\t"
      "v_mfma_f32_16x16x16_bf16 v[68:71], v[146:147], v[16:17], v[68:71]\n\t"
      "v_pk_mul_f32 v[76:77], v[76:77], v[10:11] op_sel:[0,0] op_sel_hi:[1,0]\n\t"
      "v_pk_mul_f32 v[78:79], v[78:79], v[10:11] op_sel:[0,0] op_sel_hi:[1,0]\n\t"
      "v_mfma_f32_16x16x16_bf16 v[72:75], v[148:149], v[16:17], v[72:75]\n\t"
      "v_pk_mul_f32 v[80:81], v[80:81], v[10:11] op_sel:[0,0] op_sel_hi:[1,0]\n\t"
      "v_pk_mul_f32 v[82:83], v[82:83], v[10:11] op_sel:[0,0] op_sel_hi:[1,0]\n\t"
      "v_mfma_f32_16x16x16_bf16 v[76:79], v[150:151], v[16:17], v[76:79]\n\t"
      "s_add_u32 s69, s50, 16384\n\t"
      "buffer_load_dwordx4 a[160:163], v248, s[36:39], s69 offen  sc1\n\t"
      "v_pk_mul_f32 v[84:85], v[84:85], v[10:11] op_sel:[0,0] op_sel_hi:[1,0]\n\t"
      "v_pk_mul_f32 v[86:87], v[86:87], v[10:11] op_sel:[0,0] op_sel_hi:[1,0]\n\t"
      "v_mfma_f32_16x16x16_bf16 v[80:83], v[152:153], v[16:17], v[80:83]\n\t"
      "v_pk_mul_f32 v[88:89], v[88:89], v[10:11] op_sel:[0,0] op_sel_hi:[1,0]\n\t"
      "v_pk_mul_f32 v[90:91], v[90:91], v[10:11] op_sel:[0,0] op_sel_hi:[1,0]\n\t"
      "v_mfma_f32_16x16x16_bf16 v[84:87], v[154:155], v[16:17], v[84:87]\n\t"
      "v_pk_mul_f32 v[92:93], v[92:93], v[10:11] op_sel:[0,0] op_sel_hi:[1,0]\n\t"
      "buffer_load_dwordx4 a[164:167], v248, s[36:39], s69 offen offset:1024  sc1\n\t"
      "v_pk_mul_f32 v[94:95], v[94:95], v[10:11] op_sel:[0,0] op_sel_hi:[1,0]\n\t"
      "v_cvt_pk_bf16_f32 v24, v64, v65\n\t"
      "v_cvt_pk_bf16_f32 v25, v66, v67\n\t"
      "v_cvt_pk_bf16_f32 v26, v68, v69\n\t"
      "v_cvt_pk_bf16_f32 v27, v70, v71\n\t"
      "ds_write_b128 v254, v[24:27]\n\t"
      "v_mfma_f32_16x16x16_bf16 v[88:91], v[156:157], v[16:17], v[88:91]\n\t"
      "v_pk_mul_f32 v[96:97], v[96:97], v[10:11] op_sel:[0,1] op_sel_hi:[1,1]\n\t"
      "buffer_load_dwordx4 a[168:171], v248, s[36:39], s69 offen offset:2048  sc1\n\t"
      "v_pk_mul_f32 v[98:99], v[98:99], v[10:11] op_sel:[0,1] op_sel_hi:[1,1]\n\t"
      "v_mfma_f32_16x16x16_bf16 v[92:95], v[158:159], v[16:17], v[92:95]\n\t"
      "v_pk_mul_f32 v[100:101], v[100:101], v[10:11] op_sel:[0,1] op_sel_hi:[1,1]\n\t"
      "v_pk_mul_f32 v[102:103], v[102:103], v[10:11] op_sel:[0,1] op_sel_hi:[1,1]\n\t"
      "v_cvt_pk_bf16_f32 v28, v72, v73\n\t"
      "v_cvt_pk_bf16_f32 v29, v74, v75\n\t"
      "v_cvt_pk_bf16_f32 v30, v76, v77\n\t"
      "v_cvt_pk_bf16_f32 v31, v78, v79\n\t"
      "ds_write_b128 v254, v[28:31] offset:2048\n\t"
      "buffer_load_dwordx4 a[172:175], v248, s[36:39], s69 offen offset:3072  sc1\n\t"
      "v_mfma_f32_16x16x16_bf16 v[96:99], v[144:145], v[18:19], v[96:99]\n\t"
      "v_pk_mul_f32 v[104:105], v[104:105], v[10:11] op_sel:[0,1] op_sel_hi:[1,1]\n\t"
      "v_pk_mul_f32 v[106:107], v[106:107], v[10:11] op_sel:[0,1] op_sel_hi:[1,1]\n\t"
      "v_mfma_f32_16x16x16_bf16 v[100:103], v[146:147], v[18:19], v[100:103]\n\t"
      "v_pk_mul_f32 v[108:109], v[108:109], v[10:11] op_sel:[0,1] op_sel_hi:[1,1]\n\t"
      "v_pk_mul_f32 v[110:111], v[110:111], v[10:11] op_sel:[0,1] op_sel_hi:[1,1]\n\t"
      "v_cvt_pk_bf16_f32 v24, v80, v81\n\t"
      "v_cvt_pk_bf16_f32 v25, v82, v83\n\t"
      "s_add_u32 s69, s50, 20480\n\t"
      "buffer_load_dwordx4 a[176:179], v248, s[36:39], s69 offen  sc1\n\t"
      "v_cvt_pk_bf16_f32 v26, v84, v85\n\t"
      "v_cvt_pk_bf16_f32 v27, v86, v87\n\t"
      "ds_write_b128 v254, v[24:27] offset:4096\n\t"
      "v_mfma_f32_16x16x16_bf16 v[104:107], v[148:149], v[18:19], v[104:107]\n\t"
      "v_pk_mul_f32 v[112:113], v[112:113], v[10:11] op_sel:[0,1] op_sel_hi:[1,1]\n\t"
      "v_pk_mul_f32 v[114:115], v[114:115], v[10:11] op_sel:[0,1] op_sel_hi:[1,1]\n\t"
      "v_mfma_f32_16x16x16_bf16 v[108:111], v[150:151], v[18:19], v[108:111]\n\t"
      "buffer_load_dwordx4 a[180:183], v248, s[36:39], s69 offen offset:1024  sc1\n\t"
      "v_pk_mul_f32 v[116:117], v[116:117], v[10:11] op_sel:[0,1] op_sel_hi:[1,1]\n\t"
      "v_pk_mul_f32 v[118:119], v[118:119], v[10:11] op_sel:[0,1] op_sel_hi:[1,1]\n\t"
      "v_cvt_pk_bf16_f32 v28, v88, v89\n\t"
      "v_cvt_pk_bf16_f32 v29, v90, v91\n\t"
      "v_cvt_pk_bf16_f32 v30, v92, v93\n\t"
      "v_cvt_pk_bf16_f32 v31, v94, v95\n\t"
      "ds_write_b128 v254, v[28:31] offset:6144\n\t"
      "v_mfma_f32_16x16x16_bf16 v[112:115], v[152:153], v[18:19], v[112:115]\n\t"
      "v_pk_mul_f32 v[120:121], v[120:121], v[10:11] op_sel:[0,1] op_sel_hi:[1,1]\n\t"
      "buffer_load_dwordx4 a[184:187], v248, s[36:39], s69 offen offset:2048  sc1\n\t"
      "v_pk_mul_f32 v[122:123], v[122:123], v[10:11] op_sel:[0,1] op_sel_hi:[1,1]\n\t"
      "v_mfma_f32_16x16x16_bf16 v[116:119], v[154:155], v[18:19], v[116:119]\n\t"
      "v_pk_mul_f32 v[124:125], v[124:125], v[10:11] op_sel:[0,1] op_sel_hi:[1,1]\n\t"
      "v_pk_mul_f32 v[126:127], v[126:127], v[10:11] op_sel:[0,1] op_sel_hi:[1,1]\n\t"
      "v_cvt_pk_bf16_f32 v24, v96, v97\n\t"
      "v_cvt_pk_bf16_f32 v25, v98, v99\n\t"
      "v_cvt_pk_bf16_f32 v26, v100, v101\n\t"
      "v_cvt_pk_bf16_f32 v27, v102, v103\n\t"
      "ds_write_b128 v254, v[24:27] offset:1024\n\t"
      "buffer_load_dwordx4 a[188:191], v248, s[36:39], s69 offen offset:3072  sc1\n\t"
      "v_mfma_f32_16x16x16_bf16 v[120:123], v[156:157], v[18:19], v[120:123]\n\t"
      "v_mfma_f32_16x16x16_bf16 v[124:127], v[158:159], v[18:19], v[124:127]\n\t"
      "v_cvt_pk_bf16_f32 v28, v104, v105\n\t"
      "v_cvt_pk_bf16_f32 v29, v106, v107\n\t"
      "v_cvt_pk_bf16_f32 v30, v108, v109\n\t"
      "v_cvt_pk_bf16_f32 v31, v110, v111\n\t"
      "ds_write_b128 v254, v[28:31] offset:3072\n\t"
      "v_cvt_pk_bf16_f32 v24, v112, v113\n\t"
      "v_cvt_pk_bf16_f32 v25, v114, v115\n\t"
      "v_cvt_pk_bf16_f32 v26, v116, v117\n\t"
      "v_cvt_pk_bf16_f32 v27, v118, v119\n\t"
      "ds_write_b128 v254, v[24:27] offset:5120\n\t"
      "v_cvt_pk_bf16_f32 v28, v120, v121\n\t"
      "v_cvt_pk_bf16_f32 v29, v122, v123\n\t"
      "v_cvt_pk_bf16_f32 v30, v124, v125\n\t"
      "v_cvt_pk_bf16_f32 v31, v126, v127\n\t"
      "ds_write_b128 v254, v[28:31] offset:7168\n\t"
      "s_waitcnt vmcnt(40) lgkmcnt(0)\n\t"
      "s_barrier\n\t"
      "s_branch WO%=\n\t"
      ".p2align 5\n\t"
      "WOL1%=:\n\t"
      "buffer_load_dwordx4 v[128:131], v252, s[36:39], s51 offen sc1\n\t"
      "s_waitcnt lgkmcnt(11)\n\t"
      "v_mfma_f32_16x16x32_bf16 v[0:3], a[0:3], v[64:67], 0\n\t"
      "v_mfma_f32_16x16x32_bf16 v[4:7], a[64:67], v[64:67], 0\n\t"
      "ds_read_b128 v[112:115], v255 offset:12288\n\t"
      "s_waitcnt lgkmcnt(11)\n\t"
      "v_mfma_f32_16x16x32_bf16 v[8:11], a[0:3], v[68:71], 0\n\t"
      "v_mfma_f32_16x16x32_bf16 v[12:15], a[64:67], v[68:71], 0\n\t"
      "ds_read_b128 v[116:119], v255 offset:13312\n\t"
      "s_waitcnt lgkmcnt(11)\n\t"
      "v_mfma_f32_16x16x32_bf16 v[0:3], a[4:7], v[72:75], v[0:3]\n\t"
      "v_mfma_f32_16x16x32_bf16 v[4:7], a[68:71], v[72:75], v[4:7]\n\t"
      "ds_read_b128 v[120:123], v255 offset:14336\n\t"
      "s_waitcnt lgkmcnt(11)\n\t"
      "v_mfma_f32_16x16x32_bf16 v[8:11], a[4:7], v[76:79], v[8:11]\n\t"
      "v_mfma_f32_16x16x32_bf16 v[12:15], a[68:71], v[76:79], v[12:15]\n\t"
      "buffer_load_dwordx4 v[132:135], v252, s[36:39], s51 offen offset:512 sc1\n\t"
      "ds_read_b128 v[124:127], v255 offset:15360\n\t"
      "s_waitcnt lgkmcnt(11)\n\t"
      "v_mfma_f32_16x16x32_bf16 v[0:3], a[8:11], v[80:83], v[0:3]\n\t"
      "v_mfma_f32_16x16x32_bf16 v[4:7], a[72:75], v[80:83], v[4:7]\n\t"
      "ds_read_b128 v[64:67], v255 offset:16384\n\t"
      "s_waitcnt lgkmcnt(11)\n\t"
      "v_mfma_f32_16x16x32_bf16 v[8:11], a[8:11], v[84:87], v[8:11]\n\t"
      "v_mfma_f32_16x16x32_bf16 v[12:15], a[72:75], v[84:87], v[12:15]\n\t"
      "ds_read_b128 v[68:71], v255 offset:17408\n\t"
      "s_waitcnt lgkmcnt(11)\n\t"
      "v_mfma_f32_16x16x32_bf16 v[0:3], a[12:15], v[88:91], v[0:3]\n\t"
      "v_mfma_f32_16x16x32_bf16 v[4:7], a[76:79], v[88:91], v[4:7]\n\t"
      "ds_read_b128 v[72:75], v255 offset:18432\n\t"
      "s_waitcnt lgkmcnt(11)\n\t"
      "v_mfma_f32_16x16x32_bf16 v[8:11], a[12:15], v[92:95], v[8:11]\n\t"
      "v_mfma_f32_16x16x32_bf16 v[12:15], a[76:79], v[92:95], v[12:15]\n\t"
      "buffer_load_dwordx4 v[136:139], v252, s[36:39], s51 offen offset:1024 sc1\n\t"
      "ds_read_b128 v[76:79], v255 offset:19456\n\t"
      "s_waitcnt lgkmcnt(11)\n\t"
      "v_mfma_f32_16x16x32_bf16 v[0:3], a[16:19], v[96:99], v[0:3]\n\t"
      "v_mfma_f32_16x16x32_bf16 v[4:7], a[80:83], v[96:99], v[4:7]\n\t"
      "ds_read_b128 v[80:83], v255 offset:20480\n\t"
      "s_waitcnt lgkmcnt(11)\n\t"
      "v_mfma_f32_16x16x32_bf16 v[8:11], a[16:19], v[100:103], v[8:11]\n\t"
      "v_mfma_f32_16x16x32_bf16 v[12:15], a[80:83], v[100:103], v[12:15]\n\t"
      "ds_read_b128 v[84:87], v255 offset:21504\n\t"
      "s_waitcnt lgkmcnt(11)\n\t"
      "v_mfma_f32_16x16x32_bf16 v[0:3], a[20:23], v[104:107], v[0:3]\n\t"
      "v_mfma_f32_16x16x32_bf16 v[4:7], a[84:87], v[104:107], v[4:7]\n\t"
      "ds_read_b128 v[88:91], v255 offset:22528\n\t"
      "s_waitcnt lgkmcnt(11)\n\t"
      "v_mfma_f32_16x16x32_bf16 v[8:11], a[20:23], v[108:111], v[8:11]\n\t"
      "v_mfma_f32_16x16x32_bf16 v[12:15], a[84:87], v[108:111], v[12:15]\n\t"
      "buffer_load_dwordx4 v[140:143], v252, s[36:39], s51 offen offset:1536 sc1\n\t"
      "ds_read_b128 v[92:95], v255 offset:23552\n\t"
      "s_waitcnt lgkmcnt(11)\n\t"
      "v_mfma_f32_16x16x32_bf16 v[0:3], a[24:27], v[112:115], v[0:3]\n\t"
      "v_mfma_f32_16x16x32_bf16 v[4:7], a[88:91], v[112:115], v[4:7]\n\t"
      "ds_read_b128 v[96:99], v255 offset:24576\n\t"
      "s_waitcnt lgkmcnt(11)\n\t"
      "v_mfma_f32_16x16x32_bf16 v[8:11], a[24:27], v[116:119], v[8:11]\n\t"
      "v_mfma_f32_16x16x32_bf16 v[12:15], a[88:91], v[116:119], v[12:15]\n\t"
      "ds_read_b128 v[100:103], v255 offset:25600\n\t"
      "s_waitcnt lgkmcnt(11)\n\t"
      "v_mfma_f32_16x16x32_bf16 v[0:3], a[28:31], v[120:123], v[0:3]\n\t"
      "v_mfma_f32_16x16x32_bf16 v[4:7], a[92:95], v[120:123], v[4:7]\n\t"
      "ds_read_b128 v[104:107], v255 offset:26624\n\t"
      "s_waitcnt lgkmcnt(11)\n\t"
      "v_mfma_f32_16x16x32_bf16 v[8:11], a[28:31], v[124:127], v[8:11]\n\t"
      "buffer_load_dwordx4 v[144:147], v253, s[36:39], s52 offen sc1\n\t"
      "v_mfma_f32_16x16x32_bf16 v[12:15], a[92:95], v[124:127], v[12:15]\n\t"
      "ds_read_b128 v[108:111], v255 offset:27648\n\t"
      "s_waitcnt lgkmcnt(11)\n\t"
      "v_mfma_f32_16x16x32_bf16 v[0:3], a[32:35], v[64:67], v[0:3]\n\t"
      "v_mfma_f32_16x16x32_bf16 v[4:7], a[96:99], v[64:67], v[4:7]\n\t"
      "ds_read_b128 v[112:115], v255 offset:28672\n\t"
      "s_waitcnt lgkmcnt(11)\n\t"
      "v_mfma_f32_16x16x32_bf16 v[8:11], a[32:35], v[68:71], v[8:11]\n\t"
      "v_mfma_f32_16x16x32_bf16 v[12:15], a[96:99], v[68:71], v[12:15]\n\t"
      "ds_read_b128 v[116:119], v255 offset:29696\n\t"
      "s_waitcnt lgkmcnt(11)\n\t"
      "v_mfma_f32_16x16x32_bf16 v[0:3], a[36:39], v[72:75], v[0:3]\n\t"
      "v_mfma_f32_16x16x32_bf16 v[4:7], a[100:103], v[72:75], v[4:7]\n\t"
      "ds_read_b128 v[120:123], v255 offset:30720\n\t"
      "s_waitcnt lgkmcnt(11)\n\t"
      "v_mfma_f32_16x16x32_bf16 v[8:11], a[36:39], v[76:79], v[8:11]\n\t"
      "buffer_load_dwordx4 v[148:151], v253, s[36:39], s52 offen offset:512 sc1\n\t"
      "v_mfma_f32_16x16x32_bf16 v[12:15], a[100:103], v[76:79], v[12:15]\n\t"
      "ds_read_b128 v[124:127], v255 offset:31744\n\t"
      "s_waitcnt lgkmcnt(11)\n\t"
      "v_mfma_f32_16x16x32_bf16 v[0:3], a[40:43], v[80:83], v[0:3]\n\t"
      "v_mfma_f32_16x16x32_bf16 v[4:7], a[104:107], v[80:83], v[4:7]\n\t"
      "s_waitcnt lgkmcnt(10)\n\t"
      "v_mfma_f32_16x16x32_bf16 v[8:11], a[40:43], v[84:87], v[8:11]\n\t"
      "v_mfma_f32_16x16x32_bf16 v[12:15], a[104:107], v[84:87], v[12:15]\n\t"
      "s_waitcnt lgkmcnt(9)\n\t"
      "v_mfma_f32_16x16x32_bf16 v[0:3], a[44:47], v[88:91], v[0:3]\n\t"
      "v_mfma_f32_16x16x32_bf16 v[4:7], a[108:111], v[88:91], v[4:7]\n\t"
      "s_waitcnt lgkmcnt(8)\n\t"
      "v_mfma_f32_16x16x32_bf16 v[8:11], a[44:47], v[92:95], v[8:11]\n\t"
      "buffer_load_dwordx4 v[152:155], v253, s[36:39], s52 offen offset:1024 sc1\n\t"
      "v_mfma_f32_16x16x32_bf16 v[12:15], a[108:111], v[92:95], v[12:15]\n\t"
      "s_waitcnt lgkmcnt(7)\n\t"
      "v_mfma_f32_16x16x32_bf16 v[0:3], a[48:51], v[96:99], v[0:3]\n\t"
      "v_mfma_f32_16x16x32_bf16 v[4:7], a[112:115], v[96:99], v[4:7]\n\t"
      "s_waitcnt lgkmcnt(6)\n\t"
      "v_mfma_f32_16x16x32_bf16 v[8:11], a[48:51], v[100:103], v[8:11]\n\t"
      "v_mfma_f32_16x16x32_bf16 v[12:15], a[112:115], v[100:103], v[12:15]\n\t"
      "s_waitcnt lgkmcnt(5)\n\t"
      "v_mfma_f32_16x16x32_bf16 v[0:3], a[52:55], v[104:107], v[0:3]\n\t"
      "v_mfma_f32_16x16x32_bf16 v[4:7], a[116:119], v[104:107], v[4:7]\n\t"
      "s_waitcnt lgkmcnt(4)\n\t"
      "v_mfma_f32_16x16x32_bf16 v[8:11], a[52:55], v[108:111], v[8:11]\n\t"
      "v_mfma_f32_16x16x32_bf16 v[12:15], a[116:119], v[108:111], v[12:15]\n\t"
      "buffer_load_dwordx4 v[156:159], v253, s[36:39], s52 offen offset:1536 sc1\n\t"
      "s_waitcnt lgkmcnt(3)\n\t"
      "v_mfma_f32_16x16x32_bf16 v[0:3], a[56:59], v[112:115], v[0:3]\n\t"
      "v_mfma_f32_16x16x32_bf16 v[4:7], a[120:123], v[112:115], v[4:7]\n\t"
      "s_waitcnt lgkmcnt(2)\n\t"
      "v_mfma_f32_16x16x32_bf16 v[8:11], a[56:59], v[116:119], v[8:11]\n\t"
      "v_mfma_f32_16x16x32_bf16 v[12:15], a[120:123], v[116:119], v[12:15]\n\t"
      "s_waitcnt lgkmcnt(1)\n\t"
      "v_mfma_f32_16x16x32_bf16 v[0:3], a[60:63], v[120:123], v[0:3]\n\t"
      "v_mfma_f32_16x16x32_bf16 v[4:7], a[124:127], v[120:123], v[4:7]\n\t"
      "s_waitcnt lgkmcnt(0)\n\t"
      "v_mfma_f32_16x16x32_bf16 v[8:11], a[60:63], v[124:127], v[8:11]\n\t"
      "v_mfma_f32_16x16x32_bf16 v[12:15], a[124:127], v[124:127], v[12:15]\n\t"
      "v_xor_b32 v254, 0x8000, v254\n\t"
      "v_xor_b32 v255, 0x8000, v255\n\t"
      "s_add_u32 s69, s53, 0x4000\n\t"
      "s_add_u32 s48, s48, 4\n\t"
      "s_waitcnt vmcnt(40)\n\t"
      "v_cvt_pk_bf16_f32 v0, v0, v1\n\t"
      "v_cvt_pk_bf16_f32 v1, v2, v3\n\t"
      "v_cvt_pk_bf16_f32 v2, v4, v5\n\t"
      "v_cvt_pk_bf16_f32 v3, v6, v7\n\t"
      "v_min_u32 v0, -2, v0\n\t"
      "v_min_u32 v1, -2, v1\n\t"
      "v_min_u32 v2, -2, v2\n\t"
      "v_min_u32 v3, -2, v3\n\t"
      "buffer_store_dwordx4 v[0:3], v238, s[36:39], s53 offen sc1\n\t"
      "v_cvt_pk_bf16_f32 v8, v8, v9\n\t"
      "v_cvt_pk_bf16_f32 v9, v10, v11\n\t"
      "v_cvt_pk_bf16_f32 v10, v12, v13\n\t"
      "v_cvt_pk_bf16_f32 v11, v14, v15\n\t"
      "v_min_u32 v8, -2, v8\n\t"
      "v_min_u32 v9, -2, v9\n\t"
      "v_min_u32 v10, -2, v10\n\t"
      "v_min_u32 v11, -2, v11\n\t"
      "buffer_store_dwordx4 v[8:11], v238, s[36:39], s69 offen sc1\n\t"
      "s_add_u32 s53, s53, 1048576\n\t"
      "s_nop 1\n\t"
      "s_branch WTOP%=\n\t"
      ".p2align 5\n\t"
      "WR2%=:\n\t"
      "v_cvt_pk_bf16_f32 v204, v204, v205\n\t"
      "v_and_b32 v205, s74, v204\n\t"
      "v_lshlrev_b32 v204, 16, v204\n\t"
      "v_add_f32 v205, 0x3727c5ac, v205\n\t"
      "v_pk_add_f32 v[40:41], v[192:193], v[204:205] op_sel_hi:[1,0] neg_lo:[0,1] neg_hi:[0,1]\n\t"
      "v_pk_add_f32 v[42:43], v[194:195], v[204:205] op_sel_hi:[1,0] neg_lo:[0,1] neg_hi:[0,1]\n\t"
      "v_pk_add_f32 v[44:45], v[196:197], v[204:205] op_sel_hi:[1,0] neg_lo:[0,1] neg_hi:[0,1]\n\t"
      "v_pk_add_f32 v[46:47], v[198:199], v[204:205] op_sel_hi:[1,0] neg_lo:[0,1] neg_hi:[0,1]\n\t"
      "v_cvt_pk_bf16_f32 v205, v205, v205\n\t"
      "v_and_b32 v205, s74, v205\n\t"
      "v_sqrt_f32 v205, v205\n\t"
      "v_cvt_pk_bf16_f32 v40, v40, v41\n\t"
      "v_cvt_pk_bf16_f32 v42, v42, v43\n\t"
      "v_cvt_pk_bf16_f32 v44, v44, v45\n\t"
      "v_cvt_pk_bf16_f32 v46, v46, v47\n\t"
      "v_cvt_pk_bf16_f32 v205, v205, v205\n\t"
      "v_and_b32 v205, s74, v205\n\t"
      "v_rcp_f32 v204, v205\n\t"
      "v_and_b32 v41, s74, v40\n\t"
      "v_lshlrev_b32 v40, 16, v40\n\t"
      "v_and_b32 v43, s74, v42\n\t"
      "v_lshlrev_b32 v42, 16, v42\n\t"
      "v_and_b32 v45, s74, v44\n\t"
      "v_lshlrev_b32 v44, 16, v44\n\t"
      "v_and_b32 v47, s74, v46\n\t"
      "v_lshlrev_b32 v46, 16, v46\n\t"
      "v_pk_mul_f32 v[40:41], v[40:41], v[204:205] op_sel_hi:[1,0]\n\t"
      "v_pk_mul_f32 v[42:43], v[42:43], v[204:205] op_sel_hi:[1,0]\n\t"
      "v_pk_mul_f32 v[44:45], v[44:45], v[204:205] op_sel_hi:[1,0]\n\t"
      "v_pk_mul_f32 v[46:47], v[46:47], v[204:205] op_sel_hi:[1,0]\n\t"
      "v_cvt_pk_bf16_f32 v52, v40, v41\n\t"
      "v_cvt_pk_bf16_f32 v53, v42, v43\n\t"
      "v_cvt_pk_bf16_f32 v54, v44, v45\n\t"
      "v_cvt_pk_bf16_f32 v55, v46, v47\n\t"
      "v_and_b32 v33, s74, v52\n\t"
      "v_lshlrev_b32 v32, 16, v52\n\t"
      "v_and_b32 v35, s74, v53\n\t"
      "v_lshlrev_b32 v34, 16, v53\n\t"
      "v_and_b32 v37, s74, v54\n\t"
      "v_lshlrev_b32 v36, 16, v54\n\t"
      "v_and_b32 v39, s74, v55\n\t"
      "v_lshlrev_b32 v38, 16, v55\n\t"
      "v_pk_mul_f32 v[40:41], v[32:33], v[240:241]\n\t"
      "v_pk_fma_f32 v[40:41], v[34:35], v[242:243], v[40:41]\n\t"
      "v_pk_fma_f32 v[40:41], v[36:37], v[244:245], v[40:41]\n\t"
      "v_pk_fma_f32 v[40:41], v[38:39], v[246:247], v[40:41]\n\t"
      "v_add_f32 v40, v40, v41\n\t"
      "s_lshl_b32 s73, s49, 5\n\t"
      "s_add_u32 s73, s73, s60\n\t"
      "s_lshl_b32 s69, s73, 2\n\t"
      "s_add_u32 s69, s69, s57\n\t"
      "s_lshl_b32 s70, s73, 7\n\t"
      "s_add_u32 s70, s70, s57\n\t"
      "s_sub_u32 s70, s70, 212992\n\t"
      "s_cmp_ge_u32 s49, 56\n\t"
      "s_cselect_b32 s70, s70, s69\n\t"
      "v_add_f32_dpp v40, v40, v40 quad_perm:[1,0,3,2] row_mask:0xf bank_mask:0xf\n\t"
      "s_nop 1\n\t"
      "v_add_f32_dpp v40, v40, v40 quad_perm:[2,3,0,1] row_mask:0xf bank_mask:0xf\n\t"
      "s_nop 1\n\t"
      "v_add_f32_dpp v40, v40, v40 row_ror:4 row_mask:0xf bank_mask:0xf\n\t"
      "s_nop 1\n\t"
      "v_add_f32_dpp v40, v40, v40 row_ror:8 row_mask:0xf bank_mask:0xf\n\t"
      "v_mov_b32 v41, v40\n\t"
      "s_nop 1\n\t"
      "v_permlane16_swap_b32 v40, v41\n\t"
      "v_add_f32 v40, v40, v41\n\t"
      "v_mov_b32 v41, v40\n\t"
      "s_nop 1\n\t"
      "v_permlane32_swap_b32 v40, v41\n\t"
      "v_add_f32 v40, v40, v41\n\t"
      "v_cvt_pk_bf16_f32 v40, v40, v40\n\t"
      "v_and_b32 v40, s74, v40\n\t"
      "s_mov_b64 exec, 1\n\t"
      "buffer_store_dword v40, off, s[36:39], s70 sc1\n\t"
      "s_mov_b64 exec, -1\n\t"
      "v_pk_add_f32 v[192:193], v[192:193], v[32:33]\n\t"
      "v_pk_add_f32 v[194:195], v[194:195], v[34:35]\n\t"
      "v_pk_add_f32 v[196:197], v[196:197], v[36:37]\n\t"
      "v_pk_add_f32 v[198:199], v[198:199], v[38:39]\n\t"
      "v_readfirstlane_b32 s68, v40\n\t"
      "v_cvt_pk_bf16_f32 v56, v192, v193\n\t"
      "v_cvt_pk_bf16_f32 v57, v194, v195\n\t"
      "v_cvt_pk_bf16_f32 v58, v196, v197\n\t"
      "v_cvt_pk_bf16_f32 v59, v198, v199\n\t"
      "s_add_u32 s67, s69, 8192\n\t"
      "s_lshl_b32 s73, s73, 10\n\t"
      "s_add_u32 s69, s73, s58\n\t"
      "s_add_u32 s70, s73, s59\n\t"
      "buffer_store_dwordx2 v[56:57], v249, s[44:47], s73 offen sc1\n\t"
      "buffer_store_dwordx2 v[52:53], v249, s[36:39], s69 offen sc1\n\t"
      "buffer_store_dwordx2 v[200:201], v249, s[36:39], s70 offen sc1\n\t"
      "buffer_store_dwordx2 v[58:59], v249, s[44:47], s73 offen offset:512 sc1\n\t"
      "buffer_store_dwordx2 v[54:55], v249, s[36:39], s69 offen offset:512 sc1\n\t"
      "buffer_store_dwordx2 v[202:203], v249, s[36:39], s70 offen offset:512 sc1\n\t"
      "s_mov_b32 s49, s89\n\t"
      "s_add_u32 s89, s89, 16\n\t"
      "s_lshr_b32 s69, s89, 4\n\t"
      "s_cmp_eq_u32 s69, 3\n\t"
      "s_cselect_b32 s89, s93, s89\n\t"
      "s_cmp_ge_u32 s49, 48\n\t"
      "s_cselect_b32 s60, s94, s60\n\t"
      "s_cselect_b32 s54, s95, s54\n\t"
      "s_mov_b32 s65, 0\n\t"
      "s_branch WGO2%=\n\t"
      ".p2align 5\n\t"
      "WOM1%=:\n\t"
      "s_mov_b32 s65, 2\n\t"
      "s_cmp_lg_u32 s96, 0\n\t"
      "s_cbranch_scc1 WOL3%=\n\t"
      "WOL2%=:\n\t"
      "s_waitcnt lgkmcnt(11)\n\t"
      "v_mfma_f32_16x16x32_bf16 v[0:3], a[0:3], v[64:67], 0\n\t"
      "v_mfma_f32_16x16x32_bf16 v[16:19], a[224:227], v[160:163], 0\n\t"
      "v_mfma_f32_16x16x32_bf16 v[4:7], a[64:67], v[64:67], 0\n\t"
      "v_mfma_f32_16x16x32_bf16 v[20:23], a[228:231], v[160:163], 0\n\t"
      "v_and_b32 v193, s74, v224\n\t"
      "v_lshlrev_b32 v192, 16, v224\n\t"
      "ds_read_b128 v[112:115], v255 offset:12288\n\t"
      "s_waitcnt lgkmcnt(11)\n\t"
      "v_mfma_f32_16x16x32_bf16 v[8:11], a[0:3], v[68:71], 0\n\t"
      "v_mfma_f32_16x16x32_bf16 v[16:19], a[224:227], v[164:167], v[16:19]\n\t"
      "v_mfma_f32_16x16x32_bf16 v[12:15], a[64:67], v[68:71], 0\n\t"
      "v_mfma_f32_16x16x32_bf16 v[20:23], a[228:231], v[164:167], v[20:23]\n\t"
      "v_and_b32 v195, s74, v225\n\t"
      "v_lshlrev_b32 v194, 16, v225\n\t"
      "ds_read_b128 v[116:119], v255 offset:13312\n\t"
      "s_waitcnt lgkmcnt(11)\n\t"
      "v_mfma_f32_16x16x32_bf16 v[0:3], a[4:7], v[72:75], v[0:3]\n\t"
      "v_mfma_f32_16x16x32_bf16 v[16:19], a[224:227], v[168:171], v[16:19]\n\t"
      "v_mfma_f32_16x16x32_bf16 v[4:7], a[68:71], v[72:75], v[4:7]\n\t"
      "v_mfma_f32_16x16x32_bf16 v[20:23], a[228:231], v[168:171], v[20:23]\n\t"
      "v_and_b32 v197, s74, v226\n\t"
      "v_lshlrev_b32 v196, 16, v226\n\t"
      "ds_read_b128 v[120:123], v255 offset:14336\n\t"
      "s_waitcnt lgkmcnt(11)\n\t"
      "v_mfma_f32_16x16x32_bf16 v[8:11], a[4:7], v[76:79], v[8:11]\n\t"
      "v_mfma_f32_16x16x32_bf16 v[16:19], a[224:227], v[172:175], v[16:19]\n\t"
      "v_mfma_f32_16x16x32_bf16 v[12:15], a[68:71], v[76:79], v[12:15]\n\t"
      "v_mfma_f32_16x16x32_bf16 v[20:23], a[228:231], v[172:175], v[20:23]\n\t"
      "v_and_b32 v199, s74, v227\n\t"
      "v_lshlrev_b32 v198, 16, v227\n\t"
      "ds_read_b128 v[124:127], v255 offset:15360\n\t"
      "s_waitcnt lgkmcnt(11)\n\t"
      "v_mfma_f32_16x16x32_bf16 v[0:3], a[8:11], v[80:83], v[0:3]\n\t"
      "v_mfma_f32_16x16x32_bf16 v[16:19], a[224:227], v[176:179], v[16:19]\n\t"
      "v_mfma_f32_16x16x32_bf16 v[4:7], a[72:75], v[80:83], v[4:7]\n\t"
      "v_mfma_f32_16x16x32_bf16 v[20:23], a[228:231], v[176:179], v[20:23]\n\t"
      "ds_read_b128 v[64:67], v255 offset:16384\n\t"
      "s_waitcnt lgkmcnt(11)\n\t"
      "v_mfma_f32_16x16x32_bf16 v[8:11], a[8:11], v[84:87], v[8:11]\n\t"
      "v_mfma_f32_16x16x32_bf16 v[16:19], a[224:227], v[180:183], v[16:19]\n\t"
      "v_mfma_f32_16x16x32_bf16 v[12:15], a[72:75], v[84:87], v[12:15]\n\t"
      "v_mfma_f32_16x16x32_bf16 v[20:23], a[228:231], v[180:183], v[20:23]\n\t"
      "ds_read_b128 v[68:71], v255 offset:17408\n\t"
      "s_waitcnt lgkmcnt(11)\n\t"
      "v_mfma_f32_16x16x32_bf16 v[0:3], a[12:15], v[88:91], v[0:3]\n\t"
      "v_mfma_f32_16x16x32_bf16 v[16:19], a[224:227], v[184:187], v[16:19]\n\t"
      "v_mfma_f32_16x16x32_bf16 v[4:7], a[76:79], v[88:91], v[4:7]\n\t"
      "v_mfma_f32_16x16x32_bf16 v[20:23], a[228:231], v[184:187], v[20:23]\n\t"
      "ds_read_b128 v[72:75], v255 offset:18432\n\t"
      "s_waitcnt lgkmcnt(11)\n\t"
      "v_mfma_f32_16x16x32_bf16 v[8:11], a[12:15], v[92:95], v[8:11]\n\t"
      "v_mfma_f32_16x16x32_bf16 v[16:19], a[224:227], v[188:191], v[16:19]\n\t"
      "v_mfma_f32_16x16x32_bf16 v[12:15], a[76:79], v[92:95], v[12:15]\n\t"
      "v_mfma_f32_16x16x32_bf16 v[20:23], a[228:231], v[188:191], v[20:23]\n\t"
      "ds_read_b128 v[76:79], v255 offset:19456\n\t"
      "s_waitcnt lgkmcnt(11)\n\t"
      "v_mfma_f32_16x16x32_bf16 v[0:3], a[16:19], v[96:99], v[0:3]\n\t"
      "v_cvt_pk_bf16_f32 v32, v16, v17\n\t"
      "v_cvt_pk_bf16_f32 v34, v18, v19\n\t"
      "v_mfma_f32_16x16x32_bf16 v[4:7], a[80:83], v[96:99], v[4:7]\n\t"
      "ds_read_b128 v[80:83], v255 offset:20480\n\t"
      "s_waitcnt lgkmcnt(11)\n\t"
      "v_mfma_f32_16x16x32_bf16 v[8:11], a[16:19], v[100:103], v[8:11]\n\t"
      "v_cvt_pk_bf16_f32 v36, v20, v21\n\t"
      "v_cvt_pk_bf16_f32 v38, v22, v23\n\t"
      "v_mfma_f32_16x16x32_bf16 v[12:15], a[80:83], v[100:103], v[12:15]\n\t"
      "v_and_b32 v33, s74, v32\n\t"
      "v_lshlrev_b32 v32, 16, v32\n\t"
      "ds_read_b128 v[84:87], v255 offset:21504\n\t"
      "s_waitcnt lgkmcnt(11)\n\t"
      "v_mfma_f32_16x16x32_bf16 v[0:3], a[20:23], v[104:107], v[0:3]\n\t"
      "v_and_b32 v35, s74, v34\n\t"
      "v_lshlrev_b32 v34, 16, v34\n\t"
      "v_mfma_f32_16x16x32_bf16 v[4:7], a[84:87], v[104:107], v[4:7]\n\t"
      "v_and_b32 v37, s74, v36\n\t"
      "v_lshlrev_b32 v36, 16, v36\n\t"
      "ds_read_b128 v[88:91], v255 offset:22528\n\t"
      "s_waitcnt lgkmcnt(11)\n\t"
      "v_mfma_f32_16x16x32_bf16 v[8:11], a[20:23], v[108:111], v[8:11]\n\t"
      "v_and_b32 v39, s74, v38\n\t"
      "v_lshlrev_b32 v38, 16, v38\n\t"
      "v_mfma_f32_16x16x32_bf16 v[12:15], a[84:87], v[108:111], v[12:15]\n\t"
      "v_add_f32 v192, v192, v32\n\t"
      "v_add_f32 v193, v193, v33\n\t"
      "ds_read_b128 v[92:95], v255 offset:23552\n\t"
      "s_waitcnt lgkmcnt(11)\n\t"
      "v_mfma_f32_16x16x32_bf16 v[0:3], a[24:27], v[112:115], v[0:3]\n\t"
      "v_add_f32 v194, v194, v34\n\t"
      "v_add_f32 v195, v195, v35\n\t"
      "v_mfma_f32_16x16x32_bf16 v[4:7], a[88:91], v[112:115], v[4:7]\n\t"
      "v_add_f32 v196, v196, v36\n\t"
      "v_add_f32 v197, v197, v37\n\t"
      "ds_read_b128 v[96:99], v255 offset:24576\n\t"
      "s_waitcnt lgkmcnt(11)\n\t"
      "v_mfma_f32_16x16x32_bf16 v[8:11], a[24:27], v[116:119], v[8:11]\n\t"
      "v_add_f32 v198, v198, v38\n\t"
      "v_add_f32 v199, v199, v39\n\t"
      "v_mfma_f32_16x16x32_bf16 v[12:15], a[88:91], v[116:119], v[12:15]\n\t"
      "v_cvt_pk_bf16_f32 v200, v192, v193\n\t"
      "v_cvt_pk_bf16_f32 v201, v194, v195\n\t"
      "ds_read_b128 v[100:103], v255 offset:25600\n\t"
      "s_waitcnt lgkmcnt(11)\n\t"
      "v_mfma_f32_16x16x32_bf16 v[0:3], a[28:31], v[120:123], v[0:3]\n\t"
      "v_cvt_pk_bf16_f32 v202, v196, v197\n\t"
      "v_cvt_pk_bf16_f32 v203, v198, v199\n\t"
      "v_mfma_f32_16x16x32_bf16 v[4:7], a[92:95], v[120:123], v[4:7]\n\t"
      "v_and_b32 v193, s74, v200\n\t"
      "v_lshlrev_b32 v192, 16, v200\n\t"
      "ds_read_b128 v[104:107], v255 offset:26624\n\t"
      "s_waitcnt lgkmcnt(11)\n\t"
      "v_mfma_f32_16x16x32_bf16 v[8:11], a[28:31], v[124:127], v[8:11]\n\t"
      "v_and_b32 v195, s74, v201\n\t"
      "v_lshlrev_b32 v194, 16, v201\n\t"
      "v_mfma_f32_16x16x32_bf16 v[12:15], a[92:95], v[124:127], v[12:15]\n\t"
      "v_and_b32 v197, s74, v202\n\t"
      "v_lshlrev_b32 v196, 16, v202\n\t"
      "ds_read_b128 v[108:111], v255 offset:27648\n\t"
      "s_waitcnt lgkmcnt(11)\n\t"
      "v_mfma_f32_16x16x32_bf16 v[0:3], a[32:35], v[64:67], v[0:3]\n\t"
      "v_and_b32 v199, s74, v203\n\t"
      "v_lshlrev_b32 v198, 16, v203\n\t"
      "v_mfma_f32_16x16x32_bf16 v[4:7], a[96:99], v[64:67], v[4:7]\n\t"
      "v_add_f32 v32, v192, v196\n\t"
      "v_add_f32 v33, v193, v197\n\t"
      "ds_read_b128 v[112:115], v255 offset:28672\n\t"
      "s_waitcnt lgkmcnt(11)\n\t"
      "v_mfma_f32_16x16x32_bf16 v[8:11], a[32:35], v[68:71], v[8:11]\n\t"
      "v_add_f32 v34, v194, v198\n\t"
      "v_add_f32 v35, v195, v199\n\t"
      "v_mfma_f32_16x16x32_bf16 v[12:15], a[96:99], v[68:71], v[12:15]\n\t"
      "v_add_f32 v32, v32, v34\n\t"
      "v_add_f32 v33, v33, v35\n\t"
      "ds_read_b128 v[116:119], v255 offset:29696\n\t"
      "s_waitcnt lgkmcnt(11)\n\t"
      "v_mfma_f32_16x16x32_bf16 v[0:3], a[36:39], v[72:75], v[0:3]\n\t"
      "v_add_f32 v32, v32, v33\n\t"
      "v_mfma_f32_16x16x32_bf16 v[4:7], a[100:103], v[72:75], v[4:7]\n\t"
      "ds_read_b128 v[120:123], v255 offset:30720\n\t"
      "s_waitcnt lgkmcnt(11)\n\t"
      "v_mfma_f32_16x16x32_bf16 v[8:11], a[36:39], v[76:79], v[8:11]\n\t"
      "v_add_f32_dpp v32, v32, v32 quad_perm:[1,0,3,2] row_mask:0xf bank_mask:0xf\n\t"
      "v_mfma_f32_16x16x32_bf16 v[12:15], a[100:103], v[76:79], v[12:15]\n\t"
      "ds_read_b128 v[124:127], v255 offset:31744\n\t"
      "s_waitcnt lgkmcnt(11)\n\t"
      "v_mfma_f32_16x16x32_bf16 v[0:3], a[40:43], v[80:83], v[0:3]\n\t"
      "v_add_f32_dpp v32, v32, v32 quad_perm:[2,3,0,1] row_mask:0xf bank_mask:0xf\n\t"
      "v_mfma_f32_16x16x32_bf16 v[4:7], a[104:107], v[80:83], v[4:7]\n\t"
      "s_waitcnt lgkmcnt(10)\n\t"
      "v_mfma_f32_16x16x32_bf16 v[8:11], a[40:43], v[84:87], v[8:11]\n\t"
      "v_add_f32_dpp v32, v32, v32 row_ror:4 row_mask:0xf bank_mask:0xf\n\t"
      "v_mfma_f32_16x16x32_bf16 v[12:15], a[104:107], v[84:87], v[12:15]\n\t"
      "s_waitcnt lgkmcnt(9)\n\t"
      "v_mfma_f32_16x16x32_bf16 v[0:3], a[44:47], v[88:91], v[0:3]\n\t"
      "v_add_f32_dpp v32, v32, v32 row_ror:8 row_mask:0xf bank_mask:0xf\n\t"
      "v_mov_b32 v33, v32\n\t"
      "v_mfma_f32_16x16x32_bf16 v[4:7], a[108:111], v[88:91], v[4:7]\n\t"
      "s_waitcnt lgkmcnt(8)\n\t"
      "v_mfma_f32_16x16x32_bf16 v[8:11], a[44:47], v[92:95], v[8:11]\n\t"
      "v_permlane16_swap_b32 v32, v33\n\t"
      "v_add_f32 v32, v32, v33\n\t"
      "v_mfma_f32_16x16x32_bf16 v[12:15], a[108:111], v[92:95], v[12:15]\n\t"
      "v_mov_b32 v33, v32\n\t"
      "s_waitcnt lgkmcnt(7)\n\t"
      "v_mfma_f32_16x16x32_bf16 v[0:3], a[48:51], v[96:99], v[0:3]\n\t"
      "v_permlane32_swap_b32 v32, v33\n\t"
      "v_add_f32 v32, v32, v33\n\t"
      "v_mfma_f32_16x16x32_bf16 v[4:7], a[112:115], v[96:99], v[4:7]\n\t"
      "v_mul_f32 v204, 0x3b000000, v32\n\t"
      "v_sub_f32 v40, v192, v204\n\t"
      "s_waitcnt lgkmcnt(6)\n\t"
      "v_mfma_f32_16x16x32_bf16 v[8:11], a[48:51], v[100:103], v[8:11]\n\t"
      "v_sub_f32 v41, v193, v204\n\t"
      "v_sub_f32 v42, v194, v204\n\t"
      "v_mfma_f32_16x16x32_bf16 v[12:15], a[112:115], v[100:103], v[12:15]\n\t"
      "v_sub_f32 v43, v195, v204\n\t"
      "v_sub_f32 v44, v196, v204\n\t"
      "s_waitcnt lgkmcnt(5)\n\t"
      "v_mfma_f32_16x16x32_bf16 v[0:3], a[52:55], v[104:107], v[0:3]\n\t"
      "v_sub_f32 v45, v197, v204\n\t"
      "v_sub_f32 v46, v198, v204\n\t"
      "v_mfma_f32_16x16x32_bf16 v[4:7], a[116:119], v[104:107], v[4:7]\n\t"
      "v_sub_f32 v47, v199, v204\n\t"
      "v_mul_f32 v32, v40, v40\n\t"
      "s_waitcnt lgkmcnt(4)\n\t"
      "v_mfma_f32_16x16x32_bf16 v[8:11], a[52:55], v[108:111], v[8:11]\n\t"
      "v_mul_f32 v33, v41, v41\n\t"
      "v_fma_f32 v32, v42, v42, v32\n\t"
      "v_mfma_f32_16x16x32_bf16 v[12:15], a[116:119], v[108:111], v[12:15]\n\t"
      "v_fma_f32 v33, v43, v43, v33\n\t"
      "v_fma_f32 v32, v44, v44, v32\n\t"
      "s_waitcnt lgkmcnt(3)\n\t"
      "v_mfma_f32_16x16x32_bf16 v[0:3], a[56:59], v[112:115], v[0:3]\n\t"
      "v_fma_f32 v33, v45, v45, v33\n\t"
      "v_fma_f32 v32, v46, v46, v32\n\t"
      "v_mfma_f32_16x16x32_bf16 v[4:7], a[120:123], v[112:115], v[4:7]\n\t"
      "v_fma_f32 v33, v47, v47, v33\n\t"
      "v_add_f32 v32, v32, v33\n\t"
      "s_waitcnt lgkmcnt(2)\n\t"
      "v_mfma_f32_16x16x32_bf16 v[8:11], a[56:59], v[116:119], v[8:11]\n\t"
      "v_add_f32_dpp v32, v32, v32 quad_perm:[1,0,3,2] row_mask:0xf bank_mask:0xf\n\t"
      "v_mfma_f32_16x16x32_bf16 v[12:15], a[120:123], v[116:119], v[12:15]\n\t"
      "s_waitcnt lgkmcnt(1)\n\t"
      "v_mfma_f32_16x16x32_bf16 v[0:3], a[60:63], v[120:123], v[0:3]\n\t"
      "v_add_f32_dpp v32, v32, v32 quad_perm:[2,3,0,1] row_mask:0xf bank_mask:0xf\n\t"
      "v_mfma_f32_16x16x32_bf16 v[4:7], a[124:127], v[120:123], v[4:7]\n\t"
      "s_waitcnt lgkmcnt(0)\n\t"
      "v_mfma_f32_16x16x32_bf16 v[8:11], a[60:63], v[124:127], v[8:11]\n\t"
      "v_add_f32_dpp v32, v32, v32 row_ror:4 row_mask:0xf bank_mask:0xf\n\t"
      "v_mfma_f32_16x16x32_bf16 v[12:15], a[124:127], v[124:127], v[12:15]\n\t"
      "s_nop 0\n\t"
      "v_add_f32_dpp v32, v32, v32 row_ror:8 row_mask:0xf bank_mask:0xf\n\t"
      "v_mov_b32 v33, v32\n\t"
      "s_nop 1\n\t"
      "v_permlane16_swap_b32 v32, v33\n\t"
      "v_add_f32 v32, v32, v33\n\t"
      "v_mov_b32 v33, v32\n\t"
      "s_nop 1\n\t"
      "v_permlane32_swap_b32 v32, v33\n\t"
      "v_add_f32 v32, v32, v33\n\t"
      "v_mul_f32 v205, 0x3b000000, v32\n\t"
      "v_xor_b32 v254, 0x8000, v254\n\t"
      "v_xor_b32 v255, 0x8000, v255\n\t"
      "s_add_u32 s69, s53, 0x4000\n\t"
      "s_add_u32 s48, s48, 4\n\t"
      "s_waitcnt vmcnt(40)\n\t"
      "v_cvt_pk_bf16_f32 v0, v0, v1\n\t"
      "v_cvt_pk_bf16_f32 v1, v2, v3\n\t"
      "v_cvt_pk_bf16_f32 v2, v4, v5\n\t"
      "v_cvt_pk_bf16_f32 v3, v6, v7\n\t"
      "v_min_u32 v0, -2, v0\n\t"
      "v_min_u32 v1, -2, v1\n\t"
      "v_min_u32 v2, -2, v2\n\t"
      "v_min_u32 v3, -2, v3\n\t"
      "buffer_store_dwordx4 v[0:3], v238, s[36:39], s53 offen sc1\n\t"
      "v_cvt_pk_bf16_f32 v8, v8, v9\n\t"
      "v_cvt_pk_bf16_f32 v9, v10, v11\n\t"
      "v_cvt_pk_bf16_f32 v10, v12, v13\n\t"
      "v_cvt_pk_bf16_f32 v11, v14, v15\n\t"
      "v_min_u32 v8, -2, v8\n\t"
      "v_min_u32 v9, -2, v9\n\t"
      "v_min_u32 v10, -2, v10\n\t"
      "v_min_u32 v11, -2, v11\n\t"
      "buffer_store_dwordx4 v[8:11], v238, s[36:39], s69 offen sc1\n\t"
      "s_add_u32 s53, s53, 1048576\n\t"
      "s_nop 1\n\t"
      "s_branch WTOP%=\n\t"
      ".p2align 5\n\t"
      "WOL3%=:\n\t"
      "buffer_load_dwordx4 v[128:131], v252, s[36:39], s51 offen sc1\n\t"
      "s_waitcnt lgkmcnt(11)\n\t"
      "v_mfma_f32_16x16x32_bf16 v[0:3], a[0:3], v[64:67], 0\n\t"
      "v_mfma_f32_16x16x32_bf16 v[16:19], a[224:227], v[160:163], 0\n\t"
      "v_mfma_f32_16x16x32_bf16 v[4:7], a[64:67], v[64:67], 0\n\t"
      "v_mfma_f32_16x16x32_bf16 v[20:23], a[228:231], v[160:163], 0\n\t"
      "v_and_b32 v193, s74, v224\n\t"
      "v_lshlrev_b32 v192, 16, v224\n\t"
      "ds_read_b128 v[112:115], v255 offset:12288\n\t"
      "s_waitcnt lgkmcnt(11)\n\t"
      "v_mfma_f32_16x16x32_bf16 v[8:11], a[0:3], v[68:71], 0\n\t"
      "v_mfma_f32_16x16x32_bf16 v[16:19], a[224:227], v[164:167], v[16:19]\n\t"
      "v_mfma_f32_16x16x32_bf16 v[12:15], a[64:67], v[68:71], 0\n\t"
      "v_mfma_f32_16x16x32_bf16 v[20:23], a[228:231], v[164:167], v[20:23]\n\t"
      "v_and_b32 v195, s74, v225\n\t"
      "v_lshlrev_b32 v194, 16, v225\n\t"
      "ds_read_b128 v[116:119], v255 offset:13312\n\t"
      "s_waitcnt lgkmcnt(11)\n\t"
      "v_mfma_f32_16x16x32_bf16 v[0:3], a[4:7], v[72:75], v[0:3]\n\t"
      "v_mfma_f32_16x16x32_bf16 v[16:19], a[224:227], v[168:171], v[16:19]\n\t"
      "v_mfma_f32_16x16x32_bf16 v[4:7], a[68:71], v[72:75], v[4:7]\n\t"
      "v_mfma_f32_16x16x32_bf16 v[20:23], a[228:231], v[168:171], v[20:23]\n\t"
      "v_and_b32 v197, s74, v226\n\t"
      "v_lshlrev_b32 v196, 16, v226\n\t"
      "ds_read_b128 v[120:123], v255 offset:14336\n\t"
      "s_waitcnt lgkmcnt(11)\n\t"
      "v_mfma_f32_16x16x32_bf16 v[8:11], a[4:7], v[76:79], v[8:11]\n\t"
      "v_mfma_f32_16x16x32_bf16 v[16:19], a[224:227], v[172:175], v[16:19]\n\t"
      "v_mfma_f32_16x16x32_bf16 v[12:15], a[68:71], v[76:79], v[12:15]\n\t"
      "buffer_load_dwordx4 v[132:135], v252, s[36:39], s51 offen offset:512 sc1\n\t"
      "ds_read_b128 v[124:127], v255 offset:15360\n\t"
      "s_waitcnt lgkmcnt(11)\n\t"
      "v_mfma_f32_16x16x32_bf16 v[0:3], a[8:11], v[80:83], v[0:3]\n\t"
      "v_mfma_f32_16x16x32_bf16 v[20:23], a[228:231], v[172:175], v[20:23]\n\t"
      "v_and_b32 v199, s74, v227\n\t"
      "v_lshlrev_b32 v198, 16, v227\n\t"
      "v_mfma_f32_16x16x32_bf16 v[4:7], a[72:75], v[80:83], v[4:7]\n\t"
      "v_mfma_f32_16x16x32_bf16 v[16:19], a[224:227], v[176:179], v[16:19]\n\t"
      "ds_read_b128 v[64:67], v255 offset:16384\n\t"
      "s_waitcnt lgkmcnt(11)\n\t"
      "v_mfma_f32_16x16x32_bf16 v[8:11], a[8:11], v[84:87], v[8:11]\n\t"
      "v_mfma_f32_16x16x32_bf16 v[20:23], a[228:231], v[176:179], v[20:23]\n\t"
      "v_mfma_f32_16x16x32_bf16 v[12:15], a[72:75], v[84:87], v[12:15]\n\t"
      "v_mfma_f32_16x16x32_bf16 v[16:19], a[224:227], v[180:183], v[16:19]\n\t"
      "ds_read_b128 v[68:71], v255 offset:17408\n\t"
      "s_waitcnt lgkmcnt(11)\n\t"
      "v_mfma_f32_16x16x32_bf16 v[0:3], a[12:15], v[88:91], v[0:3]\n\t"
      "v_mfma_f32_16x16x32_bf16 v[20:23], a[228:231], v[180:183], v[20:23]\n\t"
      "v_mfma_f32_16x16x32_bf16 v[4:7], a[76:79], v[88:91], v[4:7]\n\t"
      "v_mfma_f32_16x16x32_bf16 v[16:19], a[224:227], v[184:187], v[16:19]\n\t"
      "ds_read_b128 v[72:75], v255 offset:18432\n\t"
      "s_waitcnt lgkmcnt(11)\n\t"
      "v_mfma_f32_16x16x32_bf16 v[8:11], a[12:15], v[92:95], v[8:11]\n\t"
      "v_mfma_f32_16x16x32_bf16 v[20:23], a[228:231], v[184:187], v[20:23]\n\t"
      "v_mfma_f32_16x16x32_bf16 v[12:15], a[76:79], v[92:95], v[12:15]\n\t"
      "buffer_load_dwordx4 v[136:139], v252, s[36:39], s51 offen offset:1024 sc1\n\t"
      "ds_read_b128 v[76:79], v255 offset:19456\n\t"
      "s_waitcnt lgkmcnt(11)\n\t"
      "v_mfma_f32_16x16x32_bf16 v[0:3], a[16:19], v[96:99], v[0:3]\n\t"
      "v_mfma_f32_16x16x32_bf16 v[16:19], a[224:227], v[188:191], v[16:19]\n\t"
      "v_mfma_f32_16x16x32_bf16 v[4:7], a[80:83], v[96:99], v[4:7]\n\t"
      "v_mfma_f32_16x16x32_bf16 v[20:23], a[228:231], v[188:191], v[20:23]\n\t"
      "ds_read_b128 v[80:83], v255 offset:20480\n\t"
      "s_waitcnt lgkmcnt(11)\n\t"
      "v_mfma_f32_16x16x32_bf16 v[8:11], a[16:19], v[100:103], v[8:11]\n\t"
      "v_cvt_pk_bf16_f32 v32, v16, v17\n\t"
      "v_cvt_pk_bf16_f32 v34, v18, v19\n\t"
      "v_mfma_f32_16x16x32_bf16 v[12:15], a[80:83], v[100:103], v[12:15]\n\t"
      "ds_read_b128 v[84:87], v255 offset:21504\n\t"
      "s_waitcnt lgkmcnt(11)\n\t"
      "v_mfma_f32_16x16x32_bf16 v[0:3], a[20:23], v[104:107], v[0:3]\n\t"
      "v_cvt_pk_bf16_f32 v36, v20, v21\n\t"
      "v_cvt_pk_bf16_f32 v38, v22, v23\n\t"
      "v_mfma_f32_16x16x32_bf16 v[4:7], a[84:87], v[104:107], v[4:7]\n\t"
      "v_and_b32 v33, s74, v32\n\t"
      "v_lshlrev_b32 v32, 16, v32\n\t"
      "ds_read_b128 v[88:91], v255 offset:22528\n\t"
      "s_waitcnt lgkmcnt(11)\n\t"
      "v_mfma_f32_16x16x32_bf16 v[8:11], a[20:23], v[108:111], v[8:11]\n\t"
      "v_and_b32 v35, s74, v34\n\t"
      "v_lshlrev_b32 v34, 16, v34\n\t"
      "v_mfma_f32_16x16x32_bf16 v[12:15], a[84:87], v[108:111], v[12:15]\n\t"
      "buffer_load_dwordx4 v[140:143], v252, s[36:39], s51 offen offset:1536 sc1\n\t"
      "ds_read_b128 v[92:95], v255 offset:23552\n\t"
      "s_waitcnt lgkmcnt(11)\n\t"
      "v_mfma_f32_16x16x32_bf16 v[0:3], a[24:27], v[112:115], v[0:3]\n\t"
      "v_and_b32 v37, s74, v36\n\t"
      "v_lshlrev_b32 v36, 16, v36\n\t"
      "v_mfma_f32_16x16x32_bf16 v[4:7], a[88:91], v[112:115], v[4:7]\n\t"
      "v_and_b32 v39, s74, v38\n\t"
      "v_lshlrev_b32 v38, 16, v38\n\t"
      "ds_read_b128 v[96:99], v255 offset:24576\n\t"
      "s_waitcnt lgkmcnt(11)\n\t"
      "v_mfma_f32_16x16x32_bf16 v[8:11], a[24:27], v[116:119], v[8:11]\n\t"
      "v_add_f32 v192, v192, v32\n\t"
      "v_add_f32 v193, v193, v33\n\t"
      "v_mfma_f32_16x16x32_bf16 v[12:15], a[88:91], v[116:119], v[12:15]\n\t"
      "v_add_f32 v194, v194, v34\n\t"
      "v_add_f32 v195, v195, v35\n\t"
      "ds_read_b128 v[100:103], v255 offset:25600\n\t"
      "s_waitcnt lgkmcnt(11)\n\t"
      "v_mfma_f32_16x16x32_bf16 v[0:3], a[28:31], v[120:123], v[0:3]\n\t"
      "v_add_f32 v196, v196, v36\n\t"
      "v_add_f32 v197, v197, v37\n\t"
      "v_mfma_f32_16x16x32_bf16 v[4:7], a[92:95], v[120:123], v[4:7]\n\t"
      "v_add_f32 v198, v198, v38\n\t"
      "v_add_f32 v199, v199, v39\n\t"
      "ds_read_b128 v[104:107], v255 offset:26624\n\t"
      "s_waitcnt lgkmcnt(11)\n\t"
      "v_mfma_f32_16x16x32_bf16 v[8:11], a[28:31], v[124:127], v[8:11]\n\t"
      "buffer_load_dwordx4 v[144:147], v253, s[36:39], s52 offen sc1\n\t"
      "v_mfma_f32_16x16x32_bf16 v[12:15], a[92:95], v[124:127], v[12:15]\n\t"
      "v_cvt_pk_bf16_f32 v200, v192, v193\n\t"
      "v_cvt_pk_bf16_f32 v201, v194, v195\n\t"
      "ds_read_b128 v[108:111], v255 offset:27648\n\t"
      "s_waitcnt lgkmcnt(11)\n\t"
      "v_mfma_f32_16x16x32_bf16 v[0:3], a[32:35], v[64:67], v[0:3]\n\t"
      "v_cvt_pk_bf16_f32 v202, v196, v197\n\t"
      "v_cvt_pk_bf16_f32 v203, v198, v199\n\t"
      "v_mfma_f32_16x16x32_bf16 v[4:7], a[96:99], v[64:67], v[4:7]\n\t"
      "v_and_b32 v193, s74, v200\n\t"
      "v_lshlrev_b32 v192, 16, v200\n\t"
      "ds_read_b128 v[112:115], v255 offset:28672\n\t"
      "s_waitcnt lgkmcnt(11)\n\t"
      "v_mfma_f32_16x16x32_bf16 v[8:11], a[32:35], v[68:71], v[8:11]\n\t"
      "v_and_b32 v195, s74, v201\n\t"
      "v_lshlrev_b32 v194, 16, v201\n\t"
      "v_mfma_f32_16x16x32_bf16 v[12:15], a[96:99], v[68:71], v[12:15]\n\t"
      "v_and_b32 v197, s74, v202\n\t"
      "v_lshlrev_b32 v196, 16, v202\n\t"
      "ds_read_b128 v[116:119], v255 offset:29696\n\t"
      "s_waitcnt lgkmcnt(11)\n\t"
      "v_mfma_f32_16x16x32_bf16 v[0:3], a[36:39], v[72:75], v[0:3]\n\t"
      "v_and_b32 v199, s74, v203\n\t"
      "v_lshlrev_b32 v198, 16, v203\n\t"
      "v_mfma_f32_16x16x32_bf16 v[4:7], a[100:103], v[72:75], v[4:7]\n\t"
      "v_add_f32 v32, v192, v196\n\t"
      "v_add_f32 v33, v193, v197\n\t"
      "ds_read_b128 v[120:123], v255 offset:30720\n\t"
      "s_waitcnt lgkmcnt(11)\n\t"
      "v_mfma_f32_16x16x32_bf16 v[8:11], a[36:39], v[76:79], v[8:11]\n\t"
      "buffer_load_dwordx4 v[148:151], v253, s[36:39], s52 offen offset:512 sc1\n\t"
      "v_mfma_f32_16x16x32_bf16 v[12:15], a[100:103], v[76:79], v[12:15]\n\t"
      "v_add_f32 v34, v194, v198\n\t"
      "v_add_f32 v35, v195, v199\n\t"
      "ds_read_b128 v[124:127], v255 offset:31744\n\t"
      "s_waitcnt lgkmcnt(11)\n\t"
      "v_mfma_f32_16x16x32_bf16 v[0:3], a[40:43], v[80:83], v[0:3]\n\t"
      "v_add_f32 v32, v32, v34\n\t"
      "v_add_f32 v33, v33, v35\n\t"
      "v_mfma_f32_16x16x32_bf16 v[4:7], a[104:107], v[80:83], v[4:7]\n\t"
      "v_add_f32 v32, v32, v33\n\t"
      "s_waitcnt lgkmcnt(10)\n\t"
      "v_mfma_f32_16x16x32_bf16 v[8:11], a[40:43], v[84:87], v[8:11]\n\t"
      "v_add_f32_dpp v32, v32, v32 quad_perm:[1,0,3,2] row_mask:0xf bank_mask:0xf\n\t"
      "v_mfma_f32_16x16x32_bf16 v[12:15], a[104:107], v[84:87], v[12:15]\n\t"
      "s_waitcnt lgkmcnt(9)\n\t"
      "v_mfma_f32_16x16x32_bf16 v[0:3], a[44:47], v[88:91], v[0:3]\n\t"
      "v_add_f32_dpp v32, v32, v32 quad_perm:[2,3,0,1] row_mask:0xf bank_mask:0xf\n\t"
      "v_mfma_f32_16x16x32_bf16 v[4:7], a[108:111], v[88:91], v[4:7]\n\t"
      "s_waitcnt lgkmcnt(8)\n\t"
      "v_mfma_f32_16x16x32_bf16 v[8:11], a[44:47], v[92:95], v[8:11]\n\t"
      "buffer_load_dwordx4 v[152:155], v253, s[36:39], s52 offen offset:1024 sc1\n\t"
      "v_mfma_f32_16x16x32_bf16 v[12:15], a[108:111], v[92:95], v[12:15]\n\t"
      "v_add_f32_dpp v32, v32, v32 row_ror:4 row_mask:0xf bank_mask:0xf\n\t"
      "s_waitcnt lgkmcnt(7)\n\t"
      "v_mfma_f32_16x16x32_bf16 v[0:3], a[48:51], v[96:99], v[0:3]\n\t"
      "v_add_f32_dpp v32, v32, v32 row_ror:8 row_mask:0xf bank_mask:0xf\n\t"
      "v_mov_b32 v33, v32\n\t"
      "v_mfma_f32_16x16x32_bf16 v[4:7], a[112:115], v[96:99], v[4:7]\n\t"
      "s_waitcnt lgkmcnt(6)\n\t"
      "v_mfma_f32_16x16x32_bf16 v[8:11], a[48:51], v[100:103], v[8:11]\n\t"
      "v_permlane16_swap_b32 v32, v33\n\t"
      "v_add_f32 v32, v32, v33\n\t"
      "v_mfma_f32_16x16x32_bf16 v[12:15], a[112:115], v[100:103], v[12:15]\n\t"
      "v_mov_b32 v33, v32\n\t"
      "s_waitcnt lgkmcnt(5)\n\t"
      "v_mfma_f32_16x16x32_bf16 v[0:3], a[52:55], v[104:107], v[0:3]\n\t"
      "v_permlane32_swap_b32 v32, v33\n\t"
      "v_add_f32 v32, v32, v33\n\t"
      "v_mfma_f32_16x16x32_bf16 v[4:7], a[116:119], v[104:107], v[4:7]\n\t"
      "v_mul_f32 v204, 0x3b000000, v32\n\t"
      "v_sub_f32 v40, v192, v204\n\t"
      "s_waitcnt lgkmcnt(4)\n\t"
      "v_mfma_f32_16x16x32_bf16 v[8:11], a[52:55], v[108:111], v[8:11]\n\t"
      "v_sub_f32 v41, v193, v204\n\t"
      "v_sub_f32 v42, v194, v204\n\t"
      "v_mfma_f32_16x16x32_bf16 v[12:15], a[116:119], v[108:111], v[12:15]\n\t"
      "buffer_load_dwordx4 v[156:159], v253, s[36:39], s52 offen offset:1536 sc1\n\t"
      "s_waitcnt lgkmcnt(3)\n\t"
      "v_mfma_f32_16x16x32_bf16 v[0:3], a[56:59], v[112:115], v[0:3]\n\t"
      "v_sub_f32 v43, v195, v204\n\t"
      "v_sub_f32 v44, v196, v204\n\t"
      "v_mfma_f32_16x16x32_bf16 v[4:7], a[120:123], v[112:115], v[4:7]\n\t"
      "v_sub_f32 v45, v197, v204\n\t"
      "v_sub_f32 v46, v198, v204\n\t"
      "s_waitcnt lgkmcnt(2)\n\t"
      "v_mfma_f32_16x16x32_bf16 v[8:11], a[56:59], v[116:119], v[8:11]\n\t"
      "v_sub_f32 v47, v199, v204\n\t"
      "v_mul_f32 v32, v40, v40\n\t"
      "v_mfma_f32_16x16x32_bf16 v[12:15], a[120:123], v[116:119], v[12:15]\n\t"
      "v_mul_f32 v33, v41, v41\n\t"
      "v_fma_f32 v32, v42, v42, v32\n\t"
      "s_waitcnt lgkmcnt(1)\n\t"
      "v_mfma_f32_16x16x32_bf16 v[0:3], a[60:63], v[120:123], v[0:3]\n\t"
      "v_fma_f32 v33, v43, v43, v33\n\t"
      "v_fma_f32 v32, v44, v44, v32\n\t"
      "v_mfma_f32_16x16x32_bf16 v[4:7], a[124:127], v[120:123], v[4:7]\n\t"
      "v_fma_f32 v33, v45, v45, v33\n\t"
      "v_fma_f32 v32, v46, v46, v32\n\t"
      "s_waitcnt lgkmcnt(0)\n\t"
      "v_mfma_f32_16x16x32_bf16 v[8:11], a[60:63], v[124:127], v[8:11]\n\t"
      "v_fma_f32 v33, v47, v47, v33\n\t"
      "v_add_f32 v32, v32, v33\n\t"
      "v_mfma_f32_16x16x32_bf16 v[12:15], a[124:127], v[124:127], v[12:15]\n\t"
      "s_nop 0\n\t"
      "v_add_f32_dpp v32, v32, v32 quad_perm:[1,0,3,2] row_mask:0xf bank_mask:0xf\n\t"
      "s_nop 1\n\t"
      "v_add_f32_dpp v32, v32, v32 quad_perm:[2,3,0,1] row_mask:0xf bank_mask:0xf\n\t"
      "s_nop 1\n\t"
      "v_add_f32_dpp v32, v32, v32 row_ror:4 row_mask:0xf bank_mask:0xf\n\t"
      "s_nop 1\n\t"
      "v_add_f32_dpp v32, v32, v32 row_ror:8 row_mask:0xf bank_mask:0xf\n\t"
      "v_mov_b32 v33, v32\n\t"
      "s_nop 1\n\t"
      "v_permlane16_swap_b32 v32, v33\n\t"
      "v_add_f32 v32, v32, v33\n\t"
      "v_mov_b32 v33, v32\n\t"
      "s_nop 1\n\t"
      "v_permlane32_swap_b32 v32, v33\n\t"
      "v_add_f32 v32, v32, v33\n\t"
      "v_mul_f32 v205, 0x3b000000, v32\n\t"
      "v_xor_b32 v254, 0x8000, v254\n\t"
      "v_xor_b32 v255, 0x8000, v255\n\t"
      "s_add_u32 s69, s53, 0x4000\n\t"
      "s_add_u32 s48, s48, 4\n\t"
      "s_waitcnt vmcnt(40)\n\t"
      "v_cvt_pk_bf16_f32 v0, v0, v1\n\t"
      "v_cvt_pk_bf16_f32 v1, v2, v3\n\t"
      "v_cvt_pk_bf16_f32 v2, v4, v5\n\t"
      "v_cvt_pk_bf16_f32 v3, v6, v7\n\t"
      "v_min_u32 v0, -2, v0\n\t"
      "v_min_u32 v1, -2, v1\n\t"
      "v_min_u32 v2, -2, v2\n\t"
      "v_min_u32 v3, -2, v3\n\t"
      "buffer_store_dwordx4 v[0:3], v238, s[36:39], s53 offen sc1\n\t"
      "v_cvt_pk_bf16_f32 v8, v8, v9\n\t"
      "v_cvt_pk_bf16_f32 v9, v10, v11\n\t"
      "v_cvt_pk_bf16_f32 v10, v12, v13\n\t"
      "v_cvt_pk_bf16_f32 v11, v14, v15\n\t"
      "v_min_u32 v8, -2, v8\n\t"
      "v_min_u32 v9, -2, v9\n\t"
      "v_min_u32 v10, -2, v10\n\t"
      "v_min_u32 v11, -2, v11\n\t"
      "buffer_store_dwordx4 v[8:11], v238, s[36:39], s69 offen sc1\n\t"
      "s_add_u32 s53, s53, 1048576\n\t"
      "s_nop 1\n\t"
      "s_branch WTOP%=\n\t"
      ".p2align 5\n\t"
      "WOM2%=:\n\t"
      "s_mov_b32 s65, 0\n\t"
      "s_add_u32 s24, s96, 8\n\t"
      "s_cmp_lg_u32 s96, 0\n\t"
      "s_cbranch_scc1 WOL5%=\n\t"
      "WOL4%=:\n\t"
      "s_waitcnt lgkmcnt(11)\n\t"
      "v_mfma_f32_16x16x32_bf16 v[0:3], a[0:3], v[64:67], 0\n\t"
      "v_cvt_pk_bf16_f32 v204, v204, v205\n\t"
      "v_and_b32 v205, s74, v204\n\t"
      "v_mfma_f32_16x16x32_bf16 v[4:7], a[64:67], v[64:67], 0\n\t"
      "v_lshlrev_b32 v204, 16, v204\n\t"
      "v_add_f32 v205, 0x3727c5ac, v205\n\t"
      "ds_read_b128 v[112:115], v255 offset:12288\n\t"
      "s_waitcnt lgkmcnt(11)\n\t"
      "v_mfma_f32_16x16x32_bf16 v[8:11], a[0:3], v[68:71], 0\n\t"
      "v_sub_f32 v40, v192, v204\n\t"
      "v_sub_f32 v41, v193, v204\n\t"
      "v_mfma_f32_16x16x32_bf16 v[12:15], a[64:67], v[68:71], 0\n\t"
      "v_sub_f32 v42, v194, v204\n\t"
      "v_sub_f32 v43, v195, v204\n\t"
      "ds_read_b128 v[116:119], v255 offset:13312\n\t"
      "s_waitcnt lgkmcnt(11)\n\t"
      "v_mfma_f32_16x16x32_bf16 v[0:3], a[4:7], v[72:75], v[0:3]\n\t"
      "v_sub_f32 v44, v196, v204\n\t"
      "v_sub_f32 v45, v197, v204\n\t"
      "v_mfma_f32_16x16x32_bf16 v[4:7], a[68:71], v[72:75], v[4:7]\n\t"
      "v_sub_f32 v46, v198, v204\n\t"
      "v_sub_f32 v47, v199, v204\n\t"
      "ds_read_b128 v[120:123], v255 offset:14336\n\t"
      "s_waitcnt lgkmcnt(11)\n\t"
      "v_mfma_f32_16x16x32_bf16 v[8:11], a[4:7], v[76:79], v[8:11]\n\t"
      "v_cvt_pk_bf16_f32 v205, v205, v205\n\t"
      "v_and_b32 v205, s74, v205\n\t"
      "v_mfma_f32_16x16x32_bf16 v[12:15], a[68:71], v[76:79], v[12:15]\n\t"
      "v_sqrt_f32 v205, v205\n\t"
      "ds_read_b128 v[124:127], v255 offset:15360\n\t"
      "s_waitcnt lgkmcnt(11)\n\t"
      "v_mfma_f32_16x16x32_bf16 v[0:3], a[8:11], v[80:83], v[0:3]\n\t"
      "v_cvt_pk_bf16_f32 v40, v40, v41\n\t"
      "v_cvt_pk_bf16_f32 v42, v42, v43\n\t"
      "v_mfma_f32_16x16x32_bf16 v[4:7], a[72:75], v[80:83], v[4:7]\n\t"
      "v_cvt_pk_bf16_f32 v44, v44, v45\n\t"
      "v_cvt_pk_bf16_f32 v46, v46, v47\n\t"
      "ds_read_b128 v[64:67], v255 offset:16384\n\t"
      "s_waitcnt lgkmcnt(11)\n\t"
      "v_mfma_f32_16x16x32_bf16 v[8:11], a[8:11], v[84:87], v[8:11]\n\t"
      "v_cvt_pk_bf16_f32 v205, v205, v205\n\t"
      "v_and_b32 v205, s74, v205\n\t"
      "v_mfma_f32_16x16x32_bf16 v[12:15], a[72:75], v[84:87], v[12:15]\n\t"
      "v_rcp_f32 v204, v205\n\t"
      "ds_read_b128 v[68:71], v255 offset:17408\n\t"
      "s_waitcnt lgkmcnt(11)\n\t"
      "v_mfma_f32_16x16x32_bf16 v[0:3], a[12:15], v[88:91], v[0:3]\n\t"
      "v_and_b32 v41, s74, v40\n\t"
      "v_lshlrev_b32 v40, 16, v40\n\t"
      "v_mfma_f32_16x16x32_bf16 v[4:7], a[76:79], v[88:91], v[4:7]\n\t"
      "v_and_b32 v43, s74, v42\n\t"
      "v_lshlrev_b32 v42, 16, v42\n\t"
      "ds_read_b128 v[72:75], v255 offset:18432\n\t"
      "s_waitcnt lgkmcnt(11)\n\t"
      "v_mfma_f32_16x16x32_bf16 v[8:11], a[12:15], v[92:95], v[8:11]\n\t"
      "v_and_b32 v45, s74, v44\n\t"
      "v_lshlrev_b32 v44, 16, v44\n\t"
      "v_mfma_f32_16x16x32_bf16 v[12:15], a[76:79], v[92:95], v[12:15]\n\t"
      "v_and_b32 v47, s74, v46\n\t"
      "v_lshlrev_b32 v46, 16, v46\n\t"
      "ds_read_b128 v[76:79], v255 offset:19456\n\t"
      "s_waitcnt lgkmcnt(11)\n\t"
      "v_mfma_f32_16x16x32_bf16 v[0:3], a[16:19], v[96:99], v[0:3]\n\t"
      "v_mul_f32 v40, v40, v204\n\t"
      "v_mul_f32 v41, v41, v204\n\t"
      "v_mfma_f32_16x16x32_bf16 v[4:7], a[80:83], v[96:99], v[4:7]\n\t"
      "v_mul_f32 v42, v42, v204\n\t"
      "v_mul_f32 v43, v43, v204\n\t"
      "ds_read_b128 v[80:83], v255 offset:20480\n\t"
      "s_waitcnt lgkmcnt(11)\n\t"
      "v_mfma_f32_16x16x32_bf16 v[8:11], a[16:19], v[100:103], v[8:11]\n\t"
      "v_mul_f32 v44, v44, v204\n\t"
      "v_mul_f32 v45, v45, v204\n\t"
      "v_mfma_f32_16x16x32_bf16 v[12:15], a[80:83], v[100:103], v[12:15]\n\t"
      "v_mul_f32 v46, v46, v204\n\t"
      "v_mul_f32 v47, v47, v204\n\t"
      "ds_read_b128 v[84:87], v255 offset:21504\n\t"
      "s_waitcnt lgkmcnt(11)\n\t"
      "v_mfma_f32_16x16x32_bf16 v[0:3], a[20:23], v[104:107], v[0:3]\n\t"
      "v_cvt_pk_bf16_f32 v52, v40, v41\n\t"
      "v_cvt_pk_bf16_f32 v53, v42, v43\n\t"
      "v_mfma_f32_16x16x32_bf16 v[4:7], a[84:87], v[104:107], v[4:7]\n\t"
      "v_cvt_pk_bf16_f32 v54, v44, v45\n\t"
      "v_cvt_pk_bf16_f32 v55, v46, v47\n\t"
      "ds_read_b128 v[88:91], v255 offset:22528\n\t"
      "s_waitcnt lgkmcnt(11)\n\t"
      "v_mfma_f32_16x16x32_bf16 v[8:11], a[20:23], v[108:111], v[8:11]\n\t"
      "v_and_b32 v33, s74, v52\n\t"
      "v_lshlrev_b32 v32, 16, v52\n\t"
      "v_mfma_f32_16x16x32_bf16 v[12:15], a[84:87], v[108:111], v[12:15]\n\t"
      "v_and_b32 v35, s74, v53\n\t"
      "v_lshlrev_b32 v34, 16, v53\n\t"
      "ds_read_b128 v[92:95], v255 offset:23552\n\t"
      "s_waitcnt lgkmcnt(11)\n\t"
      "v_mfma_f32_16x16x32_bf16 v[0:3], a[24:27], v[112:115], v[0:3]\n\t"
      "v_and_b32 v37, s74, v54\n\t"
      "v_lshlrev_b32 v36, 16, v54\n\t"
      "v_mfma_f32_16x16x32_bf16 v[4:7], a[88:91], v[112:115], v[4:7]\n\t"
      "v_and_b32 v39, s74, v55\n\t"
      "v_lshlrev_b32 v38, 16, v55\n\t"
      "ds_read_b128 v[96:99], v255 offset:24576\n\t"
      "s_waitcnt lgkmcnt(11)\n\t"
      "v_mfma_f32_16x16x32_bf16 v[8:11], a[24:27], v[116:119], v[8:11]\n\t"
      "v_mul_f32 v40, v32, v240\n\t"
      "v_mul_f32 v41, v33, v241\n\t"
      "v_mfma_f32_16x16x32_bf16 v[12:15], a[88:91], v[116:119], v[12:15]\n\t"
      "v_fma_f32 v40, v34, v242, v40\n\t"
      "v_fma_f32 v41, v35, v243, v41\n\t"
      "ds_read_b128 v[100:103], v255 offset:25600\n\t"
      "s_waitcnt lgkmcnt(11)\n\t"
      "v_mfma_f32_16x16x32_bf16 v[0:3], a[28:31], v[120:123], v[0:3]\n\t"
      "v_fma_f32 v40, v36, v244, v40\n\t"
      "v_fma_f32 v41, v37, v245, v41\n\t"
      "v_mfma_f32_16x16x32_bf16 v[4:7], a[92:95], v[120:123], v[4:7]\n\t"
      "v_fma_f32 v40, v38, v246, v40\n\t"
      "v_fma_f32 v41, v39, v247, v41\n\t"
      "ds_read_b128 v[104:107], v255 offset:26624\n\t"
      "s_waitcnt lgkmcnt(11)\n\t"
      "v_mfma_f32_16x16x32_bf16 v[8:11], a[28:31], v[124:127], v[8:11]\n\t"
      "v_add_f32 v40, v40, v41\n\t"
      "s_lshl_b32 s73, s49, 5\n\t"
      "s_add_u32 s73, s73, s60\n\t"
      "s_lshl_b32 s69, s73, 2\n\t"
      "s_add_u32 s69, s69, s57\n\t"
      "s_lshl_b32 s70, s73, 7\n\t"
      "s_add_u32 s70, s70, s57\n\t"
      "s_sub_u32 s70, s70, 212992\n\t"
      "s_cmp_ge_u32 s49, 56\n\t"
      "s_cselect_b32 s70, s70, s69\n\t"
      "v_add_f32_dpp v40, v40, v40 quad_perm:[1,0,3,2] row_mask:0xf bank_mask:0xf\n\t"
      "v_mfma_f32_16x16x32_bf16 v[12:15], a[92:95], v[124:127], v[12:15]\n\t"
      "ds_read_b128 v[108:111], v255 offset:27648\n\t"
      "s_waitcnt lgkmcnt(11)\n\t"
      "v_mfma_f32_16x16x32_bf16 v[0:3], a[32:35], v[64:67], v[0:3]\n\t"
      "v_add_f32_dpp v40, v40, v40 quad_perm:[2,3,0,1] row_mask:0xf bank_mask:0xf\n\t"
      "v_mfma_f32_16x16x32_bf16 v[4:7], a[96:99], v[64:67], v[4:7]\n\t"
      "ds_read_b128 v[112:115], v255 offset:28672\n\t"
      "s_waitcnt lgkmcnt(11)\n\t"
      "v_mfma_f32_16x16x32_bf16 v[8:11], a[32:35], v[68:71], v[8:11]\n\t"
      "v_add_f32_dpp v40, v40, v40 row_ror:4 row_mask:0xf bank_mask:0xf\n\t"
      "v_mfma_f32_16x16x32_bf16 v[12:15], a[96:99], v[68:71], v[12:15]\n\t"
      "ds_read_b128 v[116:119], v255 offset:29696\n\t"
      "s_waitcnt lgkmcnt(11)\n\t"
      "v_mfma_f32_16x16x32_bf16 v[0:3], a[36:39], v[72:75], v[0:3]\n\t"
      "v_add_f32_dpp v40, v40, v40 row_ror:8 row_mask:0xf bank_mask:0xf\n\t"
      "v_mov_b32 v41, v40\n\t"
      "v_mfma_f32_16x16x32_bf16 v[4:7], a[100:103], v[72:75], v[4:7]\n\t"
      "ds_read_b128 v[120:123], v255 offset:30720\n\t"
      "s_waitcnt lgkmcnt(11)\n\t"
      "v_mfma_f32_16x16x32_bf16 v[8:11], a[36:39], v[76:79], v[8:11]\n\t"
      "v_permlane16_swap_b32 v40, v41\n\t"
      "v_add_f32 v40, v40, v41\n\t"
      "v_mfma_f32_16x16x32_bf16 v[12:15], a[100:103], v[76:79], v[12:15]\n\t"
      "v_mov_b32 v41, v40\n\t"
      "ds_read_b128 v[124:127], v255 offset:31744\n\t"
      "s_waitcnt lgkmcnt(11)\n\t"
      "v_mfma_f32_16x16x32_bf16 v[0:3], a[40:43], v[80:83], v[0:3]\n\t"
      "v_permlane32_swap_b32 v40, v41\n\t"
      "v_add_f32 v40, v40, v41\n\t"
      "v_mfma_f32_16x16x32_bf16 v[4:7], a[104:107], v[80:83], v[4:7]\n\t"
      "v_cvt_pk_bf16_f32 v40, v40, v40\n\t"
      "v_and_b32 v40, s74, v40\n\t"
      "s_waitcnt lgkmcnt(10)\n\t"
      "v_mfma_f32_16x16x32_bf16 v[8:11], a[40:43], v[84:87], v[8:11]\n\t"
      "s_mov_b64 exec, 1\n\t"
      "buffer_store_dword v40, off, s[36:39], s70 sc1\n\t"
      "s_mov_b64 exec, -1\n\t"
      "v_add_f32 v192, v192, v32\n\t"
      "v_add_f32 v193, v193, v33\n\t"
      "v_mfma_f32_16x16x32_bf16 v[12:15], a[104:107], v[84:87], v[12:15]\n\t"
      "v_add_f32 v194, v194, v34\n\t"
      "v_add_f32 v195, v195, v35\n\t"
      "s_waitcnt lgkmcnt(9)\n\t"
      "v_mfma_f32_16x16x32_bf16 v[0:3], a[44:47], v[88:91], v[0:3]\n\t"
      "v_add_f32 v196, v196, v36\n\t"
      "v_add_f32 v197, v197, v37\n\t"
      "v_mfma_f32_16x16x32_bf16 v[4:7], a[108:111], v[88:91], v[4:7]\n\t"
      "v_add_f32 v198, v198, v38\n\t"
      "v_add_f32 v199, v199, v39\n\t"
      "s_waitcnt lgkmcnt(8)\n\t"
      "v_mfma_f32_16x16x32_bf16 v[8:11], a[44:47], v[92:95], v[8:11]\n\t"
      "v_readfirstlane_b32 s68, v40\n\t"
      "v_cvt_pk_bf16_f32 v56, v192, v193\n\t"
      "v_mfma_f32_16x16x32_bf16 v[12:15], a[108:111], v[92:95], v[12:15]\n\t"
      "v_cvt_pk_bf16_f32 v57, v194, v195\n\t"
      "v_cvt_pk_bf16_f32 v58, v196, v197\n\t"
      "s_waitcnt lgkmcnt(7)\n\t"
      "v_mfma_f32_16x16x32_bf16 v[0:3], a[48:51], v[96:99], v[0:3]\n\t"
      "v_cvt_pk_bf16_f32 v59, v198, v199\n\t"
      "s_add_u32 s67, s69, 8192\n\t"
      "s_lshl_b32 s73, s73, 10\n\t"
      "s_add_u32 s69, s73, s58\n\t"
      "s_add_u32 s70, s73, s59\n\t"
      "buffer_store_dwordx2 v[56:57], v249, s[44:47], s73 offen sc1\n\t"
      "v_mfma_f32_16x16x32_bf16 v[4:7], a[112:115], v[96:99], v[4:7]\n\t"
      "buffer_store_dwordx2 v[52:53], v249, s[36:39], s69 offen sc1\n\t"
      "buffer_store_dwordx2 v[200:201], v249, s[36:39], s70 offen sc1\n\t"
      "s_waitcnt lgkmcnt(6)\n\t"
      "v_mfma_f32_16x16x32_bf16 v[8:11], a[48:51], v[100:103], v[8:11]\n\t"
      "buffer_store_dwordx2 v[58:59], v249, s[44:47], s73 offen offset:512 sc1\n\t"
      "buffer_store_dwordx2 v[54:55], v249, s[36:39], s69 offen offset:512 sc1\n\t"
      "v_mfma_f32_16x16x32_bf16 v[12:15], a[112:115], v[100:103], v[12:15]\n\t"
      "buffer_store_dwordx2 v[202:203], v249, s[36:39], s70 offen offset:512 sc1\n\t"
      "s_mov_b32 s49, s89\n\t"
      "s_add_u32 s89, s89, 16\n\t"
      "s_lshr_b32 s69, s89, 4\n\t"
      "s_cmp_eq_u32 s69, 3\n\t"
      "s_cselect_b32 s89, s93, s89\n\t"
      "s_cmp_ge_u32 s49, 48\n\t"
      "s_cselect_b32 s60, s94, s60\n\t"
      "s_cselect_b32 s54, s95, s54\n\t"
      "s_waitcnt lgkmcnt(5)\n\t"
      "v_mfma_f32_16x16x32_bf16 v[0:3], a[52:55], v[104:107], v[0:3]\n\t"
      "v_mfma_f32_16x16x32_bf16 v[4:7], a[116:119], v[104:107], v[4:7]\n\t"
      "s_waitcnt lgkmcnt(4)\n\t"
      "v_mfma_f32_16x16x32_bf16 v[8:11], a[52:55], v[108:111], v[8:11]\n\t"
      "v_mfma_f32_16x16x32_bf16 v[12:15], a[116:119], v[108:111], v[12:15]\n\t"
      "s_waitcnt lgkmcnt(3)\n\t"
      "v_mfma_f32_16x16x32_bf16 v[0:3], a[56:59], v[112:115], v[0:3]\n\t"
      "v_mfma_f32_16x16x32_bf16 v[4:7], a[120:123], v[112:115], v[4:7]\n\t"
      "s_waitcnt lgkmcnt(2)\n\t"
      "v_mfma_f32_16x16x32_bf16 v[8:11], a[56:59], v[116:119], v[8:11]\n\t"
      "v_mfma_f32_16x16x32_bf16 v[12:15], a[120:123], v[116:119], v[12:15]\n\t"
      "s_waitcnt lgkmcnt(1)\n\t"
      "v_mfma_f32_16x16x32_bf16 v[0:3], a[60:63], v[120:123], v[0:3]\n\t"
      "v_mfma_f32_16x16x32_bf16 v[4:7], a[124:127], v[120:123], v[4:7]\n\t"
      "s_waitcnt lgkmcnt(0)\n\t"
      "v_mfma_f32_16x16x32_bf16 v[8:11], a[60:63], v[124:127], v[8:11]\n\t"
      "v_mfma_f32_16x16x32_bf16 v[12:15], a[124:127], v[124:127], v[12:15]\n\t"
      "v_xor_b32 v254, 0x8000, v254\n\t"
      "v_xor_b32 v255, 0x8000, v255\n\t"
      "s_add_u32 s69, s53, 0x4000\n\t"
      "s_add_u32 s48, s48, 4\n\t"
      "s_waitcnt vmcnt(40)\n\t"
      "v_cvt_pk_bf16_f32 v0, v0, v1\n\t"
      "v_cvt_pk_bf16_f32 v1, v2, v3\n\t"
      "v_cvt_pk_bf16_f32 v2, v4, v5\n\t"
      "v_cvt_pk_bf16_f32 v3, v6, v7\n\t"
      "v_min_u32 v0, -2, v0\n\t"
      "v_min_u32 v1, -2, v1\n\t"
      "v_min_u32 v2, -2, v2\n\t"
      "v_min_u32 v3, -2, v3\n\t"
      "buffer_store_dwordx4 v[0:3], v238, s[36:39], s53 offen sc1\n\t"
      "v_cvt_pk_bf16_f32 v8, v8, v9\n\t"
      "v_cvt_pk_bf16_f32 v9, v10, v11\n\t"
      "v_cvt_pk_bf16_f32 v10, v12, v13\n\t"
      "v_cvt_pk_bf16_f32 v11, v14, v15\n\t"
      "v_min_u32 v8, -2, v8\n\t"
      "v_min_u32 v9, -2, v9\n\t"
      "v_min_u32 v10, -2, v10\n\t"
      "v_min_u32 v11, -2, v11\n\t"
      "buffer_store_dwordx4 v[8:11], v238, s[36:39], s69 offen sc1\n\t"
      "s_add_u32 s53, s53, 1048576\n\t"
      "s_nop 1\n\t"
      "s_branch WTOP%=\n\t"
      ".p2align 5\n\t"
      "WOL5%=:\n\t"
      "buffer_load_dwordx4 v[128:131], v252, s[36:39], s51 offen sc1\n\t"
      "s_waitcnt lgkmcnt(11)\n\t"
      "v_mfma_f32_16x16x32_bf16 v[0:3], a[0:3], v[64:67], 0\n\t"
      "v_cvt_pk_bf16_f32 v204, v204, v205\n\t"
      "v_and_b32 v205, s74, v204\n\t"
      "v_mfma_f32_16x16x32_bf16 v[4:7], a[64:67], v[64:67], 0\n\t"
      "v_lshlrev_b32 v204, 16, v204\n\t"
      "v_add_f32 v205, 0x3727c5ac, v205\n\t"
      "ds_read_b128 v[112:115], v255 offset:12288\n\t"
      "s_waitcnt lgkmcnt(11)\n\t"
      "v_mfma_f32_16x16x32_bf16 v[8:11], a[0:3], v[68:71], 0\n\t"
      "v_sub_f32 v40, v192, v204\n\t"
      "v_sub_f32 v41, v193, v204\n\t"
      "v_mfma_f32_16x16x32_bf16 v[12:15], a[64:67], v[68:71], 0\n\t"
      "v_sub_f32 v42, v194, v204\n\t"
      "v_sub_f32 v43, v195, v204\n\t"
      "ds_read_b128 v[116:119], v255 offset:13312\n\t"
      "s_waitcnt lgkmcnt(11)\n\t"
      "v_mfma_f32_16x16x32_bf16 v[0:3], a[4:7], v[72:75], v[0:3]\n\t"
      "v_sub_f32 v44, v196, v204\n\t"
      "v_sub_f32 v45, v197, v204\n\t"
      "v_mfma_f32_16x16x32_bf16 v[4:7], a[68:71], v[72:75], v[4:7]\n\t"
      "v_sub_f32 v46, v198, v204\n\t"
      "v_sub_f32 v47, v199, v204\n\t"
      "ds_read_b128 v[120:123], v255 offset:14336\n\t"
      "s_waitcnt lgkmcnt(11)\n\t"
      "v_mfma_f32_16x16x32_bf16 v[8:11], a[4:7], v[76:79], v[8:11]\n\t"
      "v_cvt_pk_bf16_f32 v205, v205, v205\n\t"
      "v_and_b32 v205, s74, v205\n\t"
      "v_mfma_f32_16x16x32_bf16 v[12:15], a[68:71], v[76:79], v[12:15]\n\t"
      "buffer_load_dwordx4 v[132:135], v252, s[36:39], s51 offen offset:512 sc1\n\t"
      "ds_read_b128 v[124:127], v255 offset:15360\n\t"
      "s_waitcnt lgkmcnt(11)\n\t"
      "v_mfma_f32_16x16x32_bf16 v[0:3], a[8:11], v[80:83], v[0:3]\n\t"
      "v_sqrt_f32 v205, v205\n\t"
      "v_mfma_f32_16x16x32_bf16 v[4:7], a[72:75], v[80:83], v[4:7]\n\t"
      "v_cvt_pk_bf16_f32 v40, v40, v41\n\t"
      "v_cvt_pk_bf16_f32 v42, v42, v43\n\t"
      "ds_read_b128 v[64:67], v255 offset:16384\n\t"
      "s_waitcnt lgkmcnt(11)\n\t"
      "v_mfma_f32_16x16x32_bf16 v[8:11], a[8:11], v[84:87], v[8:11]\n\t"
      "v_cvt_pk_bf16_f32 v44, v44, v45\n\t"
      "v_cvt_pk_bf16_f32 v46, v46, v47\n\t"
      "v_mfma_f32_16x16x32_bf16 v[12:15], a[72:75], v[84:87], v[12:15]\n\t"
      "v_cvt_pk_bf16_f32 v205, v205, v205\n\t"
      "v_and_b32 v205, s74, v205\n\t"
      "ds_read_b128 v[68:71], v255 offset:17408\n\t"
      "s_waitcnt lgkmcnt(11)\n\t"
      "v_mfma_f32_16x16x32_bf16 v[0:3], a[12:15], v[88:91], v[0:3]\n\t"
      "v_rcp_f32 v204, v205\n\t"
      "v_mfma_f32_16x16x32_bf16 v[4:7], a[76:79], v[88:91], v[4:7]\n\t"
      "v_and_b32 v41, s74, v40\n\t"
      "v_lshlrev_b32 v40, 16, v40\n\t"
      "ds_read_b128 v[72:75], v255 offset:18432\n\t"
      "s_waitcnt lgkmcnt(11)\n\t"
      "v_mfma_f32_16x16x32_bf16 v[8:11], a[12:15], v[92:95], v[8:11]\n\t"
      "v_and_b32 v43, s74, v42\n\t"
      "v_lshlrev_b32 v42, 16, v42\n\t"
      "v_mfma_f32_16x16x32_bf16 v[12:15], a[76:79], v[92:95], v[12:15]\n\t"
      "buffer_load_dwordx4 v[136:139], v252, s[36:39], s51 offen offset:1024 sc1\n\t"
      "ds_read_b128 v[76:79], v255 offset:19456\n\t"
      "s_waitcnt lgkmcnt(11)\n\t"
      "v_mfma_f32_16x16x32_bf16 v[0:3], a[16:19], v[96:99], v[0:3]\n\t"
      "v_and_b32 v45, s74, v44\n\t"
      "v_lshlrev_b32 v44, 16, v44\n\t"
      "v_mfma_f32_16x16x32_bf16 v[4:7], a[80:83], v[96:99], v[4:7]\n\t"
      "v_and_b32 v47, s74, v46\n\t"
      "v_lshlrev_b32 v46, 16, v46\n\t"
      "ds_read_b128 v[80:83], v255 offset:20480\n\t"
      "s_waitcnt lgkmcnt(11)\n\t"
      "v_mfma_f32_16x16x32_bf16 v[8:11], a[16:19], v[100:103], v[8:11]\n\t"
      "v_mul_f32 v40, v40, v204\n\t"
      "v_mul_f32 v41, v41, v204\n\t"
      "v_mfma_f32_16x16x32_bf16 v[12:15], a[80:83], v[100:103], v[12:15]\n\t"
      "v_mul_f32 v42, v42, v204\n\t"
      "v_mul_f32 v43, v43, v204\n\t"
      "ds_read_b128 v[84:87], v255 offset:21504\n\t"
      "s_waitcnt lgkmcnt(11)\n\t"
      "v_mfma_f32_16x16x32_bf16 v[0:3], a[20:23], v[104:107], v[0:3]\n\t"
      "v_mul_f32 v44, v44, v204\n\t"
      "v_mul_f32 v45, v45, v204\n\t"
      "v_mfma_f32_16x16x32_bf16 v[4:7], a[84:87], v[104:107], v[4:7]\n\t"
      "v_mul_f32 v46, v46, v204\n\t"
      "v_mul_f32 v47, v47, v204\n\t"
      "ds_read_b128 v[88:91], v255 offset:22528\n\t"
      "s_waitcnt lgkmcnt(11)\n\t"
      "v_mfma_f32_16x16x32_bf16 v[8:11], a[20:23], v[108:111], v[8:11]\n\t"
      "v_cvt_pk_bf16_f32 v52, v40, v41\n\t"
      "v_cvt_pk_bf16_f32 v53, v42, v43\n\t"
      "v_mfma_f32_16x16x32_bf16 v[12:15], a[84:87], v[108:111], v[12:15]\n\t"
      "buffer_load_dwordx4 v[140:143], v252, s[36:39], s51 offen offset:1536 sc1\n\t"
      "ds_read_b128 v[92:95], v255 offset:23552\n\t"
      "s_waitcnt lgkmcnt(11)\n\t"
      "v_mfma_f32_16x16x32_bf16 v[0:3], a[24:27], v[112:115], v[0:3]\n\t"
      "v_cvt_pk_bf16_f32 v54, v44, v45\n\t"
      "v_cvt_pk_bf16_f32 v55, v46, v47\n\t"
      "v_mfma_f32_16x16x32_bf16 v[4:7], a[88:91], v[112:115], v[4:7]\n\t"
      "v_and_b32 v33, s74, v52\n\t"
      "v_lshlrev_b32 v32, 16, v52\n\t"
      "ds_read_b128 v[96:99], v255 offset:24576\n\t"
      "s_waitcnt lgkmcnt(11)\n\t"
      "v_mfma_f32_16x16x32_bf16 v[8:11], a[24:27], v[116:119], v[8:11]\n\t"
      "v_and_b32 v35, s74, v53\n\t"
      "v_lshlrev_b32 v34, 16, v53\n\t"
      "v_mfma_f32_16x16x32_bf16 v[12:15], a[88:91], v[116:119], v[12:15]\n\t"
      "v_and_b32 v37, s74, v54\n\t"
      "v_lshlrev_b32 v36, 16, v54\n\t"
      "ds_read_b128 v[100:103], v255 offset:25600\n\t"
      "s_waitcnt lgkmcnt(11)\n\t"
      "v_mfma_f32_16x16x32_bf16 v[0:3], a[28:31], v[120:123], v[0:3]\n\t"
      "v_and_b32 v39, s74, v55\n\t"
      "v_lshlrev_b32 v38, 16, v55\n\t"
      "v_mfma_f32_16x16x32_bf16 v[4:7], a[92:95], v[120:123], v[4:7]\n\t"
      "v_mul_f32 v40, v32, v240\n\t"
      "v_mul_f32 v41, v33, v241\n\t"
      "ds_read_b128 v[104:107], v255 offset:26624\n\t"
      "s_waitcnt lgkmcnt(11)\n\t"
      "v_mfma_f32_16x16x32_bf16 v[8:11], a[28:31], v[124:127], v[8:11]\n\t"
      "buffer_load_dwordx4 v[144:147], v253, s[36:39], s52 offen sc1\n\t"
      "v_mfma_f32_16x16x32_bf16 v[12:15], a[92:95], v[124:127], v[12:15]\n\t"
      "v_fma_f32 v40, v34, v242, v40\n\t"
      "v_fma_f32 v41, v35, v243, v41\n\t"
      "ds_read_b128 v[108:111], v255 offset:27648\n\t"
      "s_waitcnt lgkmcnt(11)\n\t"
      "v_mfma_f32_16x16x32_bf16 v[0:3], a[32:35], v[64:67], v[0:3]\n\t"
      "v_fma_f32 v40, v36, v244, v40\n\t"
      "v_fma_f32 v41, v37, v245, v41\n\t"
      "v_mfma_f32_16x16x32_bf16 v[4:7], a[96:99], v[64:67], v[4:7]\n\t"
      "v_fma_f32 v40, v38, v246, v40\n\t"
      "v_fma_f32 v41, v39, v247, v41\n\t"
      "ds_read_b128 v[112:115], v255 offset:28672\n\t"
      "s_waitcnt lgkmcnt(11)\n\t"
      "v_mfma_f32_16x16x32_bf16 v[8:11], a[32:35], v[68:71], v[8:11]\n\t"
      "v_add_f32 v40, v40, v41\n\t"
      "s_lshl_b32 s73, s49, 5\n\t"
      "s_add_u32 s73, s73, s60\n\t"
      "s_lshl_b32 s69, s73, 2\n\t"
      "s_add_u32 s69, s69, s57\n\t"
      "s_lshl_b32 s70, s73, 7\n\t"
      "s_add_u32 s70, s70, s57\n\t"
      "s_sub_u32 s70, s70, 212992\n\t"
      "s_cmp_ge_u32 s49, 56\n\t"
      "s_cselect_b32 s70, s70, s69\n\t"
      "v_add_f32_dpp v40, v40, v40 quad_perm:[1,0,3,2] row_mask:0xf bank_mask:0xf\n\t"
      "v_mfma_f32_16x16x32_bf16 v[12:15], a[96:99], v[68:71], v[12:15]\n\t"
      "ds_read_b128 v[116:119], v255 offset:29696\n\t"
      "s_waitcnt lgkmcnt(11)\n\t"
      "v_mfma_f32_16x16x32_bf16 v[0:3], a[36:39], v[72:75], v[0:3]\n\t"
      "v_add_f32_dpp v40, v40, v40 quad_perm:[2,3,0,1] row_mask:0xf bank_mask:0xf\n\t"
      "v_mfma_f32_16x16x32_bf16 v[4:7], a[100:103], v[72:75], v[4:7]\n\t"
      "ds_read_b128 v[120:123], v255 offset:30720\n\t"
      "s_waitcnt lgkmcnt(11)\n\t"
      "v_mfma_f32_16x16x32_bf16 v[8:11], a[36:39], v[76:79], v[8:11]\n\t"
      "buffer_load_dwordx4 v[148:151], v253, s[36:39], s52 offen offset:512 sc1\n\t"
      "v_mfma_f32_16x16x32_bf16 v[12:15], a[100:103], v[76:79], v[12:15]\n\t"
      "v_add_f32_dpp v40, v40, v40 row_ror:4 row_mask:0xf bank_mask:0xf\n\t"
      "ds_read_b128 v[124:127], v255 offset:31744\n\t"
      "s_waitcnt lgkmcnt(11)\n\t"
      "v_mfma_f32_16x16x32_bf16 v[0:3], a[40:43], v[80:83], v[0:3]\n\t"
      "v_add_f32_dpp v40, v40, v40 row_ror:8 row_mask:0xf bank_mask:0xf\n\t"
      "v_mov_b32 v41, v40\n\t"
      "v_mfma_f32_16x16x32_bf16 v[4:7], a[104:107], v[80:83], v[4:7]\n\t"
      "s_waitcnt lgkmcnt(10)\n\t"
      "v_mfma_f32_16x16x32_bf16 v[8:11], a[40:43], v[84:87], v[8:11]\n\t"
      "v_permlane16_swap_b32 v40, v41\n\t"
      "v_add_f32 v40, v40, v41\n\t"
      "v_mfma_f32_16x16x32_bf16 v[12:15], a[104:107], v[84:87], v[12:15]\n\t"
      "v_mov_b32 v41, v40\n\t"
      "s_waitcnt lgkmcnt(9)\n\t"
      "v_mfma_f32_16x16x32_bf16 v[0:3], a[44:47], v[88:91], v[0:3]\n\t"
      "v_permlane32_swap_b32 v40, v41\n\t"
      "v_add_f32 v40, v40, v41\n\t"
      "v_mfma_f32_16x16x32_bf16 v[4:7], a[108:111], v[88:91], v[4:7]\n\t"
      "v_cvt_pk_bf16_f32 v40, v40, v40\n\t"
      "v_and_b32 v40, s74, v40\n\t"
      "s_waitcnt lgkmcnt(8)\n\t"
      "v_mfma_f32_16x16x32_bf16 v[8:11], a[44:47], v[92:95], v[8:11]\n\t"
      "buffer_load_dwordx4 v[152:155], v253, s[36:39], s52 offen offset:1024 sc1\n\t"
      "v_mfma_f32_16x16x32_bf16 v[12:15], a[108:111], v[92:95], v[12:15]\n\t"
      "s_mov_b64 exec, 1\n\t"
      "buffer_store_dword v40, off, s[36:39], s70 sc1\n\t"
      "s_mov_b64 exec, -1\n\t"
      "v_add_f32 v192, v192, v32\n\t"
      "v_add_f32 v193, v193, v33\n\t"
      "s_waitcnt lgkmcnt(7)\n\t"
      "v_mfma_f32_16x16x32_bf16 v[0:3], a[48:51], v[96:99], v[0:3]\n\t"
      "v_add_f32 v194, v194, v34\n\t"
      "v_add_f32 v195, v195, v35\n\t"
      "v_mfma_f32_16x16x32_bf16 v[4:7], a[112:115], v[96:99], v[4:7]\n\t"
      "v_add_f32 v196, v196, v36\n\t"
      "v_add_f32 v197, v197, v37\n\t"
      "s_waitcnt lgkmcnt(6)\n\t"
      "v_mfma_f32_16x16x32_bf16 v[8:11], a[48:51], v[100:103], v[8:11]\n\t"
      "v_add_f32 v198, v198, v38\n\t"
      "v_add_f32 v199, v199, v39\n\t"
      "v_mfma_f32_16x16x32_bf16 v[12:15], a[112:115], v[100:103], v[12:15]\n\t"
      "v_readfirstlane_b32 s68, v40\n\t"
      "v_cvt_pk_bf16_f32 v56, v192, v193\n\t"
      "s_waitcnt lgkmcnt(5)\n\t"
      "v_mfma_f32_16x16x32_bf16 v[0:3], a[52:55], v[104:107], v[0:3]\n\t"
      "v_cvt_pk_bf16_f32 v57, v194, v195\n\t"
      "v_cvt_pk_bf16_f32 v58, v196, v197\n\t"
      "v_mfma_f32_16x16x32_bf16 v[4:7], a[116:119], v[104:107], v[4:7]\n\t"
      "v_cvt_pk_bf16_f32 v59, v198, v199\n\t"
      "s_add_u32 s67, s69, 8192\n\t"
      "s_lshl_b32 s73, s73, 10\n\t"
      "s_add_u32 s69, s73, s58\n\t"
      "s_add_u32 s70, s73, s59\n\t"
      "buffer_store_dwordx2 v[56:57], v249, s[44:47], s73 offen sc1\n\t"
      "s_waitcnt lgkmcnt(4)\n\t"
      "v_mfma_f32_16x16x32_bf16 v[8:11], a[52:55], v[108:111], v[8:11]\n\t"
      "buffer_store_dwordx2 v[52:53], v249, s[36:39], s69 offen sc1\n\t"
      "buffer_store_dwordx2 v[200:201], v249, s[36:39], s70 offen sc1\n\t"
      "v_mfma_f32_16x16x32_bf16 v[12:15], a[116:119], v[108:111], v[12:15]\n\t"
      "buffer_load_dwordx4 v[156:159], v253, s[36:39], s52 offen offset:1536 sc1\n\t"
      "s_waitcnt lgkmcnt(3)\n\t"
      "v_mfma_f32_16x16x32_bf16 v[0:3], a[56:59], v[112:115], v[0:3]\n\t"
      "buffer_store_dwordx2 v[58:59], v249, s[44:47], s73 offen offset:512 sc1\n\t"
      "buffer_store_dwordx2 v[54:55], v249, s[36:39], s69 offen offset:512 sc1\n\t"
      "v_mfma_f32_16x16x32_bf16 v[4:7], a[120:123], v[112:115], v[4:7]\n\t"
      "buffer_store_dwordx2 v[202:203], v249, s[36:39], s70 offen offset:512 sc1\n\t"
      "s_mov_b32 s49, s89\n\t"
      "s_add_u32 s89, s89, 16\n\t"
      "s_lshr_b32 s69, s89, 4\n\t"
      "s_cmp_eq_u32 s69, 3\n\t"
      "s_cselect_b32 s89, s93, s89\n\t"
      "s_cmp_ge_u32 s49, 48\n\t"
      "s_cselect_b32 s60, s94, s60\n\t"
      "s_cselect_b32 s54, s95, s54\n\t"
      "s_waitcnt lgkmcnt(2)\n\t"
      "v_mfma_f32_16x16x32_bf16 v[8:11], a[56:59], v[116:119], v[8:11]\n\t"
      "v_mfma_f32_16x16x32_bf16 v[12:15], a[120:123], v[116:119], v[12:15]\n\t"
      "s_waitcnt lgkmcnt(1)\n\t"
      "v_mfma_f32_16x16x32_bf16 v[0:3], a[60:63], v[120:123], v[0:3]\n\t"
      "v_mfma_f32_16x16x32_bf16 v[4:7], a[124:127], v[120:123], v[4:7]\n\t"
      "s_waitcnt lgkmcnt(0)\n\t"
      "v_mfma_f32_16x16x32_bf16 v[8:11], a[60:63], v[124:127], v[8:11]\n\t"
      "v_mfma_f32_16x16x32_bf16 v[12:15], a[124:127], v[124:127], v[12:15]\n\t"
      "v_xor_b32 v254, 0x8000, v254\n\t"
      "v_xor_b32 v255, 0x8000, v255\n\t"
      "s_add_u32 s69, s53, 0x4000\n\t"
      "s_add_u32 s48, s48, 4\n\t"
      "s_waitcnt vmcnt(40)\n\t"
      "v_cvt_pk_bf16_f32 v0, v0, v1\n\t"
      "v_cvt_pk_bf16_f32 v1, v2, v3\n\t"
      "v_cvt_pk_bf16_f32 v2, v4, v5\n\t"
      "v_cvt_pk_bf16_f32 v3, v6, v7\n\t"
      "v_min_u32 v0, -2, v0\n\t"
      "v_min_u32 v1, -2, v1\n\t"
      "v_min_u32 v2, -2, v2\n\t"
      "v_min_u32 v3, -2, v3\n\t"
      "buffer_store_dwordx4 v[0:3], v238, s[36:39], s53 offen sc1\n\t"
      "v_cvt_pk_bf16_f32 v8, v8, v9\n\t"
      "v_cvt_pk_bf16_f32 v9, v10, v11\n\t"
      "v_cvt_pk_bf16_f32 v10, v12, v13\n\t"
      "v_cvt_pk_bf16_f32 v11, v14, v15\n\t"
      "v_min_u32 v8, -2, v8\n\t"
      "v_min_u32 v9, -2, v9\n\t"
      "v_min_u32 v10, -2, v10\n\t"
      "v_min_u32 v11, -2, v11\n\t"
      "buffer_store_dwordx4 v[8:11], v238, s[36:39], s69 offen sc1\n\t"
      "s_add_u32 s53, s53, 1048576\n\t"
      "s_nop 1\n\t"
      "s_branch WTOP%=\n\t"
      ".p2align 5\n\t"
      "WKF%=:\n\t"
      "s_cmp_eq_u32 s48, 63\n\t"
      "s_cselect_b32 s69, 0, 46\n\t"
      "s_add_u32 s88, s88, s69\n\t"
      "s_add_u32 s88, s88, -16\n\t"
      "WKT%=:\n\t"
      "s_memrealtime s[90:91]\n\t"
      "s_waitcnt lgkmcnt(0)\n\t"
      "s_sub_u32 s69, s88, s90\n\t"
      "s_and_b32 s69, s69, 0xffff\n\t"
      "s_cmp_lt_u32 s69, 400\n\t"
      "s_cbranch_scc1 WKT%=\n\t"
      "WKL%=:\n\t"
      "buffer_load_dwordx4 v[128:131], v252, s[36:39], s51 offen sc1\n\t"
      "buffer_load_dwordx4 v[132:135], v252, s[36:39], s51 offen offset:512 sc1\n\t"
      "buffer_load_dwordx4 v[136:139], v252, s[36:39], s51 offen offset:1024 sc1\n\t"
      "buffer_load_dwordx4 v[140:143], v252, s[36:39], s51 offen offset:1536 sc1\n\t"
      "buffer_load_dwordx4 v[144:147], v253, s[36:39], s52 offen sc1\n\t"
      "buffer_load_dwordx4 v[148:151], v253, s[36:39], s52 offen offset:512 sc1\n\t"
      "buffer_load_dwordx4 v[152:155], v253, s[36:39], s52 offen offset:1024 sc1\n\t"
      "buffer_load_dwordx4 v[156:159], v253, s[36:39], s52 offen offset:1536 sc1\n\t"
      "v_lshlrev_b32 v6, 4, v248\n\t"
      "v_mov_b64 v[4:5], 0\n\t"
      "s_cmp_ge_u32 s49, s48\n\t"
      "s_cbranch_scc1 WKL1%=\n\t"
      "buffer_load_dword v4, v6, s[36:39], s55 offen sc1\n\t"
      "buffer_load_dword v5, v6, s[36:39], s55 offen offset:128 sc1\n\t"
      "WKL1%=:\n\t"
      "s_waitcnt vmcnt(0)\n\t"
      "v_min_u32 v4, v4, v5\n\t"
      "v_cmp_ge_u32 vcc, s49, v4\n\t"
      "s_nop 0\n\t"
      "s_cmp_eq_u64 vcc, 0\n\t"
      "s_cselect_b32 s66, 1, 0\n\t"
      "s_cmp_lt_u32 s48, 40\n\t"
      "s_cbranch_scc1 WKV%=\n\t"
      "WKC%=:\n\t"
      "v_max3_u32 v0, v128, v129, v130\n\t"
      "v_max3_u32 v0, v0, v131, v132\n\t"
      "v_max3_u32 v0, v0, v133, v134\n\t"
      "v_max3_u32 v0, v0, v135, v136\n\t"
      "v_max3_u32 v0, v0, v137, v138\n\t"
      "v_max3_u32 v0, v0, v139, v140\n\t"
      "v_max3_u32 v0, v0, v141, v142\n\t"
      "v_max3_u32 v0, v0, v143, v144\n\t"
      "v_max3_u32 v0, v0, v145, v146\n\t"
      "v_max3_u32 v0, v0, v147, v148\n\t"
      "v_max3_u32 v0, v0, v149, v150\n\t"
      "v_max3_u32 v0, v0, v151, v152\n\t"
      "v_max3_u32 v0, v0, v153, v154\n\t"
      "v_max3_u32 v0, v0, v155, v156\n\t"
      "v_max3_u32 v0, v0, v157, v158\n\t"
      "v_max_u32 v0, v0, v159\n\t"
      "v_cmp_eq_u32 vcc, -1, v0\n\t"
      "s_nop 0\n\t"
      "s_cbranch_vccz WKV%=\n\t"
      "s_sleep 2\n\t"
      "s_sub_u32 s72, s72, 1\n\t"
      "s_cmp_gt_i32 s72, 0\n\t"
      "s_cbranch_scc1 WKL%=\n\t"
      "WKV%=:\n\t"
      "s_cmp_lt_u32 s48, 60\n\t"
      "s_cbranch_scc1 WKV1%=\n\t"
      "s_cmp_eq_u32 s66, 0\n\t"
      "s_cbranch_scc1 WB0%=\n\t"
      "s_lshl_b32 s70, s49, 5\n\t"
      "s_add_u32 s70, s70, s60\n\t"
      "s_lshl_b32 s70, s70, 10\n\t"
      "buffer_load_dwordx2 v[224:225], v249, s[40:43], s70 offen \n\t"
      "buffer_load_dwordx2 v[226:227], v249, s[40:43], s70 offen offset:512 \n\t"
      "s_lshl_b32 s69, s49, 18\n\t"
      "s_add_u32 s69, s69, s54\n\t"
      "buffer_load_dwordx4 v[160:163], v248, s[36:39], s69 offen sc1\n\t"
      "s_add_u32 s69, s69, 0x8000\n\t"
      "buffer_load_dwordx4 v[164:167], v248, s[36:39], s69 offen sc1\n\t"
      "s_add_u32 s69, s69, 0x8000\n\t"
      "buffer_load_dwordx4 v[168:171], v248, s[36:39], s69 offen sc1\n\t"
      "s_add_u32 s69, s69, 0x8000\n\t"
      "buffer_load_dwordx4 v[172:175], v248, s[36:39], s69 offen sc1\n\t"
      "s_add_u32 s69, s69, 0x8000\n\t"
      "buffer_load_dwordx4 v[176:179], v248, s[36:39], s69 offen sc1\n\t"
      "s_add_u32 s69, s69, 0x8000\n\t"
      "buffer_load_dwordx4 v[180:183], v248, s[36:39], s69 offen sc1\n\t"
      "s_add_u32 s69, s69, 0x8000\n\t"
      "buffer_load_dwordx4 v[184:187], v248, s[36:39], s69 offen sc1\n\t"
      "s_add_u32 s69, s69, 0x8000\n\t"
      "buffer_load_dwordx4 v[188:191], v248, s[36:39], s69 offen sc1\n\t"
      "s_mov_b32 s65, 1\n\t"
      "WB0%=:\n\t"
      "v_mfma_f32_16x16x32_bf16 v[0:3], v[128:131], a[160:163], 0\n\t"
      "v_mfma_f32_16x16x32_bf16 v[4:7], v[128:131], a[164:167], 0\n\t"
      "v_mfma_f32_16x16x32_bf16 v[0:3], v[132:135], a[168:171], v[0:3]\n\t"
      "v_mfma_f32_16x16x32_bf16 v[4:7], v[132:135], a[172:175], v[4:7]\n\t"
      "v_mfma_f32_16x16x32_bf16 v[0:3], v[136:139], a[176:179], v[0:3]\n\t"
      "v_mfma_f32_16x16x32_bf16 v[4:7], v[136:139], a[180:183], v[4:7]\n\t"
      "v_mfma_f32_16x16x32_bf16 v[0:3], v[140:143], a[184:187], v[0:3]\n\t"
      "v_mfma_f32_16x16x32_bf16 v[4:7], v[140:143], a[188:191], v[4:7]\n\t"
      "s_add_u32 s51, s51, 262144\n\t"
      "s_add_u32 s52, s52, 262144\n\t"
      "s_nop 4\n\t"
      "v_max3_f32 v8, v0, v1, v2\n\t"
      "v_max3_f32 v8, v8, v3, v234\n\t"
      "v_max3_f32 v9, v4, v5, v6\n\t"
      "v_max3_f32 v9, v9, v7, v235\n\t"
      "s_nop 1\n\t"
      "v_permlane16_swap_b32 v8, v9\n\t"
      "v_max_f32 v8, v8, v9\n\t"
      "v_mov_b32 v9, v8\n\t"
      "s_nop 1\n\t"
      "v_permlane16_swap_b32 v8, v9\n\t"
      "v_pk_mul_f32 v[8:9], v[8:9], s[86:87]\n\t"
      "v_pk_fma_f32 v[10:11], v[234:235], s[86:87], v[8:9] neg_lo:[0,0,1] neg_hi:[0,0,1]\n\t"
      "v_pk_fma_f32 v[0:1], v[0:1], s[86:87], v[8:9] op_sel:[0,0,0] op_sel_hi:[1,1,0] neg_lo:[0,0,1] neg_hi:[0,0,1]\n\t"
      "v_pk_fma_f32 v[2:3], v[2:3], s[86:87], v[8:9] op_sel:[0,0,0] op_sel_hi:[1,1,0] neg_lo:[0,0,1] neg_hi:[0,0,1]\n\t"
      "v_pk_fma_f32 v[4:5], v[4:5], s[86:87], v[8:9] op_sel:[0,0,1] op_sel_hi:[1,1,1] neg_lo:[0,0,1] neg_hi:[0,0,1]\n\t"
      "v_pk_fma_f32 v[6:7], v[6:7], s[86:87], v[8:9] op_sel:[0,0,1] op_sel_hi:[1,1,1] neg_lo:[0,0,1] neg_hi:[0,0,1]\n\t"
      "v_exp_f32 v10, v10\n\t"
      "v_exp_f32 v11, v11\n\t"
      "v_exp_f32 v0, v0\n\t"
      "v_exp_f32 v1, v1\n\t"
      "v_exp_f32 v2, v2\n\t"
      "v_exp_f32 v3, v3\n\t"
      "v_exp_f32 v4, v4\n\t"
      "v_exp_f32 v5, v5\n\t"
      "v_exp_f32 v6, v6\n\t"
      "v_exp_f32 v7, v7\n\t"
      "v_pk_mul_f32 v[12:13], v[236:237], v[10:11]\n\t"
      "v_pk_add_f32 v[14:15], v[0:1], v[2:3]\n\t"
      "v_pk_add_f32 v[16:17], v[4:5], v[6:7]\n\t"
      "v_add_f32 v14, v14, v15\n\t"
      "v_add_f32 v15, v16, v17\n\t"
      "v_fma_f32 v12, v14, 0.5, v12\n\t"
      "v_fma_f32 v13, v15, 0.5, v13\n\t"
      "s_nop 1\n\t"
      "v_permlane16_swap_b32 v12, v13\n\t"
      "v_add_f32 v12, v12, v13\n\t"
      "v_mov_b32 v13, v12\n\t"
      "s_nop 1\n\t"
      "v_permlane32_swap_b32 v12, v13\n\t"
      "v_add_f32 v12, v12, v13\n\t"
      "v_mov_b32 v13, v12\n\t"
      "s_nop 1\n\t"
      "v_permlane16_swap_b32 v12, v13\n\t"
      "v_rcp_f32 v12, v12\n\t"
      "v_rcp_f32 v13, v13\n\t"
      "s_nop 0\n\t"
      "v_pk_mul_f32 v[10:11], v[10:11], v[12:13]\n\t"
      "v_pk_mul_f32 v[12:13], v[12:13], 0.5 op_sel_hi:[1,0]\n\t"
      "v_pk_mul_f32 v[0:1], v[0:1], v[12:13] op_sel:[0,0] op_sel_hi:[1,0]\n\t"
      "v_pk_mul_f32 v[2:3], v[2:3], v[12:13] op_sel:[0,0] op_sel_hi:[1,0]\n\t"
      "v_pk_mul_f32 v[4:5], v[4:5], v[12:13] op_sel:[0,1] op_sel_hi:[1,1]\n\t"
      "v_pk_mul_f32 v[6:7], v[6:7], v[12:13] op_sel:[0,1] op_sel_hi:[1,1]\n\t"
      "v_cvt_pk_bf16_f32 v16, v0, v1\n\t"
      "v_cvt_pk_bf16_f32 v17, v2, v3\n\t"
      "v_cvt_pk_bf16_f32 v18, v4, v5\n\t"
      "v_cvt_pk_bf16_f32 v19, v6, v7\n\t"
      "v_pk_mul_f32 v[64:65], v[64:65], v[10:11] op_sel:[0,0] op_sel_hi:[1,0]\n\t"
      "v_pk_mul_f32 v[66:67], v[66:67], v[10:11] op_sel:[0,0] op_sel_hi:[1,0]\n\t"
      "v_pk_mul_f32 v[68:69], v[68:69], v[10:11] op_sel:[0,0] op_sel_hi:[1,0]\n\t"
      "v_pk_mul_f32 v[70:71], v[70:71], v[10:11] op_sel:[0,0] op_sel_hi:[1,0]\n\t"
      "v_mfma_f32_16x16x16_bf16 v[64:67], v[144:145], v[16:17], v[64:67]\n\t"
      "v_pk_mul_f32 v[72:73], v[72:73], v[10:11] op_sel:[0,0] op_sel_hi:[1,0]\n\t"
      "v_pk_mul_f32 v[74:75], v[74:75], v[10:11] op_sel:[0,0] op_sel_hi:[1,0]\n\t"
      "v_mfma_f32_16x16x16_bf16 v[68:71], v[146:147], v[16:17], v[68:71]\n\t"
      "v_pk_mul_f32 v[76:77], v[76:77], v[10:11] op_sel:[0,0] op_sel_hi:[1,0]\n\t"
      "v_pk_mul_f32 v[78:79], v[78:79], v[10:11] op_sel:[0,0] op_sel_hi:[1,0]\n\t"
      "v_mfma_f32_16x16x16_bf16 v[72:75], v[148:149], v[16:17], v[72:75]\n\t"
      "v_pk_mul_f32 v[80:81], v[80:81], v[10:11] op_sel:[0,0] op_sel_hi:[1,0]\n\t"
      "v_pk_mul_f32 v[82:83], v[82:83], v[10:11] op_sel:[0,0] op_sel_hi:[1,0]\n\t"
      "v_mfma_f32_16x16x16_bf16 v[76:79], v[150:151], v[16:17], v[76:79]\n\t"
      "v_pk_mul_f32 v[84:85], v[84:85], v[10:11] op_sel:[0,0] op_sel_hi:[1,0]\n\t"
      "v_pk_mul_f32 v[86:87], v[86:87], v[10:11] op_sel:[0,0] op_sel_hi:[1,0]\n\t"
      "v_mfma_f32_16x16x16_bf16 v[80:83], v[152:153], v[16:17], v[80:83]\n\t"
      "v_pk_mul_f32 v[88:89], v[88:89], v[10:11] op_sel:[0,0] op_sel_hi:[1,0]\n\t"
      "v_pk_mul_f32 v[90:91], v[90:91], v[10:11] op_sel:[0,0] op_sel_hi:[1,0]\n\t"
      "v_mfma_f32_16x16x16_bf16 v[84:87], v[154:155], v[16:17], v[84:87]\n\t"
      "v_pk_mul_f32 v[92:93], v[92:93], v[10:11] op_sel:[0,0] op_sel_hi:[1,0]\n\t"
      "v_pk_mul_f32 v[94:95], v[94:95], v[10:11] op_sel:[0,0] op_sel_hi:[1,0]\n\t"
      "v_cvt_pk_bf16_f32 v24, v64, v65\n\t"
      "v_cvt_pk_bf16_f32 v25, v66, v67\n\t"
      "v_cvt_pk_bf16_f32 v26, v68, v69\n\t"
      "v_cvt_pk_bf16_f32 v27, v70, v71\n\t"
      "ds_write_b128 v254, v[24:27]\n\t"
      "v_mfma_f32_16x16x16_bf16 v[88:91], v[156:157], v[16:17], v[88:91]\n\t"
      "v_pk_mul_f32 v[96:97], v[96:97], v[10:11] op_sel:[0,1] op_sel_hi:[1,1]\n\t"
      "v_pk_mul_f32 v[98:99], v[98:99], v[10:11] op_sel:[0,1] op_sel_hi:[1,1]\n\t"
      "v_mfma_f32_16x16x16_bf16 v[92:95], v[158:159], v[16:17], v[92:95]\n\t"
      "v_pk_mul_f32 v[100:101], v[100:101], v[10:11] op_sel:[0,1] op_sel_hi:[1,1]\n\t"
      "v_pk_mul_f32 v[102:103], v[102:103], v[10:11] op_sel:[0,1] op_sel_hi:[1,1]\n\t"
      "v_cvt_pk_bf16_f32 v28, v72, v73\n\t"
      "v_cvt_pk_bf16_f32 v29, v74, v75\n\t"
      "v_cvt_pk_bf16_f32 v30, v76, v77\n\t"
      "v_cvt_pk_bf16_f32 v31, v78, v79\n\t"
      "ds_write_b128 v254, v[28:31] offset:2048\n\t"
      "v_mfma_f32_16x16x16_bf16 v[96:99], v[144:145], v[18:19], v[96:99]\n\t"
      "v_pk_mul_f32 v[104:105], v[104:105], v[10:11] op_sel:[0,1] op_sel_hi:[1,1]\n\t"
      "v_pk_mul_f32 v[106:107], v[106:107], v[10:11] op_sel:[0,1] op_sel_hi:[1,1]\n\t"
      "v_mfma_f32_16x16x16_bf16 v[100:103], v[146:147], v[18:19], v[100:103]\n\t"
      "v_pk_mul_f32 v[108:109], v[108:109], v[10:11] op_sel:[0,1] op_sel_hi:[1,1]\n\t"
      "v_pk_mul_f32 v[110:111], v[110:111], v[10:11] op_sel:[0,1] op_sel_hi:[1,1]\n\t"
      "v_cvt_pk_bf16_f32 v24, v80, v81\n\t"
      "v_cvt_pk_bf16_f32 v25, v82, v83\n\t"
      "v_cvt_pk_bf16_f32 v26, v84, v85\n\t"
      "v_cvt_pk_bf16_f32 v27, v86, v87\n\t"
      "ds_write_b128 v254, v[24:27] offset:4096\n\t"
      "v_mfma_f32_16x16x16_bf16 v[104:107], v[148:149], v[18:19], v[104:107]\n\t"
      "v_pk_mul_f32 v[112:113], v[112:113], v[10:11] op_sel:[0,1] op_sel_hi:[1,1]\n\t"
      "v_pk_mul_f32 v[114:115], v[114:115], v[10:11] op_sel:[0,1] op_sel_hi:[1,1]\n\t"
      "v_mfma_f32_16x16x16_bf16 v[108:111], v[150:151], v[18:19], v[108:111]\n\t"
      "v_pk_mul_f32 v[116:117], v[116:117], v[10:11] op_sel:[0,1] op_sel_hi:[1,1]\n\t"
      "v_pk_mul_f32 v[118:119], v[118:119], v[10:11] op_sel:[0,1] op_sel_hi:[1,1]\n\t"
      "v_cvt_pk_bf16_f32 v28, v88, v89\n\t"
      "v_cvt_pk_bf16_f32 v29, v90, v91\n\t"
      "v_cvt_pk_bf16_f32 v30, v92, v93\n\t"
      "v_cvt_pk_bf16_f32 v31, v94, v95\n\t"
      "ds_write_b128 v254, v[28:31] offset:6144\n\t"
      "v_mfma_f32_16x16x16_bf16 v[112:115], v[152:153], v[18:19], v[112:115]\n\t"
      "v_pk_mul_f32 v[120:121], v[120:121], v[10:11] op_sel:[0,1] op_sel_hi:[1,1]\n\t"
      "v_pk_mul_f32 v[122:123], v[122:123], v[10:11] op_sel:[0,1] op_sel_hi:[1,1]\n\t"
      "v_mfma_f32_16x16x16_bf16 v[116:119], v[154:155], v[18:19], v[116:119]\n\t"
      "v_pk_mul_f32 v[124:125], v[124:125], v[10:11] op_sel:[0,1] op_sel_hi:[1,1]\n\t"
      "v_pk_mul_f32 v[126:127], v[126:127], v[10:11] op_sel:[0,1] op_sel_hi:[1,1]\n\t"
      "v_cvt_pk_bf16_f32 v24, v96, v97\n\t"
      "v_cvt_pk_bf16_f32 v25, v98, v99\n\t"
      "v_cvt_pk_bf16_f32 v26, v100, v101\n\t"
      "v_cvt_pk_bf16_f32 v27, v102, v103\n\t"
      "ds_write_b128 v254, v[24:27] offset:1024\n\t"
      "v_mfma_f32_16x16x16_bf16 v[120:123], v[156:157], v[18:19], v[120:123]\n\t"
      "v_mfma_f32_16x16x16_bf16 v[124:127], v[158:159], v[18:19], v[124:127]\n\t"
      "v_cvt_pk_bf16_f32 v28, v104, v105\n\t"
      "v_cvt_pk_bf16_f32 v29, v106, v107\n\t"
      "v_cvt_pk_bf16_f32 v30, v108, v109\n\t"
      "v_cvt_pk_bf16_f32 v31, v110, v111\n\t"
      "ds_write_b128 v254, v[28:31] offset:3072\n\t"
      "v_cvt_pk_bf16_f32 v24, v112, v113\n\t"
      "v_cvt_pk_bf16_f32 v25, v114, v115\n\t"
      "v_cvt_pk_bf16_f32 v26, v116, v117\n\t"
      "v_cvt_pk_bf16_f32 v27, v118, v119\n\t"
      "ds_write_b128 v254, v[24:27] offset:5120\n\t"
      "v_cvt_pk_bf16_f32 v28, v120, v121\n\t"
      "v_cvt_pk_bf16_f32 v29, v122, v123\n\t"
      "v_cvt_pk_bf16_f32 v30, v124, v125\n\t"
      "v_cvt_pk_bf16_f32 v31, v126, v127\n\t"
      "ds_write_b128 v254, v[28:31] offset:7168\n\t"
      "s_waitcnt vmcnt(40) lgkmcnt(0)\n\t"
      "s_barrier\n\t"
      "WO%=:\n\t"
      "s_waitcnt vmcnt(16)\n\t"
      "ds_read_b128 v[64:67], v255\n\t"
      "ds_read_b128 v[68:71], v255 offset:1024\n\t"
      "ds_read_b128 v[72:75], v255 offset:2048\n\t"
      "ds_read_b128 v[76:79], v255 offset:3072\n\t"
      "ds_read_b128 v[80:83], v255 offset:4096\n\t"
      "ds_read_b128 v[84:87], v255 offset:5120\n\t"
      "ds_read_b128 v[88:91], v255 offset:6144\n\t"
      "ds_read_b128 v[92:95], v255 offset:7168\n\t"
      "ds_read_b128 v[96:99], v255 offset:8192\n\t"
      "ds_read_b128 v[100:103], v255 offset:9216\n\t"
      "ds_read_b128 v[104:107], v255 offset:10240\n\t"
      "ds_read_b128 v[108:111], v255 offset:11264\n\t"
      "s_cmp_ge_u32 s48, 32\n\t"
      "s_cbranch_scc1 WO3%=\n\t"
      "v_mov_b64 v[20:21], -1\n\t"
      "v_mov_b64 v[22:23], -1\n\t"
      "buffer_store_dwordx4 v[20:23], v238, s[36:39], s98 offen sc1\n\t"
      "s_bitcmp0_b32 s48, 2\n\t"
      "s_cselect_b32 s69, 1, 63\n\t"
      "s_lshl_b32 s69, s69, 14\n\t"
      "s_add_u32 s98, s98, s69\n\t"
      "WO3%=:\n\t"
      "v_lshlrev_b32 v16, 4, v248\n\t"
      "s_cmp_ge_u32 s48, 60\n\t"
      "s_cbranch_scc1 WO1%=\n\t"
      "buffer_load_dwordx4 v[228:231], off, s[36:39], s56 sc1\n\t"
      "s_mov_b32 s97, s96\n\t"
      "v_mov_b64 v[232:233], 0\n\t"
      "s_cmp_lg_u32 s65, 0\n\t"
      "s_cselect_b32 s70, s89, s49\n\t"
      "s_cmp_ge_u32 s70, s48\n\t"
      "s_cbranch_scc1 WO1%=\n\t"
      "buffer_load_dword v232, v16, s[36:39], s55 offen sc1\n\t"
      "buffer_load_dword v233, v16, s[36:39], s55 offen offset:128 sc1\n\t"
      "WO1%=:\n\t"
      "s_cmp_lt_u32 s48, 56\n\t"
      "s_cselect_b32 s69, 3145728, 0\n\t"
      "s_add_u32 s50, s50, s69\n\t"
      "s_mov_b32 s24, 0\n\t"
      "s_cmp_eq_u32 s65, 2\n\t"
      "s_cbranch_scc1 WOM2%=\n\t"
      "s_cmp_lt_u32 s48, 60\n\t"
      "s_cselect_b32 s69, s65, 0\n\t"
      "s_cmp_lg_u32 s69, 0\n\t"
      "s_cbranch_scc1 WOM1%=\n\t"
      "s_cmp_lg_u32 s96, 0\n\t"
      "s_cbranch_scc1 WOL1%=\n\t"
      "WOL0%=:\n\t"
      "s_waitcnt lgkmcnt(11)\n\t"
      "v_mfma_f32_16x16x32_bf16 v[0:3], a[0:3], v[64:67], 0\n\t"
      "v_mfma_f32_16x16x32_bf16 v[4:7], a[64:67], v[64:67], 0\n\t"
      "ds_read_b128 v[112:115], v255 offset:12288\n\t"
      "s_waitcnt lgkmcnt(11)\n\t"
      "v_mfma_f32_16x16x32_bf16 v[8:11], a[0:3], v[68:71], 0\n\t"
      "v_mfma_f32_16x16x32_bf16 v[12:15], a[64:67], v[68:71], 0\n\t"
      "ds_read_b128 v[116:119], v255 offset:13312\n\t"
      "s_waitcnt lgkmcnt(11)\n\t"
      "v_mfma_f32_16x16x32_bf16 v[0:3], a[4:7], v[72:75], v[0:3]\n\t"
      "v_mfma_f32_16x16x32_bf16 v[4:7], a[68:71], v[72:75], v[4:7]\n\t"
      "ds_read_b128 v[120:123], v255 offset:14336\n\t"
      "s_waitcnt lgkmcnt(11)\n\t"
      "v_mfma_f32_16x16x32_bf16 v[8:11], a[4:7], v[76:79], v[8:11]\n\t"
      "v_mfma_f32_16x16x32_bf16 v[12:15], a[68:71], v[76:79], v[12:15]\n\t"
      "ds_read_b128 v[124:127], v255 offset:15360\n\t"
      "s_waitcnt lgkmcnt(11)\n\t"
      "v_mfma_f32_16x16x32_bf16 v[0:3], a[8:11], v[80:83], v[0:3]\n\t"
      "v_mfma_f32_16x16x32_bf16 v[4:7], a[72:75], v[80:83], v[4:7]\n\t"
      "ds_read_b128 v[64:67], v255 offset:16384\n\t"
      "s_waitcnt lgkmcnt(11)\n\t"
      "v_mfma_f32_16x16x32_bf16 v[8:11], a[8:11], v[84:87], v[8:11]\n\t"
      "v_mfma_f32_16x16x32_bf16 v[12:15], a[72:75], v[84:87], v[12:15]\n\t"
      "ds_read_b128 v[68:71], v255 offset:17408\n\t"
      "s_waitcnt lgkmcnt(11)\n\t"
      "v_mfma_f32_16x16x32_bf16 v[0:3], a[12:15], v[88:91], v[0:3]\n\t"
      "v_mfma_f32_16x16x32_bf16 v[4:7], a[76:79], v[88:91], v[4:7]\n\t"
      "ds_read_b128 v[72:75], v255 offset:18432\n\t"
      "s_waitcnt lgkmcnt(11)\n\t"
      "v_mfma_f32_16x16x32_bf16 v[8:11], a[12:15], v[92:95], v[8:11]\n\t"
      "v_mfma_f32_16x16x32_bf16 v[12:15], a[76:79], v[92:95], v[12:15]\n\t"
      "ds_read_b128 v[76:79], v255 offset:19456\n\t"
      "s_waitcnt lgkmcnt(11)\n\t"
      "v_mfma_f32_16x16x32_bf16 v[0:3], a[16:19], v[96:99], v[0:3]\n\t"
      "v_mfma_f32_16x16x32_bf16 v[4:7], a[80:83], v[96:99], v[4:7]\n\t"
      "ds_read_b128 v[80:83], v255 offset:20480\n\t"
      "s_waitcnt lgkmcnt(11)\n\t"
      "v_mfma_f32_16x16x32_bf16 v[8:11], a[16:19], v[100:103], v[8:11]\n\t"
      "v_mfma_f32_16x16x32_bf16 v[12:15], a[80:83], v[100:103], v[12:15]\n\t"
      "ds_read_b128 v[84:87], v255 offset:21504\n\t"
      "s_waitcnt lgkmcnt(11)\n\t"
      "v_mfma_f32_16x16x32_bf16 v[0:3], a[20:23], v[104:107], v[0:3]\n\t"
      "v_mfma_f32_16x16x32_bf16 v[4:7], a[84:87], v[104:107], v[4:7]\n\t"
      "ds_read_b128 v[88:91], v255 offset:22528\n\t"
      "s_waitcnt lgkmcnt(11)\n\t"
      "v_mfma_f32_16x16x32_bf16 v[8:11], a[20:23], v[108:111], v[8:11]\n\t"
      "v_mfma_f32_16x16x32_bf16 v[12:15], a[84:87], v[108:111], v[12:15]\n\t"
      "ds_read_b128 v[92:95], v255 offset:23552\n\t"
      "s_waitcnt lgkmcnt(11)\n\t"
      "v_mfma_f32_16x16x32_bf16 v[0:3], a[24:27], v[112:115], v[0:3]\n\t"
      "v_mfma_f32_16x16x32_bf16 v[4:7], a[88:91], v[112:115], v[4:7]\n\t"
      "ds_read_b128 v[96:99], v255 offset:24576\n\t"
      "s_waitcnt lgkmcnt(11)\n\t"
      "v_mfma_f32_16x16x32_bf16 v[8:11], a[24:27], v[116:119], v[8:11]\n\t"
      "v_mfma_f32_16x16x32_bf16 v[12:15], a[88:91], v[116:119], v[12:15]\n\t"
      "ds_read_b128 v[100:103], v255 offset:25600\n\t"
      "s_waitcnt lgkmcnt(11)\n\t"
      "v_mfma_f32_16x16x32_bf16 v[0:3], a[28:31], v[120:123], v[0:3]\n\t"
      "v_mfma_f32_16x16x32_bf16 v[4:7], a[92:95], v[120:123], v[4:7]\n\t"
      "ds_read_b128 v[104:107], v255 offset:26624\n\t"
      "s_waitcnt lgkmcnt(11)\n\t"
      "v_mfma_f32_16x16x32_bf16 v[8:11], a[28:31], v[124:127], v[8:11]\n\t"
      "v_mfma_f32_16x16x32_bf16 v[12:15], a[92:95], v[124:127], v[12:15]\n\t"
      "ds_read_b128 v[108:111], v255 offset:27648\n\t"
      "s_waitcnt lgkmcnt(11)\n\t"
      "v_mfma_f32_16x16x32_bf16 v[0:3], a[32:35], v[64:67], v[0:3]\n\t"
      "v_mfma_f32_16x16x32_bf16 v[4:7], a[96:99], v[64:67], v[4:7]\n\t"
      "ds_read_b128 v[112:115], v255 offset:28672\n\t"
      "s_waitcnt lgkmcnt(11)\n\t"
      "v_mfma_f32_16x16x32_bf16 v[8:11], a[32:35], v[68:71], v[8:11]\n\t"
      "v_mfma_f32_16x16x32_bf16 v[12:15], a[96:99], v[68:71], v[12:15]\n\t"
      "ds_read_b128 v[116:119], v255 offset:29696\n\t"
      "s_waitcnt lgkmcnt(11)\n\t"
      "v_mfma_f32_16x16x32_bf16 v[0:3], a[36:39], v[72:75], v[0:3]\n\t"
      "v_mfma_f32_16x16x32_bf16 v[4:7], a[100:103], v[72:75], v[4:7]\n\t"
      "ds_read_b128 v[120:123], v255 offset:30720\n\t"
      "s_waitcnt lgkmcnt(11)\n\t"
      "v_mfma_f32_16x16x32_bf16 v[8:11], a[36:39], v[76:79], v[8:11]\n\t"
      "v_mfma_f32_16x16x32_bf16 v[12:15], a[100:103], v[76:79], v[12:15]\n\t"
      "ds_read_b128 v[124:127], v255 offset:31744\n\t"
      "s_waitcnt lgkmcnt(11)\n\t"
      "v_mfma_f32_16x16x32_bf16 v[0:3], a[40:43], v[80:83], v[0:3]\n\t"
      "v_mfma_f32_16x16x32_bf16 v[4:7], a[104:107], v[80:83], v[4:7]\n\t"
      "s_waitcnt lgkmcnt(10)\n\t"
      "v_mfma_f32_16x16x32_bf16 v[8:11], a[40:43], v[84:87], v[8:11]\n\t"
      "v_mfma_f32_16x16x32_bf16 v[12:15], a[104:107], v[84:87], v[12:15]\n\t"
      "s_waitcnt lgkmcnt(9)\n\t"
      "v_mfma_f32_16x16x32_bf16 v[0:3], a[44:47], v[88:91], v[0:3]\n\t"
      "v_mfma_f32_16x16x32_bf16 v[4:7], a[108:111], v[88:91], v[4:7]\n\t"
      "s_waitcnt lgkmcnt(8)\n\t"
      "v_mfma_f32_16x16x32_bf16 v[8:11], a[44:47], v[92:95], v[8:11]\n\t"
      "v_mfma_f32_16x16x32_bf16 v[12:15], a[108:111], v[92:95], v[12:15]\n\t"
      "s_waitcnt lgkmcnt(7)\n\t"
      "v_mfma_f32_16x16x32_bf16 v[0:3], a[48:51], v[96:99], v[0:3]\n\t"
      "v_mfma_f32_16x16x32_bf16 v[4:7], a[112:115], v[96:99], v[4:7]\n\t"
      "s_waitcnt lgkmcnt(6)\n\t"
      "v_mfma_f32_16x16x32_bf16 v[8:11], a[48:51], v[100:103], v[8:11]\n\t"
      "v_mfma_f32_16x16x32_bf16 v[12:15], a[112:115], v[100:103], v[12:15]\n\t"
      "s_waitcnt lgkmcnt(5)\n\t"
      "v_mfma_f32_16x16x32_bf16 v[0:3], a[52:55], v[104:107], v[0:3]\n\t"
      "v_mfma_f32_16x16x32_bf16 v[4:7], a[116:119], v[104:107], v[4:7]\n\t"
      "s_waitcnt lgkmcnt(4)\n\t"
      "v_mfma_f32_16x16x32_bf16 v[8:11], a[52:55], v[108:111], v[8:11]\n\t"
      "v_mfma_f32_16x16x32_bf16 v[12:15], a[116:119], v[108:111], v[12:15]\n\t"
      "s_waitcnt lgkmcnt(3)\n\t"
      "v_mfma_f32_16x16x32_bf16 v[0:3], a[56:59], v[112:115], v[0:3]\n\t"
      "v_mfma_f32_16x16x32_bf16 v[4:7], a[120:123], v[112:115], v[4:7]\n\t"
      "s_waitcnt lgkmcnt(2)\n\t"
      "v_mfma_f32_16x16x32_bf16 v[8:11], a[56:59], v[116:119], v[8:11]\n\t"
      "v_mfma_f32_16x16x32_bf16 v[12:15], a[120:123], v[116:119], v[12:15]\n\t"
      "s_waitcnt lgkmcnt(1)\n\t"
      "v_mfma_f32_16x16x32_bf16 v[0:3], a[60:63], v[120:123], v[0:3]\n\t"
      "v_mfma_f32_16x16x32_bf16 v[4:7], a[124:127], v[120:123], v[4:7]\n\t"
      "s_waitcnt lgkmcnt(0)\n\t"
      "v_mfma_f32_16x16x32_bf16 v[8:11], a[60:63], v[124:127], v[8:11]\n\t"
      "v_mfma_f32_16x16x32_bf16 v[12:15], a[124:127], v[124:127], v[12:15]\n\t"
      "v_xor_b32 v254, 0x8000, v254\n\t"
      "v_xor_b32 v255, 0x8000, v255\n\t"
      "s_add_u32 s69, s53, 0x4000\n\t"
      "s_add_u32 s48, s48, 4\n\t"
      "s_waitcnt vmcnt(40)\n\t"
      "v_cvt_pk_bf16_f32 v0, v0, v1\n\t"
      "v_cvt_pk_bf16_f32 v1, v2, v3\n\t"
      "v_cvt_pk_bf16_f32 v2, v4, v5\n\t"
      "v_cvt_pk_bf16_f32 v3, v6, v7\n\t"
      "v_min_u32 v0, -2, v0\n\t"
      "v_min_u32 v1, -2, v1\n\t"
      "v_min_u32 v2, -2, v2\n\t"
      "v_min_u32 v3, -2, v3\n\t"
      "buffer_store_dwordx4 v[0:3], v238, s[36:39], s53 offen sc1\n\t"
      "v_cvt_pk_bf16_f32 v8, v8, v9\n\t"
      "v_cvt_pk_bf16_f32 v9, v10, v11\n\t"
      "v_cvt_pk_bf16_f32 v10, v12, v13\n\t"
      "v_cvt_pk_bf16_f32 v11, v14, v15\n\t"
      "v_min_u32 v8, -2, v8\n\t"
      "v_min_u32 v9, -2, v9\n\t"
      "v_min_u32 v10, -2, v10\n\t"
      "v_min_u32 v11, -2, v11\n\t"
      "buffer_store_dwordx4 v[8:11], v238, s[36:39], s69 offen sc1\n\t"
      "s_add_u32 s53, s53, 1048576\n\t"
      "s_nop 1\n\t"
      "s_branch WTOP%=\n\t"
      ".p2align 5\n\t"
      "WEND%=:\n\t"
      : "+{v[0:15]}"(c0), "+{v[16:31]}"(c1), "+{v[240:255]}"(c2), "+{v238}"(c3), "+{v[32:39]}"(c4), "+s"(w), "+s"(wv), "+s"(A),
        "+{a[0:31]}"(wo.a), "+{a[32:63]}"(wo.b), "+{a[64:95]}"(wo.c), "+{a[96:127]}"(wo.d)
      :
      : "memory", "vcc", "scc", "v40", "v41", "v42", "v43", "v44", "v45", "v46", "v47", "v48", "v49", "v50", "v51", "v52", "v53", "v54", "v55", "v56", "v57", "v58", "v59", "v60", "v61", "v62", "v63", "v64", "v65", "v66", "v67", "v68", "v69", "v70", "v71", "v72", "v73", "v74", "v75", "v76", "v77", "v78", "v79", "v80", "v81", "v82", "v83", "v84", "v85", "v86", "v87", "v88", "v89", "v90", "v91", "v92", "v93", "v94", "v95", "v96", "v97", "v98", "v99", "v100", "v101", "v102", "v103", "v104", "v105", "v106", "v107", "v108", "v109", "v110", "v111", "v112", "v113", "v114", "v115", "v116", "v117", "v118", "v119", "v120", "v121", "v122", "v123", "v124", "v125", "v126", "v127", "v128", "v129", "v130", "v131", "v132", "v133", "v134", "v135", "v136", "v137", "v138", "v139", "v140", "v141", "v142", "v143", "v144", "v145", "v146", "v147", "v148", "v149", "v150", "v151", "v152", "v153", "v154", "v155", "v156", "v157", "v158", "v159", "v160", "v161", "v162", "v163", "v164", "v165", "v166", "v167", "v168", "v169", "v170", "v171", "v172", "v173", "v174", "v175", "v176", "v177", "v178", "v179", "v180", "v181", "v182", "v183", "v184", "v185", "v186", "v187", "v188", "v189", "v190", "v191", "v192", "v193", "v194", "v195", "v196", "v197", "v198", "v199", "v200", "v201", "v202", "v203", "v204", "v205", "v206", "v207", "v208", "v209", "v210", "v211", "v212", "v213", "v214", "v215", "v216", "v217", "v218", "v219", "v220", "v221", "v222", "v223", "v224", "v225", "v226", "v227", "v228", "v229", "v230", "v231", "v232", "v233", "v234", "v235", "v236", "v237", "v239", "a128", "a129", "a130", "a131", "a132", "a133", "a134", "a135", "a136", "a137", "a138", "a139", "a140", "a141", "a142", "a143", "a144", "a145", "a146", "a147", "a148", "a149", "a150", "a151", "a152", "a153", "a154", "a155", "a156", "a157", "a158", "a159", "a160", "a161", "a162", "a163", "a164", "a165", "a166", "a167", "a168", "a169", "a170", "a171", "a172", "a173", "a174", "a175", "a176", "a177", "a178", "a179", "a180", "a181", "a182", "a183", "a184", "a185", "a186", "a187", "a188", "a189", "a190", "a191", "a192", "a193", "a194", "a195", "a196", "a197", "a198", "a199", "a200", "a201", "a202", "a203", "a204", "a205", "a206", "a207", "a208", "a209", "a210", "a211", "a212", "a213", "a214", "a215", "a216", "a217", "a218", "a219", "a220", "a221", "a222", "a223", "a224", "a225", "a226", "a227", "a228", "a229", "a230", "a231", "a232", "a233", "a234", "a235", "a236", "a237", "a238", "a239", "a240", "a241", "a242", "a243", "a244", "a245", "a246", "a247", "a248", "a249", "a250", "a251", "a252", "a253", "a254", "a255", "s24", "s36", "s37", "s38", "s39", "s40", "s41", "s42", "s43", "s44", "s45", "s46", "s47", "s48", "s49", "s50", "s51", "s52", "s53", "s54", "s55", "s56", "s57", "s58", "s59", "s60", "s61", "s62", "s63", "s64", "s65", "s66", "s67", "s68", "s69", "s70", "s71", "s72", "s73", "s74", "s75", "s76", "s77", "s78", "s79", "s80", "s81", "s82", "s83", "s84", "s85", "s86", "s87", "s88", "s89", "s90", "s91", "s92", "s93", "s94", "s95", "s96", "s97", "s98", "s99");
}

__global__ void __launch_bounds__(256, 1) mega_kernel(MegaParams P) {
  __shared__ __attribute__((aligned(16))) char lds[LDS_BYTES];
  int b, wv;
  KArgs A = (KArgs)__builtin_amdgcn_kernarg_segment_ptr();
  u32x16 ka; u32x8 kb;
  asm volatile("s_load_dwordx16 s[36:51], %4, 0\n\t"
               "s_load_dwordx8 s[52:59], %4, 64\n\t"
               "s_getpc_b64 s[60:61]\n"
               "KPC:\n\t"
               "s_lshr_b32 %0, %5, 3\n\t"
               "s_and_b32 %0, %0, 7\n\t"
               "s_lshl_b32 %0, %0, 14\n\t"
               "v_lshl_add_u32 v254, %6, 6, %0\n\t"
               "v_min_u32 v254, KENDW-KPC, v254\n\t"
               "global_load_dword v255, v254, s[60:61]\n\t"
               "v_readfirstlane_b32 %1, %6\n\t"
               "s_lshr_b32 %0, %5, 1\n\t"
               "s_andn2_b32 %0, %0, 3\n\t"
               "s_and_b32 s60, %5, 3\n\t"
               "s_or_b32 %0, %0, s60\n\t"
               "s_lshr_b32 %1, %1, 6\n\t"
               "s_bitcmp1_b32 %5, 2\n\t"
               "s_cbranch_scc1 GSTART\n\t"
               "s_waitcnt lgkmcnt(0)"
               : "=&{s34}"(b), "=&{s33}"(wv), "=&{s[36:51]}"(ka), "=&{s[52:59]}"(kb), "+{s[0:1]}"(A)
               : "s"(blockIdx.x), "v"(threadIdx.x) : "v252", "v253", "v254", "v255", "s60", "s61", "s62", "s63", "scc");
  static_assert(XS == 0, "");
  if (b >= NREC) {
    int w = b, wv2 = wv;
    WoRegs wo;
    const unsigned tag = worker_role(w, wv2, A, wo);
    worker_tasks(lds, w, wv2, A, tag, wo);
    asm volatile(".p2align 6");
    if (w == 0 && wv2 == 0) {
      const int lane = fresh_lane();
#pragma unroll 1
      for (unsigned spins = 0; spins < (1u << 16); ++spins) {
        const u32x2 sel = top2_wave((const float*)(A->ws + O_RBUF) + S_, lane, spins == 0, spins < 8);
        if (spins && sel[1]) {
          if (lane < 8) __hip_atomic_store((gu32*)(A->ws + O_CNT + lane * 64 + 8), (unsigned)__builtin_amdgcn_s_memrealtime(), RLX_AGENT);
          if (lane < 8) __hip_atomic_store((gu64*)(A->ws + O_CNT + lane * 64), (u64)sel[0] | ((u64)sel[1] << 32), RLX_AGENT);
          break;
        }
        __builtin_amdgcn_s_sleep(SSLEEP);
        if (spins == 0 && SWAIT) {
          unsigned f = 0;
#pragma unroll 1
          for (unsigned n = 0; n < 4096; ++n) {
            f = __builtin_amdgcn_readfirstlane(__hip_atomic_load((gu32*)(A->ws + O_KVSTEP), RLX_AGENT));
            if ((f >> 16) >= 61) break;
            __builtin_amdgcn_s_sleep(4);
          }
          f += SWAIT - ((f >> 16) - 61) * 156;
#pragma unroll 1
          while (((f - (unsigned)__builtin_amdgcn_s_memrealtime()) & 0xffffu) < 400u) __builtin_amdgcn_s_sleep(1);
        }
      }
    }
    __syncthreads();
    if ((w & 2) == 0) mlp2_role(A, lds, (w & 1) * 32 + (w >> 2), wv2 * 64 + fresh_lane());
    asm volatile("KENDW:");
    __builtin_amdgcn_endpgm();
    __builtin_unreachable();
  } else {
    KArgs kp = A;
    rec_role(lds, kp, b, wv, ka, kb);
    asm volatile(".p2align 6");
    const int tid = wv * 64 + fresh_lane();
    __syncthreads();
    if ((b & 31) < M1_WGS / 4) mlp1_role(kp, lds, (b & 31) * 4 + (b >> 5), tid);
    else if (SW2 && b == M1_WGS / 4 + 1) {
      if (wv == 0) {
        const unsigned t0 = (unsigned)__builtin_amdgcn_s_memrealtime();
#pragma unroll 1
        for (unsigned spins = 0; spins < (1u << 16); ++spins) {
          const u32x2 sel = top2_wave((const float*)(kp->ws + O_RBUF) + S_, tid, spins == 0, false);
          if (spins && sel[1]) {
            if (tid < 32) __hip_atomic_store((gu64*)(kp->ws + O_SEL2 + tid * 128), (u64)sel[0] | ((u64)sel[1] << 32), RLX_AGENT);
            break;
          }
#pragma unroll 1
          while ((unsigned)__builtin_amdgcn_s_memrealtime() - t0 < SW2) __builtin_amdgcn_s_sleep(1);
        }
      }
    }
    else if (b == M1_WGS / 4) {
      if (tid == 64) {
        gu32* e = (gu32*)(kp->ws + O_EPOCH);
        __hip_atomic_store(e, mktag(__hip_atomic_load(e, RLX_AGENT)), RLX_AGENT);
      }
    }
    __builtin_amdgcn_endpgm();
    __builtin_unreachable();
  }
}

extern "C" void kernel_launch(void* const* d_in, const int* in_sizes, int n_in, void* d_out, int out_size, void* d_ws, size_t ws_size,
                              hipStream_t stream) {
  MegaParams P;
  P.x = (const u16*)d_in[0]; P.mem0 = (const u16*)d_in[1];
  P.Wq = (const u16*)d_in[2]; P.Wk = (const u16*)d_in[3]; P.Wv = (const u16*)d_in[4]; P.Wo = (const u16*)d_in[5];
  P.w_r = (const u16*)d_in[8]; P.wg = (const u16*)d_in[9]; P.wu = (const u16*)d_in[10]; P.wd = (const u16*)d_in[11];
  P.out = (u16*)d_out; P.ws = (char*)d_ws;
  mega_kernel<<<256, 256, 0, stream>>>(P);
}
```

```cpp
#include <hip/hip_runtime.h>
#include <hip/hip_bf16.h>
#define SWAIT 0
#define SW2 0
#define ASLEEP 32
#define ASALL 0
#define PSLEEP 8
#define FSLEEP 6
#define RDLY 250
#define RDL2 100
#define ADLY 130
#define CPF 1
#define TCH 0
#define SNEED 0
#define MPF 1

typedef unsigned short u16;
typedef unsigned long long u64;
typedef __attribute__((ext_vector_type(8))) short bf16x8;
typedef __attribute__((ext_vector_type(4))) float f32x4;
typedef __attribute__((ext_vector_type(4))) unsigned u32x4;
#define GAS __attribute__((address_space(1)))
typedef GAS u64 gu64;
typedef GAS unsigned gu32;
#define RLX_AGENT __ATOMIC_RELAXED, __HIP_MEMORY_SCOPE_AGENT

constexpr int S_ = 2048, DM = 512, NH = 32, DH = 128, MM = 8, SEG = 32, NSEG = 64;
constexpr int HD = NH * DH;
constexpr int QKVN = 3 * HD;
constexpr int FF = 1792;

__device__ __forceinline__ float bf2f(u16 v) { return __builtin_bit_cast(float, (unsigned)v << 16); }
__device__ __forceinline__ u16 f2bf(float f) { unsigned u = __builtin_bit_cast(unsigned, f); return (u16)((u + 0x7fffu + ((u >> 16) & 1u)) >> 16); }
__device__ __forceinline__ float rbf(float f) { return bf2f(f2bf(f)); }

typedef __attribute__((ext_vector_type(2))) float f32x2;
typedef __attribute__((ext_vector_type(2))) __bf16 bf16x2_t;
typedef __attribute__((ext_vector_type(2))) unsigned u32x2;
__device__ __forceinline__ unsigned pk2(float a, float b) { return __builtin_bit_cast(unsigned, __builtin_convertvector((f32x2){a, b}, bf16x2_t)); }
__device__ __forceinline__ unsigned f2u(float f) { return __builtin_bit_cast(unsigned, f); }
__device__ __forceinline__ float u2f(unsigned u) { return __builtin_bit_cast(float, u); }
__device__ __forceinline__ float lo2f(unsigned u) { return __builtin_bit_cast(float, u << 16); }
__device__ __forceinline__ float hi2f(unsigned u) { return __builtin_bit_cast(float, u & 0xffff0000u); }
__device__ __forceinline__ void unpack8(const u32x4 v, float (&f)[8]) {
#pragma unroll
  for (int i = 0; i < 4; ++i) { f[2 * i] = lo2f(v[i]); f[2 * i + 1] = hi2f(v[i]); }
}

constexpr unsigned O_EPOCH = 0;
constexpr unsigned O_TFLAG = 256;
constexpr unsigned O_MFLAG = O_TFLAG + 16 * 32 * 64;
constexpr unsigned O_LFLAG = O_MFLAG + 32 * 128;
constexpr unsigned O_WM = O_LFLAG + 128 * 128;
constexpr unsigned O_KVSTEP = O_WM + 128 * 128;
constexpr unsigned O_CNT = O_KVSTEP + 2048;
constexpr unsigned O_PFLAG = O_CNT + 2048;
constexpr unsigned O_SEL2 = O_PFLAG + 4 * 128 * 128;
constexpr unsigned O_ACT = O_SEL2 + 32 * 128;
constexpr unsigned O_RBUF = O_ACT + 3 * 112 * 128;
constexpr unsigned O_INIT_END = O_RBUF + 2 * S_ * 4 + 256 * 128;
constexpr unsigned O_STAMPS = 196608 + 65 * 32768;
constexpr unsigned O_XN = O_STAMPS + (4 << 20);
constexpr unsigned O_QKV = O_XN + S_ * DM * 2;
constexpr unsigned O_KVM = O_QKV + S_ * QKVN * 2;
constexpr unsigned O_VTM = O_KVM + NSEG * 8 * HD * 2;
constexpr unsigned O_PART = O_VTM + NSEG * 8 * HD * 2;
constexpr unsigned O_HBUF = O_PART + S_ * 8 * DM * 4;
constexpr unsigned O_LNH = O_HBUF + S_ * DM * 2;
constexpr unsigned O_MFRAG = O_LNH + S_ * DM * 2;
constexpr unsigned O_SLOTS = (O_MFRAG + 2 * DM * FF * 2 + 0x1fffff) & ~0x1fffffu;
constexpr unsigned WS_BYTES = O_SLOTS + 65 * 32768;
#define QO(i) (((i) & 1) * 16384 + ((i) >> 1) * 8192)
#define CAL(b) (O_SLOTS + 128 + ((b) >> 3) * 16384 + ((b) & 7) * 1024)
static_assert(O_INIT_END <= O_STAMPS && (O_INIT_END - O_KVSTEP) / 16 <= 128 * 128, "layout");
static_assert(O_TFLAG == 256 && O_MFLAG == 33024 && O_LFLAG == 37120 && O_STAMPS == 2326528 && O_XN == 6520832 && O_QKV == 8617984 && O_KVM == 58949632 && O_VTM == 63143936 && O_PART == 67338240 && O_MFRAG == 105086976 && O_SLOTS == 109051904 && O_CNT + 1024 == 72960 && O_INIT_END <= 262144 && O_WM == 53504, "layout");
constexpr int L_BS = 520, L_QN = 49152, L_TB = 98304, L_VT = 32768;
struct MegaParams {
  const u16* x; const u16* mem0; const u16* Wq; const u16* Wk; const u16* Wv; const u16* Wo;
  const u16* w_r; const u16* wg; const u16* wu; const u16* wd;
  u16* out;
  char* ws;
};
__device__ __forceinline__ unsigned mktag(unsigned e) { return e * 0x9e3779b1u + 0x7f4a7c15u; }

typedef __attribute__((ext_vector_type(4))) short s16x4;
#define LAS __attribute__((address_space(3)))
constexpr int LDS_BYTES = 163840;
#define SB() __builtin_amdgcn_sched_barrier(0)
#define LDS_BARRIER() asm volatile("s_waitcnt lgkmcnt(0)\n\ts_barrier" ::: "memory")
#define VM_DRAIN() asm volatile("s_waitcnt vmcnt(0)" ::: "memory")
__device__ __forceinline__ float xmax16_32(float m) {
  u32x2 p = __builtin_amdgcn_permlane16_swap(f2u(m), f2u(m), false, false);
  m = fmaxf(u2f(p[0]), u2f(p[1]));
  p = __builtin_amdgcn_permlane32_swap(f2u(m), f2u(m), false, false);
  return fmaxf(u2f(p[0]), u2f(p[1]));
}
__device__ __forceinline__ float xsum16_32(float m) {
  u32x2 p = __builtin_amdgcn_permlane16_swap(f2u(m), f2u(m), false, false);
  m = u2f(p[0]) + u2f(p[1]);
  p = __builtin_amdgcn_permlane32_swap(f2u(m), f2u(m), false, false);
  return u2f(p[0]) + u2f(p[1]);
}
__device__ __forceinline__ unsigned wave_max_u32(unsigned v) {
  v = max(v, (unsigned)__builtin_amdgcn_mov_dpp(v, 0xb1, 0xf, 0xf, true));
  v = max(v, (unsigned)__builtin_amdgcn_mov_dpp(v, 0x4e, 0xf, 0xf, true));
  v = max(v, (unsigned)__builtin_amdgcn_mov_dpp(v, 0x124, 0xf, 0xf, true));
  v = max(v, (unsigned)__builtin_amdgcn_mov_dpp(v, 0x128, 0xf, 0xf, true));
  u32x2 p = __builtin_amdgcn_permlane16_swap(v, v, false, false);
  v = max(p[0], p[1]);
  p = __builtin_amdgcn_permlane32_swap(v, v, false, false);
  return max(p[0], p[1]);
}
__device__ __forceinline__ u32x4 ld_sc1(__amdgpu_buffer_rsrc_t rs, unsigned off) { return __builtin_amdgcn_raw_buffer_load_b128(rs, off, 0, 16); }
__device__ __forceinline__ u32x2 ld8_sc1(__amdgpu_buffer_rsrc_t rs, unsigned off) { return __builtin_amdgcn_raw_buffer_load_b64(rs, off, 0, 16); }

#define XS 0
#define VBX __builtin_amdgcn_readfirstlane((int)(((blockIdx.x & 4) << 5) + (blockIdx.x >> 3) * 4 + (blockIdx.x & 3)))
constexpr int NREC = 128, NWORK = 128;
typedef __attribute__((ext_vector_type(8))) unsigned u32x8;
typedef __attribute__((ext_vector_type(16))) unsigned u32x16;
typedef __attribute__((ext_vector_type(32))) unsigned u32x32;
#define RSRC(p, n) __builtin_amdgcn_make_buffer_rsrc((void*)(p), 0, (n), 0x00020000)

__device__ __forceinline__ int fresh_lane() {
  int l;
  asm volatile("v_mbcnt_lo_u32_b32 %0, -1, 0\n\tv_mbcnt_hi_u32_b32 %0, -1, %0" : "=v"(l));
  return l;
}
typedef const __attribute__((address_space(4))) MegaParams* KArgs;
#define ARGS() KArgs A = (KArgs)__builtin_amdgcn_kernarg_segment_ptr(); asm volatile("" : "+s"(A))
#define MS(k)
__device__ __forceinline__ void rec_role(char* lds, KArgs& A, int& b, int& wv, const u32x16 ka, const u32x8 kb) {
#define HMAP 0
  const int h = HMAP == 1 ? (b & 3) * 8 + ((b >> 2) & 7) : (HMAP == 2 ? (b & 1) * 16 + (b & 2) / 2 + ((b >> 2) & 7) * 2 : b & 31), part = b >> 5;
  const int lane = fresh_lane(), l15 = lane & 15, l4 = lane >> 4;
  const int b1 = (l15 >> 1) & 1, mrow = 2 * (l15 >> 2) + (l15 & 1);
  const int r4 = l15 >> 2, c4 = l15 & 3;
#define KA(v, i) ((u64)v[i] | (u64)v[(i) + 1] << 32)
  const u64 wsa = KA(kb, 6), woa = KA(ka, 10), wva = KA(ka, 8), wqa = KA(ka, 4), wka = KA(ka, 6), m0a = KA(ka, 2);
  const unsigned awv = (unsigned)((8 * wv + l4) * 8192 + h * 256 + ((l15 ^ (l4 | (wv << 2))) << 4)), rot = 1 ? part : 0;
  const int sig = 1 ? (h >> 2) & 1 : 0;
  const int sig2 = 1 ? (h >> 3) & 1 : 0;
  unsigned tch;
  asm volatile(
      "s_lshl_b32 s91, %5, 4\n\t"
      "s_add_u32 m0, %5, 0\n\t"
      "s_xor_b32 s90, s91, 0\n\t"
      "buffer_load_dwordx4 %1, %3, s90 offen lds\n\t"
      "s_add_u32 m0, %5, 1024\n\t"
      "s_xor_b32 s90, s91, 16384\n\t"
      "buffer_load_dwordx4 %1, %3, s90 offen lds\n\t"
      "s_add_u32 m0, %5, 8192\n\t"
      "s_xor_b32 s90, s91, 32768\n\t"
      "buffer_load_dwordx4 %1, %3, s90 offen lds\n\t"
      "s_add_u32 m0, %5, 9216\n\t"
      "s_xor_b32 s90, s91, 49152\n\t"
      "buffer_load_dwordx4 %1, %3, s90 offen lds\n\t"
      "s_add_u32 m0, %5, 16384\n\t"
      "s_xor_b32 s90, s91, 65536\n\t"
      "buffer_load_dwordx4 %1, %3, s90 offen lds\n\t"
      "s_add_u32 m0, %5, 17408\n\t"
      "s_xor_b32 s90, s91, 81920\n\t"
      "buffer_load_dwordx4 %1, %3, s90 offen lds\n\t"
      "s_add_u32 m0, %5, 24576\n\t"
      "s_xor_b32 s90, s91, 98304\n\t"
      "buffer_load_dwordx4 %1, %3, s90 offen lds\n\t"
      "s_add_u32 m0, %5, 25600\n\t"
      "s_xor_b32 s90, s91, 114688\n\t"
      "buffer_load_dwordx4 %1, %3, s90 offen lds\n\t"
      : "=&{s87}"(tch)
      : "v"((unsigned)((h * DH + (lane >> 2)) * DM + 128 * part + 32 * wv + 8 * (2 * (lane & 1) + (((lane >> 1) & 1) ^ sig))) * 2), "v"(awv), "s"(RSRC(woa, DM * HD * 2)), "s"(RSRC(wva, DM * HD * 2)),
        "s"(wv * 2048), "s"(rot << 19), "s"(wva), "s"(wqa), "s"(wka), "s"(wsa), "s"(m0a)
      : "memory", "scc", "s90", "s91", "s92");
  SB();
  const unsigned i0 = 1024 + b * 512 + wv * 64 + lane, o1 = O_KVSTEP + ((wv >> 1) * 8192 + b * 64 + lane) * 16;
  const unsigned oz = wv == 2 ? CAL(b ^ 1) + (lane >> 5) * 8192 + ((lane >> 3) & 3) * 256 + (lane & 7) * 16 : ((wv & 1) && o1 < O_INIT_END ? o1 : 0xffffff00u);
  const int sl = lane ^ ((l4 & 1) << 1), su = ((l15 >> 3) * 16 + c4 * 4 + l4) ^ ((l15 >> 3) << 1);
  const int jt = wv ^ rot;
  u32x8 cc = {oz, (unsigned)lane, (unsigned)lane,
              wv == 2 || o1 < O_ACT ? 0u : ~0u,
              (unsigned)(QO(jt >> 1) + (((jt & 1) * 2 + (l4 >> 1)) * 8 + (r4 & 1) * 2 + (l15 >> 3)) * 256 + ((l15 & 1) ^ sig2) * 64 + (2 * b1 + (l4 & 1)) * 16),
              (unsigned)(h * 2048 + (2 * (part & 1) + (l4 >> 1)) * 512 + (2 * b1 + (l4 & 1)) * 128 + mrow * 16),
              (unsigned)(h * 2048 + part * 512 + (l4 >> 1) * 256 + (8 * wv + 4 * (l15 >> 3) + c4) * 16 + (r4 & 1) * 8 + (l4 & 1) * 4),
              (unsigned)((2 * (wv >> 1) + (l4 >> 1)) * 2048 + ((2 * (wv & 1) + (l4 & 1)) * 16 + l15) * 16)};
  u32x8 c3 = {(unsigned)lane, O_SLOTS + (i0 >> 3) * 256 + (i0 & 7) * 16,
              (unsigned)(wv * 2048 + (8 * l4 + r4) * 64 + c4 * 16),
              (unsigned)(wv * 2048 + (r4 >> 1) * 1024 + (l4 * 16 + 8 * (c4 >> 1) + 4 * (r4 & 1) + (c4 & 1)) * 16),
              (unsigned)(L_VT + (8 * r4 + c4) * 256 + (((4 * wv + l4) ^ l15) << 4)),
              awv,
              (unsigned)(L_VT + ((wv & 1) * 32 + 8 * l4 + r4) * 256 + (((4 * part + 2 * (wv >> 1) + (c4 >> 1)) ^ (r4 | (l4 << 2))) << 4) + (c4 & 1) * 8),
              (unsigned)(QO(part) + h * 256 + (lane & 7) * 16)};
  const int L = (h & 7) * 8 + (lane & 7);
  const u64 mp = 1 ? (u64)((const u16*)m0a + (4 * (l15 >> 3) + 2 * ((l15 & 1) ^ sig2) + (r4 & 1)) * DM + 64 * jt + 16 * l4 + 2 * b1) :
                (u64)((const u16*)m0a + (2 * ((L >> 2) & 3) + ((L >> 4) & 1)) * DM + 128 * part + 32 * (h >> 3) + 8 * (L >> 5) + 16 * (L & 1) + (L & 2));
  u32x8 c0 = {(unsigned)((8 * wv + l4) * 8192 + h * 256 + ((l15 ^ (2 * l4 + 8 * (wv & 1))) << 4)),
              (unsigned)(L_TB + (8 * l4 + r4) * 256 + (((4 * wv + (c4 >> 1)) ^ (2 * r4 + 8 * (l4 & 1))) << 4) + (c4 & 1) * 8),
              (unsigned)mp, (unsigned)(mp >> 32), (unsigned)(CAL(b) + wv * 256 + ((wv ^ (b >> 2)) & 1) * 8192) & (unsigned)((b - 32) >> 31) & (unsigned)((1 + 1 - wv) >> 31), (unsigned)(QO(part) + sig * 1024 + sig2 * 512),
              b1 ? 0xEEEEu : 0x4444u, (unsigned)((wv * 8 + (lane >> 5)) * 256 + (lane & 31) * 4)};
  u32x4 c1 = {(unsigned)(sl * 8),
                    (unsigned)(L_QN + ((wv >> 1) * 2 + (r4 & 1)) * L_BS + ((wv & 1) * 32 + su) * 8),
                    (unsigned)lane, (unsigned)lane * 16};
  const unsigned k0 = O_QKV + h * 24576, v0 = O_QKV + 8192 + SEG * QKVN * 2 + h * 24576 + wv * 2048;
  const unsigned role = wv < 2 ? wv : (wv == 3 ? 4 : (part < 2 ? 2 + part : 5));
  u32x16 sc = {(unsigned)lane, (unsigned)wv, role, O_SLOTS, k0, O_MFRAG + (min(h, 27) * 4 + part) * 32768 + wv * 4096, v0, rot << 19, O_KVM, O_VTM,
                     O_TFLAG + h * 64, O_KVSTEP + h * 64 + part * 4, O_STAMPS + (b * 4 + wv) * 65 * 64,
                     O_EPOCH, O_MFLAG + h * 128, (unsigned)wv * 2048};
  u32x16 s2 = {(unsigned)woa, (unsigned)(woa >> 32) & 0xffffu, DM * HD * 2, 0x00020000u, (unsigned)wva, (unsigned)(wva >> 32) & 0xffffu, O_WM, O_WM + b * 128,
               (unsigned)wsa, (unsigned)(wsa >> 32) & 0xffffu, WS_BYTES, 0x00020000u,
               (unsigned)wqa, (unsigned)(wqa >> 32) & 0xffffu, (unsigned)wka, (unsigned)(wka >> 32) & 0xffffu};
  asm volatile(
      "s_nop 1\n\t"
      "v_readfirstlane_b32 s40, v0\n\t"
      "v_readfirstlane_b32 s41, v1\n\t"
      "v_readfirstlane_b32 s42, v2\n\t"
      "v_readfirstlane_b32 s43, v3\n\t"
      "v_readfirstlane_b32 s44, v4\n\t"
      "v_readfirstlane_b32 s45, v5\n\t"
      "v_readfirstlane_b32 s46, v6\n\t"
      "v_readfirstlane_b32 s47, v7\n\t"
      "v_readfirstlane_b32 s48, v8\n\t"
      "v_readfirstlane_b32 s49, v9\n\t"
      "v_readfirstlane_b32 s50, v10\n\t"
      "v_readfirstlane_b32 s51, v11\n\t"
      "v_readfirstlane_b32 s52, v12\n\t"
      "v_readfirstlane_b32 s53, v13\n\t"
      "v_readfirstlane_b32 s54, v14\n\t"
      "v_readfirstlane_b32 s55, v15\n\t"
      "v_readfirstlane_b32 s88, v16\n\t"
      "v_readfirstlane_b32 s89, v17\n\t"
      "v_readfirstlane_b32 s90, v18\n\t"
      "v_readfirstlane_b32 s91, v19\n\t"
      "v_readfirstlane_b32 s92, v20\n\t"
      "v_readfirstlane_b32 s93, v21\n\t"
      "v_readfirstlane_b32 s94, v22\n\t"
      "v_readfirstlane_b32 s95, v23\n\t"
      "v_readfirstlane_b32 s36, v24\n\t"
      "v_readfirstlane_b32 s37, v25\n\t"
      "v_readfirstlane_b32 s38, v26\n\t"
      "v_readfirstlane_b32 s39, v27\n\t"
      "v_readfirstlane_b32 s96, v28\n\t"
      "v_readfirstlane_b32 s97, v29\n\t"
      "v_readfirstlane_b32 s98, v30\n\t"
      "v_readfirstlane_b32 s99, v31\n\t"
      "s_nop 4\n\t"
      "s_memtime s[80:81]\n\t"
      "s_memrealtime s[82:83]\n\t"
      "v_readfirstlane_b32 s86, v120\n\t"
      "v_add_u32 v100, s55, v127\n\t"
      "s_mov_b64 s[88:89], s[92:93]\n\t"
      "s_add_u32 m0, s55, 32768\n\t"
      "s_xor_b32 s61, s47, 0\n\t"
      "buffer_load_dwordx4 v105, s[88:91], s61 offen lds\n\t"
      "s_add_u32 m0, s55, 33792\n\t"
      "s_xor_b32 s61, s47, 32768\n\t"
      "buffer_load_dwordx4 v105, s[88:91], s61 offen lds\n\t"
      "s_add_u32 m0, s55, 40960\n\t"
      "s_xor_b32 s61, s47, 262144\n\t"
      "buffer_load_dwordx4 v105, s[88:91], s61 offen lds\n\t"
      "s_add_u32 m0, s55, 41984\n\t"
      "s_xor_b32 s61, s47, 294912\n\t"
      "buffer_load_dwordx4 v105, s[88:91], s61 offen lds\n\t"
      "s_add_u32 m0, s55, 49152\n\t"
      "s_xor_b32 s61, s47, 524288\n\t"
      "buffer_load_dwordx4 v105, s[88:91], s61 offen lds\n\t"
      "s_add_u32 m0, s55, 50176\n\t"
      "s_xor_b32 s61, s47, 557056\n\t"
      "buffer_load_dwordx4 v105, s[88:91], s61 offen lds\n\t"
      "s_add_u32 m0, s55, 57344\n\t"
      "s_xor_b32 s61, s47, 786432\n\t"
      "buffer_load_dwordx4 v105, s[88:91], s61 offen lds\n\t"
      "s_add_u32 m0, s55, 58368\n\t"
      "s_xor_b32 s61, s47, 819200\n\t"
      "buffer_load_dwordx4 v105, s[88:91], s61 offen lds\n\t"
      "buffer_load_dword v126, off, s[36:39], s53 sc1\n\t"
      "global_load_dword a172, v[118:119], off offset:0\n\t"
      "global_load_dword a173, v[118:119], off offset:8\n\t"
      "global_load_dword a174, v[118:119], off offset:16\n\t"
      "global_load_dword a175, v[118:119], off offset:24\n\t"
      "global_load_dword a236, v[118:119], off offset:512\n\t"
      "global_load_dword a237, v[118:119], off offset:520\n\t"
      "global_load_dword a238, v[118:119], off offset:528\n\t"
      "global_load_dword a239, v[118:119], off offset:536\n\t"
      "s_xor_b32 s61, s86, 0x2000\n\t"
      "s_buffer_load_dword s87, s[36:39], s61\n\t"
      "s_waitcnt vmcnt(17)\n\t"
      "s_waitcnt lgkmcnt(0)\n\t"
      "ds_read_b64_tr_b16 v[64:65], v102\n\t"
      "ds_read_b64_tr_b16 v[66:67], v102 offset:256\n\t"
      "ds_read_b64_tr_b16 v[68:69], v102 offset:8\n\t"
      "ds_read_b64_tr_b16 v[70:71], v102 offset:264\n\t"
      "ds_read_b64_tr_b16 v[72:73], v102 offset:8192\n\t"
      "ds_read_b64_tr_b16 v[74:75], v102 offset:8448\n\t"
      "ds_read_b64_tr_b16 v[76:77], v102 offset:8200\n\t"
      "ds_read_b64_tr_b16 v[78:79], v102 offset:8456\n\t"
      "ds_read_b64_tr_b16 v[80:81], v102 offset:16384\n\t"
      "ds_read_b64_tr_b16 v[82:83], v102 offset:16640\n\t"
      "ds_read_b64_tr_b16 v[84:85], v102 offset:16392\n\t"
      "ds_read_b64_tr_b16 v[86:87], v102 offset:16648\n\t"
      "s_waitcnt lgkmcnt(4)\n\t"
      "ds_read_b64_tr_b16 v[88:89], v102 offset:24576\n\t"
      "ds_read_b64_tr_b16 v[90:91], v102 offset:24832\n\t"
      "ds_read_b64_tr_b16 v[92:93], v102 offset:24584\n\t"
      "ds_read_b64_tr_b16 v[94:95], v102 offset:24840\n\t"
      "s_mov_b64 s[88:89], s[96:97]\n\t"
      "s_add_u32 m0, s55, 98304\n\t"
      "s_xor_b32 s61, s47, 0\n\t"
      "buffer_load_dwordx4 v116, s[88:91], s61 offen lds\n\t"
      "s_add_u32 m0, s55, 99328\n\t"
      "s_xor_b32 s61, s47, 32768\n\t"
      "buffer_load_dwordx4 v116, s[88:91], s61 offen lds\n\t"
      "s_add_u32 m0, s55, 106496\n\t"
      "s_xor_b32 s61, s47, 262144\n\t"
      "buffer_load_dwordx4 v116, s[88:91], s61 offen lds\n\t"
      "s_add_u32 m0, s55, 107520\n\t"
      "s_xor_b32 s61, s47, 294912\n\t"
      "buffer_load_dwordx4 v116, s[88:91], s61 offen lds\n\t"
      "s_waitcnt lgkmcnt(0)\n\t"
      "ds_write_b128 v103, v[64:67]\n\t"
      "ds_write_b128 v103, v[68:71] offset:32\n\t"
      "ds_write_b128 v103, v[72:75] offset:8192\n\t"
      "ds_write_b128 v103, v[76:79] offset:8224\n\t"
      "ds_write_b128 v103, v[80:83] offset:16384\n\t"
      "ds_write_b128 v103, v[84:87] offset:16416\n\t"
      "ds_write_b128 v103, v[88:91] offset:24576\n\t"
      "ds_write_b128 v103, v[92:95] offset:24608\n\t"
      "s_waitcnt lgkmcnt(4)\n\t"
      "ds_read_b128 v[32:35], v100\n\t"
      "ds_read_b128 v[36:39], v100 offset:1024\n\t"
      "ds_read_b128 v[40:43], v100 offset:8192\n\t"
      "ds_read_b128 v[44:47], v100 offset:9216\n\t"
      "ds_read_b128 v[48:51], v100 offset:16384\n\t"
      "ds_read_b128 v[52:55], v100 offset:17408\n\t"
      "ds_read_b128 v[56:59], v100 offset:24576\n\t"
      "ds_read_b128 v[60:63], v100 offset:25600\n\t"
      "v_xor_b32 v118, 32, v117\n\t"
      "v_xor_b32 v119, 64, v104\n\t"
      "v_xor_b32 v102, 128, v104\n\t"
      "v_xor_b32 v103, 192, v104\n\t"
      "s_mov_b64 s[88:89], s[92:93]\n\t"
      "s_add_u32 m0, s55, 65536\n\t"
      "s_xor_b32 s61, s47, 1048576\n\t"
      "buffer_load_dwordx4 v105, s[88:91], s61 offen lds\n\t"
      "s_add_u32 m0, s55, 66560\n\t"
      "s_xor_b32 s61, s47, 1081344\n\t"
      "buffer_load_dwordx4 v105, s[88:91], s61 offen lds\n\t"
      "s_mov_b64 s[88:89], s[96:97]\n\t"
      "s_add_u32 m0, s55, 114688\n\t"
      "s_xor_b32 s61, s47, 524288\n\t"
      "buffer_load_dwordx4 v116, s[88:91], s61 offen lds\n\t"
      "s_add_u32 m0, s55, 115712\n\t"
      "s_xor_b32 s61, s47, 557056\n\t"
      "buffer_load_dwordx4 v116, s[88:91], s61 offen lds\n\t"
      "s_add_u32 m0, s55, 122880\n\t"
      "s_xor_b32 s61, s47, 786432\n\t"
      "buffer_load_dwordx4 v116, s[88:91], s61 offen lds\n\t"
      "s_add_u32 m0, s55, 123904\n\t"
      "s_xor_b32 s61, s47, 819200\n\t"
      "buffer_load_dwordx4 v116, s[88:91], s61 offen lds\n\t"
      "s_waitcnt lgkmcnt(0)\n\t"
      "s_waitcnt vmcnt(25)\n\t"
      "s_barrier\n\t"
      "ds_read_b128 v[0:3], v104\n\t"
      "ds_read_b128 v[4:7], v119\n\t"
      "ds_read_b128 v[8:11], v102\n\t"
      "ds_read_b128 v[12:15], v103\n\t"
      "ds_read_b128 v[64:67], v104 offset:1024\n\t"
      "ds_read_b128 v[68:71], v119 offset:1024\n\t"
      "ds_read_b128 v[72:75], v102 offset:1024\n\t"
      "ds_read_b128 v[76:79], v103 offset:1024\n\t"
      "s_mov_b64 s[88:89], s[92:93]\n\t"
      "s_add_u32 m0, s55, 73728\n\t"
      "s_xor_b32 s61, s47, 1310720\n\t"
      "buffer_load_dwordx4 v105, s[88:91], s61 offen lds\n\t"
      "s_add_u32 m0, s55, 74752\n\t"
      "s_xor_b32 s61, s47, 1343488\n\t"
      "buffer_load_dwordx4 v105, s[88:91], s61 offen lds\n\t"
      "s_mov_b64 s[88:89], s[96:97]\n\t"
      "s_add_u32 m0, s55, 131072\n\t"
      "s_xor_b32 s61, s47, 1048576\n\t"
      "buffer_load_dwordx4 v116, s[88:91], s61 offen lds\n\t"
      "s_add_u32 m0, s55, 132096\n\t"
      "s_xor_b32 s61, s47, 1081344\n\t"
      "buffer_load_dwordx4 v116, s[88:91], s61 offen lds\n\t"
      "s_add_u32 m0, s55, 139264\n\t"
      "s_xor_b32 s61, s47, 1310720\n\t"
      "buffer_load_dwordx4 v116, s[88:91], s61 offen lds\n\t"
      "s_add_u32 m0, s55, 140288\n\t"
      "s_xor_b32 s61, s47, 1343488\n\t"
      "buffer_load_dwordx4 v116, s[88:91], s61 offen lds\n\t"
      "s_mov_b64 s[88:89], s[92:93]\n\t"
      "s_add_u32 m0, s55, 81920\n\t"
      "s_xor_b32 s61, s47, 1572864\n\t"
      "buffer_load_dwordx4 v105, s[88:91], s61 offen lds\n\t"
      "s_add_u32 m0, s55, 82944\n\t"
      "s_xor_b32 s61, s47, 1605632\n\t"
      "buffer_load_dwordx4 v105, s[88:91], s61 offen lds\n\t"
      "s_waitcnt vmcnt(31)\n\t"
      "s_barrier\n\t"
      "s_waitcnt lgkmcnt(7)\n\t"
      "v_mfma_f32_16x16x32_bf16 v[16:19], v[0:3], v[32:35], 0\n\t"
      "ds_read_b128 v[80:83], v104 offset:8192\n\t"
      "v_mfma_f32_16x16x32_bf16 v[20:23], v[0:3], v[36:39], 0\n\t"
      "s_mov_b64 s[88:89], s[96:97]\n\t"
      "s_add_u32 m0, s55, 147456\n\t"
      "s_xor_b32 s61, s47, 1572864\n\t"
      "buffer_load_dwordx4 v116, s[88:91], s61 offen lds\n\t"
      "ds_read_b64_tr_b16 a[176:177], v106\n\t"
      "s_waitcnt lgkmcnt(8)\n\t"
      "v_mfma_f32_16x16x32_bf16 v[16:19], v[4:7], v[40:43], v[16:19]\n\t"
      "ds_read_b128 v[84:87], v119 offset:8192\n\t"
      "v_mfma_f32_16x16x32_bf16 v[20:23], v[4:7], v[44:47], v[20:23]\n\t"
      "s_add_u32 m0, s55, 148480\n\t"
      "s_xor_b32 s61, s47, 1605632\n\t"
      "buffer_load_dwordx4 v116, s[88:91], s61 offen lds\n\t"
      "ds_read_b64_tr_b16 a[178:179], v106 offset:1024\n\t"
      "s_waitcnt lgkmcnt(9)\n\t"
      "v_mfma_f32_16x16x32_bf16 v[16:19], v[8:11], v[48:51], v[16:19]\n\t"
      "ds_read_b128 v[88:91], v102 offset:8192\n\t"
      "v_mfma_f32_16x16x32_bf16 v[20:23], v[8:11], v[52:55], v[20:23]\n\t"
      "s_add_u32 m0, s55, 155648\n\t"
      "s_xor_b32 s61, s47, 1835008\n\t"
      "buffer_load_dwordx4 v116, s[88:91], s61 offen lds\n\t"
      "s_add_u32 m0, s55, 156672\n\t"
      "s_xor_b32 s61, s47, 1867776\n\t"
      "buffer_load_dwordx4 v116, s[88:91], s61 offen lds\n\t"
      "s_waitcnt lgkmcnt(9)\n\t"
      "v_mfma_f32_16x16x32_bf16 v[16:19], v[12:15], v[56:59], v[16:19]\n\t"
      "ds_read_b128 v[92:95], v103 offset:8192\n\t"
      "v_mfma_f32_16x16x32_bf16 v[20:23], v[12:15], v[60:63], v[20:23]\n\t"
      "s_mov_b64 s[88:89], s[92:93]\n\t"
      "s_add_u32 m0, s55, 90112\n\t"
      "s_xor_b32 s61, s47, 1835008\n\t"
      "buffer_load_dwordx4 v105, s[88:91], s61 offen lds\n\t"
      "s_add_u32 m0, s55, 91136\n\t"
      "s_xor_b32 s61, s47, 1867776\n\t"
      "buffer_load_dwordx4 v105, s[88:91], s61 offen lds\n\t"
      "s_waitcnt lgkmcnt(9)\n\t"
      "v_mfma_f32_16x16x32_bf16 v[24:27], v[64:67], v[32:35], 0\n\t"
      "ds_read_b128 v[0:3], v104 offset:9216\n\t"
      "v_mfma_f32_16x16x32_bf16 v[28:31], v[64:67], v[36:39], 0\n\t"
      "s_waitcnt lgkmcnt(9)\n\t"
      "v_mfma_f32_16x16x32_bf16 v[24:27], v[68:71], v[40:43], v[24:27]\n\t"
      "ds_read_b128 v[4:7], v119 offset:9216\n\t"
      "v_cvt_pk_bf16_f32 v128, v16, v17\n\t"
      "v_mfma_f32_16x16x32_bf16 v[28:31], v[68:71], v[44:47], v[28:31]\n\t"
      "v_cvt_pk_bf16_f32 v129, v18, v19\n\t"
      "s_waitcnt lgkmcnt(9)\n\t"
      "v_mfma_f32_16x16x32_bf16 v[24:27], v[72:75], v[48:51], v[24:27]\n\t"
      "ds_read_b128 v[8:11], v102 offset:9216\n\t"
      "v_cvt_pk_bf16_f32 v192, v20, v21\n\t"
      "v_mfma_f32_16x16x32_bf16 v[28:31], v[72:75], v[52:55], v[28:31]\n\t"
      "v_cvt_pk_bf16_f32 v193, v22, v23\n\t"
      "s_waitcnt lgkmcnt(9)\n\t"
      "v_mfma_f32_16x16x32_bf16 v[24:27], v[76:79], v[56:59], v[24:27]\n\t"
      "ds_read_b128 v[12:15], v103 offset:9216\n\t"
      "v_mfma_f32_16x16x32_bf16 v[28:31], v[76:79], v[60:63], v[28:31]\n\t"
      "s_waitcnt vmcnt(35)\n\t"
      "s_waitcnt lgkmcnt(6)\n\t"
      "s_barrier\n\t"
      "v_mfma_f32_16x16x32_bf16 v[16:19], v[80:83], v[32:35], 0\n\t"
      "ds_read_b128 v[64:67], v104 offset:16384\n\t"
      "v_mfma_f32_16x16x32_bf16 v[20:23], v[80:83], v[36:39], 0\n\t"
      "s_add_u32 m0, s55, 32768\n\t"
      "s_xor_b32 s61, s47, 2097152\n\t"
      "buffer_load_dwordx4 v105, s[88:91], s61 offen lds\n\t"
      "s_add_u32 m0, s55, 33792\n\t"
      "s_xor_b32 s61, s47, 2129920\n\t"
      "buffer_load_dwordx4 v105, s[88:91], s61 offen lds\n\t"
      "v_mfma_f32_16x16x32_bf16 v[16:19], v[84:87], v[40:43], v[16:19]\n\t"
      "ds_read_b128 v[68:71], v119 offset:16384\n\t"
      "v_cvt_pk_bf16_f32 v130, v24, v25\n\t"
      "v_mfma_f32_16x16x32_bf16 v[20:23], v[84:87], v[44:47], v[20:23]\n\t"
      "v_cvt_pk_bf16_f32 v131, v26, v27\n\t"
      "s_waitcnt lgkmcnt(7)\n\t"
      "v_mfma_f32_16x16x32_bf16 v[16:19], v[88:91], v[48:51], v[16:19]\n\t"
      "ds_read_b128 v[72:75], v102 offset:16384\n\t"
      "v_cvt_pk_bf16_f32 v194, v28, v29\n\t"
      "v_mfma_f32_16x16x32_bf16 v[20:23], v[88:91], v[52:55], v[20:23]\n\t"
      "v_cvt_pk_bf16_f32 v195, v30, v31\n\t"
      "s_waitcnt lgkmcnt(7)\n\t"
      "v_mfma_f32_16x16x32_bf16 v[16:19], v[92:95], v[56:59], v[16:19]\n\t"
      "ds_read_b128 v[76:79], v103 offset:16384\n\t"
      "v_mfma_f32_16x16x32_bf16 v[20:23], v[92:95], v[60:63], v[20:23]\n\t"
      "v_mfma_f32_16x16x32_bf16 a[168:171], v[128:131], v[128:131], 0\n\t"
      "s_waitcnt lgkmcnt(7)\n\t"
      "v_mfma_f32_16x16x32_bf16 v[24:27], v[0:3], v[32:35], 0\n\t"
      "ds_read_b128 v[80:83], v104 offset:17408\n\t"
      "v_mfma_f32_16x16x32_bf16 v[28:31], v[0:3], v[36:39], 0\n\t"
      "s_waitcnt lgkmcnt(7)\n\t"
      "v_mfma_f32_16x16x32_bf16 v[24:27], v[4:7], v[40:43], v[24:27]\n\t"
      "ds_read_b128 v[84:87], v119 offset:17408\n\t"
      "v_cvt_pk_bf16_f32 v132, v16, v17\n\t"
      "v_mfma_f32_16x16x32_bf16 v[28:31], v[4:7], v[44:47], v[28:31]\n\t"
      "v_cvt_pk_bf16_f32 v133, v18, v19\n\t"
      "s_waitcnt lgkmcnt(7)\n\t"
      "v_mfma_f32_16x16x32_bf16 v[24:27], v[8:11], v[48:51], v[24:27]\n\t"
      "ds_read_b128 v[88:91], v102 offset:17408\n\t"
      "v_cvt_pk_bf16_f32 v196, v20, v21\n\t"
      "v_mfma_f32_16x16x32_bf16 v[28:31], v[8:11], v[52:55], v[28:31]\n\t"
      "v_cvt_pk_bf16_f32 v197, v22, v23\n\t"
      "s_waitcnt lgkmcnt(7)\n\t"
      "v_mfma_f32_16x16x32_bf16 v[24:27], v[12:15], v[56:59], v[24:27]\n\t"
      "ds_read_b128 v[92:95], v103 offset:17408\n\t"
      "v_mfma_f32_16x16x32_bf16 v[28:31], v[12:15], v[60:63], v[28:31]\n\t"
      "v_mfma_f32_16x16x32_bf16 a[232:235], v[192:195], v[192:195], 0\n\t"
      "s_waitcnt vmcnt(35)\n\t"
      "s_waitcnt vmcnt(22)\n\t"
      "s_barrier\n\t"
      "s_waitcnt lgkmcnt(7)\n\t"
      "v_mfma_f32_16x16x32_bf16 v[16:19], v[64:67], v[32:35], 0\n\t"
      "ds_read_b128 v[0:3], v104 offset:24576\n\t"
      "v_mfma_f32_16x16x32_bf16 v[20:23], v[64:67], v[36:39], 0\n\t"
      "s_add_u32 m0, s55, 40960\n\t"
      "s_xor_b32 s61, s47, 2359296\n\t"
      "buffer_load_dwordx4 v105, s[88:91], s61 offen lds\n\t"
      "ds_read_b64_tr_b16 a[180:181], v106 offset:16384\n\t"
      "s_waitcnt lgkmcnt(8)\n\t"
      "v_mfma_f32_16x16x32_bf16 v[16:19], v[68:71], v[40:43], v[16:19]\n\t"
      "ds_read_b128 v[4:7], v119 offset:24576\n\t"
      "v_cvt_pk_bf16_f32 v134, v24, v25\n\t"
      "v_mfma_f32_16x16x32_bf16 v[20:23], v[68:71], v[44:47], v[20:23]\n\t"
      "s_add_u32 m0, s55, 41984\n\t"
      "s_xor_b32 s61, s47, 2392064\n\t"
      "buffer_load_dwordx4 v105, s[88:91], s61 offen lds\n\t"
      "ds_read_b64_tr_b16 a[182:183], v106 offset:17408\n\t"
      "v_cvt_pk_bf16_f32 v135, v26, v27\n\t"
      "s_waitcnt lgkmcnt(9)\n\t"
      "v_mfma_f32_16x16x32_bf16 v[16:19], v[72:75], v[48:51], v[16:19]\n\t"
      "ds_read_b128 v[8:11], v102 offset:24576\n\t"
      "v_cvt_pk_bf16_f32 v198, v28, v29\n\t"
      "v_mfma_f32_16x16x32_bf16 v[20:23], v[72:75], v[52:55], v[20:23]\n\t"
      "ds_read_b64_tr_b16 a[0:1], v117\n\t"
      "ds_read_b64_tr_b16 a[2:3], v117 offset:1024\n\t"
      "v_cvt_pk_bf16_f32 v199, v30, v31\n\t"
      "s_waitcnt lgkmcnt(11)\n\t"
      "v_mfma_f32_16x16x32_bf16 v[16:19], v[76:79], v[56:59], v[16:19]\n\t"
      "ds_read_b128 v[12:15], v103 offset:24576\n\t"
      "v_mfma_f32_16x16x32_bf16 v[20:23], v[76:79], v[60:63], v[20:23]\n\t"
      "ds_read_b64_tr_b16 a[64:65], v118\n\t"
      "ds_read_b64_tr_b16 a[66:67], v118 offset:1024\n\t"
      "v_mfma_f32_16x16x32_bf16 a[168:171], v[132:135], v[132:135], a[168:171]\n\t"
      "s_waitcnt lgkmcnt(13)\n\t"
      "v_mfma_f32_16x16x32_bf16 v[24:27], v[80:83], v[32:35], 0\n\t"
      "ds_read_b128 v[64:67], v104 offset:25600\n\t"
      "v_mfma_f32_16x16x32_bf16 v[28:31], v[80:83], v[36:39], 0\n\t"
      "ds_read_b64_tr_b16 a[4:5], v117 offset:8192\n\t"
      "s_waitcnt lgkmcnt(12)\n\t"
      "ds_read_b64_tr_b16 a[6:7], v117 offset:9216\n\t"
      "v_mfma_f32_16x16x32_bf16 v[24:27], v[84:87], v[40:43], v[24:27]\n\t"
      "ds_read_b128 v[68:71], v119 offset:25600\n\t"
      "v_cvt_pk_bf16_f32 v136, v16, v17\n\t"
      "v_mfma_f32_16x16x32_bf16 v[28:31], v[84:87], v[44:47], v[28:31]\n\t"
      "ds_read_b64_tr_b16 a[68:69], v118 offset:8192\n\t"
      "s_waitcnt lgkmcnt(12)\n\t"
      "ds_read_b64_tr_b16 a[70:71], v118 offset:9216\n\t"
      "v_cvt_pk_bf16_f32 v137, v18, v19\n\t"
      "v_mfma_f32_16x16x32_bf16 v[24:27], v[88:91], v[48:51], v[24:27]\n\t"
      "ds_read_b128 v[72:75], v102 offset:25600\n\t"
      "v_cvt_pk_bf16_f32 v200, v20, v21\n\t"
      "v_mfma_f32_16x16x32_bf16 v[28:31], v[88:91], v[52:55], v[28:31]\n\t"
      "v_cvt_pk_bf16_f32 v201, v22, v23\n\t"
      "v_mfma_f32_16x16x32_bf16 v[24:27], v[92:95], v[56:59], v[24:27]\n\t"
      "ds_read_b128 v[76:79], v103 offset:25600\n\t"
      "v_mfma_f32_16x16x32_bf16 v[28:31], v[92:95], v[60:63], v[28:31]\n\t"
      "v_mfma_f32_16x16x32_bf16 a[232:235], v[196:199], v[196:199], a[232:235]\n\t"
      "s_waitcnt vmcnt(22)\n\t"
      "s_waitcnt vmcnt(18)\n\t"
      "s_waitcnt lgkmcnt(2)\n\t"
      "s_barrier\n\t"
      "v_mfma_f32_16x16x32_bf16 v[16:19], v[0:3], v[32:35], 0\n\t"
      "ds_read_b128 v[80:83], v104 offset:32768\n\t"
      "v_mfma_f32_16x16x32_bf16 v[20:23], v[0:3], v[36:39], 0\n\t"
      "s_add_u32 m0, s55, 49152\n\t"
      "s_xor_b32 s61, s47, 2621440\n\t"
      "buffer_load_dwordx4 v105, s[88:91], s61 offen lds\n\t"
      "ds_read_b64_tr_b16 a[8:9], v117 offset:16384\n\t"
      "v_mfma_f32_16x16x32_bf16 v[16:19], v[4:7], v[40:43], v[16:19]\n\t"
      "ds_read_b128 v[84:87], v119 offset:32768\n\t"
      "v_cvt_pk_bf16_f32 v138, v24, v25\n\t"
      "v_mfma_f32_16x16x32_bf16 v[20:23], v[4:7], v[44:47], v[20:23]\n\t"
      "s_add_u32 m0, s55, 50176\n\t"
      "s_xor_b32 s61, s47, 2654208\n\t"
      "buffer_load_dwordx4 v105, s[88:91], s61 offen lds\n\t"
      "ds_read_b64_tr_b16 a[10:11], v117 offset:17408\n\t"
      "v_cvt_pk_bf16_f32 v139, v26, v27\n\t"
      "v_mfma_f32_16x16x32_bf16 v[16:19], v[8:11], v[48:51], v[16:19]\n\t"
      "ds_read_b128 v[88:91], v102 offset:32768\n\t"
      "v_cvt_pk_bf16_f32 v202, v28, v29\n\t"
      "v_mfma_f32_16x16x32_bf16 v[20:23], v[8:11], v[52:55], v[20:23]\n\t"
      "s_mov_b64 s[88:89], s[96:97]\n\t"
      "s_add_u32 m0, s55, 98304\n\t"
      "s_xor_b32 s61, s47, 2097152\n\t"
      "buffer_load_dwordx4 v116, s[88:91], s61 offen lds\n\t"
      "ds_read_b64_tr_b16 a[72:73], v118 offset:16384\n\t"
      "v_cvt_pk_bf16_f32 v203, v30, v31\n\t"
      "v_mfma_f32_16x16x32_bf16 v[16:19], v[12:15], v[56:59], v[16:19]\n\t"
      "ds_read_b128 v[92:95], v103 offset:32768\n\t"
      "v_mfma_f32_16x16x32_bf16 v[20:23], v[12:15], v[60:63], v[20:23]\n\t"
      "s_add_u32 m0, s55, 99328\n\t"
      "s_xor_b32 s61, s47, 2129920\n\t"
      "buffer_load_dwordx4 v116, s[88:91], s61 offen lds\n\t"
      "ds_read_b64_tr_b16 a[74:75], v118 offset:17408\n\t"
      "v_mfma_f32_16x16x32_bf16 a[168:171], v[136:139], v[136:139], a[168:171]\n\t"
      "v_mfma_f32_16x16x32_bf16 v[24:27], v[64:67], v[32:35], 0\n\t"
      "ds_read_b128 v[0:3], v104 offset:33792\n\t"
      "v_mfma_f32_16x16x32_bf16 v[28:31], v[64:67], v[36:39], 0\n\t"
      "s_add_u32 m0, s55, 106496\n\t"
      "s_xor_b32 s61, s47, 2359296\n\t"
      "buffer_load_dwordx4 v116, s[88:91], s61 offen lds\n\t"
      "ds_read_b64_tr_b16 a[12:13], v117 offset:24576\n\t"
      "v_mfma_f32_16x16x32_bf16 v[24:27], v[68:71], v[40:43], v[24:27]\n\t"
      "ds_read_b128 v[4:7], v119 offset:33792\n\t"
      "v_cvt_pk_bf16_f32 v140, v16, v17\n\t"
      "v_mfma_f32_16x16x32_bf16 v[28:31], v[68:71], v[44:47], v[28:31]\n\t"
      "s_add_u32 m0, s55, 107520\n\t"
      "s_xor_b32 s61, s47, 2392064\n\t"
      "buffer_load_dwordx4 v116, s[88:91], s61 offen lds\n\t"
      "ds_read_b64_tr_b16 a[14:15], v117 offset:25600\n\t"
      "v_cvt_pk_bf16_f32 v141, v18, v19\n\t"
      "s_waitcnt lgkmcnt(13)\n\t"
      "v_mfma_f32_16x16x32_bf16 v[24:27], v[72:75], v[48:51], v[24:27]\n\t"
      "ds_read_b128 v[8:11], v102 offset:33792\n\t"
      "v_cvt_pk_bf16_f32 v204, v20, v21\n\t"
      "v_mfma_f32_16x16x32_bf16 v[28:31], v[72:75], v[52:55], v[28:31]\n\t"
      "ds_read_b64_tr_b16 a[76:77], v118 offset:24576\n\t"
      "s_waitcnt lgkmcnt(12)\n\t"
      "ds_read_b64_tr_b16 a[78:79], v118 offset:25600\n\t"
      "v_cvt_pk_bf16_f32 v205, v22, v23\n\t"
      "v_mfma_f32_16x16x32_bf16 v[24:27], v[76:79], v[56:59], v[24:27]\n\t"
      "ds_read_b128 v[12:15], v103 offset:33792\n\t"
      "v_mfma_f32_16x16x32_bf16 v[28:31], v[76:79], v[60:63], v[28:31]\n\t"
      "v_mfma_f32_16x16x32_bf16 a[232:235], v[200:203], v[200:203], a[232:235]\n\t"
      "s_waitcnt vmcnt(22)\n\t"
      "s_waitcnt vmcnt(18)\n\t"
      "s_waitcnt lgkmcnt(1)\n\t"
      "s_barrier\n\t"
      "v_mfma_f32_16x16x32_bf16 v[16:19], v[80:83], v[32:35], 0\n\t"
      "ds_read_b128 v[64:67], v104 offset:40960\n\t"
      "v_mfma_f32_16x16x32_bf16 v[20:23], v[80:83], v[36:39], 0\n\t"
      "s_mov_b64 s[88:89], s[92:93]\n\t"
      "s_add_u32 m0, s55, 57344\n\t"
      "s_xor_b32 s61, s47, 2883584\n\t"
      "buffer_load_dwordx4 v105, s[88:91], s61 offen lds\n\t"
      "ds_read_b64_tr_b16 a[184:185], v106 offset:32768\n\t"
      "v_mfma_f32_16x16x32_bf16 v[16:19], v[84:87], v[40:43], v[16:19]\n\t"
      "ds_read_b128 v[68:71], v119 offset:40960\n\t"
      "v_cvt_pk_bf16_f32 v142, v24, v25\n\t"
      "v_mfma_f32_16x16x32_bf16 v[20:23], v[84:87], v[44:47], v[20:23]\n\t"
      "s_add_u32 m0, s55, 58368\n\t"
      "s_xor_b32 s61, s47, 2916352\n\t"
      "buffer_load_dwordx4 v105, s[88:91], s61 offen lds\n\t"
      "ds_read_b64_tr_b16 a[186:187], v106 offset:33792\n\t"
      "v_cvt_pk_bf16_f32 v143, v26, v27\n\t"
      "v_mfma_f32_16x16x32_bf16 v[16:19], v[88:91], v[48:51], v[16:19]\n\t"
      "ds_read_b128 v[72:75], v102 offset:40960\n\t"
      "v_cvt_pk_bf16_f32 v206, v28, v29\n\t"
      "v_mfma_f32_16x16x32_bf16 v[20:23], v[88:91], v[52:55], v[20:23]\n\t"
      "s_mov_b64 s[88:89], s[96:97]\n\t"
      "s_add_u32 m0, s55, 114688\n\t"
      "s_xor_b32 s61, s47, 2621440\n\t"
      "buffer_load_dwordx4 v116, s[88:91], s61 offen lds\n\t"
      "ds_read_b64_tr_b16 a[16:17], v117 offset:32768\n\t"
      "v_cvt_pk_bf16_f32 v207, v30, v31\n\t"
      "v_mfma_f32_16x16x32_bf16 v[16:19], v[92:95], v[56:59], v[16:19]\n\t"
      "ds_read_b128 v[76:79], v103 offset:40960\n\t"
      "v_mfma_f32_16x16x32_bf16 v[20:23], v[92:95], v[60:63], v[20:23]\n\t"
      "s_add_u32 m0, s55, 115712\n\t"
      "s_xor_b32 s61, s47, 2654208\n\t"
      "buffer_load_dwordx4 v116, s[88:91], s61 offen lds\n\t"
      "ds_read_b64_tr_b16 a[18:19], v117 offset:33792\n\t"
      "v_mfma_f32_16x16x32_bf16 a[168:171], v[140:143], v[140:143], a[168:171]\n\t"
      "v_mfma_f32_16x16x32_bf16 v[24:27], v[0:3], v[32:35], 0\n\t"
      "ds_read_b128 v[80:83], v104 offset:41984\n\t"
      "v_mfma_f32_16x16x32_bf16 v[28:31], v[0:3], v[36:39], 0\n\t"
      "s_add_u32 m0, s55, 122880\n\t"
      "s_xor_b32 s61, s47, 2883584\n\t"
      "buffer_load_dwordx4 v116, s[88:91], s61 offen lds\n\t"
      "ds_read_b64_tr_b16 a[80:81], v118 offset:32768\n\t"
      "v_mfma_f32_16x16x32_bf16 v[24:27], v[4:7], v[40:43], v[24:27]\n\t"
      "ds_read_b128 v[84:87], v119 offset:41984\n\t"
      "v_cvt_pk_bf16_f32 v144, v16, v17\n\t"
      "v_mfma_f32_16x16x32_bf16 v[28:31], v[4:7], v[44:47], v[28:31]\n\t"
      "s_add_u32 m0, s55, 123904\n\t"
      "s_xor_b32 s61, s47, 2916352\n\t"
      "buffer_load_dwordx4 v116, s[88:91], s61 offen lds\n\t"
      "ds_read_b64_tr_b16 a[82:83], v118 offset:33792\n\t"
      "v_cvt_pk_bf16_f32 v145, v18, v19\n\t"
      "v_mfma_f32_16x16x32_bf16 v[24:27], v[8:11], v[48:51], v[24:27]\n\t"
      "ds_read_b128 v[88:91], v102 offset:41984\n\t"
      "v_cvt_pk_bf16_f32 v208, v20, v21\n\t"
      "v_mfma_f32_16x16x32_bf16 v[28:31], v[8:11], v[52:55], v[28:31]\n\t"
      "ds_read_b64_tr_b16 a[20:21], v117 offset:40960\n\t"
      "s_waitcnt lgkmcnt(12)\n\t"
      "ds_read_b64_tr_b16 a[22:23], v117 offset:41984\n\t"
      "v_cvt_pk_bf16_f32 v209, v22, v23\n\t"
      "v_mfma_f32_16x16x32_bf16 v[24:27], v[12:15], v[56:59], v[24:27]\n\t"
      "ds_read_b128 v[92:95], v103 offset:41984\n\t"
      "v_mfma_f32_16x16x32_bf16 v[28:31], v[12:15], v[60:63], v[28:31]\n\t"
      "ds_read_b64_tr_b16 a[84:85], v118 offset:40960\n\t"
      "s_waitcnt lgkmcnt(12)\n\t"
      "ds_read_b64_tr_b16 a[86:87], v118 offset:41984\n\t"
      "v_mfma_f32_16x16x32_bf16 a[232:235], v[204:207], v[204:207], a[232:235]\n\t"
      "s_waitcnt vmcnt(22)\n\t"
      "s_waitcnt vmcnt(18)\n\t"
      "s_waitcnt lgkmcnt(0)\n\t"
      "s_barrier\n\t"
      "v_mfma_f32_16x16x32_bf16 v[16:19], v[64:67], v[32:35], 0\n\t"
      "ds_read_b128 v[0:3], v104 offset:49152\n\t"
      "v_mfma_f32_16x16x32_bf16 v[20:23], v[64:67], v[36:39], 0\n\t"
      "s_mov_b64 s[88:89], s[92:93]\n\t"
      "s_add_u32 m0, s55, 65536\n\t"
      "s_xor_b32 s61, s47, 3145728\n\t"
      "buffer_load_dwordx4 v105, s[88:91], s61 offen lds\n\t"
      "ds_read_b64_tr_b16 a[24:25], v117 offset:49152\n\t"
      "v_mfma_f32_16x16x32_bf16 v[16:19], v[68:71], v[40:43], v[16:19]\n\t"
      "ds_read_b128 v[4:7], v119 offset:49152\n\t"
      "v_cvt_pk_bf16_f32 v146, v24, v25\n\t"
      "v_mfma_f32_16x16x32_bf16 v[20:23], v[68:71], v[44:47], v[20:23]\n\t"
      "s_add_u32 m0, s55, 66560\n\t"
      "s_xor_b32 s61, s47, 3178496\n\t"
      "buffer_load_dwordx4 v105, s[88:91], s61 offen lds\n\t"
      "ds_read_b64_tr_b16 a[26:27], v117 offset:50176\n\t"
      "v_cvt_pk_bf16_f32 v147, v26, v27\n\t"
      "v_mfma_f32_16x16x32_bf16 v[16:19], v[72:75], v[48:51], v[16:19]\n\t"
      "ds_read_b128 v[8:11], v102 offset:49152\n\t"
      "v_cvt_pk_bf16_f32 v210, v28, v29\n\t"
      "v_mfma_f32_16x16x32_bf16 v[20:23], v[72:75], v[52:55], v[20:23]\n\t"
      "s_mov_b64 s[88:89], s[96:97]\n\t"
      "s_add_u32 m0, s55, 131072\n\t"
      "s_xor_b32 s61, s47, 3145728\n\t"
      "buffer_load_dwordx4 v116, s[88:91], s61 offen lds\n\t"
      "ds_read_b64_tr_b16 a[88:89], v118 offset:49152\n\t"
      "v_cvt_pk_bf16_f32 v211, v30, v31\n\t"
      "v_mfma_f32_16x16x32_bf16 v[16:19], v[76:79], v[56:59], v[16:19]\n\t"
      "ds_read_b128 v[12:15], v103 offset:49152\n\t"
      "v_mfma_f32_16x16x32_bf16 v[20:23], v[76:79], v[60:63], v[20:23]\n\t"
      "s_add_u32 m0, s55, 132096\n\t"
      "s_xor_b32 s61, s47, 3178496\n\t"
      "buffer_load_dwordx4 v116, s[88:91], s61 offen lds\n\t"
      "ds_read_b64_tr_b16 a[90:91], v118 offset:50176\n\t"
      "v_mfma_f32_16x16x32_bf16 a[168:171], v[144:147], v[144:147], a[168:171]\n\t"
      "v_mfma_f32_16x16x32_bf16 v[24:27], v[80:83], v[32:35], 0\n\t"
      "ds_read_b128 v[64:67], v104 offset:50176\n\t"
      "v_mfma_f32_16x16x32_bf16 v[28:31], v[80:83], v[36:39], 0\n\t"
      "s_add_u32 m0, s55, 139264\n\t"
      "s_xor_b32 s61, s47, 3407872\n\t"
      "buffer_load_dwordx4 v116, s[88:91], s61 offen lds\n\t"
      "ds_read_b64_tr_b16 a[28:29], v117 offset:57344\n\t"
      "v_mfma_f32_16x16x32_bf16 v[24:27], v[84:87], v[40:43], v[24:27]\n\t"
      "ds_read_b128 v[68:71], v119 offset:50176\n\t"
      "v_cvt_pk_bf16_f32 v148, v16, v17\n\t"
      "v_mfma_f32_16x16x32_bf16 v[28:31], v[84:87], v[44:47], v[28:31]\n\t"
      "s_add_u32 m0, s55, 140288\n\t"
      "s_xor_b32 s61, s47, 3440640\n\t"
      "buffer_load_dwordx4 v116, s[88:91], s61 offen lds\n\t"
      "ds_read_b64_tr_b16 a[30:31], v117 offset:58368\n\t"
      "v_cvt_pk_bf16_f32 v149, v18, v19\n\t"
      "v_mfma_f32_16x16x32_bf16 v[24:27], v[88:91], v[48:51], v[24:27]\n\t"
      "ds_read_b128 v[72:75], v102 offset:50176\n\t"
      "v_cvt_pk_bf16_f32 v212, v20, v21\n\t"
      "v_mfma_f32_16x16x32_bf16 v[28:31], v[88:91], v[52:55], v[28:31]\n\t"
      "ds_read_b64_tr_b16 a[92:93], v118 offset:57344\n\t"
      "ds_read_b64_tr_b16 a[94:95], v118 offset:58368\n\t"
      "v_cvt_pk_bf16_f32 v213, v22, v23\n\t"
      "v_mfma_f32_16x16x32_bf16 v[24:27], v[92:95], v[56:59], v[24:27]\n\t"
      "s_waitcnt lgkmcnt(12)\n\t"
      "ds_read_b128 v[76:79], v103 offset:50176\n\t"
      "v_mfma_f32_16x16x32_bf16 v[28:31], v[92:95], v[60:63], v[28:31]\n\t"
      "v_mfma_f32_16x16x32_bf16 a[232:235], v[208:211], v[208:211], a[232:235]\n\t"
      "s_waitcnt vmcnt(22)\n\t"
      "s_waitcnt vmcnt(12)\n\t"
      "s_waitcnt lgkmcnt(1)\n\t"
      "s_barrier\n\t"
      "v_mfma_f32_16x16x32_bf16 v[16:19], v[0:3], v[32:35], 0\n\t"
      "ds_read_b128 v[80:83], v104 offset:57344\n\t"
      "v_mfma_f32_16x16x32_bf16 v[20:23], v[0:3], v[36:39], 0\n\t"
      "s_mov_b64 s[88:89], s[92:93]\n\t"
      "s_add_u32 m0, s55, 73728\n\t"
      "s_xor_b32 s61, s47, 3407872\n\t"
      "buffer_load_dwordx4 v105, s[88:91], s61 offen lds\n\t"
      "ds_read_b64_tr_b16 a[188:189], v106 offset:49152\n\t"
      "v_mfma_f32_16x16x32_bf16 v[16:19], v[4:7], v[40:43], v[16:19]\n\t"
      "ds_read_b128 v[84:87], v119 offset:57344\n\t"
      "v_cvt_pk_bf16_f32 v150, v24, v25\n\t"
      "v_mfma_f32_16x16x32_bf16 v[20:23], v[4:7], v[44:47], v[20:23]\n\t"
      "s_add_u32 m0, s55, 74752\n\t"
      "s_xor_b32 s61, s47, 3440640\n\t"
      "buffer_load_dwordx4 v105, s[88:91], s61 offen lds\n\t"
      "ds_read_b64_tr_b16 a[190:191], v106 offset:50176\n\t"
      "v_cvt_pk_bf16_f32 v151, v26, v27\n\t"
      "v_mfma_f32_16x16x32_bf16 v[16:19], v[8:11], v[48:51], v[16:19]\n\t"
      "ds_read_b128 v[88:91], v102 offset:57344\n\t"
      "v_cvt_pk_bf16_f32 v214, v28, v29\n\t"
      "v_mfma_f32_16x16x32_bf16 v[20:23], v[8:11], v[52:55], v[20:23]\n\t"
      "s_mov_b64 s[88:89], s[96:97]\n\t"
      "s_add_u32 m0, s55, 147456\n\t"
      "s_xor_b32 s61, s47, 3670016\n\t"
      "buffer_load_dwordx4 v116, s[88:91], s61 offen lds\n\t"
      "ds_read_b64_tr_b16 a[32:33], v117\n\t"
      "v_cvt_pk_bf16_f32 v215, v30, v31\n\t"
      "v_mfma_f32_16x16x32_bf16 v[16:19], v[12:15], v[56:59], v[16:19]\n\t"
      "ds_read_b128 v[92:95], v103 offset:57344\n\t"
      "v_mfma_f32_16x16x32_bf16 v[20:23], v[12:15], v[60:63], v[20:23]\n\t"
      "s_add_u32 m0, s55, 148480\n\t"
      "s_xor_b32 s61, s47, 3702784\n\t"
      "buffer_load_dwordx4 v116, s[88:91], s61 offen lds\n\t"
      "ds_read_b64_tr_b16 a[34:35], v117 offset:1024\n\t"
      "v_mfma_f32_16x16x32_bf16 a[168:171], v[148:151], v[148:151], a[168:171]\n\t"
      "v_readfirstlane_b32 s53, v126\n\t"
      "s_nop 0\n\t"
      "s_mul_i32 s53, s53, 0x9e3779b1\n\t"
      "s_add_u32 s53, s53, 0x7f4a7c15\n\t"
      "v_mov_b32 v96, 0x20000000\n\t"
      "v_mov_b32 v97, 0x20000000\n\t"
      "v_mov_b32 v98, 0x20000000\n\t"
      "v_mov_b32 v99, 0x20000000\n\t"
      "s_mov_b32 s61, 8192\n\t"
      "buffer_store_dwordx4 v[96:99], v101, s[36:39], 0 offen sc1\n\t"
      "buffer_store_dwordx4 v[96:99], v101, s[36:39], s61 offen sc1\n\t"
      "s_nop 1\n\t"
      "v_mov_b32 v96, v111\n\t"
      "v_mov_b32 v97, v111\n\t"
      "v_mov_b32 v98, v111\n\t"
      "v_mov_b32 v99, v111\n\t"
      "buffer_store_dwordx4 v[96:99], v108, s[36:39], 0 offen sc1\n\t"
      "v_mfma_f32_16x16x32_bf16 v[24:27], v[64:67], v[32:35], 0\n\t"
      "ds_read_b128 v[0:3], v104 offset:58368\n\t"
      "v_mfma_f32_16x16x32_bf16 v[28:31], v[64:67], v[36:39], 0\n\t"
      "s_add_u32 m0, s55, 155648\n\t"
      "s_xor_b32 s61, s47, 3932160\n\t"
      "buffer_load_dwordx4 v116, s[88:91], s61 offen lds\n\t"
      "ds_read_b64_tr_b16 a[96:97], v118\n\t"
      "v_mfma_f32_16x16x32_bf16 v[24:27], v[68:71], v[40:43], v[24:27]\n\t"
      "ds_read_b128 v[4:7], v119 offset:58368\n\t"
      "v_cvt_pk_bf16_f32 v152, v16, v17\n\t"
      "v_mfma_f32_16x16x32_bf16 v[28:31], v[68:71], v[44:47], v[28:31]\n\t"
      "s_add_u32 m0, s55, 156672\n\t"
      "s_xor_b32 s61, s47, 3964928\n\t"
      "buffer_load_dwordx4 v116, s[88:91], s61 offen lds\n\t"
      "ds_read_b64_tr_b16 a[98:99], v118 offset:1024\n\t"
      "v_cvt_pk_bf16_f32 v153, v18, v19\n\t"
      "v_mfma_f32_16x16x32_bf16 v[24:27], v[72:75], v[48:51], v[24:27]\n\t"
      "ds_read_b128 v[8:11], v102 offset:58368\n\t"
      "v_cvt_pk_bf16_f32 v216, v20, v21\n\t"
      "v_mfma_f32_16x16x32_bf16 v[28:31], v[72:75], v[52:55], v[28:31]\n\t"
      "ds_read_b64_tr_b16 a[36:37], v117 offset:8192\n\t"
      "s_waitcnt lgkmcnt(12)\n\t"
      "ds_read_b64_tr_b16 a[38:39], v117 offset:9216\n\t"
      "v_cvt_pk_bf16_f32 v217, v22, v23\n\t"
      "v_mfma_f32_16x16x32_bf16 v[24:27], v[76:79], v[56:59], v[24:27]\n\t"
      "ds_read_b128 v[12:15], v103 offset:58368\n\t"
      "v_mfma_f32_16x16x32_bf16 v[28:31], v[76:79], v[60:63], v[28:31]\n\t"
      "ds_read_b64_tr_b16 a[100:101], v118 offset:8192\n\t"
      "s_waitcnt lgkmcnt(12)\n\t"
      "ds_read_b64_tr_b16 a[102:103], v118 offset:9216\n\t"
      "v_mfma_f32_16x16x32_bf16 a[232:235], v[212:215], v[212:215], a[232:235]\n\t"
      "s_waitcnt vmcnt(15)\n\t"
      "s_waitcnt lgkmcnt(0)\n\t"
      "s_barrier\n\t"
      "v_mfma_f32_16x16x32_bf16 v[16:19], v[80:83], v[32:35], 0\n\t"
      "ds_read_b128 v[64:67], v104\n\t"
      "v_mfma_f32_16x16x32_bf16 v[20:23], v[80:83], v[36:39], 0\n\t"
      "s_mov_b64 s[88:89], s[92:93]\n\t"
      "s_add_u32 m0, s55, 81920\n\t"
      "s_xor_b32 s61, s47, 3670016\n\t"
      "buffer_load_dwordx4 v105, s[88:91], s61 offen lds\n\t"
      "ds_read_b64_tr_b16 a[40:41], v117 offset:16384\n\t"
      "v_mfma_f32_16x16x32_bf16 v[16:19], v[84:87], v[40:43], v[16:19]\n\t"
      "ds_read_b128 v[68:71], v119\n\t"
      "v_cvt_pk_bf16_f32 v154, v24, v25\n\t"
      "v_mfma_f32_16x16x32_bf16 v[20:23], v[84:87], v[44:47], v[20:23]\n\t"
      "s_add_u32 m0, s55, 82944\n\t"
      "s_xor_b32 s61, s47, 3702784\n\t"
      "buffer_load_dwordx4 v105, s[88:91], s61 offen lds\n\t"
      "ds_read_b64_tr_b16 a[42:43], v117 offset:17408\n\t"
      "v_cvt_pk_bf16_f32 v155, v26, v27\n\t"
      "v_mfma_f32_16x16x32_bf16 v[16:19], v[88:91], v[48:51], v[16:19]\n\t"
      "ds_read_b128 v[72:75], v102\n\t"
      "v_cvt_pk_bf16_f32 v218, v28, v29\n\t"
      "v_mfma_f32_16x16x32_bf16 v[20:23], v[88:91], v[52:55], v[20:23]\n\t"
      "s_mov_b64 s[88:89], s[98:99]\n\t"
      "s_add_u32 m0, s55, 98304\n\t"
      "s_xor_b32 s61, s47, 0\n\t"
      "buffer_load_dwordx4 v116, s[88:91], s61 offen lds\n\t"
      "ds_read_b64_tr_b16 a[104:105], v118 offset:16384\n\t"
      "v_cvt_pk_bf16_f32 v219, v30, v31\n\t"
      "v_mfma_f32_16x16x32_bf16 v[16:19], v[92:95], v[56:59], v[16:19]\n\t"
      "ds_read_b128 v[76:79], v103\n\t"
      "v_mfma_f32_16x16x32_bf16 v[20:23], v[92:95], v[60:63], v[20:23]\n\t"
      "s_add_u32 m0, s55, 99328\n\t"
      "s_xor_b32 s61, s47, 32768\n\t"
      "buffer_load_dwordx4 v116, s[88:91], s61 offen lds\n\t"
      "ds_read_b64_tr_b16 a[106:107], v118 offset:17408\n\t"
      "v_mfma_f32_16x16x32_bf16 a[168:171], v[152:155], v[152:155], a[168:171]\n\t"
      "v_mfma_f32_16x16x32_bf16 v[24:27], v[0:3], v[32:35], 0\n\t"
      "ds_read_b128 v[80:83], v104 offset:1024\n\t"
      "v_mfma_f32_16x16x32_bf16 v[28:31], v[0:3], v[36:39], 0\n\t"
      "s_add_u32 m0, s55, 106496\n\t"
      "s_xor_b32 s61, s47, 262144\n\t"
      "buffer_load_dwordx4 v116, s[88:91], s61 offen lds\n\t"
      "ds_read_b64_tr_b16 a[44:45], v117 offset:24576\n\t"
      "v_mfma_f32_16x16x32_bf16 v[24:27], v[4:7], v[40:43], v[24:27]\n\t"
      "ds_read_b128 v[84:87], v119 offset:1024\n\t"
      "v_cvt_pk_bf16_f32 v156, v16, v17\n\t"
      "v_mfma_f32_16x16x32_bf16 v[28:31], v[4:7], v[44:47], v[28:31]\n\t"
      "s_add_u32 m0, s55, 107520\n\t"
      "s_xor_b32 s61, s47, 294912\n\t"
      "buffer_load_dwordx4 v116, s[88:91], s61 offen lds\n\t"
      "ds_read_b64_tr_b16 a[46:47], v117 offset:25600\n\t"
      "v_cvt_pk_bf16_f32 v157, v18, v19\n\t"
      "v_mfma_f32_16x16x32_bf16 v[24:27], v[8:11], v[48:51], v[24:27]\n\t"
      "ds_read_b128 v[88:91], v102 offset:1024\n\t"
      "v_cvt_pk_bf16_f32 v220, v20, v21\n\t"
      "v_mfma_f32_16x16x32_bf16 v[28:31], v[8:11], v[52:55], v[28:31]\n\t"
      "ds_read_b64_tr_b16 a[108:109], v118 offset:24576\n\t"
      "ds_read_b64_tr_b16 a[110:111], v118 offset:25600\n\t"
      "v_cvt_pk_bf16_f32 v221, v22, v23\n\t"
      "v_mfma_f32_16x16x32_bf16 v[24:27], v[12:15], v[56:59], v[24:27]\n\t"
      "s_waitcnt lgkmcnt(12)\n\t"
      "ds_read_b128 v[92:95], v103 offset:1024\n\t"
      "v_mfma_f32_16x16x32_bf16 v[28:31], v[12:15], v[60:63], v[28:31]\n\t"
      "v_mfma_f32_16x16x32_bf16 a[232:235], v[216:219], v[216:219], a[232:235]\n\t"
      "s_waitcnt vmcnt(15)\n\t"
      "s_waitcnt lgkmcnt(1)\n\t"
      "s_barrier\n\t"
      "v_mfma_f32_16x16x32_bf16 v[16:19], v[64:67], v[32:35], 0\n\t"
      "ds_read_b128 v[0:3], v104 offset:8192\n\t"
      "v_mfma_f32_16x16x32_bf16 v[20:23], v[64:67], v[36:39], 0\n\t"
      "s_mov_b64 s[88:89], s[92:93]\n\t"
      "s_add_u32 m0, s55, 90112\n\t"
      "s_xor_b32 s61, s47, 3932160\n\t"
      "buffer_load_dwordx4 v105, s[88:91], s61 offen lds\n\t"
      "ds_read_b64_tr_b16 a[240:241], v106\n\t"
      "v_mfma_f32_16x16x32_bf16 v[16:19], v[68:71], v[40:43], v[16:19]\n\t"
      "ds_read_b128 v[4:7], v119 offset:8192\n\t"
      "v_cvt_pk_bf16_f32 v158, v24, v25\n\t"
      "v_mfma_f32_16x16x32_bf16 v[20:23], v[68:71], v[44:47], v[20:23]\n\t"
      "s_add_u32 m0, s55, 91136\n\t"
      "s_xor_b32 s61, s47, 3964928\n\t"
      "buffer_load_dwordx4 v105, s[88:91], s61 offen lds\n\t"
      "ds_read_b64_tr_b16 a[242:243], v106 offset:1024\n\t"
      "v_cvt_pk_bf16_f32 v159, v26, v27\n\t"
      "v_mfma_f32_16x16x32_bf16 v[16:19], v[72:75], v[48:51], v[16:19]\n\t"
      "ds_read_b128 v[8:11], v102 offset:8192\n\t"
      "v_cvt_pk_bf16_f32 v222, v28, v29\n\t"
      "v_mfma_f32_16x16x32_bf16 v[20:23], v[72:75], v[52:55], v[20:23]\n\t"
      "s_mov_b64 s[88:89], s[98:99]\n\t"
      "s_add_u32 m0, s55, 114688\n\t"
      "s_xor_b32 s61, s47, 524288\n\t"
      "buffer_load_dwordx4 v116, s[88:91], s61 offen lds\n\t"
      "ds_read_b64_tr_b16 a[48:49], v117 offset:32768\n\t"
      "v_cvt_pk_bf16_f32 v223, v30, v31\n\t"
      "v_mfma_f32_16x16x32_bf16 v[16:19], v[76:79], v[56:59], v[16:19]\n\t"
      "ds_read_b128 v[12:15], v103 offset:8192\n\t"
      "v_mfma_f32_16x16x32_bf16 v[20:23], v[76:79], v[60:63], v[20:23]\n\t"
      "s_add_u32 m0, s55, 115712\n\t"
      "s_xor_b32 s61, s47, 557056\n\t"
      "buffer_load_dwordx4 v116, s[88:91], s61 offen lds\n\t"
      "ds_read_b64_tr_b16 a[50:51], v117 offset:33792\n\t"
      "v_mfma_f32_16x16x32_bf16 a[168:171], v[156:159], v[156:159], a[168:171]\n\t"
      "v_mfma_f32_16x16x32_bf16 v[24:27], v[80:83], v[32:35], 0\n\t"
      "ds_read_b128 v[64:67], v104 offset:9216\n\t"
      "v_mfma_f32_16x16x32_bf16 v[28:31], v[80:83], v[36:39], 0\n\t"
      "s_add_u32 m0, s55, 122880\n\t"
      "s_xor_b32 s61, s47, 786432\n\t"
      "buffer_load_dwordx4 v116, s[88:91], s61 offen lds\n\t"
      "ds_read_b64_tr_b16 a[112:113], v118 offset:32768\n\t"
      "v_mfma_f32_16x16x32_bf16 v[24:27], v[84:87], v[40:43], v[24:27]\n\t"
      "ds_read_b128 v[68:71], v119 offset:9216\n\t"
      "v_cvt_pk_bf16_f32 v160, v16, v17\n\t"
      "v_mfma_f32_16x16x32_bf16 v[28:31], v[84:87], v[44:47], v[28:31]\n\t"
      "s_add_u32 m0, s55, 123904\n\t"
      "s_xor_b32 s61, s47, 819200\n\t"
      "buffer_load_dwordx4 v116, s[88:91], s61 offen lds\n\t"
      "ds_read_b64_tr_b16 a[114:115], v118 offset:33792\n\t"
      "v_cvt_pk_bf16_f32 v161, v18, v19\n\t"
      "v_mfma_f32_16x16x32_bf16 v[24:27], v[88:91], v[48:51], v[24:27]\n\t"
      "ds_read_b128 v[72:75], v102 offset:9216\n\t"
      "v_cvt_pk_bf16_f32 v224, v20, v21\n\t"
      "v_mfma_f32_16x16x32_bf16 v[28:31], v[88:91], v[52:55], v[28:31]\n\t"
      "ds_read_b64_tr_b16 a[52:53], v117 offset:40960\n\t"
      "s_waitcnt lgkmcnt(12)\n\t"
      "ds_read_b64_tr_b16 a[54:55], v117 offset:41984\n\t"
      "v_cvt_pk_bf16_f32 v225, v22, v23\n\t"
      "v_mfma_f32_16x16x32_bf16 v[24:27], v[92:95], v[56:59], v[24:27]\n\t"
      "ds_read_b128 v[76:79], v103 offset:9216\n\t"
      "v_mfma_f32_16x16x32_bf16 v[28:31], v[92:95], v[60:63], v[28:31]\n\t"
      "ds_read_b64_tr_b16 a[116:117], v118 offset:40960\n\t"
      "s_waitcnt lgkmcnt(12)\n\t"
      "ds_read_b64_tr_b16 a[118:119], v118 offset:41984\n\t"
      "v_mfma_f32_16x16x32_bf16 a[232:235], v[220:223], v[220:223], a[232:235]\n\t"
      "s_waitcnt vmcnt(12)\n\t"
      "s_waitcnt lgkmcnt(0)\n\t"
      "s_barrier\n\t"
      "v_mfma_f32_16x16x32_bf16 v[16:19], v[0:3], v[32:35], 0\n\t"
      "ds_read_b128 v[80:83], v104 offset:16384\n\t"
      "v_mfma_f32_16x16x32_bf16 v[20:23], v[0:3], v[36:39], 0\n\t"
      "s_add_u32 m0, s55, 131072\n\t"
      "s_xor_b32 s61, s47, 1048576\n\t"
      "buffer_load_dwordx4 v116, s[88:91], s61 offen lds\n\t"
      "ds_read_b64_tr_b16 a[56:57], v117 offset:49152\n\t"
      "v_mfma_f32_16x16x32_bf16 v[16:19], v[4:7], v[40:43], v[16:19]\n\t"
      "ds_read_b128 v[84:87], v119 offset:16384\n\t"
      "v_cvt_pk_bf16_f32 v162, v24, v25\n\t"
      "v_mfma_f32_16x16x32_bf16 v[20:23], v[4:7], v[44:47], v[20:23]\n\t"
      "s_add_u32 m0, s55, 132096\n\t"
      "s_xor_b32 s61, s47, 1081344\n\t"
      "buffer_load_dwordx4 v116, s[88:91], s61 offen lds\n\t"
      "ds_read_b64_tr_b16 a[58:59], v117 offset:50176\n\t"
      "v_cvt_pk_bf16_f32 v163, v26, v27\n\t"
      "v_mfma_f32_16x16x32_bf16 v[16:19], v[8:11], v[48:51], v[16:19]\n\t"
      "ds_read_b128 v[88:91], v102 offset:16384\n\t"
      "v_cvt_pk_bf16_f32 v226, v28, v29\n\t"
      "v_mfma_f32_16x16x32_bf16 v[20:23], v[8:11], v[52:55], v[20:23]\n\t"
      "s_add_u32 m0, s55, 139264\n\t"
      "s_xor_b32 s61, s47, 1310720\n\t"
      "buffer_load_dwordx4 v116, s[88:91], s61 offen lds\n\t"
      "ds_read_b64_tr_b16 a[120:121], v118 offset:49152\n\t"
      "v_cvt_pk_bf16_f32 v227, v30, v31\n\t"
      "v_mfma_f32_16x16x32_bf16 v[16:19], v[12:15], v[56:59], v[16:19]\n\t"
      "ds_read_b128 v[92:95], v103 offset:16384\n\t"
      "v_mfma_f32_16x16x32_bf16 v[20:23], v[12:15], v[60:63], v[20:23]\n\t"
      "s_add_u32 m0, s55, 140288\n\t"
      "s_xor_b32 s61, s47, 1343488\n\t"
      "buffer_load_dwordx4 v116, s[88:91], s61 offen lds\n\t"
      "ds_read_b64_tr_b16 a[122:123], v118 offset:50176\n\t"
      "v_mfma_f32_16x16x32_bf16 a[168:171], v[160:163], v[160:163], a[168:171]\n\t"
      "v_mfma_f32_16x16x32_bf16 v[24:27], v[64:67], v[32:35], 0\n\t"
      "ds_read_b128 v[0:3], v104 offset:17408\n\t"
      "v_mfma_f32_16x16x32_bf16 v[28:31], v[64:67], v[36:39], 0\n\t"
      "ds_read_b64_tr_b16 a[60:61], v117 offset:57344\n\t"
      "ds_read_b64_tr_b16 a[62:63], v117 offset:58368\n\t"
      "v_mfma_f32_16x16x32_bf16 v[24:27], v[68:71], v[40:43], v[24:27]\n\t"
      "ds_read_b128 v[4:7], v119 offset:17408\n\t"
      "v_cvt_pk_bf16_f32 v164, v16, v17\n\t"
      "v_mfma_f32_16x16x32_bf16 v[28:31], v[68:71], v[44:47], v[28:31]\n\t"
      "ds_read_b64_tr_b16 a[124:125], v118 offset:57344\n\t"
      "ds_read_b64_tr_b16 a[126:127], v118 offset:58368\n\t"
      "v_cvt_pk_bf16_f32 v165, v18, v19\n\t"
      "v_mfma_f32_16x16x32_bf16 v[24:27], v[72:75], v[48:51], v[24:27]\n\t"
      "ds_read_b128 v[8:11], v102 offset:17408\n\t"
      "v_cvt_pk_bf16_f32 v228, v20, v21\n\t"
      "v_mfma_f32_16x16x32_bf16 v[28:31], v[72:75], v[52:55], v[28:31]\n\t"
      "v_cvt_pk_bf16_f32 v229, v22, v23\n\t"
      "v_mfma_f32_16x16x32_bf16 v[24:27], v[76:79], v[56:59], v[24:27]\n\t"
      "s_waitcnt lgkmcnt(12)\n\t"
      "ds_read_b128 v[12:15], v103 offset:17408\n\t"
      "v_mfma_f32_16x16x32_bf16 v[28:31], v[76:79], v[60:63], v[28:31]\n\t"
      "v_mfma_f32_16x16x32_bf16 a[232:235], v[224:227], v[224:227], a[232:235]\n\t"
      "s_waitcnt vmcnt(10)\n\t"
      "s_waitcnt lgkmcnt(2)\n\t"
      "s_barrier\n\t"
      "v_mfma_f32_16x16x32_bf16 v[16:19], v[80:83], v[32:35], 0\n\t"
      "ds_read_b128 v[64:67], v104 offset:24576\n\t"
      "v_mfma_f32_16x16x32_bf16 v[20:23], v[80:83], v[36:39], 0\n\t"
      "s_add_u32 m0, s55, 147456\n\t"
      "s_xor_b32 s61, s47, 1572864\n\t"
      "buffer_load_dwordx4 v116, s[88:91], s61 offen lds\n\t"
      "ds_read_b64_tr_b16 a[244:245], v106 offset:16384\n\t"
      "v_mfma_f32_16x16x32_bf16 v[16:19], v[84:87], v[40:43], v[16:19]\n\t"
      "ds_read_b128 v[68:71], v119 offset:24576\n\t"
      "v_cvt_pk_bf16_f32 v166, v24, v25\n\t"
      "v_mfma_f32_16x16x32_bf16 v[20:23], v[84:87], v[44:47], v[20:23]\n\t"
      "s_add_u32 m0, s55, 148480\n\t"
      "s_xor_b32 s61, s47, 1605632\n\t"
      "buffer_load_dwordx4 v116, s[88:91], s61 offen lds\n\t"
      "ds_read_b64_tr_b16 a[246:247], v106 offset:17408\n\t"
      "v_cvt_pk_bf16_f32 v167, v26, v27\n\t"
      "v_mfma_f32_16x16x32_bf16 v[16:19], v[88:91], v[48:51], v[16:19]\n\t"
      "ds_read_b128 v[72:75], v102 offset:24576\n\t"
      "v_cvt_pk_bf16_f32 v230, v28, v29\n\t"
      "v_mfma_f32_16x16x32_bf16 v[20:23], v[88:91], v[52:55], v[20:23]\n\t"
      "s_add_u32 m0, s55, 155648\n\t"
      "s_xor_b32 s61, s47, 1835008\n\t"
      "buffer_load_dwordx4 v116, s[88:91], s61 offen lds\n\t"
      "ds_read_b64_tr_b16 a[128:129], v117\n\t"
      "v_cvt_pk_bf16_f32 v231, v30, v31\n\t"
      "v_mfma_f32_16x16x32_bf16 v[16:19], v[92:95], v[56:59], v[16:19]\n\t"
      "ds_read_b128 v[76:79], v103 offset:24576\n\t"
      "v_mfma_f32_16x16x32_bf16 v[20:23], v[92:95], v[60:63], v[20:23]\n\t"
      "s_add_u32 m0, s55, 156672\n\t"
      "s_xor_b32 s61, s47, 1867776\n\t"
      "buffer_load_dwordx4 v116, s[88:91], s61 offen lds\n\t"
      "ds_read_b64_tr_b16 a[130:131], v117 offset:1024\n\t"
      "v_mfma_f32_16x16x32_bf16 a[168:171], v[164:167], v[164:167], a[168:171]\n\t"
      "v_mfma_f32_16x16x32_bf16 v[24:27], v[0:3], v[32:35], 0\n\t"
      "ds_read_b128 v[80:83], v104 offset:25600\n\t"
      "v_mfma_f32_16x16x32_bf16 v[28:31], v[0:3], v[36:39], 0\n\t"
      "ds_read_b64_tr_b16 a[192:193], v118\n\t"
      "ds_read_b64_tr_b16 a[194:195], v118 offset:1024\n\t"
      "v_mfma_f32_16x16x32_bf16 v[24:27], v[4:7], v[40:43], v[24:27]\n\t"
      "ds_read_b128 v[84:87], v119 offset:25600\n\t"
      "v_cvt_pk_bf16_f32 v168, v16, v17\n\t"
      "v_mfma_f32_16x16x32_bf16 v[28:31], v[4:7], v[44:47], v[28:31]\n\t"
      "ds_read_b64_tr_b16 a[132:133], v117 offset:8192\n\t"
      "s_waitcnt lgkmcnt(12)\n\t"
      "ds_read_b64_tr_b16 a[134:135], v117 offset:9216\n\t"
      "v_cvt_pk_bf16_f32 v169, v18, v19\n\t"
      "v_mfma_f32_16x16x32_bf16 v[24:27], v[8:11], v[48:51], v[24:27]\n\t"
      "ds_read_b128 v[88:91], v102 offset:25600\n\t"
      "v_cvt_pk_bf16_f32 v232, v20, v21\n\t"
      "v_mfma_f32_16x16x32_bf16 v[28:31], v[8:11], v[52:55], v[28:31]\n\t"
      "ds_read_b64_tr_b16 a[196:197], v118 offset:8192\n\t"
      "s_waitcnt lgkmcnt(12)\n\t"
      "ds_read_b64_tr_b16 a[198:199], v118 offset:9216\n\t"
      "v_cvt_pk_bf16_f32 v233, v22, v23\n\t"
      "v_mfma_f32_16x16x32_bf16 v[24:27], v[12:15], v[56:59], v[24:27]\n\t"
      "ds_read_b128 v[92:95], v103 offset:25600\n\t"
      "v_mfma_f32_16x16x32_bf16 v[28:31], v[12:15], v[60:63], v[28:31]\n\t"
      "v_mfma_f32_16x16x32_bf16 a[232:235], v[228:231], v[228:231], a[232:235]\n\t"
      "s_waitcnt vmcnt(8)\n\t"
      "s_waitcnt lgkmcnt(1)\n\t"
      "s_barrier\n\t"
      "v_mfma_f32_16x16x32_bf16 v[16:19], v[64:67], v[32:35], 0\n\t"
      "ds_read_b128 v[0:3], v104 offset:32768\n\t"
      "v_mfma_f32_16x16x32_bf16 v[20:23], v[64:67], v[36:39], 0\n\t"
      "s_add_u32 m0, s55, 98304\n\t"
      "s_xor_b32 s61, s47, 2097152\n\t"
      "buffer_load_dwordx4 v116, s[88:91], s61 offen lds\n\t"
      "ds_read_b64_tr_b16 a[136:137], v117 offset:16384\n\t"
      "v_mfma_f32_16x16x32_bf16 v[16:19], v[68:71], v[40:43], v[16:19]\n\t"
      "ds_read_b128 v[4:7], v119 offset:32768\n\t"
      "v_cvt_pk_bf16_f32 v170, v24, v25\n\t"
      "v_mfma_f32_16x16x32_bf16 v[20:23], v[68:71], v[44:47], v[20:23]\n\t"
      "s_add_u32 m0, s55, 99328\n\t"
      "s_xor_b32 s61, s47, 2129920\n\t"
      "buffer_load_dwordx4 v116, s[88:91], s61 offen lds\n\t"
      "ds_read_b64_tr_b16 a[138:139], v117 offset:17408\n\t"
      "v_cvt_pk_bf16_f32 v171, v26, v27\n\t"
      "v_mfma_f32_16x16x32_bf16 v[16:19], v[72:75], v[48:51], v[16:19]\n\t"
      "ds_read_b128 v[8:11], v102 offset:32768\n\t"
      "v_cvt_pk_bf16_f32 v234, v28, v29\n\t"
      "v_mfma_f32_16x16x32_bf16 v[20:23], v[72:75], v[52:55], v[20:23]\n\t"
      "s_add_u32 m0, s55, 106496\n\t"
      "s_xor_b32 s61, s47, 2359296\n\t"
      "buffer_load_dwordx4 v116, s[88:91], s61 offen lds\n\t"
      "ds_read_b64_tr_b16 a[200:201], v118 offset:16384\n\t"
      "v_cvt_pk_bf16_f32 v235, v30, v31\n\t"
      "v_mfma_f32_16x16x32_bf16 v[16:19], v[76:79], v[56:59], v[16:19]\n\t"
      "ds_read_b128 v[12:15], v103 offset:32768\n\t"
      "v_mfma_f32_16x16x32_bf16 v[20:23], v[76:79], v[60:63], v[20:23]\n\t"
      "s_add_u32 m0, s55, 107520\n\t"
      "s_xor_b32 s61, s47, 2392064\n\t"
      "buffer_load_dwordx4 v116, s[88:91], s61 offen lds\n\t"
      "ds_read_b64_tr_b16 a[202:203], v118 offset:17408\n\t"
      "v_mfma_f32_16x16x32_bf16 a[168:171], v[168:171], v[168:171], a[168:171]\n\t"
      "v_mfma_f32_16x16x32_bf16 v[24:27], v[80:83], v[32:35], 0\n\t"
      "ds_read_b128 v[64:67], v104 offset:33792\n\t"
      "v_mfma_f32_16x16x32_bf16 v[28:31], v[80:83], v[36:39], 0\n\t"
      "ds_read_b64_tr_b16 a[140:141], v117 offset:24576\n\t"
      "ds_read_b64_tr_b16 a[142:143], v117 offset:25600\n\t"
      "v_mfma_f32_16x16x32_bf16 v[24:27], v[84:87], v[40:43], v[24:27]\n\t"
      "ds_read_b128 v[68:71], v119 offset:33792\n\t"
      "v_cvt_pk_bf16_f32 v172, v16, v17\n\t"
      "v_mfma_f32_16x16x32_bf16 v[28:31], v[84:87], v[44:47], v[28:31]\n\t"
      "ds_read_b64_tr_b16 a[204:205], v118 offset:24576\n\t"
      "ds_read_b64_tr_b16 a[206:207], v118 offset:25600\n\t"
      "v_cvt_pk_bf16_f32 v173, v18, v19\n\t"
      "v_mfma_f32_16x16x32_bf16 v[24:27], v[88:91], v[48:51], v[24:27]\n\t"
      "s_waitcnt lgkmcnt(12)\n\t"
      "ds_read_b128 v[72:75], v102 offset:33792\n\t"
      "v_cvt_pk_bf16_f32 v236, v20, v21\n\t"
      "v_mfma_f32_16x16x32_bf16 v[28:31], v[88:91], v[52:55], v[28:31]\n\t"
      "v_cvt_pk_bf16_f32 v237, v22, v23\n\t"
      "v_mfma_f32_16x16x32_bf16 v[24:27], v[92:95], v[56:59], v[24:27]\n\t"
      "ds_read_b128 v[76:79], v103 offset:33792\n\t"
      "v_mfma_f32_16x16x32_bf16 v[28:31], v[92:95], v[60:63], v[28:31]\n\t"
      "v_mfma_f32_16x16x32_bf16 a[232:235], v[232:235], v[232:235], a[232:235]\n\t"
      "s_waitcnt vmcnt(8)\n\t"
      "s_waitcnt lgkmcnt(2)\n\t"
      "s_barrier\n\t"
      "v_mfma_f32_16x16x32_bf16 v[16:19], v[0:3], v[32:35], 0\n\t"
      "ds_read_b128 v[80:83], v104 offset:40960\n\t"
      "v_mfma_f32_16x16x32_bf16 v[20:23], v[0:3], v[36:39], 0\n\t"
      "s_add_u32 m0, s55, 114688\n\t"
      "s_xor_b32 s61, s47, 2621440\n\t"
      "buffer_load_dwordx4 v116, s[88:91], s61 offen lds\n\t"
      "ds_read_b64_tr_b16 a[248:249], v106 offset:32768\n\t"
      "v_mfma_f32_16x16x32_bf16 v[16:19], v[4:7], v[40:43], v[16:19]\n\t"
      "ds_read_b128 v[84:87], v119 offset:40960\n\t"
      "v_cvt_pk_bf16_f32 v174, v24, v25\n\t"
      "v_mfma_f32_16x16x32_bf16 v[20:23], v[4:7], v[44:47], v[20:23]\n\t"
      "s_add_u32 m0, s55, 115712\n\t"
      "s_xor_b32 s61, s47, 2654208\n\t"
      "buffer_load_dwordx4 v116, s[88:91], s61 offen lds\n\t"
      "ds_read_b64_tr_b16 a[250:251], v106 offset:33792\n\t"
      "v_cvt_pk_bf16_f32 v175, v26, v27\n\t"
      "v_mfma_f32_16x16x32_bf16 v[16:19], v[8:11], v[48:51], v[16:19]\n\t"
      "ds_read_b128 v[88:91], v102 offset:40960\n\t"
      "v_cvt_pk_bf16_f32 v238, v28, v29\n\t"
      "v_mfma_f32_16x16x32_bf16 v[20:23], v[8:11], v[52:55], v[20:23]\n\t"
      "s_add_u32 m0, s55, 122880\n\t"
      "s_xor_b32 s61, s47, 2883584\n\t"
      "buffer_load_dwordx4 v116, s[88:91], s61 offen lds\n\t"
      "ds_read_b64_tr_b16 a[144:145], v117 offset:32768\n\t"
      "v_cvt_pk_bf16_f32 v239, v30, v31\n\t"
      "v_mfma_f32_16x16x32_bf16 v[16:19], v[12:15], v[56:59], v[16:19]\n\t"
      "ds_read_b128 v[92:95], v103 offset:40960\n\t"
      "v_mfma_f32_16x16x32_bf16 v[20:23], v[12:15], v[60:63], v[20:23]\n\t"
      "s_add_u32 m0, s55, 123904\n\t"
      "s_xor_b32 s61, s47, 2916352\n\t"
      "buffer_load_dwordx4 v116, s[88:91], s61 offen lds\n\t"
      "ds_read_b64_tr_b16 a[146:147], v117 offset:33792\n\t"
      "v_mfma_f32_16x16x32_bf16 a[168:171], v[172:175], v[172:175], a[168:171]\n\t"
      "v_mfma_f32_16x16x32_bf16 v[24:27], v[64:67], v[32:35], 0\n\t"
      "ds_read_b128 v[0:3], v104 offset:41984\n\t"
      "v_mfma_f32_16x16x32_bf16 v[28:31], v[64:67], v[36:39], 0\n\t"
      "ds_read_b64_tr_b16 a[208:209], v118 offset:32768\n\t"
      "ds_read_b64_tr_b16 a[210:211], v118 offset:33792\n\t"
      "v_mfma_f32_16x16x32_bf16 v[24:27], v[68:71], v[40:43], v[24:27]\n\t"
      "ds_read_b128 v[4:7], v119 offset:41984\n\t"
      "v_cvt_pk_bf16_f32 v176, v16, v17\n\t"
      "v_mfma_f32_16x16x32_bf16 v[28:31], v[68:71], v[44:47], v[28:31]\n\t"
      "ds_read_b64_tr_b16 a[148:149], v117 offset:40960\n\t"
      "s_waitcnt lgkmcnt(12)\n\t"
      "ds_read_b64_tr_b16 a[150:151], v117 offset:41984\n\t"
      "v_cvt_pk_bf16_f32 v177, v18, v19\n\t"
      "v_mfma_f32_16x16x32_bf16 v[24:27], v[72:75], v[48:51], v[24:27]\n\t"
      "ds_read_b128 v[8:11], v102 offset:41984\n\t"
      "v_cvt_pk_bf16_f32 v240, v20, v21\n\t"
      "v_mfma_f32_16x16x32_bf16 v[28:31], v[72:75], v[52:55], v[28:31]\n\t"
      "ds_read_b64_tr_b16 a[212:213], v118 offset:40960\n\t"
      "s_waitcnt lgkmcnt(12)\n\t"
      "ds_read_b64_tr_b16 a[214:215], v118 offset:41984\n\t"
      "v_cvt_pk_bf16_f32 v241, v22, v23\n\t"
      "v_mfma_f32_16x16x32_bf16 v[24:27], v[76:79], v[56:59], v[24:27]\n\t"
      "ds_read_b128 v[12:15], v103 offset:41984\n\t"
      "v_mfma_f32_16x16x32_bf16 v[28:31], v[76:79], v[60:63], v[28:31]\n\t"
      "v_mfma_f32_16x16x32_bf16 a[232:235], v[236:239], v[236:239], a[232:235]\n\t"
      "s_waitcnt vmcnt(8)\n\t"
      "s_waitcnt lgkmcnt(1)\n\t"
      "s_barrier\n\t"
      "v_mfma_f32_16x16x32_bf16 v[16:19], v[80:83], v[32:35], 0\n\t"
      "ds_read_b128 v[64:67], v104 offset:49152\n\t"
      "v_mfma_f32_16x16x32_bf16 v[20:23], v[80:83], v[36:39], 0\n\t"
      "s_add_u32 m0, s55, 131072\n\t"
      "s_xor_b32 s61, s47, 3145728\n\t"
      "buffer_load_dwordx4 v116, s[88:91], s61 offen lds\n\t"
      "ds_read_b64_tr_b16 a[152:153], v117 offset:49152\n\t"
      "v_mfma_f32_16x16x32_bf16 v[16:19], v[84:87], v[40:43], v[16:19]\n\t"
      "ds_read_b128 v[68:71], v119 offset:49152\n\t"
      "v_cvt_pk_bf16_f32 v178, v24, v25\n\t"
      "v_mfma_f32_16x16x32_bf16 v[20:23], v[84:87], v[44:47], v[20:23]\n\t"
      "s_add_u32 m0, s55, 132096\n\t"
      "s_xor_b32 s61, s47, 3178496\n\t"
      "buffer_load_dwordx4 v116, s[88:91], s61 offen lds\n\t"
      "ds_read_b64_tr_b16 a[154:155], v117 offset:50176\n\t"
      "v_cvt_pk_bf16_f32 v179, v26, v27\n\t"
      "v_mfma_f32_16x16x32_bf16 v[16:19], v[88:91], v[48:51], v[16:19]\n\t"
      "ds_read_b128 v[72:75], v102 offset:49152\n\t"
      "v_cvt_pk_bf16_f32 v242, v28, v29\n\t"
      "v_mfma_f32_16x16x32_bf16 v[20:23], v[88:91], v[52:55], v[20:23]\n\t"
      "s_add_u32 m0, s55, 139264\n\t"
      "s_xor_b32 s61, s47, 3407872\n\t"
      "buffer_load_dwordx4 v116, s[88:91], s61 offen lds\n\t"
      "ds_read_b64_tr_b16 a[216:217], v118 offset:49152\n\t"
      "v_cvt_pk_bf16_f32 v243, v30, v31\n\t"
      "v_mfma_f32_16x16x32_bf16 v[16:19], v[92:95], v[56:59], v[16:19]\n\t"
      "ds_read_b128 v[76:79], v103 offset:49152\n\t"
      "v_mfma_f32_16x16x32_bf16 v[20:23], v[92:95], v[60:63], v[20:23]\n\t"
      "s_add_u32 m0, s55, 140288\n\t"
      "s_xor_b32 s61, s47, 3440640\n\t"
      "buffer_load_dwordx4 v116, s[88:91], s61 offen lds\n\t"
      "ds_read_b64_tr_b16 a[218:219], v118 offset:50176\n\t"
      "v_mfma_f32_16x16x32_bf16 a[168:171], v[176:179], v[176:179], a[168:171]\n\t"
      "v_mfma_f32_16x16x32_bf16 v[24:27], v[0:3], v[32:35], 0\n\t"
      "ds_read_b128 v[80:83], v104 offset:50176\n\t"
      "v_mfma_f32_16x16x32_bf16 v[28:31], v[0:3], v[36:39], 0\n\t"
      "ds_read_b64_tr_b16 a[156:157], v117 offset:57344\n\t"
      "ds_read_b64_tr_b16 a[158:159], v117 offset:58368\n\t"
      "v_mfma_f32_16x16x32_bf16 v[24:27], v[4:7], v[40:43], v[24:27]\n\t"
      "ds_read_b128 v[84:87], v119 offset:50176\n\t"
      "v_cvt_pk_bf16_f32 v180, v16, v17\n\t"
      "v_mfma_f32_16x16x32_bf16 v[28:31], v[4:7], v[44:47], v[28:31]\n\t"
      "ds_read_b64_tr_b16 a[220:221], v118 offset:57344\n\t"
      "ds_read_b64_tr_b16 a[222:223], v118 offset:58368\n\t"
      "v_cvt_pk_bf16_f32 v181, v18, v19\n\t"
      "v_mfma_f32_16x16x32_bf16 v[24:27], v[8:11], v[48:51], v[24:27]\n\t"
      "s_waitcnt lgkmcnt(12)\n\t"
      "ds_read_b128 v[88:91], v102 offset:50176\n\t"
      "v_cvt_pk_bf16_f32 v244, v20, v21\n\t"
      "v_mfma_f32_16x16x32_bf16 v[28:31], v[8:11], v[52:55], v[28:31]\n\t"
      "v_cvt_pk_bf16_f32 v245, v22, v23\n\t"
      "v_mfma_f32_16x16x32_bf16 v[24:27], v[12:15], v[56:59], v[24:27]\n\t"
      "ds_read_b128 v[92:95], v103 offset:50176\n\t"
      "v_mfma_f32_16x16x32_bf16 v[28:31], v[12:15], v[60:63], v[28:31]\n\t"
      "v_mfma_f32_16x16x32_bf16 a[232:235], v[240:243], v[240:243], a[232:235]\n\t"
      "s_waitcnt vmcnt(8)\n\t"
      "s_waitcnt lgkmcnt(2)\n\t"
      "s_barrier\n\t"
      "v_mfma_f32_16x16x32_bf16 v[16:19], v[64:67], v[32:35], 0\n\t"
      "ds_read_b128 v[0:3], v104 offset:57344\n\t"
      "v_mfma_f32_16x16x32_bf16 v[20:23], v[64:67], v[36:39], 0\n\t"
      "s_add_u32 m0, s55, 147456\n\t"
      "s_xor_b32 s61, s47, 3670016\n\t"
      "buffer_load_dwordx4 v116, s[88:91], s61 offen lds\n\t"
      "ds_read_b64_tr_b16 a[252:253], v106 offset:49152\n\t"
      "v_mfma_f32_16x16x32_bf16 v[16:19], v[68:71], v[40:43], v[16:19]\n\t"
      "ds_read_b128 v[4:7], v119 offset:57344\n\t"
      "v_cvt_pk_bf16_f32 v182, v24, v25\n\t"
      "v_mfma_f32_16x16x32_bf16 v[20:23], v[68:71], v[44:47], v[20:23]\n\t"
      "s_add_u32 m0, s55, 148480\n\t"
      "s_xor_b32 s61, s47, 3702784\n\t"
      "buffer_load_dwordx4 v116, s[88:91], s61 offen lds\n\t"
      "ds_read_b64_tr_b16 a[254:255], v106 offset:50176\n\t"
      "v_cvt_pk_bf16_f32 v183, v26, v27\n\t"
      "v_mfma_f32_16x16x32_bf16 v[16:19], v[72:75], v[48:51], v[16:19]\n\t"
      "ds_read_b128 v[8:11], v102 offset:57344\n\t"
      "v_cvt_pk_bf16_f32 v246, v28, v29\n\t"
      "v_mfma_f32_16x16x32_bf16 v[20:23], v[72:75], v[52:55], v[20:23]\n\t"
      "s_add_u32 m0, s55, 155648\n\t"
      "s_xor_b32 s61, s47, 3932160\n\t"
      "buffer_load_dwordx4 v116, s[88:91], s61 offen lds\n\t"
      "ds_read_b64_tr_b16 a[160:161], v117\n\t"
      "v_cvt_pk_bf16_f32 v247, v30, v31\n\t"
      "v_mfma_f32_16x16x32_bf16 v[16:19], v[76:79], v[56:59], v[16:19]\n\t"
      "ds_read_b128 v[12:15], v103 offset:57344\n\t"
      "v_mfma_f32_16x16x32_bf16 v[20:23], v[76:79], v[60:63], v[20:23]\n\t"
      "s_add_u32 m0, s55, 156672\n\t"
      "s_xor_b32 s61, s47, 3964928\n\t"
      "buffer_load_dwordx4 v116, s[88:91], s61 offen lds\n\t"
      "ds_read_b64_tr_b16 a[162:163], v117 offset:1024\n\t"
      "v_mfma_f32_16x16x32_bf16 a[168:171], v[180:183], v[180:183], a[168:171]\n\t"
      "v_mfma_f32_16x16x32_bf16 v[24:27], v[80:83], v[32:35], 0\n\t"
      "ds_read_b128 v[64:67], v104 offset:58368\n\t"
      "v_mfma_f32_16x16x32_bf16 v[28:31], v[80:83], v[36:39], 0\n\t"
      "ds_read_b64_tr_b16 a[224:225], v118\n\t"
      "ds_read_b64_tr_b16 a[226:227], v118 offset:1024\n\t"
      "v_mfma_f32_16x16x32_bf16 v[24:27], v[84:87], v[40:43], v[24:27]\n\t"
      "ds_read_b128 v[68:71], v119 offset:58368\n\t"
      "v_cvt_pk_bf16_f32 v184, v16, v17\n\t"
      "v_mfma_f32_16x16x32_bf16 v[28:31], v[84:87], v[44:47], v[28:31]\n\t"
      "ds_read_b64_tr_b16 a[164:165], v117 offset:8192\n\t"
      "s_waitcnt lgkmcnt(12)\n\t"
      "ds_read_b64_tr_b16 a[166:167], v117 offset:9216\n\t"
      "v_cvt_pk_bf16_f32 v185, v18, v19\n\t"
      "v_mfma_f32_16x16x32_bf16 v[24:27], v[88:91], v[48:51], v[24:27]\n\t"
      "ds_read_b128 v[72:75], v102 offset:58368\n\t"
      "v_cvt_pk_bf16_f32 v248, v20, v21\n\t"
      "v_mfma_f32_16x16x32_bf16 v[28:31], v[88:91], v[52:55], v[28:31]\n\t"
      "ds_read_b64_tr_b16 a[228:229], v118 offset:8192\n\t"
      "s_waitcnt lgkmcnt(12)\n\t"
      "ds_read_b64_tr_b16 a[230:231], v118 offset:9216\n\t"
      "v_cvt_pk_bf16_f32 v249, v22, v23\n\t"
      "v_mfma_f32_16x16x32_bf16 v[24:27], v[92:95], v[56:59], v[24:27]\n\t"
      "ds_read_b128 v[76:79], v103 offset:58368\n\t"
      "v_mfma_f32_16x16x32_bf16 v[28:31], v[92:95], v[60:63], v[28:31]\n\t"
      "v_mfma_f32_16x16x32_bf16 a[232:235], v[244:247], v[244:247], a[232:235]\n\t"
      "v_mfma_f32_16x16x32_bf16 v[16:19], v[0:3], v[32:35], 0\n\t"
      "v_mfma_f32_16x16x32_bf16 v[20:23], v[0:3], v[36:39], 0\n\t"
      "v_mfma_f32_16x16x32_bf16 v[16:19], v[4:7], v[40:43], v[16:19]\n\t"
      "v_cvt_pk_bf16_f32 v186, v24, v25\n\t"
      "v_mfma_f32_16x16x32_bf16 v[20:23], v[4:7], v[44:47], v[20:23]\n\t"
      "v_cvt_pk_bf16_f32 v187, v26, v27\n\t"
      "s_waitcnt lgkmcnt(13)\n\t"
      "v_mfma_f32_16x16x32_bf16 v[16:19], v[8:11], v[48:51], v[16:19]\n\t"
      "v_cvt_pk_bf16_f32 v250, v28, v29\n\t"
      "v_mfma_f32_16x16x32_bf16 v[20:23], v[8:11], v[52:55], v[20:23]\n\t"
      "v_cvt_pk_bf16_f32 v251, v30, v31\n\t"
      "s_waitcnt lgkmcnt(11)\n\t"
      "v_mfma_f32_16x16x32_bf16 v[16:19], v[12:15], v[56:59], v[16:19]\n\t"
      "v_mfma_f32_16x16x32_bf16 v[20:23], v[12:15], v[60:63], v[20:23]\n\t"
      "v_mfma_f32_16x16x32_bf16 a[168:171], v[184:187], v[184:187], a[168:171]\n\t"
      "buffer_load_dwordx4 v[96:99], off, s[36:39], s54 sc1\n\t"
      "s_waitcnt lgkmcnt(9)\n\t"
      "v_mfma_f32_16x16x32_bf16 v[24:27], v[64:67], v[32:35], 0\n\t"
      "v_mfma_f32_16x16x32_bf16 v[28:31], v[64:67], v[36:39], 0\n\t"
      "s_waitcnt lgkmcnt(6)\n\t"
      "v_mfma_f32_16x16x32_bf16 v[24:27], v[68:71], v[40:43], v[24:27]\n\t"
      "v_cvt_pk_bf16_f32 v188, v16, v17\n\t"
      "v_mfma_f32_16x16x32_bf16 v[28:31], v[68:71], v[44:47], v[28:31]\n\t"
      "v_cvt_pk_bf16_f32 v189, v18, v19\n\t"
      "s_waitcnt lgkmcnt(3)\n\t"
      "v_mfma_f32_16x16x32_bf16 v[24:27], v[72:75], v[48:51], v[24:27]\n\t"
      "v_cvt_pk_bf16_f32 v252, v20, v21\n\t"
      "v_mfma_f32_16x16x32_bf16 v[28:31], v[72:75], v[52:55], v[28:31]\n\t"
      "v_cvt_pk_bf16_f32 v253, v22, v23\n\t"
      "s_waitcnt lgkmcnt(0)\n\t"
      "v_mfma_f32_16x16x32_bf16 v[24:27], v[76:79], v[56:59], v[24:27]\n\t"
      "v_mfma_f32_16x16x32_bf16 v[28:31], v[76:79], v[60:63], v[28:31]\n\t"
      "v_mfma_f32_16x16x32_bf16 a[232:235], v[248:251], v[248:251], a[232:235]\n\t"
      "v_lshl_add_u32 v8, s41, 10, v127\n\t"
      "v_add_u32 v8, 65536, v8\n\t"
      "v_mov_b32 v12, 0\n\t"
      "v_mov_b32 v9, s41\n\t"
      "v_lshlrev_b32 v9, 2, v9\n\t"
      "v_cvt_pk_bf16_f32 v190, v24, v25\n\t"
      "v_cvt_pk_bf16_f32 v191, v26, v27\n\t"
      "v_cvt_pk_bf16_f32 v254, v28, v29\n\t"
      "v_cvt_pk_bf16_f32 v255, v30, v31\n\t"
      "s_nop 1\n\t"
      "v_mfma_f32_16x16x32_bf16 a[168:171], v[188:191], v[188:191], a[168:171]\n\t"
      "v_mfma_f32_16x16x32_bf16 a[232:235], v[252:255], v[252:255], a[232:235]\n\t"
      "s_sub_u32 s64, s46, 786432\n\t"
      "s_mov_b32 s71, 98304\n\t"
      "s_mov_b32 s63, 0x10000\n\t"
      "s_nop 4\n\t"
      "v_accvgpr_read_b32 v0, a168\n\t"
      "v_accvgpr_read_b32 v1, a169\n\t"
      "v_accvgpr_read_b32 v2, a170\n\t"
      "v_accvgpr_read_b32 v3, a171\n\t"
      "v_accvgpr_read_b32 v4, a232\n\t"
      "v_accvgpr_read_b32 v5, a233\n\t"
      "v_accvgpr_read_b32 v6, a234\n\t"
      "v_accvgpr_read_b32 v7, a235\n\t"
      "v_and_b32 v0, 0x7fffffff, v0\n\t"
      "v_and_b32 v1, 0x7fffffff, v1\n\t"
      "v_and_b32 v2, 0x7fffffff, v2\n\t"
      "v_and_b32 v3, 0x7fffffff, v3\n\t"
      "v_and_b32 v4, 0x7fffffff, v4\n\t"
      "v_and_b32 v5, 0x7fffffff, v5\n\t"
      "v_and_b32 v6, 0x7fffffff, v6\n\t"
      "v_and_b32 v7, 0x7fffffff, v7\n\t"
      "v_max3_u32 v0, v0, v1, v2\n\t"
      "v_max3_u32 v3, v3, v4, v5\n\t"
      "v_max3_u32 v0, v0, v6, v7\n\t"
      "v_max_u32 v0, v0, v3\n\t"
      "s_nop 1\n\t"
      "v_max_u32_dpp v0, v0, v0 quad_perm:[1,0,3,2] row_mask:0xf bank_mask:0xf\n\t"
      "s_nop 1\n\t"
      "v_max_u32_dpp v0, v0, v0 quad_perm:[2,3,0,1] row_mask:0xf bank_mask:0xf\n\t"
      "s_nop 1\n\t"
      "v_max_u32_dpp v0, v0, v0 row_ror:4 row_mask:0xf bank_mask:0xf\n\t"
      "s_nop 1\n\t"
      "v_max_u32_dpp v0, v0, v0 row_ror:8 row_mask:0xf bank_mask:0xf\n\t"
      "v_mov_b32 v1, v0\n\t"
      "s_nop 1\n\t"
      "v_permlane16_swap_b32 v0, v1\n\t"
      "v_max_u32 v0, v0, v1\n\t"
      "v_mov_b32 v1, v0\n\t"
      "s_nop 1\n\t"
      "v_permlane32_swap_b32 v0, v1\n\t"
      "v_max_u32 v0, v0, v1\n\t"
      "ds_write_b32 v9, v0 offset:32768\n\t"
      "s_waitcnt lgkmcnt(0)\n\t"
      "s_barrier\n\t"
      "ds_read_b128 v[4:7], v12 offset:32768\n\t"
      "s_waitcnt lgkmcnt(0)\n\t"
      "v_max_u32 v4, v4, v5\n\t"
      "v_max3_u32 v4, v4, v6, v7\n\t"
      "v_mov_b32 v5, s53\n\t"
      "s_memrealtime s[92:93]\n\t"
      "v_add_u32 v4, 0x10000, v4\n\t"
      "s_mov_b64 exec, 1\n\t"
      "s_mov_b32 s61, 0xffff\n\t"
      "s_waitcnt lgkmcnt(0)\n\t"
      "v_mov_b32 v6, s92\n\t"
      "v_bfi_b32 v4, s61, v6, v4\n\t"
      "buffer_store_dwordx2 v[4:5], off, s[36:39], s95 sc1\n\t"
      "s_mov_b64 exec, -1\n\t"
      "s_waitcnt vmcnt(1)\n\t"
      "v_bitop3_b32 v96, v96, v97, s53 bitop3:0x7e\n\t"
      "v_bitop3_b32 v98, v98, v99, s53 bitop3:0x7e\n\t"
      "v_or_b32 v96, v96, v98\n\t"
      "s_add_u32 m0, s55, 98304\n\t"
      "v_readfirstlane_b32 s56, v96\n\t"
      "s_nop 1\n\t"
      "s_cmp_eq_u32 s56, 0\n\t"
      "s_cbranch_scc0 LF2%=\n\t"
      "buffer_load_dwordx4 v127, s[36:39], s64 offen sc1 lds\n\t"
      "buffer_load_dwordx4 v127, s[36:39], s64 offen offset:1024 sc1 lds\n\t"
      "LF2%=:\n\t"
      "buffer_load_dwordx4 v[96:99], off, s[36:39], s54 sc1\n\t"
      "ds_write_b128 v8, a[176:179]\n\t"
      "ds_write_b128 v8, a[180:183] offset:4096\n\t"
      "ds_write_b128 v8, a[184:187] offset:8192\n\t"
      "ds_write_b128 v8, a[188:191] offset:12288\n\t"
      "ds_write_b128 v8, a[240:243] offset:16384\n\t"
      "ds_write_b128 v8, a[244:247] offset:20480\n\t"
      "ds_write_b128 v8, a[248:251] offset:24576\n\t"
      "ds_write_b128 v8, a[252:255] offset:28672\n\t"
      "ds_write_b128 v100, v[112:115] offset:57344\n\t"
      "s_cbranch_scc1 LF3%=\n\t"
      "LF0%=:\n\t"
      "s_waitcnt vmcnt(0)\n\t"
      "v_bitop3_b32 v96, v96, v97, s53 bitop3:0x7e\n\t"
      "v_bitop3_b32 v98, v98, v99, s53 bitop3:0x7e\n\t"
      "v_or_b32 v96, v96, v98\n\t"
      "s_add_u32 m0, s55, 98304\n\t"
      "v_readfirstlane_b32 s56, v96\n\t"
      "s_nop 1\n\t"
      "s_cmp_eq_u32 s56, 0\n\t"
      "s_cbranch_scc1 LF1%=\n\t"
      "buffer_load_dwordx4 v[96:99], off, s[36:39], s54 sc1\n\t"
      "s_sub_u32 s63, s63, 1\n\t"
      "s_cmp_lg_u32 s63, 0\n\t"
      "s_cbranch_scc1 LF0%=\n\t"
      "LF1%=:\n\t"
      "buffer_load_dwordx4 v127, s[36:39], s64 offen sc1 lds\n\t"
      "buffer_load_dwordx4 v127, s[36:39], s64 offen offset:1024 sc1 lds\n\t"
      "LF3%=:\n\t"
      "v_lshlrev_b32 v52, 4, v127\n\t"
      "s_mov_b32 s62, 0xffff0000\n\t"
      "v_accvgpr_read_b32 v85, a172\n\t"
      "v_accvgpr_read_b32 v87, a173\n\t"
      "v_accvgpr_read_b32 v89, a174\n\t"
      "v_accvgpr_read_b32 v91, a175\n\t"
      "v_accvgpr_read_b32 v93, a236\n\t"
      "v_accvgpr_read_b32 v95, a237\n\t"
      "v_lshl_or_b32 v84, v85, 16, 1\n\t"
      "v_and_or_b32 v85, v85, s62, 1\n\t"
      "v_lshl_or_b32 v86, v87, 16, 1\n\t"
      "v_and_or_b32 v87, v87, s62, 1\n\t"
      "v_lshl_or_b32 v88, v89, 16, 1\n\t"
      "v_and_or_b32 v89, v89, s62, 1\n\t"
      "v_lshl_or_b32 v90, v91, 16, 1\n\t"
      "v_and_or_b32 v91, v91, s62, 1\n\t"
      "v_lshl_or_b32 v92, v93, 16, 1\n\t"
      "v_and_or_b32 v93, v93, s62, 1\n\t"
      "v_lshl_or_b32 v94, v95, 16, 1\n\t"
      "v_and_or_b32 v95, v95, s62, 1\n\t"
      "s_cmp_eq_u32 s86, 0\n\t"
      "s_cbranch_scc1 LC9%=\n\t"
      "s_waitcnt lgkmcnt(0)\n\t"
      "s_memtime s[96:97]\n\t"
      "s_waitcnt lgkmcnt(0)\n\t"
      "s_buffer_load_dword s87, s[36:39], s86\n\t"
      "s_waitcnt lgkmcnt(0)\n\t"
      "s_memtime s[98:99]\n\t"
      "s_waitcnt lgkmcnt(0)\n\t"
      "LC9%=:\n\t"
      "s_sub_u32 s61, s98, s96\n\t"
      "s_lshr_b32 s61, s61, 2\n\t"
      "s_min_u32 s61, s61, 511\n\t"
      "s_sub_u32 s62, 0, s61\n\t"
      "s_bitcmp1_b32 s86, 13\n\t"
      "s_cselect_b32 s61, s62, s61\n\t"
      "s_cmp_eq_u32 s86, 0\n\t"
      "s_cselect_b32 s65, 0, s61\n\t"
      "v_readfirstlane_b32 s97, v121\n\t"
      "v_mov_b32 v65, v115\n\t"
      "v_mov_b32 v64, 0\n\t"
      "v_mov_b32 v66, 0\n\t"
      "v_mov_b32 v67, 0\n\t"
      "v_mov_b32 v109, 0\n\t"
      "v_mov_b32 v110, 0\n\t"
      "v_mov_b32 v111, 0\n\t"
      "s_mov_b32 s40, -1\n\t"
      "s_add_u32 s45, s55, 32768\n\t"
      "v_mov_b32 v126, 0\n\t"
      "s_mov_b32 s56, 0\n\t"
      "s_mov_b32 s58, 1\n\t"
      "s_mov_b32 s59, 0x3e0293ee\n\t"
      "s_mov_b32 s96, 0x40820000\n\t"
      "s_mov_b32 s57, 1.0\n\t"
      "s_add_u32 s68, s43, 8192\n\t"
      "s_add_u32 s97, s97, s43\n\t"
      "s_add_u32 s97, s97, 0x8000\n\t"
      "s_mov_b32 s99, 0\n\t"
      "s_mov_b32 s84, 3\n\t"
      "s_mov_b32 s85, 0x80400000\n\t"
      "s_mov_b32 s26, 2460\n\t"
      "s_mov_b32 s25, 0\n\t"
      "s_memtime s[78:79]\n\t"
      "s_waitcnt lgkmcnt(0)\n\t"
      "s_sub_u32 s78, s78, 4000\n\t"
      "s_mov_b32 s88, s78\n\t"
      "s_mov_b32 s27, s78\n\t"
      "s_mov_b32 s24, s26\n\t"
      "s_mov_b32 s23, s80\n\t"
      "s_mov_b32 s30, 0\n\t"
      "s_add_u32 s50, s50, 32\n\t"
      "s_mov_b32 s72, 0xcccccccc\n\t"
      "s_mov_b32 s73, 0xcccccccc\n\t"
      "s_waitcnt vmcnt(0) lgkmcnt(0)\n\t"
      "s_barrier\n\t"
      "s_mov_b32 m0, s45\n\t"
      "s_sub_u32 s61, 204800, s71\n\t"
      "buffer_load_dwordx4 v65, s[36:39], s44 offen sc1 lds\n\t"
      "buffer_load_dwordx4 v65, s[36:39], s44 offen offset:1024 sc1 lds\n\t"
      "s_add_u32 m0, s55, s61\n\t"
      "s_nop 0\n\t"
      "buffer_load_dwordx4 v127, s[36:39], s46 offen sc1 lds\n\t"
      "buffer_load_dwordx4 v127, s[36:39], s46 offen offset:1024 sc1 lds\n\t"
      "buffer_load_dwordx2 v[48:49], v52, s[36:39], s94 offen sc1\n\t"
      "buffer_load_dwordx2 v[50:51], v52, s[36:39], s94 offen offset:128 sc1\n\t"
      "s_mov_b32 s62, 0xffff0000\n\t"
      "v_accvgpr_read_b32 v97, a238\n\t"
      "v_accvgpr_read_b32 v99, a239\n\t"
      "v_lshl_or_b32 v96, v97, 16, 1\n\t"
      "v_and_or_b32 v97, v97, s62, 1\n\t"
      "v_lshl_or_b32 v98, v99, 16, 1\n\t"
      "v_and_or_b32 v99, v99, s62, 1\n\t"
      "ds_read_b64_tr_b16 a[168:169], v117 offset:16384\n\t"
      "ds_read_b64_tr_b16 a[170:171], v117 offset:17408\n\t"
      "ds_read_b64_tr_b16 a[232:233], v118 offset:16384\n\t"
      "ds_read_b64_tr_b16 a[234:235], v118 offset:17408\n\t"
      "ds_read_b64_tr_b16 a[172:173], v117 offset:24576\n\t"
      "ds_read_b64_tr_b16 a[174:175], v117 offset:25600\n\t"
      "ds_read_b64_tr_b16 a[236:237], v118 offset:24576\n\t"
      "ds_read_b64_tr_b16 a[238:239], v118 offset:25600\n\t"
      "ds_read_b64_tr_b16 a[176:177], v117 offset:32768\n\t"
      "ds_read_b64_tr_b16 a[178:179], v117 offset:33792\n\t"
      "ds_read_b64_tr_b16 a[240:241], v118 offset:32768\n\t"
      "ds_read_b64_tr_b16 a[242:243], v118 offset:33792\n\t"
      "ds_read_b64_tr_b16 a[180:181], v117 offset:40960\n\t"
      "ds_read_b64_tr_b16 a[182:183], v117 offset:41984\n\t"
      "ds_read_b64_tr_b16 a[244:245], v118 offset:40960\n\t"
      "s_waitcnt lgkmcnt(12)\n\t"
      "ds_read_b64_tr_b16 a[246:247], v118 offset:41984\n\t"
      "ds_read_b64_tr_b16 a[184:185], v117 offset:49152\n\t"
      "ds_read_b64_tr_b16 a[186:187], v117 offset:50176\n\t"
      "s_waitcnt lgkmcnt(12)\n\t"
      "ds_read_b64_tr_b16 a[248:249], v118 offset:49152\n\t"
      "ds_read_b64_tr_b16 a[250:251], v118 offset:50176\n\t"
      "ds_read_b64_tr_b16 a[188:189], v117 offset:57344\n\t"
      "s_waitcnt lgkmcnt(12)\n\t"
      "ds_read_b64_tr_b16 a[190:191], v117 offset:58368\n\t"
      "ds_read_b64_tr_b16 a[252:253], v118 offset:57344\n\t"
      "ds_read_b64_tr_b16 a[254:255], v118 offset:58368\n\t"
      "s_waitcnt lgkmcnt(6)\n\t"
      "s_cmp_eq_u32 s41, 1\n\t"
      "s_cbranch_scc1 LWIN_1%=\n\t"
      "s_cmp_eq_u32 s41, 2\n\t"
      "s_cbranch_scc1 LWIN_2%=\n\t"
      "s_cmp_eq_u32 s41, 3\n\t"
      "s_cbranch_scc1 LWIN_3%=\n\t"
      "s_branch LWIN_0%=\n\t"
      ".p2align 6\n\t.fill 16, 4, 0xbf800000\n\t"
      "LPOLL_0%=:\n\t"
      "buffer_load_dwordx4 v[84:87], v60, s[36:39], s43 offen sc1\n\t"
      "buffer_load_dwordx4 v[88:91], v60, s[36:39], s43 offen offset:1024 sc1\n\t"
      "s_waitcnt lgkmcnt(4)\n\t"
      "v_mfma_f32_16x16x32_bf16 v[32:35], v[0:3], v[8:11], v[32:35]\n\t"
      "v_mfma_f32_16x16x32_bf16 v[36:39], v[4:7], v[8:11], v[36:39]\n\t"
      "v_mfma_f32_16x16x32_bf16 v[40:43], v[0:3], v[12:15], v[40:43]\n\t"
      "v_mfma_f32_16x16x32_bf16 v[44:47], v[4:7], v[12:15], v[44:47]\n\t"
      "buffer_load_dwordx4 v[92:95], v60, s[36:39], s68 offen sc1\n\t"
      "buffer_load_dwordx4 v[96:99], v60, s[36:39], s68 offen offset:1024 sc1\n\t"
      "ds_read_b64 v[54:55], v124 offset:53312\n\t"
      "s_xor_b32 s69, s97, 1024\n\t"
      "s_xor_b32 s98, s97, 512\n\t"
      "s_xor_b32 s23, s97, 1536\n\t"
      "s_waitcnt lgkmcnt(1)\n\t"
      "v_mfma_f32_16x16x32_bf16 v[32:35], v[76:79], v[112:115], v[32:35]\n\t"
      "v_mfma_f32_16x16x32_bf16 v[36:39], v[80:83], v[112:115], v[36:39]\n\t"
      "v_mfma_f32_16x16x32_bf16 v[40:43], v[76:79], v[116:119], v[40:43]\n\t"
      "v_mfma_f32_16x16x32_bf16 v[44:47], v[80:83], v[116:119], v[44:47]\n\t"
      "s_waitcnt lgkmcnt(0)\n\t"
      "v_add_f32 v48, v48, v54\n\t"
      "v_add_f32_e64 v49, v49, v55\n\t"
      "v_cvt_pk_bf16_f32 v48, v48, v49\n\t"
      "buffer_store_dword v48, v62, s[36:39], s28 offen sc1\n\t"
      "s_mov_b64 exec, 1\n\t"
      "ds_write_b64 v109, v[110:111] offset:55392\n\t"
      "s_mov_b64 exec, -1\n\t"
      "s_nop 4\n\t"
      "v_cvt_pk_bf16_f32 v68, v32, v33\n\t"
      "v_cvt_pk_bf16_f32 v69, v34, v35\n\t"
      "v_cvt_pk_bf16_f32 v70, v36, v37\n\t"
      "v_cvt_pk_bf16_f32 v71, v38, v39\n\t"
      "v_cvt_pk_bf16_f32 v74, v40, v41\n\t"
      "v_cvt_pk_bf16_f32 v75, v42, v43\n\t"
      "v_cvt_pk_bf16_f32 v72, v44, v45\n\t"
      "v_cvt_pk_bf16_f32 v73, v46, v47\n\t"
      "v_mov_b64 v[32:33], 0\n\t"
      "v_mov_b64 v[34:35], 0\n\t"
      "v_mov_b64 v[36:37], 0\n\t"
      "v_mov_b64 v[38:39], 0\n\t"
      "v_mov_b64 v[40:41], 0\n\t"
      "v_mov_b64 v[42:43], 0\n\t"
      "v_mov_b64 v[44:45], 0\n\t"
      "v_mov_b64 v[46:47], 0\n\t"
      "v_mov_b64 v[48:49], 0\n\t"
      "v_mov_b64 v[50:51], 0\n\t"
      "v_mov_b64 v[52:53], 0\n\t"
      "v_mov_b64 v[54:55], 0\n\t"
      "LGO_0%=:\n\t"
      "s_nop 0\n\t"
      "s_nop 0\n\t"
      "s_nop 0\n\t"
      "s_nop 0\n\t"
      "s_waitcnt vmcnt(4)\n\t"
      "v_pk_fma_f32 v[6:7], v[86:87], s[56:57], v[126:127] op_sel:[0,1,0] op_sel_hi:[1,1,0]\n\t"
      "v_pk_fma_f32 v[4:5], v[84:85], s[56:57], v[126:127] op_sel:[0,1,0] op_sel_hi:[1,1,0]\n\t"
      "v_cvt_pk_bf16_f32 v0, v4, v5\n\t"
      "v_cvt_pk_bf16_f32 v1, v6, v7\n\t"
      "s_nop 0\n\t"
      "ds_write_b64 v127, v[0:1] offset:40960\n\t"
      "s_waitcnt vmcnt(3)\n\t"
      "v_pk_fma_f32 v[8:9], v[88:89], s[56:57], v[126:127] op_sel:[0,1,0] op_sel_hi:[1,1,0]\n\t"
      "v_pk_fma_f32 v[10:11], v[90:91], s[56:57], v[126:127] op_sel:[0,1,0] op_sel_hi:[1,1,0]\n\t"
      "v_cvt_pk_bf16_f32 v2, v8, v9\n\t"
      "v_cvt_pk_bf16_f32 v3, v10, v11\n\t"
      "ds_write_b64 v127, v[2:3] offset:40968\n\t"
      "v_bitop3_b32 v64, v84, v85, v86 bitop3:0x80\n\t"
      "v_mfma_f32_16x16x32_bf16 v[56:59], v[0:3], v[0:3], 0\n\t"
      "v_bitop3_b32 v64, v64, v87, v88 bitop3:0x80\n\t"
      "v_bitop3_b32 v64, v64, v89, v90 bitop3:0x80\n\t"
      "v_bitop3_b32 v64, v64, v91, s58 bitop3:0x80\n\t"
      "v_cmp_eq_u32_e64 vcc, 0, v64\n\t"
      "v_smfmac_f32_16x16x64_bf16 v[36:39], v[0:3], a[64:71], v122\n\t"
      "s_nop 0\n\t"
      "v_bitop3_b32 v120, v120, v121, s53 bitop3:0x7e\n\t"
      "v_smfmac_f32_16x16x64_bf16 v[44:47], v[0:3], a[192:199], v122\n\t"
      "s_cbranch_vccnz LR0_0%=\n\t"
      "LC0_0%=:\n\t"
      "s_waitcnt lgkmcnt(0)\n\t"
      "s_barrier\n\t"
      "ds_read_b128 v[4:7], v127 offset:41984\n\t"
      "v_smfmac_f32_16x16x64_bf16 v[52:55], v[0:3], v[192:199], v122\n\t"
      "ds_read_b128 v[8:11], v127 offset:43008\n\t"
      "v_smfmac_f32_16x16x64_bf16 v[48:51], v[0:3], v[128:135], v122\n\t"
      "ds_read_b128 v[12:15], v127 offset:44032\n\t"
      "v_smfmac_f32_16x16x64_bf16 v[32:35], v[0:3], a[0:7], v122\n\t"
      "ds_read_b128 v[84:87], v127 offset:36864\n\t"
      "v_smfmac_f32_16x16x64_bf16 v[40:43], v[0:3], a[128:135], v122\n\t"
      "s_waitcnt lgkmcnt(3)\n\t"
      "v_smfmac_f32_16x16x64_bf16 v[32:35], v[4:7], a[8:15], v122\n\t"
      "ds_read_b128 v[80:83], v127 offset:35840\n\t"
      "v_smfmac_f32_16x16x64_bf16 v[40:43], v[4:7], a[136:143], v122\n\t"
      "s_waitcnt vmcnt(2)\n\t"
      "v_fma_f32 v23, v95, s57, v126\n\t"
      "v_fma_f32 v20, v92, s57, v126\n\t"
      "v_smfmac_f32_16x16x64_bf16 v[36:39], v[4:7], a[72:79], v122\n\t"
      "v_fma_f32 v22, v94, s57, v126\n\t"
      "v_cvt_pk_bf16_f32 v17, v22, v23\n\t"
      "v_bitop3_b32 v64, v92, v93, v94 bitop3:0x80\n\t"
      "v_smfmac_f32_16x16x64_bf16 v[44:47], v[4:7], a[200:207], v122\n\t"
      "s_waitcnt lgkmcnt(2)\n\t"
      "v_fma_f32 v21, v93, s57, v126\n\t"
      "v_cvt_pk_bf16_f32 v16, v20, v21\n\t"
      "v_smfmac_f32_16x16x64_bf16 v[36:39], v[8:11], a[80:87], v122\n\t"
      "s_waitcnt vmcnt(1)\n\t"
      "ds_write_b64 v127, v[16:17] offset:45056\n\t"
      "v_fma_f32 v27, v99, s57, v126\n\t"
      "v_fma_f32 v26, v98, s57, v126\n\t"
      "v_cvt_pk_bf16_f32 v19, v26, v27\n\t"
      "v_fma_f32 v25, v97, s57, v126\n\t"
      "v_fma_f32 v24, v96, s57, v126\n\t"
      "v_cvt_pk_bf16_f32 v18, v24, v25\n\t"
      "v_smfmac_f32_16x16x64_bf16 v[44:47], v[8:11], a[208:215], v122\n\t"
      "ds_write_b64 v127, v[18:19] offset:45064\n\t"
      "v_smfmac_f32_16x16x64_bf16 v[40:43], v[8:11], a[144:151], v122\n\t"
      "ds_read_b128 v[88:91], v127 offset:39936\n\t"
      "v_bitop3_b32 v64, v64, v95, v96 bitop3:0x80\n\t"
      "v_bitop3_b32 v64, v64, v97, v98 bitop3:0x80\n\t"
      "v_smfmac_f32_16x16x64_bf16 v[40:43], v[12:15], a[152:159], v122\n\t"
      "v_bitop3_b32 v64, v64, v99, s60 bitop3:0x80\n\t"
      "v_smfmac_f32_16x16x64_bf16 v[36:39], v[12:15], a[88:95], v122\n\t"
      "v_cmp_eq_u32_e64 vcc, 0, v64\n\t"
      "v_smfmac_f32_16x16x64_bf16 v[44:47], v[12:15], a[216:223], v122\n\t"
      "s_waitcnt lgkmcnt(1)\n\t"
      "v_smfmac_f32_16x16x64_bf16 v[32:35], v[8:11], a[16:23], v122\n\t"
      "s_cbranch_vccnz LR1_0%=\n\t"
      "LC1_0%=:\n\t"
      "v_smfmac_f32_16x16x64_bf16 v[40:43], v[16:19], a[160:167], v122\n\t"
      "s_barrier\n\t"
      "v_mfma_f32_16x16x32_bf16 v[56:59], v[16:19], v[16:19], v[56:59]\n\t"
      "ds_read_b128 v[20:23], v127 offset:46080\n\t"
      "s_nop 0\n\t"
      "v_smfmac_f32_16x16x64_bf16 v[32:35], v[12:15], a[24:31], v122\n\t"
      "ds_read_b128 v[24:27], v127 offset:47104\n\t"
      "v_smfmac_f32_16x16x64_bf16 v[44:47], v[16:19], a[224:231], v122\n\t"
      "ds_read_b128 v[28:31], v127 offset:48128\n\t"
      "v_max3_f32 v56, v56, v57, v58\n\t"
      "v_smfmac_f32_16x16x64_bf16 v[36:39], v[16:19], a[96:103], v122\n\t"
      "ds_read_b128 v[76:79], v127 offset:32768\n\t"
      "v_max_f32_e64 v56, v56, v59\n\t"
      "v_smfmac_f32_16x16x64_bf16 v[32:35], v[16:19], a[32:39], v122\n\t"
      "s_waitcnt lgkmcnt(3)\n\t"
      "v_readfirstlane_b32 s56, v120\n\t"
      "v_smfmac_f32_16x16x64_bf16 v[32:35], v[20:23], a[40:47], v122\n\t"
      "s_waitcnt lgkmcnt(2)\n\t"
      "v_max_f32_dpp v56, v56, v56 quad_perm:[1,0,3,2] row_mask:0xf bank_mask:0xf\n\t"
      "v_smfmac_f32_16x16x64_bf16 v[32:35], v[24:27], a[48:55], v122\n\t"
      "s_waitcnt lgkmcnt(1)\n\t"
      "v_max_f32_dpp v56, v56, v56 quad_perm:[2,3,0,1] row_mask:0xf bank_mask:0xf\n\t"
      "v_smfmac_f32_16x16x64_bf16 v[32:35], v[28:31], a[56:63], v122\n\t"
      "s_mov_b32 exec_lo, 0x100001\n\t"
      "s_mov_b32 exec_hi, 0x10000100\n\t"
      "v_smfmac_f32_16x16x64_bf16 v[36:39], v[20:23], a[104:111], v122\n\t"
      "ds_max_i32 v109, v56 offset:55392\n\t"
      "v_smfmac_f32_16x16x64_bf16 v[40:43], v[20:23], a[168:175], v122\n\t"
      "s_mov_b64 exec, -1\n\t"
      "v_add_f32 v33, v33, v35\n\t"
      "v_smfmac_f32_16x16x64_bf16 v[36:39], v[24:27], a[112:119], v122\n\t"
      "ds_read_b128 v[60:63], v127 offset:37888\n\t"
      "v_mul_f32_e64 v33, s59, v33\n\t"
      "v_smfmac_f32_16x16x64_bf16 v[36:39], v[28:31], a[120:127], v122\n\t"
      "v_add_f32_e64 v32, v32, v34\n\t"
      "v_smfmac_f32_16x16x64_bf16 v[44:47], v[20:23], a[232:239], v122\n\t"
      "ds_read_b128 v[56:59], v127 offset:38912\n\t"
      "v_mul_f32_e64 v32, s59, v32\n\t"
      "v_smfmac_f32_16x16x64_bf16 v[44:47], v[24:27], a[240:247], v122\n\t"
      "v_add_f32 v36, v36, v38\n\t"
      "v_add_f32 v37, v37, v39\n\t"
      "v_mul_f32_e64 v37, s59, v37\n\t"
      "v_smfmac_f32_16x16x64_bf16 v[40:43], v[24:27], a[176:183], v122\n\t"
      "ds_read_b128 v[92:95], v127 offset:34816\n\t"
      "v_cvt_pk_bf16_f32 v112, v32, v33\n\t"
      "v_smfmac_f32_16x16x64_bf16 v[44:47], v[28:31], a[248:255], v122\n\t"
      "ds_read_b128 v[96:99], v127 offset:33792\n\t"
      "v_mul_f32_e64 v36, s59, v36\n\t"
      "v_cvt_pk_bf16_f32 v113, v36, v37\n\t"
      "v_smfmac_f32_16x16x64_bf16 v[40:43], v[28:31], a[184:191], v122\n\t"
      "ds_write_b64 v125, v[112:113]\n\t"
      "v_smfmac_f32_16x16x64_bf16 v[48:51], v[4:7], v[136:143], v122\n\t"
      "s_nop 0\n\t"
      "v_add_f32 v45, v45, v47\n\t"
      "v_add_f32_e64 v44, v44, v46\n\t"
      "v_cvt_pk_bf16_f32 v121, v44, v45\n\t"
      "v_add_f32 v41, v41, v43\n\t"
      "v_add_f32 v40, v40, v42\n\t"
      "v_cvt_pk_bf16_f32 v120, v40, v41\n\t"
      "v_smfmac_f32_16x16x64_bf16 v[48:51], v[8:11], v[144:151], v122\n\t"
      "s_mov_b32 s58, 0x10000000\n\t"
      "ds_write_b64 v125, v[120:121] offset:2080\n\t"
      "v_smfmac_f32_16x16x64_bf16 v[48:51], v[12:15], v[152:159], v122\n\t"
      "s_waitcnt lgkmcnt(1)\n\t"
      "s_barrier\n\t"
      "ds_read_b64_tr_b16 v[36:37], v124 offset:50192\n\t"
      "v_smfmac_f32_16x16x64_bf16 v[48:51], v[16:19], v[160:167], v122\n\t"
      "ds_read_b32 v108, v109 offset:55392\n\t"
      "ds_read_b64_tr_b16 v[32:33], v124 offset:49152\n\t"
      "v_smfmac_f32_16x16x64_bf16 v[52:55], v[4:7], v[200:207], v122\n\t"
      "ds_read_b64_tr_b16 v[34:35], v124 offset:49672\n\t"
      "s_waitcnt vmcnt(0) lgkmcnt(4)\n\t"
      "ds_read_b64_tr_b16 v[38:39], v124 offset:50712\n\t"
      "v_smfmac_f32_16x16x64_bf16 v[48:51], v[20:23], v[168:175], v122\n\t"
      "s_barrier\n\t"
      "ds_read_b64_tr_b16 v[46:47], v124 offset:52792\n\t"
      "v_smfmac_f32_16x16x64_bf16 v[48:51], v[24:27], v[176:183], v122\n\t"
      "s_waitcnt lgkmcnt(4)\n\t"
      "ds_read_b64_tr_b16 v[44:45], v124 offset:52272\n\t"
      "v_readfirstlane_b32 s61, v108\n\t"
      "v_smfmac_f32_16x16x64_bf16 v[48:51], v[28:31], v[184:191], v122\n\t"
      "s_waitcnt lgkmcnt(3)\n\t"
      "ds_read_b64_tr_b16 v[42:43], v124 offset:51752\n\t"
      "ds_read_b64_tr_b16 v[40:41], v124 offset:51232\n\t"
      "s_waitcnt lgkmcnt(4)\n\t"
      "v_mfma_f32_16x16x32_bf16 v[112:115], v[76:79], v[32:35], 0\n\t"
      "s_lshr_b32 s61, s61, 23\n\t"
      "s_add_u32 s61, s61, s67\n\t"
      "v_mov_b32_dpp v102, v34 quad_perm:[2,3,0,1] row_mask:0xf bank_mask:0xf\n\t"
      "v_mov_b32_dpp v106, v38 quad_perm:[2,3,0,1] row_mask:0xf bank_mask:0xf\n\t"
      "s_max_u32 s61, s61, s66\n\t"
      "v_mfma_f32_16x16x32_bf16 v[116:119], v[80:83], v[32:35], 0\n\t"
      "s_lshr_b32 s61, s61, 1\n\t"
      "v_add_f32_e64 v48, v48, v50\n\t"
      "v_mov_b32_dpp v100, v32 quad_perm:[2,3,0,1] row_mask:0xf bank_mask:0xf\n\t"
      "v_mfma_f32_16x16x32_bf16 v[116:119], v[88:91], v[36:39], v[116:119]\n\t"
      "s_min_u32 s61, s61, 200\n\t"
      "v_mov_b32_dpp v107, v39 quad_perm:[2,3,0,1] row_mask:0xf bank_mask:0xf\n\t"
      "s_waitcnt lgkmcnt(0)\n\t"
      "v_mfma_f32_16x16x32_bf16 v[76:79], v[40:43], v[32:35], 0\n\t"
      "buffer_load_dwordx4 v127, s[36:39], s46 offen sc1 lds\n\t"
      "s_lshl_b32 s61, s61, 23\n\t"
      "v_mfma_f32_16x16x32_bf16 v[76:79], v[44:47], v[36:39], v[76:79]\n\t"
      "s_add_u32 s65, s61, 33554432\n\t"
      "v_mov_b32_dpp v104, v36 quad_perm:[2,3,0,1] row_mask:0xf bank_mask:0xf\n\t"
      "v_mov_b32_dpp v103, v35 quad_perm:[2,3,0,1] row_mask:0xf bank_mask:0xf\n\t"
      "v_mov_b32_dpp v101, v33 quad_perm:[2,3,0,1] row_mask:0xf bank_mask:0xf\n\t"
      "v_mfma_f32_16x16x32_bf16 v[112:115], v[84:87], v[36:39], v[112:115]\n\t"
      "s_pack_hh_b32_b16 s75, s65, s65\n\t"
      "v_add_f32 v49, v49, v51\n\t"
      "v_cvt_pk_bf16_f32 v64, v48, v49\n\t"
      "v_cndmask_b32_e64 v120, v76, v78, s[72:73]\n\t"
      "v_cndmask_b32_e64 v121, v77, v79, s[72:73]\n\t"
      "v_mov_b32_dpp v105, v37 quad_perm:[2,3,0,1] row_mask:0xf bank_mask:0xf\n\t"
      "v_mfma_f32_16x16x32_bf16 v[112:115], v[92:95], v[100:103], v[112:115]\n\t"
      "buffer_load_dwordx4 v127, s[36:39], s46 offen offset:1024 sc1 lds\n\t"
      "v_mfma_f32_16x16x32_bf16 v[116:119], v[96:99], v[100:103], v[116:119]\n\t"
      "s_pack_hh_b32_b16 s74, s65, s65\n\t"
      "v_add_f32_dpp v121, v121, v121 quad_perm:[2,3,0,1] row_mask:0xf bank_mask:0xf\n\t"
      "v_add_f32_dpp v120, v120, v120 quad_perm:[2,3,0,1] row_mask:0xf bank_mask:0xf\n\t"
      "v_mfma_f32_16x16x32_bf16 v[112:115], v[56:59], v[104:107], v[112:115]\n\t"
      "v_exp_f32 v81, v121\n\t"
      "v_mfma_f32_16x16x32_bf16 v[116:119], v[60:63], v[104:107], v[116:119]\n\t"
      "s_mov_b32 m0, s45\n\t"
      "v_mov_b64 v[90:91], s[74:75]\n\t"
      "v_exp_f32 v80, v120\n\t"
      "v_mov_b64 v[88:89], s[74:75]\n\t"
      "v_cvt_pk_bf16_f32 v108, v80, v81\n\t"
      "v_smfmac_f32_16x16x64_bf16 v[52:55], v[8:11], v[208:215], v122\n\t"
      "buffer_load_dwordx4 v65, s[36:39], s44 offen offset:1024 sc1 lds\n\t"
      "v_mfma_f32_16x16x32_bf16 v[84:87], v[108:111], v[64:67], 0\n\t"
      "s_add_u32 s57, s61, 310378496\n\t"
      "v_cndmask_b32_e64 v112, v112, v116, s[72:73]\n\t"
      "v_exp_f32_e64 v56, v112\n\t"
      "v_mfma_f32_16x16x32_bf16 v[48:51], v[108:111], v[88:91], 0\n\t"
      "s_add_u32 s43, s43, 0x8000\n\t"
      "v_cndmask_b32_e64 v113, v113, v117, s[72:73]\n\t"
      "v_cndmask_b32_e64 v114, v114, v118, s[72:73]\n\t"
      "v_exp_f32_e64 v58, v114\n\t"
      "v_cndmask_b32_e64 v115, v115, v119, s[72:73]\n\t"
      "v_exp_f32 v57, v113\n\t"
      "v_exp_f32 v59, v115\n\t"
      "v_cvt_pk_bf16_f32 v96, v56, v57\n\t"
      "v_cvt_pk_bf16_f32 v97, v58, v59\n\t"
      "v_max3_f32 v82, |v112|, |v113|, |v114|\n\t"
      "v_mov_b32_dpp v98, v96 quad_perm:[2,3,0,1] row_mask:0xf bank_mask:0xf\n\t"
      "v_mov_b32_dpp v99, v97 quad_perm:[2,3,0,1] row_mask:0xf bank_mask:0xf\n\t"
      "v_smfmac_f32_16x16x64_bf16 v[52:55], v[12:15], v[216:223], v122\n\t"
      "v_max3_f32 v83, |v115|, |v120|, |v121|\n\t"
      "v_mfma_f32_16x16x32_bf16 v[48:51], v[96:99], v[88:91], v[48:51]\n\t"
      "v_max_f32_e64 v82, v82, v83\n\t"
      "v_cmp_nge_f32 vcc, 0x42000000, v82\n\t"
      "v_mfma_f32_16x16x32_bf16 v[84:87], v[96:99], v[68:71], v[84:87]\n\t"
      "buffer_load_dwordx4 v65, s[36:39], s44 offen sc1 lds\n\t"
      "v_smfmac_f32_16x16x64_bf16 v[52:55], v[16:19], v[224:231], v122\n\t"
      "s_add_u32 s68, s68, 0x8000\n\t"
      "s_cbranch_vccnz LMX_0%=\n\t"
      "v_rcp_f32 v49, v49\n\t"
      "v_rcp_f32_e64 v48, v48\n\t"
      "v_smfmac_f32_16x16x64_bf16 v[52:55], v[20:23], v[232:239], v122\n\t"
      "v_fma_f32 v85, v85, v49, s96\n\t"
      "v_fma_f32 v84, v84, v48, s96\n\t"
      "v_smfmac_f32_16x16x64_bf16 v[52:55], v[24:27], v[240:247], v122\n\t"
      "buffer_atomic_add v84, v123, s[36:39], s97 offen\n\t"
      "buffer_atomic_add v85, v123, s[36:39], s98 offen\n\t"
      "v_smfmac_f32_16x16x64_bf16 v[52:55], v[28:31], v[248:255], v122\n\t"
      "v_mfma_f32_16x16x32_bf16 v[92:95], v[96:99], v[72:75], 0\n\t"
      "s_nop 3\n\t"
      "v_add_f32 v53, v53, v55\n\t"
      "v_add_f32_e64 v52, v52, v54\n\t"
      "v_cvt_pk_bf16_f32 v66, v52, v53\n\t"
      "s_add_u32 s62, s61, 0x83400000\n\t"
      "v_mov_b32_e64 v126, s62\n\t"
      "v_mfma_f32_16x16x32_bf16 v[92:95], v[108:111], v[66:69], v[92:95]\n\t"
      "s_add_u32 s97, s97, 0x8000\n\t"
      "s_nop 6\n\t"
      "v_fma_f32 v95, v95, v49, s96\n\t"
      "buffer_atomic_add v95, v123, s[36:39], s23 offen\n\t"
      "v_fma_f32 v94, v94, v48, s96\n\t"
      "buffer_atomic_add v94, v123, s[36:39], s69 offen\n\t"
      "LWIN_0%=:\n\t"
      "v_add_u32 v100, s55, v127\n\t"
      "ds_read_b128 v[60:63], v100 offset:57344\n\t"
      "s_cmp_eq_u32 s56, 0\n\t"
      "s_cbranch_scc0 LSLOW_0%=\n\t"
      "LWIN1_0%=:\n\t"
      "buffer_load_dwordx2 v[120:121], off, s[36:39], s50 sc1\n\t"
      "s_mov_b32 s64, s50\n\t"
      "s_bitcmp1_b64 s[84:85], s40\n\t"
      "s_cbranch_scc1 LEARLY_0%=\n\t"
      "LROLE_0%=:\n\t"
      "v_add_u32 v52, 65536, v127\n\t"
      "ds_read_b128 v[88:91], v52\n\t"
      "ds_read_b128 v[92:95], v52 offset:1024\n\t"
      "ds_read_b128 v[32:35], v52 offset:4096\n\t"
      "ds_read_b128 v[36:39], v52 offset:5120\n\t"
      "ds_read_b128 v[40:43], v52 offset:8192\n\t"
      "ds_read_b128 v[44:47], v52 offset:9216\n\t"
      "v_mov_b64 v[48:49], 0\n\t"
      "v_mov_b64 v[50:51], 0\n\t"
      "v_add_u32 v104, s71, v100\n\t"
      "ds_read_b128 v[76:79], v104\n\t"
      "ds_read_b128 v[80:83], v104 offset:1024\n\t"
      "ds_read_b128 v[112:115], v100\n\t"
      "ds_read_b128 v[116:119], v100 offset:1024\n\t"
      "v_xor_b32 v101, 2048, v104\n\t"
      "v_xor_b32 v102, 4096, v104\n\t"
      "v_xor_b32 v103, 6144, v104\n\t"
      "s_waitcnt lgkmcnt(8)\n\t"
      "v_smfmac_f32_16x16x64_bf16 v[48:51], v[0:3], v[88:95], v122\n\t"
      "ds_read_b128 v[88:91], v52 offset:12288\n\t"
      "ds_read_b128 v[92:95], v52 offset:13312\n\t"
      "s_add_u32 m0, s55, s71\n\t"
      "s_sub_u32 s71, 204800, s71\n\t"
      "s_waitcnt lgkmcnt(8)\n\t"
      "v_smfmac_f32_16x16x64_bf16 v[48:51], v[4:7], v[32:39], v122\n\t"
      "ds_read_b128 v[16:19], v101\n\t"
      "ds_read_b128 v[20:23], v101 offset:1024\n\t"
      "s_add_u32 s40, s40, 1\n\t"
      "s_cmp_lt_u32 s40, 63\n\t"
      "s_waitcnt lgkmcnt(8)\n\t"
      "v_smfmac_f32_16x16x64_bf16 v[48:51], v[8:11], v[40:47], v122\n\t"
      "ds_read_b128 v[24:27], v100 offset:8192\n\t"
      "ds_read_b128 v[28:31], v100 offset:9216\n\t"
      "s_cselect_b32 s61, 786432, 0\n\t"
      "s_cselect_b32 s60, s58, 0\n\t"
      "s_waitcnt lgkmcnt(4)\n\t"
      "v_smfmac_f32_16x16x64_bf16 v[48:51], v[12:15], v[88:95], v122\n\t"
      "s_add_u32 s44, s44, s61\n\t"
      "s_cmp_lt_u32 s40, 62\n\t"
      "ds_read_b128 v[0:3], v102\n\t"
      "ds_read_b128 v[4:7], v102 offset:1024\n\t"
      "ds_read_b128 v[8:11], v100 offset:16384\n\t"
      "ds_read_b128 v[12:15], v100 offset:17408\n\t"
      "s_cselect_b32 s61, 786432, 0\n\t"
      "s_add_u32 s46, s46, s61\n\t"
      "s_add_u32 s61, s40, 3\n\t"
      "s_and_b32 s62, s61, 3\n\t"
      "s_cselect_b32 s62, 16, 2000\n\t"
      "s_cmp_lt_u32 s61, 64\n\t"
      "s_cselect_b32 s62, s62, 0\n\t"
      "s_add_u32 s50, s50, s62\n\t"
      "s_mov_b32 s28, s49\n\t"
      "s_add_u32 s49, s49, 65536\n\t"
      "s_cmp_eq_u32 s40, 64\n\t"
      "v_add_f32 v48, v48, v50\n\t"
      "v_add_f32 v49, v49, v51\n\t"
      "s_cbranch_scc1 LFIN_0%=\n\t"
      "v_mfma_f32_16x16x32_bf16 v[32:35], v[76:79], v[112:115], 0\n\t"
      "s_mov_b32 s63, 0x2000\n\t"
      "v_mfma_f32_16x16x32_bf16 v[36:39], v[80:83], v[112:115], 0\n\t"
      "v_mfma_f32_16x16x32_bf16 v[40:43], v[76:79], v[116:119], 0\n\t"
      "v_mfma_f32_16x16x32_bf16 v[44:47], v[80:83], v[116:119], 0\n\t"
      "ds_read_b128 v[76:79], v103\n\t"
      "ds_read_b128 v[80:83], v103 offset:1024\n\t"
      "ds_read_b128 v[112:115], v100 offset:24576\n\t"
      "ds_read_b128 v[116:119], v100 offset:25600\n\t"
      "s_waitcnt lgkmcnt(8)\n\t"
      "v_mfma_f32_16x16x32_bf16 v[32:35], v[16:19], v[24:27], v[32:35]\n\t"
      "v_mfma_f32_16x16x32_bf16 v[36:39], v[20:23], v[24:27], v[36:39]\n\t"
      "v_mfma_f32_16x16x32_bf16 v[40:43], v[16:19], v[28:31], v[40:43]\n\t"
      "v_mfma_f32_16x16x32_bf16 v[44:47], v[20:23], v[28:31], v[44:47]\n\t"
      "LGATE_0%=:\n\t"
      "LBAR_0%=:\n\t"
      "s_cmp_gt_u32 s40, 1\n\t"
      "s_barrier\n\t"
      "s_cbranch_scc1 LPOLL_0%=\n\t"
      "s_branch LFIRST_0%=\n\t"
      ".p2align 6\n\t.fill 16, 4, 0xbf800000\n\t"
      "LPOLL_1%=:\n\t"
      "buffer_load_dwordx4 v[84:87], v60, s[36:39], s43 offen sc1\n\t"
      "buffer_load_dwordx4 v[88:91], v60, s[36:39], s43 offen offset:1024 sc1\n\t"
      "s_waitcnt lgkmcnt(4)\n\t"
      "v_mfma_f32_16x16x32_bf16 v[32:35], v[0:3], v[8:11], v[32:35]\n\t"
      "v_mfma_f32_16x16x32_bf16 v[36:39], v[4:7], v[8:11], v[36:39]\n\t"
      "v_mfma_f32_16x16x32_bf16 v[40:43], v[0:3], v[12:15], v[40:43]\n\t"
      "v_mfma_f32_16x16x32_bf16 v[44:47], v[4:7], v[12:15], v[44:47]\n\t"
      "buffer_load_dwordx4 v[92:95], v60, s[36:39], s68 offen sc1\n\t"
      "buffer_load_dwordx4 v[96:99], v60, s[36:39], s68 offen offset:1024 sc1\n\t"
      "ds_read_b64 v[54:55], v124 offset:53832\n\t"
      "s_xor_b32 s69, s97, 1024\n\t"
      "s_xor_b32 s98, s97, 512\n\t"
      "s_xor_b32 s23, s97, 1536\n\t"
      "s_waitcnt lgkmcnt(1)\n\t"
      "v_mfma_f32_16x16x32_bf16 v[32:35], v[76:79], v[112:115], v[32:35]\n\t"
      "v_mfma_f32_16x16x32_bf16 v[36:39], v[80:83], v[112:115], v[36:39]\n\t"
      "v_mfma_f32_16x16x32_bf16 v[40:43], v[76:79], v[116:119], v[40:43]\n\t"
      "v_mfma_f32_16x16x32_bf16 v[44:47], v[80:83], v[116:119], v[44:47]\n\t"
      "s_waitcnt lgkmcnt(0)\n\t"
      "v_add_f32 v48, v48, v54\n\t"
      "v_add_f32_e64 v49, v49, v55\n\t"
      "v_cvt_pk_bf16_f32 v48, v48, v49\n\t"
      "buffer_store_dword v48, v62, s[36:39], s28 offen sc1\n\t"
      "s_mov_b64 exec, 1\n\t"
      "ds_write_b64 v109, v[110:111] offset:55392\n\t"
      "s_mov_b64 exec, -1\n\t"
      "s_nop 4\n\t"
      "v_cvt_pk_bf16_f32 v68, v32, v33\n\t"
      "v_cvt_pk_bf16_f32 v69, v34, v35\n\t"
      "v_cvt_pk_bf16_f32 v70, v36, v37\n\t"
      "v_cvt_pk_bf16_f32 v71, v38, v39\n\t"
      "v_cvt_pk_bf16_f32 v74, v40, v41\n\t"
      "v_cvt_pk_bf16_f32 v75, v42, v43\n\t"
      "v_cvt_pk_bf16_f32 v72, v44, v45\n\t"
      "v_cvt_pk_bf16_f32 v73, v46, v47\n\t"
      "v_mov_b64 v[32:33], 0\n\t"
      "v_mov_b64 v[34:35], 0\n\t"
      "v_mov_b64 v[36:37], 0\n\t"
      "v_mov_b64 v[38:39], 0\n\t"
      "v_mov_b64 v[40:41], 0\n\t"
      "v_mov_b64 v[42:43], 0\n\t"
      "v_mov_b64 v[44:45], 0\n\t"
      "v_mov_b64 v[46:47], 0\n\t"
      "v_mov_b64 v[48:49], 0\n\t"
      "v_mov_b64 v[50:51], 0\n\t"
      "v_mov_b64 v[52:53], 0\n\t"
      "v_mov_b64 v[54:55], 0\n\t"
      "LGO_1%=:\n\t"
      "s_nop 0\n\t"
      "s_nop 0\n\t"
      "s_nop 0\n\t"
      "s_nop 0\n\t"
      "s_waitcnt vmcnt(4)\n\t"
      "v_pk_fma_f32 v[2:3], v[86:87], s[56:57], v[126:127] op_sel:[0,1,0] op_sel_hi:[1,1,0]\n\t"
      "v_pk_fma_f32 v[0:1], v[84:85], s[56:57], v[126:127] op_sel:[0,1,0] op_sel_hi:[1,1,0]\n\t"
      "v_cvt_pk_bf16_f32 v4, v0, v1\n\t"
      "v_cvt_pk_bf16_f32 v5, v2, v3\n\t"
      "s_nop 0\n\t"
      "ds_write_b64 v127, v[4:5] offset:41984\n\t"
      "s_waitcnt vmcnt(3)\n\t"
      "v_pk_fma_f32 v[12:13], v[88:89], s[56:57], v[126:127] op_sel:[0,1,0] op_sel_hi:[1,1,0]\n\t"
      "v_pk_fma_f32 v[14:15], v[90:91], s[56:57], v[126:127] op_sel:[0,1,0] op_sel_hi:[1,1,0]\n\t"
      "v_cvt_pk_bf16_f32 v6, v12, v13\n\t"
      "v_cvt_pk_bf16_f32 v7, v14, v15\n\t"
      "ds_write_b64 v127, v[6:7] offset:41992\n\t"
      "v_bitop3_b32 v64, v84, v85, v86 bitop3:0x80\n\t"
      "v_mfma_f32_16x16x32_bf16 v[56:59], v[4:7], v[4:7], 0\n\t"
      "v_bitop3_b32 v64, v64, v87, v88 bitop3:0x80\n\t"
      "v_bitop3_b32 v64, v64, v89, v90 bitop3:0x80\n\t"
      "v_bitop3_b32 v64, v64, v91, s58 bitop3:0x80\n\t"
      "v_cmp_eq_u32_e64 vcc, 0, v64\n\t"
      "v_smfmac_f32_16x16x64_bf16 v[36:39], v[4:7], a[72:79], v122\n\t"
      "s_nop 0\n\t"
      "v_bitop3_b32 v120, v120, v121, s53 bitop3:0x7e\n\t"
      "v_smfmac_f32_16x16x64_bf16 v[44:47], v[4:7], a[200:207], v122\n\t"
      "s_cbranch_vccnz LR0_1%=\n\t"
      "LC0_1%=:\n\t"
      "s_waitcnt lgkmcnt(0)\n\t"
      "s_barrier\n\t"
      "ds_read_b128 v[0:3], v127 offset:40960\n\t"
      "v_smfmac_f32_16x16x64_bf16 v[52:55], v[4:7], v[200:207], v122\n\t"
      "ds_read_b128 v[12:15], v127 offset:44032\n\t"
      "v_smfmac_f32_16x16x64_bf16 v[48:51], v[4:7], v[136:143], v122\n\t"
      "ds_read_b128 v[8:11], v127 offset:43008\n\t"
      "v_smfmac_f32_16x16x64_bf16 v[32:35], v[4:7], a[8:15], v122\n\t"
      "ds_read_b128 v[84:87], v127 offset:36864\n\t"
      "v_smfmac_f32_16x16x64_bf16 v[40:43], v[4:7], a[136:143], v122\n\t"
      "s_waitcnt lgkmcnt(3)\n\t"
      "v_smfmac_f32_16x16x64_bf16 v[32:35], v[0:3], a[0:7], v122\n\t"
      "ds_read_b128 v[80:83], v127 offset:35840\n\t"
      "v_smfmac_f32_16x16x64_bf16 v[40:43], v[0:3], a[128:135], v122\n\t"
      "s_waitcnt vmcnt(2)\n\t"
      "v_fma_f32 v19, v95, s57, v126\n\t"
      "v_fma_f32 v16, v92, s57, v126\n\t"
      "v_smfmac_f32_16x16x64_bf16 v[36:39], v[0:3], a[64:71], v122\n\t"
      "v_fma_f32 v18, v94, s57, v126\n\t"
      "v_cvt_pk_bf16_f32 v21, v18, v19\n\t"
      "v_bitop3_b32 v64, v92, v93, v94 bitop3:0x80\n\t"
      "v_smfmac_f32_16x16x64_bf16 v[44:47], v[0:3], a[192:199], v122\n\t"
      "s_waitcnt lgkmcnt(2)\n\t"
      "v_fma_f32 v17, v93, s57, v126\n\t"
      "v_cvt_pk_bf16_f32 v20, v16, v17\n\t"
      "v_smfmac_f32_16x16x64_bf16 v[36:39], v[12:15], a[88:95], v122\n\t"
      "s_waitcnt vmcnt(1)\n\t"
      "ds_write_b64 v127, v[20:21] offset:46080\n\t"
      "v_fma_f32 v31, v99, s57, v126\n\t"
      "v_fma_f32 v30, v98, s57, v126\n\t"
      "v_cvt_pk_bf16_f32 v23, v30, v31\n\t"
      "v_fma_f32 v29, v97, s57, v126\n\t"
      "v_fma_f32 v28, v96, s57, v126\n\t"
      "v_cvt_pk_bf16_f32 v22, v28, v29\n\t"
      "v_smfmac_f32_16x16x64_bf16 v[44:47], v[12:15], a[216:223], v122\n\t"
      "ds_write_b64 v127, v[22:23] offset:46088\n\t"
      "v_smfmac_f32_16x16x64_bf16 v[40:43], v[12:15], a[152:159], v122\n\t"
      "ds_read_b128 v[88:91], v127 offset:39936\n\t"
      "v_bitop3_b32 v64, v64, v95, v96 bitop3:0x80\n\t"
      "v_bitop3_b32 v64, v64, v97, v98 bitop3:0x80\n\t"
      "v_smfmac_f32_16x16x64_bf16 v[40:43], v[8:11], a[144:151], v122\n\t"
      "v_bitop3_b32 v64, v64, v99, s60 bitop3:0x80\n\t"
      "v_smfmac_f32_16x16x64_bf16 v[36:39], v[8:11], a[80:87], v122\n\t"
      "v_cmp_eq_u32_e64 vcc, 0, v64\n\t"
      "v_smfmac_f32_16x16x64_bf16 v[44:47], v[8:11], a[208:215], v122\n\t"
      "s_waitcnt lgkmcnt(1)\n\t"
      "v_smfmac_f32_16x16x64_bf16 v[32:35], v[12:15], a[24:31], v122\n\t"
      "s_cbranch_vccnz LR1_1%=\n\t"
      "LC1_1%=:\n\t"
      "v_smfmac_f32_16x16x64_bf16 v[40:43], v[20:23], a[168:175], v122\n\t"
      "s_barrier\n\t"
      "v_mfma_f32_16x16x32_bf16 v[56:59], v[20:23], v[20:23], v[56:59]\n\t"
      "ds_read_b128 v[16:19], v127 offset:45056\n\t"
      "s_nop 0\n\t"
      "v_smfmac_f32_16x16x64_bf16 v[32:35], v[8:11], a[16:23], v122\n\t"
      "ds_read_b128 v[28:31], v127 offset:48128\n\t"
      "v_smfmac_f32_16x16x64_bf16 v[44:47], v[20:23], a[232:239], v122\n\t"
      "ds_read_b128 v[24:27], v127 offset:47104\n\t"
      "v_max3_f32 v56, v56, v57, v58\n\t"
      "v_smfmac_f32_16x16x64_bf16 v[36:39], v[20:23], a[104:111], v122\n\t"
      "ds_read_b128 v[76:79], v127 offset:32768\n\t"
      "v_max_f32_e64 v56, v56, v59\n\t"
      "v_smfmac_f32_16x16x64_bf16 v[32:35], v[20:23], a[40:47], v122\n\t"
      "s_waitcnt lgkmcnt(3)\n\t"
      "v_readfirstlane_b32 s56, v120\n\t"
      "v_smfmac_f32_16x16x64_bf16 v[32:35], v[16:19], a[32:39], v122\n\t"
      "s_waitcnt lgkmcnt(2)\n\t"
      "v_max_f32_dpp v56, v56, v56 quad_perm:[1,0,3,2] row_mask:0xf bank_mask:0xf\n\t"
      "v_smfmac_f32_16x16x64_bf16 v[32:35], v[28:31], a[56:63], v122\n\t"
      "s_waitcnt lgkmcnt(1)\n\t"
      "v_max_f32_dpp v56, v56, v56 quad_perm:[2,3,0,1] row_mask:0xf bank_mask:0xf\n\t"
      "v_smfmac_f32_16x16x64_bf16 v[32:35], v[24:27], a[48:55], v122\n\t"
      "s_mov_b32 exec_lo, 0x100001\n\t"
      "s_mov_b32 exec_hi, 0x10000100\n\t"
      "v_smfmac_f32_16x16x64_bf16 v[36:39], v[16:19], a[96:103], v122\n\t"
      "ds_max_i32 v109, v56 offset:55392\n\t"
      "v_smfmac_f32_16x16x64_bf16 v[40:43], v[16:19], a[160:167], v122\n\t"
      "s_mov_b64 exec, -1\n\t"
      "v_add_f32 v33, v33, v35\n\t"
      "v_smfmac_f32_16x16x64_bf16 v[36:39], v[28:31], a[120:127], v122\n\t"
      "ds_read_b128 v[60:63], v127 offset:37888\n\t"
      "v_mul_f32_e64 v33, s59, v33\n\t"
      "v_smfmac_f32_16x16x64_bf16 v[36:39], v[24:27], a[112:119], v122\n\t"
      "v_add_f32_e64 v32, v32, v34\n\t"
      "v_smfmac_f32_16x16x64_bf16 v[44:47], v[16:19], a[224:231], v122\n\t"
      "ds_read_b128 v[56:59], v127 offset:38912\n\t"
      "v_mul_f32_e64 v32, s59, v32\n\t"
      "v_smfmac_f32_16x16x64_bf16 v[44:47], v[28:31], a[248:255], v122\n\t"
      "v_add_f32 v36, v36, v38\n\t"
      "v_add_f32 v37, v37, v39\n\t"
      "v_mul_f32_e64 v37, s59, v37\n\t"
      "v_smfmac_f32_16x16x64_bf16 v[40:43], v[28:31], a[184:191], v122\n\t"
      "ds_read_b128 v[92:95], v127 offset:34816\n\t"
      "v_cvt_pk_bf16_f32 v112, v32, v33\n\t"
      "v_smfmac_f32_16x16x64_bf16 v[44:47], v[24:27], a[240:247], v122\n\t"
      "ds_read_b128 v[96:99], v127 offset:33792\n\t"
      "v_mul_f32_e64 v36, s59, v36\n\t"
      "v_cvt_pk_bf16_f32 v113, v36, v37\n\t"
      "v_smfmac_f32_16x16x64_bf16 v[40:43], v[24:27], a[176:183], v122\n\t"
      "ds_write_b64 v125, v[112:113]\n\t"
      "v_smfmac_f32_16x16x64_bf16 v[48:51], v[0:3], v[128:135], v122\n\t"
      "s_nop 0\n\t"
      "v_add_f32 v45, v45, v47\n\t"
      "v_add_f32_e64 v44, v44, v46\n\t"
      "v_cvt_pk_bf16_f32 v121, v44, v45\n\t"
      "v_add_f32 v41, v41, v43\n\t"
      "v_add_f32 v40, v40, v42\n\t"
      "v_cvt_pk_bf16_f32 v120, v40, v41\n\t"
      "v_smfmac_f32_16x16x64_bf16 v[48:51], v[12:15], v[152:159], v122\n\t"
      "s_mov_b32 s58, 0x10000000\n\t"
      "ds_write_b64 v125, v[120:121] offset:2080\n\t"
      "v_smfmac_f32_16x16x64_bf16 v[48:51], v[8:11], v[144:151], v122\n\t"
      "s_waitcnt lgkmcnt(1)\n\t"
      "s_barrier\n\t"
      "ds_read_b64_tr_b16 v[36:37], v124 offset:50192\n\t"
      "v_smfmac_f32_16x16x64_bf16 v[48:51], v[20:23], v[168:175], v122\n\t"
      "ds_read_b32 v108, v109 offset:55392\n\t"
      "ds_read_b64_tr_b16 v[32:33], v124 offset:49152\n\t"
      "v_smfmac_f32_16x16x64_bf16 v[52:55], v[0:3], v[192:199], v122\n\t"
      "ds_read_b64_tr_b16 v[34:35], v124 offset:49672\n\t"
      "s_waitcnt vmcnt(0) lgkmcnt(4)\n\t"
      "ds_read_b64_tr_b16 v[38:39], v124 offset:50712\n\t"
      "v_smfmac_f32_16x16x64_bf16 v[48:51], v[16:19], v[160:167], v122\n\t"
      "s_barrier\n\t"
      "ds_read_b64_tr_b16 v[46:47], v124 offset:52792\n\t"
      "v_smfmac_f32_16x16x64_bf16 v[48:51], v[28:31], v[184:191], v122\n\t"
      "s_waitcnt lgkmcnt(4)\n\t"
      "ds_read_b64_tr_b16 v[44:45], v124 offset:52272\n\t"
      "v_readfirstlane_b32 s61, v108\n\t"
      "v_smfmac_f32_16x16x64_bf16 v[48:51], v[24:27], v[176:183], v122\n\t"
      "s_waitcnt lgkmcnt(3)\n\t"
      "ds_read_b64_tr_b16 v[42:43], v124 offset:51752\n\t"
      "ds_read_b64_tr_b16 v[40:41], v124 offset:51232\n\t"
      "s_waitcnt lgkmcnt(4)\n\t"
      "v_mfma_f32_16x16x32_bf16 v[112:115], v[76:79], v[32:35], 0\n\t"
      "s_lshr_b32 s61, s61, 23\n\t"
      "s_add_u32 s61, s61, s67\n\t"
      "v_mov_b32_dpp v102, v34 quad_perm:[2,3,0,1] row_mask:0xf bank_mask:0xf\n\t"
      "v_mov_b32_dpp v106, v38 quad_perm:[2,3,0,1] row_mask:0xf bank_mask:0xf\n\t"
      "s_max_u32 s61, s61, s66\n\t"
      "v_mfma_f32_16x16x32_bf16 v[116:119], v[80:83], v[32:35], 0\n\t"
      "s_lshr_b32 s61, s61, 1\n\t"
      "v_add_f32_e64 v48, v48, v50\n\t"
      "v_mov_b32_dpp v100, v32 quad_perm:[2,3,0,1] row_mask:0xf bank_mask:0xf\n\t"
      "v_mfma_f32_16x16x32_bf16 v[116:119], v[88:91], v[36:39], v[116:119]\n\t"
      "s_min_u32 s61, s61, 200\n\t"
      "v_mov_b32_dpp v107, v39 quad_perm:[2,3,0,1] row_mask:0xf bank_mask:0xf\n\t"
      "s_waitcnt lgkmcnt(0)\n\t"
      "v_mfma_f32_16x16x32_bf16 v[76:79], v[40:43], v[32:35], 0\n\t"
      "buffer_load_dwordx4 v127, s[36:39], s46 offen sc1 lds\n\t"
      "s_lshl_b32 s61, s61, 23\n\t"
      "v_mfma_f32_16x16x32_bf16 v[76:79], v[44:47], v[36:39], v[76:79]\n\t"
      "s_add_u32 s65, s61, 33554432\n\t"
      "v_mov_b32_dpp v104, v36 quad_perm:[2,3,0,1] row_mask:0xf bank_mask:0xf\n\t"
      "v_mov_b32_dpp v103, v35 quad_perm:[2,3,0,1] row_mask:0xf bank_mask:0xf\n\t"
      "v_mov_b32_dpp v101, v33 quad_perm:[2,3,0,1] row_mask:0xf bank_mask:0xf\n\t"
      "v_mfma_f32_16x16x32_bf16 v[112:115], v[84:87], v[36:39], v[112:115]\n\t"
      "s_pack_hh_b32_b16 s75, s65, s65\n\t"
      "v_add_f32 v49, v49, v51\n\t"
      "v_cvt_pk_bf16_f32 v64, v48, v49\n\t"
      "v_cndmask_b32_e64 v120, v76, v78, s[72:73]\n\t"
      "v_cndmask_b32_e64 v121, v77, v79, s[72:73]\n\t"
      "v_mov_b32_dpp v105, v37 quad_perm:[2,3,0,1] row_mask:0xf bank_mask:0xf\n\t"
      "v_mfma_f32_16x16x32_bf16 v[112:115], v[92:95], v[100:103], v[112:115]\n\t"
      "buffer_load_dwordx4 v127, s[36:39], s46 offen offset:1024 sc1 lds\n\t"
      "v_mfma_f32_16x16x32_bf16 v[116:119], v[96:99], v[100:103], v[116:119]\n\t"
      "s_pack_hh_b32_b16 s74, s65, s65\n\t"
      "v_add_f32_dpp v121, v121, v121 quad_perm:[2,3,0,1] row_mask:0xf bank_mask:0xf\n\t"
      "v_add_f32_dpp v120, v120, v120 quad_perm:[2,3,0,1] row_mask:0xf bank_mask:0xf\n\t"
      "v_mfma_f32_16x16x32_bf16 v[112:115], v[56:59], v[104:107], v[112:115]\n\t"
      "v_exp_f32 v81, v121\n\t"
      "v_mfma_f32_16x16x32_bf16 v[116:119], v[60:63], v[104:107], v[116:119]\n\t"
      "s_mov_b32 m0, s45\n\t"
      "v_mov_b64 v[90:91], s[74:75]\n\t"
      "v_exp_f32 v80, v120\n\t"
      "v_mov_b64 v[88:89], s[74:75]\n\t"
      "v_cvt_pk_bf16_f32 v108, v80, v81\n\t"
      "v_smfmac_f32_16x16x64_bf16 v[52:55], v[12:15], v[216:223], v122\n\t"
      "buffer_load_dwordx4 v65, s[36:39], s44 offen offset:1024 sc1 lds\n\t"
      "v_mfma_f32_16x16x32_bf16 v[84:87], v[108:111], v[64:67], 0\n\t"
      "s_add_u32 s57, s61, 310378496\n\t"
      "v_cndmask_b32_e64 v112, v112, v116, s[72:73]\n\t"
      "v_exp_f32_e64 v56, v112\n\t"
      "v_mfma_f32_16x16x32_bf16 v[48:51], v[108:111], v[88:91], 0\n\t"
      "s_add_u32 s43, s43, 0x8000\n\t"
      "v_cndmask_b32_e64 v113, v113, v117, s[72:73]\n\t"
      "v_cndmask_b32_e64 v114, v114, v118, s[72:73]\n\t"
      "v_exp_f32_e64 v58, v114\n\t"
      "v_cndmask_b32_e64 v115, v115, v119, s[72:73]\n\t"
      "v_exp_f32 v57, v113\n\t"
      "v_exp_f32 v59, v115\n\t"
      "v_cvt_pk_bf16_f32 v96, v56, v57\n\t"
      "v_cvt_pk_bf16_f32 v97, v58, v59\n\t"
      "v_max3_f32 v82, |v112|, |v113|, |v114|\n\t"
      "v_mov_b32_dpp v98, v96 quad_perm:[2,3,0,1] row_mask:0xf bank_mask:0xf\n\t"
      "v_mov_b32_dpp v99, v97 quad_perm:[2,3,0,1] row_mask:0xf bank_mask:0xf\n\t"
      "v_smfmac_f32_16x16x64_bf16 v[52:55], v[8:11], v[208:215], v122\n\t"
      "v_max3_f32 v83, |v115|, |v120|, |v121|\n\t"
      "v_mfma_f32_16x16x32_bf16 v[48:51], v[96:99], v[88:91], v[48:51]\n\t"
      "v_max_f32_e64 v82, v82, v83\n\t"
      "v_cmp_nge_f32 vcc, 0x42000000, v82\n\t"
      "v_mfma_f32_16x16x32_bf16 v[84:87], v[96:99], v[68:71], v[84:87]\n\t"
      "buffer_load_dwordx4 v65, s[36:39], s44 offen sc1 lds\n\t"
      "v_smfmac_f32_16x16x64_bf16 v[52:55], v[20:23], v[232:239], v122\n\t"
      "s_add_u32 s68, s68, 0x8000\n\t"
      "s_cbranch_vccnz LMX_1%=\n\t"
      "v_rcp_f32 v49, v49\n\t"
      "v_rcp_f32_e64 v48, v48\n\t"
      "v_smfmac_f32_16x16x64_bf16 v[52:55], v[16:19], v[224:231], v122\n\t"
      "v_fma_f32 v85, v85, v49, s96\n\t"
      "v_fma_f32 v84, v84, v48, s96\n\t"
      "v_smfmac_f32_16x16x64_bf16 v[52:55], v[28:31], v[248:255], v122\n\t"
      "buffer_atomic_add v84, v123, s[36:39], s97 offen\n\t"
      "buffer_atomic_add v85, v123, s[36:39], s98 offen\n\t"
      "v_smfmac_f32_16x16x64_bf16 v[52:55], v[24:27], v[240:247], v122\n\t"
      "v_mfma_f32_16x16x32_bf16 v[92:95], v[96:99], v[72:75], 0\n\t"
      "s_nop 3\n\t"
      "v_add_f32 v53, v53, v55\n\t"
      "v_add_f32_e64 v52, v52, v54\n\t"
      "v_cvt_pk_bf16_f32 v66, v52, v53\n\t"
      "s_add_u32 s62, s61, 0x83400000\n\t"
      "v_mov_b32_e64 v126, s62\n\t"
      "v_mfma_f32_16x16x32_bf16 v[92:95], v[108:111], v[66:69], v[92:95]\n\t"
      "s_add_u32 s97, s97, 0x8000\n\t"
      "s_nop 6\n\t"
      "v_fma_f32 v95, v95, v49, s96\n\t"
      "buffer_atomic_add v95, v123, s[36:39], s23 offen\n\t"
      "v_fma_f32 v94, v94, v48, s96\n\t"
      "buffer_atomic_add v94, v123, s[36:39], s69 offen\n\t"
      "LWIN_1%=:\n\t"
      "v_add_u32 v100, s55, v127\n\t"
      "ds_read_b128 v[60:63], v100 offset:57344\n\t"
      "s_cmp_eq_u32 s56, 0\n\t"
      "s_cbranch_scc0 LSLOW_1%=\n\t"
      "LWIN1_1%=:\n\t"
      "buffer_load_dwordx2 v[120:121], off, s[36:39], s50 sc1\n\t"
      "s_mov_b32 s64, s50\n\t"
      "s_bitcmp1_b64 s[84:85], s40\n\t"
      "s_cbranch_scc1 LEARLY_1%=\n\t"
      "LROLE_1%=:\n\t"
      "v_add_u32 v52, 67584, v127\n\t"
      "ds_read_b128 v[88:91], v52\n\t"
      "ds_read_b128 v[92:95], v52 offset:1024\n\t"
      "ds_read_b128 v[32:35], v52 offset:4096\n\t"
      "ds_read_b128 v[36:39], v52 offset:5120\n\t"
      "ds_read_b128 v[40:43], v52 offset:8192\n\t"
      "ds_read_b128 v[44:47], v52 offset:9216\n\t"
      "v_mov_b64 v[48:49], 0\n\t"
      "v_mov_b64 v[50:51], 0\n\t"
      "v_add_u32 v104, s71, v100\n\t"
      "ds_read_b128 v[76:79], v104\n\t"
      "ds_read_b128 v[80:83], v104 offset:1024\n\t"
      "ds_read_b128 v[112:115], v100\n\t"
      "ds_read_b128 v[116:119], v100 offset:1024\n\t"
      "v_xor_b32 v101, 2048, v104\n\t"
      "v_xor_b32 v102, 4096, v104\n\t"
      "v_xor_b32 v103, 6144, v104\n\t"
      "s_waitcnt lgkmcnt(8)\n\t"
      "v_smfmac_f32_16x16x64_bf16 v[48:51], v[0:3], v[88:95], v122\n\t"
      "ds_read_b128 v[88:91], v52 offset:12288\n\t"
      "ds_read_b128 v[92:95], v52 offset:13312\n\t"
      "s_add_u32 m0, s55, s71\n\t"
      "s_sub_u32 s71, 204800, s71\n\t"
      "s_waitcnt lgkmcnt(8)\n\t"
      "v_smfmac_f32_16x16x64_bf16 v[48:51], v[4:7], v[32:39], v122\n\t"
      "ds_read_b128 v[16:19], v101\n\t"
      "ds_read_b128 v[20:23], v101 offset:1024\n\t"
      "s_add_u32 s40, s40, 1\n\t"
      "s_cmp_lt_u32 s40, 63\n\t"
      "s_waitcnt lgkmcnt(8)\n\t"
      "v_smfmac_f32_16x16x64_bf16 v[48:51], v[8:11], v[40:47], v122\n\t"
      "ds_read_b128 v[24:27], v100 offset:8192\n\t"
      "ds_read_b128 v[28:31], v100 offset:9216\n\t"
      "s_cselect_b32 s61, 786432, 0\n\t"
      "s_cselect_b32 s60, s58, 0\n\t"
      "s_waitcnt lgkmcnt(4)\n\t"
      "v_smfmac_f32_16x16x64_bf16 v[48:51], v[12:15], v[88:95], v122\n\t"
      "s_add_u32 s44, s44, s61\n\t"
      "s_cmp_lt_u32 s40, 62\n\t"
      "ds_read_b128 v[0:3], v102\n\t"
      "ds_read_b128 v[4:7], v102 offset:1024\n\t"
      "ds_read_b128 v[8:11], v100 offset:16384\n\t"
      "ds_read_b128 v[12:15], v100 offset:17408\n\t"
      "s_cselect_b32 s61, 786432, 0\n\t"
      "s_add_u32 s46, s46, s61\n\t"
      "s_add_u32 s61, s40, 3\n\t"
      "s_and_b32 s62, s61, 3\n\t"
      "s_cselect_b32 s62, 16, 2000\n\t"
      "s_cmp_lt_u32 s61, 64\n\t"
      "s_cselect_b32 s62, s62, 0\n\t"
      "s_add_u32 s50, s50, s62\n\t"
      "s_mov_b32 s28, s49\n\t"
      "s_add_u32 s49, s49, 65536\n\t"
      "s_cmp_eq_u32 s40, 64\n\t"
      "v_add_f32 v48, v48, v50\n\t"
      "v_add_f32 v49, v49, v51\n\t"
      "s_cbranch_scc1 LFIN_1%=\n\t"
      "v_mfma_f32_16x16x32_bf16 v[32:35], v[76:79], v[112:115], 0\n\t"
      "s_mov_b32 s63, 0x2000\n\t"
      "v_mfma_f32_16x16x32_bf16 v[36:39], v[80:83], v[112:115], 0\n\t"
      "v_mfma_f32_16x16x32_bf16 v[40:43], v[76:79], v[116:119], 0\n\t"
      "v_mfma_f32_16x16x32_bf16 v[44:47], v[80:83], v[116:119], 0\n\t"
      "ds_read_b128 v[76:79], v103\n\t"
      "ds_read_b128 v[80:83], v103 offset:1024\n\t"
      "ds_read_b128 v[112:115], v100 offset:24576\n\t"
      "ds_read_b128 v[116:119], v100 offset:25600\n\t"
      "s_waitcnt lgkmcnt(8)\n\t"
      "v_mfma_f32_16x16x32_bf16 v[32:35], v[16:19], v[24:27], v[32:35]\n\t"
      "v_mfma_f32_16x16x32_bf16 v[36:39], v[20:23], v[24:27], v[36:39]\n\t"
      "v_mfma_f32_16x16x32_bf16 v[40:43], v[16:19], v[28:31], v[40:43]\n\t"
      "v_mfma_f32_16x16x32_bf16 v[44:47], v[20:23], v[28:31], v[44:47]\n\t"
      "LGATE_1%=:\n\t"
      "LBAR_1%=:\n\t"
      "s_cmp_gt_u32 s40, 1\n\t"
      "s_barrier\n\t"
      "s_cbranch_scc1 LPOLL_1%=\n\t"
      "s_branch LFIRST_1%=\n\t"
      ".p2align 6\n\t.fill 16, 4, 0xbf800000\n\t"
      "LPOLL_2%=:\n\t"
      "buffer_load_dwordx4 v[84:87], v60, s[36:39], s43 offen sc1\n\t"
      "buffer_load_dwordx4 v[88:91], v60, s[36:39], s43 offen offset:1024 sc1\n\t"
      "s_waitcnt lgkmcnt(8)\n\t"
      "v_mfma_f32_16x16x32_bf16 v[32:35], v[0:3], v[8:11], v[32:35]\n\t"
      "v_mfma_f32_16x16x32_bf16 v[36:39], v[4:7], v[8:11], v[36:39]\n\t"
      "v_mfma_f32_16x16x32_bf16 v[40:43], v[0:3], v[12:15], v[40:43]\n\t"
      "v_mfma_f32_16x16x32_bf16 v[44:47], v[4:7], v[12:15], v[44:47]\n\t"
      "buffer_load_dwordx4 v[92:95], v60, s[36:39], s68 offen sc1\n\t"
      "buffer_load_dwordx4 v[96:99], v60, s[36:39], s68 offen offset:1024 sc1\n\t"
      "v_mov_b64 v[48:49], 0\n\t"
      "v_mov_b64 v[50:51], 0\n\t"
      "v_mov_b64 v[52:53], 0\n\t"
      "v_mov_b64 v[54:55], 0\n\t"
      "s_xor_b32 s69, s97, 1024\n\t"
      "s_xor_b32 s98, s97, 512\n\t"
      "s_xor_b32 s23, s97, 1536\n\t"
      "s_waitcnt lgkmcnt(4)\n\t"
      "v_mfma_f32_16x16x32_bf16 v[32:35], v[16:19], v[24:27], v[32:35]\n\t"
      "v_mfma_f32_16x16x32_bf16 v[36:39], v[20:23], v[24:27], v[36:39]\n\t"
      "v_mfma_f32_16x16x32_bf16 v[40:43], v[16:19], v[28:31], v[40:43]\n\t"
      "v_mfma_f32_16x16x32_bf16 v[44:47], v[20:23], v[28:31], v[44:47]\n\t"
      "s_waitcnt lgkmcnt(0)\n\t"
      "v_mfma_f32_16x16x32_bf16 v[32:35], v[76:79], v[112:115], v[32:35]\n\t"
      "v_mfma_f32_16x16x32_bf16 v[36:39], v[80:83], v[112:115], v[36:39]\n\t"
      "v_mfma_f32_16x16x32_bf16 v[40:43], v[76:79], v[116:119], v[40:43]\n\t"
      "v_mfma_f32_16x16x32_bf16 v[44:47], v[80:83], v[116:119], v[44:47]\n\t"
      "s_mov_b64 exec, 1\n\t"
      "ds_write_b64 v109, v[110:111] offset:55392\n\t"
      "s_mov_b64 exec, -1\n\t"
      "s_nop 4\n\t"
      "v_cvt_pk_bf16_f32 v68, v32, v33\n\t"
      "v_cvt_pk_bf16_f32 v69, v34, v35\n\t"
      "v_cvt_pk_bf16_f32 v70, v36, v37\n\t"
      "v_cvt_pk_bf16_f32 v71, v38, v39\n\t"
      "v_cvt_pk_bf16_f32 v74, v40, v41\n\t"
      "v_cvt_pk_bf16_f32 v75, v42, v43\n\t"
      "v_cvt_pk_bf16_f32 v72, v44, v45\n\t"
      "v_cvt_pk_bf16_f32 v73, v46, v47\n\t"
      "v_mov_b64 v[32:33], 0\n\t"
      "v_mov_b64 v[34:35], 0\n\t"
      "v_mov_b64 v[36:37], 0\n\t"
      "v_mov_b64 v[38:39], 0\n\t"
      "v_mov_b64 v[40:41], 0\n\t"
      "v_mov_b64 v[42:43], 0\n\t"
      "v_mov_b64 v[44:45], 0\n\t"
      "v_mov_b64 v[46:47], 0\n\t"
      "LGO_2%=:\n\t"
      "s_nop 0\n\t"
      "s_nop 0\n\t"
      "s_nop 0\n\t"
      "s_nop 0\n\t"
      "s_nop 0\n\t"
      "s_waitcnt vmcnt(3)\n\t"
      "v_pk_fma_f32 v[14:15], v[86:87], s[56:57], v[126:127] op_sel:[0,1,0] op_sel_hi:[1,1,0]\n\t"
      "v_pk_fma_f32 v[12:13], v[84:85], s[56:57], v[126:127] op_sel:[0,1,0] op_sel_hi:[1,1,0]\n\t"
      "v_cvt_pk_bf16_f32 v8, v12, v13\n\t"
      "v_cvt_pk_bf16_f32 v9, v14, v15\n\t"
      "s_nop 0\n\t"
      "ds_write_b64 v127, v[8:9] offset:43008\n\t"
      "s_waitcnt vmcnt(2)\n\t"
      "v_pk_fma_f32 v[0:1], v[88:89], s[56:57], v[126:127] op_sel:[0,1,0] op_sel_hi:[1,1,0]\n\t"
      "v_pk_fma_f32 v[2:3], v[90:91], s[56:57], v[126:127] op_sel:[0,1,0] op_sel_hi:[1,1,0]\n\t"
      "v_cvt_pk_bf16_f32 v10, v0, v1\n\t"
      "v_cvt_pk_bf16_f32 v11, v2, v3\n\t"
      "ds_write_b64 v127, v[10:11] offset:43016\n\t"
      "v_bitop3_b32 v64, v84, v85, v86 bitop3:0x80\n\t"
      "v_mfma_f32_16x16x32_bf16 v[56:59], v[8:11], v[8:11], 0\n\t"
      "v_bitop3_b32 v64, v64, v87, v88 bitop3:0x80\n\t"
      "v_bitop3_b32 v64, v64, v89, v90 bitop3:0x80\n\t"
      "v_bitop3_b32 v64, v64, v91, s58 bitop3:0x80\n\t"
      "v_cmp_eq_u32_e64 vcc, 0, v64\n\t"
      "v_smfmac_f32_16x16x64_bf16 v[36:39], v[8:11], a[80:87], v122\n\t"
      "s_nop 0\n\t"
      "v_bitop3_b32 v120, v120, v121, s53 bitop3:0x7e\n\t"
      "v_smfmac_f32_16x16x64_bf16 v[44:47], v[8:11], a[208:215], v122\n\t"
      "s_cbranch_vccnz LR0_2%=\n\t"
      "LC0_2%=:\n\t"
      "s_waitcnt lgkmcnt(0)\n\t"
      "s_barrier\n\t"
      "ds_read_b128 v[12:15], v127 offset:44032\n\t"
      "v_smfmac_f32_16x16x64_bf16 v[52:55], v[8:11], v[208:215], v122\n\t"
      "ds_read_b128 v[0:3], v127 offset:40960\n\t"
      "v_smfmac_f32_16x16x64_bf16 v[48:51], v[8:11], v[144:151], v122\n\t"
      "ds_read_b128 v[4:7], v127 offset:41984\n\t"
      "v_smfmac_f32_16x16x64_bf16 v[32:35], v[8:11], a[16:23], v122\n\t"
      "ds_read_b128 v[84:87], v127 offset:36864\n\t"
      "v_smfmac_f32_16x16x64_bf16 v[40:43], v[8:11], a[144:151], v122\n\t"
      "s_waitcnt lgkmcnt(3)\n\t"
      "v_smfmac_f32_16x16x64_bf16 v[32:35], v[12:15], a[24:31], v122\n\t"
      "ds_read_b128 v[80:83], v127 offset:35840\n\t"
      "v_smfmac_f32_16x16x64_bf16 v[40:43], v[12:15], a[152:159], v122\n\t"
      "s_waitcnt vmcnt(1)\n\t"
      "v_fma_f32 v31, v95, s57, v126\n\t"
      "v_fma_f32 v28, v92, s57, v126\n\t"
      "v_smfmac_f32_16x16x64_bf16 v[36:39], v[12:15], a[88:95], v122\n\t"
      "v_fma_f32 v30, v94, s57, v126\n\t"
      "v_cvt_pk_bf16_f32 v25, v30, v31\n\t"
      "v_bitop3_b32 v64, v92, v93, v94 bitop3:0x80\n\t"
      "v_smfmac_f32_16x16x64_bf16 v[44:47], v[12:15], a[216:223], v122\n\t"
      "s_waitcnt lgkmcnt(2)\n\t"
      "v_fma_f32 v29, v93, s57, v126\n\t"
      "v_cvt_pk_bf16_f32 v24, v28, v29\n\t"
      "v_smfmac_f32_16x16x64_bf16 v[36:39], v[0:3], a[64:71], v122\n\t"
      "s_waitcnt vmcnt(0)\n\t"
      "ds_write_b64 v127, v[24:25] offset:47104\n\t"
      "v_fma_f32 v19, v99, s57, v126\n\t"
      "v_fma_f32 v18, v98, s57, v126\n\t"
      "v_cvt_pk_bf16_f32 v27, v18, v19\n\t"
      "v_fma_f32 v17, v97, s57, v126\n\t"
      "v_fma_f32 v16, v96, s57, v126\n\t"
      "v_cvt_pk_bf16_f32 v26, v16, v17\n\t"
      "v_smfmac_f32_16x16x64_bf16 v[44:47], v[0:3], a[192:199], v122\n\t"
      "ds_write_b64 v127, v[26:27] offset:47112\n\t"
      "v_smfmac_f32_16x16x64_bf16 v[40:43], v[0:3], a[128:135], v122\n\t"
      "ds_read_b128 v[88:91], v127 offset:39936\n\t"
      "v_bitop3_b32 v64, v64, v95, v96 bitop3:0x80\n\t"
      "v_bitop3_b32 v64, v64, v97, v98 bitop3:0x80\n\t"
      "v_smfmac_f32_16x16x64_bf16 v[40:43], v[4:7], a[136:143], v122\n\t"
      "v_bitop3_b32 v64, v64, v99, s60 bitop3:0x80\n\t"
      "v_smfmac_f32_16x16x64_bf16 v[36:39], v[4:7], a[72:79], v122\n\t"
      "v_cmp_eq_u32_e64 vcc, 0, v64\n\t"
      "v_smfmac_f32_16x16x64_bf16 v[44:47], v[4:7], a[200:207], v122\n\t"
      "s_waitcnt lgkmcnt(1)\n\t"
      "v_smfmac_f32_16x16x64_bf16 v[32:35], v[0:3], a[0:7], v122\n\t"
      "s_cbranch_vccnz LR1_2%=\n\t"
      "LC1_2%=:\n\t"
      "v_smfmac_f32_16x16x64_bf16 v[40:43], v[24:27], a[176:183], v122\n\t"
      "s_barrier\n\t"
      "v_mfma_f32_16x16x32_bf16 v[56:59], v[24:27], v[24:27], v[56:59]\n\t"
      "ds_read_b128 v[28:31], v127 offset:48128\n\t"
      "s_nop 0\n\t"
      "v_smfmac_f32_16x16x64_bf16 v[32:35], v[4:7], a[8:15], v122\n\t"
      "ds_read_b128 v[16:19], v127 offset:45056\n\t"
      "v_smfmac_f32_16x16x64_bf16 v[44:47], v[24:27], a[240:247], v122\n\t"
      "ds_read_b128 v[20:23], v127 offset:46080\n\t"
      "v_max3_f32 v56, v56, v57, v58\n\t"
      "v_smfmac_f32_16x16x64_bf16 v[36:39], v[24:27], a[112:119], v122\n\t"
      "ds_read_b128 v[76:79], v127 offset:32768\n\t"
      "v_max_f32_e64 v56, v56, v59\n\t"
      "v_smfmac_f32_16x16x64_bf16 v[32:35], v[24:27], a[48:55], v122\n\t"
      "s_waitcnt lgkmcnt(3)\n\t"
      "v_readfirstlane_b32 s56, v120\n\t"
      "v_smfmac_f32_16x16x64_bf16 v[32:35], v[28:31], a[56:63], v122\n\t"
      "s_waitcnt lgkmcnt(2)\n\t"
      "v_max_f32_dpp v56, v56, v56 quad_perm:[1,0,3,2] row_mask:0xf bank_mask:0xf\n\t"
      "v_smfmac_f32_16x16x64_bf16 v[32:35], v[16:19], a[32:39], v122\n\t"
      "s_waitcnt lgkmcnt(1)\n\t"
      "v_max_f32_dpp v56, v56, v56 quad_perm:[2,3,0,1] row_mask:0xf bank_mask:0xf\n\t"
      "v_smfmac_f32_16x16x64_bf16 v[32:35], v[20:23], a[40:47], v122\n\t"
      "s_mov_b32 exec_lo, 0x100001\n\t"
      "s_mov_b32 exec_hi, 0x10000100\n\t"
      "v_smfmac_f32_16x16x64_bf16 v[36:39], v[28:31], a[120:127], v122\n\t"
      "ds_max_i32 v109, v56 offset:55392\n\t"
      "v_smfmac_f32_16x16x64_bf16 v[40:43], v[28:31], a[184:191], v122\n\t"
      "s_mov_b64 exec, -1\n\t"
      "v_add_f32 v33, v33, v35\n\t"
      "v_smfmac_f32_16x16x64_bf16 v[36:39], v[16:19], a[96:103], v122\n\t"
      "ds_read_b128 v[60:63], v127 offset:37888\n\t"
      "v_mul_f32_e64 v33, s59, v33\n\t"
      "v_smfmac_f32_16x16x64_bf16 v[36:39], v[20:23], a[104:111], v122\n\t"
      "v_add_f32_e64 v32, v32, v34\n\t"
      "v_smfmac_f32_16x16x64_bf16 v[44:47], v[28:31], a[248:255], v122\n\t"
      "ds_read_b128 v[56:59], v127 offset:38912\n\t"
      "v_mul_f32_e64 v32, s59, v32\n\t"
      "v_smfmac_f32_16x16x64_bf16 v[44:47], v[16:19], a[224:231], v122\n\t"
      "v_add_f32 v36, v36, v38\n\t"
      "v_add_f32 v37, v37, v39\n\t"
      "v_mul_f32_e64 v37, s59, v37\n\t"
      "v_smfmac_f32_16x16x64_bf16 v[40:43], v[16:19], a[160:167], v122\n\t"
      "ds_read_b128 v[92:95], v127 offset:34816\n\t"
      "v_cvt_pk_bf16_f32 v112, v32, v33\n\t"
      "v_smfmac_f32_16x16x64_bf16 v[44:47], v[20:23], a[232:239], v122\n\t"
      "ds_read_b128 v[96:99], v127 offset:33792\n\t"
      "v_mul_f32_e64 v36, s59, v36\n\t"
      "v_cvt_pk_bf16_f32 v113, v36, v37\n\t"
      "v_smfmac_f32_16x16x64_bf16 v[40:43], v[20:23], a[168:175], v122\n\t"
      "ds_write_b64 v125, v[112:113]\n\t"
      "v_smfmac_f32_16x16x64_bf16 v[48:51], v[12:15], v[152:159], v122\n\t"
      "s_nop 0\n\t"
      "v_add_f32 v45, v45, v47\n\t"
      "v_add_f32_e64 v44, v44, v46\n\t"
      "v_cvt_pk_bf16_f32 v121, v44, v45\n\t"
      "v_add_f32 v41, v41, v43\n\t"
      "v_add_f32 v40, v40, v42\n\t"
      "v_cvt_pk_bf16_f32 v120, v40, v41\n\t"
      "v_smfmac_f32_16x16x64_bf16 v[48:51], v[0:3], v[128:135], v122\n\t"
      "s_mov_b32 s58, 0x10000000\n\t"
      "ds_write_b64 v125, v[120:121] offset:2080\n\t"
      "v_smfmac_f32_16x16x64_bf16 v[48:51], v[4:7], v[136:143], v122\n\t"
      "s_waitcnt lgkmcnt(1)\n\t"
      "s_barrier\n\t"
      "ds_read_b64_tr_b16 v[36:37], v124 offset:50192\n\t"
      "v_smfmac_f32_16x16x64_bf16 v[48:51], v[24:27], v[176:183], v122\n\t"
      "ds_read_b32 v108, v109 offset:55392\n\t"
      "ds_read_b64_tr_b16 v[32:33], v124 offset:49152\n\t"
      "v_smfmac_f32_16x16x64_bf16 v[52:55], v[12:15], v[216:223], v122\n\t"
      "ds_read_b64_tr_b16 v[34:35], v124 offset:49672\n\t"
      "s_waitcnt lgkmcnt(4)\n\t"
      "ds_read_b64_tr_b16 v[38:39], v124 offset:50712\n\t"
      "v_smfmac_f32_16x16x64_bf16 v[48:51], v[28:31], v[184:191], v122\n\t"
      "s_barrier\n\t"
      "ds_read_b64_tr_b16 v[46:47], v124 offset:52792\n\t"
      "v_smfmac_f32_16x16x64_bf16 v[48:51], v[16:19], v[160:167], v122\n\t"
      "s_waitcnt lgkmcnt(4)\n\t"
      "ds_read_b64_tr_b16 v[44:45], v124 offset:52272\n\t"
      "v_readfirstlane_b32 s61, v108\n\t"
      "v_smfmac_f32_16x16x64_bf16 v[48:51], v[20:23], v[168:175], v122\n\t"
      "s_waitcnt lgkmcnt(3)\n\t"
      "ds_read_b64_tr_b16 v[42:43], v124 offset:51752\n\t"
      "ds_read_b64_tr_b16 v[40:41], v124 offset:51232\n\t"
      "s_waitcnt lgkmcnt(4)\n\t"
      "v_mfma_f32_16x16x32_bf16 v[112:115], v[76:79], v[32:35], 0\n\t"
      "s_lshr_b32 s61, s61, 23\n\t"
      "s_add_u32 s61, s61, s67\n\t"
      "v_mov_b32_dpp v102, v34 quad_perm:[2,3,0,1] row_mask:0xf bank_mask:0xf\n\t"
      "v_mov_b32_dpp v106, v38 quad_perm:[2,3,0,1] row_mask:0xf bank_mask:0xf\n\t"
      "s_max_u32 s61, s61, s66\n\t"
      "v_mfma_f32_16x16x32_bf16 v[116:119], v[80:83], v[32:35], 0\n\t"
      "s_lshr_b32 s61, s61, 1\n\t"
      "v_add_f32_e64 v48, v48, v50\n\t"
      "v_mov_b32_dpp v100, v32 quad_perm:[2,3,0,1] row_mask:0xf bank_mask:0xf\n\t"
      "v_mfma_f32_16x16x32_bf16 v[116:119], v[88:91], v[36:39], v[116:119]\n\t"
      "s_min_u32 s61, s61, 200\n\t"
      "v_mov_b32_dpp v107, v39 quad_perm:[2,3,0,1] row_mask:0xf bank_mask:0xf\n\t"
      "s_waitcnt lgkmcnt(0)\n\t"
      "v_mfma_f32_16x16x32_bf16 v[76:79], v[40:43], v[32:35], 0\n\t"
      "buffer_load_dwordx4 v127, s[36:39], s46 offen sc1 lds\n\t"
      "s_lshl_b32 s61, s61, 23\n\t"
      "v_mfma_f32_16x16x32_bf16 v[76:79], v[44:47], v[36:39], v[76:79]\n\t"
      "s_add_u32 s65, s61, 33554432\n\t"
      "v_mov_b32_dpp v104, v36 quad_perm:[2,3,0,1] row_mask:0xf bank_mask:0xf\n\t"
      "v_mov_b32_dpp v103, v35 quad_perm:[2,3,0,1] row_mask:0xf bank_mask:0xf\n\t"
      "v_mov_b32_dpp v101, v33 quad_perm:[2,3,0,1] row_mask:0xf bank_mask:0xf\n\t"
      "v_mfma_f32_16x16x32_bf16 v[112:115], v[84:87], v[36:39], v[112:115]\n\t"
      "s_pack_hh_b32_b16 s75, s65, s65\n\t"
      "v_add_f32 v49, v49, v51\n\t"
      "v_cvt_pk_bf16_f32 v64, v48, v49\n\t"
      "v_cndmask_b32_e64 v120, v76, v78, s[72:73]\n\t"
      "v_cndmask_b32_e64 v121, v77, v79, s[72:73]\n\t"
      "v_mov_b32_dpp v105, v37 quad_perm:[2,3,0,1] row_mask:0xf bank_mask:0xf\n\t"
      "v_mfma_f32_16x16x32_bf16 v[112:115], v[92:95], v[100:103], v[112:115]\n\t"
      "buffer_load_dwordx4 v127, s[36:39], s46 offen offset:1024 sc1 lds\n\t"
      "v_mfma_f32_16x16x32_bf16 v[116:119], v[96:99], v[100:103], v[116:119]\n\t"
      "s_pack_hh_b32_b16 s74, s65, s65\n\t"
      "v_add_f32_dpp v121, v121, v121 quad_perm:[2,3,0,1] row_mask:0xf bank_mask:0xf\n\t"
      "v_add_f32_dpp v120, v120, v120 quad_perm:[2,3,0,1] row_mask:0xf bank_mask:0xf\n\t"
      "v_mfma_f32_16x16x32_bf16 v[112:115], v[56:59], v[104:107], v[112:115]\n\t"
      "v_exp_f32 v81, v121\n\t"
      "v_mfma_f32_16x16x32_bf16 v[116:119], v[60:63], v[104:107], v[116:119]\n\t"
      "s_mov_b32 m0, s45\n\t"
      "v_mov_b64 v[90:91], s[74:75]\n\t"
      "v_exp_f32 v80, v120\n\t"
      "v_mov_b64 v[88:89], s[74:75]\n\t"
      "v_cvt_pk_bf16_f32 v108, v80, v81\n\t"
      "v_smfmac_f32_16x16x64_bf16 v[52:55], v[0:3], v[192:199], v122\n\t"
      "buffer_load_dwordx4 v65, s[36:39], s44 offen offset:1024 sc1 lds\n\t"
      "v_mfma_f32_16x16x32_bf16 v[84:87], v[108:111], v[64:67], 0\n\t"
      "s_add_u32 s57, s61, 310378496\n\t"
      "v_cndmask_b32_e64 v112, v112, v116, s[72:73]\n\t"
      "v_exp_f32_e64 v56, v112\n\t"
      "v_mfma_f32_16x16x32_bf16 v[48:51], v[108:111], v[88:91], 0\n\t"
      "s_add_u32 s43, s43, 0x8000\n\t"
      "v_cndmask_b32_e64 v113, v113, v117, s[72:73]\n\t"
      "v_cndmask_b32_e64 v114, v114, v118, s[72:73]\n\t"
      "v_exp_f32_e64 v58, v114\n\t"
      "v_cndmask_b32_e64 v115, v115, v119, s[72:73]\n\t"
      "v_exp_f32 v57, v113\n\t"
      "v_exp_f32 v59, v115\n\t"
      "v_cvt_pk_bf16_f32 v96, v56, v57\n\t"
      "v_cvt_pk_bf16_f32 v97, v58, v59\n\t"
      "v_max3_f32 v82, |v112|, |v113|, |v114|\n\t"
      "v_mov_b32_dpp v98, v96 quad_perm:[2,3,0,1] row_mask:0xf bank_mask:0xf\n\t"
      "v_mov_b32_dpp v99, v97 quad_perm:[2,3,0,1] row_mask:0xf bank_mask:0xf\n\t"
      "v_smfmac_f32_16x16x64_bf16 v[52:55], v[4:7], v[200:207], v122\n\t"
      "v_max3_f32 v83, |v115|, |v120|, |v121|\n\t"
      "v_mfma_f32_16x16x32_bf16 v[48:51], v[96:99], v[88:91], v[48:51]\n\t"
      "v_max_f32_e64 v82, v82, v83\n\t"
      "v_cmp_nge_f32 vcc, 0x42000000, v82\n\t"
      "v_mfma_f32_16x16x32_bf16 v[84:87], v[96:99], v[68:71], v[84:87]\n\t"
      "buffer_load_dwordx4 v65, s[36:39], s44 offen sc1 lds\n\t"
      "v_smfmac_f32_16x16x64_bf16 v[52:55], v[24:27], v[240:247], v122\n\t"
      "s_add_u32 s68, s68, 0x8000\n\t"
      "s_cbranch_vccnz LMX_2%=\n\t"
      "v_rcp_f32 v49, v49\n\t"
      "v_rcp_f32_e64 v48, v48\n\t"
      "v_smfmac_f32_16x16x64_bf16 v[52:55], v[28:31], v[248:255], v122\n\t"
      "v_fma_f32 v85, v85, v49, s96\n\t"
      "v_fma_f32 v84, v84, v48, s96\n\t"
      "v_smfmac_f32_16x16x64_bf16 v[52:55], v[16:19], v[224:231], v122\n\t"
      "buffer_atomic_add v84, v123, s[36:39], s97 offen\n\t"
      "buffer_atomic_add v85, v123, s[36:39], s98 offen\n\t"
      "v_smfmac_f32_16x16x64_bf16 v[52:55], v[20:23], v[232:239], v122\n\t"
      "v_mfma_f32_16x16x32_bf16 v[92:95], v[96:99], v[72:75], 0\n\t"
      "s_nop 3\n\t"
      "v_add_f32 v53, v53, v55\n\t"
      "v_add_f32_e64 v52, v52, v54\n\t"
      "v_cvt_pk_bf16_f32 v66, v52, v53\n\t"
      "s_add_u32 s62, s61, 0x83400000\n\t"
      "v_mov_b32_e64 v126, s62\n\t"
      "v_mfma_f32_16x16x32_bf16 v[92:95], v[108:111], v[66:69], v[92:95]\n\t"
      "s_add_u32 s97, s97, 0x8000\n\t"
      "s_nop 6\n\t"
      "v_fma_f32 v95, v95, v49, s96\n\t"
      "buffer_atomic_add v95, v123, s[36:39], s23 offen\n\t"
      "v_fma_f32 v94, v94, v48, s96\n\t"
      "buffer_atomic_add v94, v123, s[36:39], s69 offen\n\t"
      "LWIN_2%=:\n\t"
      "v_add_u32 v100, s55, v127\n\t"
      "ds_read_b128 v[60:63], v100 offset:57344\n\t"
      "s_cmp_eq_u32 s56, 0\n\t"
      "s_cbranch_scc0 LSLOW_2%=\n\t"
      "LWIN1_2%=:\n\t"
      "buffer_load_dwordx2 v[120:121], off, s[36:39], s50 sc1\n\t"
      "s_mov_b32 s64, s50\n\t"
      "s_bitcmp1_b64 s[84:85], s40\n\t"
      "s_cbranch_scc1 LEARLY_2%=\n\t"
      "LROLE_2%=:\n\t"
      "v_add_u32 v52, 65536, v127\n\t"
      "ds_read_b128 v[88:91], v52 offset:16384\n\t"
      "ds_read_b128 v[92:95], v52 offset:17408\n\t"
      "ds_read_b128 v[32:35], v52 offset:20480\n\t"
      "ds_read_b128 v[36:39], v52 offset:21504\n\t"
      "s_cmp_eq_u32 s42, 3\n\t"
      "s_waitcnt lgkmcnt(4)\n\t"
      "s_cbranch_scc1 LWK1_2%=\n\t"
      "s_cmp_eq_u32 s42, 2\n\t"
      "s_cbranch_scc0 LW4_2%=\n\t"
      "buffer_store_dwordx4 v[40:43], v61, s[36:39], s48 offen sc1\n\t"
      "LW4_2%=:\n\t"
      "s_add_u32 s48, s48, 65536\n\t"
      "s_nop 0\n\t"
      "ds_read_b128 v[40:43], v52 offset:24576\n\t"
      "ds_read_b128 v[44:47], v52 offset:25600\n\t"
      "v_mov_b64 v[48:49], 0\n\t"
      "v_mov_b64 v[50:51], 0\n\t"
      "s_memrealtime s[92:93]\n\t"
      "s_waitcnt lgkmcnt(0)\n\t"
      "s_sub_u32 s61, s92, s95\n\t"
      "s_mov_b32 s95, s92\n\t"
      "s_min_u32 s61, s61, 190\n\t"
      "s_add_u32 s61, s61, s92\n\t"
      "s_pack_ll_b32_b16 s61, s61, s40\n\t"
      "v_mov_b32 v63, s61\n\t"
      "s_mov_b64 exec, 1\n\t"
      "buffer_store_dword v63, off, s[36:39], s51 sc1\n\t"
      "s_mov_b64 exec, -1\n\t"
      "v_add_u32 v104, s71, v100\n\t"
      "ds_read_b128 v[76:79], v104\n\t"
      "ds_read_b128 v[80:83], v104 offset:1024\n\t"
      "ds_read_b128 v[112:115], v100\n\t"
      "ds_read_b128 v[116:119], v100 offset:1024\n\t"
      "v_xor_b32 v101, 2048, v104\n\t"
      "v_xor_b32 v102, 4096, v104\n\t"
      "v_xor_b32 v103, 6144, v104\n\t"
      "v_smfmac_f32_16x16x64_bf16 v[48:51], v[16:19], v[88:95], v122\n\t"
      "ds_read_b128 v[88:91], v52 offset:28672\n\t"
      "ds_read_b128 v[92:95], v52 offset:29696\n\t"
      "s_add_u32 m0, s55, s71\n\t"
      "s_sub_u32 s71, 204800, s71\n\t"
      "v_smfmac_f32_16x16x64_bf16 v[48:51], v[20:23], v[32:39], v122\n\t"
      "ds_read_b128 v[0:3], v101\n\t"
      "ds_read_b128 v[4:7], v101 offset:1024\n\t"
      "s_add_u32 s40, s40, 1\n\t"
      "s_cmp_lt_u32 s40, 63\n\t"
      "v_smfmac_f32_16x16x64_bf16 v[48:51], v[24:27], v[40:47], v122\n\t"
      "ds_read_b128 v[8:11], v100 offset:8192\n\t"
      "ds_read_b128 v[12:15], v100 offset:9216\n\t"
      "s_cselect_b32 s61, 786432, 0\n\t"
      "s_cselect_b32 s60, s58, 0\n\t"
      "s_waitcnt lgkmcnt(4)\n\t"
      "v_smfmac_f32_16x16x64_bf16 v[48:51], v[28:31], v[88:95], v122\n\t"
      "s_add_u32 s44, s44, s61\n\t"
      "s_cmp_lt_u32 s40, 62\n\t"
      "ds_read_b128 v[16:19], v102\n\t"
      "ds_read_b128 v[20:23], v102 offset:1024\n\t"
      "ds_read_b128 v[24:27], v100 offset:16384\n\t"
      "ds_read_b128 v[28:31], v100 offset:17408\n\t"
      "s_cselect_b32 s61, 786432, 0\n\t"
      "s_add_u32 s46, s46, s61\n\t"
      "s_add_u32 s61, s40, 3\n\t"
      "s_and_b32 s62, s61, 3\n\t"
      "s_cselect_b32 s62, 16, 2000\n\t"
      "s_cmp_lt_u32 s61, 64\n\t"
      "s_cselect_b32 s62, s62, 0\n\t"
      "s_add_u32 s50, s50, s62\n\t"
      "s_cmp_eq_u32 s40, 64\n\t"
      "v_add_f32 v48, v48, v50\n\t"
      "v_add_f32 v49, v49, v51\n\t"
      "ds_write_b64 v124, v[48:49] offset:53312\n\t"
      "s_cbranch_scc1 LFIN_2%=\n\t"
      "v_mfma_f32_16x16x32_bf16 v[32:35], v[76:79], v[112:115], 0\n\t"
      "s_mov_b32 s63, 0x2000\n\t"
      "v_mfma_f32_16x16x32_bf16 v[36:39], v[80:83], v[112:115], 0\n\t"
      "v_mfma_f32_16x16x32_bf16 v[40:43], v[76:79], v[116:119], 0\n\t"
      "v_mfma_f32_16x16x32_bf16 v[44:47], v[80:83], v[116:119], 0\n\t"
      "ds_read_b128 v[76:79], v103\n\t"
      "ds_read_b128 v[80:83], v103 offset:1024\n\t"
      "ds_read_b128 v[112:115], v100 offset:24576\n\t"
      "ds_read_b128 v[116:119], v100 offset:25600\n\t"
      "LGATE_2%=:\n\t"
      "LBAR_2%=:\n\t"
      "s_cmp_gt_u32 s40, 1\n\t"
      "s_waitcnt lgkmcnt(0)\n\t"
      "s_barrier\n\t"
      "s_cbranch_scc1 LPOLL_2%=\n\t"
      "s_branch LFIRST_2%=\n\t"
      ".p2align 6\n\t.fill 16, 4, 0xbf800000\n\t"
      "LPOLL_3%=:\n\t"
      "s_memtime s[78:79]\n\t"
      "buffer_load_dwordx4 v[84:87], v60, s[36:39], s43 offen sc1\n\t"
      "buffer_load_dwordx4 v[88:91], v60, s[36:39], s43 offen offset:1024 sc1\n\t"
      "s_waitcnt lgkmcnt(8)\n\t"
      "v_mfma_f32_16x16x32_bf16 v[32:35], v[0:3], v[8:11], v[32:35]\n\t"
      "v_mfma_f32_16x16x32_bf16 v[36:39], v[4:7], v[8:11], v[36:39]\n\t"
      "v_mfma_f32_16x16x32_bf16 v[40:43], v[0:3], v[12:15], v[40:43]\n\t"
      "v_mfma_f32_16x16x32_bf16 v[44:47], v[4:7], v[12:15], v[44:47]\n\t"
      "buffer_load_dwordx4 v[92:95], v60, s[36:39], s68 offen sc1\n\t"
      "buffer_load_dwordx4 v[96:99], v60, s[36:39], s68 offen offset:1024 sc1\n\t"
      "v_mov_b64 v[48:49], 0\n\t"
      "v_mov_b64 v[50:51], 0\n\t"
      "v_mov_b64 v[52:53], 0\n\t"
      "v_mov_b64 v[54:55], 0\n\t"
      "s_xor_b32 s69, s97, 1024\n\t"
      "s_xor_b32 s98, s97, 512\n\t"
      "s_xor_b32 s23, s97, 1536\n\t"
      "s_waitcnt lgkmcnt(4)\n\t"
      "v_mfma_f32_16x16x32_bf16 v[32:35], v[16:19], v[24:27], v[32:35]\n\t"
      "v_mfma_f32_16x16x32_bf16 v[36:39], v[20:23], v[24:27], v[36:39]\n\t"
      "v_mfma_f32_16x16x32_bf16 v[40:43], v[16:19], v[28:31], v[40:43]\n\t"
      "v_mfma_f32_16x16x32_bf16 v[44:47], v[20:23], v[28:31], v[44:47]\n\t"
      "s_waitcnt lgkmcnt(0)\n\t"
      "v_mfma_f32_16x16x32_bf16 v[32:35], v[76:79], v[112:115], v[32:35]\n\t"
      "v_mfma_f32_16x16x32_bf16 v[36:39], v[80:83], v[112:115], v[36:39]\n\t"
      "v_mfma_f32_16x16x32_bf16 v[40:43], v[76:79], v[116:119], v[40:43]\n\t"
      "v_mfma_f32_16x16x32_bf16 v[44:47], v[80:83], v[116:119], v[44:47]\n\t"
      "s_mov_b64 exec, 1\n\t"
      "ds_write_b64 v109, v[110:111] offset:55392\n\t"
      "s_mov_b64 exec, -1\n\t"
      "s_nop 4\n\t"
      "v_cvt_pk_bf16_f32 v68, v32, v33\n\t"
      "v_cvt_pk_bf16_f32 v69, v34, v35\n\t"
      "v_cvt_pk_bf16_f32 v70, v36, v37\n\t"
      "v_cvt_pk_bf16_f32 v71, v38, v39\n\t"
      "v_cvt_pk_bf16_f32 v74, v40, v41\n\t"
      "v_cvt_pk_bf16_f32 v75, v42, v43\n\t"
      "v_cvt_pk_bf16_f32 v72, v44, v45\n\t"
      "v_cvt_pk_bf16_f32 v73, v46, v47\n\t"
      "v_mov_b64 v[32:33], 0\n\t"
      "v_mov_b64 v[34:35], 0\n\t"
      "v_mov_b64 v[36:37], 0\n\t"
      "v_mov_b64 v[38:39], 0\n\t"
      "v_mov_b64 v[40:41], 0\n\t"
      "v_mov_b64 v[42:43], 0\n\t"
      "v_mov_b64 v[44:45], 0\n\t"
      "v_mov_b64 v[46:47], 0\n\t"
      "LGO_3%=:\n\t"
      "s_nop 0\n\t"
      "s_waitcnt vmcnt(3)\n\t"
      "s_memtime s[88:89]\n\t"
      "v_pk_fma_f32 v[10:11], v[86:87], s[56:57], v[126:127] op_sel:[0,1,0] op_sel_hi:[1,1,0]\n\t"
      "v_pk_fma_f32 v[8:9], v[84:85], s[56:57], v[126:127] op_sel:[0,1,0] op_sel_hi:[1,1,0]\n\t"
      "v_cvt_pk_bf16_f32 v12, v8, v9\n\t"
      "v_cvt_pk_bf16_f32 v13, v10, v11\n\t"
      "s_nop 0\n\t"
      "ds_write_b64 v127, v[12:13] offset:44032\n\t"
      "s_waitcnt vmcnt(2)\n\t"
      "v_pk_fma_f32 v[4:5], v[88:89], s[56:57], v[126:127] op_sel:[0,1,0] op_sel_hi:[1,1,0]\n\t"
      "v_pk_fma_f32 v[6:7], v[90:91], s[56:57], v[126:127] op_sel:[0,1,0] op_sel_hi:[1,1,0]\n\t"
      "v_cvt_pk_bf16_f32 v14, v4, v5\n\t"
      "v_cvt_pk_bf16_f32 v15, v6, v7\n\t"
      "ds_write_b64 v127, v[14:15] offset:44040\n\t"
      "v_bitop3_b32 v64, v84, v85, v86 bitop3:0x80\n\t"
      "v_mfma_f32_16x16x32_bf16 v[56:59], v[12:15], v[12:15], 0\n\t"
      "v_bitop3_b32 v64, v64, v87, v88 bitop3:0x80\n\t"
      "v_bitop3_b32 v64, v64, v89, v90 bitop3:0x80\n\t"
      "v_bitop3_b32 v64, v64, v91, s58 bitop3:0x80\n\t"
      "v_cmp_eq_u32_e64 vcc, 0, v64\n\t"
      "v_smfmac_f32_16x16x64_bf16 v[36:39], v[12:15], a[88:95], v122\n\t"
      "s_nop 0\n\t"
      "v_bitop3_b32 v120, v120, v121, s53 bitop3:0x7e\n\t"
      "v_smfmac_f32_16x16x64_bf16 v[44:47], v[12:15], a[216:223], v122\n\t"
      "s_cbranch_vccnz LR0_3%=\n\t"
      "LC0_3%=:\n\t"
      "s_waitcnt lgkmcnt(0)\n\t"
      "s_barrier\n\t"
      "ds_read_b128 v[8:11], v127 offset:43008\n\t"
      "v_smfmac_f32_16x16x64_bf16 v[52:55], v[12:15], v[216:223], v122\n\t"
      "ds_read_b128 v[4:7], v127 offset:41984\n\t"
      "v_smfmac_f32_16x16x64_bf16 v[48:51], v[12:15], v[152:159], v122\n\t"
      "ds_read_b128 v[0:3], v127 offset:40960\n\t"
      "v_smfmac_f32_16x16x64_bf16 v[32:35], v[12:15], a[24:31], v122\n\t"
      "ds_read_b128 v[84:87], v127 offset:36864\n\t"
      "v_smfmac_f32_16x16x64_bf16 v[40:43], v[12:15], a[152:159], v122\n\t"
      "s_waitcnt lgkmcnt(3)\n\t"
      "v_smfmac_f32_16x16x64_bf16 v[32:35], v[8:11], a[16:23], v122\n\t"
      "ds_read_b128 v[80:83], v127 offset:35840\n\t"
      "v_smfmac_f32_16x16x64_bf16 v[40:43], v[8:11], a[144:151], v122\n\t"
      "s_waitcnt vmcnt(1)\n\t"
      "v_fma_f32 v27, v95, s57, v126\n\t"
      "v_fma_f32 v24, v92, s57, v126\n\t"
      "v_smfmac_f32_16x16x64_bf16 v[36:39], v[8:11], a[80:87], v122\n\t"
      "v_fma_f32 v26, v94, s57, v126\n\t"
      "v_cvt_pk_bf16_f32 v29, v26, v27\n\t"
      "v_bitop3_b32 v64, v92, v93, v94 bitop3:0x80\n\t"
      "v_smfmac_f32_16x16x64_bf16 v[44:47], v[8:11], a[208:215], v122\n\t"
      "s_waitcnt lgkmcnt(2)\n\t"
      "v_fma_f32 v25, v93, s57, v126\n\t"
      "v_cvt_pk_bf16_f32 v28, v24, v25\n\t"
      "v_smfmac_f32_16x16x64_bf16 v[36:39], v[4:7], a[72:79], v122\n\t"
      "s_waitcnt vmcnt(0)\n\t"
      "ds_write_b64 v127, v[28:29] offset:48128\n\t"
      "v_fma_f32 v23, v99, s57, v126\n\t"
      "v_fma_f32 v22, v98, s57, v126\n\t"
      "v_cvt_pk_bf16_f32 v31, v22, v23\n\t"
      "v_fma_f32 v21, v97, s57, v126\n\t"
      "v_fma_f32 v20, v96, s57, v126\n\t"
      "v_cvt_pk_bf16_f32 v30, v20, v21\n\t"
      "v_smfmac_f32_16x16x64_bf16 v[44:47], v[4:7], a[200:207], v122\n\t"
      "ds_write_b64 v127, v[30:31] offset:48136\n\t"
      "v_smfmac_f32_16x16x64_bf16 v[40:43], v[4:7], a[136:143], v122\n\t"
      "ds_read_b128 v[88:91], v127 offset:39936\n\t"
      "v_bitop3_b32 v64, v64, v95, v96 bitop3:0x80\n\t"
      "v_bitop3_b32 v64, v64, v97, v98 bitop3:0x80\n\t"
      "v_smfmac_f32_16x16x64_bf16 v[40:43], v[0:3], a[128:135], v122\n\t"
      "v_bitop3_b32 v64, v64, v99, s60 bitop3:0x80\n\t"
      "v_smfmac_f32_16x16x64_bf16 v[36:39], v[0:3], a[64:71], v122\n\t"
      "v_cmp_eq_u32_e64 vcc, 0, v64\n\t"
      "v_smfmac_f32_16x16x64_bf16 v[44:47], v[0:3], a[192:199], v122\n\t"
      "s_waitcnt lgkmcnt(1)\n\t"
      "v_smfmac_f32_16x16x64_bf16 v[32:35], v[4:7], a[8:15], v122\n\t"
      "s_cbranch_vccnz LR1_3%=\n\t"
      "LC1_3%=:\n\t"
      "v_smfmac_f32_16x16x64_bf16 v[40:43], v[28:31], a[184:191], v122\n\t"
      "s_barrier\n\t"
      "v_mfma_f32_16x16x32_bf16 v[56:59], v[28:31], v[28:31], v[56:59]\n\t"
      "ds_read_b128 v[24:27], v127 offset:47104\n\t"
      "s_nop 0\n\t"
      "v_smfmac_f32_16x16x64_bf16 v[32:35], v[0:3], a[0:7], v122\n\t"
      "ds_read_b128 v[20:23], v127 offset:46080\n\t"
      "v_smfmac_f32_16x16x64_bf16 v[44:47], v[28:31], a[248:255], v122\n\t"
      "ds_read_b128 v[16:19], v127 offset:45056\n\t"
      "v_max3_f32 v56, v56, v57, v58\n\t"
      "v_smfmac_f32_16x16x64_bf16 v[36:39], v[28:31], a[120:127], v122\n\t"
      "ds_read_b128 v[76:79], v127 offset:32768\n\t"
      "v_max_f32_e64 v56, v56, v59\n\t"
      "v_smfmac_f32_16x16x64_bf16 v[32:35], v[28:31], a[56:63], v122\n\t"
      "s_waitcnt lgkmcnt(3)\n\t"
      "v_readfirstlane_b32 s56, v120\n\t"
      "v_smfmac_f32_16x16x64_bf16 v[32:35], v[24:27], a[48:55], v122\n\t"
      "s_waitcnt lgkmcnt(2)\n\t"
      "v_max_f32_dpp v56, v56, v56 quad_perm:[1,0,3,2] row_mask:0xf bank_mask:0xf\n\t"
      "v_smfmac_f32_16x16x64_bf16 v[32:35], v[20:23], a[40:47], v122\n\t"
      "s_waitcnt lgkmcnt(1)\n\t"
      "v_max_f32_dpp v56, v56, v56 quad_perm:[2,3,0,1] row_mask:0xf bank_mask:0xf\n\t"
      "v_smfmac_f32_16x16x64_bf16 v[32:35], v[16:19], a[32:39], v122\n\t"
      "s_mov_b32 exec_lo, 0x100001\n\t"
      "s_mov_b32 exec_hi, 0x10000100\n\t"
      "v_smfmac_f32_16x16x64_bf16 v[36:39], v[24:27], a[112:119], v122\n\t"
      "ds_max_i32 v109, v56 offset:55392\n\t"
      "v_smfmac_f32_16x16x64_bf16 v[40:43], v[24:27], a[176:183], v122\n\t"
      "s_mov_b64 exec, -1\n\t"
      "v_add_f32 v33, v33, v35\n\t"
      "v_smfmac_f32_16x16x64_bf16 v[36:39], v[20:23], a[104:111], v122\n\t"
      "ds_read_b128 v[60:63], v127 offset:37888\n\t"
      "v_mul_f32_e64 v33, s59, v33\n\t"
      "v_smfmac_f32_16x16x64_bf16 v[36:39], v[16:19], a[96:103], v122\n\t"
      "v_add_f32_e64 v32, v32, v34\n\t"
      "v_smfmac_f32_16x16x64_bf16 v[44:47], v[24:27], a[240:247], v122\n\t"
      "ds_read_b128 v[56:59], v127 offset:38912\n\t"
      "v_mul_f32_e64 v32, s59, v32\n\t"
      "v_smfmac_f32_16x16x64_bf16 v[44:47], v[20:23], a[232:239], v122\n\t"
      "v_add_f32 v36, v36, v38\n\t"
      "v_add_f32 v37, v37, v39\n\t"
      "v_mul_f32_e64 v37, s59, v37\n\t"
      "v_smfmac_f32_16x16x64_bf16 v[40:43], v[20:23], a[168:175], v122\n\t"
      "ds_read_b128 v[92:95], v127 offset:34816\n\t"
      "v_cvt_pk_bf16_f32 v112, v32, v33\n\t"
      "v_smfmac_f32_16x16x64_bf16 v[44:47], v[16:19], a[224:231], v122\n\t"
      "ds_read_b128 v[96:99], v127 offset:33792\n\t"
      "v_mul_f32_e64 v36, s59, v36\n\t"
      "v_cvt_pk_bf16_f32 v113, v36, v37\n\t"
      "v_smfmac_f32_16x16x64_bf16 v[40:43], v[16:19], a[160:167], v122\n\t"
      "ds_write_b64 v125, v[112:113]\n\t"
      "v_smfmac_f32_16x16x64_bf16 v[48:51], v[8:11], v[144:151], v122\n\t"
      "s_nop 0\n\t"
      "v_add_f32 v45, v45, v47\n\t"
      "v_add_f32_e64 v44, v44, v46\n\t"
      "v_cvt_pk_bf16_f32 v121, v44, v45\n\t"
      "v_add_f32 v41, v41, v43\n\t"
      "v_add_f32 v40, v40, v42\n\t"
      "v_cvt_pk_bf16_f32 v120, v40, v41\n\t"
      "v_smfmac_f32_16x16x64_bf16 v[48:51], v[4:7], v[136:143], v122\n\t"
      "s_mov_b32 s58, 0x10000000\n\t"
      "ds_write_b64 v125, v[120:121] offset:2080\n\t"
      "v_smfmac_f32_16x16x64_bf16 v[48:51], v[0:3], v[128:135], v122\n\t"
      "s_waitcnt lgkmcnt(1)\n\t"
      "s_barrier\n\t"
      "ds_read_b64_tr_b16 v[36:37], v124 offset:50192\n\t"
      "v_smfmac_f32_16x16x64_bf16 v[48:51], v[28:31], v[184:191], v122\n\t"
      "ds_read_b32 v108, v109 offset:55392\n\t"
      "ds_read_b64_tr_b16 v[32:33], v124 offset:49152\n\t"
      "v_smfmac_f32_16x16x64_bf16 v[52:55], v[8:11], v[208:215], v122\n\t"
      "ds_read_b64_tr_b16 v[34:35], v124 offset:49672\n\t"
      "s_waitcnt lgkmcnt(4)\n\t"
      "ds_read_b64_tr_b16 v[38:39], v124 offset:50712\n\t"
      "v_smfmac_f32_16x16x64_bf16 v[48:51], v[24:27], v[176:183], v122\n\t"
      "s_barrier\n\t"
      "ds_read_b64_tr_b16 v[46:47], v124 offset:52792\n\t"
      "v_smfmac_f32_16x16x64_bf16 v[48:51], v[20:23], v[168:175], v122\n\t"
      "s_waitcnt lgkmcnt(4)\n\t"
      "ds_read_b64_tr_b16 v[44:45], v124 offset:52272\n\t"
      "v_readfirstlane_b32 s61, v108\n\t"
      "v_smfmac_f32_16x16x64_bf16 v[48:51], v[16:19], v[160:167], v122\n\t"
      "s_waitcnt lgkmcnt(3)\n\t"
      "ds_read_b64_tr_b16 v[42:43], v124 offset:51752\n\t"
      "ds_read_b64_tr_b16 v[40:41], v124 offset:51232\n\t"
      "s_waitcnt lgkmcnt(4)\n\t"
      "v_mfma_f32_16x16x32_bf16 v[112:115], v[76:79], v[32:35], 0\n\t"
      "s_lshr_b32 s61, s61, 23\n\t"
      "s_add_u32 s61, s61, s67\n\t"
      "v_mov_b32_dpp v102, v34 quad_perm:[2,3,0,1] row_mask:0xf bank_mask:0xf\n\t"
      "v_mov_b32_dpp v106, v38 quad_perm:[2,3,0,1] row_mask:0xf bank_mask:0xf\n\t"
      "s_max_u32 s61, s61, s66\n\t"
      "v_mfma_f32_16x16x32_bf16 v[116:119], v[80:83], v[32:35], 0\n\t"
      "s_lshr_b32 s61, s61, 1\n\t"
      "v_add_f32_e64 v48, v48, v50\n\t"
      "v_mov_b32_dpp v100, v32 quad_perm:[2,3,0,1] row_mask:0xf bank_mask:0xf\n\t"
      "v_mfma_f32_16x16x32_bf16 v[116:119], v[88:91], v[36:39], v[116:119]\n\t"
      "s_min_u32 s61, s61, 200\n\t"
      "v_mov_b32_dpp v107, v39 quad_perm:[2,3,0,1] row_mask:0xf bank_mask:0xf\n\t"
      "s_waitcnt lgkmcnt(0)\n\t"
      "v_mfma_f32_16x16x32_bf16 v[76:79], v[40:43], v[32:35], 0\n\t"
      "buffer_load_dwordx4 v127, s[36:39], s46 offen sc1 lds\n\t"
      "s_lshl_b32 s61, s61, 23\n\t"
      "v_mfma_f32_16x16x32_bf16 v[76:79], v[44:47], v[36:39], v[76:79]\n\t"
      "s_add_u32 s65, s61, 33554432\n\t"
      "v_mov_b32_dpp v104, v36 quad_perm:[2,3,0,1] row_mask:0xf bank_mask:0xf\n\t"
      "v_mov_b32_dpp v103, v35 quad_perm:[2,3,0,1] row_mask:0xf bank_mask:0xf\n\t"
      "v_mov_b32_dpp v101, v33 quad_perm:[2,3,0,1] row_mask:0xf bank_mask:0xf\n\t"
      "v_mfma_f32_16x16x32_bf16 v[112:115], v[84:87], v[36:39], v[112:115]\n\t"
      "s_pack_hh_b32_b16 s75, s65, s65\n\t"
      "v_add_f32 v49, v49, v51\n\t"
      "v_cvt_pk_bf16_f32 v64, v48, v49\n\t"
      "v_cndmask_b32_e64 v120, v76, v78, s[72:73]\n\t"
      "v_cndmask_b32_e64 v121, v77, v79, s[72:73]\n\t"
      "v_mov_b32_dpp v105, v37 quad_perm:[2,3,0,1] row_mask:0xf bank_mask:0xf\n\t"
      "v_mfma_f32_16x16x32_bf16 v[112:115], v[92:95], v[100:103], v[112:115]\n\t"
      "buffer_load_dwordx4 v127, s[36:39], s46 offen offset:1024 sc1 lds\n\t"
      "v_mfma_f32_16x16x32_bf16 v[116:119], v[96:99], v[100:103], v[116:119]\n\t"
      "s_pack_hh_b32_b16 s74, s65, s65\n\t"
      "v_add_f32_dpp v121, v121, v121 quad_perm:[2,3,0,1] row_mask:0xf bank_mask:0xf\n\t"
      "v_add_f32_dpp v120, v120, v120 quad_perm:[2,3,0,1] row_mask:0xf bank_mask:0xf\n\t"
      "v_mfma_f32_16x16x32_bf16 v[112:115], v[56:59], v[104:107], v[112:115]\n\t"
      "v_exp_f32 v81, v121\n\t"
      "v_mfma_f32_16x16x32_bf16 v[116:119], v[60:63], v[104:107], v[116:119]\n\t"
      "s_mov_b32 m0, s45\n\t"
      "v_mov_b64 v[90:91], s[74:75]\n\t"
      "v_exp_f32 v80, v120\n\t"
      "v_mov_b64 v[88:89], s[74:75]\n\t"
      "v_cvt_pk_bf16_f32 v108, v80, v81\n\t"
      "v_smfmac_f32_16x16x64_bf16 v[52:55], v[4:7], v[200:207], v122\n\t"
      "buffer_load_dwordx4 v65, s[36:39], s44 offen offset:1024 sc1 lds\n\t"
      "v_mfma_f32_16x16x32_bf16 v[84:87], v[108:111], v[64:67], 0\n\t"
      "s_add_u32 s57, s61, 310378496\n\t"
      "v_cndmask_b32_e64 v112, v112, v116, s[72:73]\n\t"
      "v_exp_f32_e64 v56, v112\n\t"
      "v_mfma_f32_16x16x32_bf16 v[48:51], v[108:111], v[88:91], 0\n\t"
      "s_add_u32 s43, s43, 0x8000\n\t"
      "v_cndmask_b32_e64 v113, v113, v117, s[72:73]\n\t"
      "v_cndmask_b32_e64 v114, v114, v118, s[72:73]\n\t"
      "v_exp_f32_e64 v58, v114\n\t"
      "v_cndmask_b32_e64 v115, v115, v119, s[72:73]\n\t"
      "v_exp_f32 v57, v113\n\t"
      "v_exp_f32 v59, v115\n\t"
      "v_cvt_pk_bf16_f32 v96, v56, v57\n\t"
      "v_cvt_pk_bf16_f32 v97, v58, v59\n\t"
      "v_max3_f32 v82, |v112|, |v113|, |v114|\n\t"
      "v_mov_b32_dpp v98, v96 quad_perm:[2,3,0,1] row_mask:0xf bank_mask:0xf\n\t"
      "v_mov_b32_dpp v99, v97 quad_perm:[2,3,0,1] row_mask:0xf bank_mask:0xf\n\t"
      "v_smfmac_f32_16x16x64_bf16 v[52:55], v[0:3], v[192:199], v122\n\t"
      "v_max3_f32 v83, |v115|, |v120|, |v121|\n\t"
      "v_mfma_f32_16x16x32_bf16 v[48:51], v[96:99], v[88:91], v[48:51]\n\t"
      "v_max_f32_e64 v82, v82, v83\n\t"
      "v_cmp_nge_f32 vcc, 0x42000000, v82\n\t"
      "v_mfma_f32_16x16x32_bf16 v[84:87], v[96:99], v[68:71], v[84:87]\n\t"
      "buffer_load_dwordx4 v65, s[36:39], s44 offen sc1 lds\n\t"
      "v_smfmac_f32_16x16x64_bf16 v[52:55], v[28:31], v[248:255], v122\n\t"
      "s_add_u32 s68, s68, 0x8000\n\t"
      "s_cbranch_vccnz LMX_3%=\n\t"
      "v_rcp_f32 v49, v49\n\t"
      "v_rcp_f32_e64 v48, v48\n\t"
      "v_smfmac_f32_16x16x64_bf16 v[52:55], v[24:27], v[240:247], v122\n\t"
      "v_fma_f32 v85, v85, v49, s96\n\t"
      "v_fma_f32 v84, v84, v48, s96\n\t"
      "v_smfmac_f32_16x16x64_bf16 v[52:55], v[20:23], v[232:239], v122\n\t"
      "buffer_atomic_add v84, v123, s[36:39], s97 offen\n\t"
      "buffer_atomic_add v85, v123, s[36:39], s98 offen\n\t"
      "v_smfmac_f32_16x16x64_bf16 v[52:55], v[16:19], v[224:231], v122\n\t"
      "v_mfma_f32_16x16x32_bf16 v[92:95], v[96:99], v[72:75], 0\n\t"
      "s_nop 3\n\t"
      "v_add_f32 v53, v53, v55\n\t"
      "v_add_f32_e64 v52, v52, v54\n\t"
      "v_cvt_pk_bf16_f32 v66, v52, v53\n\t"
      "s_add_u32 s62, s61, 0x83400000\n\t"
      "v_mov_b32_e64 v126, s62\n\t"
      "v_mfma_f32_16x16x32_bf16 v[92:95], v[108:111], v[66:69], v[92:95]\n\t"
      "s_add_u32 s97, s97, 0x8000\n\t"
      "s_nop 6\n\t"
      "v_fma_f32 v95, v95, v49, s96\n\t"
      "buffer_atomic_add v95, v123, s[36:39], s23 offen\n\t"
      "v_fma_f32 v94, v94, v48, s96\n\t"
      "buffer_atomic_add v94, v123, s[36:39], s69 offen\n\t"
      "LWIN_3%=:\n\t"
      "s_memtime s[76:77]\n\t"
      "s_memrealtime s[80:81]\n\t"
      "v_add_u32 v100, s55, v127\n\t"
      "ds_read_b128 v[60:63], v100 offset:57344\n\t"
      "s_cmp_eq_u32 s56, 0\n\t"
      "s_cbranch_scc0 LSLOW_3%=\n\t"
      "LWIN1_3%=:\n\t"
      "buffer_load_dwordx2 v[120:121], off, s[36:39], s50 sc1\n\t"
      "s_mov_b32 s64, s50\n\t"
      "s_bitcmp1_b64 s[84:85], s40\n\t"
      "s_cbranch_scc1 LEARLY_3%=\n\t"
      "LROLE_3%=:\n\t"
      "v_add_u32 v52, 67584, v127\n\t"
      "ds_read_b128 v[88:91], v52 offset:16384\n\t"
      "ds_read_b128 v[92:95], v52 offset:17408\n\t"
      "ds_read_b128 v[32:35], v52 offset:20480\n\t"
      "ds_read_b128 v[36:39], v52 offset:21504\n\t"
      "ds_read_b128 v[40:43], v52 offset:24576\n\t"
      "ds_read_b128 v[44:47], v52 offset:25600\n\t"
      "v_mov_b64 v[48:49], 0\n\t"
      "v_mov_b64 v[50:51], 0\n\t"
      "v_add_u32 v104, s71, v100\n\t"
      "ds_read_b128 v[76:79], v104\n\t"
      "ds_read_b128 v[80:83], v104 offset:1024\n\t"
      "ds_read_b128 v[112:115], v100\n\t"
      "ds_read_b128 v[116:119], v100 offset:1024\n\t"
      "v_xor_b32 v101, 2048, v104\n\t"
      "v_xor_b32 v102, 4096, v104\n\t"
      "v_xor_b32 v103, 6144, v104\n\t"
      "s_waitcnt lgkmcnt(8)\n\t"
      "v_smfmac_f32_16x16x64_bf16 v[48:51], v[16:19], v[88:95], v122\n\t"
      "ds_read_b128 v[88:91], v52 offset:28672\n\t"
      "ds_read_b128 v[92:95], v52 offset:29696\n\t"
      "s_add_u32 m0, s55, s71\n\t"
      "s_sub_u32 s71, 204800, s71\n\t"
      "s_waitcnt lgkmcnt(8)\n\t"
      "v_smfmac_f32_16x16x64_bf16 v[48:51], v[20:23], v[32:39], v122\n\t"
      "ds_read_b128 v[0:3], v101\n\t"
      "ds_read_b128 v[4:7], v101 offset:1024\n\t"
      "s_add_u32 s40, s40, 1\n\t"
      "s_cmp_lt_u32 s40, 63\n\t"
      "s_waitcnt lgkmcnt(8)\n\t"
      "v_smfmac_f32_16x16x64_bf16 v[48:51], v[24:27], v[40:47], v122\n\t"
      "ds_read_b128 v[8:11], v100 offset:8192\n\t"
      "ds_read_b128 v[12:15], v100 offset:9216\n\t"
      "s_cselect_b32 s61, 786432, 0\n\t"
      "s_cselect_b32 s60, s58, 0\n\t"
      "s_waitcnt lgkmcnt(4)\n\t"
      "v_smfmac_f32_16x16x64_bf16 v[48:51], v[28:31], v[88:95], v122\n\t"
      "s_add_u32 s44, s44, s61\n\t"
      "s_cmp_lt_u32 s40, 62\n\t"
      "ds_read_b128 v[16:19], v102\n\t"
      "ds_read_b128 v[20:23], v102 offset:1024\n\t"
      "ds_read_b128 v[24:27], v100 offset:16384\n\t"
      "ds_read_b128 v[28:31], v100 offset:17408\n\t"
      "s_cselect_b32 s61, 786432, 0\n\t"
      "s_add_u32 s46, s46, s61\n\t"
      "s_add_u32 s61, s40, 3\n\t"
      "s_and_b32 s62, s61, 3\n\t"
      "s_cselect_b32 s62, 16, 2000\n\t"
      "s_cmp_lt_u32 s61, 64\n\t"
      "s_cselect_b32 s62, s62, 0\n\t"
      "s_add_u32 s50, s50, s62\n\t"
      "s_cmp_eq_u32 s40, 64\n\t"
      "v_add_f32 v48, v48, v50\n\t"
      "v_add_f32 v49, v49, v51\n\t"
      "ds_write_b64 v124, v[48:49] offset:53832\n\t"
      "s_cbranch_scc1 LFIN_3%=\n\t"
      "v_mfma_f32_16x16x32_bf16 v[32:35], v[76:79], v[112:115], 0\n\t"
      "s_sub_u32 s61, s88, s78\n\t"
      "s_sub_i32 s61, s61, 560\n\t"
      "s_lshr_b32 s62, s25, 3\n\t"
      "v_mfma_f32_16x16x32_bf16 v[36:39], v[80:83], v[112:115], 0\n\t"
      "s_sub_u32 s25, s25, s62\n\t"
      "s_sub_i32 s25, s25, 24\n\t"
      "s_max_i32 s25, s25, 0\n\t"
      "v_mfma_f32_16x16x32_bf16 v[40:43], v[76:79], v[116:119], 0\n\t"
      "s_sub_i32 s62, s61, 96\n\t"
      "s_max_i32 s62, s62, 0\n\t"
      "s_min_i32 s62, s62, 320\n\t"
      "v_mfma_f32_16x16x32_bf16 v[44:47], v[80:83], v[116:119], 0\n\t"
      "s_lshr_b32 s62, s62, 2\n\t"
      "s_add_u32 s25, s25, s62\n\t"
      "s_min_i32 s61, s61, 96\n\t"
      "ds_read_b128 v[76:79], v103\n\t"
      "ds_read_b128 v[80:83], v103 offset:1024\n\t"
      "ds_read_b128 v[112:115], v100 offset:24576\n\t"
      "ds_read_b128 v[116:119], v100 offset:25600\n\t"
      "s_max_i32 s61, s61, -96\n\t"
      "s_ashr_i32 s61, s61, 3\n\t"
      "s_add_u32 s26, s26, s61\n\t"
      "s_max_u32 s26, s26, 1500\n\t"
      "s_min_u32 s26, s26, 2300\n\t"
      "s_sub_u32 s62, s88, s27\n\t"
      "s_mov_b32 s27, s88\n\t"
      "s_sub_i32 s62, s62, s24\n\t"
      "s_cmp_gt_i32 s62, 1150\n\t"
      "s_cselect_b32 s62, 200, 0\n\t"
      "s_max_u32 s25, s25, s62\n\t"
      "s_sub_u32 s61, s76, s28\n\t"
      "s_sub_u32 s62, s80, s82\n\t"
      "s_mov_b32 s28, s76\n\t"
      "s_mov_b32 s82, s80\n\t"
      "s_lshl_b32 s61, s61, 8\n\t"
      "s_mul_i32 s62, s62, s83\n\t"
      "s_sub_i32 s61, s61, s62\n\t"
      "s_ashr_i32 s61, s61, 10\n\t"
      "s_min_i32 s61, s61, 100\n\t"
      "s_max_i32 s61, s61, -100\n\t"
      "s_add_u32 s83, s83, s61\n\t"
      "s_mul_i32 s24, s26, s83\n\t"
      "s_lshr_b32 s24, s24, 12\n\t"
      "s_add_u32 s24, s24, s25\n\t"
      "s_min_u32 s61, s30, 300\n\t"
      "s_sub_u32 s30, s30, s61\n\t"
      "s_sub_u32 s24, s24, s61\n\t"
      "s_mov_b32 s63, 0x2000\n\t"
      "LGATE_3%=:\n\t"
      "s_memtime s[92:93]\n\t"
      "s_waitcnt lgkmcnt(0)\n\t"
      "s_sub_u32 s61, s92, s88\n\t"
      "s_sub_i32 s61, s24, s61\n\t"
      "s_sub_i32 s61, s61, 70\n\t"
      "s_min_i32 s61, s61, 3000\n\t"
      "s_getpc_b64 s[92:93]\n\t"
      "LDPC_3%=:\n\t"
      "s_max_i32 s61, s61, 0\n\t"
      "s_andn2_b32 s62, 28, s61\n\t"
      "s_add_u32 s62, s62, LDSL_3%=-LDPC_3%=\n\t"
      "s_add_u32 s92, s92, s62\n\t"
      "s_addc_u32 s93, s93, 0\n\t"
      "LDLY_3%=:\n\t"
      "s_sub_i32 s61, s61, 32\n\t"
      "s_cmp_ge_i32 s61, 0\n\t"
      "s_cbranch_scc1 LDLY_3%=\n\t"
      "s_setpc_b64 s[92:93]\n\t"
      "LDSL_3%=:\n\t"
      "s_nop 0\n\t"
      "s_nop 0\n\t"
      "s_nop 0\n\t"
      "s_nop 0\n\t"
      "s_nop 0\n\t"
      "s_nop 0\n\t"
      "s_nop 0\n\t"
      "LBAR_3%=:\n\t"
      "s_cmp_gt_u32 s40, 1\n\t"
      "s_barrier\n\t"
      "s_cbranch_scc1 LPOLL_3%=\n\t"
      "s_branch LFIRST_3%=\n\t"
      "LUTOK_0%=:\n\t"
      "v_add_u32 v104, s71, v100\n\t"
      "ds_read_b128 v[76:79], v104\n\t"
      "ds_read_b128 v[80:83], v104 offset:1024\n\t"
      "ds_read_b128 v[112:115], v100\n\t"
      "ds_read_b128 v[116:119], v100 offset:1024\n\t"
      "v_xor_b32 v101, 2048, v104\n\t"
      "v_xor_b32 v102, 4096, v104\n\t"
      "v_xor_b32 v103, 6144, v104\n\t"
      "ds_read_b128 v[16:19], v101\n\t"
      "ds_read_b128 v[20:23], v101 offset:1024\n\t"
      "ds_read_b128 v[24:27], v100 offset:8192\n\t"
      "ds_read_b128 v[28:31], v100 offset:9216\n\t"
      "ds_read_b128 v[0:3], v102\n\t"
      "ds_read_b128 v[4:7], v102 offset:1024\n\t"
      "ds_read_b128 v[8:11], v100 offset:16384\n\t"
      "ds_read_b128 v[12:15], v100 offset:17408\n\t"
      "s_mov_b32 s28, s52\n\t"
      "s_add_u32 m0, s55, s71\n\t"
      "s_sub_u32 s71, 204800, s71\n\t"
      "s_add_u32 s40, s40, 1\n\t"
      "s_cmp_lt_u32 s40, 63\n\t"
      "s_cselect_b32 s61, 786432, 0\n\t"
      "s_cselect_b32 s60, s58, 0\n\t"
      "s_add_u32 s44, s44, s61\n\t"
      "s_cmp_lt_u32 s40, 62\n\t"
      "s_cselect_b32 s61, 786432, 0\n\t"
      "s_add_u32 s46, s46, s61\n\t"
      "s_add_u32 s61, s40, 3\n\t"
      "s_and_b32 s62, s61, 3\n\t"
      "s_cselect_b32 s62, 16, 2000\n\t"
      "s_cmp_lt_u32 s61, 64\n\t"
      "s_cselect_b32 s62, s62, 0\n\t"
      "s_add_u32 s50, s50, s62\n\t"
      "s_mov_b32 s63, 0x2000\n\t"
      "s_waitcnt lgkmcnt(8)\n\t"
      "v_mfma_f32_16x16x32_bf16 v[32:35], v[76:79], v[112:115], 0\n\t"
      "v_mfma_f32_16x16x32_bf16 v[36:39], v[80:83], v[112:115], 0\n\t"
      "v_mfma_f32_16x16x32_bf16 v[40:43], v[76:79], v[116:119], 0\n\t"
      "v_mfma_f32_16x16x32_bf16 v[44:47], v[80:83], v[116:119], 0\n\t"
      "ds_read_b128 v[76:79], v103\n\t"
      "ds_read_b128 v[80:83], v103 offset:1024\n\t"
      "ds_read_b128 v[112:115], v100 offset:24576\n\t"
      "ds_read_b128 v[116:119], v100 offset:25600\n\t"
      "s_waitcnt lgkmcnt(8)\n\t"
      "v_mfma_f32_16x16x32_bf16 v[32:35], v[16:19], v[24:27], v[32:35]\n\t"
      "v_mfma_f32_16x16x32_bf16 v[36:39], v[20:23], v[24:27], v[36:39]\n\t"
      "v_mfma_f32_16x16x32_bf16 v[40:43], v[16:19], v[28:31], v[40:43]\n\t"
      "v_mfma_f32_16x16x32_bf16 v[44:47], v[20:23], v[28:31], v[44:47]\n\t"
      "s_branch LGATE_0%=\n\t"
      "LFIRST_0%=:\n\t"
      "s_cmp_eq_u32 s40, 0\n\t"
      "s_cbranch_scc1 LWM0_0%=\n\t"
      "s_barrier\n\t"
      "ds_read_b32 v99, v109 offset:55392\n\t"
      "s_waitcnt lgkmcnt(0)\n\t"
      "v_readfirstlane_b32 s61, v99\n\t"
      "s_nop 1\n\t"
      "s_cmp_gt_i32 s61, 0\n\t"
      "s_cselect_b32 s99, 0x2000, 0\n\t"
      "s_xor_b32 s97, s97, s99\n\t"
      "s_barrier\n\t"
      "s_mov_b32 s63, 0x2000\n\t"
      "s_branch LPOLL_0%=\n\t"
      "LWM0_0%=:\n\t"
      "s_mov_b32 s63, 0x10000\n\t"
      "LWM_0%=:\n\t"
      "s_waitcnt vmcnt(0) lgkmcnt(0)\n\t"
      "v_bitop3_b32 v49, v49, v51, s53 bitop3:0x7e\n\t"
      "v_max_u32 v48, v48, v50\n\t"
      "v_cmp_ne_u32 vcc, 0, v49\n\t"
      "s_nop 1\n\t"
      "v_max_u32_dpp v48, v48, v48 quad_perm:[1,0,3,2] row_mask:0xf bank_mask:0xf\n\t"
      "s_nop 1\n\t"
      "v_max_u32_dpp v48, v48, v48 quad_perm:[2,3,0,1] row_mask:0xf bank_mask:0xf\n\t"
      "s_nop 1\n\t"
      "v_max_u32_dpp v48, v48, v48 row_ror:4 row_mask:0xf bank_mask:0xf\n\t"
      "s_nop 1\n\t"
      "v_max_u32_dpp v48, v48, v48 row_ror:8 row_mask:0xf bank_mask:0xf\n\t"
      "v_mov_b32 v49, v48\n\t"
      "s_nop 1\n\t"
      "v_permlane16_swap_b32 v48, v49\n\t"
      "v_max_u32 v48, v48, v49\n\t"
      "v_mov_b32 v49, v48\n\t"
      "s_nop 1\n\t"
      "v_permlane32_swap_b32 v48, v49\n\t"
      "v_max_u32 v48, v48, v49\n\t"
      "v_mul_f32 v49, 0x40a00000, v48\n\t"
      "s_nop 0\n\t"
      "v_readfirstlane_b32 s67, v49\n\t"
      "v_mul_f32 v49, 0x43030000, v49\n\t"
      "s_nop 0\n\t"
      "v_readfirstlane_b32 s66, v49\n\t"
      "s_cbranch_vccz LWM1_0%=\n\t"
      "s_sleep 4\n\t"
      "buffer_load_dwordx2 v[48:49], v52, s[36:39], s94 offen sc1\n\t"
      "buffer_load_dwordx2 v[50:51], v52, s[36:39], s94 offen offset:128 sc1\n\t"
      "s_sub_u32 s63, s63, 1\n\t"
      "s_cmp_lg_u32 s63, 0\n\t"
      "s_cbranch_scc1 LWM_0%=\n\t"
      "s_waitcnt vmcnt(0)\n\t"
      "LWM1_0%=:\n\t"
      "buffer_load_dwordx2 v[120:121], off, s[36:39], s64 sc1\n\t"
      "s_lshr_b32 s67, s67, 23\n\t"
      "s_add_u32 s67, s67, 5\n\t"
      "s_lshr_b32 s66, s66, 23\n\t"
      "s_add_u32 s66, s66, 129\n\t"
      "s_max_u32 s66, s66, 80\n\t"
      "s_mov_b32 s63, 0x2000\n\t"
      "LPOL0_0%=:\n\t"
      "s_waitcnt lgkmcnt(4)\n\t"
      "v_mfma_f32_16x16x32_bf16 v[32:35], v[0:3], v[8:11], v[32:35]\n\t"
      "v_mfma_f32_16x16x32_bf16 v[36:39], v[4:7], v[8:11], v[36:39]\n\t"
      "v_mfma_f32_16x16x32_bf16 v[40:43], v[0:3], v[12:15], v[40:43]\n\t"
      "v_mfma_f32_16x16x32_bf16 v[44:47], v[4:7], v[12:15], v[44:47]\n\t"
      "v_mov_b64 v[48:49], 0\n\t"
      "v_mov_b64 v[50:51], 0\n\t"
      "v_mov_b64 v[52:53], 0\n\t"
      "v_mov_b64 v[54:55], 0\n\t"
      "s_xor_b32 s69, s97, 1024\n\t"
      "s_xor_b32 s98, s97, 512\n\t"
      "s_xor_b32 s23, s97, 1536\n\t"
      "s_waitcnt lgkmcnt(0)\n\t"
      "v_mfma_f32_16x16x32_bf16 v[32:35], v[76:79], v[112:115], v[32:35]\n\t"
      "v_mfma_f32_16x16x32_bf16 v[36:39], v[80:83], v[112:115], v[36:39]\n\t"
      "v_mfma_f32_16x16x32_bf16 v[40:43], v[76:79], v[116:119], v[40:43]\n\t"
      "v_mfma_f32_16x16x32_bf16 v[44:47], v[80:83], v[116:119], v[44:47]\n\t"
      "s_mov_b64 exec, 1\n\t"
      "ds_write_b64 v109, v[110:111] offset:55392\n\t"
      "s_mov_b64 exec, -1\n\t"
      "s_nop 4\n\t"
      "v_cvt_pk_bf16_f32 v68, v32, v33\n\t"
      "v_cvt_pk_bf16_f32 v69, v34, v35\n\t"
      "v_cvt_pk_bf16_f32 v70, v36, v37\n\t"
      "v_cvt_pk_bf16_f32 v71, v38, v39\n\t"
      "v_cvt_pk_bf16_f32 v74, v40, v41\n\t"
      "v_cvt_pk_bf16_f32 v75, v42, v43\n\t"
      "v_cvt_pk_bf16_f32 v72, v44, v45\n\t"
      "v_cvt_pk_bf16_f32 v73, v46, v47\n\t"
      "v_mov_b64 v[32:33], 0\n\t"
      "v_mov_b64 v[34:35], 0\n\t"
      "v_mov_b64 v[36:37], 0\n\t"
      "v_mov_b64 v[38:39], 0\n\t"
      "v_mov_b64 v[40:41], 0\n\t"
      "v_mov_b64 v[42:43], 0\n\t"
      "v_mov_b64 v[44:45], 0\n\t"
      "v_mov_b64 v[46:47], 0\n\t"
      "s_waitcnt vmcnt(0)\n\t"
      "s_branch LGO_0%=\n\t"
      "LEARLY_0%=:\n\t"
      "s_cmp_eq_u32 s40, 54\n\t"
      "s_cbranch_scc1 LPW_0%=\n\t"
      "s_cmp_lt_i32 s40, 0\n\t"
      "s_waitcnt lgkmcnt(0)\n\t"
      "s_cbranch_scc1 LUTOK_0%=\n\t"
      "s_xor_b32 s43, s43, s99\n\t"
      "s_xor_b32 s68, s68, s99\n\t"
      "s_branch LROLE_0%=\n\t"
      "LPW_0%=:\n\t"
      "s_and_b32 s61, s51, 0x70c\n\t"
      "s_cmp_lg_u32 s61, 0x100\n\t"
      "s_cbranch_scc1 LROLE_0%=\n\t"
      "s_getpc_b64 s[92:93]\n\t"
      "LPX_0%=:\n\t"
      "s_add_u32 s92, s92, LCOLD%=-LPX_0%=+0\n\t"
      "s_addc_u32 s93, s93, (LCOLD%=-LPX_0%=+0)>>32\n\t"
      "v_lshlrev_b32 v99, 2, v127\n\t"
      "global_load_dword v98, v99, s[92:93]\n\t"
      "v_add_u32 v99, 4096, v99\n\t"
      "global_load_dword v98, v99, s[92:93]\n\t"
      "v_add_u32 v99, 4096, v99\n\t"
      "s_branch LROLE_0%=\n\t"
      "LSLOW_0%=:\n\t"
      "s_mov_b32 s63, 0x10000\n\t"
      "LSLOW1_0%=:\n\t"
      "buffer_load_dwordx2 v[92:93], off, s[36:39], s64 sc1\n\t"
      "s_waitcnt vmcnt(0)\n\t"
      "v_bitop3_b32 v92, v92, v93, s53 bitop3:0x7e\n\t"
      "s_nop 0\n\t"
      "v_readfirstlane_b32 s56, v92\n\t"
      "s_nop 1\n\t"
      "s_cmp_eq_u32 s56, 0\n\t"
      "s_cbranch_scc1 LSLOW2_0%=\n\t"
      "s_sleep 8\n\t"
      "s_sub_u32 s63, s63, 1\n\t"
      "s_cmp_lg_u32 s63, 0\n\t"
      "s_cbranch_scc1 LSLOW1_0%=\n\t"
      "LSLOW2_0%=:\n\t"
      "s_mov_b32 m0, s45\n\t"
      "s_sub_u32 s61, 204800, s71\n\t"
      "buffer_load_dwordx4 v65, s[36:39], s44 offen sc1 lds\n\t"
      "buffer_load_dwordx4 v65, s[36:39], s44 offen offset:1024 sc1 lds\n\t"
      "s_add_u32 m0, s55, s61\n\t"
      "s_nop 0\n\t"
      "buffer_load_dwordx4 v127, s[36:39], s46 offen sc1 lds\n\t"
      "buffer_load_dwordx4 v127, s[36:39], s46 offen offset:1024 sc1 lds\n\t"
      "s_branch LWIN1_0%=\n\t"
      "LMX_0%=:\n\t"
      "s_waitcnt lgkmcnt(0)\n\t"
      "s_nop 7\n\t"
      "v_smfmac_f32_16x16x64_bf16 v[52:55], v[20:23], v[232:239], v122\n\t"
      "v_smfmac_f32_16x16x64_bf16 v[52:55], v[24:27], v[240:247], v122\n\t"
      "v_smfmac_f32_16x16x64_bf16 v[52:55], v[28:31], v[248:255], v122\n\t"
      "s_nop 7\n\t"
      "v_add_f32 v53, v53, v55\n\t"
      "v_add_f32_e64 v52, v52, v54\n\t"
      "v_cvt_pk_bf16_f32 v66, v52, v53\n\t"
      "s_add_u32 s62, s61, 0x83400000\n\t"
      "v_mov_b32_e64 v126, s62\n\t"
      "v_max3_f32 v82, v112, v113, v114\n\t"
      "v_max_f32 v82, v82, v115\n\t"
      "s_nop 1\n\t"
      "v_max_f32_dpp v82, v82, v82 quad_perm:[2,3,0,1] row_mask:0xf bank_mask:0xf\n\t"
      "v_max3_f32 v83, v82, v120, v121\n\t"
      "v_max3_f32 v60, v82, v120, v121\n\t"
      "s_nop 1\n\t"
      "v_permlane16_swap_b32 v83, v60\n\t"
      "v_max_f32 v61, v83, v60\n\t"
      "v_max_f32 v60, v83, v60\n\t"
      "s_nop 1\n\t"
      "v_permlane32_swap_b32 v61, v60\n\t"
      "v_max_f32 v83, v61, v60\n\t"
      "v_sub_f32 v112, v112, v83\n\t"
      "v_sub_f32 v113, v113, v83\n\t"
      "v_sub_f32 v114, v114, v83\n\t"
      "v_sub_f32 v115, v115, v83\n\t"
      "v_sub_f32 v120, v120, v83\n\t"
      "v_sub_f32 v121, v121, v83\n\t"
      "v_exp_f32_e64 v56, v112\n\t"
      "v_exp_f32 v57, v113\n\t"
      "v_exp_f32_e64 v58, v114\n\t"
      "v_exp_f32 v59, v115\n\t"
      "v_exp_f32 v80, v120\n\t"
      "v_exp_f32 v81, v121\n\t"
      "v_cvt_pk_bf16_f32 v96, v56, v57\n\t"
      "v_cvt_pk_bf16_f32 v97, v58, v59\n\t"
      "v_cvt_pk_bf16_f32 v108, v80, v81\n\t"
      "v_mov_b32_dpp v98, v96 quad_perm:[2,3,0,1] row_mask:0xf bank_mask:0xf\n\t"
      "v_mov_b32_dpp v99, v97 quad_perm:[2,3,0,1] row_mask:0xf bank_mask:0xf\n\t"
      "v_mfma_f32_16x16x32_bf16 v[48:51], v[108:111], v[88:91], 0\n\t"
      "s_nop 0\n\t"
      "v_mfma_f32_16x16x32_bf16 v[48:51], v[96:99], v[88:91], v[48:51]\n\t"
      "v_mfma_f32_16x16x32_bf16 v[84:87], v[108:111], v[64:67], 0\n\t"
      "v_mfma_f32_16x16x32_bf16 v[84:87], v[96:99], v[68:71], v[84:87]\n\t"
      "v_mfma_f32_16x16x32_bf16 v[92:95], v[96:99], v[72:75], 0\n\t"
      "v_mfma_f32_16x16x32_bf16 v[92:95], v[108:111], v[66:69], v[92:95]\n\t"
      "s_nop 3\n\t"
      "v_rcp_f32_e64 v48, v48\n\t"
      "v_rcp_f32 v49, v49\n\t"
      "v_fma_f32 v84, v84, v48, s96\n\t"
      "buffer_atomic_add v84, v123, s[36:39], s97 offen\n\t"
      "v_fma_f32 v85, v85, v49, s96\n\t"
      "buffer_atomic_add v85, v123, s[36:39], s98 offen\n\t"
      "v_fma_f32 v94, v94, v48, s96\n\t"
      "buffer_atomic_add v94, v123, s[36:39], s69 offen\n\t"
      "v_fma_f32 v95, v95, v49, s96\n\t"
      "buffer_atomic_add v95, v123, s[36:39], s23 offen\n\t"
      "s_add_u32 s97, s97, 0x8000\n\t"
      "s_branch LWIN_0%=\n\t"
      "LR0_0%=:\n\t"
      "buffer_load_dwordx4 v[84:87], v60, s[36:39], s43 offen sc1\n\t"
      "buffer_load_dwordx4 v[88:91], v60, s[36:39], s43 offen offset:1024 sc1\n\t"
      "s_waitcnt vmcnt(2)\n\t"
      "buffer_load_dwordx4 v[92:95], v60, s[36:39], s68 offen sc1\n\t"
      "buffer_load_dwordx4 v[96:99], v60, s[36:39], s68 offen offset:1024 sc1\n\t"
      "s_sub_u32 s63, s63, 1\n\t"
      "s_mov_b64 exec, 1\n\t"
      "buffer_store_dword v109, off, s[36:39], s52 sc1\n\t"
      "s_mov_b64 exec, -1\n\t"
      "s_waitcnt vmcnt(3) lgkmcnt(0)\n\t"
      "v_bitop3_b32 v64, v84, v85, v86 bitop3:0x80\n\t"
      "v_bitop3_b32 v64, v64, v87, v88 bitop3:0x80\n\t"
      "v_bitop3_b32 v64, v64, v89, v90 bitop3:0x80\n\t"
      "v_bitop3_b32 v64, v64, v91, s58 bitop3:0x80\n\t"
      "v_cmp_eq_u32 vcc, 0, v64\n\t"
      "v_pk_fma_f32 v[4:5], v[84:85], s[56:57], v[126:127] op_sel:[0,1,0] op_sel_hi:[1,1,0]\n\t"
      "v_pk_fma_f32 v[6:7], v[86:87], s[56:57], v[126:127] op_sel:[0,1,0] op_sel_hi:[1,1,0]\n\t"
      "v_cvt_pk_bf16_f32 v0, v4, v5\n\t"
      "v_cvt_pk_bf16_f32 v1, v6, v7\n\t"
      "ds_write_b64 v127, v[0:1] offset:40960\n\t"
      "v_pk_fma_f32 v[8:9], v[88:89], s[56:57], v[126:127] op_sel:[0,1,0] op_sel_hi:[1,1,0]\n\t"
      "v_pk_fma_f32 v[10:11], v[90:91], s[56:57], v[126:127] op_sel:[0,1,0] op_sel_hi:[1,1,0]\n\t"
      "v_cvt_pk_bf16_f32 v2, v8, v9\n\t"
      "v_cvt_pk_bf16_f32 v3, v10, v11\n\t"
      "ds_write_b64 v127, v[2:3] offset:40968\n\t"
      "s_cmp_le_i32 s63, 0\n\t"
      "s_waitcnt lgkmcnt(0)\n\t"
      "s_cbranch_scc1 LRZ_0%=\n\t"
      "s_cbranch_vccnz LR0_0%=\n\t"
      "LRZ_0%=:\n\t"
      "v_mov_b64 v[32:33], 0\n\t"
      "v_mov_b64 v[34:35], 0\n\t"
      "v_mov_b64 v[36:37], 0\n\t"
      "v_mov_b64 v[38:39], 0\n\t"
      "v_mov_b64 v[40:41], 0\n\t"
      "v_mov_b64 v[42:43], 0\n\t"
      "v_mov_b64 v[44:45], 0\n\t"
      "v_mov_b64 v[46:47], 0\n\t"
      "v_mov_b64 v[48:49], 0\n\t"
      "v_mov_b64 v[50:51], 0\n\t"
      "v_mov_b64 v[52:53], 0\n\t"
      "v_mov_b64 v[54:55], 0\n\t"
      "v_mfma_f32_16x16x32_bf16 v[56:59], v[0:3], v[0:3], 0\n\t"
      "v_smfmac_f32_16x16x64_bf16 v[36:39], v[0:3], a[64:71], v122\n\t"
      "v_smfmac_f32_16x16x64_bf16 v[44:47], v[0:3], a[192:199], v122\n\t"
      "s_branch LC0_0%=\n\t"
      "LUTOK_1%=:\n\t"
      "v_add_u32 v104, s71, v100\n\t"
      "ds_read_b128 v[76:79], v104\n\t"
      "ds_read_b128 v[80:83], v104 offset:1024\n\t"
      "ds_read_b128 v[112:115], v100\n\t"
      "ds_read_b128 v[116:119], v100 offset:1024\n\t"
      "v_xor_b32 v101, 2048, v104\n\t"
      "v_xor_b32 v102, 4096, v104\n\t"
      "v_xor_b32 v103, 6144, v104\n\t"
      "ds_read_b128 v[16:19], v101\n\t"
      "ds_read_b128 v[20:23], v101 offset:1024\n\t"
      "ds_read_b128 v[24:27], v100 offset:8192\n\t"
      "ds_read_b128 v[28:31], v100 offset:9216\n\t"
      "ds_read_b128 v[0:3], v102\n\t"
      "ds_read_b128 v[4:7], v102 offset:1024\n\t"
      "ds_read_b128 v[8:11], v100 offset:16384\n\t"
      "ds_read_b128 v[12:15], v100 offset:17408\n\t"
      "s_mov_b32 s28, s52\n\t"
      "s_add_u32 m0, s55, s71\n\t"
      "s_sub_u32 s71, 204800, s71\n\t"
      "s_add_u32 s40, s40, 1\n\t"
      "s_cmp_lt_u32 s40, 63\n\t"
      "s_cselect_b32 s61, 786432, 0\n\t"
      "s_cselect_b32 s60, s58, 0\n\t"
      "s_add_u32 s44, s44, s61\n\t"
      "s_cmp_lt_u32 s40, 62\n\t"
      "s_cselect_b32 s61, 786432, 0\n\t"
      "s_add_u32 s46, s46, s61\n\t"
      "s_add_u32 s61, s40, 3\n\t"
      "s_and_b32 s62, s61, 3\n\t"
      "s_cselect_b32 s62, 16, 2000\n\t"
      "s_cmp_lt_u32 s61, 64\n\t"
      "s_cselect_b32 s62, s62, 0\n\t"
      "s_add_u32 s50, s50, s62\n\t"
      "s_mov_b32 s63, 0x2000\n\t"
      "s_waitcnt lgkmcnt(8)\n\t"
      "v_mfma_f32_16x16x32_bf16 v[32:35], v[76:79], v[112:115], 0\n\t"
      "v_mfma_f32_16x16x32_bf16 v[36:39], v[80:83], v[112:115], 0\n\t"
      "v_mfma_f32_16x16x32_bf16 v[40:43], v[76:79], v[116:119], 0\n\t"
      "v_mfma_f32_16x16x32_bf16 v[44:47], v[80:83], v[116:119], 0\n\t"
      "ds_read_b128 v[76:79], v103\n\t"
      "ds_read_b128 v[80:83], v103 offset:1024\n\t"
      "ds_read_b128 v[112:115], v100 offset:24576\n\t"
      "ds_read_b128 v[116:119], v100 offset:25600\n\t"
      "s_waitcnt lgkmcnt(8)\n\t"
      "v_mfma_f32_16x16x32_bf16 v[32:35], v[16:19], v[24:27], v[32:35]\n\t"
      "v_mfma_f32_16x16x32_bf16 v[36:39], v[20:23], v[24:27], v[36:39]\n\t"
      "v_mfma_f32_16x16x32_bf16 v[40:43], v[16:19], v[28:31], v[40:43]\n\t"
      "v_mfma_f32_16x16x32_bf16 v[44:47], v[20:23], v[28:31], v[44:47]\n\t"
      "s_branch LGATE_1%=\n\t"
      "LFIRST_1%=:\n\t"
      "s_cmp_eq_u32 s40, 0\n\t"
      "s_cbranch_scc1 LWM0_1%=\n\t"
      "s_barrier\n\t"
      "ds_read_b32 v99, v109 offset:55392\n\t"
      "s_waitcnt lgkmcnt(0)\n\t"
      "v_readfirstlane_b32 s61, v99\n\t"
      "s_nop 1\n\t"
      "s_cmp_gt_i32 s61, 0\n\t"
      "s_cselect_b32 s99, 0x2000, 0\n\t"
      "s_xor_b32 s97, s97, s99\n\t"
      "s_barrier\n\t"
      "s_mov_b32 s63, 0x2000\n\t"
      "s_branch LPOLL_1%=\n\t"
      "LWM0_1%=:\n\t"
      "s_mov_b32 s63, 0x10000\n\t"
      "LWM_1%=:\n\t"
      "s_waitcnt vmcnt(0) lgkmcnt(0)\n\t"
      "v_bitop3_b32 v49, v49, v51, s53 bitop3:0x7e\n\t"
      "v_max_u32 v48, v48, v50\n\t"
      "v_cmp_ne_u32 vcc, 0, v49\n\t"
      "s_nop 1\n\t"
      "v_max_u32_dpp v48, v48, v48 quad_perm:[1,0,3,2] row_mask:0xf bank_mask:0xf\n\t"
      "s_nop 1\n\t"
      "v_max_u32_dpp v48, v48, v48 quad_perm:[2,3,0,1] row_mask:0xf bank_mask:0xf\n\t"
      "s_nop 1\n\t"
      "v_max_u32_dpp v48, v48, v48 row_ror:4 row_mask:0xf bank_mask:0xf\n\t"
      "s_nop 1\n\t"
      "v_max_u32_dpp v48, v48, v48 row_ror:8 row_mask:0xf bank_mask:0xf\n\t"
      "v_mov_b32 v49, v48\n\t"
      "s_nop 1\n\t"
      "v_permlane16_swap_b32 v48, v49\n\t"
      "v_max_u32 v48, v48, v49\n\t"
      "v_mov_b32 v49, v48\n\t"
      "s_nop 1\n\t"
      "v_permlane32_swap_b32 v48, v49\n\t"
      "v_max_u32 v48, v48, v49\n\t"
      "v_mul_f32 v49, 0x40a00000, v48\n\t"
      "s_nop 0\n\t"
      "v_readfirstlane_b32 s67, v49\n\t"
      "v_mul_f32 v49, 0x43030000, v49\n\t"
      "s_nop 0\n\t"
      "v_readfirstlane_b32 s66, v49\n\t"
      "s_cbranch_vccz LWM1_1%=\n\t"
      "s_sleep 4\n\t"
      "buffer_load_dwordx2 v[48:49], v52, s[36:39], s94 offen sc1\n\t"
      "buffer_load_dwordx2 v[50:51], v52, s[36:39], s94 offen offset:128 sc1\n\t"
      "s_sub_u32 s63, s63, 1\n\t"
      "s_cmp_lg_u32 s63, 0\n\t"
      "s_cbranch_scc1 LWM_1%=\n\t"
      "s_waitcnt vmcnt(0)\n\t"
      "LWM1_1%=:\n\t"
      "buffer_load_dwordx2 v[120:121], off, s[36:39], s64 sc1\n\t"
      "s_lshr_b32 s67, s67, 23\n\t"
      "s_add_u32 s67, s67, 5\n\t"
      "s_lshr_b32 s66, s66, 23\n\t"
      "s_add_u32 s66, s66, 129\n\t"
      "s_max_u32 s66, s66, 80\n\t"
      "s_mov_b32 s63, 0x2000\n\t"
      "LPOL0_1%=:\n\t"
      "s_waitcnt lgkmcnt(4)\n\t"
      "v_mfma_f32_16x16x32_bf16 v[32:35], v[0:3], v[8:11], v[32:35]\n\t"
      "v_mfma_f32_16x16x32_bf16 v[36:39], v[4:7], v[8:11], v[36:39]\n\t"
      "v_mfma_f32_16x16x32_bf16 v[40:43], v[0:3], v[12:15], v[40:43]\n\t"
      "v_mfma_f32_16x16x32_bf16 v[44:47], v[4:7], v[12:15], v[44:47]\n\t"
      "v_mov_b64 v[48:49], 0\n\t"
      "v_mov_b64 v[50:51], 0\n\t"
      "v_mov_b64 v[52:53], 0\n\t"
      "v_mov_b64 v[54:55], 0\n\t"
      "s_xor_b32 s69, s97, 1024\n\t"
      "s_xor_b32 s98, s97, 512\n\t"
      "s_xor_b32 s23, s97, 1536\n\t"
      "s_waitcnt lgkmcnt(0)\n\t"
      "v_mfma_f32_16x16x32_bf16 v[32:35], v[76:79], v[112:115], v[32:35]\n\t"
      "v_mfma_f32_16x16x32_bf16 v[36:39], v[80:83], v[112:115], v[36:39]\n\t"
      "v_mfma_f32_16x16x32_bf16 v[40:43], v[76:79], v[116:119], v[40:43]\n\t"
      "v_mfma_f32_16x16x32_bf16 v[44:47], v[80:83], v[116:119], v[44:47]\n\t"
      "s_mov_b64 exec, 1\n\t"
      "ds_write_b64 v109, v[110:111] offset:55392\n\t"
      "s_mov_b64 exec, -1\n\t"
      "s_nop 4\n\t"
      "v_cvt_pk_bf16_f32 v68, v32, v33\n\t"
      "v_cvt_pk_bf16_f32 v69, v34, v35\n\t"
      "v_cvt_pk_bf16_f32 v70, v36, v37\n\t"
      "v_cvt_pk_bf16_f32 v71, v38, v39\n\t"
      "v_cvt_pk_bf16_f32 v74, v40, v41\n\t"
      "v_cvt_pk_bf16_f32 v75, v42, v43\n\t"
      "v_cvt_pk_bf16_f32 v72, v44, v45\n\t"
      "v_cvt_pk_bf16_f32 v73, v46, v47\n\t"
      "v_mov_b64 v[32:33], 0\n\t"
      "v_mov_b64 v[34:35], 0\n\t"
      "v_mov_b64 v[36:37], 0\n\t"
      "v_mov_b64 v[38:39], 0\n\t"
      "v_mov_b64 v[40:41], 0\n\t"
      "v_mov_b64 v[42:43], 0\n\t"
      "v_mov_b64 v[44:45], 0\n\t"
      "v_mov_b64 v[46:47], 0\n\t"
      "s_waitcnt vmcnt(0)\n\t"
      "s_branch LGO_1%=\n\t"
      "LEARLY_1%=:\n\t"
      "s_cmp_eq_u32 s40, 54\n\t"
      "s_cbranch_scc1 LROLE_1%=\n\t"
      "s_cmp_lt_i32 s40, 0\n\t"
      "s_waitcnt lgkmcnt(0)\n\t"
      "s_cbranch_scc1 LUTOK_1%=\n\t"
      "s_xor_b32 s43, s43, s99\n\t"
      "s_xor_b32 s68, s68, s99\n\t"
      "s_branch LROLE_1%=\n\t"
      "LSLOW_1%=:\n\t"
      "s_mov_b32 s63, 0x10000\n\t"
      "LSLOW1_1%=:\n\t"
      "buffer_load_dwordx2 v[92:93], off, s[36:39], s64 sc1\n\t"
      "s_waitcnt vmcnt(0)\n\t"
      "v_bitop3_b32 v92, v92, v93, s53 bitop3:0x7e\n\t"
      "s_nop 0\n\t"
      "v_readfirstlane_b32 s56, v92\n\t"
      "s_nop 1\n\t"
      "s_cmp_eq_u32 s56, 0\n\t"
      "s_cbranch_scc1 LSLOW2_1%=\n\t"
      "s_sleep 8\n\t"
      "s_sub_u32 s63, s63, 1\n\t"
      "s_cmp_lg_u32 s63, 0\n\t"
      "s_cbranch_scc1 LSLOW1_1%=\n\t"
      "LSLOW2_1%=:\n\t"
      "s_mov_b32 m0, s45\n\t"
      "s_sub_u32 s61, 204800, s71\n\t"
      "buffer_load_dwordx4 v65, s[36:39], s44 offen sc1 lds\n\t"
      "buffer_load_dwordx4 v65, s[36:39], s44 offen offset:1024 sc1 lds\n\t"
      "s_add_u32 m0, s55, s61\n\t"
      "s_nop 0\n\t"
      "buffer_load_dwordx4 v127, s[36:39], s46 offen sc1 lds\n\t"
      "buffer_load_dwordx4 v127, s[36:39], s46 offen offset:1024 sc1 lds\n\t"
      "s_branch LWIN1_1%=\n\t"
      "LMX_1%=:\n\t"
      "s_waitcnt lgkmcnt(0)\n\t"
      "s_nop 7\n\t"
      "v_smfmac_f32_16x16x64_bf16 v[52:55], v[16:19], v[224:231], v122\n\t"
      "v_smfmac_f32_16x16x64_bf16 v[52:55], v[28:31], v[248:255], v122\n\t"
      "v_smfmac_f32_16x16x64_bf16 v[52:55], v[24:27], v[240:247], v122\n\t"
      "s_nop 7\n\t"
      "v_add_f32 v53, v53, v55\n\t"
      "v_add_f32_e64 v52, v52, v54\n\t"
      "v_cvt_pk_bf16_f32 v66, v52, v53\n\t"
      "s_add_u32 s62, s61, 0x83400000\n\t"
      "v_mov_b32_e64 v126, s62\n\t"
      "v_max3_f32 v82, v112, v113, v114\n\t"
      "v_max_f32 v82, v82, v115\n\t"
      "s_nop 1\n\t"
      "v_max_f32_dpp v82, v82, v82 quad_perm:[2,3,0,1] row_mask:0xf bank_mask:0xf\n\t"
      "v_max3_f32 v83, v82, v120, v121\n\t"
      "v_max3_f32 v60, v82, v120, v121\n\t"
      "s_nop 1\n\t"
      "v_permlane16_swap_b32 v83, v60\n\t"
      "v_max_f32 v61, v83, v60\n\t"
      "v_max_f32 v60, v83, v60\n\t"
      "s_nop 1\n\t"
      "v_permlane32_swap_b32 v61, v60\n\t"
      "v_max_f32 v83, v61, v60\n\t"
      "v_sub_f32 v112, v112, v83\n\t"
      "v_sub_f32 v113, v113, v83\n\t"
      "v_sub_f32 v114, v114, v83\n\t"
      "v_sub_f32 v115, v115, v83\n\t"
      "v_sub_f32 v120, v120, v83\n\t"
      "v_sub_f32 v121, v121, v83\n\t"
      "v_exp_f32_e64 v56, v112\n\t"
      "v_exp_f32 v57, v113\n\t"
      "v_exp_f32_e64 v58, v114\n\t"
      "v_exp_f32 v59, v115\n\t"
      "v_exp_f32 v80, v120\n\t"
      "v_exp_f32 v81, v121\n\t"
      "v_cvt_pk_bf16_f32 v96, v56, v57\n\t"
      "v_cvt_pk_bf16_f32 v97, v58, v59\n\t"
      "v_cvt_pk_bf16_f32 v108, v80, v81\n\t"
      "v_mov_b32_dpp v98, v96 quad_perm:[2,3,0,1] row_mask:0xf bank_mask:0xf\n\t"
      "v_mov_b32_dpp v99, v97 quad_perm:[2,3,0,1] row_mask:0xf bank_mask:0xf\n\t"
      "v_mfma_f32_16x16x32_bf16 v[48:51], v[108:111], v[88:91], 0\n\t"
      "s_nop 0\n\t"
      "v_mfma_f32_16x16x32_bf16 v[48:51], v[96:99], v[88:91], v[48:51]\n\t"
      "v_mfma_f32_16x16x32_bf16 v[84:87], v[108:111], v[64:67], 0\n\t"
      "v_mfma_f32_16x16x32_bf16 v[84:87], v[96:99], v[68:71], v[84:87]\n\t"
      "v_mfma_f32_16x16x32_bf16 v[92:95], v[96:99], v[72:75], 0\n\t"
      "v_mfma_f32_16x16x32_bf16 v[92:95], v[108:111], v[66:69], v[92:95]\n\t"
      "s_nop 3\n\t"
      "v_rcp_f32_e64 v48, v48\n\t"
      "v_rcp_f32 v49, v49\n\t"
      "v_fma_f32 v84, v84, v48, s96\n\t"
      "buffer_atomic_add v84, v123, s[36:39], s97 offen\n\t"
      "v_fma_f32 v85, v85, v49, s96\n\t"
      "buffer_atomic_add v85, v123, s[36:39], s98 offen\n\t"
      "v_fma_f32 v94, v94, v48, s96\n\t"
      "buffer_atomic_add v94, v123, s[36:39], s69 offen\n\t"
      "v_fma_f32 v95, v95, v49, s96\n\t"
      "buffer_atomic_add v95, v123, s[36:39], s23 offen\n\t"
      "s_add_u32 s97, s97, 0x8000\n\t"
      "s_branch LWIN_1%=\n\t"
      "LR0_1%=:\n\t"
      "buffer_load_dwordx4 v[84:87], v60, s[36:39], s43 offen sc1\n\t"
      "buffer_load_dwordx4 v[88:91], v60, s[36:39], s43 offen offset:1024 sc1\n\t"
      "s_waitcnt vmcnt(2)\n\t"
      "buffer_load_dwordx4 v[92:95], v60, s[36:39], s68 offen sc1\n\t"
      "buffer_load_dwordx4 v[96:99], v60, s[36:39], s68 offen offset:1024 sc1\n\t"
      "s_sub_u32 s63, s63, 1\n\t"
      "s_mov_b64 exec, 1\n\t"
      "buffer_store_dword v109, off, s[36:39], s52 sc1\n\t"
      "s_mov_b64 exec, -1\n\t"
      "s_waitcnt vmcnt(3) lgkmcnt(0)\n\t"
      "v_bitop3_b32 v64, v84, v85, v86 bitop3:0x80\n\t"
      "v_bitop3_b32 v64, v64, v87, v88 bitop3:0x80\n\t"
      "v_bitop3_b32 v64, v64, v89, v90 bitop3:0x80\n\t"
      "v_bitop3_b32 v64, v64, v91, s58 bitop3:0x80\n\t"
      "v_cmp_eq_u32 vcc, 0, v64\n\t"
      "v_pk_fma_f32 v[0:1], v[84:85], s[56:57], v[126:127] op_sel:[0,1,0] op_sel_hi:[1,1,0]\n\t"
      "v_pk_fma_f32 v[2:3], v[86:87], s[56:57], v[126:127] op_sel:[0,1,0] op_sel_hi:[1,1,0]\n\t"
      "v_cvt_pk_bf16_f32 v4, v0, v1\n\t"
      "v_cvt_pk_bf16_f32 v5, v2, v3\n\t"
      "ds_write_b64 v127, v[4:5] offset:41984\n\t"
      "v_pk_fma_f32 v[12:13], v[88:89], s[56:57], v[126:127] op_sel:[0,1,0] op_sel_hi:[1,1,0]\n\t"
      "v_pk_fma_f32 v[14:15], v[90:91], s[56:57], v[126:127] op_sel:[0,1,0] op_sel_hi:[1,1,0]\n\t"
      "v_cvt_pk_bf16_f32 v6, v12, v13\n\t"
      "v_cvt_pk_bf16_f32 v7, v14, v15\n\t"
      "ds_write_b64 v127, v[6:7] offset:41992\n\t"
      "s_cmp_le_i32 s63, 0\n\t"
      "s_waitcnt lgkmcnt(0)\n\t"
      "s_cbranch_scc1 LRZ_1%=\n\t"
      "s_cbranch_vccnz LR0_1%=\n\t"
      "LRZ_1%=:\n\t"
      "v_mov_b64 v[32:33], 0\n\t"
      "v_mov_b64 v[34:35], 0\n\t"
      "v_mov_b64 v[36:37], 0\n\t"
      "v_mov_b64 v[38:39], 0\n\t"
      "v_mov_b64 v[40:41], 0\n\t"
      "v_mov_b64 v[42:43], 0\n\t"
      "v_mov_b64 v[44:45], 0\n\t"
      "v_mov_b64 v[46:47], 0\n\t"
      "v_mov_b64 v[48:49], 0\n\t"
      "v_mov_b64 v[50:51], 0\n\t"
      "v_mov_b64 v[52:53], 0\n\t"
      "v_mov_b64 v[54:55], 0\n\t"
      "v_mfma_f32_16x16x32_bf16 v[56:59], v[4:7], v[4:7], 0\n\t"
      "v_smfmac_f32_16x16x64_bf16 v[36:39], v[4:7], a[72:79], v122\n\t"
      "v_smfmac_f32_16x16x64_bf16 v[44:47], v[4:7], a[200:207], v122\n\t"
      "s_branch LC0_1%=\n\t"
      "LUTOK_2%=:\n\t"
      "v_add_u32 v104, s71, v100\n\t"
      "ds_read_b128 v[76:79], v104\n\t"
      "ds_read_b128 v[80:83], v104 offset:1024\n\t"
      "ds_read_b128 v[112:115], v100\n\t"
      "ds_read_b128 v[116:119], v100 offset:1024\n\t"
      "v_xor_b32 v101, 2048, v104\n\t"
      "v_xor_b32 v102, 4096, v104\n\t"
      "v_xor_b32 v103, 6144, v104\n\t"
      "ds_read_b128 v[0:3], v101\n\t"
      "ds_read_b128 v[4:7], v101 offset:1024\n\t"
      "ds_read_b128 v[8:11], v100 offset:8192\n\t"
      "ds_read_b128 v[12:15], v100 offset:9216\n\t"
      "ds_read_b128 v[16:19], v102\n\t"
      "ds_read_b128 v[20:23], v102 offset:1024\n\t"
      "ds_read_b128 v[24:27], v100 offset:16384\n\t"
      "ds_read_b128 v[28:31], v100 offset:17408\n\t"
      "s_add_u32 m0, s55, s71\n\t"
      "s_sub_u32 s71, 204800, s71\n\t"
      "s_add_u32 s40, s40, 1\n\t"
      "s_cmp_lt_u32 s40, 63\n\t"
      "s_cselect_b32 s61, 786432, 0\n\t"
      "s_cselect_b32 s60, s58, 0\n\t"
      "s_add_u32 s44, s44, s61\n\t"
      "s_cmp_lt_u32 s40, 62\n\t"
      "s_cselect_b32 s61, 786432, 0\n\t"
      "s_add_u32 s46, s46, s61\n\t"
      "s_add_u32 s61, s40, 3\n\t"
      "s_and_b32 s62, s61, 3\n\t"
      "s_cselect_b32 s62, 16, 2000\n\t"
      "s_cmp_lt_u32 s61, 64\n\t"
      "s_cselect_b32 s62, s62, 0\n\t"
      "s_add_u32 s50, s50, s62\n\t"
      "s_mov_b32 s63, 0x2000\n\t"
      "s_waitcnt lgkmcnt(8)\n\t"
      "v_mfma_f32_16x16x32_bf16 v[32:35], v[76:79], v[112:115], 0\n\t"
      "v_mfma_f32_16x16x32_bf16 v[36:39], v[80:83], v[112:115], 0\n\t"
      "v_mfma_f32_16x16x32_bf16 v[40:43], v[76:79], v[116:119], 0\n\t"
      "v_mfma_f32_16x16x32_bf16 v[44:47], v[80:83], v[116:119], 0\n\t"
      "ds_read_b128 v[76:79], v103\n\t"
      "ds_read_b128 v[80:83], v103 offset:1024\n\t"
      "ds_read_b128 v[112:115], v100 offset:24576\n\t"
      "ds_read_b128 v[116:119], v100 offset:25600\n\t"
      "s_branch LGATE_2%=\n\t"
      "LFIRST_2%=:\n\t"
      "s_cmp_eq_u32 s40, 0\n\t"
      "s_cbranch_scc1 LWM0_2%=\n\t"
      "s_barrier\n\t"
      "ds_read_b32 v99, v109 offset:55392\n\t"
      "s_waitcnt lgkmcnt(0)\n\t"
      "v_readfirstlane_b32 s61, v99\n\t"
      "s_nop 1\n\t"
      "s_cmp_gt_i32 s61, 0\n\t"
      "s_cselect_b32 s99, 0x2000, 0\n\t"
      "s_xor_b32 s97, s97, s99\n\t"
      "s_barrier\n\t"
      "s_mov_b32 s63, 0x2000\n\t"
      "s_branch LPOLL_2%=\n\t"
      "LWM0_2%=:\n\t"
      "s_mov_b32 s63, 0x10000\n\t"
      "LWM_2%=:\n\t"
      "s_waitcnt vmcnt(0) lgkmcnt(0)\n\t"
      "v_bitop3_b32 v49, v49, v51, s53 bitop3:0x7e\n\t"
      "v_max_u32 v48, v48, v50\n\t"
      "v_cmp_ne_u32 vcc, 0, v49\n\t"
      "s_nop 1\n\t"
      "v_max_u32_dpp v48, v48, v48 quad_perm:[1,0,3,2] row_mask:0xf bank_mask:0xf\n\t"
      "s_nop 1\n\t"
      "v_max_u32_dpp v48, v48, v48 quad_perm:[2,3,0,1] row_mask:0xf bank_mask:0xf\n\t"
      "s_nop 1\n\t"
      "v_max_u32_dpp v48, v48, v48 row_ror:4 row_mask:0xf bank_mask:0xf\n\t"
      "s_nop 1\n\t"
      "v_max_u32_dpp v48, v48, v48 row_ror:8 row_mask:0xf bank_mask:0xf\n\t"
      "v_mov_b32 v49, v48\n\t"
      "s_nop 1\n\t"
      "v_permlane16_swap_b32 v48, v49\n\t"
      "v_max_u32 v48, v48, v49\n\t"
      "v_mov_b32 v49, v48\n\t"
      "s_nop 1\n\t"
      "v_permlane32_swap_b32 v48, v49\n\t"
      "v_max_u32 v48, v48, v49\n\t"
      "v_mul_f32 v49, 0x40a00000, v48\n\t"
      "s_nop 0\n\t"
      "v_readfirstlane_b32 s67, v49\n\t"
      "v_mul_f32 v49, 0x43030000, v49\n\t"
      "s_nop 0\n\t"
      "v_readfirstlane_b32 s66, v49\n\t"
      "s_cbranch_vccz LWM1_2%=\n\t"
      "s_sleep 4\n\t"
      "buffer_load_dwordx2 v[48:49], v52, s[36:39], s94 offen sc1\n\t"
      "buffer_load_dwordx2 v[50:51], v52, s[36:39], s94 offen offset:128 sc1\n\t"
      "s_sub_u32 s63, s63, 1\n\t"
      "s_cmp_lg_u32 s63, 0\n\t"
      "s_cbranch_scc1 LWM_2%=\n\t"
      "s_waitcnt vmcnt(0)\n\t"
      "LWM1_2%=:\n\t"
      "buffer_load_dwordx2 v[120:121], off, s[36:39], s64 sc1\n\t"
      "s_lshr_b32 s67, s67, 23\n\t"
      "s_add_u32 s67, s67, 5\n\t"
      "s_lshr_b32 s66, s66, 23\n\t"
      "s_add_u32 s66, s66, 129\n\t"
      "s_max_u32 s66, s66, 80\n\t"
      "s_mov_b32 s63, 0x2000\n\t"
      "LPOL0_2%=:\n\t"
      "s_waitcnt lgkmcnt(8)\n\t"
      "v_mfma_f32_16x16x32_bf16 v[32:35], v[0:3], v[8:11], v[32:35]\n\t"
      "v_mfma_f32_16x16x32_bf16 v[36:39], v[4:7], v[8:11], v[36:39]\n\t"
      "v_mfma_f32_16x16x32_bf16 v[40:43], v[0:3], v[12:15], v[40:43]\n\t"
      "v_mfma_f32_16x16x32_bf16 v[44:47], v[4:7], v[12:15], v[44:47]\n\t"
      "v_mov_b64 v[48:49], 0\n\t"
      "v_mov_b64 v[50:51], 0\n\t"
      "v_mov_b64 v[52:53], 0\n\t"
      "v_mov_b64 v[54:55], 0\n\t"
      "s_xor_b32 s69, s97, 1024\n\t"
      "s_xor_b32 s98, s97, 512\n\t"
      "s_xor_b32 s23, s97, 1536\n\t"
      "s_waitcnt lgkmcnt(4)\n\t"
      "v_mfma_f32_16x16x32_bf16 v[32:35], v[16:19], v[24:27], v[32:35]\n\t"
      "v_mfma_f32_16x16x32_bf16 v[36:39], v[20:23], v[24:27], v[36:39]\n\t"
      "v_mfma_f32_16x16x32_bf16 v[40:43], v[16:19], v[28:31], v[40:43]\n\t"
      "v_mfma_f32_16x16x32_bf16 v[44:47], v[20:23], v[28:31], v[44:47]\n\t"
      "s_waitcnt lgkmcnt(0)\n\t"
      "v_mfma_f32_16x16x32_bf16 v[32:35], v[76:79], v[112:115], v[32:35]\n\t"
      "v_mfma_f32_16x16x32_bf16 v[36:39], v[80:83], v[112:115], v[36:39]\n\t"
      "v_mfma_f32_16x16x32_bf16 v[40:43], v[76:79], v[116:119], v[40:43]\n\t"
      "v_mfma_f32_16x16x32_bf16 v[44:47], v[80:83], v[116:119], v[44:47]\n\t"
      "s_mov_b64 exec, 1\n\t"
      "ds_write_b64 v109, v[110:111] offset:55392\n\t"
      "s_mov_b64 exec, -1\n\t"
      "s_nop 4\n\t"
      "v_cvt_pk_bf16_f32 v68, v32, v33\n\t"
      "v_cvt_pk_bf16_f32 v69, v34, v35\n\t"
      "v_cvt_pk_bf16_f32 v70, v36, v37\n\t"
      "v_cvt_pk_bf16_f32 v71, v38, v39\n\t"
      "v_cvt_pk_bf16_f32 v74, v40, v41\n\t"
      "v_cvt_pk_bf16_f32 v75, v42, v43\n\t"
      "v_cvt_pk_bf16_f32 v72, v44, v45\n\t"
      "v_cvt_pk_bf16_f32 v73, v46, v47\n\t"
      "v_mov_b64 v[32:33], 0\n\t"
      "v_mov_b64 v[34:35], 0\n\t"
      "v_mov_b64 v[36:37], 0\n\t"
      "v_mov_b64 v[38:39], 0\n\t"
      "v_mov_b64 v[40:41], 0\n\t"
      "v_mov_b64 v[42:43], 0\n\t"
      "v_mov_b64 v[44:45], 0\n\t"
      "v_mov_b64 v[46:47], 0\n\t"
      "s_waitcnt vmcnt(0)\n\t"
      "s_branch LGO_2%=\n\t"
      "LEARLY_2%=:\n\t"
      "s_cmp_eq_u32 s40, 54\n\t"
      "s_cbranch_scc1 LROLE_2%=\n\t"
      "s_cmp_lt_i32 s40, 0\n\t"
      "s_waitcnt lgkmcnt(0)\n\t"
      "s_cbranch_scc1 LUTOK_2%=\n\t"
      "s_xor_b32 s43, s43, s99\n\t"
      "s_xor_b32 s68, s68, s99\n\t"
      "s_branch LROLE_2%=\n\t"
      "LSLOW_2%=:\n\t"
      "s_mov_b32 s63, 0x10000\n\t"
      "LSLOW1_2%=:\n\t"
      "buffer_load_dwordx2 v[92:93], off, s[36:39], s64 sc1\n\t"
      "s_waitcnt vmcnt(0)\n\t"
      "v_bitop3_b32 v92, v92, v93, s53 bitop3:0x7e\n\t"
      "s_nop 0\n\t"
      "v_readfirstlane_b32 s56, v92\n\t"
      "s_nop 1\n\t"
      "s_cmp_eq_u32 s56, 0\n\t"
      "s_cbranch_scc1 LSLOW2_2%=\n\t"
      "s_sleep 8\n\t"
      "s_sub_u32 s63, s63, 1\n\t"
      "s_cmp_lg_u32 s63, 0\n\t"
      "s_cbranch_scc1 LSLOW1_2%=\n\t"
      "LSLOW2_2%=:\n\t"
      "s_mov_b32 m0, s45\n\t"
      "s_sub_u32 s61, 204800, s71\n\t"
      "buffer_load_dwordx4 v65, s[36:39], s44 offen sc1 lds\n\t"
      "buffer_load_dwordx4 v65, s[36:39], s44 offen offset:1024 sc1 lds\n\t"
      "s_add_u32 m0, s55, s61\n\t"
      "s_nop 0\n\t"
      "buffer_load_dwordx4 v127, s[36:39], s46 offen sc1 lds\n\t"
      "buffer_load_dwordx4 v127, s[36:39], s46 offen offset:1024 sc1 lds\n\t"
      "s_branch LWIN1_2%=\n\t"
      "LMX_2%=:\n\t"
      "s_waitcnt lgkmcnt(0)\n\t"
      "s_nop 7\n\t"
      "v_smfmac_f32_16x16x64_bf16 v[52:55], v[28:31], v[248:255], v122\n\t"
      "v_smfmac_f32_16x16x64_bf16 v[52:55], v[16:19], v[224:231], v122\n\t"
      "v_smfmac_f32_16x16x64_bf16 v[52:55], v[20:23], v[232:239], v122\n\t"
      "s_nop 7\n\t"
      "v_add_f32 v53, v53, v55\n\t"
      "v_add_f32_e64 v52, v52, v54\n\t"
      "v_cvt_pk_bf16_f32 v66, v52, v53\n\t"
      "s_add_u32 s62, s61, 0x83400000\n\t"
      "v_mov_b32_e64 v126, s62\n\t"
      "v_max3_f32 v82, v112, v113, v114\n\t"
      "v_max_f32 v82, v82, v115\n\t"
      "s_nop 1\n\t"
      "v_max_f32_dpp v82, v82, v82 quad_perm:[2,3,0,1] row_mask:0xf bank_mask:0xf\n\t"
      "v_max3_f32 v83, v82, v120, v121\n\t"
      "v_max3_f32 v60, v82, v120, v121\n\t"
      "s_nop 1\n\t"
      "v_permlane16_swap_b32 v83, v60\n\t"
      "v_max_f32 v61, v83, v60\n\t"
      "v_max_f32 v60, v83, v60\n\t"
      "s_nop 1\n\t"
      "v_permlane32_swap_b32 v61, v60\n\t"
      "v_max_f32 v83, v61, v60\n\t"
      "v_sub_f32 v112, v112, v83\n\t"
      "v_sub_f32 v113, v113, v83\n\t"
      "v_sub_f32 v114, v114, v83\n\t"
      "v_sub_f32 v115, v115, v83\n\t"
      "v_sub_f32 v120, v120, v83\n\t"
      "v_sub_f32 v121, v121, v83\n\t"
      "v_exp_f32_e64 v56, v112\n\t"
      "v_exp_f32 v57, v113\n\t"
      "v_exp_f32_e64 v58, v114\n\t"
      "v_exp_f32 v59, v115\n\t"
      "v_exp_f32 v80, v120\n\t"
      "v_exp_f32 v81, v121\n\t"
      "v_cvt_pk_bf16_f32 v96, v56, v57\n\t"
      "v_cvt_pk_bf16_f32 v97, v58, v59\n\t"
      "v_cvt_pk_bf16_f32 v108, v80, v81\n\t"
      "v_mov_b32_dpp v98, v96 quad_perm:[2,3,0,1] row_mask:0xf bank_mask:0xf\n\t"
      "v_mov_b32_dpp v99, v97 quad_perm:[2,3,0,1] row_mask:0xf bank_mask:0xf\n\t"
      "v_mfma_f32_16x16x32_bf16 v[48:51], v[108:111], v[88:91], 0\n\t"
      "s_nop 0\n\t"
      "v_mfma_f32_16x16x32_bf16 v[48:51], v[96:99], v[88:91], v[48:51]\n\t"
      "v_mfma_f32_16x16x32_bf16 v[84:87], v[108:111], v[64:67], 0\n\t"
      "v_mfma_f32_16x16x32_bf16 v[84:87], v[96:99], v[68:71], v[84:87]\n\t"
      "v_mfma_f32_16x16x32_bf16 v[92:95], v[96:99], v[72:75], 0\n\t"
      "v_mfma_f32_16x16x32_bf16 v[92:95], v[108:111], v[66:69], v[92:95]\n\t"
      "s_nop 3\n\t"
      "v_rcp_f32_e64 v48, v48\n\t"
      "v_rcp_f32 v49, v49\n\t"
      "v_fma_f32 v84, v84, v48, s96\n\t"
      "buffer_atomic_add v84, v123, s[36:39], s97 offen\n\t"
      "v_fma_f32 v85, v85, v49, s96\n\t"
      "buffer_atomic_add v85, v123, s[36:39], s98 offen\n\t"
      "v_fma_f32 v94, v94, v48, s96\n\t"
      "buffer_atomic_add v94, v123, s[36:39], s69 offen\n\t"
      "v_fma_f32 v95, v95, v49, s96\n\t"
      "buffer_atomic_add v95, v123, s[36:39], s23 offen\n\t"
      "s_add_u32 s97, s97, 0x8000\n\t"
      "s_branch LWIN_2%=\n\t"
      "LR0_2%=:\n\t"
      "buffer_load_dwordx4 v[84:87], v60, s[36:39], s43 offen sc1\n\t"
      "buffer_load_dwordx4 v[88:91], v60, s[36:39], s43 offen offset:1024 sc1\n\t"
      "s_waitcnt vmcnt(2)\n\t"
      "buffer_load_dwordx4 v[92:95], v60, s[36:39], s68 offen sc1\n\t"
      "buffer_load_dwordx4 v[96:99], v60, s[36:39], s68 offen offset:1024 sc1\n\t"
      "s_sub_u32 s63, s63, 1\n\t"
      "s_waitcnt vmcnt(2) lgkmcnt(0)\n\t"
      "v_bitop3_b32 v64, v84, v85, v86 bitop3:0x80\n\t"
      "v_bitop3_b32 v64, v64, v87, v88 bitop3:0x80\n\t"
      "v_bitop3_b32 v64, v64, v89, v90 bitop3:0x80\n\t"
      "v_bitop3_b32 v64, v64, v91, s58 bitop3:0x80\n\t"
      "v_cmp_eq_u32 vcc, 0, v64\n\t"
      "v_pk_fma_f32 v[12:13], v[84:85], s[56:57], v[126:127] op_sel:[0,1,0] op_sel_hi:[1,1,0]\n\t"
      "v_pk_fma_f32 v[14:15], v[86:87], s[56:57], v[126:127] op_sel:[0,1,0] op_sel_hi:[1,1,0]\n\t"
      "v_cvt_pk_bf16_f32 v8, v12, v13\n\t"
      "v_cvt_pk_bf16_f32 v9, v14, v15\n\t"
      "ds_write_b64 v127, v[8:9] offset:43008\n\t"
      "v_pk_fma_f32 v[0:1], v[88:89], s[56:57], v[126:127] op_sel:[0,1,0] op_sel_hi:[1,1,0]\n\t"
      "v_pk_fma_f32 v[2:3], v[90:91], s[56:57], v[126:127] op_sel:[0,1,0] op_sel_hi:[1,1,0]\n\t"
      "v_cvt_pk_bf16_f32 v10, v0, v1\n\t"
      "v_cvt_pk_bf16_f32 v11, v2, v3\n\t"
      "ds_write_b64 v127, v[10:11] offset:43016\n\t"
      "s_cmp_le_i32 s63, 0\n\t"
      "s_waitcnt lgkmcnt(0)\n\t"
      "s_cbranch_scc1 LRZ_2%=\n\t"
      "s_cbranch_vccnz LR0_2%=\n\t"
      "LRZ_2%=:\n\t"
      "v_mov_b64 v[32:33], 0\n\t"
      "v_mov_b64 v[34:35], 0\n\t"
      "v_mov_b64 v[36:37], 0\n\t"
      "v_mov_b64 v[38:39], 0\n\t"
      "v_mov_b64 v[40:41], 0\n\t"
      "v_mov_b64 v[42:43], 0\n\t"
      "v_mov_b64 v[44:45], 0\n\t"
      "v_mov_b64 v[46:47], 0\n\t"
      "v_mov_b64 v[48:49], 0\n\t"
      "v_mov_b64 v[50:51], 0\n\t"
      "v_mov_b64 v[52:53], 0\n\t"
      "v_mov_b64 v[54:55], 0\n\t"
      "v_mfma_f32_16x16x32_bf16 v[56:59], v[8:11], v[8:11], 0\n\t"
      "v_smfmac_f32_16x16x64_bf16 v[36:39], v[8:11], a[80:87], v122\n\t"
      "v_smfmac_f32_16x16x64_bf16 v[44:47], v[8:11], a[208:215], v122\n\t"
      "s_branch LC0_2%=\n\t"
      "LUTOK_3%=:\n\t"
      "v_add_u32 v104, s71, v100\n\t"
      "ds_read_b128 v[76:79], v104\n\t"
      "ds_read_b128 v[80:83], v104 offset:1024\n\t"
      "ds_read_b128 v[112:115], v100\n\t"
      "ds_read_b128 v[116:119], v100 offset:1024\n\t"
      "v_xor_b32 v101, 2048, v104\n\t"
      "v_xor_b32 v102, 4096, v104\n\t"
      "v_xor_b32 v103, 6144, v104\n\t"
      "ds_read_b128 v[0:3], v101\n\t"
      "ds_read_b128 v[4:7], v101 offset:1024\n\t"
      "ds_read_b128 v[8:11], v100 offset:8192\n\t"
      "ds_read_b128 v[12:15], v100 offset:9216\n\t"
      "ds_read_b128 v[16:19], v102\n\t"
      "ds_read_b128 v[20:23], v102 offset:1024\n\t"
      "ds_read_b128 v[24:27], v100 offset:16384\n\t"
      "ds_read_b128 v[28:31], v100 offset:17408\n\t"
      "s_sub_u32 s61, s88, s78\n\t"
      "s_sub_i32 s61, s61, 560\n\t"
      "s_lshr_b32 s62, s25, 3\n\t"
      "s_sub_u32 s25, s25, s62\n\t"
      "s_sub_i32 s25, s25, 24\n\t"
      "s_max_i32 s25, s25, 0\n\t"
      "s_sub_i32 s62, s61, 96\n\t"
      "s_max_i32 s62, s62, 0\n\t"
      "s_min_i32 s62, s62, 320\n\t"
      "s_lshr_b32 s62, s62, 2\n\t"
      "s_add_u32 s25, s25, s62\n\t"
      "s_min_i32 s61, s61, 96\n\t"
      "s_max_i32 s61, s61, -96\n\t"
      "s_ashr_i32 s61, s61, 3\n\t"
      "s_add_u32 s26, s26, s61\n\t"
      "s_max_u32 s26, s26, 1500\n\t"
      "s_min_u32 s26, s26, 2300\n\t"
      "s_sub_u32 s62, s88, s27\n\t"
      "s_mov_b32 s27, s88\n\t"
      "s_sub_i32 s62, s62, s24\n\t"
      "s_cmp_gt_i32 s62, 1150\n\t"
      "s_cselect_b32 s62, 200, 0\n\t"
      "s_max_u32 s25, s25, s62\n\t"
      "s_sub_u32 s61, s76, s28\n\t"
      "s_sub_u32 s62, s80, s82\n\t"
      "s_mov_b32 s28, s76\n\t"
      "s_mov_b32 s82, s80\n\t"
      "s_lshl_b32 s61, s61, 8\n\t"
      "s_mul_i32 s62, s62, s83\n\t"
      "s_sub_i32 s61, s61, s62\n\t"
      "s_ashr_i32 s61, s61, 10\n\t"
      "s_min_i32 s61, s61, 100\n\t"
      "s_max_i32 s61, s61, -100\n\t"
      "s_add_u32 s83, s83, s61\n\t"
      "s_mul_i32 s24, s26, s83\n\t"
      "s_lshr_b32 s24, s24, 12\n\t"
      "s_add_u32 s24, s24, s25\n\t"
      "s_min_u32 s61, s30, 300\n\t"
      "s_sub_u32 s30, s30, s61\n\t"
      "s_sub_u32 s24, s24, s61\n\t"
      "s_add_u32 m0, s55, s71\n\t"
      "s_sub_u32 s71, 204800, s71\n\t"
      "s_add_u32 s40, s40, 1\n\t"
      "s_cmp_lt_u32 s40, 63\n\t"
      "s_cselect_b32 s61, 786432, 0\n\t"
      "s_cselect_b32 s60, s58, 0\n\t"
      "s_add_u32 s44, s44, s61\n\t"
      "s_cmp_lt_u32 s40, 62\n\t"
      "s_cselect_b32 s61, 786432, 0\n\t"
      "s_add_u32 s46, s46, s61\n\t"
      "s_add_u32 s61, s40, 3\n\t"
      "s_and_b32 s62, s61, 3\n\t"
      "s_cselect_b32 s62, 16, 2000\n\t"
      "s_cmp_lt_u32 s61, 64\n\t"
      "s_cselect_b32 s62, s62, 0\n\t"
      "s_add_u32 s50, s50, s62\n\t"
      "s_mov_b32 s63, 0x2000\n\t"
      "s_waitcnt lgkmcnt(8)\n\t"
      "v_mfma_f32_16x16x32_bf16 v[32:35], v[76:79], v[112:115], 0\n\t"
      "v_mfma_f32_16x16x32_bf16 v[36:39], v[80:83], v[112:115], 0\n\t"
      "v_mfma_f32_16x16x32_bf16 v[40:43], v[76:79], v[116:119], 0\n\t"
      "v_mfma_f32_16x16x32_bf16 v[44:47], v[80:83], v[116:119], 0\n\t"
      "ds_read_b128 v[76:79], v103\n\t"
      "ds_read_b128 v[80:83], v103 offset:1024\n\t"
      "ds_read_b128 v[112:115], v100 offset:24576\n\t"
      "ds_read_b128 v[116:119], v100 offset:25600\n\t"
      "s_branch LGATE_3%=\n\t"
      "LFIRST_3%=:\n\t"
      "s_cmp_eq_u32 s40, 0\n\t"
      "s_cbranch_scc1 LWM0_3%=\n\t"
      "s_mov_b32 s63, 0x10000\n\t"
      "LST0_3%=:\n\t"
      "s_waitcnt vmcnt(0)\n\t"
      "v_bitop3_b32 v97, v57, v85, s53 bitop3:0x7e\n\t"
      "v_cmp_ne_u32 vcc, 0, v97\n\t"
      "v_add_u32 v99, v58, v86\n\t"
      "v_subrev_u32 v97, s87, v56\n\t"
      "v_subrev_u32 v98, s87, v84\n\t"
      "v_max_i32 v97, v97, v98\n\t"
      "s_cbranch_vccz LST3_3%=\n\t"
      "buffer_load_dwordx4 v[56:59], v96, s[36:39], s86 offen sc1\n\t"
      "buffer_load_dwordx4 v[84:87], v96, s[36:39], s86 offen offset:128 sc1\n\t"
      "s_sub_u32 s63, s63, 1\n\t"
      "s_cmp_eq_u32 s63, 0\n\t"
      "s_cbranch_scc0 LST0_3%=\n\t"
      "s_waitcnt vmcnt(0)\n\t"
      "LST3_3%=:\n\t"
      "s_nop 1\n\t"
      "v_add_u32_dpp v99, v99, v99 quad_perm:[1,0,3,2] row_mask:0xf bank_mask:0xf\n\t"
      "s_nop 1\n\t"
      "v_add_u32_dpp v99, v99, v99 quad_perm:[2,3,0,1] row_mask:0xf bank_mask:0xf\n\t"
      "s_nop 1\n\t"
      "v_add_u32_dpp v99, v99, v99 row_ror:4 row_mask:0xf bank_mask:0xf\n\t"
      "s_nop 1\n\t"
      "v_add_u32_dpp v99, v99, v99 row_ror:8 row_mask:0xf bank_mask:0xf\n\t"
      "v_mov_b32 v98, v99\n\t"
      "s_nop 1\n\t"
      "v_permlane16_swap_b32 v99, v98\n\t"
      "v_add_u32 v99, v99, v98\n\t"
      "v_mov_b32 v98, v99\n\t"
      "s_nop 1\n\t"
      "v_permlane32_swap_b32 v99, v98\n\t"
      "v_add_u32 v99, v99, v98\n\t"
      "ds_write_b32 v109, v99 offset:55392\n\t"
      "s_nop 1\n\t"
      "v_max_i32_dpp v97, v97, v97 quad_perm:[1,0,3,2] row_mask:0xf bank_mask:0xf\n\t"
      "s_nop 1\n\t"
      "v_max_i32_dpp v97, v97, v97 quad_perm:[2,3,0,1] row_mask:0xf bank_mask:0xf\n\t"
      "s_nop 1\n\t"
      "v_max_i32_dpp v97, v97, v97 row_ror:4 row_mask:0xf bank_mask:0xf\n\t"
      "s_nop 1\n\t"
      "v_max_i32_dpp v97, v97, v97 row_ror:8 row_mask:0xf bank_mask:0xf\n\t"
      "v_mov_b32 v98, v97\n\t"
      "s_nop 1\n\t"
      "v_permlane16_swap_b32 v97, v98\n\t"
      "v_max_i32 v97, v97, v98\n\t"
      "v_mov_b32 v98, v97\n\t"
      "s_nop 1\n\t"
      "v_permlane32_swap_b32 v97, v98\n\t"
      "v_max_i32 v97, v97, v98\n\t"
      "v_max_u32 v99, v59, v87\n\t"
      "s_nop 1\n\t"
      "v_max_u32_dpp v99, v99, v99 quad_perm:[1,0,3,2] row_mask:0xf bank_mask:0xf\n\t"
      "s_nop 1\n\t"
      "v_max_u32_dpp v99, v99, v99 quad_perm:[2,3,0,1] row_mask:0xf bank_mask:0xf\n\t"
      "s_nop 1\n\t"
      "v_max_u32_dpp v99, v99, v99 row_ror:4 row_mask:0xf bank_mask:0xf\n\t"
      "s_nop 1\n\t"
      "v_max_u32_dpp v99, v99, v99 row_ror:8 row_mask:0xf bank_mask:0xf\n\t"
      "v_mov_b32 v98, v99\n\t"
      "s_nop 1\n\t"
      "v_permlane16_swap_b32 v99, v98\n\t"
      "v_max_u32 v99, v99, v98\n\t"
      "v_mov_b32 v98, v99\n\t"
      "s_nop 1\n\t"
      "v_permlane32_swap_b32 v99, v98\n\t"
      "v_max_u32 v99, v99, v98\n\t"
      "v_rcp_f32 v99, v99\n\t"
      "s_nop 0\n\t"
      "v_mul_f32 v99, 0x41800000, v99\n\t"
      "s_mov_b32 s25, 0\n\t"
      "v_mul_f32 v99, 0x4519c000, v99\n\t"
      "v_cvt_u32_f32 v99, v99\n\t"
      "s_nop 0\n\t"
      "v_readfirstlane_b32 s26, v99\n\t"
      "s_waitcnt lgkmcnt(0)\n\t"
      "s_barrier\n\t"
      "ds_read_b32 v99, v109 offset:55392\n\t"
      "v_readfirstlane_b32 s62, v97\n\t"
      "s_waitcnt lgkmcnt(0)\n\t"
      "v_readfirstlane_b32 s61, v99\n\t"
      "s_nop 1\n\t"
      "s_cmp_gt_i32 s61, 0\n\t"
      "s_cselect_b32 s99, 0x2000, 0\n\t"
      "s_xor_b32 s97, s97, s99\n\t"
      "s_min_u32 s62, s62, 500\n\t"
      "s_add_u32 s62, s62, 200\n\t"
      "LST1_3%=:\n\t"
      "s_memrealtime s[92:93]\n\t"
      "s_waitcnt lgkmcnt(0)\n\t"
      "s_sub_u32 s61, s92, s87\n\t"
      "s_cmp_lt_u32 s61, s62\n\t"
      "s_cbranch_scc1 LST1_3%=\n\t"
      "s_sub_u32 s61, s61, s62\n\t"
      "s_mul_i32 s30, s61, 21\n\t"
      "s_barrier\n\t"
      "s_mov_b32 s63, 0x2000\n\t"
      "s_branch LPOLL_3%=\n\t"
      "LWM0_3%=:\n\t"
      "s_mov_b32 s63, 0x10000\n\t"
      "LWM_3%=:\n\t"
      "s_waitcnt vmcnt(0) lgkmcnt(0)\n\t"
      "v_bitop3_b32 v49, v49, v51, s53 bitop3:0x7e\n\t"
      "v_max_u32 v48, v48, v50\n\t"
      "v_cmp_ne_u32 vcc, 0, v49\n\t"
      "s_nop 1\n\t"
      "v_max_u32_dpp v48, v48, v48 quad_perm:[1,0,3,2] row_mask:0xf bank_mask:0xf\n\t"
      "s_nop 1\n\t"
      "v_max_u32_dpp v48, v48, v48 quad_perm:[2,3,0,1] row_mask:0xf bank_mask:0xf\n\t"
      "s_nop 1\n\t"
      "v_max_u32_dpp v48, v48, v48 row_ror:4 row_mask:0xf bank_mask:0xf\n\t"
      "s_nop 1\n\t"
      "v_max_u32_dpp v48, v48, v48 row_ror:8 row_mask:0xf bank_mask:0xf\n\t"
      "v_mov_b32 v49, v48\n\t"
      "s_nop 1\n\t"
      "v_permlane16_swap_b32 v48, v49\n\t"
      "v_max_u32 v48, v48, v49\n\t"
      "v_mov_b32 v49, v48\n\t"
      "s_nop 1\n\t"
      "v_permlane32_swap_b32 v48, v49\n\t"
      "v_max_u32 v48, v48, v49\n\t"
      "v_mul_f32 v49, 0x40a00000, v48\n\t"
      "s_nop 0\n\t"
      "v_readfirstlane_b32 s67, v49\n\t"
      "v_mul_f32 v49, 0x43030000, v49\n\t"
      "s_nop 0\n\t"
      "v_readfirstlane_b32 s66, v49\n\t"
      "s_cbranch_vccz LWM1_3%=\n\t"
      "s_sleep 4\n\t"
      "buffer_load_dwordx2 v[48:49], v52, s[36:39], s94 offen sc1\n\t"
      "buffer_load_dwordx2 v[50:51], v52, s[36:39], s94 offen offset:128 sc1\n\t"
      "s_sub_u32 s63, s63, 1\n\t"
      "s_cmp_lg_u32 s63, 0\n\t"
      "s_cbranch_scc1 LWM_3%=\n\t"
      "s_waitcnt vmcnt(0)\n\t"
      "LWM1_3%=:\n\t"
      "buffer_load_dwordx2 v[120:121], off, s[36:39], s64 sc1\n\t"
      "s_lshr_b32 s67, s67, 23\n\t"
      "s_add_u32 s67, s67, 5\n\t"
      "s_lshr_b32 s66, s66, 23\n\t"
      "s_add_u32 s66, s66, 129\n\t"
      "s_max_u32 s66, s66, 80\n\t"
      "s_mov_b32 s63, 0x2000\n\t"
      "LPOL0_3%=:\n\t"
      "s_memtime s[78:79]\n\t"
      "s_waitcnt lgkmcnt(8)\n\t"
      "v_mfma_f32_16x16x32_bf16 v[32:35], v[0:3], v[8:11], v[32:35]\n\t"
      "v_mfma_f32_16x16x32_bf16 v[36:39], v[4:7], v[8:11], v[36:39]\n\t"
      "v_mfma_f32_16x16x32_bf16 v[40:43], v[0:3], v[12:15], v[40:43]\n\t"
      "v_mfma_f32_16x16x32_bf16 v[44:47], v[4:7], v[12:15], v[44:47]\n\t"
      "v_mov_b64 v[48:49], 0\n\t"
      "v_mov_b64 v[50:51], 0\n\t"
      "v_mov_b64 v[52:53], 0\n\t"
      "v_mov_b64 v[54:55], 0\n\t"
      "s_xor_b32 s69, s97, 1024\n\t"
      "s_xor_b32 s98, s97, 512\n\t"
      "s_xor_b32 s23, s97, 1536\n\t"
      "s_waitcnt lgkmcnt(4)\n\t"
      "v_mfma_f32_16x16x32_bf16 v[32:35], v[16:19], v[24:27], v[32:35]\n\t"
      "v_mfma_f32_16x16x32_bf16 v[36:39], v[20:23], v[24:27], v[36:39]\n\t"
      "v_mfma_f32_16x16x32_bf16 v[40:43], v[16:19], v[28:31], v[40:43]\n\t"
      "v_mfma_f32_16x16x32_bf16 v[44:47], v[20:23], v[28:31], v[44:47]\n\t"
      "s_waitcnt lgkmcnt(0)\n\t"
      "v_mfma_f32_16x16x32_bf16 v[32:35], v[76:79], v[112:115], v[32:35]\n\t"
      "v_mfma_f32_16x16x32_bf16 v[36:39], v[80:83], v[112:115], v[36:39]\n\t"
      "v_mfma_f32_16x16x32_bf16 v[40:43], v[76:79], v[116:119], v[40:43]\n\t"
      "v_mfma_f32_16x16x32_bf16 v[44:47], v[80:83], v[116:119], v[44:47]\n\t"
      "s_mov_b64 exec, 1\n\t"
      "ds_write_b64 v109, v[110:111] offset:55392\n\t"
      "s_mov_b64 exec, -1\n\t"
      "s_nop 4\n\t"
      "v_cvt_pk_bf16_f32 v68, v32, v33\n\t"
      "v_cvt_pk_bf16_f32 v69, v34, v35\n\t"
      "v_cvt_pk_bf16_f32 v70, v36, v37\n\t"
      "v_cvt_pk_bf16_f32 v71, v38, v39\n\t"
      "v_cvt_pk_bf16_f32 v74, v40, v41\n\t"
      "v_cvt_pk_bf16_f32 v75, v42, v43\n\t"
      "v_cvt_pk_bf16_f32 v72, v44, v45\n\t"
      "v_cvt_pk_bf16_f32 v73, v46, v47\n\t"
      "v_mov_b64 v[32:33], 0\n\t"
      "v_mov_b64 v[34:35], 0\n\t"
      "v_mov_b64 v[36:37], 0\n\t"
      "v_mov_b64 v[38:39], 0\n\t"
      "v_mov_b64 v[40:41], 0\n\t"
      "v_mov_b64 v[42:43], 0\n\t"
      "v_mov_b64 v[44:45], 0\n\t"
      "v_mov_b64 v[46:47], 0\n\t"
      "s_waitcnt vmcnt(0)\n\t"
      "s_branch LGO_3%=\n\t"
      "LEARLY_3%=:\n\t"
      "s_cmp_eq_u32 s40, 54\n\t"
      "s_cbranch_scc1 LROLE_3%=\n\t"
      "s_cmp_lt_i32 s40, 0\n\t"
      "s_waitcnt lgkmcnt(0)\n\t"
      "s_cbranch_scc1 LSTART_3%=\n\t"
      "s_xor_b32 s43, s43, s99\n\t"
      "s_xor_b32 s68, s68, s99\n\t"
      "s_cmp_lg_u32 s40, 0\n\t"
      "s_cbranch_scc1 LROLE_3%=\n\t"
      "v_lshlrev_b32 v96, 4, v127\n\t"
      "buffer_load_dwordx4 v[56:59], v96, s[36:39], s86 offen sc1\n\t"
      "buffer_load_dwordx4 v[84:87], v96, s[36:39], s86 offen offset:128 sc1\n\t"
      "s_branch LROLE_3%=\n\t"
      "LSTART_3%=:\n\t"
      "s_memrealtime s[92:93]\n\t"
      "s_memtime s[90:91]\n\t"
      "s_mov_b64 exec, 1\n\t"
      "v_mov_b32 v57, s53\n\t"
      "v_mov_b32 v58, s65\n\t"
      "s_add_u32 s86, s94, 208640\n\t"
      "s_add_u32 s61, s95, 208640\n\t"
      "s_waitcnt lgkmcnt(0)\n\t"
      "v_mov_b32 v56, s92\n\t"
      "s_mov_b32 s87, s92\n\t"
      "s_sub_u32 s80, s90, s23\n\t"
      "s_sub_u32 s82, s92, s82\n\t"
      "v_cvt_f32_u32 v59, s82\n\t"
      "v_cvt_f32_u32 v63, s80\n\t"
      "v_rcp_f32 v59, v59\n\t"
      "s_nop 0\n\t"
      "v_mul_f32 v59, v59, v63\n\t"
      "v_mul_f32 v63, 0x43800000, v59\n\t"
      "v_cvt_u32_f32 v63, v63\n\t"
      "s_nop 0\n\t"
      "v_readfirstlane_b32 s83, v63\n\t"
      "s_mov_b32 s76, s90\n\t"
      "s_mov_b32 s80, s92\n\t"
      "s_sub_u32 s28, s90, 2100\n\t"
      "s_sub_u32 s82, s92, 100\n\t"
      "s_mov_b32 s26, 1800\n\t"
      "buffer_store_dwordx4 v[56:59], off, s[36:39], s61 sc1\n\t"
      "s_mov_b64 exec, -1\n\t"
      "s_branch LUTOK_3%=\n\t"
      "LSLOW_3%=:\n\t"
      "s_mov_b32 s63, 0x10000\n\t"
      "LSLOW1_3%=:\n\t"
      "buffer_load_dwordx2 v[92:93], off, s[36:39], s64 sc1\n\t"
      "s_waitcnt vmcnt(0)\n\t"
      "v_bitop3_b32 v92, v92, v93, s53 bitop3:0x7e\n\t"
      "s_nop 0\n\t"
      "v_readfirstlane_b32 s56, v92\n\t"
      "s_nop 1\n\t"
      "s_cmp_eq_u32 s56, 0\n\t"
      "s_cbranch_scc1 LSLOW2_3%=\n\t"
      "s_sleep 8\n\t"
      "s_sub_u32 s63, s63, 1\n\t"
      "s_cmp_lg_u32 s63, 0\n\t"
      "s_cbranch_scc1 LSLOW1_3%=\n\t"
      "LSLOW2_3%=:\n\t"
      "s_mov_b32 m0, s45\n\t"
      "s_sub_u32 s61, 204800, s71\n\t"
      "buffer_load_dwordx4 v65, s[36:39], s44 offen sc1 lds\n\t"
      "buffer_load_dwordx4 v65, s[36:39], s44 offen offset:1024 sc1 lds\n\t"
      "s_add_u32 m0, s55, s61\n\t"
      "s_nop 0\n\t"
      "buffer_load_dwordx4 v127, s[36:39], s46 offen sc1 lds\n\t"
      "buffer_load_dwordx4 v127, s[36:39], s46 offen offset:1024 sc1 lds\n\t"
      "s_branch LWIN1_3%=\n\t"
      "LMX_3%=:\n\t"
      "s_waitcnt lgkmcnt(0)\n\t"
      "s_nop 7\n\t"
      "v_smfmac_f32_16x16x64_bf16 v[52:55], v[24:27], v[240:247], v122\n\t"
      "v_smfmac_f32_16x16x64_bf16 v[52:55], v[20:23], v[232:239], v122\n\t"
      "v_smfmac_f32_16x16x64_bf16 v[52:55], v[16:19], v[224:231], v122\n\t"
      "s_nop 7\n\t"
      "v_add_f32 v53, v53, v55\n\t"
      "v_add_f32_e64 v52, v52, v54\n\t"
      "v_cvt_pk_bf16_f32 v66, v52, v53\n\t"
      "s_add_u32 s62, s61, 0x83400000\n\t"
      "v_mov_b32_e64 v126, s62\n\t"
      "v_max3_f32 v82, v112, v113, v114\n\t"
      "v_max_f32 v82, v82, v115\n\t"
      "s_nop 1\n\t"
      "v_max_f32_dpp v82, v82, v82 quad_perm:[2,3,0,1] row_mask:0xf bank_mask:0xf\n\t"
      "v_max3_f32 v83, v82, v120, v121\n\t"
      "v_max3_f32 v60, v82, v120, v121\n\t"
      "s_nop 1\n\t"
      "v_permlane16_swap_b32 v83, v60\n\t"
      "v_max_f32 v61, v83, v60\n\t"
      "v_max_f32 v60, v83, v60\n\t"
      "s_nop 1\n\t"
      "v_permlane32_swap_b32 v61, v60\n\t"
      "v_max_f32 v83, v61, v60\n\t"
      "v_sub_f32 v112, v112, v83\n\t"
      "v_sub_f32 v113, v113, v83\n\t"
      "v_sub_f32 v114, v114, v83\n\t"
      "v_sub_f32 v115, v115, v83\n\t"
      "v_sub_f32 v120, v120, v83\n\t"
      "v_sub_f32 v121, v121, v83\n\t"
      "v_exp_f32_e64 v56, v112\n\t"
      "v_exp_f32 v57, v113\n\t"
      "v_exp_f32_e64 v58, v114\n\t"
      "v_exp_f32 v59, v115\n\t"
      "v_exp_f32 v80, v120\n\t"
      "v_exp_f32 v81, v121\n\t"
      "v_cvt_pk_bf16_f32 v96, v56, v57\n\t"
      "v_cvt_pk_bf16_f32 v97, v58, v59\n\t"
      "v_cvt_pk_bf16_f32 v108, v80, v81\n\t"
      "v_mov_b32_dpp v98, v96 quad_perm:[2,3,0,1] row_mask:0xf bank_mask:0xf\n\t"
      "v_mov_b32_dpp v99, v97 quad_perm:[2,3,0,1] row_mask:0xf bank_mask:0xf\n\t"
      "v_mfma_f32_16x16x32_bf16 v[48:51], v[108:111], v[88:91], 0\n\t"
      "s_nop 0\n\t"
      "v_mfma_f32_16x16x32_bf16 v[48:51], v[96:99], v[88:91], v[48:51]\n\t"
      "v_mfma_f32_16x16x32_bf16 v[84:87], v[108:111], v[64:67], 0\n\t"
      "v_mfma_f32_16x16x32_bf16 v[84:87], v[96:99], v[68:71], v[84:87]\n\t"
      "v_mfma_f32_16x16x32_bf16 v[92:95], v[96:99], v[72:75], 0\n\t"
      "v_mfma_f32_16x16x32_bf16 v[92:95], v[108:111], v[66:69], v[92:95]\n\t"
      "s_nop 3\n\t"
      "v_rcp_f32_e64 v48, v48\n\t"
      "v_rcp_f32 v49, v49\n\t"
      "v_fma_f32 v84, v84, v48, s96\n\t"
      "buffer_atomic_add v84, v123, s[36:39], s97 offen\n\t"
      "v_fma_f32 v85, v85, v49, s96\n\t"
      "buffer_atomic_add v85, v123, s[36:39], s98 offen\n\t"
      "v_fma_f32 v94, v94, v48, s96\n\t"
      "buffer_atomic_add v94, v123, s[36:39], s69 offen\n\t"
      "v_fma_f32 v95, v95, v49, s96\n\t"
      "buffer_atomic_add v95, v123, s[36:39], s23 offen\n\t"
      "s_add_u32 s97, s97, 0x8000\n\t"
      "s_branch LWIN_3%=\n\t"
      "LR0_3%=:\n\t"
      "buffer_load_dwordx4 v[84:87], v60, s[36:39], s43 offen sc1\n\t"
      "buffer_load_dwordx4 v[88:91], v60, s[36:39], s43 offen offset:1024 sc1\n\t"
      "s_waitcnt vmcnt(2)\n\t"
      "buffer_load_dwordx4 v[92:95], v60, s[36:39], s68 offen sc1\n\t"
      "buffer_load_dwordx4 v[96:99], v60, s[36:39], s68 offen offset:1024 sc1\n\t"
      "s_sub_u32 s63, s63, 1\n\t"
      "s_waitcnt vmcnt(2) lgkmcnt(0)\n\t"
      "v_bitop3_b32 v64, v84, v85, v86 bitop3:0x80\n\t"
      "v_bitop3_b32 v64, v64, v87, v88 bitop3:0x80\n\t"
      "v_bitop3_b32 v64, v64, v89, v90 bitop3:0x80\n\t"
      "v_bitop3_b32 v64, v64, v91, s58 bitop3:0x80\n\t"
      "v_cmp_eq_u32 vcc, 0, v64\n\t"
      "v_pk_fma_f32 v[8:9], v[84:85], s[56:57], v[126:127] op_sel:[0,1,0] op_sel_hi:[1,1,0]\n\t"
      "v_pk_fma_f32 v[10:11], v[86:87], s[56:57], v[126:127] op_sel:[0,1,0] op_sel_hi:[1,1,0]\n\t"
      "v_cvt_pk_bf16_f32 v12, v8, v9\n\t"
      "v_cvt_pk_bf16_f32 v13, v10, v11\n\t"
      "ds_write_b64 v127, v[12:13] offset:44032\n\t"
      "v_pk_fma_f32 v[4:5], v[88:89], s[56:57], v[126:127] op_sel:[0,1,0] op_sel_hi:[1,1,0]\n\t"
      "v_pk_fma_f32 v[6:7], v[90:91], s[56:57], v[126:127] op_sel:[0,1,0] op_sel_hi:[1,1,0]\n\t"
      "v_cvt_pk_bf16_f32 v14, v4, v5\n\t"
      "v_cvt_pk_bf16_f32 v15, v6, v7\n\t"
      "ds_write_b64 v127, v[14:15] offset:44040\n\t"
      "s_memtime s[88:89]\n\t"
      "s_cmp_le_i32 s63, 0\n\t"
      "s_waitcnt lgkmcnt(0)\n\t"
      "s_cbranch_scc1 LRZ_3%=\n\t"
      "s_cbranch_vccnz LR0_3%=\n\t"
      "LRZ_3%=:\n\t"
      "s_sub_u32 s88, s88, 0\n\t"
      "v_mov_b64 v[32:33], 0\n\t"
      "v_mov_b64 v[34:35], 0\n\t"
      "v_mov_b64 v[36:37], 0\n\t"
      "v_mov_b64 v[38:39], 0\n\t"
      "v_mov_b64 v[40:41], 0\n\t"
      "v_mov_b64 v[42:43], 0\n\t"
      "v_mov_b64 v[44:45], 0\n\t"
      "v_mov_b64 v[46:47], 0\n\t"
      "v_mov_b64 v[48:49], 0\n\t"
      "v_mov_b64 v[50:51], 0\n\t"
      "v_mov_b64 v[52:53], 0\n\t"
      "v_mov_b64 v[54:55], 0\n\t"
      "v_mfma_f32_16x16x32_bf16 v[56:59], v[12:15], v[12:15], 0\n\t"
      "v_smfmac_f32_16x16x64_bf16 v[36:39], v[12:15], a[88:95], v122\n\t"
      "v_smfmac_f32_16x16x64_bf16 v[44:47], v[12:15], a[216:223], v122\n\t"
      "s_branch LC0_3%=\n\t"
      "LCOLD%=:\n\t"
      "LR1_0%=:\n\t"
      "v_bitop3_b32 v64, v92, v93, v94 bitop3:0x80\n\t"
      "v_bitop3_b32 v64, v64, v95, v96 bitop3:0x80\n\t"
      "v_bitop3_b32 v64, v64, v97, v98 bitop3:0x80\n\t"
      "v_bitop3_b32 v64, v64, v99, s58 bitop3:0x80\n\t"
      "v_cmp_eq_u32 vcc, 0, v64\n\t"
      "s_nop 0\n\t"
      "s_cbranch_vccz LRX_0%=\n\t"
      "LRY_0%=:\n\t"
      "buffer_load_dwordx4 v[92:95], v60, s[36:39], s68 offen sc1\n\t"
      "buffer_load_dwordx4 v[96:99], v60, s[36:39], s68 offen offset:1024 sc1\n\t"
      "s_sub_u32 s63, s63, 1\n\t"
      "s_waitcnt vmcnt(0) lgkmcnt(0)\n\t"
      "v_bitop3_b32 v64, v92, v93, v94 bitop3:0x80\n\t"
      "v_bitop3_b32 v64, v64, v95, v96 bitop3:0x80\n\t"
      "v_bitop3_b32 v64, v64, v97, v98 bitop3:0x80\n\t"
      "v_bitop3_b32 v64, v64, v99, s58 bitop3:0x80\n\t"
      "v_cmp_eq_u32 vcc, 0, v64\n\t"
      "v_pk_fma_f32 v[20:21], v[92:93], s[56:57], v[126:127] op_sel:[0,1,0] op_sel_hi:[1,1,0]\n\t"
      "v_pk_fma_f32 v[22:23], v[94:95], s[56:57], v[126:127] op_sel:[0,1,0] op_sel_hi:[1,1,0]\n\t"
      "v_cvt_pk_bf16_f32 v16, v20, v21\n\t"
      "v_cvt_pk_bf16_f32 v17, v22, v23\n\t"
      "ds_write_b64 v127, v[16:17] offset:45056\n\t"
      "v_pk_fma_f32 v[24:25], v[96:97], s[56:57], v[126:127] op_sel:[0,1,0] op_sel_hi:[1,1,0]\n\t"
      "v_pk_fma_f32 v[26:27], v[98:99], s[56:57], v[126:127] op_sel:[0,1,0] op_sel_hi:[1,1,0]\n\t"
      "v_cvt_pk_bf16_f32 v18, v24, v25\n\t"
      "v_cvt_pk_bf16_f32 v19, v26, v27\n\t"
      "ds_write_b64 v127, v[18:19] offset:45064\n\t"
      "s_cmp_le_i32 s63, 0\n\t"
      "s_waitcnt lgkmcnt(0)\n\t"
      "s_cbranch_scc1 LRX_0%=\n\t"
      "s_cbranch_vccnz LRY_0%=\n\t"
      "LRX_0%=:\n\t"
      "s_cmp_eq_u32 s40, 63\n\t"
      "s_cbranch_scc1 LLAST_0%=\n\t"
      "s_branch LC1_0%=\n\t"
      "LLAST_0%=:\n\t"
      "s_waitcnt vmcnt(0) lgkmcnt(0)\n\t"
      "s_barrier\n\t"
      "ds_read_b128 v[20:23], v127 offset:46080\n\t"
      "ds_read_b128 v[24:27], v127 offset:47104\n\t"
      "ds_read_b128 v[28:31], v127 offset:48128\n\t"
      "v_smfmac_f32_16x16x64_bf16 v[40:43], v[16:19], a[160:167], v122\n\t"
      "v_smfmac_f32_16x16x64_bf16 v[44:47], v[16:19], a[224:231], v122\n\t"
      "s_waitcnt lgkmcnt(2)\n\t"
      "v_smfmac_f32_16x16x64_bf16 v[40:43], v[20:23], a[168:175], v122\n\t"
      "v_smfmac_f32_16x16x64_bf16 v[44:47], v[20:23], a[232:239], v122\n\t"
      "s_waitcnt lgkmcnt(1)\n\t"
      "v_smfmac_f32_16x16x64_bf16 v[44:47], v[24:27], a[240:247], v122\n\t"
      "v_smfmac_f32_16x16x64_bf16 v[40:43], v[24:27], a[176:183], v122\n\t"
      "s_waitcnt lgkmcnt(0)\n\t"
      "v_smfmac_f32_16x16x64_bf16 v[44:47], v[28:31], a[248:255], v122\n\t"
      "v_smfmac_f32_16x16x64_bf16 v[40:43], v[28:31], a[184:191], v122\n\t"
      "s_nop 7\n\t"
      "v_add_f32 v40, v40, v42\n\t"
      "v_add_f32 v41, v41, v43\n\t"
      "v_cvt_pk_bf16_f32 v120, v40, v41\n\t"
      "v_add_f32 v44, v44, v46\n\t"
      "v_add_f32 v45, v45, v47\n\t"
      "v_cvt_pk_bf16_f32 v121, v44, v45\n\t"
      "ds_write_b64 v125, v[120:121] offset:2080\n\t"
      "s_waitcnt lgkmcnt(0)\n\t"
      "s_barrier\n\t"
      "ds_read_b64_tr_b16 v[40:41], v124 offset:51232\n\t"
      "ds_read_b64_tr_b16 v[42:43], v124 offset:51752\n\t"
      "ds_read_b64_tr_b16 v[44:45], v124 offset:52272\n\t"
      "ds_read_b64_tr_b16 v[46:47], v124 offset:52792\n\t"
      "v_add_u32 v100, s55, v127\n\t"
      "ds_read_b128 v[60:63], v100 offset:57344\n\t"
      "s_waitcnt lgkmcnt(0)\n\t"
      "s_branch LROLE_0%=\n\t"
      "LFIN_0%=:\n\t"
      "s_waitcnt lgkmcnt(0)\n\t"
      "s_barrier\n\t"
      "ds_read_b64 v[54:55], v124 offset:53312\n\t"
      "s_waitcnt lgkmcnt(0)\n\t"
      "v_add_f32 v48, v48, v54\n\t"
      "v_add_f32 v49, v49, v55\n\t"
      "v_cvt_pk_bf16_f32 v48, v48, v49\n\t"
      "buffer_store_dword v48, v62, s[36:39], s28 offen sc1\n\t"
      "s_branch LEND%=\n\t"
      "LR1_1%=:\n\t"
      "v_bitop3_b32 v64, v92, v93, v94 bitop3:0x80\n\t"
      "v_bitop3_b32 v64, v64, v95, v96 bitop3:0x80\n\t"
      "v_bitop3_b32 v64, v64, v97, v98 bitop3:0x80\n\t"
      "v_bitop3_b32 v64, v64, v99, s58 bitop3:0x80\n\t"
      "v_cmp_eq_u32 vcc, 0, v64\n\t"
      "s_nop 0\n\t"
      "s_cbranch_vccz LRX_1%=\n\t"
      "LRY_1%=:\n\t"
      "buffer_load_dwordx4 v[92:95], v60, s[36:39], s68 offen sc1\n\t"
      "buffer_load_dwordx4 v[96:99], v60, s[36:39], s68 offen offset:1024 sc1\n\t"
      "s_sub_u32 s63, s63, 1\n\t"
      "s_waitcnt vmcnt(0) lgkmcnt(0)\n\t"
      "v_bitop3_b32 v64, v92, v93, v94 bitop3:0x80\n\t"
      "v_bitop3_b32 v64, v64, v95, v96 bitop3:0x80\n\t"
      "v_bitop3_b32 v64, v64, v97, v98 bitop3:0x80\n\t"
      "v_bitop3_b32 v64, v64, v99, s58 bitop3:0x80\n\t"
      "v_cmp_eq_u32 vcc, 0, v64\n\t"
      "v_pk_fma_f32 v[16:17], v[92:93], s[56:57], v[126:127] op_sel:[0,1,0] op_sel_hi:[1,1,0]\n\t"
      "v_pk_fma_f32 v[18:19], v[94:95], s[56:57], v[126:127] op_sel:[0,1,0] op_sel_hi:[1,1,0]\n\t"
      "v_cvt_pk_bf16_f32 v20, v16, v17\n\t"
      "v_cvt_pk_bf16_f32 v21, v18, v19\n\t"
      "ds_write_b64 v127, v[20:21] offset:46080\n\t"
      "v_pk_fma_f32 v[28:29], v[96:97], s[56:57], v[126:127] op_sel:[0,1,0] op_sel_hi:[1,1,0]\n\t"
      "v_pk_fma_f32 v[30:31], v[98:99], s[56:57], v[126:127] op_sel:[0,1,0] op_sel_hi:[1,1,0]\n\t"
      "v_cvt_pk_bf16_f32 v22, v28, v29\n\t"
      "v_cvt_pk_bf16_f32 v23, v30, v31\n\t"
      "ds_write_b64 v127, v[22:23] offset:46088\n\t"
      "s_cmp_le_i32 s63, 0\n\t"
      "s_waitcnt lgkmcnt(0)\n\t"
      "s_cbranch_scc1 LRX_1%=\n\t"
      "s_cbranch_vccnz LRY_1%=\n\t"
      "LRX_1%=:\n\t"
      "s_cmp_eq_u32 s40, 63\n\t"
      "s_cbranch_scc1 LLAST_1%=\n\t"
      "s_branch LC1_1%=\n\t"
      "LLAST_1%=:\n\t"
      "s_waitcnt vmcnt(0) lgkmcnt(0)\n\t"
      "s_barrier\n\t"
      "ds_read_b128 v[16:19], v127 offset:45056\n\t"
      "ds_read_b128 v[28:31], v127 offset:48128\n\t"
      "ds_read_b128 v[24:27], v127 offset:47104\n\t"
      "v_smfmac_f32_16x16x64_bf16 v[40:43], v[20:23], a[168:175], v122\n\t"
      "v_smfmac_f32_16x16x64_bf16 v[44:47], v[20:23], a[232:239], v122\n\t"
      "s_waitcnt lgkmcnt(2)\n\t"
      "v_smfmac_f32_16x16x64_bf16 v[40:43], v[16:19], a[160:167], v122\n\t"
      "v_smfmac_f32_16x16x64_bf16 v[44:47], v[16:19], a[224:231], v122\n\t"
      "s_waitcnt lgkmcnt(1)\n\t"
      "v_smfmac_f32_16x16x64_bf16 v[44:47], v[28:31], a[248:255], v122\n\t"
      "v_smfmac_f32_16x16x64_bf16 v[40:43], v[28:31], a[184:191], v122\n\t"
      "s_waitcnt lgkmcnt(0)\n\t"
      "v_smfmac_f32_16x16x64_bf16 v[44:47], v[24:27], a[240:247], v122\n\t"
      "v_smfmac_f32_16x16x64_bf16 v[40:43], v[24:27], a[176:183], v122\n\t"
      "s_nop 7\n\t"
      "v_add_f32 v40, v40, v42\n\t"
      "v_add_f32 v41, v41, v43\n\t"
      "v_cvt_pk_bf16_f32 v120, v40, v41\n\t"
      "v_add_f32 v44, v44, v46\n\t"
      "v_add_f32 v45, v45, v47\n\t"
      "v_cvt_pk_bf16_f32 v121, v44, v45\n\t"
      "ds_write_b64 v125, v[120:121] offset:2080\n\t"
      "s_waitcnt lgkmcnt(0)\n\t"
      "s_barrier\n\t"
      "ds_read_b64_tr_b16 v[40:41], v124 offset:51232\n\t"
      "ds_read_b64_tr_b16 v[42:43], v124 offset:51752\n\t"
      "ds_read_b64_tr_b16 v[44:45], v124 offset:52272\n\t"
      "ds_read_b64_tr_b16 v[46:47], v124 offset:52792\n\t"
      "v_add_u32 v100, s55, v127\n\t"
      "ds_read_b128 v[60:63], v100 offset:57344\n\t"
      "s_waitcnt lgkmcnt(0)\n\t"
      "s_branch LROLE_1%=\n\t"
      "LFIN_1%=:\n\t"
      "s_waitcnt lgkmcnt(0)\n\t"
      "s_barrier\n\t"
      "ds_read_b64 v[54:55], v124 offset:53832\n\t"
      "s_waitcnt lgkmcnt(0)\n\t"
      "v_add_f32 v48, v48, v54\n\t"
      "v_add_f32 v49, v49, v55\n\t"
      "v_cvt_pk_bf16_f32 v48, v48, v49\n\t"
      "buffer_store_dword v48, v62, s[36:39], s28 offen sc1\n\t"
      "s_branch LEND%=\n\t"
      "LWK1_2%=:\n\t"
      "buffer_store_dwordx4 v[44:47], v61, s[36:39], s48 offen sc1\n\t"
      "s_branch LW4_2%=\n\t"
      "LR1_2%=:\n\t"
      "v_bitop3_b32 v64, v92, v93, v94 bitop3:0x80\n\t"
      "v_bitop3_b32 v64, v64, v95, v96 bitop3:0x80\n\t"
      "v_bitop3_b32 v64, v64, v97, v98 bitop3:0x80\n\t"
      "v_bitop3_b32 v64, v64, v99, s58 bitop3:0x80\n\t"
      "v_cmp_eq_u32 vcc, 0, v64\n\t"
      "s_nop 0\n\t"
      "s_cbranch_vccz LRX_2%=\n\t"
      "LRY_2%=:\n\t"
      "buffer_load_dwordx4 v[92:95], v60, s[36:39], s68 offen sc1\n\t"
      "buffer_load_dwordx4 v[96:99], v60, s[36:39], s68 offen offset:1024 sc1\n\t"
      "s_sub_u32 s63, s63, 1\n\t"
      "s_waitcnt vmcnt(0) lgkmcnt(0)\n\t"
      "v_bitop3_b32 v64, v92, v93, v94 bitop3:0x80\n\t"
      "v_bitop3_b32 v64, v64, v95, v96 bitop3:0x80\n\t"
      "v_bitop3_b32 v64, v64, v97, v98 bitop3:0x80\n\t"
      "v_bitop3_b32 v64, v64, v99, s58 bitop3:0x80\n\t"
      "v_cmp_eq_u32 vcc, 0, v64\n\t"
      "v_pk_fma_f32 v[28:29], v[92:93], s[56:57], v[126:127] op_sel:[0,1,0] op_sel_hi:[1,1,0]\n\t"
      "v_pk_fma_f32 v[30:31], v[94:95], s[56:57], v[126:127] op_sel:[0,1,0] op_sel_hi:[1,1,0]\n\t"
      "v_cvt_pk_bf16_f32 v24, v28, v29\n\t"
      "v_cvt_pk_bf16_f32 v25, v30, v31\n\t"
      "ds_write_b64 v127, v[24:25] offset:47104\n\t"
      "v_pk_fma_f32 v[16:17], v[96:97], s[56:57], v[126:127] op_sel:[0,1,0] op_sel_hi:[1,1,0]\n\t"
      "v_pk_fma_f32 v[18:19], v[98:99], s[56:57], v[126:127] op_sel:[0,1,0] op_sel_hi:[1,1,0]\n\t"
      "v_cvt_pk_bf16_f32 v26, v16, v17\n\t"
      "v_cvt_pk_bf16_f32 v27, v18, v19\n\t"
      "ds_write_b64 v127, v[26:27] offset:47112\n\t"
      "s_cmp_le_i32 s63, 0\n\t"
      "s_waitcnt lgkmcnt(0)\n\t"
      "s_cbranch_scc1 LRX_2%=\n\t"
      "s_cbranch_vccnz LRY_2%=\n\t"
      "LRX_2%=:\n\t"
      "s_cmp_eq_u32 s40, 63\n\t"
      "s_cbranch_scc1 LLAST_2%=\n\t"
      "s_branch LC1_2%=\n\t"
      "LLAST_2%=:\n\t"
      "s_waitcnt vmcnt(0) lgkmcnt(0)\n\t"
      "s_barrier\n\t"
      "ds_read_b128 v[28:31], v127 offset:48128\n\t"
      "ds_read_b128 v[16:19], v127 offset:45056\n\t"
      "ds_read_b128 v[20:23], v127 offset:46080\n\t"
      "v_smfmac_f32_16x16x64_bf16 v[40:43], v[24:27], a[176:183], v122\n\t"
      "v_smfmac_f32_16x16x64_bf16 v[44:47], v[24:27], a[240:247], v122\n\t"
      "s_waitcnt lgkmcnt(2)\n\t"
      "v_smfmac_f32_16x16x64_bf16 v[40:43], v[28:31], a[184:191], v122\n\t"
      "v_smfmac_f32_16x16x64_bf16 v[44:47], v[28:31], a[248:255], v122\n\t"
      "s_waitcnt lgkmcnt(1)\n\t"
      "v_smfmac_f32_16x16x64_bf16 v[44:47], v[16:19], a[224:231], v122\n\t"
      "v_smfmac_f32_16x16x64_bf16 v[40:43], v[16:19], a[160:167], v122\n\t"
      "s_waitcnt lgkmcnt(0)\n\t"
      "v_smfmac_f32_16x16x64_bf16 v[44:47], v[20:23], a[232:239], v122\n\t"
      "v_smfmac_f32_16x16x64_bf16 v[40:43], v[20:23], a[168:175], v122\n\t"
      "s_nop 7\n\t"
      "v_add_f32 v40, v40, v42\n\t"
      "v_add_f32 v41, v41, v43\n\t"
      "v_cvt_pk_bf16_f32 v120, v40, v41\n\t"
      "v_add_f32 v44, v44, v46\n\t"
      "v_add_f32 v45, v45, v47\n\t"
      "v_cvt_pk_bf16_f32 v121, v44, v45\n\t"
      "ds_write_b64 v125, v[120:121] offset:2080\n\t"
      "s_waitcnt lgkmcnt(0)\n\t"
      "s_barrier\n\t"
      "ds_read_b64_tr_b16 v[40:41], v124 offset:51232\n\t"
      "ds_read_b64_tr_b16 v[42:43], v124 offset:51752\n\t"
      "ds_read_b64_tr_b16 v[44:45], v124 offset:52272\n\t"
      "ds_read_b64_tr_b16 v[46:47], v124 offset:52792\n\t"
      "v_add_u32 v100, s55, v127\n\t"
      "ds_read_b128 v[60:63], v100 offset:57344\n\t"
      "s_waitcnt lgkmcnt(0)\n\t"
      "s_branch LROLE_2%=\n\t"
      "LFIN_2%=:\n\t"
      "s_waitcnt lgkmcnt(0)\n\t"
      "s_barrier\n\t"
      "s_branch LEND%=\n\t"
      "LR1_3%=:\n\t"
      "v_bitop3_b32 v64, v92, v93, v94 bitop3:0x80\n\t"
      "v_bitop3_b32 v64, v64, v95, v96 bitop3:0x80\n\t"
      "v_bitop3_b32 v64, v64, v97, v98 bitop3:0x80\n\t"
      "v_bitop3_b32 v64, v64, v99, s58 bitop3:0x80\n\t"
      "v_cmp_eq_u32 vcc, 0, v64\n\t"
      "s_nop 0\n\t"
      "s_cbranch_vccz LRX_3%=\n\t"
      "LRY_3%=:\n\t"
      "buffer_load_dwordx4 v[92:95], v60, s[36:39], s68 offen sc1\n\t"
      "buffer_load_dwordx4 v[96:99], v60, s[36:39], s68 offen offset:1024 sc1\n\t"
      "s_sub_u32 s63, s63, 1\n\t"
      "s_waitcnt vmcnt(0) lgkmcnt(0)\n\t"
      "v_bitop3_b32 v64, v92, v93, v94 bitop3:0x80\n\t"
      "v_bitop3_b32 v64, v64, v95, v96 bitop3:0x80\n\t"
      "v_bitop3_b32 v64, v64, v97, v98 bitop3:0x80\n\t"
      "v_bitop3_b32 v64, v64, v99, s58 bitop3:0x80\n\t"
      "v_cmp_eq_u32 vcc, 0, v64\n\t"
      "v_pk_fma_f32 v[24:25], v[92:93], s[56:57], v[126:127] op_sel:[0,1,0] op_sel_hi:[1,1,0]\n\t"
      "v_pk_fma_f32 v[26:27], v[94:95], s[56:57], v[126:127] op_sel:[0,1,0] op_sel_hi:[1,1,0]\n\t"
      "v_cvt_pk_bf16_f32 v28, v24, v25\n\t"
      "v_cvt_pk_bf16_f32 v29, v26, v27\n\t"
      "ds_write_b64 v127, v[28:29] offset:48128\n\t"
      "v_pk_fma_f32 v[20:21], v[96:97], s[56:57], v[126:127] op_sel:[0,1,0] op_sel_hi:[1,1,0]\n\t"
      "v_pk_fma_f32 v[22:23], v[98:99], s[56:57], v[126:127] op_sel:[0,1,0] op_sel_hi:[1,1,0]\n\t"
      "v_cvt_pk_bf16_f32 v30, v20, v21\n\t"
      "v_cvt_pk_bf16_f32 v31, v22, v23\n\t"
      "ds_write_b64 v127, v[30:31] offset:48136\n\t"
      "s_memtime s[88:89]\n\t"
      "s_cmp_le_i32 s63, 0\n\t"
      "s_waitcnt lgkmcnt(0)\n\t"
      "s_cbranch_scc1 LRX_3%=\n\t"
      "s_cbranch_vccnz LRY_3%=\n\t"
      "s_sub_u32 s88, s88, 336\n\t"
      "LRX_3%=:\n\t"
      "s_cmp_eq_u32 s40, 63\n\t"
      "s_cbranch_scc1 LLAST_3%=\n\t"
      "s_branch LC1_3%=\n\t"
      "LLAST_3%=:\n\t"
      "s_waitcnt vmcnt(0) lgkmcnt(0)\n\t"
      "s_barrier\n\t"
      "ds_read_b128 v[24:27], v127 offset:47104\n\t"
      "ds_read_b128 v[20:23], v127 offset:46080\n\t"
      "ds_read_b128 v[16:19], v127 offset:45056\n\t"
      "v_smfmac_f32_16x16x64_bf16 v[40:43], v[28:31], a[184:191], v122\n\t"
      "v_smfmac_f32_16x16x64_bf16 v[44:47], v[28:31], a[248:255], v122\n\t"
      "s_waitcnt lgkmcnt(2)\n\t"
      "v_smfmac_f32_16x16x64_bf16 v[40:43], v[24:27], a[176:183], v122\n\t"
      "v_smfmac_f32_16x16x64_bf16 v[44:47], v[24:27], a[240:247], v122\n\t"
      "s_waitcnt lgkmcnt(1)\n\t"
      "v_smfmac_f32_16x16x64_bf16 v[44:47], v[20:23], a[232:239], v122\n\t"
      "v_smfmac_f32_16x16x64_bf16 v[40:43], v[20:23], a[168:175], v122\n\t"
      "s_waitcnt lgkmcnt(0)\n\t"
      "v_smfmac_f32_16x16x64_bf16 v[44:47], v[16:19], a[224:231], v122\n\t"
      "v_smfmac_f32_16x16x64_bf16 v[40:43], v[16:19], a[160:167], v122\n\t"
      "s_nop 7\n\t"
      "v_add_f32 v40, v40, v42\n\t"
      "v_add_f32 v41, v41, v43\n\t"
      "v_cvt_pk_bf16_f32 v120, v40, v41\n\t"
      "v_add_f32 v44, v44, v46\n\t"
      "v_add_f32 v45, v45, v47\n\t"
      "v_cvt_pk_bf16_f32 v121, v44, v45\n\t"
      "ds_write_b64 v125, v[120:121] offset:2080\n\t"
      "s_waitcnt lgkmcnt(0)\n\t"
      "s_barrier\n\t"
      "ds_read_b64_tr_b16 v[40:41], v124 offset:51232\n\t"
      "ds_read_b64_tr_b16 v[42:43], v124 offset:51752\n\t"
      "ds_read_b64_tr_b16 v[44:45], v124 offset:52272\n\t"
      "ds_read_b64_tr_b16 v[46:47], v124 offset:52792\n\t"
      "v_add_u32 v100, s55, v127\n\t"
      "ds_read_b128 v[60:63], v100 offset:57344\n\t"
      "s_waitcnt lgkmcnt(0)\n\t"
      "s_branch LROLE_3%=\n\t"
      "LFIN_3%=:\n\t"
      "s_waitcnt lgkmcnt(0)\n\t"
      "s_barrier\n\t"
      "s_branch LEND%=\n\t"
      "LEND%=:\n\t"
      "s_waitcnt vmcnt(2) lgkmcnt(0)\n\t"
      "s_cmp_lg_u32 s42, 9\n\t"
      "s_cbranch_scc1 LOUT%=\n\t"
      "s_waitcnt vmcnt(0)\n\t"
      "LOUT%=:\n\t"
      : "+{v[116:123]}"(c0), "+{v[124:127]}"(c1), "+{v[0:15]}"(sc), "+{v[16:31]}"(s2), "+{v[100:107]}"(c3), "+{v[108:115]}"(cc), "+s"(A), "+s"(b), "+s"(wv), "+{s87}"(tch)
      :
      : "memory", "vcc", "scc", "v32", "v33", "v34", "v35", "v36", "v37", "v38", "v39", "v40", "v41", "v42", "v43", "v44", "v45", "v46", "v47", "v48", "v49", "v50", "v51", "v52", "v53", "v54", "v55", "v56", "v57", "v58", "v59", "v60", "v61", "v62", "v63", "v64", "v65", "v66", "v67", "v68", "v69", "v70", "v71", "v72", "v73", "v74", "v75", "v76", "v77", "v78", "v79", "v80", "v81", "v82", "v83", "v84", "v85", "v86", "v87", "v88", "v89", "v90", "v91", "v92", "v93", "v94", "v95", "v96", "v97", "v98", "v99", "v128", "v129", "v130", "v131", "v132", "v133", "v134", "v135", "v136", "v137", "v138", "v139", "v140", "v141", "v142", "v143", "v144", "v145", "v146", "v147", "v148", "v149", "v150", "v151", "v152", "v153", "v154", "v155", "v156", "v157", "v158", "v159", "v160", "v161", "v162", "v163", "v164", "v165", "v166", "v167", "v168", "v169", "v170", "v171", "v172", "v173", "v174", "v175", "v176", "v177", "v178", "v179", "v180", "v181", "v182", "v183", "v184", "v185", "v186", "v187", "v188", "v189", "v190", "v191", "v192", "v193", "v194", "v195", "v196", "v197", "v198", "v199", "v200", "v201", "v202", "v203", "v204", "v205", "v206", "v207", "v208", "v209", "v210", "v211", "v212", "v213", "v214", "v215", "v216", "v217", "v218", "v219", "v220", "v221", "v222", "v223", "v224", "v225", "v226", "v227", "v228", "v229", "v230", "v231", "v232", "v233", "v234", "v235", "v236", "v237", "v238", "v239", "v240", "v241", "v242", "v243", "v244", "v245", "v246", "v247", "v248", "v249", "v250", "v251", "v252", "v253", "v254", "v255", "a0", "a1", "a2", "a3", "a4", "a5", "a6", "a7", "a8", "a9", "a10", "a11", "a12", "a13", "a14", "a15", "a16", "a17", "a18", "a19", "a20", "a21", "a22", "a23", "a24", "a25", "a26", "a27", "a28", "a29", "a30", "a31", "a32", "a33", "a34", "a35", "a36", "a37", "a38", "a39", "a40", "a41", "a42", "a43", "a44", "a45", "a46", "a47", "a48", "a49", "a50", "a51", "a52", "a53", "a54", "a55", "a56", "a57", "a58", "a59", "a60", "a61", "a62", "a63", "a64", "a65", "a66", "a67", "a68", "a69", "a70", "a71", "a72", "a73", "a74", "a75", "a76", "a77", "a78", "a79", "a80", "a81", "a82", "a83", "a84", "a85", "a86", "a87", "a88", "a89", "a90", "a91", "a92", "a93", "a94", "a95", "a96", "a97", "a98", "a99", "a100", "a101", "a102", "a103", "a104", "a105", "a106", "a107", "a108", "a109", "a110", "a111", "a112", "a113", "a114", "a115", "a116", "a117", "a118", "a119", "a120", "a121", "a122", "a123", "a124", "a125", "a126", "a127", "a128", "a129", "a130", "a131", "a132", "a133", "a134", "a135", "a136", "a137", "a138", "a139", "a140", "a141", "a142", "a143", "a144", "a145", "a146", "a147", "a148", "a149", "a150", "a151", "a152", "a153", "a154", "a155", "a156", "a157", "a158", "a159", "a160", "a161", "a162", "a163", "a164", "a165", "a166", "a167", "a168", "a169", "a170", "a171", "a172", "a173", "a174", "a175", "a176", "a177", "a178", "a179", "a180", "a181", "a182", "a183", "a184", "a185", "a186", "a187", "a188", "a189", "a190", "a191", "a192", "a193", "a194", "a195", "a196", "a197", "a198", "a199", "a200", "a201", "a202", "a203", "a204", "a205", "a206", "a207", "a208", "a209", "a210", "a211", "a212", "a213", "a214", "a215", "a216", "a217", "a218", "a219", "a220", "a221", "a222", "a223", "a224", "a225", "a226", "a227", "a228", "a229", "a230", "a231", "a232", "a233", "a234", "a235", "a236", "a237", "a238", "a239", "a240", "a241", "a242", "a243", "a244", "a245", "a246", "a247", "a248", "a249", "a250", "a251", "a252", "a253", "a254", "a255", "s22", "s23", "s24", "s25", "s26", "s27", "s28", "s29", "s30", "s31", "s36", "s37", "s38", "s39", "s40", "s41", "s42", "s43", "s44", "s45", "s46", "s47", "s48", "s49", "s50", "s51", "s52", "s53", "s54", "s55", "s56", "s57", "s58", "s59", "s60", "s61", "s62", "s63", "s64", "s65", "s66", "s67", "s68", "s69", "s70", "s71", "s72", "s73", "s74", "s75", "s76", "s77", "s78", "s79", "s80", "s81", "s82", "s83", "s84", "s85", "s86", "s88", "s89", "s90", "s91", "s92", "s93", "s94", "s95", "s96", "s97", "s98", "s99");
}

#define SSLEEP 1
#define TSLEEP 4
__device__ __forceinline__ unsigned mkkey(unsigned w, int idx) {
  unsigned u = w >> 16;
  u = (u & 0x7fffu) == 0 ? 0u : u;
  unsigned o = (u & 0x8000u) ? (~u & 0xffffu) : (u | 0x8000u);
  o = (u & 0x7fffu) > 0x7f80u ? 0u : o;
  return (o << 16) | (0xffffu - (unsigned)idx);
}
template <bool spread>
__device__ __forceinline__ u32x2 top2(const float* rbuf, int tid, int need, unsigned* sk) {
  unsigned key[9];
  const __amdgpu_buffer_rsrc_t rrs = __builtin_amdgcn_make_buffer_rsrc((void*)rbuf, 0, spread ? 2 * S_ * 4 + 256 * 128 : S_ * 4, 0x00020000);
  const int t8 = spread && tid >= 224 ? 0 : tid * 8;
#pragma unroll 1
  for (unsigned spins = 0; spins < (1u << 20); ++spins) {
    asm volatile("" ::: "memory");
    const u32x4 a = ld_sc1(rrs, t8 * 4), b = ld_sc1(rrs, t8 * 4 + 16);
    key[8] = spread ? __builtin_amdgcn_raw_buffer_load_b32(rrs, 2 * S_ * 4 + tid * 128, 0, 16) : a[0];
    unsigned mx = key[8];
#pragma unroll
    for (int k = 0; k < 4; ++k) { key[k] = a[k]; key[4 + k] = b[k]; mx = max(mx, max(a[k], b[k])); }
    if (mx != ~0u || tid * 8 >= need) break;
    __builtin_amdgcn_s_sleep(TSLEEP);
  }
  unsigned m = 0;
#pragma unroll
  for (int k = 0; k < 9; ++k) {
    key[k] = mkkey(key[k], k < 8 ? t8 + k : (spread ? S_ - 256 + tid : t8));
    m = max(m, key[k]);
  }
  m = wave_max_u32(m);
  __syncthreads();
  if ((tid & 63) == 0) sk[tid >> 6] = m;
  __syncthreads();
  const unsigned k0 = max(max(sk[0], sk[1]), max(sk[2], sk[3]));
  m = 0;
#pragma unroll
  for (int k = 0; k < 9; ++k) m = max(m, key[k] == k0 ? 0u : key[k]);
  m = wave_max_u32(m);
  if ((tid & 63) == 0) sk[4 + (tid >> 6)] = m;
  __syncthreads();
  const unsigned k1 = max(max(sk[4], sk[5]), max(sk[6], sk[7]));
  return (u32x2){(unsigned)__builtin_amdgcn_readfirstlane(k0), (unsigned)__builtin_amdgcn_readfirstlane(k1)};
}
__device__ __forceinline__ u32x2 top2_wave(const float* rbuf, int lane, bool dry, bool strict) {
  const __amdgpu_buffer_rsrc_t rrs = __builtin_amdgcn_make_buffer_rsrc((void*)rbuf, 0, S_ * 4, 0x00020000);
  asm volatile("" ::: "memory");
  u32x4 a[8];
#pragma unroll
  for (int i = 0; i < 8; ++i) a[i] = (u32x4){0u, 0u, 0u, 0u};
  if (!dry) {
#pragma unroll
    for (int i = 0; i < 8; ++i) a[i] = ld_sc1(rrs, (i * 64 + lane) * 16);
  }
  if (SNEED) {
    unsigned mx = 0;
#pragma unroll
    for (int i = 0; i < 8; ++i) {
      const unsigned x = max(max(a[i][0], a[i][1]), max(a[i][2], a[i][3]));
      mx = max(mx, i * 8 + (lane >> 3) < SNEED ? x : 0u);
    }
    if (strict && __builtin_amdgcn_ballot_w64(mx == ~0u)) return (u32x2){0u, 0u};
  }
  unsigned m1 = 0, m2 = 0;
#pragma unroll
  for (int i = 0; i < 32; ++i) {
    const unsigned w = a[i >> 2][i & 3], o = w ^ ((unsigned)((int)w >> 31) | 0x80000000u);
    const unsigned k = (o & 0xffff0000u) | ((0xffffu - (unsigned)(((i >> 2) * 64 + lane) * 4 + (i & 3))) & ~w);
    m2 = max(min(m1, k), m2);
    m1 = max(m1, k);
  }
  unsigned k[2];
  k[0] = wave_max_u32(m1);
  k[1] = wave_max_u32(m1 == k[0] ? m2 : m1);
  if (!k[1]) return (u32x2){0u, 0u};
#pragma unroll
  for (int i = 0; i < 2; ++i) k[i] = mkkey((k[i] >> 31) ? k[i] & 0x7fffffffu : ~k[i], 0xffff - (k[i] & 0xffff));
  return (u32x2){max(k[0], k[1]), min(k[0], k[1])};
}
__device__ __forceinline__ bool other_top2a(const float* rbuf, int tid, unsigned k0, unsigned k1) {
  const __amdgpu_buffer_rsrc_t rrs = __builtin_amdgcn_make_buffer_rsrc((void*)rbuf, 0, S_ * 4, 0x00020000);
  const int t8 = tid >= 224 ? 0 : tid * 8;
  bool bad = false;
#pragma unroll 1
  for (unsigned spins = 0; spins < (1u << 20); ++spins) {
    asm volatile("" ::: "memory");
    const u32x4 a = ld_sc1(rrs, t8 * 4), b = ld_sc1(rrs, t8 * 4 + 16);
    unsigned mx = 0;
    bad = false;
#pragma unroll
    for (int k = 0; k < 4; ++k) {
      const unsigned ka = mkkey(a[k], t8 + k), kb = mkkey(b[k], t8 + 4 + k);
      bad |= (ka > k1 && ka != k0) || (kb > k1 && kb != k0);
      mx = max(mx, max(a[k], b[k]));
    }
    if (__builtin_amdgcn_ballot_w64(mx == ~0u) == 0) break;
    __builtin_amdgcn_s_sleep(TSLEEP);
  }
  return __builtin_amdgcn_ballot_w64(bad) != 0;
}
__device__ __forceinline__ bool other_top2b(const float* rbuf, int tid, unsigned k0, unsigned k1, bool bad, unsigned* sk, unsigned& tmp, unsigned f) {
  if (RDLY && (f >> 16) == 63) {
    const unsigned t = f + RDLY - (3 - (tid >> 6)) * RDL2;
#pragma unroll 1
    while (((t - (unsigned)__builtin_amdgcn_s_memrealtime()) & 0xffffu) < 400u) __builtin_amdgcn_s_sleep(1);
  }
  const __amdgpu_buffer_rsrc_t rrs = __builtin_amdgcn_make_buffer_rsrc((void*)rbuf, 0, 2 * S_ * 4 + 256 * 128, 0x00020000);
  unsigned w = RDLY ? __builtin_amdgcn_raw_buffer_load_b32(rrs, 2 * S_ * 4 + tid * 128, 0, 16) : ~0u, n;
  tmp = 0;
  if (!RDLY || __builtin_amdgcn_ballot_w64(w == ~0u)) {
  asm volatile("buffer_load_dword %1, %3, %4, 0 offen sc1\n\ts_sleep %5\n\t"
               "buffer_load_dword %1, %3, %4, 0 offen sc1\n\ts_sleep %5\n\t"
               "s_mov_b32 %2, 0x100000\n"
               "TP%=:\n\t"
               "buffer_load_dword %1, %3, %4, 0 offen sc1\n\t"
               "s_sub_u32 %2, %2, 1\n\t"
               "s_waitcnt vmcnt(2)\n\t"
               "v_mov_b32 %0, %1\n\t"
               "v_cmp_eq_u32 vcc, -1, %0\n\t"
               "s_cbranch_scc1 TQ%=\n\t"
               "s_cbranch_vccnz TP%=\n"
               "TQ%=:"
               : "=&v"(w), "=&v"(tmp), "=&s"(n) : "v"(2 * S_ * 4 + tid * 128), "s"(rrs), "n"(PSLEEP) : "memory", "vcc", "scc");
  }
  const unsigned k = mkkey(w, S_ - 256 + tid);
  bad |= k > k1 && k != k0;
  const unsigned any = __builtin_amdgcn_ballot_w64(bad) != 0;
  if ((tid & 63) == 0) sk[tid >> 6] = any;
  LDS_BARRIER();
  const u32x4 v = *(const u32x4*)sk;
  return __builtin_amdgcn_readfirstlane(v[0] | v[1] | v[2] | v[3]) != 0;
}

constexpr int M1_WGS = FF / 16;
constexpr int L_WD = 131072;
__device__ __forceinline__ void wait_set(gu32* f) {
#pragma unroll 1
  for (unsigned spins = 0; spins < (1u << 18); ++spins) {
    if (__hip_atomic_load(f, RLX_AGENT) != ~0u) return;
    __builtin_amdgcn_s_sleep(4);
  }
}
__device__ __forceinline__ u32x2 wait_selt(char* ws, int b, unsigned& tm) {
  const __amdgpu_buffer_rsrc_t rs = __builtin_amdgcn_make_buffer_rsrc((void*)(ws + O_CNT), 0, 1024, 0x00020000);
  u32x4 v;
#pragma unroll 1
  for (unsigned spins = 0; spins < (1u << 18); ++spins) {
    asm volatile("" ::: "memory");
    v = ld_sc1(rs, (b & 7) * 64);
    if (v[1]) break;
    __builtin_amdgcn_s_sleep(FSLEEP);
  }
  tm = __builtin_amdgcn_readfirstlane(v[2]);
  return (u32x2){(unsigned)__builtin_amdgcn_readfirstlane(v[0]), (unsigned)__builtin_amdgcn_readfirstlane(v[1])};
}
__device__ __forceinline__ u32x2 wait_sel(gu32* f, u64 v = 0) {
#pragma unroll 1
  for (unsigned spins = 0; spins < (1u << 18) && !v; ++spins) {
    v = __hip_atomic_load((gu64*)f, RLX_AGENT);
    if (v) break;
    __builtin_amdgcn_s_sleep(FSLEEP);
  }
  return (u32x2){(unsigned)__builtin_amdgcn_readfirstlane((unsigned)v), (unsigned)__builtin_amdgcn_readfirstlane((unsigned)(v >> 32))};
}
__device__ __forceinline__ unsigned code_ahead(int tid) {
  unsigned d = 0;
  if (MPF) { u64 pc; asm volatile("s_getpc_b64 %1\n\tglobal_load_dword %0, %2, %1" : "=v"(d), "=&s"(pc) : "v"((tid & 127) * 64) : "memory"); }
  return d;
}
__device__ __forceinline__ bf16x8 as_frag(u32x4 v) { return __builtin_bit_cast(bf16x8, v); }
__device__ __forceinline__ u32x4 tr_frag(char* p0, char* p1) {
  const s16x4 a = __builtin_amdgcn_ds_read_tr16_b64_v4i16((LAS s16x4*)p0), b = __builtin_amdgcn_ds_read_tr16_b64_v4i16((LAS s16x4*)p1);
  const u32x2 x = __builtin_bit_cast(u32x2, a), y = __builtin_bit_cast(u32x2, b);
  return (u32x4){x[0], x[1], y[0], y[1]};
}
__device__ __forceinline__ void mlp1_role(KArgs P, char* lds, int b, int tid) {
  float* part = (float*)lds;
  unsigned* si = (unsigned*)(lds + 4096);
  const int lane = tid & 63, wv = tid >> 6, l15 = lane & 15, l4 = lane >> 4;
  const __amdgpu_buffer_rsrc_t lrs = __builtin_amdgcn_make_buffer_rsrc((void*)(P->ws + O_LNH), 0, S_ * DM * 2, 0x00020000);
  const __amdgpu_buffer_rsrc_t mrs = __builtin_amdgcn_make_buffer_rsrc((void*)(P->ws + O_MFRAG), 0, 2 * DM * FF * 2, 0x00020000);
  u32x4 aw[2][4];
  MS(0);
  unsigned ca = code_ahead(tid);
  const u64 s0 = __hip_atomic_load((gu64*)(P->ws + O_CNT + (b & 7) * 64), RLX_AGENT);
  const unsigned fl = __hip_atomic_load((gu32*)(P->ws + O_KVSTEP + (b >> 2) * 64 + (b & 3) * 4), RLX_AGENT);
#pragma unroll
  for (int i = 0; i < 8; ++i) aw[i >> 2][i & 3] = ld_sc1(mrs, (unsigned)(b * 32768 + (i >> 2) * 16384 + (wv * 4 + (i & 3)) * 1024 + lane * 16));
  u32x2 sel = wait_sel((gu32*)(P->ws + O_CNT) + (b & 7) * 16, s0);
  asm volatile("" :: "v"(ca));
  MS(1);
  int round = 0;
#pragma unroll 1
  for (;;) {
    const int idx = 0xffff - (((l15 & 1) ? sel[1] : sel[0]) & 0xffff);
    if (round == 2) wait_set((gu32*)(P->ws + O_RBUF) + S_ + idx);
    u32x4 bx[4];
#pragma unroll
    for (int i = 0; i < 4; ++i) bx[i] = ld_sc1(lrs, (unsigned)(idx * 1024 + (wv * 4 + i) * 64 + l4 * 16));
    f32x4 ag = {0.f, 0.f, 0.f, 0.f}, au = {0.f, 0.f, 0.f, 0.f};
#pragma unroll
    for (int i = 0; i < 4; ++i) {
      ag = __builtin_amdgcn_mfma_f32_16x16x32_bf16(as_frag(aw[0][i]), as_frag(bx[i]), ag, 0, 0, 0);
      au = __builtin_amdgcn_mfma_f32_16x16x32_bf16(as_frag(aw[1][i]), as_frag(bx[i]), au, 0, 0, 0);
    }
    if (l15 < 2) {
      *(f32x4*)(part + wv * 64 + l15 * 16 + l4 * 4) = ag;
      *(f32x4*)(part + wv * 64 + 32 + l15 * 16 + l4 * 4) = au;
    }
    LDS_BARRIER();
    MS(2);
    if (tid < 16) {
      float a[2];
#pragma unroll
      for (int e = 0; e < 2; ++e) {
        const float* q = part + tid * 2 + e;
        const float g = rbf((q[0] + q[64]) + (q[128] + q[192])), u = rbf((q[32] + q[96]) + (q[160] + q[224]));
        a[e] = rbf(g / (1.0f + __expf(-g))) * u;
      }
      unsigned av = pk2(a[0], a[1]);
      av = av == ~0u ? 0x7fc07fc0u : av;
      __hip_atomic_store((gu32*)(P->ws + O_ACT) + (round * M1_WGS + b) * 32 + tid, av, RLX_AGENT);
    }
    MS(4);
    if (round == 2) break;
    if (SW2 && !round) {
      const u32x2 s2 = wait_sel((gu32*)(P->ws + O_SEL2) + (b & 31) * 32);
      if (s2[0] != sel[0] || s2[1] != sel[1]) { sel = s2; round = 1; __syncthreads(); continue; }
    }
    unsigned tmp;
    const bool again = other_top2b((const float*)(P->ws + O_RBUF), tid, sel[0], sel[1], other_top2a((const float*)(P->ws + O_RBUF), tid, sel[0], sel[1]), si, tmp, __builtin_amdgcn_readfirstlane(fl));
    MS(5);
    asm volatile("s_waitcnt vmcnt(0)" :: "v"(tmp) : "memory");
    MS(6);
    if (!again) break;
    sel = top2<true>((const float*)(P->ws + O_RBUF), tid, S_, si + 4);
    round = 2;
  }
}
__device__ __forceinline__ void mlp2_role(KArgs P, char* lds, int b, int tid) {
  float* part = (float*)lds;
  unsigned* si = (unsigned*)(lds + 4096);
  const int lane = tid & 63, wv = tid >> 6, l15 = lane & 15, l4 = lane >> 4;
  const __amdgpu_buffer_rsrc_t hrs = __builtin_amdgcn_make_buffer_rsrc((void*)(P->ws + O_HBUF), 0, S_ * DM * 2, 0x00020000);
  const __amdgpu_buffer_rsrc_t lrs = __builtin_amdgcn_make_buffer_rsrc((void*)(P->ws + O_LNH), 0, S_ * DM * 2, 0x00020000);
  const __amdgpu_buffer_rsrc_t ors = __builtin_amdgcn_make_buffer_rsrc((void*)P->out, 0, S_ * DM * 2, 0x00020000);
  const __amdgpu_buffer_rsrc_t ars = __builtin_amdgcn_make_buffer_rsrc((void*)(P->ws + O_ACT), 0, 3 * M1_WGS * 128, 0x00020000);
  u32x4 aw[7];
  const int c0 = b * 8;
  MS(0);
  unsigned ca = code_ahead(tid);
  {
    char* p = lds + L_WD + (((l15 >> 1) & 1) * 896 + wv * 224 + l4 * 8 + (l15 >> 2)) * 16 + (l15 & 1) * 8;
#pragma unroll
    for (int i = 0; i < 7; ++i) aw[i] = tr_frag(p + i * 512, p + i * 512 + 64);
  }
  unsigned tsel;
  u32x2 sel = wait_selt(P->ws, b, tsel);
  asm volatile("" :: "v"(ca));
  const unsigned fl = __hip_atomic_load((gu32*)(P->ws + O_KVSTEP + (c0 >> 4) * 64), RLX_AGENT);
  MS(1);
  int round = 0;
#pragma unroll 1
  for (;;) {
    const int idx = 0xffff - (((tid & 8) ? sel[1] : sel[0]) & 0xffff), col = c0 + (tid & 7);
    if (round == 2) wait_set((gu32*)(P->ws + O_RBUF) + S_ + idx);
    const float rr = __hip_atomic_load((const GAS float*)(P->ws + O_RBUF) + S_ + idx, RLX_AGENT);
    const float lv = bf2f(__builtin_amdgcn_raw_buffer_load_b16(lrs, (unsigned)(idx * DM + col) * 2, 0, 16));
    const float hv = bf2f(__builtin_amdgcn_raw_buffer_load_b16(hrs, (unsigned)(idx * DM + col) * 2, 0, 16));
    const float sg = rbf(1.0f / (1.0f + __expf(-rr)));
    const bool bad = other_top2a((const float*)(P->ws + O_RBUF), tid, sel[0], sel[1]);
    if (ADLY && !round) {
#pragma unroll 1
      while (tsel + ADLY - (unsigned)__builtin_amdgcn_s_memrealtime() < 400u) __builtin_amdgcn_s_sleep(1);
    } else if (!round && (ASALL || c0 < 256)) __builtin_amdgcn_s_sleep(ASLEEP);
    u32x4 av[7];
    u64 v2 = 0;
#pragma unroll 1
    for (unsigned spins = 0; spins < (1u << 20); ++spins) {
      asm volatile("" ::: "memory");
      unsigned mx = 0;
      if (SW2) v2 = __hip_atomic_load((gu64*)(P->ws + O_SEL2 + (b & 31) * 128), RLX_AGENT);
#pragma unroll
      for (int i = 0; i < 7; ++i) {
        av[i] = ld_sc1(ars, (unsigned)((round * M1_WGS + ((l15 >> 1) & 1) * 56 + (wv * 7 + i) * 2 + (l4 >> 1)) * 128 + (l15 & 1) * 32 + (l4 & 1) * 16));
        mx = max(max(mx, max(av[i][0], av[i][1])), max(av[i][2], av[i][3]));
      }
      if (__builtin_amdgcn_ballot_w64(mx == ~0u) == 0) break;
      __builtin_amdgcn_s_sleep(4);
    }
    MS(2);
    if (SW2 && !round) {
      const u32x2 s2 = wait_sel((gu32*)(P->ws + O_SEL2) + (b & 31) * 32, v2);
      if (s2[0] != sel[0] || s2[1] != sel[1]) { sel = s2; round = 1; continue; }
    }
    f32x4 a0 = {0.f, 0.f, 0.f, 0.f}, a1 = {0.f, 0.f, 0.f, 0.f};
#pragma unroll
    for (int i = 0; i < 7; ++i) {
      if (i & 1) a1 = __builtin_amdgcn_mfma_f32_16x16x32_bf16(as_frag(aw[i]), as_frag(av[i]), a1, 0, 0, 0);
      else a0 = __builtin_amdgcn_mfma_f32_16x16x32_bf16(as_frag(aw[i]), as_frag(av[i]), a0, 0, 0, 0);
    }
    if ((l15 >> 1) == (l4 >> 1)) *(f32x4*)(part + wv * 32 + l15 * 8 + (l4 & 1) * 4) = a0 + a1;
    LDS_BARRIER();
    const float* q = part + (tid & 15);
    const float y = rbf(((q[0] + q[16]) + (q[32] + q[48])) + ((q[64] + q[80]) + (q[96] + q[112])));
    const float upd = rbf(y * sg);
    const float bb = rbf(lv + upd);
    const u16 res = f2bf(hv + bb);
    MS(4);
    if (tid < 16) __builtin_amdgcn_raw_buffer_store_b16(res, ors, (unsigned)(idx * DM + col) * 2, 0, 16);
    if (round == 2) break;
    unsigned tmp;
    const bool again = other_top2b((const float*)(P->ws + O_RBUF), tid, sel[0], sel[1], bad, si, tmp, __builtin_amdgcn_readfirstlane(fl));
    MS(5);
    asm volatile("s_waitcnt vmcnt(0)" :: "v"(tmp) : "memory");
    MS(6);
    if (!again) break;
    if (tid < 16) __builtin_amdgcn_raw_buffer_store_b16(f2bf(hv + lv), ors, (unsigned)(idx * DM + col) * 2, 0, 16);
    VM_DRAIN();
    sel = top2<true>((const float*)(P->ws + O_RBUF), tid, S_, si + 4);
    round = 2;
  }
}

struct WoRegs { u32x32 a, b, c, d; };
__device__ __forceinline__ unsigned worker_role(int& w, int& wv, KArgs& A, WoRegs& wo) {
  unsigned tag;
  asm volatile(
      "GSTART:\n\t"
      "v_mbcnt_lo_u32_b32 v96, -1, 0\n\t"
      "v_mbcnt_hi_u32_b32 v96, -1, v96\n\t"
      "s_and_b32 s82, %0, 31\n\t"
      "s_mov_b32 s66, 4194304\n\t"
      "s_mov_b32 s67, 0x20000\n\t"
      "s_mov_b32 s62, 111181824\n\t"
      "s_mov_b32 s63, 0x20000\n\t"
      "v_and_b32 v104, 3, v96\n\t"
      "v_lshrrev_b32 v105, 2, v96\n\t"
      "s_lshl_b32 s78, %0, 14\n\t"
      "s_lshl_b32 s79, %1, 12\n\t"
      "s_add_u32 s79, s79, s78\n\t"
      "v_lshlrev_b32 v106, 10, v104\n\t"
      "v_lshl_add_u32 v106, v105, 4, v106\n\t"
      "v_lshlrev_b32 v99, 13, v105\n\t"
      "v_lshl_add_u32 v99, v104, 4, v99\n\t"
      "s_lshl_b32 s80, s82, 8\n\t"
      "s_lshl_b32 s72, %1, 6\n\t"
      "s_add_u32 s80, s80, s72\n\t"
      "v_add_u32 v99, s80, v99\n\t"
      "s_bitcmp1_b32 %0, 5\n\t"
      "s_waitcnt lgkmcnt(0)\n\t"
      "s_mov_b64 s[64:65], s[36:37]\n\t"
      "s_mov_b64 s[60:61], s[58:59]\n\t"
      "buffer_load_dwordx4 v[64:67], v106, s[64:67], s79 offen \n\t"
      "buffer_load_dwordx4 v[68:71], v106, s[64:67], s79 offen offset:256 \n\t"
      "buffer_load_dwordx4 v[72:75], v106, s[64:67], s79 offen offset:512 \n\t"
      "buffer_load_dwordx4 v[76:79], v106, s[64:67], s79 offen offset:768 \n\t"
      "s_cselect_b64 s[38:39], s[44:45], s[42:43]\n\t"
      "s_mov_b32 s78, 0\n\t"
      "s_load_dword s87, s[38:39], s78\n\t"
      "s_mov_b32 s78, 4194300\n\t"
      "s_load_dword s87, s[38:39], s78\n\t"
      "s_mov_b32 s78, 128\n\t"
      "s_load_dword s87, s[60:61], s78\n\t"
      "s_mov_b32 s78, 6520704\n\t"
      "s_load_dword s87, s[60:61], s78\n\t"
      "v_mov_b32 v118, v106\n\t"
      "s_mov_b64 s[64:65], s[36:37]\n\t"
      "s_lshr_b32 s78, %0, 6\n\t"
      "s_lshl_b32 s78, s78, 15\n\t"
      "s_lshl_b32 s79, %1, 12\n\t"
      "s_add_u32 s79, s79, s78\n\t"
      "buffer_load_dwordx4 v[128:131], v118, s[64:67], s79 offen \n\t"
      "buffer_load_dwordx4 v[132:135], v118, s[64:67], s79 offen offset:256 \n\t"
      "buffer_load_dwordx4 v[136:139], v118, s[64:67], s79 offen offset:512 \n\t"
      "buffer_load_dwordx4 v[140:143], v118, s[64:67], s79 offen offset:768 \n\t"
      "s_add_u32 s79, s79, 16384\n\t"
      "buffer_load_dwordx4 v[144:147], v118, s[64:67], s79 offen \n\t"
      "buffer_load_dwordx4 v[148:151], v118, s[64:67], s79 offen offset:256 \n\t"
      "buffer_load_dwordx4 v[152:155], v118, s[64:67], s79 offen offset:512 \n\t"
      "buffer_load_dwordx4 v[156:159], v118, s[64:67], s79 offen offset:768 \n\t"
      "s_mov_b64 s[64:65], s[38:39]\n\t"
      "s_mov_b64 s[64:65], s[38:39]\n\t"
      "buffer_load_dword v107, off, s[60:63], 0 sc1\n\t"
      "v_lshrrev_b32 v101, 4, v96\n\t"
      "v_and_b32 v102, 15, v96\n\t"
      "v_lshlrev_b32 v103, 2, v101\n\t"
      "v_xor_b32 v102, v102, v103\n\t"
      "v_lshlrev_b32 v108, 13, v101\n\t"
      "v_lshl_add_u32 v108, v102, 4, v108\n\t"
      "s_lshl_b32 s78, s82, 8\n\t"
      "s_lshl_b32 s79, %1, 16\n\t"
      "s_add_u32 s78, s78, s79\n\t"
      "v_add_u32 v108, s78, v108\n\t"
      "s_lshl_b32 s76, %1, 11\n\t"
      "s_mov_b32 s77, 0\n\t"
      "s_add_u32 m0, s76, 0\n\t"
      "s_add_u32 s68, s77, 0\n\t"
      "buffer_load_dwordx4 v108, s[64:67], s68 offen lds\n\t"
      "s_add_u32 m0, s76, 1024\n\t"
      "s_add_u32 s68, s77, 32768\n\t"
      "s_add_u32 s76, s76, 8192\n\t"
      "s_add_u32 s77, s77, 0x40000\n\t"
      "buffer_load_dwordx4 v108, s[64:67], s68 offen lds\n\t"
      "s_mov_b32 s84, 0xffff0000\n\t"
      "s_mov_b32 s86, 0\n\t"
      "s_lshl_b32 s85, %1, 13\n\t"
      "v_lshlrev_b32 v97, 4, v96\n\t"
      "v_lshrrev_b32 v101, 4, v96\n\t"
      "v_and_b32 v102, 3, v105\n\t"
      "v_lshlrev_b32 v100, 9, v101\n\t"
      "v_lshl_add_u32 v100, v102, 6, v100\n\t"
      "v_lshl_add_u32 v100, v104, 4, v100\n\t"
      "v_lshlrev_b32 v106, 10, v101\n\t"
      "v_lshl_add_u32 v106, v102, 8, v106\n\t"
      "v_lshl_add_u32 v106, v104, 4, v106\n\t"
      "s_lshl_b32 s74, %0, 14\n\t"
      "s_add_u32 s74, s74, s72\n\t"
      "s_add_u32 s74, s74, 6520832\n\t"
      "s_lshr_b32 s78, %0, 6\n\t"
      "v_min_u32 v104, 31, v96\n\t"
      "v_lshl_add_u32 v104, v104, 1, s78\n\t"
      "s_lshr_b32 s79, %0, 5\n\t"
      "s_and_b32 s83, s79, 1\n\t"
      "s_add_u32 s80, s85, 6520832\n\t"
      "v_lshl_add_u32 v101, v104, 15, s80\n\t"
      "s_mul_i32 s80, s82, 24576\n\t"
      "s_lshl_b32 s72, %1, 11\n\t"
      "s_add_u32 s80, s80, s72\n\t"
      "s_add_u32 s80, s80, 8617984\n\t"
      "s_lshl_b32 s72, s83, 13\n\t"
      "s_add_u32 s80, s80, s72\n\t"
      "v_mov_b32 v102, s80\n\t"
      "s_mov_b32 s72, 786432\n\t"
      "v_mad_u32_u24 v102, v104, s72, v102\n\t"
      "s_lshl_b32 s80, s82, 6\n\t"
      "s_lshl_b32 s72, s83, 2\n\t"
      "s_add_u32 s80, s80, s72\n\t"
      "s_add_u32 s80, s80, 256\n\t"
      "v_lshrrev_b32 v105, 2, v104\n\t"
      "v_lshl_add_u32 v103, v105, 11, s80\n\t"
      "v_and_b32 v104, 3, v104\n\t"
      "v_lshl_add_u32 v103, v104, 4, v103\n\t"
      "s_lshl_b32 s80, s82, 7\n\t"
      "s_lshl_b32 s79, s79, 2\n\t"
      "s_add_u32 s80, s80, s79\n\t"
      "s_add_u32 s80, s80, 33024\n\t"
      "v_writelane_b32 v103, s80, 0\n\t"
      "v_writelane_b32 v103, 0, 63\n\t"
      "s_cmp_eq_u32 %1, 0\n\t"
      "s_cselect_b64 s[90:91], 1, 0\n\t"
      "v_lshrrev_b32 v104, 4, v96\n\t"
      "v_bfe_u32 v105, v96, 2, 2\n\t"
      "v_and_b32 v110, 3, v96\n\t"
      "s_lshl_b32 s78, %1, 2\n\t"
      "v_lshl_add_u32 v109, v104, 3, v105\n\t"
      "v_lshlrev_b32 v109, 8, v109\n\t"
      "v_or_b32 v110, s78, v110\n\t"
      "v_lshlrev_b32 v105, 2, v105\n\t"
      "v_xor_b32 v110, v110, v105\n\t"
      "v_lshl_add_u32 v109, v110, 4, v109\n\t"
      "s_lshl_b32 s78, %1, 6\n\t"
      "s_add_u32 s78, s78, 131072\n\t"
      "v_add_u32 v110, s78, v106\n\t"
      "s_bfe_u32 s80, %0, 0x10005\n\t"
      "s_and_b32 s79, %1, 1\n\t"
      "v_bfe_u32 v105, v96, 4, 1\n\t"
      "v_lshlrev_b32 v105, 1, v105\n\t"
      "v_and_b32 v116, 7, v96\n\t"
      "v_xor_b32 v116, v116, v105\n\t"
      "s_lshl_b32 s78, s79, 2\n\t"
      "v_xor_b32 v116, s78, v116\n\t"
      "v_lshlrev_b32 v116, 4, v116\n\t"
      "v_lshrrev_b32 v105, 3, v96\n\t"
      "v_lshl_add_u32 v116, v105, 13, v116\n\t"
      "s_lshl_b32 s78, s82, 8\n\t"
      "s_lshl_b32 s72, s80, 7\n\t"
      "s_add_u32 s78, s78, s72\n\t"
      "s_lshl_b32 s72, %1, 16\n\t"
      "s_add_u32 s78, s78, s72\n\t"
      "v_add_u32 v116, s78, v116\n\t"
      "v_bfe_u32 v105, v96, 2, 2\n\t"
      "v_lshl_add_u32 v117, v104, 3, v105\n\t"
      "v_lshlrev_b32 v117, 7, v117\n\t"
      "v_bfe_u32 v105, v96, 3, 2\n\t"
      "v_xor_b32 v105, %1, v105\n\t"
      "v_lshl_add_u32 v117, v105, 5, v117\n\t"
      "v_and_b32 v105, 3, v96\n\t"
      "v_lshl_add_u32 v117, v105, 3, v117\n\t"
      "v_and_b32 v111, 15, v96\n\t"
      "v_lshlrev_b32 v111, 4, v111\n\t"
      "v_and_b32 v105, 1, v104\n\t"
      "v_lshl_add_u32 v111, v105, 10, v111\n\t"
      "v_lshrrev_b32 v105, 1, v104\n\t"
      "v_lshl_add_u32 v111, v105, 8, v111\n\t"
      "s_lshl_b32 s78, s79, 9\n\t"
      "s_add_u32 s78, s78, 16384\n\t"
      "s_lshl_b32 s80, s80, 12\n\t"
      "s_sub_u32 s78, s78, s80\n\t"
      "s_add_u32 s80, %1, 1\n\t"
      "s_lshr_b32 s80, s80, 1\n\t"
      "s_lshl_b32 s80, s80, 11\n\t"
      "s_sub_u32 s78, s78, s80\n\t"
      "v_add_u32 v111, s78, v111\n\t"
      "s_mov_b32 s69, 0\n\t"
      "s_add_u32 m0, s76, 0\n\t"
      "s_add_u32 s68, s77, 0\n\t"
      "buffer_load_dwordx4 v108, s[64:67], s68 offen lds\n\t"
      "s_add_u32 m0, s76, 1024\n\t"
      "s_add_u32 s68, s77, 32768\n\t"
      "s_add_u32 s76, s76, 8192\n\t"
      "s_add_u32 s77, s77, 0x40000\n\t"
      "buffer_load_dwordx4 v108, s[64:67], s68 offen lds\n\t"
      "s_add_u32 m0, s76, 0\n\t"
      "s_add_u32 s68, s77, 0\n\t"
      "buffer_load_dwordx4 v108, s[64:67], s68 offen lds\n\t"
      "s_add_u32 m0, s76, 1024\n\t"
      "s_add_u32 s68, s77, 32768\n\t"
      "s_add_u32 s76, s76, 8192\n\t"
      "s_add_u32 s77, s77, 0x40000\n\t"
      "buffer_load_dwordx4 v108, s[64:67], s68 offen lds\n\t"
      "s_add_u32 m0, s76, 0\n\t"
      "s_add_u32 s68, s77, 0\n\t"
      "buffer_load_dwordx4 v108, s[64:67], s68 offen lds\n\t"
      "s_add_u32 m0, s76, 1024\n\t"
      "s_add_u32 s68, s77, 32768\n\t"
      "s_add_u32 s76, s76, 8192\n\t"
      "s_add_u32 s77, s77, 0x40000\n\t"
      "buffer_load_dwordx4 v108, s[64:67], s68 offen lds\n\t"
      "s_mov_b64 s[88:89], 0\n\t"
      "s_waitcnt vmcnt(8)\n\t"
      "GLN%=:\n\t"
      "v_and_b32 v1, s84, v64\n\t"
      "v_lshlrev_b32 v0, 16, v64\n\t"
      "v_and_b32 v3, s84, v65\n\t"
      "v_lshlrev_b32 v2, 16, v65\n\t"
      "v_and_b32 v5, s84, v66\n\t"
      "v_lshlrev_b32 v4, 16, v66\n\t"
      "v_and_b32 v7, s84, v67\n\t"
      "v_lshlrev_b32 v6, 16, v67\n\t"
      "v_and_b32 v9, s84, v68\n\t"
      "v_lshlrev_b32 v8, 16, v68\n\t"
      "v_and_b32 v11, s84, v69\n\t"
      "v_lshlrev_b32 v10, 16, v69\n\t"
      "v_and_b32 v13, s84, v70\n\t"
      "v_lshlrev_b32 v12, 16, v70\n\t"
      "v_and_b32 v15, s84, v71\n\t"
      "v_lshlrev_b32 v14, 16, v71\n\t"
      "s_mov_b64 exec, s[88:89]\n\t"
      "s_add_u32 m0, s76, 0\n\t"
      "s_add_u32 s68, s77, 0\n\t"
      "buffer_load_dwordx4 v108, s[64:67], s68 offen lds\n\t"
      "s_mov_b64 exec, -1\n\t"
      "v_and_b32 v17, s84, v72\n\t"
      "v_lshlrev_b32 v16, 16, v72\n\t"
      "v_and_b32 v19, s84, v73\n\t"
      "v_lshlrev_b32 v18, 16, v73\n\t"
      "v_and_b32 v21, s84, v74\n\t"
      "v_lshlrev_b32 v20, 16, v74\n\t"
      "v_and_b32 v23, s84, v75\n\t"
      "v_lshlrev_b32 v22, 16, v75\n\t"
      "v_and_b32 v25, s84, v76\n\t"
      "v_lshlrev_b32 v24, 16, v76\n\t"
      "v_and_b32 v27, s84, v77\n\t"
      "v_lshlrev_b32 v26, 16, v77\n\t"
      "v_and_b32 v29, s84, v78\n\t"
      "v_lshlrev_b32 v28, 16, v78\n\t"
      "v_and_b32 v31, s84, v79\n\t"
      "v_lshlrev_b32 v30, 16, v79\n\t"
      "s_mov_b64 exec, s[88:89]\n\t"
      "s_add_u32 m0, s76, 1024\n\t"
      "s_add_u32 s68, s77, 32768\n\t"
      "s_add_u32 s76, s76, 8192\n\t"
      "s_add_u32 s77, s77, 0x40000\n\t"
      "buffer_load_dwordx4 v108, s[64:67], s68 offen lds\n\t"
      "s_mov_b64 exec, -1\n\t"
      "v_pk_add_f32 v[32:33], v[0:1], v[2:3]\n\t"
      "v_pk_add_f32 v[34:35], v[4:5], v[6:7]\n\t"
      "v_pk_add_f32 v[36:37], v[8:9], v[10:11]\n\t"
      "v_pk_add_f32 v[38:39], v[12:13], v[14:15]\n\t"
      "v_pk_add_f32 v[40:41], v[16:17], v[18:19]\n\t"
      "v_pk_add_f32 v[42:43], v[20:21], v[22:23]\n\t"
      "v_pk_add_f32 v[44:45], v[24:25], v[26:27]\n\t"
      "v_pk_add_f32 v[46:47], v[28:29], v[30:31]\n\t"
      "v_pk_add_f32 v[32:33], v[32:33], v[40:41]\n\t"
      "v_pk_add_f32 v[34:35], v[34:35], v[42:43]\n\t"
      "v_pk_add_f32 v[36:37], v[36:37], v[44:45]\n\t"
      "v_pk_add_f32 v[38:39], v[38:39], v[46:47]\n\t"
      "v_pk_add_f32 v[32:33], v[32:33], v[36:37]\n\t"
      "v_pk_add_f32 v[34:35], v[34:35], v[38:39]\n\t"
      "v_pk_add_f32 v[32:33], v[32:33], v[34:35]\n\t"
      "v_add_f32 v32, v32, v33\n\t"
      "s_mov_b64 exec, s[88:89]\n\t"
      "s_add_u32 m0, s76, 0\n\t"
      "s_add_u32 s68, s77, 0\n\t"
      "buffer_load_dwordx4 v108, s[64:67], s68 offen lds\n\t"
      "s_mov_b64 exec, -1\n\t"
      "v_add_f32_dpp v32, v32, v32 row_ror:4 row_mask:0xf bank_mask:0xf\n\t"
      "s_nop 1\n\t"
      "v_add_f32_dpp v32, v32, v32 row_ror:8 row_mask:0xf bank_mask:0xf\n\t"
      "v_mov_b32 v33, v32\n\t"
      "s_nop 1\n\t"
      "v_permlane16_swap_b32 v32, v33\n\t"
      "v_add_f32 v32, v32, v33\n\t"
      "v_mov_b32 v33, v32\n\t"
      "s_nop 1\n\t"
      "v_permlane32_swap_b32 v32, v33\n\t"
      "v_add_f32 v32, v32, v33\n\t"
      "v_mul_f32 v80, 0x3b000000, v32\n\t"
      "v_pk_add_f32 v[32:33], v[0:1], v[80:81] op_sel_hi:[1,0] neg_lo:[0,1] neg_hi:[0,1]\n\t"
      "v_pk_add_f32 v[34:35], v[2:3], v[80:81] op_sel_hi:[1,0] neg_lo:[0,1] neg_hi:[0,1]\n\t"
      "v_pk_add_f32 v[36:37], v[4:5], v[80:81] op_sel_hi:[1,0] neg_lo:[0,1] neg_hi:[0,1]\n\t"
      "v_pk_add_f32 v[38:39], v[6:7], v[80:81] op_sel_hi:[1,0] neg_lo:[0,1] neg_hi:[0,1]\n\t"
      "v_pk_add_f32 v[40:41], v[8:9], v[80:81] op_sel_hi:[1,0] neg_lo:[0,1] neg_hi:[0,1]\n\t"
      "v_pk_add_f32 v[42:43], v[10:11], v[80:81] op_sel_hi:[1,0] neg_lo:[0,1] neg_hi:[0,1]\n\t"
      "v_pk_add_f32 v[44:45], v[12:13], v[80:81] op_sel_hi:[1,0] neg_lo:[0,1] neg_hi:[0,1]\n\t"
      "s_mov_b64 exec, s[88:89]\n\t"
      "s_add_u32 m0, s76, 1024\n\t"
      "s_add_u32 s68, s77, 32768\n\t"
      "s_add_u32 s76, s76, 8192\n\t"
      "s_add_u32 s77, s77, 0x40000\n\t"
      "buffer_load_dwordx4 v108, s[64:67], s68 offen lds\n\t"
      "s_mov_b64 exec, -1\n\t"
      "v_pk_add_f32 v[46:47], v[14:15], v[80:81] op_sel_hi:[1,0] neg_lo:[0,1] neg_hi:[0,1]\n\t"
      "v_pk_add_f32 v[48:49], v[16:17], v[80:81] op_sel_hi:[1,0] neg_lo:[0,1] neg_hi:[0,1]\n\t"
      "v_pk_add_f32 v[50:51], v[18:19], v[80:81] op_sel_hi:[1,0] neg_lo:[0,1] neg_hi:[0,1]\n\t"
      "v_pk_add_f32 v[52:53], v[20:21], v[80:81] op_sel_hi:[1,0] neg_lo:[0,1] neg_hi:[0,1]\n\t"
      "v_pk_add_f32 v[54:55], v[22:23], v[80:81] op_sel_hi:[1,0] neg_lo:[0,1] neg_hi:[0,1]\n\t"
      "v_pk_add_f32 v[56:57], v[24:25], v[80:81] op_sel_hi:[1,0] neg_lo:[0,1] neg_hi:[0,1]\n\t"
      "v_pk_add_f32 v[58:59], v[26:27], v[80:81] op_sel_hi:[1,0] neg_lo:[0,1] neg_hi:[0,1]\n\t"
      "v_pk_add_f32 v[60:61], v[28:29], v[80:81] op_sel_hi:[1,0] neg_lo:[0,1] neg_hi:[0,1]\n\t"
      "v_pk_add_f32 v[62:63], v[30:31], v[80:81] op_sel_hi:[1,0] neg_lo:[0,1] neg_hi:[0,1]\n\t"
      "v_pk_mul_f32 v[64:65], v[32:33], v[32:33]\n\t"
      "v_pk_mul_f32 v[66:67], v[34:35], v[34:35]\n\t"
      "v_pk_mul_f32 v[68:69], v[36:37], v[36:37]\n\t"
      "v_pk_mul_f32 v[70:71], v[38:39], v[38:39]\n\t"
      "v_pk_fma_f32 v[64:65], v[40:41], v[40:41], v[64:65]\n\t"
      "v_pk_fma_f32 v[66:67], v[42:43], v[42:43], v[66:67]\n\t"
      "v_pk_fma_f32 v[68:69], v[44:45], v[44:45], v[68:69]\n\t"
      "s_mov_b64 exec, s[88:89]\n\t"
      "s_add_u32 m0, s76, 0\n\t"
      "s_add_u32 s68, s77, 0\n\t"
      "buffer_load_dwordx4 v108, s[64:67], s68 offen lds\n\t"
      "s_mov_b64 exec, -1\n\t"
      "v_pk_fma_f32 v[70:71], v[46:47], v[46:47], v[70:71]\n\t"
      "v_pk_fma_f32 v[64:65], v[48:49], v[48:49], v[64:65]\n\t"
      "v_pk_fma_f32 v[66:67], v[50:51], v[50:51], v[66:67]\n\t"
      "v_pk_fma_f32 v[68:69], v[52:53], v[52:53], v[68:69]\n\t"
      "v_pk_fma_f32 v[70:71], v[54:55], v[54:55], v[70:71]\n\t"
      "v_pk_fma_f32 v[64:65], v[56:57], v[56:57], v[64:65]\n\t"
      "v_pk_fma_f32 v[66:67], v[58:59], v[58:59], v[66:67]\n\t"
      "v_pk_fma_f32 v[68:69], v[60:61], v[60:61], v[68:69]\n\t"
      "v_pk_fma_f32 v[70:71], v[62:63], v[62:63], v[70:71]\n\t"
      "v_pk_add_f32 v[64:65], v[64:65], v[66:67]\n\t"
      "v_pk_add_f32 v[68:69], v[68:69], v[70:71]\n\t"
      "v_pk_add_f32 v[64:65], v[64:65], v[68:69]\n\t"
      "v_add_f32 v64, v64, v65\n\t"
      "s_nop 1\n\t"
      "v_add_f32_dpp v64, v64, v64 row_ror:4 row_mask:0xf bank_mask:0xf\n\t"
      "s_nop 1\n\t"
      "v_add_f32_dpp v64, v64, v64 row_ror:8 row_mask:0xf bank_mask:0xf\n\t"
      "v_mov_b32 v65, v64\n\t"
      "s_mov_b64 exec, s[88:89]\n\t"
      "s_add_u32 m0, s76, 1024\n\t"
      "s_add_u32 s68, s77, 32768\n\t"
      "s_add_u32 s76, s76, 8192\n\t"
      "s_add_u32 s77, s77, 0x40000\n\t"
      "buffer_load_dwordx4 v108, s[64:67], s68 offen lds\n\t"
      "s_mov_b64 exec, -1\n\t"
      "v_permlane16_swap_b32 v64, v65\n\t"
      "v_add_f32 v64, v64, v65\n\t"
      "v_mov_b32 v65, v64\n\t"
      "s_nop 1\n\t"
      "v_permlane32_swap_b32 v64, v65\n\t"
      "v_add_f32 v64, v64, v65\n\t"
      "v_mul_f32 v81, 0x3b000000, v64\n\t"
      "v_cvt_pk_bf16_f32 v80, v80, v81\n\t"
      "v_and_b32 v81, s84, v80\n\t"
      "v_lshlrev_b32 v80, 16, v80\n\t"
      "v_add_f32 v81, 0x3727c5ac, v81\n\t"
      "v_pk_add_f32 v[32:33], v[0:1], v[80:81] op_sel_hi:[1,0] neg_lo:[0,1] neg_hi:[0,1]\n\t"
      "v_pk_add_f32 v[34:35], v[2:3], v[80:81] op_sel_hi:[1,0] neg_lo:[0,1] neg_hi:[0,1]\n\t"
      "v_pk_add_f32 v[36:37], v[4:5], v[80:81] op_sel_hi:[1,0] neg_lo:[0,1] neg_hi:[0,1]\n\t"
      "v_pk_add_f32 v[38:39], v[6:7], v[80:81] op_sel_hi:[1,0] neg_lo:[0,1] neg_hi:[0,1]\n\t"
      "v_pk_add_f32 v[40:41], v[8:9], v[80:81] op_sel_hi:[1,0] neg_lo:[0,1] neg_hi:[0,1]\n\t"
      "v_pk_add_f32 v[42:43], v[10:11], v[80:81] op_sel_hi:[1,0] neg_lo:[0,1] neg_hi:[0,1]\n\t"
      "s_mov_b64 exec, s[88:89]\n\t"
      "s_add_u32 m0, s76, 0\n\t"
      "s_add_u32 s68, s77, 0\n\t"
      "buffer_load_dwordx4 v108, s[64:67], s68 offen lds\n\t"
      "s_mov_b64 exec, -1\n\t"
      "v_pk_add_f32 v[44:45], v[12:13], v[80:81] op_sel_hi:[1,0] neg_lo:[0,1] neg_hi:[0,1]\n\t"
      "v_pk_add_f32 v[46:47], v[14:15], v[80:81] op_sel_hi:[1,0] neg_lo:[0,1] neg_hi:[0,1]\n\t"
      "v_pk_add_f32 v[48:49], v[16:17], v[80:81] op_sel_hi:[1,0] neg_lo:[0,1] neg_hi:[0,1]\n\t"
      "v_pk_add_f32 v[50:51], v[18:19], v[80:81] op_sel_hi:[1,0] neg_lo:[0,1] neg_hi:[0,1]\n\t"
      "v_pk_add_f32 v[52:53], v[20:21], v[80:81] op_sel_hi:[1,0] neg_lo:[0,1] neg_hi:[0,1]\n\t"
      "v_pk_add_f32 v[54:55], v[22:23], v[80:81] op_sel_hi:[1,0] neg_lo:[0,1] neg_hi:[0,1]\n\t"
      "v_pk_add_f32 v[56:57], v[24:25], v[80:81] op_sel_hi:[1,0] neg_lo:[0,1] neg_hi:[0,1]\n\t"
      "v_pk_add_f32 v[58:59], v[26:27], v[80:81] op_sel_hi:[1,0] neg_lo:[0,1] neg_hi:[0,1]\n\t"
      "v_pk_add_f32 v[60:61], v[28:29], v[80:81] op_sel_hi:[1,0] neg_lo:[0,1] neg_hi:[0,1]\n\t"
      "v_pk_add_f32 v[62:63], v[30:31], v[80:81] op_sel_hi:[1,0] neg_lo:[0,1] neg_hi:[0,1]\n\t"
      "v_cvt_pk_bf16_f32 v81, v81, v81\n\t"
      "v_and_b32 v81, s84, v81\n\t"
      "v_sqrt_f32 v81, v81\n\t"
      "v_cvt_pk_bf16_f32 v32, v32, v33\n\t"
      "v_cvt_pk_bf16_f32 v34, v34, v35\n\t"
      "v_cvt_pk_bf16_f32 v36, v36, v37\n\t"
      "s_mov_b64 exec, s[88:89]\n\t"
      "s_add_u32 m0, s76, 1024\n\t"
      "s_add_u32 s68, s77, 32768\n\t"
      "s_add_u32 s76, s76, 8192\n\t"
      "s_add_u32 s77, s77, 0x40000\n\t"
      "buffer_load_dwordx4 v108, s[64:67], s68 offen lds\n\t"
      "s_mov_b64 exec, -1\n\t"
      "v_cvt_pk_bf16_f32 v38, v38, v39\n\t"
      "v_cvt_pk_bf16_f32 v40, v40, v41\n\t"
      "v_cvt_pk_bf16_f32 v42, v42, v43\n\t"
      "v_cvt_pk_bf16_f32 v44, v44, v45\n\t"
      "v_cvt_pk_bf16_f32 v46, v46, v47\n\t"
      "v_cvt_pk_bf16_f32 v48, v48, v49\n\t"
      "v_cvt_pk_bf16_f32 v50, v50, v51\n\t"
      "v_cvt_pk_bf16_f32 v52, v52, v53\n\t"
      "v_cvt_pk_bf16_f32 v54, v54, v55\n\t"
      "v_cvt_pk_bf16_f32 v56, v56, v57\n\t"
      "v_cvt_pk_bf16_f32 v58, v58, v59\n\t"
      "v_cvt_pk_bf16_f32 v60, v60, v61\n\t"
      "v_cvt_pk_bf16_f32 v62, v62, v63\n\t"
      "v_cvt_pk_bf16_f32 v81, v81, v81\n\t"
      "v_and_b32 v81, s84, v81\n\t"
      "v_rcp_f32 v80, v81\n\t"
      "s_mov_b64 exec, s[88:89]\n\t"
      "s_add_u32 m0, s76, 0\n\t"
      "s_add_u32 s68, s77, 0\n\t"
      "buffer_load_dwordx4 v108, s[64:67], s68 offen lds\n\t"
      "s_mov_b64 exec, -1\n\t"
      "v_and_b32 v33, s84, v32\n\t"
      "v_lshlrev_b32 v32, 16, v32\n\t"
      "v_and_b32 v35, s84, v34\n\t"
      "v_lshlrev_b32 v34, 16, v34\n\t"
      "v_and_b32 v37, s84, v36\n\t"
      "v_lshlrev_b32 v36, 16, v36\n\t"
      "v_and_b32 v39, s84, v38\n\t"
      "v_lshlrev_b32 v38, 16, v38\n\t"
      "v_pk_mul_f32 v[32:33], v[32:33], v[80:81] op_sel_hi:[1,0]\n\t"
      "v_pk_mul_f32 v[34:35], v[34:35], v[80:81] op_sel_hi:[1,0]\n\t"
      "v_pk_mul_f32 v[36:37], v[36:37], v[80:81] op_sel_hi:[1,0]\n\t"
      "v_pk_mul_f32 v[38:39], v[38:39], v[80:81] op_sel_hi:[1,0]\n\t"
      "v_cvt_pk_bf16_f32 v64, v32, v33\n\t"
      "v_cvt_pk_bf16_f32 v65, v34, v35\n\t"
      "v_cvt_pk_bf16_f32 v66, v36, v37\n\t"
      "v_cvt_pk_bf16_f32 v67, v38, v39\n\t"
      "s_mov_b64 exec, s[88:89]\n\t"
      "s_add_u32 m0, s76, 1024\n\t"
      "s_add_u32 s68, s77, 32768\n\t"
      "s_add_u32 s76, s76, 8192\n\t"
      "s_add_u32 s77, s77, 0x40000\n\t"
      "buffer_load_dwordx4 v108, s[64:67], s68 offen lds\n\t"
      "s_mov_b64 exec, -1\n\t"
      "v_and_b32 v41, s84, v40\n\t"
      "v_lshlrev_b32 v40, 16, v40\n\t"
      "v_and_b32 v43, s84, v42\n\t"
      "v_lshlrev_b32 v42, 16, v42\n\t"
      "v_and_b32 v45, s84, v44\n\t"
      "v_lshlrev_b32 v44, 16, v44\n\t"
      "v_and_b32 v47, s84, v46\n\t"
      "v_lshlrev_b32 v46, 16, v46\n\t"
      "v_pk_mul_f32 v[40:41], v[40:41], v[80:81] op_sel_hi:[1,0]\n\t"
      "v_pk_mul_f32 v[42:43], v[42:43], v[80:81] op_sel_hi:[1,0]\n\t"
      "v_pk_mul_f32 v[44:45], v[44:45], v[80:81] op_sel_hi:[1,0]\n\t"
      "v_pk_mul_f32 v[46:47], v[46:47], v[80:81] op_sel_hi:[1,0]\n\t"
      "v_cvt_pk_bf16_f32 v68, v40, v41\n\t"
      "v_cvt_pk_bf16_f32 v69, v42, v43\n\t"
      "v_cvt_pk_bf16_f32 v70, v44, v45\n\t"
      "v_cvt_pk_bf16_f32 v71, v46, v47\n\t"
      "s_mov_b64 exec, s[88:89]\n\t"
      "s_add_u32 m0, s76, 0\n\t"
      "s_add_u32 s68, s77, 0\n\t"
      "buffer_load_dwordx4 v108, s[64:67], s68 offen lds\n\t"
      "s_mov_b64 exec, -1\n\t"
      "v_and_b32 v49, s84, v48\n\t"
      "v_lshlrev_b32 v48, 16, v48\n\t"
      "v_and_b32 v51, s84, v50\n\t"
      "v_lshlrev_b32 v50, 16, v50\n\t"
      "v_and_b32 v53, s84, v52\n\t"
      "v_lshlrev_b32 v52, 16, v52\n\t"
      "v_and_b32 v55, s84, v54\n\t"
      "v_lshlrev_b32 v54, 16, v54\n\t"
      "v_pk_mul_f32 v[48:49], v[48:49], v[80:81] op_sel_hi:[1,0]\n\t"
      "v_pk_mul_f32 v[50:51], v[50:51], v[80:81] op_sel_hi:[1,0]\n\t"
      "v_pk_mul_f32 v[52:53], v[52:53], v[80:81] op_sel_hi:[1,0]\n\t"
      "v_pk_mul_f32 v[54:55], v[54:55], v[80:81] op_sel_hi:[1,0]\n\t"
      "v_cvt_pk_bf16_f32 v72, v48, v49\n\t"
      "v_cvt_pk_bf16_f32 v73, v50, v51\n\t"
      "v_cvt_pk_bf16_f32 v74, v52, v53\n\t"
      "v_cvt_pk_bf16_f32 v75, v54, v55\n\t"
      "s_mov_b64 exec, s[88:89]\n\t"
      "s_add_u32 m0, s76, 1024\n\t"
      "s_add_u32 s68, s77, 32768\n\t"
      "s_add_u32 s76, s76, 8192\n\t"
      "s_add_u32 s77, s77, 0x40000\n\t"
      "buffer_load_dwordx4 v108, s[64:67], s68 offen lds\n\t"
      "s_mov_b64 exec, -1\n\t"
      "v_and_b32 v57, s84, v56\n\t"
      "v_lshlrev_b32 v56, 16, v56\n\t"
      "v_and_b32 v59, s84, v58\n\t"
      "v_lshlrev_b32 v58, 16, v58\n\t"
      "v_and_b32 v61, s84, v60\n\t"
      "v_lshlrev_b32 v60, 16, v60\n\t"
      "v_and_b32 v63, s84, v62\n\t"
      "v_lshlrev_b32 v62, 16, v62\n\t"
      "v_pk_mul_f32 v[56:57], v[56:57], v[80:81] op_sel_hi:[1,0]\n\t"
      "v_pk_mul_f32 v[58:59], v[58:59], v[80:81] op_sel_hi:[1,0]\n\t"
      "v_pk_mul_f32 v[60:61], v[60:61], v[80:81] op_sel_hi:[1,0]\n\t"
      "v_pk_mul_f32 v[62:63], v[62:63], v[80:81] op_sel_hi:[1,0]\n\t"
      "v_cvt_pk_bf16_f32 v76, v56, v57\n\t"
      "v_cvt_pk_bf16_f32 v77, v58, v59\n\t"
      "v_cvt_pk_bf16_f32 v78, v60, v61\n\t"
      "v_cvt_pk_bf16_f32 v79, v62, v63\n\t"
      "s_cmp_lg_u32 s69, 0\n\t"
      "s_cbranch_scc1 GLN1%=\n\t"
      "buffer_store_dwordx4 v[64:67], v106, s[60:63], s74 offen sc1\n\t"
      "s_add_u32 s74, s74, 4096\n\t"
      "buffer_store_dwordx4 v[68:71], v106, s[60:63], s74 offen sc1\n\t"
      "s_add_u32 s74, s74, 4096\n\t"
      "buffer_store_dwordx4 v[72:75], v106, s[60:63], s74 offen sc1\n\t"
      "s_add_u32 s74, s74, 4096\n\t"
      "buffer_store_dwordx4 v[76:79], v106, s[60:63], s74 offen sc1\n\t"
      "s_add_u32 s74, s74, 4096\n\t"
      "s_sub_u32 s76, s76, 49152\n\t"
      "s_sub_u32 s77, s77, 1572864\n\t"
      "s_mov_b64 s[88:89], -1\n\t"
      "v_readfirstlane_b32 %3, v107\n\t"
      "v_mov_b64 v[64:65], v[128:129]\n\t"
      "v_mov_b64 v[66:67], v[130:131]\n\t"
      "v_mov_b64 v[68:69], v[132:133]\n\t"
      "v_mov_b64 v[70:71], v[134:135]\n\t"
      "v_mov_b64 v[72:73], v[136:137]\n\t"
      "v_mov_b64 v[74:75], v[138:139]\n\t"
      "v_mov_b64 v[76:77], v[140:141]\n\t"
      "v_mov_b64 v[78:79], v[142:143]\n\t"
      "s_mul_i32 %3, %3, 0x9e3779b1\n\t"
      "s_add_u32 %3, %3, 0x7f4a7c15\n\t"
      "s_mov_b32 s69, 1\n\t"
      "s_branch GLN%=\n\t"
      "GLN1%=:\n\t"
      "ds_write_b128 v110, v[64:67]\n\t"
      "ds_write_b128 v110, v[68:71] offset:4096\n\t"
      "ds_write_b128 v110, v[72:75] offset:8192\n\t"
      "ds_write_b128 v110, v[76:79] offset:12288\n\t"
      "s_cmp_eq_u32 s69, 2\n\t"
      "s_cbranch_scc1 GLN2%=\n\t"
      "v_mov_b32 v107, %3\n\t"
      "v_add_u32 v110, 0x4000, v110\n\t"
      "s_lshl_b32 s79, %0, 7\n\t"
      "s_add_u32 s79, s79, 37120\n\t"
      "s_waitcnt vmcnt(12) lgkmcnt(0)\n\t"
      "s_barrier\n\t"
      "s_mov_b64 exec, s[90:91]\n\t"
      "buffer_store_dword v107, off, s[60:63], s79 sc1\n\t"
      "s_mov_b64 exec, -1\n\t"
      "v_mov_b64 v[64:65], v[144:145]\n\t"
      "v_mov_b64 v[66:67], v[146:147]\n\t"
      "v_mov_b64 v[68:69], v[148:149]\n\t"
      "v_mov_b64 v[70:71], v[150:151]\n\t"
      "v_mov_b64 v[72:73], v[152:153]\n\t"
      "v_mov_b64 v[74:75], v[154:155]\n\t"
      "v_mov_b64 v[76:77], v[156:157]\n\t"
      "v_mov_b64 v[78:79], v[158:159]\n\t"
      "s_mov_b32 s69, 2\n\t"
      "s_branch GLN%=\n\t"
      "GLN2%=:\n\t"
      "s_waitcnt lgkmcnt(10)\n\t"
      "ds_read_b64_tr_b16 a[0:1], v109\n\t"
      "ds_read_b64_tr_b16 a[2:3], v109 offset:1024\n\t"
      "ds_read_b64_tr_b16 a[4:5], v109 offset:8\n\t"
      "ds_read_b64_tr_b16 a[6:7], v109 offset:1032\n\t"
      "s_waitcnt lgkmcnt(10)\n\t"
      "ds_read_b64_tr_b16 a[8:9], v109 offset:8192\n\t"
      "ds_read_b64_tr_b16 a[10:11], v109 offset:9216\n\t"
      "ds_read_b64_tr_b16 a[12:13], v109 offset:8200\n\t"
      "ds_read_b64_tr_b16 a[14:15], v109 offset:9224\n\t"
      "s_waitcnt lgkmcnt(10)\n\t"
      "ds_read_b64_tr_b16 a[16:17], v109 offset:16384\n\t"
      "ds_read_b64_tr_b16 a[18:19], v109 offset:17408\n\t"
      "ds_read_b64_tr_b16 a[20:21], v109 offset:16392\n\t"
      "ds_read_b64_tr_b16 a[22:23], v109 offset:17416\n\t"
      "s_waitcnt lgkmcnt(10)\n\t"
      "ds_read_b64_tr_b16 a[24:25], v109 offset:24576\n\t"
      "ds_read_b64_tr_b16 a[26:27], v109 offset:25600\n\t"
      "ds_read_b64_tr_b16 a[28:29], v109 offset:24584\n\t"
      "ds_read_b64_tr_b16 a[30:31], v109 offset:25608\n\t"
      "v_lshlrev_b32 v106, 8, v96\n\t"
      "s_mov_b32 s79, 37120\n\t"
      "s_mov_b32 s81, 0x40000\n\t"
      "v_add_u32 v98, 131072, v97\n\t"
      "v_readlane_b32 s73, v102, 0\n\t"
      "v_readlane_b32 s75, v103, 0\n\t"
      "s_mov_b64 s[64:65], s[40:41]\n\t"
      "s_lshl_b32 s74, %1, 10\n\t"
      "v_readlane_b32 s72, v101, 1\n\t"
      "s_add_u32 s76, s85, 0\n\t"
      "s_waitcnt vmcnt(12)\n\t"
      "s_barrier\n\t"
      "s_waitcnt lgkmcnt(10)\n\t"
      "ds_read_b64_tr_b16 a[32:33], v109 offset:32768\n\t"
      "ds_read_b64_tr_b16 a[34:35], v109 offset:33792\n\t"
      "ds_read_b64_tr_b16 a[36:37], v109 offset:32776\n\t"
      "ds_read_b64_tr_b16 a[38:39], v109 offset:33800\n\t"
      "s_waitcnt lgkmcnt(10)\n\t"
      "ds_read_b64_tr_b16 a[40:41], v109 offset:40960\n\t"
      "ds_read_b64_tr_b16 a[42:43], v109 offset:41984\n\t"
      "ds_read_b64_tr_b16 a[44:45], v109 offset:40968\n\t"
      "ds_read_b64_tr_b16 a[46:47], v109 offset:41992\n\t"
      "s_waitcnt lgkmcnt(10)\n\t"
      "ds_read_b64_tr_b16 a[48:49], v109 offset:49152\n\t"
      "ds_read_b64_tr_b16 a[50:51], v109 offset:50176\n\t"
      "ds_read_b64_tr_b16 a[52:53], v109 offset:49160\n\t"
      "ds_read_b64_tr_b16 a[54:55], v109 offset:50184\n\t"
      "s_waitcnt lgkmcnt(10)\n\t"
      "ds_read_b64_tr_b16 a[56:57], v109 offset:57344\n\t"
      "ds_read_b64_tr_b16 a[58:59], v109 offset:58368\n\t"
      "ds_read_b64_tr_b16 a[60:61], v109 offset:57352\n\t"
      "ds_read_b64_tr_b16 a[62:63], v109 offset:58376\n\t"
      "v_add_u32 v109, 0x10000, v109\n\t"
      "s_waitcnt lgkmcnt(10)\n\t"
      "ds_read_b64_tr_b16 a[64:65], v109\n\t"
      "ds_read_b64_tr_b16 a[66:67], v109 offset:1024\n\t"
      "ds_read_b64_tr_b16 a[68:69], v109 offset:8\n\t"
      "ds_read_b64_tr_b16 a[70:71], v109 offset:1032\n\t"
      "s_waitcnt lgkmcnt(10)\n\t"
      "ds_read_b64_tr_b16 a[72:73], v109 offset:8192\n\t"
      "ds_read_b64_tr_b16 a[74:75], v109 offset:9216\n\t"
      "ds_read_b64_tr_b16 a[76:77], v109 offset:8200\n\t"
      "ds_read_b64_tr_b16 a[78:79], v109 offset:9224\n\t"
      "s_waitcnt vmcnt(0)\n\t"
      "s_barrier\n\t"
      "buffer_load_dword v128, v106, s[60:63], s79 offen sc1\n\t"
      "buffer_load_dword v129, v106, s[60:63], s79 offen offset:128 sc1\n\t"
      "s_waitcnt lgkmcnt(10)\n\t"
      "ds_read_b64_tr_b16 a[80:81], v109 offset:16384\n\t"
      "ds_read_b64_tr_b16 a[82:83], v109 offset:17408\n\t"
      "ds_read_b64_tr_b16 a[84:85], v109 offset:16392\n\t"
      "ds_read_b64_tr_b16 a[86:87], v109 offset:17416\n\t"
      "s_waitcnt lgkmcnt(10)\n\t"
      "ds_read_b64_tr_b16 a[88:89], v109 offset:24576\n\t"
      "ds_read_b64_tr_b16 a[90:91], v109 offset:25600\n\t"
      "ds_read_b64_tr_b16 a[92:93], v109 offset:24584\n\t"
      "ds_read_b64_tr_b16 a[94:95], v109 offset:25608\n\t"
      "s_waitcnt lgkmcnt(10)\n\t"
      "ds_read_b64_tr_b16 a[96:97], v109 offset:32768\n\t"
      "ds_read_b64_tr_b16 a[98:99], v109 offset:33792\n\t"
      "ds_read_b64_tr_b16 a[100:101], v109 offset:32776\n\t"
      "ds_read_b64_tr_b16 a[102:103], v109 offset:33800\n\t"
      "s_waitcnt lgkmcnt(10)\n\t"
      "ds_read_b64_tr_b16 a[104:105], v109 offset:40960\n\t"
      "ds_read_b64_tr_b16 a[106:107], v109 offset:41984\n\t"
      "ds_read_b64_tr_b16 a[108:109], v109 offset:40968\n\t"
      "ds_read_b64_tr_b16 a[110:111], v109 offset:41992\n\t"
      "s_waitcnt lgkmcnt(10)\n\t"
      "ds_read_b64_tr_b16 a[112:113], v109 offset:49152\n\t"
      "ds_read_b64_tr_b16 a[114:115], v109 offset:50176\n\t"
      "ds_read_b64_tr_b16 a[116:117], v109 offset:49160\n\t"
      "ds_read_b64_tr_b16 a[118:119], v109 offset:50184\n\t"
      "s_waitcnt lgkmcnt(10)\n\t"
      "ds_read_b64_tr_b16 a[120:121], v109 offset:57344\n\t"
      "ds_read_b64_tr_b16 a[122:123], v109 offset:58368\n\t"
      "ds_read_b64_tr_b16 a[124:125], v109 offset:57352\n\t"
      "ds_read_b64_tr_b16 a[126:127], v109 offset:58376\n\t"
      "s_mov_b64 s[88:89], 0\n\t"
      "s_mov_b64 s[38:39], 0\n\t"
      "s_waitcnt lgkmcnt(4)\n\t"
      "s_mov_b32 s71, 0\n\t"
      "GKV%=:\n\t"
      "ds_read_b128 v[0:3], v98\n\t"
      "ds_read_b128 v[4:7], v98 offset:1024\n\t"
      "ds_read_b128 v[8:11], v98 offset:2048\n\t"
      "ds_read_b128 v[12:15], v98 offset:3072\n\t"
      "ds_read_b128 v[16:19], v98 offset:4096\n\t"
      "ds_read_b128 v[20:23], v98 offset:5120\n\t"
      "ds_read_b128 v[24:27], v98 offset:6144\n\t"
      "ds_read_b128 v[28:31], v98 offset:7168\n\t"
      "ds_read_b128 v[32:35], v98 offset:8192\n\t"
      "ds_read_b128 v[36:39], v98 offset:9216\n\t"
      "s_waitcnt lgkmcnt(9)\n\t"
      "v_mfma_f32_16x16x32_bf16 v[64:67], a[0:3], v[0:3], 0\n\t"
      "ds_read_b128 v[40:43], v98 offset:10240\n\t"
      "v_mfma_f32_16x16x32_bf16 v[68:71], a[4:7], v[0:3], 0\n\t"
      "s_waitcnt lgkmcnt(9)\n\t"
      "v_mfma_f32_16x16x32_bf16 v[64:67], a[8:11], v[4:7], v[64:67]\n\t"
      "ds_read_b128 v[44:47], v98 offset:11264\n\t"
      "v_mfma_f32_16x16x32_bf16 v[68:71], a[12:15], v[4:7], v[68:71]\n\t"
      "s_waitcnt lgkmcnt(9)\n\t"
      "v_mfma_f32_16x16x32_bf16 v[64:67], a[16:19], v[8:11], v[64:67]\n\t"
      "ds_read_b128 v[48:51], v98 offset:12288\n\t"
      "v_mfma_f32_16x16x32_bf16 v[68:71], a[20:23], v[8:11], v[68:71]\n\t"
      "s_waitcnt lgkmcnt(9)\n\t"
      "v_mfma_f32_16x16x32_bf16 v[64:67], a[24:27], v[12:15], v[64:67]\n\t"
      "ds_read_b128 v[52:55], v98 offset:13312\n\t"
      "v_mfma_f32_16x16x32_bf16 v[68:71], a[28:31], v[12:15], v[68:71]\n\t"
      "s_waitcnt lgkmcnt(9)\n\t"
      "v_mfma_f32_16x16x32_bf16 v[64:67], a[32:35], v[16:19], v[64:67]\n\t"
      "ds_read_b128 v[56:59], v98 offset:14336\n\t"
      "v_mfma_f32_16x16x32_bf16 v[68:71], a[36:39], v[16:19], v[68:71]\n\t"
      "s_waitcnt lgkmcnt(9)\n\t"
      "v_mfma_f32_16x16x32_bf16 v[64:67], a[40:43], v[20:23], v[64:67]\n\t"
      "ds_read_b128 v[60:63], v98 offset:15360\n\t"
      "v_mfma_f32_16x16x32_bf16 v[68:71], a[44:47], v[20:23], v[68:71]\n\t"
      "s_waitcnt lgkmcnt(9)\n\t"
      "v_mfma_f32_16x16x32_bf16 v[64:67], a[48:51], v[24:27], v[64:67]\n\t"
      "ds_read_b128 v[0:3], v98 offset:16384\n\t"
      "v_mfma_f32_16x16x32_bf16 v[68:71], a[52:55], v[24:27], v[68:71]\n\t"
      "s_waitcnt lgkmcnt(9)\n\t"
      "v_mfma_f32_16x16x32_bf16 v[64:67], a[56:59], v[28:31], v[64:67]\n\t"
      "ds_read_b128 v[4:7], v98 offset:17408\n\t"
      "v_mfma_f32_16x16x32_bf16 v[68:71], a[60:63], v[28:31], v[68:71]\n\t"
      "s_waitcnt lgkmcnt(9)\n\t"
      "v_mfma_f32_16x16x32_bf16 v[64:67], a[64:67], v[32:35], v[64:67]\n\t"
      "ds_read_b128 v[8:11], v98 offset:18432\n\t"
      "v_mfma_f32_16x16x32_bf16 v[68:71], a[68:71], v[32:35], v[68:71]\n\t"
      "s_waitcnt lgkmcnt(9)\n\t"
      "v_mfma_f32_16x16x32_bf16 v[64:67], a[72:75], v[36:39], v[64:67]\n\t"
      "ds_read_b128 v[12:15], v98 offset:19456\n\t"
      "v_mfma_f32_16x16x32_bf16 v[68:71], a[76:79], v[36:39], v[68:71]\n\t"
      "s_waitcnt lgkmcnt(9)\n\t"
      "v_mfma_f32_16x16x32_bf16 v[64:67], a[80:83], v[40:43], v[64:67]\n\t"
      "ds_read_b128 v[16:19], v98 offset:20480\n\t"
      "v_mfma_f32_16x16x32_bf16 v[68:71], a[84:87], v[40:43], v[68:71]\n\t"
      "s_waitcnt lgkmcnt(9)\n\t"
      "v_mfma_f32_16x16x32_bf16 v[64:67], a[88:91], v[44:47], v[64:67]\n\t"
      "ds_read_b128 v[20:23], v98 offset:21504\n\t"
      "v_mfma_f32_16x16x32_bf16 v[68:71], a[92:95], v[44:47], v[68:71]\n\t"
      "s_waitcnt lgkmcnt(9)\n\t"
      "v_mfma_f32_16x16x32_bf16 v[64:67], a[96:99], v[48:51], v[64:67]\n\t"
      "ds_read_b128 v[24:27], v98 offset:22528\n\t"
      "v_mfma_f32_16x16x32_bf16 v[68:71], a[100:103], v[48:51], v[68:71]\n\t"
      "s_cmp_lg_u32 s71, 0\n\t"
      "s_barrier\n\t"
      "s_cbranch_scc1 GKV0%=\n\t"
      "s_waitcnt vmcnt(0)\n\t"
      "v_bitop3_b32 v128, v128, v129, %3 bitop3:0x7e\n\t"
      "v_cmp_ne_u32 vcc, 0, v128\n\t"
      "s_nop 0\n\t"
      "s_cmp_eq_u64 vcc, 0\n\t"
      "s_cselect_b64 s[36:37], -1, 0\n\t"
      "GKV0%=:\n\t"
      "s_waitcnt lgkmcnt(9)\n\t"
      "v_mfma_f32_16x16x32_bf16 v[64:67], a[104:107], v[52:55], v[64:67]\n\t"
      "ds_read_b128 v[28:31], v98 offset:23552\n\t"
      "v_mfma_f32_16x16x32_bf16 v[68:71], a[108:111], v[52:55], v[68:71]\n\t"
      "s_add_u32 m0, s76, 0\n\t"
      "s_mov_b64 exec, s[36:37]\n\t"
      "buffer_load_dwordx4 v97, s[60:63], s72 offen sc1 lds\n\t"
      "s_mov_b64 exec, -1\n\t"
      "s_waitcnt lgkmcnt(9)\n\t"
      "v_mfma_f32_16x16x32_bf16 v[64:67], a[112:115], v[56:59], v[64:67]\n\t"
      "ds_read_b128 v[32:35], v98 offset:24576\n\t"
      "v_mfma_f32_16x16x32_bf16 v[68:71], a[116:119], v[56:59], v[68:71]\n\t"
      "s_waitcnt lgkmcnt(9)\n\t"
      "v_mfma_f32_16x16x32_bf16 v[64:67], a[120:123], v[60:63], v[64:67]\n\t"
      "ds_read_b128 v[36:39], v98 offset:25600\n\t"
      "v_mfma_f32_16x16x32_bf16 v[68:71], a[124:127], v[60:63], v[68:71]\n\t"
      "s_add_u32 m0, s76, 0\n\t"
      "s_mov_b64 exec, s[36:37]\n\t"
      "buffer_load_dwordx4 v97, s[60:63], s72 offen offset:1024 sc1 lds\n\t"
      "s_mov_b64 exec, -1\n\t"
      "s_waitcnt lgkmcnt(9)\n\t"
      "v_mfma_f32_16x16x32_bf16 v[72:75], a[0:3], v[0:3], 0\n\t"
      "ds_read_b128 v[40:43], v98 offset:26624\n\t"
      "v_mfma_f32_16x16x32_bf16 v[76:79], a[4:7], v[0:3], 0\n\t"
      "s_waitcnt lgkmcnt(9)\n\t"
      "v_mfma_f32_16x16x32_bf16 v[72:75], a[8:11], v[4:7], v[72:75]\n\t"
      "ds_read_b128 v[44:47], v98 offset:27648\n\t"
      "v_mfma_f32_16x16x32_bf16 v[76:79], a[12:15], v[4:7], v[76:79]\n\t"
      "s_add_u32 m0, s76, 0\n\t"
      "s_mov_b64 exec, s[36:37]\n\t"
      "buffer_load_dwordx4 v97, s[60:63], s72 offen offset:2048 sc1 lds\n\t"
      "s_mov_b64 exec, -1\n\t"
      "s_cmp_eq_u32 s71, 1\n\t"
      "s_cbranch_scc0 GKV2%=\n\t"
      "s_waitcnt vmcnt(3)\n\t"
      "s_barrier\n\t"
      "s_mov_b64 exec, s[38:39]\n\t"
      "buffer_store_dword v107, off, s[60:63], s75 sc1\n\t"
      "s_mov_b64 exec, -1\n\t"
      "GKV2%=:\n\t"
      "s_waitcnt lgkmcnt(9)\n\t"
      "v_mfma_f32_16x16x32_bf16 v[72:75], a[16:19], v[8:11], v[72:75]\n\t"
      "ds_read_b128 v[48:51], v98 offset:28672\n\t"
      "v_mfma_f32_16x16x32_bf16 v[76:79], a[20:23], v[8:11], v[76:79]\n\t"
      "s_add_u32 m0, s74, 65536\n\t"
      "s_mov_b32 s78, 0\n\t"
      "s_mov_b64 exec, s[88:89]\n\t"
      "buffer_load_dwordx4 v116, s[64:67], s78 offen lds\n\t"
      "s_mov_b64 exec, -1\n\t"
      "s_waitcnt lgkmcnt(9)\n\t"
      "v_mfma_f32_16x16x32_bf16 v[72:75], a[24:27], v[12:15], v[72:75]\n\t"
      "ds_read_b128 v[52:55], v98 offset:29696\n\t"
      "v_mfma_f32_16x16x32_bf16 v[76:79], a[28:31], v[12:15], v[76:79]\n\t"
      "s_add_u32 m0, s76, 0\n\t"
      "s_mov_b64 exec, s[36:37]\n\t"
      "buffer_load_dwordx4 v97, s[60:63], s72 offen offset:3072 sc1 lds\n\t"
      "s_mov_b64 exec, -1\n\t"
      "s_waitcnt lgkmcnt(9)\n\t"
      "v_mfma_f32_16x16x32_bf16 v[72:75], a[32:35], v[16:19], v[72:75]\n\t"
      "ds_read_b128 v[56:59], v98 offset:30720\n\t"
      "v_mfma_f32_16x16x32_bf16 v[76:79], a[36:39], v[16:19], v[76:79]\n\t"
      "s_add_u32 m0, s74, 69632\n\t"
      "s_mov_b32 s78, 262144\n\t"
      "s_mov_b64 exec, s[88:89]\n\t"
      "buffer_load_dwordx4 v116, s[64:67], s78 offen lds\n\t"
      "s_mov_b64 exec, -1\n\t"
      "s_waitcnt lgkmcnt(9)\n\t"
      "v_mfma_f32_16x16x32_bf16 v[72:75], a[40:43], v[20:23], v[72:75]\n\t"
      "ds_read_b128 v[60:63], v98 offset:31744\n\t"
      "v_mfma_f32_16x16x32_bf16 v[76:79], a[44:47], v[20:23], v[76:79]\n\t"
      "s_add_u32 m0, s76, 4096\n\t"
      "s_add_u32 s72, s72, 4096\n\t"
      "s_mov_b64 exec, s[36:37]\n\t"
      "buffer_load_dwordx4 v97, s[60:63], s72 offen sc1 lds\n\t"
      "s_mov_b64 exec, -1\n\t"
      "v_cvt_pk_bf16_f32 v80, v64, v65\n\t"
      "v_cvt_pk_bf16_f32 v81, v66, v67\n\t"
      "v_cvt_pk_bf16_f32 v82, v68, v69\n\t"
      "v_cvt_pk_bf16_f32 v83, v70, v71\n\t"
      "buffer_store_dwordx4 v[80:83], v97, s[60:63], s73 offen sc1\n\t"
      "s_waitcnt lgkmcnt(9)\n\t"
      "v_mfma_f32_16x16x32_bf16 v[72:75], a[48:51], v[24:27], v[72:75]\n\t"
      "v_mfma_f32_16x16x32_bf16 v[76:79], a[52:55], v[24:27], v[76:79]\n\t"
      "s_add_u32 m0, s74, 73728\n\t"
      "s_mov_b32 s78, 524288\n\t"
      "s_mov_b64 exec, s[88:89]\n\t"
      "buffer_load_dwordx4 v116, s[64:67], s78 offen lds\n\t"
      "s_mov_b64 exec, -1\n\t"
      "s_waitcnt lgkmcnt(8)\n\t"
      "v_mfma_f32_16x16x32_bf16 v[72:75], a[56:59], v[28:31], v[72:75]\n\t"
      "v_mfma_f32_16x16x32_bf16 v[76:79], a[60:63], v[28:31], v[76:79]\n\t"
      "s_add_u32 m0, s76, 4096\n\t"
      "s_mov_b64 exec, s[36:37]\n\t"
      "buffer_load_dwordx4 v97, s[60:63], s72 offen offset:1024 sc1 lds\n\t"
      "s_mov_b64 exec, -1\n\t"
      "s_waitcnt lgkmcnt(7)\n\t"
      "v_mfma_f32_16x16x32_bf16 v[72:75], a[64:67], v[32:35], v[72:75]\n\t"
      "v_mfma_f32_16x16x32_bf16 v[76:79], a[68:71], v[32:35], v[76:79]\n\t"
      "s_add_u32 m0, s74, 77824\n\t"
      "s_mov_b32 s78, 786432\n\t"
      "s_mov_b64 exec, s[88:89]\n\t"
      "buffer_load_dwordx4 v116, s[64:67], s78 offen lds\n\t"
      "s_mov_b64 exec, -1\n\t"
      "s_waitcnt lgkmcnt(6)\n\t"
      "v_mfma_f32_16x16x32_bf16 v[72:75], a[72:75], v[36:39], v[72:75]\n\t"
      "v_mfma_f32_16x16x32_bf16 v[76:79], a[76:79], v[36:39], v[76:79]\n\t"
      "s_add_u32 m0, s76, 4096\n\t"
      "s_mov_b64 exec, s[36:37]\n\t"
      "buffer_load_dwordx4 v97, s[60:63], s72 offen offset:2048 sc1 lds\n\t"
      "s_mov_b64 exec, -1\n\t"
      "s_waitcnt lgkmcnt(5)\n\t"
      "v_mfma_f32_16x16x32_bf16 v[72:75], a[80:83], v[40:43], v[72:75]\n\t"
      "v_mfma_f32_16x16x32_bf16 v[76:79], a[84:87], v[40:43], v[76:79]\n\t"
      "s_add_u32 m0, s74, 81920\n\t"
      "s_mov_b32 s78, 1048576\n\t"
      "s_mov_b64 exec, s[88:89]\n\t"
      "buffer_load_dwordx4 v116, s[64:67], s78 offen lds\n\t"
      "s_mov_b64 exec, -1\n\t"
      "s_waitcnt lgkmcnt(4)\n\t"
      "v_mfma_f32_16x16x32_bf16 v[72:75], a[88:91], v[44:47], v[72:75]\n\t"
      "v_mfma_f32_16x16x32_bf16 v[76:79], a[92:95], v[44:47], v[76:79]\n\t"
      "s_add_u32 m0, s76, 4096\n\t"
      "s_mov_b64 exec, s[36:37]\n\t"
      "buffer_load_dwordx4 v97, s[60:63], s72 offen offset:3072 sc1 lds\n\t"
      "s_mov_b64 exec, -1\n\t"
      "s_waitcnt lgkmcnt(3)\n\t"
      "v_mfma_f32_16x16x32_bf16 v[72:75], a[96:99], v[48:51], v[72:75]\n\t"
      "v_mfma_f32_16x16x32_bf16 v[76:79], a[100:103], v[48:51], v[76:79]\n\t"
      "s_add_u32 m0, s74, 86016\n\t"
      "s_mov_b32 s78, 1310720\n\t"
      "s_mov_b64 exec, s[88:89]\n\t"
      "buffer_load_dwordx4 v116, s[64:67], s78 offen lds\n\t"
      "s_mov_b64 exec, -1\n\t"
      "s_waitcnt lgkmcnt(2)\n\t"
      "v_mfma_f32_16x16x32_bf16 v[72:75], a[104:107], v[52:55], v[72:75]\n\t"
      "v_mfma_f32_16x16x32_bf16 v[76:79], a[108:111], v[52:55], v[76:79]\n\t"
      "s_waitcnt lgkmcnt(1)\n\t"
      "v_mfma_f32_16x16x32_bf16 v[72:75], a[112:115], v[56:59], v[72:75]\n\t"
      "v_mfma_f32_16x16x32_bf16 v[76:79], a[116:119], v[56:59], v[76:79]\n\t"
      "s_add_u32 m0, s74, 90112\n\t"
      "s_mov_b32 s78, 1572864\n\t"
      "s_mov_b64 exec, s[88:89]\n\t"
      "buffer_load_dwordx4 v116, s[64:67], s78 offen lds\n\t"
      "s_mov_b64 exec, -1\n\t"
      "s_waitcnt lgkmcnt(0)\n\t"
      "v_mfma_f32_16x16x32_bf16 v[72:75], a[120:123], v[60:63], v[72:75]\n\t"
      "v_mfma_f32_16x16x32_bf16 v[76:79], a[124:127], v[60:63], v[76:79]\n\t"
      "s_nop 6\n\t"
      "v_cvt_pk_bf16_f32 v84, v72, v73\n\t"
      "v_cvt_pk_bf16_f32 v85, v74, v75\n\t"
      "v_cvt_pk_bf16_f32 v86, v76, v77\n\t"
      "v_cvt_pk_bf16_f32 v87, v78, v79\n\t"
      "buffer_store_dwordx4 v[84:87], v97, s[60:63], s73 offen offset:1024 sc1\n\t"
      "s_cmp_eq_u32 s71, 2\n\t"
      "s_cbranch_scc1 GKV3%=\n\t"
      "s_cmp_lg_u32 s71, 0\n\t"
      "s_cbranch_scc1 GKV1%=\n\t"
      "s_cmp_lg_u64 s[36:37], 0\n\t"
      "s_cbranch_scc1 GL2%=\n\t"
      "GL0%=:\n\t"
      "buffer_load_dword v128, v106, s[60:63], s79 offen sc1\n\t"
      "buffer_load_dword v129, v106, s[60:63], s79 offen offset:128 sc1\n\t"
      "s_sub_u32 s81, s81, 1\n\t"
      "s_waitcnt vmcnt(0)\n\t"
      "v_bitop3_b32 v128, v128, v129, %3 bitop3:0x7e\n\t"
      "v_cmp_ne_u32 vcc, 0, v128\n\t"
      "s_cmp_eq_u32 s81, 0\n\t"
      "s_cbranch_scc1 GL1%=\n\t"
      "s_cbranch_vccnz GL0%=\n\t"
      "GL1%=:\n\t"
      "v_readlane_b32 s72, v101, 1\n\t"
      "s_add_u32 s76, s85, 0\n\t"
      "s_add_u32 m0, s76, 0\n\t"
      "s_nop 2\n\t"
      "buffer_load_dwordx4 v97, s[60:63], s72 offen sc1 lds\n\t"
      "buffer_load_dwordx4 v97, s[60:63], s72 offen offset:1024 sc1 lds\n\t"
      "buffer_load_dwordx4 v97, s[60:63], s72 offen offset:2048 sc1 lds\n\t"
      "buffer_load_dwordx4 v97, s[60:63], s72 offen offset:3072 sc1 lds\n\t"
      "s_add_u32 m0, s76, 4096\n\t"
      "s_add_u32 s72, s72, 4096\n\t"
      "buffer_load_dwordx4 v97, s[60:63], s72 offen sc1 lds\n\t"
      "buffer_load_dwordx4 v97, s[60:63], s72 offen offset:1024 sc1 lds\n\t"
      "buffer_load_dwordx4 v97, s[60:63], s72 offen offset:2048 sc1 lds\n\t"
      "buffer_load_dwordx4 v97, s[60:63], s72 offen offset:3072 sc1 lds\n\t"
      "s_waitcnt vmcnt(0)\n\t"
      "GL2%=:\n\t"
      "s_mov_b32 s77, s73\n\t"
      "v_readlane_b32 s73, v102, 1\n\t"
      "v_mov_b32 v98, v97\n\t"
      "s_mov_b64 s[88:89], -1\n\t"
      "s_mov_b64 s[38:39], s[90:91]\n\t"
      "s_mov_b64 s[36:37], -1\n\t"
      "s_mov_b32 s71, 1\n\t"
      "v_readlane_b32 s72, v101, 2\n\t"
      "s_add_u32 s76, s85, 32768\n\t"
      "s_waitcnt vmcnt(1)\n\t"
      "s_barrier\n\t"
      "s_branch GKV%=\n\t"
      "GKV1%=:\n\t"
      "v_readlane_b32 s75, v103, 1\n\t"
      "v_add_u32 v117, 0x10000, v117\n\t"
      "v_add_u32 v98, 131072, v97\n\t"
      "s_mov_b32 s68, 0\n\t"
      "s_add_u32 m0, s74, 94208\n\t"
      "s_mov_b32 s78, 1835008\n\t"
      "buffer_load_dwordx4 v116, s[64:67], s78 offen lds\n\t"
      "s_add_u32 m0, s74, 98304\n\t"
      "s_mov_b32 s78, 2097152\n\t"
      "buffer_load_dwordx4 v116, s[64:67], s78 offen lds\n\t"
      "s_add_u32 m0, s74, 102400\n\t"
      "s_mov_b32 s78, 2359296\n\t"
      "buffer_load_dwordx4 v116, s[64:67], s78 offen lds\n\t"
      "s_add_u32 m0, s74, 106496\n\t"
      "s_mov_b32 s78, 2621440\n\t"
      "buffer_load_dwordx4 v116, s[64:67], s78 offen lds\n\t"
      "s_add_u32 m0, s74, 110592\n\t"
      "s_mov_b32 s78, 2883584\n\t"
      "buffer_load_dwordx4 v116, s[64:67], s78 offen lds\n\t"
      "s_add_u32 m0, s74, 114688\n\t"
      "s_mov_b32 s78, 3145728\n\t"
      "buffer_load_dwordx4 v116, s[64:67], s78 offen lds\n\t"
      "s_add_u32 m0, s74, 118784\n\t"
      "s_mov_b32 s78, 3407872\n\t"
      "buffer_load_dwordx4 v116, s[64:67], s78 offen lds\n\t"
      "s_add_u32 m0, s74, 122880\n\t"
      "s_mov_b32 s78, 3670016\n\t"
      "buffer_load_dwordx4 v116, s[64:67], s78 offen lds\n\t"
      "s_add_u32 m0, s74, 126976\n\t"
      "s_mov_b32 s78, 3932160\n\t"
      "buffer_load_dwordx4 v116, s[64:67], s78 offen lds\n\t"
      "v_add_u32 v98, 0x8000, v97\n\t"
      "v_readlane_b32 s73, v102, 2\n\t"
      "s_mov_b64 s[88:89], 0\n\t"
      "s_mov_b64 s[36:37], 0\n\t"
      "s_mov_b32 s71, 2\n\t"
      "s_waitcnt vmcnt(9)\n\t"
      "s_barrier\n\t"
      "s_mov_b64 exec, s[90:91]\n\t"
      "buffer_store_dword v107, off, s[60:63], s75 sc1\n\t"
      "s_mov_b64 exec, -1\n\t"
      "s_branch GKV%=\n\t"
      "GKV3%=:\n\t"
      "v_readlane_b32 s75, v103, 2\n\t"
      "v_add_u32 v98, 131072, v97\n\t"
      "s_waitcnt vmcnt(0)\n\t"
      "s_barrier\n\t"
      "s_mov_b64 exec, s[90:91]\n\t"
      "s_nop 0\n\t"
      "buffer_store_dword v107, off, s[60:63], s75 sc1\n\t"
      "s_mov_b64 exec, -1\n\t"
      "s_waitcnt lgkmcnt(12)\n\t"
      "ds_read_b64_tr_b16 a[128:129], v117\n\t"
      "ds_read_b64_tr_b16 a[130:131], v117 offset:512\n\t"
      "s_waitcnt lgkmcnt(12)\n\t"
      "ds_read_b64_tr_b16 a[132:133], v117 offset:4096\n\t"
      "ds_read_b64_tr_b16 a[134:135], v117 offset:4608\n\t"
      "s_waitcnt lgkmcnt(12)\n\t"
      "ds_read_b64_tr_b16 a[136:137], v117 offset:8192\n\t"
      "ds_read_b64_tr_b16 a[138:139], v117 offset:8704\n\t"
      "s_waitcnt lgkmcnt(12)\n\t"
      "ds_read_b64_tr_b16 a[140:141], v117 offset:12288\n\t"
      "ds_read_b64_tr_b16 a[142:143], v117 offset:12800\n\t"
      "s_waitcnt lgkmcnt(12)\n\t"
      "ds_read_b64_tr_b16 a[144:145], v117 offset:16384\n\t"
      "ds_read_b64_tr_b16 a[146:147], v117 offset:16896\n\t"
      "s_waitcnt lgkmcnt(12)\n\t"
      "ds_read_b64_tr_b16 a[148:149], v117 offset:20480\n\t"
      "ds_read_b64_tr_b16 a[150:151], v117 offset:20992\n\t"
      "s_waitcnt lgkmcnt(12)\n\t"
      "ds_read_b64_tr_b16 a[152:153], v117 offset:24576\n\t"
      "ds_read_b64_tr_b16 a[154:155], v117 offset:25088\n\t"
      "s_waitcnt lgkmcnt(12)\n\t"
      "ds_read_b64_tr_b16 a[156:157], v117 offset:28672\n\t"
      "ds_read_b64_tr_b16 a[158:159], v117 offset:29184\n\t"
      "s_waitcnt lgkmcnt(12)\n\t"
      "ds_read_b64_tr_b16 a[160:161], v117 offset:32768\n\t"
      "ds_read_b64_tr_b16 a[162:163], v117 offset:33280\n\t"
      "s_waitcnt lgkmcnt(12)\n\t"
      "ds_read_b64_tr_b16 a[164:165], v117 offset:36864\n\t"
      "ds_read_b64_tr_b16 a[166:167], v117 offset:37376\n\t"
      "s_waitcnt lgkmcnt(12)\n\t"
      "ds_read_b64_tr_b16 a[168:169], v117 offset:40960\n\t"
      "ds_read_b64_tr_b16 a[170:171], v117 offset:41472\n\t"
      "s_waitcnt lgkmcnt(12)\n\t"
      "ds_read_b64_tr_b16 a[172:173], v117 offset:45056\n\t"
      "ds_read_b64_tr_b16 a[174:175], v117 offset:45568\n\t"
      "s_waitcnt lgkmcnt(12)\n\t"
      "ds_read_b64_tr_b16 a[176:177], v117 offset:49152\n\t"
      "ds_read_b64_tr_b16 a[178:179], v117 offset:49664\n\t"
      "s_waitcnt lgkmcnt(12)\n\t"
      "ds_read_b64_tr_b16 a[180:181], v117 offset:53248\n\t"
      "ds_read_b64_tr_b16 a[182:183], v117 offset:53760\n\t"
      "s_waitcnt lgkmcnt(12)\n\t"
      "ds_read_b64_tr_b16 a[184:185], v117 offset:57344\n\t"
      "ds_read_b64_tr_b16 a[186:187], v117 offset:57856\n\t"
      "s_waitcnt lgkmcnt(12)\n\t"
      "ds_read_b64_tr_b16 a[188:189], v117 offset:61440\n\t"
      "ds_read_b64_tr_b16 a[190:191], v117 offset:61952\n\t"
      "s_waitcnt lgkmcnt(4)\n\t"
      "GQ%=:\n\t"
      "ds_read_b128 v[0:3], v98\n\t"
      "ds_read_b128 v[4:7], v98 offset:1024\n\t"
      "ds_read_b128 v[8:11], v98 offset:2048\n\t"
      "ds_read_b128 v[12:15], v98 offset:3072\n\t"
      "ds_read_b128 v[16:19], v98 offset:4096\n\t"
      "ds_read_b128 v[20:23], v98 offset:5120\n\t"
      "ds_read_b128 v[24:27], v98 offset:6144\n\t"
      "ds_read_b128 v[28:31], v98 offset:7168\n\t"
      "ds_read_b128 v[32:35], v98 offset:8192\n\t"
      "ds_read_b128 v[36:39], v98 offset:9216\n\t"
      "s_waitcnt lgkmcnt(9)\n\t"
      "v_mfma_f32_16x16x32_bf16 v[88:91], a[128:131], v[0:3], 0\n\t"
      "ds_read_b128 v[40:43], v98 offset:10240\n\t"
      "s_waitcnt lgkmcnt(9)\n\t"
      "v_mfma_f32_16x16x32_bf16 v[88:91], a[132:135], v[4:7], v[88:91]\n\t"
      "ds_read_b128 v[44:47], v98 offset:11264\n\t"
      "s_waitcnt lgkmcnt(9)\n\t"
      "v_mfma_f32_16x16x32_bf16 v[88:91], a[136:139], v[8:11], v[88:91]\n\t"
      "ds_read_b128 v[48:51], v98 offset:12288\n\t"
      "s_waitcnt lgkmcnt(9)\n\t"
      "v_mfma_f32_16x16x32_bf16 v[88:91], a[140:143], v[12:15], v[88:91]\n\t"
      "ds_read_b128 v[52:55], v98 offset:13312\n\t"
      "s_waitcnt lgkmcnt(9)\n\t"
      "v_mfma_f32_16x16x32_bf16 v[88:91], a[144:147], v[16:19], v[88:91]\n\t"
      "ds_read_b128 v[56:59], v98 offset:14336\n\t"
      "s_waitcnt lgkmcnt(9)\n\t"
      "v_mfma_f32_16x16x32_bf16 v[88:91], a[148:151], v[20:23], v[88:91]\n\t"
      "ds_read_b128 v[60:63], v98 offset:15360\n\t"
      "s_waitcnt lgkmcnt(9)\n\t"
      "v_mfma_f32_16x16x32_bf16 v[88:91], a[152:155], v[24:27], v[88:91]\n\t"
      "ds_read_b128 v[0:3], v98 offset:16384\n\t"
      "s_waitcnt lgkmcnt(9)\n\t"
      "v_mfma_f32_16x16x32_bf16 v[88:91], a[156:159], v[28:31], v[88:91]\n\t"
      "ds_read_b128 v[4:7], v98 offset:17408\n\t"
      "s_waitcnt lgkmcnt(9)\n\t"
      "v_mfma_f32_16x16x32_bf16 v[88:91], a[160:163], v[32:35], v[88:91]\n\t"
      "ds_read_b128 v[8:11], v98 offset:18432\n\t"
      "s_waitcnt lgkmcnt(9)\n\t"
      "v_mfma_f32_16x16x32_bf16 v[88:91], a[164:167], v[36:39], v[88:91]\n\t"
      "ds_read_b128 v[12:15], v98 offset:19456\n\t"
      "s_waitcnt lgkmcnt(9)\n\t"
      "v_mfma_f32_16x16x32_bf16 v[88:91], a[168:171], v[40:43], v[88:91]\n\t"
      "ds_read_b128 v[16:19], v98 offset:20480\n\t"
      "s_waitcnt lgkmcnt(9)\n\t"
      "v_mfma_f32_16x16x32_bf16 v[88:91], a[172:175], v[44:47], v[88:91]\n\t"
      "ds_read_b128 v[20:23], v98 offset:21504\n\t"
      "s_waitcnt lgkmcnt(9)\n\t"
      "v_mfma_f32_16x16x32_bf16 v[88:91], a[176:179], v[48:51], v[88:91]\n\t"
      "ds_read_b128 v[24:27], v98 offset:22528\n\t"
      "s_waitcnt lgkmcnt(9)\n\t"
      "v_mfma_f32_16x16x32_bf16 v[88:91], a[180:183], v[52:55], v[88:91]\n\t"
      "ds_read_b128 v[28:31], v98 offset:23552\n\t"
      "s_waitcnt lgkmcnt(9)\n\t"
      "v_mfma_f32_16x16x32_bf16 v[88:91], a[184:187], v[56:59], v[88:91]\n\t"
      "ds_read_b128 v[32:35], v98 offset:24576\n\t"
      "s_waitcnt lgkmcnt(9)\n\t"
      "v_mfma_f32_16x16x32_bf16 v[88:91], a[188:191], v[60:63], v[88:91]\n\t"
      "ds_read_b128 v[36:39], v98 offset:25600\n\t"
      "s_waitcnt lgkmcnt(9)\n\t"
      "v_mfma_f32_16x16x32_bf16 v[92:95], a[128:131], v[0:3], 0\n\t"
      "ds_read_b128 v[40:43], v98 offset:26624\n\t"
      "s_waitcnt lgkmcnt(9)\n\t"
      "v_mfma_f32_16x16x32_bf16 v[92:95], a[132:135], v[4:7], v[92:95]\n\t"
      "ds_read_b128 v[44:47], v98 offset:27648\n\t"
      "s_waitcnt lgkmcnt(9)\n\t"
      "v_mfma_f32_16x16x32_bf16 v[92:95], a[136:139], v[8:11], v[92:95]\n\t"
      "ds_read_b128 v[48:51], v98 offset:28672\n\t"
      "s_waitcnt lgkmcnt(9)\n\t"
      "v_mfma_f32_16x16x32_bf16 v[92:95], a[140:143], v[12:15], v[92:95]\n\t"
      "ds_read_b128 v[52:55], v98 offset:29696\n\t"
      "s_waitcnt lgkmcnt(9)\n\t"
      "v_mfma_f32_16x16x32_bf16 v[92:95], a[144:147], v[16:19], v[92:95]\n\t"
      "ds_read_b128 v[56:59], v98 offset:30720\n\t"
      "s_waitcnt lgkmcnt(9)\n\t"
      "v_mfma_f32_16x16x32_bf16 v[92:95], a[148:151], v[20:23], v[92:95]\n\t"
      "ds_read_b128 v[60:63], v98 offset:31744\n\t"
      "s_waitcnt lgkmcnt(9)\n\t"
      "v_mfma_f32_16x16x32_bf16 v[92:95], a[152:155], v[24:27], v[92:95]\n\t"
      "s_waitcnt lgkmcnt(8)\n\t"
      "v_mfma_f32_16x16x32_bf16 v[92:95], a[156:159], v[28:31], v[92:95]\n\t"
      "s_waitcnt lgkmcnt(7)\n\t"
      "v_mfma_f32_16x16x32_bf16 v[92:95], a[160:163], v[32:35], v[92:95]\n\t"
      "s_waitcnt lgkmcnt(6)\n\t"
      "v_mfma_f32_16x16x32_bf16 v[92:95], a[164:167], v[36:39], v[92:95]\n\t"
      "s_waitcnt lgkmcnt(5)\n\t"
      "v_mfma_f32_16x16x32_bf16 v[92:95], a[168:171], v[40:43], v[92:95]\n\t"
      "s_waitcnt lgkmcnt(4)\n\t"
      "v_mfma_f32_16x16x32_bf16 v[92:95], a[172:175], v[44:47], v[92:95]\n\t"
      "s_waitcnt lgkmcnt(3)\n\t"
      "v_mfma_f32_16x16x32_bf16 v[92:95], a[176:179], v[48:51], v[92:95]\n\t"
      "s_waitcnt lgkmcnt(2)\n\t"
      "v_mfma_f32_16x16x32_bf16 v[92:95], a[180:183], v[52:55], v[92:95]\n\t"
      "s_waitcnt lgkmcnt(1)\n\t"
      "v_mfma_f32_16x16x32_bf16 v[92:95], a[184:187], v[56:59], v[92:95]\n\t"
      "s_waitcnt lgkmcnt(0)\n\t"
      "v_mfma_f32_16x16x32_bf16 v[92:95], a[188:191], v[60:63], v[92:95]\n\t"
      "v_cvt_pk_bf16_f32 v112, v88, v89\n\t"
      "v_cvt_pk_bf16_f32 v113, v90, v91\n\t"
      "s_cmp_eq_u32 s68, 3\n\t"
      "s_waitcnt vmcnt(17)\n\t"
      "s_barrier\n\t"
      "s_cbranch_scc1 GQ1%=\n\t"
      "s_nop 3\n\t"
      "v_cvt_pk_bf16_f32 v114, v92, v93\n\t"
      "v_cvt_pk_bf16_f32 v115, v94, v95\n\t"
      "s_nop 0\n\t"
      "v_permlane16_swap_b32 v112, v114\n\t"
      "v_permlane16_swap_b32 v113, v115\n\t"
      "buffer_store_dwordx4 v[112:115], v111, s[60:63], s77 offen sc1\n\t"
      "s_cmp_lg_u32 s68, 0\n\t"
      "s_cbranch_scc1 GQ2%=\n\t"
      "v_readlane_b32 s72, v101, 3\n\t"
      "s_add_u32 s76, s85, 65536\n\t"
      "s_add_u32 m0, s76, 0\n\t"
      "s_nop 2\n\t"
      "buffer_load_dwordx4 v97, s[60:63], s72 offen sc1 lds\n\t"
      "buffer_load_dwordx4 v97, s[60:63], s72 offen offset:1024 sc1 lds\n\t"
      "buffer_load_dwordx4 v97, s[60:63], s72 offen offset:2048 sc1 lds\n\t"
      "buffer_load_dwordx4 v97, s[60:63], s72 offen offset:3072 sc1 lds\n\t"
      "s_add_u32 m0, s76, 4096\n\t"
      "s_add_u32 s72, s72, 4096\n\t"
      "buffer_load_dwordx4 v97, s[60:63], s72 offen sc1 lds\n\t"
      "buffer_load_dwordx4 v97, s[60:63], s72 offen offset:1024 sc1 lds\n\t"
      "buffer_load_dwordx4 v97, s[60:63], s72 offen offset:2048 sc1 lds\n\t"
      "buffer_load_dwordx4 v97, s[60:63], s72 offen offset:3072 sc1 lds\n\t"
      "v_readlane_b32 s72, v101, 4\n\t"
      "s_add_u32 s76, s85, 98304\n\t"
      "s_add_u32 m0, s76, 0\n\t"
      "s_nop 2\n\t"
      "buffer_load_dwordx4 v97, s[60:63], s72 offen sc1 lds\n\t"
      "buffer_load_dwordx4 v97, s[60:63], s72 offen offset:1024 sc1 lds\n\t"
      "buffer_load_dwordx4 v97, s[60:63], s72 offen offset:2048 sc1 lds\n\t"
      "buffer_load_dwordx4 v97, s[60:63], s72 offen offset:3072 sc1 lds\n\t"
      "s_add_u32 m0, s76, 4096\n\t"
      "s_add_u32 s72, s72, 4096\n\t"
      "buffer_load_dwordx4 v97, s[60:63], s72 offen sc1 lds\n\t"
      "buffer_load_dwordx4 v97, s[60:63], s72 offen offset:1024 sc1 lds\n\t"
      "buffer_load_dwordx4 v97, s[60:63], s72 offen offset:2048 sc1 lds\n\t"
      "buffer_load_dwordx4 v97, s[60:63], s72 offen offset:3072 sc1 lds\n\t"
      "v_mov_b32 v98, v97\n\t"
      "v_readlane_b32 s77, v102, 1\n\t"
      "s_mov_b32 s68, 1\n\t"
      "s_branch GQ%=\n\t"
      "GQ2%=:\n\t"
      "v_readlane_b32 s72, v101, 5\n\t"
      "s_add_u32 s76, s85, 0\n\t"
      "s_add_u32 m0, s76, 0\n\t"
      "s_nop 2\n\t"
      "buffer_load_dwordx4 v97, s[60:63], s72 offen sc1 lds\n\t"
      "buffer_load_dwordx4 v97, s[60:63], s72 offen offset:1024 sc1 lds\n\t"
      "buffer_load_dwordx4 v97, s[60:63], s72 offen offset:2048 sc1 lds\n\t"
      "buffer_load_dwordx4 v97, s[60:63], s72 offen offset:3072 sc1 lds\n\t"
      "s_add_u32 m0, s76, 4096\n\t"
      "s_add_u32 s72, s72, 4096\n\t"
      "buffer_load_dwordx4 v97, s[60:63], s72 offen sc1 lds\n\t"
      "buffer_load_dwordx4 v97, s[60:63], s72 offen offset:1024 sc1 lds\n\t"
      "buffer_load_dwordx4 v97, s[60:63], s72 offen offset:2048 sc1 lds\n\t"
      "buffer_load_dwordx4 v97, s[60:63], s72 offen offset:3072 sc1 lds\n\t"
      "v_add_u32 v98, 0x8000, v97\n\t"
      "s_mov_b32 s68, 3\n\t"
      "s_branch GQ%=\n\t"
      "GQ1%=:\n\t"
      "s_mov_b32 s69, 6\n\t"
      "s_mov_b32 s70, 63\n\t"
      "s_waitcnt vmcnt(9)\n\t"
      "s_mov_b32 s77, 0x10000\n\t"
      "s_mov_b32 s71, 5\n\t"
      "s_getpc_b64 s[88:89]\n\t"
      "GPC%=:\n\t"
      "s_add_u32 s88, s88, GJOBS%=-GPC%=\n\t"
      "s_addc_u32 s89, s89, 0\n\t"
      "v_add_u32 v98, s77, v97\n\t"
      "s_add_u32 s76, s85, 0x8000\n\t"
      "ds_read_b128 v[0:3], v98\n\t"
      "ds_read_b128 v[4:7], v98 offset:1024\n\t"
      "ds_read_b128 v[8:11], v98 offset:2048\n\t"
      "ds_read_b128 v[12:15], v98 offset:3072\n\t"
      "ds_read_b128 v[16:19], v98 offset:4096\n\t"
      "ds_read_b128 v[20:23], v98 offset:5120\n\t"
      "ds_read_b128 v[24:27], v98 offset:6144\n\t"
      "ds_read_b128 v[28:31], v98 offset:7168\n\t"
      "ds_read_b128 v[32:35], v98 offset:8192\n\t"
      "ds_read_b128 v[36:39], v98 offset:9216\n\t"
      ".p2align 6\n\t"
      "GU%=:\n\t"
      "s_cmp_eq_u32 s68, s71\n\t"
      "s_cbranch_scc1 GCALL%=\n\t"
      "GBACK%=:\n\t"
      "s_waitcnt lgkmcnt(9)\n\t"
      "v_mfma_f32_16x16x32_bf16 v[64:67], a[0:3], v[0:3], 0\n\t"
      "ds_read_b128 v[40:43], v98 offset:10240\n\t"
      "v_mfma_f32_16x16x32_bf16 v[68:71], a[4:7], v[0:3], 0\n\t"
      "v_readlane_b32 s72, v101, s69\n\t"
      "v_mfma_f32_16x16x32_bf16 v[88:91], a[128:131], v[0:3], 0\n\t"
      "s_waitcnt lgkmcnt(9)\n\t"
      "v_mfma_f32_16x16x32_bf16 v[64:67], a[8:11], v[4:7], v[64:67]\n\t"
      "ds_read_b128 v[44:47], v98 offset:11264\n\t"
      "v_mfma_f32_16x16x32_bf16 v[68:71], a[12:15], v[4:7], v[68:71]\n\t"
      "v_readlane_b32 s75, v103, s70\n\t"
      "v_mfma_f32_16x16x32_bf16 v[88:91], a[132:135], v[4:7], v[88:91]\n\t"
      "s_waitcnt lgkmcnt(9)\n\t"
      "v_mfma_f32_16x16x32_bf16 v[64:67], a[16:19], v[8:11], v[64:67]\n\t"
      "ds_read_b128 v[48:51], v98 offset:12288\n\t"
      "v_mfma_f32_16x16x32_bf16 v[68:71], a[20:23], v[8:11], v[68:71]\n\t"
      "v_cvt_pk_bf16_f32 v84, v72, v73\n\t"
      "v_cvt_pk_bf16_f32 v85, v74, v75\n\t"
      "v_mfma_f32_16x16x32_bf16 v[88:91], a[136:139], v[8:11], v[88:91]\n\t"
      "v_cvt_pk_bf16_f32 v114, v92, v93\n\t"
      "v_cvt_pk_bf16_f32 v115, v94, v95\n\t"
      "s_waitcnt lgkmcnt(9)\n\t"
      "v_mfma_f32_16x16x32_bf16 v[64:67], a[24:27], v[12:15], v[64:67]\n\t"
      "ds_read_b128 v[52:55], v98 offset:13312\n\t"
      "v_mfma_f32_16x16x32_bf16 v[68:71], a[28:31], v[12:15], v[68:71]\n\t"
      "v_cvt_pk_bf16_f32 v86, v76, v77\n\t"
      "v_cvt_pk_bf16_f32 v87, v78, v79\n\t"
      "v_mfma_f32_16x16x32_bf16 v[88:91], a[140:143], v[12:15], v[88:91]\n\t"
      "v_permlane16_swap_b32 v112, v114\n\t"
      "v_permlane16_swap_b32 v113, v115\n\t"
      "s_waitcnt lgkmcnt(9)\n\t"
      "v_mfma_f32_16x16x32_bf16 v[64:67], a[32:35], v[16:19], v[64:67]\n\t"
      "ds_read_b128 v[56:59], v98 offset:14336\n\t"
      "v_mfma_f32_16x16x32_bf16 v[68:71], a[36:39], v[16:19], v[68:71]\n\t"
      "buffer_store_dwordx4 v[84:87], v97, s[60:63], s73 offen offset:1024 sc1\n\t"
      "buffer_store_dwordx4 v[112:115], v111, s[60:63], s73 offen sc1\n\t"
      "v_mfma_f32_16x16x32_bf16 v[88:91], a[144:147], v[16:19], v[88:91]\n\t"
      "s_add_u32 m0, s76, 0\n\t"
      "buffer_load_dwordx4 v97, s[60:63], s72 offen sc1 lds\n\t"
      "s_waitcnt lgkmcnt(9)\n\t"
      "v_mfma_f32_16x16x32_bf16 v[64:67], a[40:43], v[20:23], v[64:67]\n\t"
      "ds_read_b128 v[60:63], v98 offset:15360\n\t"
      "v_mfma_f32_16x16x32_bf16 v[68:71], a[44:47], v[20:23], v[68:71]\n\t"
      "v_mfma_f32_16x16x32_bf16 v[88:91], a[148:151], v[20:23], v[88:91]\n\t"
      "s_waitcnt lgkmcnt(9)\n\t"
      "v_mfma_f32_16x16x32_bf16 v[64:67], a[48:51], v[24:27], v[64:67]\n\t"
      "ds_read_b128 v[0:3], v98 offset:16384\n\t"
      "v_mfma_f32_16x16x32_bf16 v[68:71], a[52:55], v[24:27], v[68:71]\n\t"
      "v_mfma_f32_16x16x32_bf16 v[88:91], a[152:155], v[24:27], v[88:91]\n\t"
      "s_waitcnt lgkmcnt(9)\n\t"
      "v_mfma_f32_16x16x32_bf16 v[64:67], a[56:59], v[28:31], v[64:67]\n\t"
      "ds_read_b128 v[4:7], v98 offset:17408\n\t"
      "v_mfma_f32_16x16x32_bf16 v[68:71], a[60:63], v[28:31], v[68:71]\n\t"
      "buffer_load_dwordx4 v97, s[60:63], s72 offen offset:1024 sc1 lds\n\t"
      "v_mfma_f32_16x16x32_bf16 v[88:91], a[156:159], v[28:31], v[88:91]\n\t"
      "s_waitcnt lgkmcnt(9)\n\t"
      "v_mfma_f32_16x16x32_bf16 v[64:67], a[64:67], v[32:35], v[64:67]\n\t"
      "ds_read_b128 v[8:11], v98 offset:18432\n\t"
      "v_mfma_f32_16x16x32_bf16 v[68:71], a[68:71], v[32:35], v[68:71]\n\t"
      "v_mfma_f32_16x16x32_bf16 v[88:91], a[160:163], v[32:35], v[88:91]\n\t"
      "s_waitcnt lgkmcnt(9)\n\t"
      "v_mfma_f32_16x16x32_bf16 v[64:67], a[72:75], v[36:39], v[64:67]\n\t"
      "ds_read_b128 v[12:15], v98 offset:19456\n\t"
      "v_mfma_f32_16x16x32_bf16 v[68:71], a[76:79], v[36:39], v[68:71]\n\t"
      "v_readlane_b32 s73, v102, s68\n\t"
      "v_mfma_f32_16x16x32_bf16 v[88:91], a[164:167], v[36:39], v[88:91]\n\t"
      "s_waitcnt lgkmcnt(9)\n\t"
      "v_mfma_f32_16x16x32_bf16 v[64:67], a[80:83], v[40:43], v[64:67]\n\t"
      "ds_read_b128 v[16:19], v98 offset:20480\n\t"
      "v_mfma_f32_16x16x32_bf16 v[68:71], a[84:87], v[40:43], v[68:71]\n\t"
      "buffer_load_dwordx4 v97, s[60:63], s72 offen offset:2048 sc1 lds\n\t"
      "v_mfma_f32_16x16x32_bf16 v[88:91], a[168:171], v[40:43], v[88:91]\n\t"
      "s_waitcnt lgkmcnt(9)\n\t"
      "v_mfma_f32_16x16x32_bf16 v[64:67], a[88:91], v[44:47], v[64:67]\n\t"
      "ds_read_b128 v[20:23], v98 offset:21504\n\t"
      "v_mfma_f32_16x16x32_bf16 v[68:71], a[92:95], v[44:47], v[68:71]\n\t"
      "v_mfma_f32_16x16x32_bf16 v[88:91], a[172:175], v[44:47], v[88:91]\n\t"
      "s_waitcnt lgkmcnt(9)\n\t"
      "v_mfma_f32_16x16x32_bf16 v[64:67], a[96:99], v[48:51], v[64:67]\n\t"
      "ds_read_b128 v[24:27], v98 offset:22528\n\t"
      "v_mfma_f32_16x16x32_bf16 v[68:71], a[100:103], v[48:51], v[68:71]\n\t"
      "v_mfma_f32_16x16x32_bf16 v[88:91], a[176:179], v[48:51], v[88:91]\n\t"
      "s_waitcnt lgkmcnt(9)\n\t"
      "v_mfma_f32_16x16x32_bf16 v[64:67], a[104:107], v[52:55], v[64:67]\n\t"
      "ds_read_b128 v[28:31], v98 offset:23552\n\t"
      "v_mfma_f32_16x16x32_bf16 v[68:71], a[108:111], v[52:55], v[68:71]\n\t"
      "buffer_load_dwordx4 v97, s[60:63], s72 offen offset:3072 sc1 lds\n\t"
      "v_mfma_f32_16x16x32_bf16 v[88:91], a[180:183], v[52:55], v[88:91]\n\t"
      "s_waitcnt lgkmcnt(9)\n\t"
      "v_mfma_f32_16x16x32_bf16 v[64:67], a[112:115], v[56:59], v[64:67]\n\t"
      "ds_read_b128 v[32:35], v98 offset:24576\n\t"
      "v_mfma_f32_16x16x32_bf16 v[68:71], a[116:119], v[56:59], v[68:71]\n\t"
      "v_mfma_f32_16x16x32_bf16 v[88:91], a[184:187], v[56:59], v[88:91]\n\t"
      "s_waitcnt lgkmcnt(9)\n\t"
      "v_mfma_f32_16x16x32_bf16 v[64:67], a[120:123], v[60:63], v[64:67]\n\t"
      "ds_read_b128 v[36:39], v98 offset:25600\n\t"
      "v_mfma_f32_16x16x32_bf16 v[68:71], a[124:127], v[60:63], v[68:71]\n\t"
      "s_add_u32 s68, s68, 1\n\t"
      "v_mfma_f32_16x16x32_bf16 v[88:91], a[188:191], v[60:63], v[88:91]\n\t"
      "s_waitcnt lgkmcnt(9)\n\t"
      "v_mfma_f32_16x16x32_bf16 v[72:75], a[0:3], v[0:3], 0\n\t"
      "ds_read_b128 v[40:43], v98 offset:26624\n\t"
      "v_mfma_f32_16x16x32_bf16 v[76:79], a[4:7], v[0:3], 0\n\t"
      "s_add_u32 m0, s76, 4096\n\t"
      "s_add_u32 s72, s72, 4096\n\t"
      "buffer_load_dwordx4 v97, s[60:63], s72 offen sc1 lds\n\t"
      "v_mfma_f32_16x16x32_bf16 v[92:95], a[128:131], v[0:3], 0\n\t"
      "s_add_u32 s69, s69, 1\n\t"
      "s_add_u32 s70, s70, 1\n\t"
      "s_waitcnt lgkmcnt(9)\n\t"
      "v_mfma_f32_16x16x32_bf16 v[72:75], a[8:11], v[4:7], v[72:75]\n\t"
      "ds_read_b128 v[44:47], v98 offset:27648\n\t"
      "v_mfma_f32_16x16x32_bf16 v[76:79], a[12:15], v[4:7], v[76:79]\n\t"
      "s_cmp_lt_u32 s68, 7\n\t"
      "s_cselect_b32 s70, 1, 3\n\t"
      "v_mfma_f32_16x16x32_bf16 v[92:95], a[132:135], v[4:7], v[92:95]\n\t"
      "s_sub_u32 s70, s68, s70\n\t"
      "s_add_u32 s76, s77, s85\n\t"
      "s_add_u32 s77, s77, 0x8000\n\t"
      "s_and_b32 s77, s77, 0x1ffff\n\t"
      "s_waitcnt lgkmcnt(9)\n\t"
      "v_mfma_f32_16x16x32_bf16 v[72:75], a[16:19], v[8:11], v[72:75]\n\t"
      "ds_read_b128 v[48:51], v98 offset:28672\n\t"
      "v_mfma_f32_16x16x32_bf16 v[76:79], a[20:23], v[8:11], v[76:79]\n\t"
      "v_cvt_pk_bf16_f32 v80, v64, v65\n\t"
      "v_cvt_pk_bf16_f32 v81, v66, v67\n\t"
      "v_mfma_f32_16x16x32_bf16 v[92:95], a[136:139], v[8:11], v[92:95]\n\t"
      "v_cvt_pk_bf16_f32 v112, v88, v89\n\t"
      "s_waitcnt lgkmcnt(9)\n\t"
      "v_mfma_f32_16x16x32_bf16 v[72:75], a[24:27], v[12:15], v[72:75]\n\t"
      "ds_read_b128 v[52:55], v98 offset:29696\n\t"
      "v_mfma_f32_16x16x32_bf16 v[76:79], a[28:31], v[12:15], v[76:79]\n\t"
      "buffer_load_dwordx4 v97, s[60:63], s72 offen offset:1024 sc1 lds\n\t"
      "v_cvt_pk_bf16_f32 v82, v68, v69\n\t"
      "v_mfma_f32_16x16x32_bf16 v[92:95], a[140:143], v[12:15], v[92:95]\n\t"
      "v_cvt_pk_bf16_f32 v83, v70, v71\n\t"
      "v_cvt_pk_bf16_f32 v113, v90, v91\n\t"
      "s_waitcnt lgkmcnt(9)\n\t"
      "v_mfma_f32_16x16x32_bf16 v[72:75], a[32:35], v[16:19], v[72:75]\n\t"
      "ds_read_b128 v[56:59], v98 offset:30720\n\t"
      "v_mfma_f32_16x16x32_bf16 v[76:79], a[36:39], v[16:19], v[76:79]\n\t"
      "buffer_store_dwordx4 v[80:83], v97, s[60:63], s73 offen sc1\n\t"
      "v_mfma_f32_16x16x32_bf16 v[92:95], a[144:147], v[16:19], v[92:95]\n\t"
      "s_waitcnt lgkmcnt(9)\n\t"
      "v_mfma_f32_16x16x32_bf16 v[72:75], a[40:43], v[20:23], v[72:75]\n\t"
      "ds_read_b128 v[60:63], v98 offset:31744\n\t"
      "v_mfma_f32_16x16x32_bf16 v[76:79], a[44:47], v[20:23], v[76:79]\n\t"
      "v_mfma_f32_16x16x32_bf16 v[92:95], a[148:151], v[20:23], v[92:95]\n\t"
      "s_waitcnt lgkmcnt(9)\n\t"
      "v_mfma_f32_16x16x32_bf16 v[72:75], a[48:51], v[24:27], v[72:75]\n\t"
      "v_mfma_f32_16x16x32_bf16 v[76:79], a[52:55], v[24:27], v[76:79]\n\t"
      "buffer_load_dwordx4 v97, s[60:63], s72 offen offset:2048 sc1 lds\n\t"
      "v_add_u32 v98, s77, v97\n\t"
      "v_mfma_f32_16x16x32_bf16 v[92:95], a[152:155], v[24:27], v[92:95]\n\t"
      "s_cmp_lg_u32 s75, 0\n\t"
      "s_waitcnt lgkmcnt(8)\n\t"
      "v_mfma_f32_16x16x32_bf16 v[72:75], a[56:59], v[28:31], v[72:75]\n\t"
      "v_mfma_f32_16x16x32_bf16 v[76:79], a[60:63], v[28:31], v[76:79]\n\t"
      "s_cselect_b64 vcc, s[90:91], 0\n\t"
      "v_mfma_f32_16x16x32_bf16 v[92:95], a[156:159], v[28:31], v[92:95]\n\t"
      "s_waitcnt lgkmcnt(7)\n\t"
      "v_mfma_f32_16x16x32_bf16 v[72:75], a[64:67], v[32:35], v[72:75]\n\t"
      "v_mfma_f32_16x16x32_bf16 v[76:79], a[68:71], v[32:35], v[76:79]\n\t"
      "buffer_load_dwordx4 v97, s[60:63], s72 offen offset:3072 sc1 lds\n\t"
      "v_mfma_f32_16x16x32_bf16 v[92:95], a[160:163], v[32:35], v[92:95]\n\t"
      "s_cmp_gt_u32 s68, 7\n\t"
      "s_waitcnt lgkmcnt(6)\n\t"
      "v_mfma_f32_16x16x32_bf16 v[72:75], a[72:75], v[36:39], v[72:75]\n\t"
      "v_mfma_f32_16x16x32_bf16 v[76:79], a[76:79], v[36:39], v[76:79]\n\t"
      "v_mfma_f32_16x16x32_bf16 v[92:95], a[164:167], v[36:39], v[92:95]\n\t"
      "s_waitcnt vmcnt(22) lgkmcnt(0)\n\t"
      "s_cbranch_scc1 GEF%=\n\t"
      "s_waitcnt vmcnt(9)\n\t"
      "GEF%=:\n\t"
      "s_barrier\n\t"
      "v_mfma_f32_16x16x32_bf16 v[72:75], a[80:83], v[40:43], v[72:75]\n\t"
      "ds_read_b128 v[0:3], v98\n\t"
      "v_mfma_f32_16x16x32_bf16 v[76:79], a[84:87], v[40:43], v[76:79]\n\t"
      "ds_read_b128 v[4:7], v98 offset:1024\n\t"
      "v_mfma_f32_16x16x32_bf16 v[92:95], a[168:171], v[40:43], v[92:95]\n\t"
      "v_mfma_f32_16x16x32_bf16 v[72:75], a[88:91], v[44:47], v[72:75]\n\t"
      "ds_read_b128 v[8:11], v98 offset:2048\n\t"
      "v_mfma_f32_16x16x32_bf16 v[76:79], a[92:95], v[44:47], v[76:79]\n\t"
      "ds_read_b128 v[12:15], v98 offset:3072\n\t"
      "v_mfma_f32_16x16x32_bf16 v[92:95], a[172:175], v[44:47], v[92:95]\n\t"
      "v_mfma_f32_16x16x32_bf16 v[72:75], a[96:99], v[48:51], v[72:75]\n\t"
      "ds_read_b128 v[16:19], v98 offset:4096\n\t"
      "v_mfma_f32_16x16x32_bf16 v[76:79], a[100:103], v[48:51], v[76:79]\n\t"
      "ds_read_b128 v[20:23], v98 offset:5120\n\t"
      "v_mfma_f32_16x16x32_bf16 v[92:95], a[176:179], v[48:51], v[92:95]\n\t"
      "v_mfma_f32_16x16x32_bf16 v[72:75], a[104:107], v[52:55], v[72:75]\n\t"
      "ds_read_b128 v[24:27], v98 offset:6144\n\t"
      "v_mfma_f32_16x16x32_bf16 v[76:79], a[108:111], v[52:55], v[76:79]\n\t"
      "ds_read_b128 v[28:31], v98 offset:7168\n\t"
      "v_mfma_f32_16x16x32_bf16 v[92:95], a[180:183], v[52:55], v[92:95]\n\t"
      "v_mfma_f32_16x16x32_bf16 v[72:75], a[112:115], v[56:59], v[72:75]\n\t"
      "ds_read_b128 v[32:35], v98 offset:8192\n\t"
      "v_mfma_f32_16x16x32_bf16 v[76:79], a[116:119], v[56:59], v[76:79]\n\t"
      "ds_read_b128 v[36:39], v98 offset:9216\n\t"
      "v_mfma_f32_16x16x32_bf16 v[92:95], a[184:187], v[56:59], v[92:95]\n\t"
      "s_cbranch_vccnz GFLAG%=\n\t"
      "GFB%=:\n\t"
      "v_mfma_f32_16x16x32_bf16 v[72:75], a[120:123], v[60:63], v[72:75]\n\t"
      "v_mfma_f32_16x16x32_bf16 v[76:79], a[124:127], v[60:63], v[76:79]\n\t"
      "v_mfma_f32_16x16x32_bf16 v[92:95], a[188:191], v[60:63], v[92:95]\n\t"
      "s_branch GU%=\n\t"
      "GFLAG%=:\n\t"
      "s_mov_b64 exec, 1\n\t"
      "buffer_store_dword v107, off, s[60:63], s75 sc1\n\t"
      "s_mov_b64 exec, -1\n\t"
      "s_branch GFB%=\n\t"
      "GCALL%=:\n\t"
      "s_setpc_b64 s[88:89]\n\t"
      "GJOBS%=:\n\t"
      "s_nop 7\n\t"
      "v_readlane_b32 s75, v103, 3\n\t"
      "s_waitcnt vmcnt(9)\n\t"
      "s_barrier\n\t"
      "s_mov_b64 exec, s[90:91]\n\t"
      "s_nop 1\n\t"
      "buffer_store_dword v107, off, s[60:63], s75 sc1\n\t"
      "s_mov_b64 exec, -1\n\t"
      "s_mov_b32 s71, 6\n\t"
      "s_getpc_b64 s[88:89]\n\t"
      "s_add_u32 s88, s88, 12\n\t"
      "s_addc_u32 s89, s89, 0\n\t"
      "s_branch GBACK%=\n\t"
      "s_bitcmp0_b32 %0, 1\n\t"
      "s_cselect_b64 s[36:37], -1, 0\n\t"
      "s_cmp_lt_u32 s82, 28\n\t"
      "s_cselect_b64 s[38:39], -1, 0\n\t"
      "s_min_u32 s74, s82, 27\n\t"
      "v_lshrrev_b32 v104, 3, v96\n\t"
      "v_and_b32 v105, 7, v96\n\t"
      "s_mov_b32 s78, 3584\n\t"
      "v_mul_u32_u24 v104, s78, v104\n\t"
      "v_lshl_add_u32 v104, v105, 4, v104\n\t"
      "s_lshr_b32 s78, %0, 5\n\t"
      "s_lshl_b32 s78, s78, 2\n\t"
      "s_add_u32 s78, s78, %1\n\t"
      "s_mul_i32 s78, s78, 114688\n\t"
      "s_lshl_b32 s80, s74, 7\n\t"
      "s_add_u32 s80, s80, s78\n\t"
      "s_mov_b64 s[64:65], s[50:51]\n\t"
      "s_add_u32 s78, s80, 0\n\t"
      "buffer_load_dwordx4 v[128:131], v104, s[64:67], s78 offen \n\t"
      "s_add_u32 s78, s80, 28672\n\t"
      "buffer_load_dwordx4 v[132:135], v104, s[64:67], s78 offen \n\t"
      "s_add_u32 s78, s80, 57344\n\t"
      "buffer_load_dwordx4 v[136:139], v104, s[64:67], s78 offen \n\t"
      "s_add_u32 s71, s71, 1\n\t"
      "s_getpc_b64 s[88:89]\n\t"
      "s_add_u32 s88, s88, 12\n\t"
      "s_addc_u32 s89, s89, 0\n\t"
      "s_branch GBACK%=\n\t"
      "s_add_u32 s78, s80, 86016\n\t"
      "buffer_load_dwordx4 v[140:143], v104, s[64:67], s78 offen \n\t"
      "s_mov_b64 s[64:65], s[52:53]\n\t"
      "s_add_u32 s78, s80, 0\n\t"
      "buffer_load_dwordx4 v[144:147], v104, s[64:67], s78 offen \n\t"
      "s_add_u32 s78, s80, 28672\n\t"
      "buffer_load_dwordx4 v[148:151], v104, s[64:67], s78 offen \n\t"
      "s_add_u32 s71, s71, 1\n\t"
      "s_getpc_b64 s[88:89]\n\t"
      "s_add_u32 s88, s88, 12\n\t"
      "s_addc_u32 s89, s89, 0\n\t"
      "s_branch GBACK%=\n\t"
      "s_add_u32 s78, s80, 57344\n\t"
      "buffer_load_dwordx4 v[152:155], v104, s[64:67], s78 offen \n\t"
      "s_add_u32 s78, s80, 86016\n\t"
      "buffer_load_dwordx4 v[156:159], v104, s[64:67], s78 offen \n\t"
      "v_mov_b32 v120, -1\n\t"
      "v_mov_b32 v121, -1\n\t"
      "v_mov_b32 v122, -1\n\t"
      "v_mov_b32 v123, -1\n\t"
      "s_lshr_b32 s78, %0, 5\n\t"
      "s_lshl_b32 s78, s78, 2\n\t"
      "s_add_u32 s78, s78, %1\n\t"
      "s_lshr_b32 s79, s78, 1\n\t"
      "s_lshl_b32 s79, s79, 16\n\t"
      "s_lshl_b32 s80, s82, 11\n\t"
      "s_add_u32 s79, s79, s80\n\t"
      "s_and_b32 s78, s78, 1\n\t"
      "s_lshl_b32 s78, s78, 10\n\t"
      "s_add_u32 s79, s79, s78\n\t"
      "s_add_u32 s79, s79, 66813952\n\t"
      "buffer_store_dwordx4 v[120:123], v97, s[60:63], s79 offen sc1\n\t"
      "s_add_u32 s71, s71, 1\n\t"
      "s_getpc_b64 s[88:89]\n\t"
      "s_add_u32 s88, s88, 12\n\t"
      "s_addc_u32 s89, s89, 0\n\t"
      "s_branch GBACK%=\n\t"
      "s_sub_u32 s79, s79, 4194304\n\t"
      "buffer_store_dwordx4 v[120:123], v97, s[60:63], s79 offen sc1\n\t"
      "s_add_u32 s79, s79, 3670016\n\t"
      "buffer_store_dwordx4 v[120:123], v97, s[60:63], s79 offen sc1\n\t"
      "s_sub_u32 s79, s79, 4194304\n\t"
      "buffer_store_dwordx4 v[120:123], v97, s[60:63], s79 offen sc1\n\t"
      "s_add_u32 s71, s71, 1\n\t"
      "s_getpc_b64 s[88:89]\n\t"
      "s_add_u32 s88, s88, 12\n\t"
      "s_addc_u32 s89, s89, 0\n\t"
      "s_branch GBACK%=\n\t"
      "s_add_u32 s79, s79, 3670016\n\t"
      "buffer_store_dwordx4 v[120:123], v97, s[60:63], s79 offen sc1\n\t"
      "s_sub_u32 s79, s79, 4194304\n\t"
      "buffer_store_dwordx4 v[120:123], v97, s[60:63], s79 offen sc1\n\t"
      "s_mov_b64 s[64:65], s[54:55]\n\t"
      "v_lshrrev_b32 v104, 5, v96\n\t"
      "v_and_b32 v105, 31, v96\n\t"
      "v_lshlrev_b32 v104, 10, v104\n\t"
      "v_lshl_add_u32 v104, v105, 4, v104\n\t"
      "s_andn2_b32 s78, %0, 3\n\t"
      "s_add_u32 s78, s78, %1\n\t"
      "s_mul_i32 s78, s78, 14336\n\t"
      "s_and_b32 s79, %0, 1\n\t"
      "s_lshl_b32 s79, s79, 9\n\t"
      "s_add_u32 s78, s78, s79\n\t"
      "s_mov_b64 exec, s[36:37]\n\t"
      "buffer_load_dwordx4 v[160:163], v104, s[64:67], s78 offen \n\t"
      "s_mov_b64 exec, -1\n\t"
      "s_add_u32 s71, s71, 1\n\t"
      "s_getpc_b64 s[88:89]\n\t"
      "s_add_u32 s88, s88, 12\n\t"
      "s_addc_u32 s89, s89, 0\n\t"
      "s_branch GBACK%=\n\t"
      "s_mov_b64 exec, s[36:37]\n\t"
      "buffer_load_dwordx4 v[164:167], v104, s[64:67], s78 offen offset:2048 \n\t"
      "s_add_u32 s78, s78, 4096\n\t"
      "buffer_load_dwordx4 v[168:171], v104, s[64:67], s78 offen \n\t"
      "buffer_load_dwordx4 v[172:175], v104, s[64:67], s78 offen offset:2048 \n\t"
      "s_mov_b64 exec, -1\n\t"
      "s_add_u32 s71, s71, 1\n\t"
      "s_getpc_b64 s[88:89]\n\t"
      "s_add_u32 s88, s88, 12\n\t"
      "s_addc_u32 s89, s89, 0\n\t"
      "s_branch GBACK%=\n\t"
      "s_mov_b64 exec, s[36:37]\n\t"
      "s_add_u32 s78, s78, 4096\n\t"
      "buffer_load_dwordx4 v[176:179], v104, s[64:67], s78 offen \n\t"
      "buffer_load_dwordx4 v[180:183], v104, s[64:67], s78 offen offset:2048 \n\t"
      "s_add_u32 s78, s78, 4096\n\t"
      "buffer_load_dwordx4 v[184:187], v104, s[64:67], s78 offen \n\t"
      "s_mov_b64 exec, -1\n\t"
      "s_add_u32 s71, s71, 1\n\t"
      "s_getpc_b64 s[88:89]\n\t"
      "s_add_u32 s88, s88, 12\n\t"
      "s_addc_u32 s89, s89, 0\n\t"
      "s_branch GBACK%=\n\t"
      "s_mov_b64 exec, s[36:37]\n\t"
      "s_mov_b64 exec, -1\n\t"
      "v_and_b32 v106, 3, v96\n\t"
      "v_mul_u32_u24 v106, 24, v106\n\t"
      "v_lshlrev_b32 v104, 1, v100\n\t"
      "v_sub_u32 v106, v104, v106\n\t"
      "v_add_u32 v104, s76, v97\n\t"
      "v_add_u32 v105, s76, v106\n\t"
      "s_waitcnt vmcnt(30) lgkmcnt(0)\n\t"
      "ds_write_b128 v104, v[128:131]\n\t"
      "ds_write_b128 v104, v[132:135] offset:1024\n\t"
      "ds_write_b128 v104, v[136:139] offset:2048\n\t"
      "ds_write_b128 v104, v[140:143] offset:3072\n\t"
      "ds_write_b128 v104, v[144:147] offset:4096\n\t"
      "ds_write_b128 v104, v[148:151] offset:5120\n\t"
      "ds_write_b128 v104, v[152:155] offset:6144\n\t"
      "ds_write_b128 v104, v[156:159] offset:7168\n\t"
      "s_lshr_b32 s78, %0, 5\n\t"
      "s_lshl_b32 s78, s78, 2\n\t"
      "s_add_u32 s78, s78, %1\n\t"
      "s_lshl_b32 s78, s78, 10\n\t"
      "s_lshl_b32 s79, s74, 17\n\t"
      "s_add_u32 s78, s78, s79\n\t"
      "s_add_u32 s78, s78, 105086976\n\t"
      "s_waitcnt lgkmcnt(6)\n\t"
      "ds_read_b64_tr_b16 v[128:129], v105\n\t"
      "ds_read_b64_tr_b16 v[130:131], v105 offset:512\n\t"
      "ds_read_b64_tr_b16 v[132:133], v105 offset:4096\n\t"
      "ds_read_b64_tr_b16 v[134:135], v105 offset:4608\n\t"
      "ds_read_b64_tr_b16 v[136:137], v105 offset:32\n\t"
      "ds_read_b64_tr_b16 v[138:139], v105 offset:544\n\t"
      "ds_read_b64_tr_b16 v[140:141], v105 offset:4128\n\t"
      "ds_read_b64_tr_b16 v[142:143], v105 offset:4640\n\t"
      "s_waitcnt lgkmcnt(6)\n\t"
      "ds_read_b64_tr_b16 v[144:145], v105 offset:64\n\t"
      "ds_read_b64_tr_b16 v[146:147], v105 offset:576\n\t"
      "ds_read_b64_tr_b16 v[148:149], v105 offset:4160\n\t"
      "ds_read_b64_tr_b16 v[150:151], v105 offset:4672\n\t"
      "ds_read_b64_tr_b16 v[152:153], v105 offset:96\n\t"
      "ds_read_b64_tr_b16 v[154:155], v105 offset:608\n\t"
      "ds_read_b64_tr_b16 v[156:157], v105 offset:4192\n\t"
      "ds_read_b64_tr_b16 v[158:159], v105 offset:4704\n\t"
      "s_waitcnt lgkmcnt(0)\n\t"
      "s_mov_b64 exec, s[38:39]\n\t"
      "buffer_store_dwordx4 v[128:131], v97, s[60:63], s78 offen sc1\n\t"
      "s_add_u32 s78, s78, 16384\n\t"
      "buffer_store_dwordx4 v[132:135], v97, s[60:63], s78 offen sc1\n\t"
      "s_add_u32 s78, s78, 16384\n\t"
      "buffer_store_dwordx4 v[136:139], v97, s[60:63], s78 offen sc1\n\t"
      "s_mov_b64 exec, -1\n\t"
      "s_add_u32 s71, s71, 1\n\t"
      "s_getpc_b64 s[88:89]\n\t"
      "s_add_u32 s88, s88, 12\n\t"
      "s_addc_u32 s89, s89, 0\n\t"
      "s_branch GBACK%=\n\t"
      "s_mov_b64 exec, s[38:39]\n\t"
      "s_add_u32 s78, s78, 16384\n\t"
      "buffer_store_dwordx4 v[140:143], v97, s[60:63], s78 offen sc1\n\t"
      "s_add_u32 s78, s78, 16384\n\t"
      "buffer_store_dwordx4 v[144:147], v97, s[60:63], s78 offen sc1\n\t"
      "s_add_u32 s78, s78, 16384\n\t"
      "buffer_store_dwordx4 v[148:151], v97, s[60:63], s78 offen sc1\n\t"
      "s_mov_b64 exec, -1\n\t"
      "s_add_u32 s71, s71, 1\n\t"
      "s_getpc_b64 s[88:89]\n\t"
      "s_add_u32 s88, s88, 12\n\t"
      "s_addc_u32 s89, s89, 0\n\t"
      "s_branch GBACK%=\n\t"
      "s_mov_b64 exec, s[38:39]\n\t"
      "s_add_u32 s78, s78, 16384\n\t"
      "buffer_store_dwordx4 v[152:155], v97, s[60:63], s78 offen sc1\n\t"
      "s_add_u32 s78, s78, 16384\n\t"
      "buffer_store_dwordx4 v[156:159], v97, s[60:63], s78 offen sc1\n\t"
      "s_add_u32 s78, s78, 16384\n\t"
      "s_mov_b64 exec, -1\n\t"
      "v_lshlrev_b32 v104, 10, v96\n\t"
      "s_and_b32 s78, %0, 1\n\t"
      "s_lshl_b32 s78, s78, 5\n\t"
      "s_lshr_b32 s79, %0, 2\n\t"
      "s_add_u32 s78, s78, s79\n\t"
      "s_lshl_b32 s78, s78, 4\n\t"
      "s_lshl_b32 s79, %1, 16\n\t"
      "s_add_u32 s78, s78, s79\n\t"
      "s_lshl_b32 s79, %1, 10\n\t"
      "s_mov_b64 exec, s[36:37]\n\t"
      "s_add_u32 m0, s79, 131072\n\t"
      "s_nop 0\n\t"
      "buffer_load_dwordx4 v104, s[64:67], s78 offen lds\n\t"
      "s_mov_b64 exec, -1\n\t"
      "s_add_u32 s71, s71, 1\n\t"
      "s_getpc_b64 s[88:89]\n\t"
      "s_add_u32 s88, s88, 12\n\t"
      "s_addc_u32 s89, s89, 0\n\t"
      "s_branch GBACK%=\n\t"
      "s_mov_b64 exec, s[36:37]\n\t"
      "s_add_u32 s78, s78, 0x40000\n\t"
      "s_add_u32 m0, s79, 135168\n\t"
      "s_nop 0\n\t"
      "buffer_load_dwordx4 v104, s[64:67], s78 offen lds\n\t"
      "s_add_u32 s78, s78, 0x40000\n\t"
      "s_add_u32 m0, s79, 139264\n\t"
      "s_nop 0\n\t"
      "buffer_load_dwordx4 v104, s[64:67], s78 offen lds\n\t"
      "s_add_u32 s78, s78, 0x40000\n\t"
      "s_add_u32 m0, s79, 143360\n\t"
      "s_nop 0\n\t"
      "buffer_load_dwordx4 v104, s[64:67], s78 offen lds\n\t"
      "s_mov_b64 exec, -1\n\t"
      "s_add_u32 s71, s71, 1\n\t"
      "s_getpc_b64 s[88:89]\n\t"
      "s_add_u32 s88, s88, 12\n\t"
      "s_addc_u32 s89, s89, 0\n\t"
      "s_branch GBACK%=\n\t"
      "s_mov_b64 exec, s[36:37]\n\t"
      "s_add_u32 s78, s78, 0x40000\n\t"
      "s_add_u32 m0, s79, 147456\n\t"
      "s_nop 0\n\t"
      "buffer_load_dwordx4 v104, s[64:67], s78 offen lds\n\t"
      "s_add_u32 s78, s78, 0x40000\n\t"
      "s_add_u32 m0, s79, 151552\n\t"
      "s_nop 0\n\t"
      "buffer_load_dwordx4 v104, s[64:67], s78 offen lds\n\t"
      "s_add_u32 s78, s78, 0x40000\n\t"
      "s_add_u32 m0, s79, 155648\n\t"
      "s_nop 0\n\t"
      "buffer_load_dwordx4 v104, s[64:67], s78 offen lds\n\t"
      "s_mov_b64 exec, -1\n\t"
      "s_add_u32 s71, s71, 1\n\t"
      "s_getpc_b64 s[88:89]\n\t"
      "s_add_u32 s88, s88, 12\n\t"
      "s_addc_u32 s89, s89, 0\n\t"
      "s_branch GBACK%=\n\t"
      "s_mov_b64 exec, s[36:37]\n\t"
      "s_add_u32 s78, s78, 0x40000\n\t"
      "s_mov_b64 exec, -1\n\t"
      "s_mov_b64 s[64:65], s[46:47]\n\t"
      "v_lshrrev_b32 v99, 2, v96\n\t"
      "v_and_b32 v106, 3, v96\n\t"
      "v_lshlrev_b32 v99, 10, v99\n\t"
      "v_lshl_add_u32 v99, v106, 4, v99\n\t"
      "s_lshr_b32 s78, %0, 4\n\t"
      "s_lshl_b32 s78, s78, 19\n\t"
      "s_bfe_u32 s79, %0, 0x20002\n\t"
      "s_lshl_b32 s79, s79, 8\n\t"
      "s_add_u32 s78, s78, s79\n\t"
      "s_lshl_b32 s79, %1, 6\n\t"
      "s_add_u32 s78, s78, s79\n\t"
      "v_add_u32 v99, s78, v99\n\t"
      "s_mov_b32 s78, 262144\n\t"
      "buffer_load_dwordx4 v[192:195], v99, s[64:67], s78 offen \n\t"
      "s_mov_b32 s78, 278528\n\t"
      "buffer_load_dwordx4 v[196:199], v99, s[64:67], s78 offen \n\t"
      "s_mov_b32 s78, 294912\n\t"
      "buffer_load_dwordx4 v[200:203], v99, s[64:67], s78 offen \n\t"
      "s_add_u32 s71, s71, 1\n\t"
      "s_getpc_b64 s[88:89]\n\t"
      "s_add_u32 s88, s88, 12\n\t"
      "s_addc_u32 s89, s89, 0\n\t"
      "s_branch GBACK%=\n\t"
      "s_mov_b32 s78, 311296\n\t"
      "buffer_load_dwordx4 v[204:207], v99, s[64:67], s78 offen \n\t"
      "s_mov_b32 s78, 327680\n\t"
      "buffer_load_dwordx4 v[208:211], v99, s[64:67], s78 offen \n\t"
      "s_mov_b32 s78, 344064\n\t"
      "buffer_load_dwordx4 v[212:215], v99, s[64:67], s78 offen \n\t"
      "s_add_u32 s71, s71, 1\n\t"
      "s_getpc_b64 s[88:89]\n\t"
      "s_add_u32 s88, s88, 12\n\t"
      "s_addc_u32 s89, s89, 0\n\t"
      "s_branch GBACK%=\n\t"
      "s_mov_b32 s78, 360448\n\t"
      "buffer_load_dwordx4 v[216:219], v99, s[64:67], s78 offen \n\t"
      "s_mov_b32 s78, 376832\n\t"
      "buffer_load_dwordx4 v[220:223], v99, s[64:67], s78 offen \n\t"
      "s_mov_b32 s78, 393216\n\t"
      "buffer_load_dwordx4 v[224:227], v99, s[64:67], s78 offen \n\t"
      "s_add_u32 s71, s71, 1\n\t"
      "s_getpc_b64 s[88:89]\n\t"
      "s_add_u32 s88, s88, 12\n\t"
      "s_addc_u32 s89, s89, 0\n\t"
      "s_branch GBACK%=\n\t"
      "s_mov_b32 s78, 409600\n\t"
      "buffer_load_dwordx4 v[228:231], v99, s[64:67], s78 offen \n\t"
      "s_mov_b32 s78, 425984\n\t"
      "buffer_load_dwordx4 v[232:235], v99, s[64:67], s78 offen \n\t"
      "s_mov_b32 s78, 442368\n\t"
      "buffer_load_dwordx4 v[236:239], v99, s[64:67], s78 offen \n\t"
      "s_add_u32 s71, s71, 1\n\t"
      "s_getpc_b64 s[88:89]\n\t"
      "s_add_u32 s88, s88, 12\n\t"
      "s_addc_u32 s89, s89, 0\n\t"
      "s_branch GBACK%=\n\t"
      "s_mov_b32 s78, 458752\n\t"
      "buffer_load_dwordx4 v[240:243], v99, s[64:67], s78 offen \n\t"
      "s_mov_b32 s78, 475136\n\t"
      "buffer_load_dwordx4 v[244:247], v99, s[64:67], s78 offen \n\t"
      "s_mov_b32 s78, 491520\n\t"
      "buffer_load_dwordx4 v[248:251], v99, s[64:67], s78 offen \n\t"
      "s_add_u32 s71, s71, 1\n\t"
      "s_getpc_b64 s[88:89]\n\t"
      "s_add_u32 s88, s88, 12\n\t"
      "s_addc_u32 s89, s89, 0\n\t"
      "s_branch GBACK%=\n\t"
      "s_mov_b32 s78, 507904\n\t"
      "buffer_load_dwordx4 v[252:255], v99, s[64:67], s78 offen \n\t"
      "s_mov_b32 s78, 0\n\t"
      "buffer_load_dwordx4 v[128:131], v99, s[64:67], s78 offen \n\t"
      "s_mov_b32 s78, 16384\n\t"
      "buffer_load_dwordx4 v[132:135], v99, s[64:67], s78 offen \n\t"
      "s_add_u32 s71, s71, 1\n\t"
      "s_getpc_b64 s[88:89]\n\t"
      "s_add_u32 s88, s88, 12\n\t"
      "s_addc_u32 s89, s89, 0\n\t"
      "s_branch GBACK%=\n\t"
      "s_mov_b32 s78, 32768\n\t"
      "buffer_load_dwordx4 v[136:139], v99, s[64:67], s78 offen \n\t"
      "s_mov_b32 s78, 49152\n\t"
      "buffer_load_dwordx4 v[140:143], v99, s[64:67], s78 offen \n\t"
      "s_mov_b32 s78, 65536\n\t"
      "buffer_load_dwordx4 v[144:147], v99, s[64:67], s78 offen \n\t"
      "s_add_u32 s71, s71, 1\n\t"
      "s_getpc_b64 s[88:89]\n\t"
      "s_add_u32 s88, s88, 12\n\t"
      "s_addc_u32 s89, s89, 0\n\t"
      "s_branch GBACK%=\n\t"
      "s_mov_b32 s78, 81920\n\t"
      "buffer_load_dwordx4 v[148:151], v99, s[64:67], s78 offen \n\t"
      "s_mov_b32 s78, 98304\n\t"
      "buffer_load_dwordx4 v[152:155], v99, s[64:67], s78 offen \n\t"
      "s_mov_b32 s78, 114688\n\t"
      "buffer_load_dwordx4 v[156:159], v99, s[64:67], s78 offen \n\t"
      "s_add_u32 s71, s71, 1\n\t"
      "s_getpc_b64 s[88:89]\n\t"
      "s_add_u32 s88, s88, 12\n\t"
      "s_addc_u32 s89, s89, 0\n\t"
      "s_branch GBACK%=\n\t"
      "s_mov_b32 s78, 131072\n\t"
      "buffer_load_dwordx4 v[160:163], v99, s[64:67], s78 offen \n\t"
      "s_mov_b32 s78, 147456\n\t"
      "buffer_load_dwordx4 v[164:167], v99, s[64:67], s78 offen \n\t"
      "s_mov_b32 s78, 163840\n\t"
      "buffer_load_dwordx4 v[168:171], v99, s[64:67], s78 offen \n\t"
      "s_add_u32 s71, s71, 1\n\t"
      "s_getpc_b64 s[88:89]\n\t"
      "s_add_u32 s88, s88, 12\n\t"
      "s_addc_u32 s89, s89, 0\n\t"
      "s_branch GBACK%=\n\t"
      "s_mov_b32 s78, 180224\n\t"
      "buffer_load_dwordx4 v[172:175], v99, s[64:67], s78 offen \n\t"
      "s_mov_b32 s78, 196608\n\t"
      "buffer_load_dwordx4 v[176:179], v99, s[64:67], s78 offen \n\t"
      "s_mov_b32 s78, 212992\n\t"
      "buffer_load_dwordx4 v[180:183], v99, s[64:67], s78 offen \n\t"
      "s_add_u32 s71, s71, 1\n\t"
      "s_getpc_b64 s[88:89]\n\t"
      "s_add_u32 s88, s88, 12\n\t"
      "s_addc_u32 s89, s89, 0\n\t"
      "s_branch GBACK%=\n\t"
      "s_mov_b32 s78, 229376\n\t"
      "buffer_load_dwordx4 v[184:187], v99, s[64:67], s78 offen \n\t"
      "s_mov_b32 s78, 245760\n\t"
      "buffer_load_dwordx4 v[188:191], v99, s[64:67], s78 offen \n\t"
      "s_mov_b32 s71, 32\n\t"
      "s_getpc_b64 s[88:89]\n\t"
      "s_add_u32 s88, s88, 12\n\t"
      "s_addc_u32 s89, s89, 0\n\t"
      "s_branch GBACK%=\n\t"
      "s_nop 7\n\t"
      "v_add_u32 v104, s76, v97\n\t"
      "v_add_u32 v105, s76, v100\n\t"
      "s_sub_u32 s68, s68, 1\n\t"
      "s_waitcnt vmcnt(11) lgkmcnt(0)\n\t"
      "v_cvt_pk_bf16_f32 v84, v72, v73\n\t"
      "v_cvt_pk_bf16_f32 v85, v74, v75\n\t"
      "v_cvt_pk_bf16_f32 v86, v76, v77\n\t"
      "v_cvt_pk_bf16_f32 v87, v78, v79\n\t"
      "buffer_store_dwordx4 v[84:87], v97, s[60:63], s73 offen offset:1024 sc1\n\t"
      "v_cvt_pk_bf16_f32 v114, v92, v93\n\t"
      "v_cvt_pk_bf16_f32 v115, v94, v95\n\t"
      "s_nop 0\n\t"
      "v_permlane16_swap_b32 v112, v114\n\t"
      "v_permlane16_swap_b32 v113, v115\n\t"
      "buffer_store_dwordx4 v[112:115], v111, s[60:63], s73 offen sc1\n\t"
      "v_readlane_b32 s75, v103, s68\n\t"
      "s_waitcnt lgkmcnt(6)\n\t"
      "ds_write_b128 v104, v[128:131]\n\t"
      "ds_write_b128 v104, v[132:135] offset:1024\n\t"
      "ds_read_b64_tr_b16 a[0:1], v105\n\t"
      "ds_read_b64_tr_b16 a[2:3], v105 offset:256\n\t"
      "ds_read_b64_tr_b16 a[64:65], v105 offset:8\n\t"
      "ds_read_b64_tr_b16 a[66:67], v105 offset:264\n\t"
      "s_waitcnt lgkmcnt(6)\n\t"
      "ds_write_b128 v104, v[136:139] offset:2048\n\t"
      "ds_write_b128 v104, v[140:143] offset:3072\n\t"
      "ds_read_b64_tr_b16 a[4:5], v105 offset:2048\n\t"
      "ds_read_b64_tr_b16 a[6:7], v105 offset:2304\n\t"
      "ds_read_b64_tr_b16 a[68:69], v105 offset:2056\n\t"
      "ds_read_b64_tr_b16 a[70:71], v105 offset:2312\n\t"
      "s_waitcnt lgkmcnt(6)\n\t"
      "ds_write_b128 v104, v[144:147] offset:4096\n\t"
      "ds_write_b128 v104, v[148:151] offset:5120\n\t"
      "ds_read_b64_tr_b16 a[8:9], v105 offset:4096\n\t"
      "ds_read_b64_tr_b16 a[10:11], v105 offset:4352\n\t"
      "ds_read_b64_tr_b16 a[72:73], v105 offset:4104\n\t"
      "ds_read_b64_tr_b16 a[74:75], v105 offset:4360\n\t"
      "s_waitcnt lgkmcnt(6)\n\t"
      "ds_write_b128 v104, v[152:155] offset:6144\n\t"
      "ds_write_b128 v104, v[156:159] offset:7168\n\t"
      "ds_read_b64_tr_b16 a[12:13], v105 offset:6144\n\t"
      "ds_read_b64_tr_b16 a[14:15], v105 offset:6400\n\t"
      "ds_read_b64_tr_b16 a[76:77], v105 offset:6152\n\t"
      "ds_read_b64_tr_b16 a[78:79], v105 offset:6408\n\t"
      "s_waitcnt lgkmcnt(6)\n\t"
      "ds_write_b128 v104, v[160:163]\n\t"
      "ds_write_b128 v104, v[164:167] offset:1024\n\t"
      "ds_read_b64_tr_b16 a[16:17], v105\n\t"
      "ds_read_b64_tr_b16 a[18:19], v105 offset:256\n\t"
      "ds_read_b64_tr_b16 a[80:81], v105 offset:8\n\t"
      "ds_read_b64_tr_b16 a[82:83], v105 offset:264\n\t"
      "s_waitcnt lgkmcnt(6)\n\t"
      "ds_write_b128 v104, v[168:171] offset:2048\n\t"
      "ds_write_b128 v104, v[172:175] offset:3072\n\t"
      "ds_read_b64_tr_b16 a[20:21], v105 offset:2048\n\t"
      "ds_read_b64_tr_b16 a[22:23], v105 offset:2304\n\t"
      "ds_read_b64_tr_b16 a[84:85], v105 offset:2056\n\t"
      "ds_read_b64_tr_b16 a[86:87], v105 offset:2312\n\t"
      "s_waitcnt lgkmcnt(6)\n\t"
      "ds_write_b128 v104, v[176:179] offset:4096\n\t"
      "ds_write_b128 v104, v[180:183] offset:5120\n\t"
      "ds_read_b64_tr_b16 a[24:25], v105 offset:4096\n\t"
      "ds_read_b64_tr_b16 a[26:27], v105 offset:4352\n\t"
      "ds_read_b64_tr_b16 a[88:89], v105 offset:4104\n\t"
      "ds_read_b64_tr_b16 a[90:91], v105 offset:4360\n\t"
      "s_waitcnt lgkmcnt(6)\n\t"
      "ds_write_b128 v104, v[184:187] offset:6144\n\t"
      "ds_write_b128 v104, v[188:191] offset:7168\n\t"
      "ds_read_b64_tr_b16 a[28:29], v105 offset:6144\n\t"
      "ds_read_b64_tr_b16 a[30:31], v105 offset:6400\n\t"
      "ds_read_b64_tr_b16 a[92:93], v105 offset:6152\n\t"
      "ds_read_b64_tr_b16 a[94:95], v105 offset:6408\n\t"
      "s_waitcnt lgkmcnt(6)\n\t"
      "ds_write_b128 v104, v[192:195]\n\t"
      "ds_write_b128 v104, v[196:199] offset:1024\n\t"
      "ds_read_b64_tr_b16 a[32:33], v105\n\t"
      "ds_read_b64_tr_b16 a[34:35], v105 offset:256\n\t"
      "ds_read_b64_tr_b16 a[96:97], v105 offset:8\n\t"
      "ds_read_b64_tr_b16 a[98:99], v105 offset:264\n\t"
      "s_waitcnt lgkmcnt(6)\n\t"
      "ds_write_b128 v104, v[200:203] offset:2048\n\t"
      "ds_write_b128 v104, v[204:207] offset:3072\n\t"
      "ds_read_b64_tr_b16 a[36:37], v105 offset:2048\n\t"
      "ds_read_b64_tr_b16 a[38:39], v105 offset:2304\n\t"
      "ds_read_b64_tr_b16 a[100:101], v105 offset:2056\n\t"
      "ds_read_b64_tr_b16 a[102:103], v105 offset:2312\n\t"
      "s_waitcnt lgkmcnt(6)\n\t"
      "ds_write_b128 v104, v[208:211] offset:4096\n\t"
      "ds_write_b128 v104, v[212:215] offset:5120\n\t"
      "ds_read_b64_tr_b16 a[40:41], v105 offset:4096\n\t"
      "ds_read_b64_tr_b16 a[42:43], v105 offset:4352\n\t"
      "ds_read_b64_tr_b16 a[104:105], v105 offset:4104\n\t"
      "ds_read_b64_tr_b16 a[106:107], v105 offset:4360\n\t"
      "s_waitcnt lgkmcnt(6)\n\t"
      "ds_write_b128 v104, v[216:219] offset:6144\n\t"
      "ds_write_b128 v104, v[220:223] offset:7168\n\t"
      "ds_read_b64_tr_b16 a[44:45], v105 offset:6144\n\t"
      "ds_read_b64_tr_b16 a[46:47], v105 offset:6400\n\t"
      "ds_read_b64_tr_b16 a[108:109], v105 offset:6152\n\t"
      "ds_read_b64_tr_b16 a[110:111], v105 offset:6408\n\t"
      "s_waitcnt lgkmcnt(6)\n\t"
      "ds_write_b128 v104, v[224:227]\n\t"
      "ds_write_b128 v104, v[228:231] offset:1024\n\t"
      "ds_read_b64_tr_b16 a[48:49], v105\n\t"
      "ds_read_b64_tr_b16 a[50:51], v105 offset:256\n\t"
      "ds_read_b64_tr_b16 a[112:113], v105 offset:8\n\t"
      "ds_read_b64_tr_b16 a[114:115], v105 offset:264\n\t"
      "s_waitcnt lgkmcnt(6)\n\t"
      "ds_write_b128 v104, v[232:235] offset:2048\n\t"
      "ds_write_b128 v104, v[236:239] offset:3072\n\t"
      "ds_read_b64_tr_b16 a[52:53], v105 offset:2048\n\t"
      "ds_read_b64_tr_b16 a[54:55], v105 offset:2304\n\t"
      "ds_read_b64_tr_b16 a[116:117], v105 offset:2056\n\t"
      "ds_read_b64_tr_b16 a[118:119], v105 offset:2312\n\t"
      "s_waitcnt lgkmcnt(6)\n\t"
      "ds_write_b128 v104, v[240:243] offset:4096\n\t"
      "ds_write_b128 v104, v[244:247] offset:5120\n\t"
      "ds_read_b64_tr_b16 a[56:57], v105 offset:4096\n\t"
      "ds_read_b64_tr_b16 a[58:59], v105 offset:4352\n\t"
      "ds_read_b64_tr_b16 a[120:121], v105 offset:4104\n\t"
      "ds_read_b64_tr_b16 a[122:123], v105 offset:4360\n\t"
      "s_waitcnt lgkmcnt(6)\n\t"
      "ds_write_b128 v104, v[248:251] offset:6144\n\t"
      "ds_write_b128 v104, v[252:255] offset:7168\n\t"
      "ds_read_b64_tr_b16 a[60:61], v105 offset:6144\n\t"
      "ds_read_b64_tr_b16 a[62:63], v105 offset:6400\n\t"
      "ds_read_b64_tr_b16 a[124:125], v105 offset:6152\n\t"
      "ds_read_b64_tr_b16 a[126:127], v105 offset:6408\n\t"
      "s_waitcnt vmcnt(0) lgkmcnt(0)\n\t"
      "s_barrier\n\t"
      "s_mov_b64 exec, s[90:91]\n\t"
      "buffer_store_dword v107, off, s[60:63], s75 sc1\n\t"
      "s_sub_u32 s75, s75, 32\n\t"
      "buffer_store_dword v107, off, s[60:63], s75 sc1\n\t"
      "s_sub_u32 s75, s75, 2016\n\t"
      "buffer_store_dword v107, off, s[60:63], s75 sc1\n\t"
      "s_mov_b64 exec, -1\n\t"
      : "+{s34}"(w), "+{s33}"(wv), "+{s[0:1]}"(A), "=&s"(tag), "={a[0:31]}"(wo.a), "={a[32:63]}"(wo.b), "={a[64:95]}"(wo.c), "={a[96:127]}"(wo.d)
      :
      : "memory", "vcc", "scc", "v0", "v1", "v2", "v3", "v4", "v5", "v6", "v7", "v8", "v9", "v10", "v11", "v12", "v13", "v14", "v15", "v16", "v17", "v18", "v19", "v20", "v21", "v22", "v23", "v24", "v25", "v26", "v27", "v28", "v29", "v30", "v31", "v32", "v33", "v34", "v35", "v36", "v37", "v38", "v39", "v40", "v41", "v42", "v43", "v44", "v45", "v46", "v47", "v48", "v49", "v50", "v51", "v52", "v53", "v54", "v55", "v56", "v57", "v58", "v59", "v60", "v61", "v62", "v63", "v64", "v65", "v66", "v67", "v68", "v69", "v70", "v71", "v72", "v73", "v74", "v75", "v76", "v77", "v78", "v79", "v80", "v81", "v82", "v83", "v84", "v85", "v86", "v87", "v88", "v89", "v90", "v91", "v92", "v93", "v94", "v95", "v96", "v97", "v98", "v99", "v100", "v101", "v102", "v103", "v104", "v105", "v106", "v107", "v108", "v109", "v110", "v111", "v112", "v113", "v114", "v115", "v116", "v117", "v118", "v119", "v120", "v121", "v122", "v123", "v124", "v125", "v126", "v127", "v128", "v129", "v130", "v131", "v132", "v133", "v134", "v135", "v136", "v137", "v138", "v139", "v140", "v141", "v142", "v143", "v144", "v145", "v146", "v147", "v148", "v149", "v150", "v151", "v152", "v153", "v154", "v155", "v156", "v157", "v158", "v159", "v160", "v161", "v162", "v163", "v164", "v165", "v166", "v167", "v168", "v169", "v170", "v171", "v172", "v173", "v174", "v175", "v176", "v177", "v178", "v179", "v180", "v181", "v182", "v183", "v184", "v185", "v186", "v187", "v188", "v189", "v190", "v191", "v192", "v193", "v194", "v195", "v196", "v197", "v198", "v199", "v200", "v201", "v202", "v203", "v204", "v205", "v206", "v207", "v208", "v209", "v210", "v211", "v212", "v213", "v214", "v215", "v216", "v217", "v218", "v219", "v220", "v221", "v222", "v223", "v224", "v225", "v226", "v227", "v228", "v229", "v230", "v231", "v232", "v233", "v234", "v235", "v236", "v237", "v238", "v239", "v240", "v241", "v242", "v243", "v244", "v245", "v246", "v247", "v248", "v249", "v250", "v251", "v252", "v253", "v254", "v255", "a128", "a129", "a130", "a131", "a132", "a133", "a134", "a135", "a136", "a137", "a138", "a139", "a140", "a141", "a142", "a143", "a144", "a145", "a146", "a147", "a148", "a149", "a150", "a151", "a152", "a153", "a154", "a155", "a156", "a157", "a158", "a159", "a160", "a161", "a162", "a163", "a164", "a165", "a166", "a167", "a168", "a169", "a170", "a171", "a172", "a173", "a174", "a175", "a176", "a177", "a178", "a179", "a180", "a181", "a182", "a183", "a184", "a185", "a186", "a187", "a188", "a189", "a190", "a191", "a192", "a193", "a194", "a195", "a196", "a197", "a198", "a199", "a200", "a201", "a202", "a203", "a204", "a205", "a206", "a207", "a208", "a209", "a210", "a211", "a212", "a213", "a214", "a215", "a216", "a217", "a218", "a219", "a220", "a221", "a222", "a223", "a224", "a225", "a226", "a227", "a228", "a229", "a230", "a231", "a232", "a233", "a234", "a235", "a236", "a237", "a238", "a239", "a240", "a241", "a242", "a243", "a244", "a245", "a246", "a247", "a248", "a249", "a250", "a251", "a252", "a253", "a254", "a255", "s36", "s37", "s38", "s39", "s40", "s41", "s42", "s43", "s44", "s45", "s46", "s47", "s48", "s49", "s50", "s51", "s52", "s53", "s54", "s55", "s56", "s57", "s58", "s59", "s60", "s61", "s62", "s63", "s64", "s65", "s66", "s67", "s68", "s69", "s70", "s71", "s72", "s73", "s74", "s75", "s76", "s77", "s78", "s79", "s80", "s81", "s82", "s83", "s84", "s85", "s86", "s87", "s88", "s89", "s90", "s91", "s92", "s93", "s94", "s95");
  return tag;
}

__device__ __forceinline__ void worker_tasks(char* lds, int& w, int& wv, KArgs& A, unsigned tag, WoRegs& wo) {
  asm volatile("" : "+s"(w), "+s"(wv), "+s"(A));
  const int lane = fresh_lane(), l15 = lane & 15, l4 = lane >> 4;
  const int team = w & 3, j = w >> 2, g = j >> 2, c = j & 3, h = g * 4 + wv;
  const u64 wsa = (u64)A->ws, xa = (u64)A->x, oa = (u64)A->out;
  u32x16 c0 = {(unsigned)wsa, (unsigned)(wsa >> 32) & 0xffffu, WS_BYTES, 0x00020000u, (unsigned)xa, (unsigned)(xa >> 32) & 0xffffu, S_ * DM * 2, 0x00020000u,
               (unsigned)oa, (unsigned)(oa >> 32) & 0xffffu, S_ * DM * 2, 0x00020000u, O_STAMPS + (310000 + j * 4 + wv) * 8, (unsigned)(l15 - l4 * 4),
               (unsigned)(team + 4 * wv + 48), (unsigned)j};
  const int r0 = 0 ? h : j;
  u32x16 c1 = {(unsigned)team, (unsigned)(team + 4 * (0 ? c : wv)), O_QKV + (team * 32 + h) * 24576, O_KVM + (team * 32 + h) * 2048, O_VTM + (team * 32 + h) * 2048,
               O_PART + (team * 8 + g) * 32768, O_PART + r0 * 1024, O_PFLAG + team * 16384, O_KVSTEP + h * 64, O_RBUF, O_LNH, O_HBUF, (unsigned)r0,
               (unsigned)wv * 8192, tag, O_PFLAG + team * 16384 + (j * 4 + wv) * 128};
  u32x8 c4 = {(unsigned)lane, (unsigned)lane, (unsigned)lane, (unsigned)lane, O_TFLAG + (6 * 32 + h) * 64 + 32 + (l15 & 1) * 16, O_TFLAG + (15 * 32 + h) * 64 + 32, O_WM + lane * 256 + 4, (unsigned)lane};
  const int rc = 8 * (l15 + 16 * (l4 >> 1)) + 4 * (l4 & 1);
  const u32x2 w0 = *(const u32x2*)(A->w_r + rc), w1 = *(const u32x2*)(A->w_r + 256 + rc);
  u32x16 c2 = {w0[0] << 16, w0[0] & 0xffff0000u, w0[1] << 16, w0[1] & 0xffff0000u, w1[0] << 16, w1[0] & 0xffff0000u, w1[1] << 16, w1[1] & 0xffff0000u,
               (unsigned)lane * 16, (unsigned)rc * 2, (unsigned)(lane ^ ((l4 & 1) * 8)) * 16,
               (unsigned)(wv * 8192 + (l15 & 3) * 256 + (((l4 * 4 + (l15 >> 2)) ^ ((l15 & 1) * 8)) << 4)),
               (unsigned)(l4 * 128 + (l15 & 7) * 16), (unsigned)(lane & 31) * 16,
               (unsigned)(65536 + wv * 8192 + lane * 16), (unsigned)(65536 + lane * 16)};
  unsigned c3 = (unsigned)(l15 * DM + c * 128 + wv * 32 + l4 * 8) * 2;
  asm volatile(
      ".p2align 5\n\t"
      "s_nop 1\n\t"
      "v_readfirstlane_b32 s36, v0\n\t"
      "v_readfirstlane_b32 s37, v1\n\t"
      "v_readfirstlane_b32 s38, v2\n\t"
      "v_readfirstlane_b32 s39, v3\n\t"
      "v_readfirstlane_b32 s40, v4\n\t"
      "v_readfirstlane_b32 s41, v5\n\t"
      "v_readfirstlane_b32 s42, v6\n\t"
      "v_readfirstlane_b32 s43, v7\n\t"
      "v_readfirstlane_b32 s44, v8\n\t"
      "v_readfirstlane_b32 s45, v9\n\t"
      "v_readfirstlane_b32 s46, v10\n\t"
      "v_readfirstlane_b32 s47, v11\n\t"
      "v_readfirstlane_b32 s48, v16\n\t"
      "v_readfirstlane_b32 s49, v17\n\t"
      "v_readfirstlane_b32 s50, v18\n\t"
      "v_readfirstlane_b32 s51, v19\n\t"
      "v_readfirstlane_b32 s52, v20\n\t"
      "v_readfirstlane_b32 s53, v21\n\t"
      "v_readfirstlane_b32 s54, v22\n\t"
      "v_readfirstlane_b32 s55, v23\n\t"
      "v_readfirstlane_b32 s56, v24\n\t"
      "v_readfirstlane_b32 s57, v25\n\t"
      "v_readfirstlane_b32 s58, v26\n\t"
      "v_readfirstlane_b32 s59, v27\n\t"
      "v_readfirstlane_b32 s60, v28\n\t"
      "v_readfirstlane_b32 s61, v29\n\t"
      "v_readfirstlane_b32 s62, v30\n\t"
      "v_readfirstlane_b32 s63, v31\n\t"
      "v_readfirstlane_b32 s75, v12\n\t"
      "v_readfirstlane_b32 s93, v14\n\t"
      "v_readfirstlane_b32 s94, v15\n\t"
      "v_cmp_ge_i32 s[76:77], v13, 0\n\t"
      "v_cmp_ge_i32 s[78:79], v13, 1\n\t"
      "v_cmp_ge_i32 s[80:81], v13, 2\n\t"
      "v_cmp_ge_i32 s[82:83], v13, 3\n\t"
      "s_nop 4\n\t"
      "v_readfirstlane_b32 s99, v37\n\t"
      "s_add_u32 s98, s53, 12582912\n\t"
      "v_lshrrev_b32 v40, 4, v248\n\t"
      "v_and_b32 v41, 15, v40\n\t"
      "v_lshrrev_b32 v40, 1, v40\n\t"
      "v_and_b32 v40, 24, v40\n\t"
      "v_sub_u32 v41, v41, v40\n\t"
      "s_mov_b32 s69, 0x3f80\n\t"
      "s_mov_b32 s70, 0x3f800000\n\t"
      "v_mov_b32 v42, 0\n\t"
      "v_cmp_eq_i32 vcc, 0, v41\n\t"
      "v_mov_b32 v43, s69\n\t"
      "s_nop 0\n\t"
      "v_cndmask_b32 v42, v42, v43\n\t"
      "v_cmp_eq_i32 vcc, 1, v41\n\t"
      "v_mov_b32 v43, s70\n\t"
      "s_nop 0\n\t"
      "v_cndmask_b32 v42, v42, v43\n\t"
      "v_accvgpr_write_b32 a224, v42\n\t"
      "v_mov_b32 v42, 0\n\t"
      "v_cmp_eq_i32 vcc, 2, v41\n\t"
      "v_mov_b32 v43, s69\n\t"
      "s_nop 0\n\t"
      "v_cndmask_b32 v42, v42, v43\n\t"
      "v_cmp_eq_i32 vcc, 3, v41\n\t"
      "v_mov_b32 v43, s70\n\t"
      "s_nop 0\n\t"
      "v_cndmask_b32 v42, v42, v43\n\t"
      "v_accvgpr_write_b32 a225, v42\n\t"
      "v_mov_b32 v42, 0\n\t"
      "v_cmp_eq_i32 vcc, 4, v41\n\t"
      "v_mov_b32 v43, s69\n\t"
      "s_nop 0\n\t"
      "v_cndmask_b32 v42, v42, v43\n\t"
      "v_cmp_eq_i32 vcc, 5, v41\n\t"
      "v_mov_b32 v43, s70\n\t"
      "s_nop 0\n\t"
      "v_cndmask_b32 v42, v42, v43\n\t"
      "v_accvgpr_write_b32 a226, v42\n\t"
      "v_mov_b32 v42, 0\n\t"
      "v_cmp_eq_i32 vcc, 6, v41\n\t"
      "v_mov_b32 v43, s69\n\t"
      "s_nop 0\n\t"
      "v_cndmask_b32 v42, v42, v43\n\t"
      "v_cmp_eq_i32 vcc, 7, v41\n\t"
      "v_mov_b32 v43, s70\n\t"
      "s_nop 0\n\t"
      "v_cndmask_b32 v42, v42, v43\n\t"
      "v_accvgpr_write_b32 a227, v42\n\t"
      "v_mov_b32 v42, 0\n\t"
      "v_cmp_eq_i32 vcc, -16, v41\n\t"
      "v_mov_b32 v43, s69\n\t"
      "s_nop 0\n\t"
      "v_cndmask_b32 v42, v42, v43\n\t"
      "v_cmp_eq_i32 vcc, -15, v41\n\t"
      "v_mov_b32 v43, s70\n\t"
      "s_nop 0\n\t"
      "v_cndmask_b32 v42, v42, v43\n\t"
      "v_accvgpr_write_b32 a228, v42\n\t"
      "v_mov_b32 v42, 0\n\t"
      "v_cmp_eq_i32 vcc, -14, v41\n\t"
      "v_mov_b32 v43, s69\n\t"
      "s_nop 0\n\t"
      "v_cndmask_b32 v42, v42, v43\n\t"
      "v_cmp_eq_i32 vcc, -13, v41\n\t"
      "v_mov_b32 v43, s70\n\t"
      "s_nop 0\n\t"
      "v_cndmask_b32 v42, v42, v43\n\t"
      "v_accvgpr_write_b32 a229, v42\n\t"
      "v_mov_b32 v42, 0\n\t"
      "v_cmp_eq_i32 vcc, -12, v41\n\t"
      "v_mov_b32 v43, s69\n\t"
      "s_nop 0\n\t"
      "v_cndmask_b32 v42, v42, v43\n\t"
      "v_cmp_eq_i32 vcc, -11, v41\n\t"
      "v_mov_b32 v43, s70\n\t"
      "s_nop 0\n\t"
      "v_cndmask_b32 v42, v42, v43\n\t"
      "v_accvgpr_write_b32 a230, v42\n\t"
      "v_mov_b32 v42, 0\n\t"
      "v_cmp_eq_i32 vcc, -10, v41\n\t"
      "v_mov_b32 v43, s69\n\t"
      "s_nop 0\n\t"
      "v_cndmask_b32 v42, v42, v43\n\t"
      "v_cmp_eq_i32 vcc, -9, v41\n\t"
      "v_mov_b32 v43, s70\n\t"
      "s_nop 0\n\t"
      "v_cndmask_b32 v42, v42, v43\n\t"
      "v_accvgpr_write_b32 a231, v42\n\t"
      "s_mov_b32 s72, 0x40000\n\t"
      "WE0%=:\n\t"
      "buffer_load_dwordx4 v[40:43], v36, s[36:39], 0 offen sc1\n\t"
      "buffer_load_dword v48, v38, s[36:39], 0 offen sc1\n\t"
      "buffer_load_dword v49, v38, s[36:39], 0 offen offset:128 sc1\n\t"
      "s_waitcnt vmcnt(0)\n\t"
      "v_bitop3_b32 v40, v40, v41, s62 bitop3:0x7e\n\t"
      "v_bitop3_b32 v48, v48, v49, s62 bitop3:0x7e\n\t"
      "v_or_b32 v40, v40, v48\n\t"
      "v_cmp_ne_u32 vcc, 0, v40\n\t"
      "s_nop 0\n\t"
      "s_cbranch_vccz WE1%=\n\t"
      "s_sleep 8\n\t"
      "s_sub_u32 s72, s72, 1\n\t"
      "s_cmp_lg_u32 s72, 0\n\t"
      "s_cbranch_scc1 WE0%=\n\t"
      "WE1%=:\n\t"
      "buffer_load_dwordx4 a[128:131], v248, s[36:39], s50 offen  sc1\n\t"
      "buffer_load_dwordx4 a[132:135], v248, s[36:39], s50 offen offset:1024  sc1\n\t"
      "buffer_load_dwordx4 a[136:139], v248, s[36:39], s50 offen offset:2048  sc1\n\t"
      "buffer_load_dwordx4 a[140:143], v248, s[36:39], s50 offen offset:3072  sc1\n\t"
      "s_add_u32 s69, s50, 4096\n\t"
      "buffer_load_dwordx4 a[144:147], v248, s[36:39], s69 offen  sc1\n\t"
      "buffer_load_dwordx4 a[148:151], v248, s[36:39], s69 offen offset:1024  sc1\n\t"
      "buffer_load_dwordx4 a[152:155], v248, s[36:39], s69 offen offset:2048  sc1\n\t"
      "buffer_load_dwordx4 a[156:159], v248, s[36:39], s69 offen offset:3072  sc1\n\t"
      "s_add_u32 s69, s50, 8192\n\t"
      "s_add_u32 m0, s61, 0\n\t"
      "s_nop 0\n\t"
      "buffer_load_dwordx4 v250, s[36:39], s69 offen sc1 lds\n\t"
      "buffer_load_dwordx4 v250, s[36:39], s69 offen offset:1024 sc1 lds\n\t"
      "buffer_load_dwordx4 v250, s[36:39], s69 offen offset:2048 sc1 lds\n\t"
      "buffer_load_dwordx4 v250, s[36:39], s69 offen offset:3072 sc1 lds\n\t"
      "s_add_u32 s69, s50, 12288\n\t"
      "s_add_u32 m0, s61, 4096\n\t"
      "s_nop 0\n\t"
      "buffer_load_dwordx4 v250, s[36:39], s69 offen sc1 lds\n\t"
      "buffer_load_dwordx4 v250, s[36:39], s69 offen offset:1024 sc1 lds\n\t"
      "buffer_load_dwordx4 v250, s[36:39], s69 offen offset:2048 sc1 lds\n\t"
      "buffer_load_dwordx4 v250, s[36:39], s69 offen offset:3072 sc1 lds\n\t"
      "s_add_u32 s69, s50, 16384\n\t"
      "buffer_load_dwordx4 a[160:163], v248, s[36:39], s69 offen  sc1\n\t"
      "buffer_load_dwordx4 a[164:167], v248, s[36:39], s69 offen offset:1024  sc1\n\t"
      "buffer_load_dwordx4 a[168:171], v248, s[36:39], s69 offen offset:2048  sc1\n\t"
      "buffer_load_dwordx4 a[172:175], v248, s[36:39], s69 offen offset:3072  sc1\n\t"
      "s_add_u32 s69, s50, 20480\n\t"
      "buffer_load_dwordx4 a[176:179], v248, s[36:39], s69 offen  sc1\n\t"
      "buffer_load_dwordx4 a[180:183], v248, s[36:39], s69 offen offset:1024  sc1\n\t"
      "buffer_load_dwordx4 a[184:187], v248, s[36:39], s69 offen offset:2048  sc1\n\t"
      "buffer_load_dwordx4 a[188:191], v248, s[36:39], s69 offen offset:3072  sc1\n\t"
      "s_add_u32 s50, s50, 3145728\n\t"
      "s_mov_b32 s64, 0\n\t"
      "s_mov_b32 s65, 0\n\t"
      "s_mov_b32 s66, 0\n\t"
      "s_mov_b32 s96, 0\n\t"
      "s_mov_b32 s97, 0\n\t"
      "s_add_u32 s89, s49, 16\n\t"
      "s_sub_u32 s95, s94, s60\n\t"
      "s_lshl_b32 s95, s95, 10\n\t"
      "s_add_u32 s95, s95, s54\n\t"
      "s_mov_b32 s67, -1\n\t"
      "s_mov_b32 s74, 0xffff0000\n\t"
      "s_mov_b32 s84, 0x3db504f3\n\t"
      "s_mov_b32 s86, 0x3e0293ee\n\t"
      "s_mov_b32 s85, 0x3db504f3\n\t"
      "s_mov_b32 s87, 0x3e0293ee\n\t"
      "v_mov_b32 v239, 0xff61b1e6\n\t"
      "s_waitcnt vmcnt(0) lgkmcnt(0)\n\t"
      "s_branch WA%=\n\t"
      ".p2align 5\n\t"
      "WTOP%=:\n\t"
      "s_cmp_ge_u32 s48, 64\n\t"
      "s_cbranch_scc1 WFIN%=\n\t"
      "s_lshr_b32 s69, s48, 2\n\t"
      "s_cmp_eq_u32 s69, 5\n\t"
      "s_cbranch_scc1 WH2%=\n\t"
      "WT0%=:\n\t"
      "s_waitcnt vmcnt(17)\n\t"
      "s_cmp_eq_u32 s24, 9\n\t"
      "s_cbranch_scc1 WT1%=\n\t"
      "s_waitcnt vmcnt(10)\n\t"
      "s_cmp_lg_u32 s97, 0\n\t"
      "s_cbranch_scc1 WT1%=\n\t"
      "s_waitcnt vmcnt(9)\n\t"
      "s_cmp_eq_u32 s24, 8\n\t"
      "s_cbranch_scc1 WT1%=\n\t"
      "s_waitcnt vmcnt(2)\n\t"
      "WT1%=:\n\t"
      "v_min_u32 v228, v228, v229\n\t"
      "v_min3_u32 v228, v228, v230, v231\n\t"
      "v_min_u32 v232, v232, v233\n\t"
      "s_cmp_lg_u32 s65, 0\n\t"
      "s_cselect_b32 s70, s89, s49\n\t"
      "v_readfirstlane_b32 s88, v228\n\t"
      "s_memrealtime s[90:91]\n\t"
      "v_cmp_ge_u32 vcc, s70, v232\n\t"
      "s_lshr_b32 s69, s88, 16\n\t"
      "s_cmp_gt_u32 s69, s48\n\t"
      "s_cselect_b32 s64, 1, 0\n\t"
      "s_cmp_eq_u64 vcc, 0\n\t"
      "s_cselect_b32 s66, 1, 0\n\t"
      "s_cmp_eq_u32 s65, 2\n\t"
      "s_cselect_b32 s66, 0, s66\n\t"
      "s_add_u32 s70, s48, 4\n\t"
      "s_cmp_gt_u32 s69, s70\n\t"
      "s_cselect_b32 s96, 1, 0\n\t"
      "s_or_b32 s64, s64, s97\n\t"
      "s_cmp_lg_u32 s97, 0\n\t"
      "s_cbranch_scc1 WA%=\n\t"
      "s_cmp_lg_u32 s64, 0\n\t"
      "s_cbranch_scc1 WT2%=\n\t"
      "s_sub_u32 s69, s48, s69\n\t"
      "s_mul_i32 s69, s69, 170\n\t"
      "s_waitcnt lgkmcnt(0)\n\t"
      "s_sub_u32 s70, s90, s88\n\t"
      "s_add_u32 s70, s70, 200\n\t"
      "s_and_b32 s70, s70, 0xffff\n\t"
      "s_add_u32 s69, s69, 60\n\t"
      "s_cmp_ge_u32 s70, s69\n\t"
      "s_cselect_b32 s69, s48, 0\n\t"
      "s_cmp_ge_u32 s69, 40\n\t"
      "s_cselect_b32 s64, 2, 0\n\t"
      "s_cmp_eq_u32 s64, 0\n\t"
      "s_cbranch_scc1 WA%=\n\t"
      "WT2%=:\n\t"
      "buffer_load_dwordx4 v[128:131], v252, s[36:39], s51 offen sc1\n\t"
      "buffer_load_dwordx4 v[132:135], v252, s[36:39], s51 offen offset:512 sc1\n\t"
      "buffer_load_dwordx4 v[136:139], v252, s[36:39], s51 offen offset:1024 sc1\n\t"
      "buffer_load_dwordx4 v[140:143], v252, s[36:39], s51 offen offset:1536 sc1\n\t"
      "buffer_load_dwordx4 v[144:147], v253, s[36:39], s52 offen sc1\n\t"
      "buffer_load_dwordx4 v[148:151], v253, s[36:39], s52 offen offset:512 sc1\n\t"
      "buffer_load_dwordx4 v[152:155], v253, s[36:39], s52 offen offset:1024 sc1\n\t"
      "buffer_load_dwordx4 v[156:159], v253, s[36:39], s52 offen offset:1536 sc1\n\t"
      "WA%=:\n\t"
      "v_mfma_f32_16x16x32_bf16 v[0:3], a[128:131], a[160:163], 0\n\t"
      "ds_read_b64_tr_b16 a[192:193], v251\n\t"
      "ds_read_b64_tr_b16 a[194:195], v251 offset:1024\n\t"
      "v_mfma_f32_16x16x32_bf16 v[4:7], a[128:131], a[164:167], 0\n\t"
      "ds_read_b64_tr_b16 a[196:197], v251 offset:8\n\t"
      "ds_read_b64_tr_b16 a[198:199], v251 offset:1032\n\t"
      "v_mfma_f32_16x16x32_bf16 v[8:11], a[132:135], a[164:167], 0\n\t"
      "ds_read_b64_tr_b16 a[200:201], v251 offset:2048\n\t"
      "ds_read_b64_tr_b16 a[202:203], v251 offset:3072\n\t"
      "v_mfma_f32_16x16x32_bf16 v[0:3], a[136:139], a[168:171], v[0:3]\n\t"
      "ds_read_b64_tr_b16 a[204:205], v251 offset:2056\n\t"
      "ds_read_b64_tr_b16 a[206:207], v251 offset:3080\n\t"
      "v_mfma_f32_16x16x32_bf16 v[4:7], a[136:139], a[172:175], v[4:7]\n\t"
      "v_mfma_f32_16x16x32_bf16 v[8:11], a[140:143], a[172:175], v[8:11]\n\t"
      "s_waitcnt lgkmcnt(0)\n\t"
      "ds_read_b64_tr_b16 a[208:209], v251 offset:4096\n\t"
      "ds_read_b64_tr_b16 a[210:211], v251 offset:5120\n\t"
      "ds_read_b64_tr_b16 a[212:213], v251 offset:4104\n\t"
      "ds_read_b64_tr_b16 a[214:215], v251 offset:5128\n\t"
      "ds_read_b64_tr_b16 a[216:217], v251 offset:6144\n\t"
      "ds_read_b64_tr_b16 a[218:219], v251 offset:7168\n\t"
      "ds_read_b64_tr_b16 a[220:221], v251 offset:6152\n\t"
      "ds_read_b64_tr_b16 a[222:223], v251 offset:7176\n\t"
      "v_mfma_f32_16x16x32_bf16 v[0:3], a[144:147], a[176:179], v[0:3]\n\t"
      "v_mfma_f32_16x16x32_bf16 v[4:7], a[144:147], a[180:183], v[4:7]\n\t"
      "v_mfma_f32_16x16x32_bf16 v[8:11], a[148:151], a[180:183], v[8:11]\n\t"
      "v_mfma_f32_16x16x32_bf16 v[0:3], a[152:155], a[184:187], v[0:3]\n\t"
      "v_mfma_f32_16x16x32_bf16 v[4:7], a[152:155], a[188:191], v[4:7]\n\t"
      "v_mfma_f32_16x16x32_bf16 v[8:11], a[156:159], a[188:191], v[8:11]\n\t"
      "s_waitcnt lgkmcnt(0)\n\t"
      "buffer_load_dwordx4 a[128:131], v248, s[36:39], s50 offen  sc1\n\t"
      "s_nop 3\n\t"
      "v_cndmask_b32_e64 v0, v239, v0, s[76:77]\n\t"
      "s_nop 0\n\t"
      "v_cndmask_b32_e64 v8, v239, v8, s[76:77]\n\t"
      "v_cndmask_b32_e64 v1, v239, v1, s[78:79]\n\t"
      "v_cndmask_b32_e64 v9, v239, v9, s[78:79]\n\t"
      "v_cndmask_b32_e64 v2, v239, v2, s[80:81]\n\t"
      "v_cndmask_b32_e64 v10, v239, v10, s[80:81]\n\t"
      "v_cndmask_b32_e64 v3, v239, v3, s[82:83]\n\t"
      "v_cndmask_b32_e64 v11, v239, v11, s[82:83]\n\t"
      "v_max3_f32 v234, v0, v1, v2\n\t"
      "v_max3_f32 v235, v4, v5, v6\n\t"
      "v_max_f32 v234, v234, v3\n\t"
      "v_max3_f32 v235, v235, v7, v8\n\t"
      "v_max3_f32 v235, v235, v9, v10\n\t"
      "v_max_f32 v235, v235, v11\n\t"
      "s_nop 1\n\t"
      "v_permlane16_swap_b32 v234, v235\n\t"
      "v_max_f32 v234, v234, v235\n\t"
      "buffer_load_dwordx4 a[132:135], v248, s[36:39], s50 offen offset:1024  sc1\n\t"
      "v_mov_b32 v235, v234\n\t"
      "s_nop 1\n\t"
      "v_permlane32_swap_b32 v234, v235\n\t"
      "v_max_f32 v234, v234, v235\n\t"
      "v_mov_b32 v235, v234\n\t"
      "s_nop 1\n\t"
      "v_permlane16_swap_b32 v234, v235\n\t"
      "v_pk_mul_f32 v[12:13], v[234:235], s[86:87]\n\t"
      "v_pk_fma_f32 v[0:1], v[0:1], s[86:87], v[12:13] op_sel:[0,0,0] op_sel_hi:[1,1,0] neg_lo:[0,0,1] neg_hi:[0,0,1]\n\t"
      "v_pk_fma_f32 v[2:3], v[2:3], s[86:87], v[12:13] op_sel:[0,0,0] op_sel_hi:[1,1,0] neg_lo:[0,0,1] neg_hi:[0,0,1]\n\t"
      "v_pk_fma_f32 v[4:5], v[4:5], s[86:87], v[12:13] op_sel:[0,0,1] op_sel_hi:[1,1,1] neg_lo:[0,0,1] neg_hi:[0,0,1]\n\t"
      "v_pk_fma_f32 v[6:7], v[6:7], s[86:87], v[12:13] op_sel:[0,0,1] op_sel_hi:[1,1,1] neg_lo:[0,0,1] neg_hi:[0,0,1]\n\t"
      "v_pk_fma_f32 v[8:9], v[8:9], s[86:87], v[12:13] op_sel:[0,0,1] op_sel_hi:[1,1,1] neg_lo:[0,0,1] neg_hi:[0,0,1]\n\t"
      "v_pk_fma_f32 v[10:11], v[10:11], s[86:87], v[12:13] op_sel:[0,0,1] op_sel_hi:[1,1,1] neg_lo:[0,0,1] neg_hi:[0,0,1]\n\t"
      "v_exp_f32 v0, v0\n\t"
      "v_exp_f32 v1, v1\n\t"
      "buffer_load_dwordx4 a[136:139], v248, s[36:39], s50 offen offset:2048  sc1\n\t"
      "v_exp_f32 v2, v2\n\t"
      "v_exp_f32 v3, v3\n\t"
      "v_exp_f32 v4, v4\n\t"
      "v_exp_f32 v5, v5\n\t"
      "v_exp_f32 v6, v6\n\t"
      "v_exp_f32 v7, v7\n\t"
      "v_exp_f32 v8, v8\n\t"
      "v_exp_f32 v9, v9\n\t"
      "buffer_load_dwordx4 a[140:143], v248, s[36:39], s50 offen offset:3072  sc1\n\t"
      "v_exp_f32 v10, v10\n\t"
      "v_exp_f32 v11, v11\n\t"
      "v_mov_b64 v[18:19], 0\n\t"
      "v_cvt_pk_bf16_f32 v16, v0, v1\n\t"
      "v_cvt_pk_bf16_f32 v17, v2, v3\n\t"
      "v_cvt_pk_bf16_f32 v20, v4, v5\n\t"
      "v_cvt_pk_bf16_f32 v21, v6, v7\n\t"
      "v_cvt_pk_bf16_f32 v22, v8, v9\n\t"
      "v_cvt_pk_bf16_f32 v23, v10, v11\n\t"
      "v_mfma_f32_16x16x32_bf16 v[64:67], a[192:195], v[16:19], 0\n\t"
      "v_pk_add_f32 v[0:1], v[0:1], v[2:3]\n\t"
      "s_add_u32 s69, s50, 4096\n\t"
      "buffer_load_dwordx4 a[144:147], v248, s[36:39], s69 offen  sc1\n\t"
      "v_mfma_f32_16x16x32_bf16 v[96:99], a[192:195], v[20:23], 0\n\t"
      "v_pk_add_f32 v[4:5], v[4:5], v[6:7]\n\t"
      "v_mfma_f32_16x16x32_bf16 v[68:71], a[196:199], v[16:19], 0\n\t"
      "v_pk_add_f32 v[8:9], v[8:9], v[10:11]\n\t"
      "v_mfma_f32_16x16x32_bf16 v[100:103], a[196:199], v[20:23], 0\n\t"
      "v_add_f32 v236, v0, v1\n\t"
      "v_mfma_f32_16x16x32_bf16 v[72:75], a[200:203], v[16:19], 0\n\t"
      "buffer_load_dwordx4 a[148:151], v248, s[36:39], s69 offen offset:1024  sc1\n\t"
      "v_pk_add_f32 v[4:5], v[4:5], v[8:9]\n\t"
      "v_mfma_f32_16x16x32_bf16 v[104:107], a[200:203], v[20:23], 0\n\t"
      "v_add_f32 v237, v4, v5\n\t"
      "v_mfma_f32_16x16x32_bf16 v[76:79], a[204:207], v[16:19], 0\n\t"
      "v_mfma_f32_16x16x32_bf16 v[108:111], a[204:207], v[20:23], 0\n\t"
      "buffer_load_dwordx4 a[152:155], v248, s[36:39], s69 offen offset:2048  sc1\n\t"
      "v_mfma_f32_16x16x32_bf16 v[80:83], a[208:211], v[16:19], 0\n\t"
      "v_mfma_f32_16x16x32_bf16 v[112:115], a[208:211], v[20:23], 0\n\t"
      "v_mfma_f32_16x16x32_bf16 v[84:87], a[212:215], v[16:19], 0\n\t"
      "v_mfma_f32_16x16x32_bf16 v[116:119], a[212:215], v[20:23], 0\n\t"
      "buffer_load_dwordx4 a[156:159], v248, s[36:39], s69 offen offset:3072  sc1\n\t"
      "v_mfma_f32_16x16x32_bf16 v[88:91], a[216:219], v[16:19], 0\n\t"
      "v_mfma_f32_16x16x32_bf16 v[120:123], a[216:219], v[20:23], 0\n\t"
      "v_mfma_f32_16x16x32_bf16 v[92:95], a[220:223], v[16:19], 0\n\t"
      "v_mfma_f32_16x16x32_bf16 v[124:127], a[220:223], v[20:23], 0\n\t"
      "s_waitcnt vmcnt(8)\n\t"
      "s_cmp_lt_u32 s48, 4\n\t"
      "s_cbranch_scc1 WGO%=\n\t"
      "v_mov_b32 v0, s48\n\t"
      "s_mov_b64 exec, 1\n\t"
      "buffer_store_dword v0, off, s[36:39], s63 sc1\n\t"
      "s_mov_b64 exec, -1\n\t"
      "s_cmp_lt_i32 s67, 0\n\t"
      "s_cbranch_scc1 WPB1%=\n\t"
      "v_mov_b32 v8, s68\n\t"
      "s_mov_b64 exec, 1\n\t"
      "buffer_store_dword v8, off, s[36:39], s67 sc1\n\t"
      "s_mov_b64 exec, -1\n\t"
      "s_mov_b32 s67, -1\n\t"
      "WPB1%=:\n\t"
      "WGO%=:\n\t"
      "s_cmp_eq_u32 s64, 1\n\t"
      "s_cbranch_scc1 WKV%=\n\t"
      "s_cmp_lg_u32 s65, 0\n\t"
      "s_cbranch_scc1 WR2%=\n\t"
      "WGO2%=:\n\t"
      "s_cmp_lt_u32 s48, 40\n\t"
      "s_cselect_b32 s71, 1, -1\n\t"
      "s_add_u32 s71, s71, s48\n\t"
      "s_mov_b32 s72, 0x40000\n\t"
      "WLOOP%=:\n\t"
      "s_cmp_ge_u32 s49, 48\n\t"
      "s_cselect_b32 s69, s49, 99\n\t"
      "s_cmp_lt_u32 s69, s48\n\t"
      "s_cbranch_scc1 WRW%=\n\t"
      "s_cmp_eq_u32 s64, 2\n\t"
      "s_cbranch_scc1 WKC%=\n\t"
      "v_lshlrev_b32 v6, 4, v248\n\t"
      "v_mov_b64 v[4:5], 0\n\t"
      "buffer_load_dwordx4 v[0:3], off, s[36:39], s56 sc1\n\t"
      "s_cmp_ge_u32 s49, s48\n\t"
      "s_cbranch_scc1 WL1%=\n\t"
      "buffer_load_dword v4, v6, s[36:39], s55 offen sc1\n\t"
      "buffer_load_dword v5, v6, s[36:39], s55 offen offset:128 sc1\n\t"
      "WL1%=:\n\t"
      "s_waitcnt vmcnt(0)\n\t"
      "s_cmp_lt_i32 s67, 0\n\t"
      "s_cbranch_scc1 WL2%=\n\t"
      "v_mov_b32 v8, s68\n\t"
      "s_mov_b64 exec, 1\n\t"
      "buffer_store_dword v8, off, s[36:39], s67 sc1\n\t"
      "s_mov_b64 exec, -1\n\t"
      "s_mov_b32 s67, -1\n\t"
      "WL2%=:\n\t"
      "v_min_u32 v0, v0, v1\n\t"
      "v_min3_u32 v0, v0, v2, v3\n\t"
      "v_min_u32 v4, v4, v5\n\t"
      "v_readfirstlane_b32 s88, v0\n\t"
      "v_cmp_ge_u32 vcc, s49, v4\n\t"
      "s_lshr_b32 s69, s88, 16\n\t"
      "s_max_u32 s70, s71, s48\n\t"
      "s_cmp_ge_u32 s69, s70\n\t"
      "s_cbranch_scc1 WKL%=\n\t"
      "s_cmp_ge_u32 s69, s71\n\t"
      "s_cbranch_scc1 WKF%=\n\t"
      "s_cbranch_vccz WRW%=\n\t"
      "s_sleep 6\n\t"
      "s_cmp_lt_u32 s49, s48\n\t"
      "s_cbranch_scc1 WKM%=\n\t"
      "s_sub_u32 s69, s71, s69\n\t"
      "s_min_u32 s69, s69, 9\n\t"
      "WKN%=:\n\t"
      "s_sub_u32 s69, s69, 1\n\t"
      "s_cmp_eq_u32 s69, 0\n\t"
      "s_cbranch_scc1 WKM%=\n\t"
      "s_sleep 48\n\t"
      "s_branch WKN%=\n\t"
      "WKM%=:\n\t"
      "s_sub_u32 s72, s72, 1\n\t"
      "s_cmp_gt_i32 s72, 0\n\t"
      "s_cbranch_scc1 WLOOP%=\n\t"
      "s_branch WKL%=\n\t"
      ".p2align 5\n\t"
      "WFIN%=:\n\t"
      "s_mov_b32 s64, 0\n\t"
      "s_mov_b32 s72, 0x40000\n\t"
      "s_waitcnt vmcnt(2)\n\t"
      "WFIN1%=:\n\t"
      "s_cmp_lt_i32 s67, 0\n\t"
      "s_cbranch_scc1 WF1%=\n\t"
      "v_mov_b32 v8, s68\n\t"
      "s_mov_b64 exec, 1\n\t"
      "buffer_store_dword v8, off, s[36:39], s67 sc1\n\t"
      "s_mov_b64 exec, -1\n\t"
      "s_mov_b32 s67, -1\n\t"
      "WF1%=:\n\t"
      "s_cmp_lg_u32 s65, 0\n\t"
      "s_cbranch_scc1 WRC%=\n\t"
      "s_cmp_ge_u32 s49, 64\n\t"
      "s_cbranch_scc1 WEND%=\n\t"
      "s_cmp_ge_u32 s49, 48\n\t"
      "s_cbranch_scc1 WRS%=\n\t"
      "v_lshlrev_b32 v6, 4, v248\n\t"
      "WRF%=:\n\t"
      "buffer_load_dword v4, v6, s[36:39], s55 offen sc1\n\t"
      "buffer_load_dword v5, v6, s[36:39], s55 offen offset:128 sc1\n\t"
      "s_waitcnt vmcnt(0)\n\t"
      "v_min_u32 v4, v4, v5\n\t"
      "v_cmp_ge_u32 vcc, s49, v4\n\t"
      "s_nop 0\n\t"
      "s_cbranch_vccz WRW%=\n\t"
      "s_sleep 8\n\t"
      "s_sub_u32 s72, s72, 1\n\t"
      "s_cmp_gt_i32 s72, 0\n\t"
      "s_cbranch_scc1 WRF%=\n\t"
      "WRS%=:\n\t"
      "s_waitcnt vmcnt(0)\n\t"
      "WRW%=:\n\t"
      "s_lshl_b32 s70, s49, 5\n\t"
      "s_add_u32 s70, s70, s60\n\t"
      "s_lshl_b32 s70, s70, 10\n\t"
      "buffer_load_dwordx2 v[224:225], v249, s[40:43], s70 offen \n\t"
      "buffer_load_dwordx2 v[226:227], v249, s[40:43], s70 offen offset:512 \n\t"
      "WRL%=:\n\t"
      "s_lshl_b32 s69, s49, 18\n\t"
      "s_add_u32 s69, s69, s54\n\t"
      "buffer_load_dwordx4 v[160:163], v248, s[36:39], s69 offen sc1\n\t"
      "s_add_u32 s69, s69, 0x8000\n\t"
      "buffer_load_dwordx4 v[164:167], v248, s[36:39], s69 offen sc1\n\t"
      "s_add_u32 s69, s69, 0x8000\n\t"
      "buffer_load_dwordx4 v[168:171], v248, s[36:39], s69 offen sc1\n\t"
      "s_add_u32 s69, s69, 0x8000\n\t"
      "buffer_load_dwordx4 v[172:175], v248, s[36:39], s69 offen sc1\n\t"
      "s_add_u32 s69, s69, 0x8000\n\t"
      "buffer_load_dwordx4 v[176:179], v248, s[36:39], s69 offen sc1\n\t"
      "s_add_u32 s69, s69, 0x8000\n\t"
      "buffer_load_dwordx4 v[180:183], v248, s[36:39], s69 offen sc1\n\t"
      "s_add_u32 s69, s69, 0x8000\n\t"
      "buffer_load_dwordx4 v[184:187], v248, s[36:39], s69 offen sc1\n\t"
      "s_add_u32 s69, s69, 0x8000\n\t"
      "buffer_load_dwordx4 v[188:191], v248, s[36:39], s69 offen sc1\n\t"
      "s_waitcnt vmcnt(0)\n\t"
      "s_cmp_lt_i32 s67, 0\n\t"
      "s_cbranch_scc1 WF2%=\n\t"
      "v_mov_b32 v8, s68\n\t"
      "s_mov_b64 exec, 1\n\t"
      "buffer_store_dword v8, off, s[36:39], s67 sc1\n\t"
      "s_mov_b64 exec, -1\n\t"
      "s_mov_b32 s67, -1\n\t"
      "WF2%=:\n\t"
      "WRC%=:\n\t"
      "v_mfma_f32_16x16x32_bf16 v[16:19], a[224:227], v[160:163], 0\n\t"
      "v_mfma_f32_16x16x32_bf16 v[20:23], a[228:231], v[160:163], 0\n\t"
      "v_and_b32 v193, s74, v224\n\t"
      "v_lshlrev_b32 v192, 16, v224\n\t"
      "v_mfma_f32_16x16x32_bf16 v[16:19], a[224:227], v[164:167], v[16:19]\n\t"
      "v_mfma_f32_16x16x32_bf16 v[20:23], a[228:231], v[164:167], v[20:23]\n\t"
      "v_and_b32 v195, s74, v225\n\t"
      "v_lshlrev_b32 v194, 16, v225\n\t"
      "v_mfma_f32_16x16x32_bf16 v[16:19], a[224:227], v[168:171], v[16:19]\n\t"
      "v_mfma_f32_16x16x32_bf16 v[20:23], a[228:231], v[168:171], v[20:23]\n\t"
      "v_and_b32 v197, s74, v226\n\t"
      "v_lshlrev_b32 v196, 16, v226\n\t"
      "v_mfma_f32_16x16x32_bf16 v[16:19], a[224:227], v[172:175], v[16:19]\n\t"
      "v_mfma_f32_16x16x32_bf16 v[20:23], a[228:231], v[172:175], v[20:23]\n\t"
      "v_and_b32 v199, s74, v227\n\t"
      "v_lshlrev_b32 v198, 16, v227\n\t"
      "v_mfma_f32_16x16x32_bf16 v[16:19], a[224:227], v[176:179], v[16:19]\n\t"
      "v_mfma_f32_16x16x32_bf16 v[20:23], a[228:231], v[176:179], v[20:23]\n\t"
      "v_mfma_f32_16x16x32_bf16 v[16:19], a[224:227], v[180:183], v[16:19]\n\t"
      "v_mfma_f32_16x16x32_bf16 v[20:23], a[228:231], v[180:183], v[20:23]\n\t"
      "v_mfma_f32_16x16x32_bf16 v[16:19], a[224:227], v[184:187], v[16:19]\n\t"
      "v_mfma_f32_16x16x32_bf16 v[20:23], a[228:231], v[184:187], v[20:23]\n\t"
      "v_mfma_f32_16x16x32_bf16 v[16:19], a[224:227], v[188:191], v[16:19]\n\t"
      "v_mfma_f32_16x16x32_bf16 v[20:23], a[228:231], v[188:191], v[20:23]\n\t"
      "s_nop 6\n\t"
      "v_cvt_pk_bf16_f32 v32, v16, v17\n\t"
      "v_cvt_pk_bf16_f32 v34, v18, v19\n\t"
      "v_cvt_pk_bf16_f32 v36, v20, v21\n\t"
      "v_cvt_pk_bf16_f32 v38, v22, v23\n\t"
      "v_and_b32 v33, s74, v32\n\t"
      "v_lshlrev_b32 v32, 16, v32\n\t"
      "v_and_b32 v35, s74, v34\n\t"
      "v_lshlrev_b32 v34, 16, v34\n\t"
      "v_and_b32 v37, s74, v36\n\t"
      "v_lshlrev_b32 v36, 16, v36\n\t"
      "v_and_b32 v39, s74, v38\n\t"
      "v_lshlrev_b32 v38, 16, v38\n\t"
      "v_pk_add_f32 v[192:193], v[192:193], v[32:33]\n\t"
      "v_pk_add_f32 v[194:195], v[194:195], v[34:35]\n\t"
      "v_pk_add_f32 v[196:197], v[196:197], v[36:37]\n\t"
      "v_pk_add_f32 v[198:199], v[198:199], v[38:39]\n\t"
      "v_cvt_pk_bf16_f32 v200, v192, v193\n\t"
      "v_cvt_pk_bf16_f32 v201, v194, v195\n\t"
      "v_cvt_pk_bf16_f32 v202, v196, v197\n\t"
      "v_cvt_pk_bf16_f32 v203, v198, v199\n\t"
      "v_and_b32 v193, s74, v200\n\t"
      "v_lshlrev_b32 v192, 16, v200\n\t"
      "v_and_b32 v195, s74, v201\n\t"
      "v_lshlrev_b32 v194, 16, v201\n\t"
      "v_and_b32 v197, s74, v202\n\t"
      "v_lshlrev_b32 v196, 16, v202\n\t"
      "v_and_b32 v199, s74, v203\n\t"
      "v_lshlrev_b32 v198, 16, v203\n\t"
      "v_pk_add_f32 v[32:33], v[192:193], v[196:197]\n\t"
      "v_pk_add_f32 v[34:35], v[194:195], v[198:199]\n\t"
      "v_pk_add_f32 v[32:33], v[32:33], v[34:35]\n\t"
      "v_add_f32 v32, v32, v33\n\t"
      "s_nop 1\n\t"
      "v_add_f32_dpp v32, v32, v32 quad_perm:[1,0,3,2] row_mask:0xf bank_mask:0xf\n\t"
      "s_nop 1\n\t"
      "v_add_f32_dpp v32, v32, v32 quad_perm:[2,3,0,1] row_mask:0xf bank_mask:0xf\n\t"
      "s_nop 1\n\t"
      "v_add_f32_dpp v32, v32, v32 row_ror:4 row_mask:0xf bank_mask:0xf\n\t"
      "s_nop 1\n\t"
      "v_add_f32_dpp v32, v32, v32 row_ror:8 row_mask:0xf bank_mask:0xf\n\t"
      "v_mov_b32 v33, v32\n\t"
      "s_nop 1\n\t"
      "v_permlane16_swap_b32 v32, v33\n\t"
      "v_add_f32 v32, v32, v33\n\t"
      "v_mov_b32 v33, v32\n\t"
      "s_nop 1\n\t"
      "v_permlane32_swap_b32 v32, v33\n\t"
      "v_add_f32 v32, v32, v33\n\t"
      "v_cmp_u_f32 vcc, v32, v32\n\t"
      "v_mul_f32 v204, 0x3b000000, v32\n\t"
      "s_cbranch_vccnz WRN%=\n\t"
      "WRD%=:\n\t"
      "v_pk_add_f32 v[40:41], v[192:193], v[204:205] op_sel_hi:[1,0] neg_lo:[0,1] neg_hi:[0,1]\n\t"
      "v_pk_add_f32 v[42:43], v[194:195], v[204:205] op_sel_hi:[1,0] neg_lo:[0,1] neg_hi:[0,1]\n\t"
      "v_pk_add_f32 v[44:45], v[196:197], v[204:205] op_sel_hi:[1,0] neg_lo:[0,1] neg_hi:[0,1]\n\t"
      "v_pk_add_f32 v[46:47], v[198:199], v[204:205] op_sel_hi:[1,0] neg_lo:[0,1] neg_hi:[0,1]\n\t"
      "v_pk_mul_f32 v[32:33], v[40:41], v[40:41]\n\t"
      "v_pk_fma_f32 v[32:33], v[42:43], v[42:43], v[32:33]\n\t"
      "v_pk_fma_f32 v[32:33], v[44:45], v[44:45], v[32:33]\n\t"
      "v_pk_fma_f32 v[32:33], v[46:47], v[46:47], v[32:33]\n\t"
      "v_add_f32 v32, v32, v33\n\t"
      "s_nop 1\n\t"
      "v_add_f32_dpp v32, v32, v32 quad_perm:[1,0,3,2] row_mask:0xf bank_mask:0xf\n\t"
      "s_nop 1\n\t"
      "v_add_f32_dpp v32, v32, v32 quad_perm:[2,3,0,1] row_mask:0xf bank_mask:0xf\n\t"
      "s_nop 1\n\t"
      "v_add_f32_dpp v32, v32, v32 row_ror:4 row_mask:0xf bank_mask:0xf\n\t"
      "s_nop 1\n\t"
      "v_add_f32_dpp v32, v32, v32 row_ror:8 row_mask:0xf bank_mask:0xf\n\t"
      "v_mov_b32 v33, v32\n\t"
      "s_nop 1\n\t"
      "v_permlane16_swap_b32 v32, v33\n\t"
      "v_add_f32 v32, v32, v33\n\t"
      "v_mov_b32 v33, v32\n\t"
      "s_nop 1\n\t"
      "v_permlane32_swap_b32 v32, v33\n\t"
      "v_add_f32 v32, v32, v33\n\t"
      "v_mul_f32 v205, 0x3b000000, v32\n\t"
      "v_cvt_pk_bf16_f32 v204, v204, v205\n\t"
      "v_and_b32 v205, s74, v204\n\t"
      "v_lshlrev_b32 v204, 16, v204\n\t"
      "v_add_f32 v205, 0x3727c5ac, v205\n\t"
      "v_pk_add_f32 v[40:41], v[192:193], v[204:205] op_sel_hi:[1,0] neg_lo:[0,1] neg_hi:[0,1]\n\t"
      "v_pk_add_f32 v[42:43], v[194:195], v[204:205] op_sel_hi:[1,0] neg_lo:[0,1] neg_hi:[0,1]\n\t"
      "v_pk_add_f32 v[44:45], v[196:197], v[204:205] op_sel_hi:[1,0] neg_lo:[0,1] neg_hi:[0,1]\n\t"
      "v_pk_add_f32 v[46:47], v[198:199], v[204:205] op_sel_hi:[1,0] neg_lo:[0,1] neg_hi:[0,1]\n\t"
      "v_cvt_pk_bf16_f32 v205, v205, v205\n\t"
      "v_and_b32 v205, s74, v205\n\t"
      "v_sqrt_f32 v205, v205\n\t"
      "v_cvt_pk_bf16_f32 v40, v40, v41\n\t"
      "v_cvt_pk_bf16_f32 v42, v42, v43\n\t"
      "v_cvt_pk_bf16_f32 v44, v44, v45\n\t"
      "v_cvt_pk_bf16_f32 v46, v46, v47\n\t"
      "v_cvt_pk_bf16_f32 v205, v205, v205\n\t"
      "v_and_b32 v205, s74, v205\n\t"
      "v_rcp_f32 v204, v205\n\t"
      "v_and_b32 v41, s74, v40\n\t"
      "v_lshlrev_b32 v40, 16, v40\n\t"
      "v_and_b32 v43, s74, v42\n\t"
      "v_lshlrev_b32 v42, 16, v42\n\t"
      "v_and_b32 v45, s74, v44\n\t"
      "v_lshlrev_b32 v44, 16, v44\n\t"
      "v_and_b32 v47, s74, v46\n\t"
      "v_lshlrev_b32 v46, 16, v46\n\t"
      "v_pk_mul_f32 v[40:41], v[40:41], v[204:205] op_sel_hi:[1,0]\n\t"
      "v_pk_mul_f32 v[42:43], v[42:43], v[204:205] op_sel_hi:[1,0]\n\t"
      "v_pk_mul_f32 v[44:45], v[44:45], v[204:205] op_sel_hi:[1,0]\n\t"
      "v_pk_mul_f32 v[46:47], v[46:47], v[204:205] op_sel_hi:[1,0]\n\t"
      "v_cvt_pk_bf16_f32 v52, v40, v41\n\t"
      "v_cvt_pk_bf16_f32 v53, v42, v43\n\t"
      "v_cvt_pk_bf16_f32 v54, v44, v45\n\t"
      "v_cvt_pk_bf16_f32 v55, v46, v47\n\t"
      "v_and_b32 v33, s74, v52\n\t"
      "v_lshlrev_b32 v32, 16, v52\n\t"
      "v_and_b32 v35, s74, v53\n\t"
      "v_lshlrev_b32 v34, 16, v53\n\t"
      "v_and_b32 v37, s74, v54\n\t"
      "v_lshlrev_b32 v36, 16, v54\n\t"
      "v_and_b32 v39, s74, v55\n\t"
      "v_lshlrev_b32 v38, 16, v55\n\t"
      "v_pk_mul_f32 v[40:41], v[32:33], v[240:241]\n\t"
      "v_pk_fma_f32 v[40:41], v[34:35], v[242:243], v[40:41]\n\t"
      "v_pk_fma_f32 v[40:41], v[36:37], v[244:245], v[40:41]\n\t"
      "v_pk_fma_f32 v[40:41], v[38:39], v[246:247], v[40:41]\n\t"
      "v_add_f32 v40, v40, v41\n\t"
      "s_lshl_b32 s73, s49, 5\n\t"
      "s_add_u32 s73, s73, s60\n\t"
      "s_lshl_b32 s69, s73, 2\n\t"
      "s_add_u32 s69, s69, s57\n\t"
      "s_lshl_b32 s70, s73, 7\n\t"
      "s_add_u32 s70, s70, s57\n\t"
      "s_sub_u32 s70, s70, 212992\n\t"
      "s_cmp_ge_u32 s49, 56\n\t"
      "s_cselect_b32 s70, s70, s69\n\t"
      "v_add_f32_dpp v40, v40, v40 quad_perm:[1,0,3,2] row_mask:0xf bank_mask:0xf\n\t"
      "s_nop 1\n\t"
      "v_add_f32_dpp v40, v40, v40 quad_perm:[2,3,0,1] row_mask:0xf bank_mask:0xf\n\t"
      "s_nop 1\n\t"
      "v_add_f32_dpp v40, v40, v40 row_ror:4 row_mask:0xf bank_mask:0xf\n\t"
      "s_nop 1\n\t"
      "v_add_f32_dpp v40, v40, v40 row_ror:8 row_mask:0xf bank_mask:0xf\n\t"
      "v_mov_b32 v41, v40\n\t"
      "s_nop 1\n\t"
      "v_permlane16_swap_b32 v40, v41\n\t"
      "v_add_f32 v40, v40, v41\n\t"
      "v_mov_b32 v41, v40\n\t"
      "s_nop 1\n\t"
      "v_permlane32_swap_b32 v40, v41\n\t"
      "v_add_f32 v40, v40, v41\n\t"
      "v_cvt_pk_bf16_f32 v40, v40, v40\n\t"
      "v_and_b32 v40, s74, v40\n\t"
      "s_mov_b64 exec, 1\n\t"
      "buffer_store_dword v40, off, s[36:39], s70 sc1\n\t"
      "s_mov_b64 exec, -1\n\t"
      "v_pk_add_f32 v[192:193], v[192:193], v[32:33]\n\t"
      "v_pk_add_f32 v[194:195], v[194:195], v[34:35]\n\t"
      "v_pk_add_f32 v[196:197], v[196:197], v[36:37]\n\t"
      "v_pk_add_f32 v[198:199], v[198:199], v[38:39]\n\t"
      "v_readfirstlane_b32 s68, v40\n\t"
      "v_cvt_pk_bf16_f32 v56, v192, v193\n\t"
      "v_cvt_pk_bf16_f32 v57, v194, v195\n\t"
      "v_cvt_pk_bf16_f32 v58, v196, v197\n\t"
      "v_cvt_pk_bf16_f32 v59, v198, v199\n\t"
      "s_add_u32 s67, s69, 8192\n\t"
      "s_lshl_b32 s73, s73, 10\n\t"
      "s_add_u32 s69, s73, s58\n\t"
      "s_add_u32 s70, s73, s59\n\t"
      "buffer_store_dwordx2 v[56:57], v249, s[44:47], s73 offen sc1\n\t"
      "buffer_store_dwordx2 v[52:53], v249, s[36:39], s69 offen sc1\n\t"
      "buffer_store_dwordx2 v[200:201], v249, s[36:39], s70 offen sc1\n\t"
      "buffer_store_dwordx2 v[58:59], v249, s[44:47], s73 offen offset:512 sc1\n\t"
      "buffer_store_dwordx2 v[54:55], v249, s[36:39], s69 offen offset:512 sc1\n\t"
      "buffer_store_dwordx2 v[202:203], v249, s[36:39], s70 offen offset:512 sc1\n\t"
      "s_mov_b32 s49, s89\n\t"
      "s_add_u32 s89, s89, 16\n\t"
      "s_lshr_b32 s69, s89, 4\n\t"
      "s_cmp_eq_u32 s69, 3\n\t"
      "s_cselect_b32 s89, s93, s89\n\t"
      "s_cmp_ge_u32 s49, 48\n\t"
      "s_cselect_b32 s60, s94, s60\n\t"
      "s_cselect_b32 s54, s95, s54\n\t"
      "s_cmp_ge_u32 s48, 64\n\t"
      "s_cbranch_scc1 WRX%=\n\t"
      "s_cmp_lg_u32 s65, 0\n\t"
      "s_mov_b32 s65, 0\n\t"
      "s_cbranch_scc1 WGO%=\n\t"
      "s_mov_b32 s66, 0\n\t"
      "s_branch WLOOP%=\n\t"
      ".p2align 5\n\t"
      "WRX%=:\n\t"
      "s_mov_b32 s65, 0\n\t"
      "s_waitcnt vmcnt(0)\n\t"
      "s_branch WFIN1%=\n\t"
      ".p2align 5\n\t"
      "WRN%=:\n\t"
      "v_max3_u32 v40, v160, v161, v162\n\t"
      "v_max3_u32 v40, v40, v163, v164\n\t"
      "v_max3_u32 v40, v40, v165, v166\n\t"
      "v_max3_u32 v40, v40, v167, v168\n\t"
      "v_max3_u32 v40, v40, v169, v170\n\t"
      "v_max3_u32 v40, v40, v171, v172\n\t"
      "v_max3_u32 v40, v40, v173, v174\n\t"
      "v_max3_u32 v40, v40, v175, v176\n\t"
      "v_max3_u32 v40, v40, v177, v178\n\t"
      "v_max3_u32 v40, v40, v179, v180\n\t"
      "v_max3_u32 v40, v40, v181, v182\n\t"
      "v_max3_u32 v40, v40, v183, v184\n\t"
      "v_max3_u32 v40, v40, v185, v186\n\t"
      "v_max3_u32 v40, v40, v187, v188\n\t"
      "v_max3_u32 v40, v40, v189, v190\n\t"
      "v_max_u32 v40, v40, v191\n\t"
      "v_cmp_eq_u32 vcc, -1, v40\n\t"
      "s_nop 0\n\t"
      "s_cbranch_vccz WRD%=\n\t"
      "s_cmp_lg_u32 s65, 0\n\t"
      "s_cbranch_scc1 WRD%=\n\t"
      "s_sub_u32 s72, s72, 1\n\t"
      "s_cmp_gt_i32 s72, 0\n\t"
      "s_cbranch_scc1 WRL%=\n\t"
      "s_branch WRD%=\n\t"
      ".p2align 5\n\t"
      "WH2%=:\n\t"
      "s_mov_b32 s72, 0x40000\n\t"
      "WH3%=:\n\t"
      "buffer_load_dwordx2 v[0:1], off, s[36:39], s99 sc1\n\t"
      "buffer_load_dwordx2 v[2:3], off, s[36:39], s99 offset:16 sc1\n\t"
      "s_sub_u32 s72, s72, 1\n\t"
      "s_waitcnt vmcnt(0)\n\t"
      "v_bitop3_b32 v0, v0, v1, s62 bitop3:0x7e\n\t"
      "v_bitop3_b32 v2, v2, v3, s62 bitop3:0x7e\n\t"
      "v_or_b32 v0, v0, v2\n\t"
      "v_cmp_ne_u32 vcc, 0, v0\n\t"
      "s_cmp_eq_u32 s72, 0\n\t"
      "s_cbranch_scc1 WT0%=\n\t"
      "s_cbranch_vccnz WH3%=\n\t"
      "s_branch WT0%=\n\t"
      ".p2align 5\n\t"
      "WKV1%=:\n\t"
      "s_cmp_eq_u32 s66, 0\n\t"
      "s_cbranch_scc1 WB1%=\n\t"
      "WB2%=:\n\t"
      "s_mov_b32 s65, 1\n\t"
      "v_mfma_f32_16x16x32_bf16 v[0:3], v[128:131], a[160:163], 0\n\t"
      "v_mfma_f32_16x16x32_bf16 v[4:7], v[128:131], a[164:167], 0\n\t"
      "v_mfma_f32_16x16x32_bf16 v[0:3], v[132:135], a[168:171], v[0:3]\n\t"
      "v_mfma_f32_16x16x32_bf16 v[4:7], v[132:135], a[172:175], v[4:7]\n\t"
      "v_mfma_f32_16x16x32_bf16 v[0:3], v[136:139], a[176:179], v[0:3]\n\t"
      "v_mfma_f32_16x16x32_bf16 v[4:7], v[136:139], a[180:183], v[4:7]\n\t"
      "v_mfma_f32_16x16x32_bf16 v[0:3], v[140:143], a[184:187], v[0:3]\n\t"
      "v_mfma_f32_16x16x32_bf16 v[4:7], v[140:143], a[188:191], v[4:7]\n\t"
      "s_add_u32 s51, s51, 262144\n\t"
      "s_add_u32 s52, s52, 262144\n\t"
      "s_lshl_b32 s70, s49, 5\n\t"
      "s_add_u32 s70, s70, s60\n\t"
      "s_lshl_b32 s70, s70, 10\n\t"
      "buffer_load_dwordx2 v[224:225], v249, s[40:43], s70 offen \n\t"
      "s_nop 0\n\t"
      "v_max3_f32 v8, v0, v1, v2\n\t"
      "v_max3_f32 v8, v8, v3, v234\n\t"
      "v_max3_f32 v9, v4, v5, v6\n\t"
      "v_max3_f32 v9, v9, v7, v235\n\t"
      "s_nop 1\n\t"
      "v_permlane16_swap_b32 v8, v9\n\t"
      "v_max_f32 v8, v8, v9\n\t"
      "buffer_load_dwordx2 v[226:227], v249, s[40:43], s70 offen offset:512 \n\t"
      "v_mov_b32 v9, v8\n\t"
      "s_nop 1\n\t"
      "v_permlane16_swap_b32 v8, v9\n\t"
      "v_pk_mul_f32 v[8:9], v[8:9], s[86:87]\n\t"
      "v_pk_fma_f32 v[10:11], v[234:235], s[86:87], v[8:9] neg_lo:[0,0,1] neg_hi:[0,0,1]\n\t"
      "v_pk_fma_f32 v[0:1], v[0:1], s[86:87], v[8:9] op_sel:[0,0,0] op_sel_hi:[1,1,0] neg_lo:[0,0,1] neg_hi:[0,0,1]\n\t"
      "v_pk_fma_f32 v[2:3], v[2:3], s[86:87], v[8:9] op_sel:[0,0,0] op_sel_hi:[1,1,0] neg_lo:[0,0,1] neg_hi:[0,0,1]\n\t"
      "s_lshl_b32 s69, s49, 18\n\t"
      "s_add_u32 s69, s69, s54\n\t"
      "buffer_load_dwordx4 v[160:163], v248, s[36:39], s69 offen sc1\n\t"
      "v_pk_fma_f32 v[4:5], v[4:5], s[86:87], v[8:9] op_sel:[0,0,1] op_sel_hi:[1,1,1] neg_lo:[0,0,1] neg_hi:[0,0,1]\n\t"
      "v_pk_fma_f32 v[6:7], v[6:7], s[86:87], v[8:9] op_sel:[0,0,1] op_sel_hi:[1,1,1] neg_lo:[0,0,1] neg_hi:[0,0,1]\n\t"
      "v_exp_f32 v10, v10\n\t"
      "v_exp_f32 v11, v11\n\t"
      "s_add_u32 s69, s69, 0x8000\n\t"
      "buffer_load_dwordx4 v[164:167], v248, s[36:39], s69 offen sc1\n\t"
      "v_exp_f32 v0, v0\n\t"
      "v_exp_f32 v1, v1\n\t"
      "v_exp_f32 v2, v2\n\t"
      "v_exp_f32 v3, v3\n\t"
      "s_add_u32 s69, s69, 0x8000\n\t"
      "buffer_load_dwordx4 v[168:171], v248, s[36:39], s69 offen sc1\n\t"
      "v_exp_f32 v4, v4\n\t"
      "v_exp_f32 v5, v5\n\t"
      "v_exp_f32 v6, v6\n\t"
      "s_add_u32 s69, s69, 0x8000\n\t"
      "buffer_load_dwordx4 v[172:175], v248, s[36:39], s69 offen sc1\n\t"
      "v_exp_f32 v7, v7\n\t"
      "v_pk_mul_f32 v[12:13], v[236:237], v[10:11]\n\t"
      "v_pk_add_f32 v[14:15], v[0:1], v[2:3]\n\t"
      "v_pk_add_f32 v[16:17], v[4:5], v[6:7]\n\t"
      "s_add_u32 s69, s69, 0x8000\n\t"
      "buffer_load_dwordx4 v[176:179], v248, s[36:39], s69 offen sc1\n\t"
      "v_add_f32 v14, v14, v15\n\t"
      "v_add_f32 v15, v16, v17\n\t"
      "v_fma_f32 v12, v14, 0.5, v12\n\t"
      "v_fma_f32 v13, v15, 0.5, v13\n\t"
      "s_nop 1\n\t"
      "v_permlane16_swap_b32 v12, v13\n\t"
      "v_add_f32 v12, v12, v13\n\t"
      "s_add_u32 s69, s69, 0x8000\n\t"
      "buffer_load_dwordx4 v[180:183], v248, s[36:39], s69 offen sc1\n\t"
      "v_mov_b32 v13, v12\n\t"
      "s_nop 1\n\t"
      "v_permlane32_swap_b32 v12, v13\n\t"
      "v_add_f32 v12, v12, v13\n\t"
      "v_mov_b32 v13, v12\n\t"
      "s_nop 1\n\t"
      "v_permlane16_swap_b32 v12, v13\n\t"
      "v_rcp_f32 v12, v12\n\t"
      "s_add_u32 s69, s69, 0x8000\n\t"
      "buffer_load_dwordx4 v[184:187], v248, s[36:39], s69 offen sc1\n\t"
      "v_rcp_f32 v13, v13\n\t"
      "s_nop 0\n\t"
      "v_pk_mul_f32 v[10:11], v[10:11], v[12:13]\n\t"
      "v_pk_mul_f32 v[12:13], v[12:13], 0.5 op_sel_hi:[1,0]\n\t"
      "v_pk_mul_f32 v[0:1], v[0:1], v[12:13] op_sel:[0,0] op_sel_hi:[1,0]\n\t"
      "s_add_u32 s69, s69, 0x8000\n\t"
      "buffer_load_dwordx4 v[188:191], v248, s[36:39], s69 offen sc1\n\t"
      "v_pk_mul_f32 v[2:3], v[2:3], v[12:13] op_sel:[0,0] op_sel_hi:[1,0]\n\t"
      "v_pk_mul_f32 v[4:5], v[4:5], v[12:13] op_sel:[0,1] op_sel_hi:[1,1]\n\t"
      "v_pk_mul_f32 v[6:7], v[6:7], v[12:13] op_sel:[0,1] op_sel_hi:[1,1]\n\t"
      "v_cvt_pk_bf16_f32 v16, v0, v1\n\t"
      "v_cvt_pk_bf16_f32 v17, v2, v3\n\t"
      "v_cvt_pk_bf16_f32 v18, v4, v5\n\t"
      "s_add_u32 s69, s50, 8192\n\t"
      "s_add_u32 m0, s61, 0\n\t"
      "s_nop 0\n\t"
      "buffer_load_dwordx4 v250, s[36:39], s69 offen sc1 lds\n\t"
      "v_cvt_pk_bf16_f32 v19, v6, v7\n\t"
      "v_pk_mul_f32 v[64:65], v[64:65], v[10:11] op_sel:[0,0] op_sel_hi:[1,0]\n\t"
      "v_pk_mul_f32 v[66:67], v[66:67], v[10:11] op_sel:[0,0] op_sel_hi:[1,0]\n\t"
      "v_pk_mul_f32 v[68:69], v[68:69], v[10:11] op_sel:[0,0] op_sel_hi:[1,0]\n\t"
      "v_pk_mul_f32 v[70:71], v[70:71], v[10:11] op_sel:[0,0] op_sel_hi:[1,0]\n\t"
      "v_mfma_f32_16x16x16_bf16 v[64:67], v[144:145], v[16:17], v[64:67]\n\t"
      "buffer_load_dwordx4 v250, s[36:39], s69 offen offset:1024 sc1 lds\n\t"
      "v_pk_mul_f32 v[72:73], v[72:73], v[10:11] op_sel:[0,0] op_sel_hi:[1,0]\n\t"
      "v_pk_mul_f32 v[74:75], v[74:75], v[10:11] op_sel:[0,0] op_sel_hi:[1,0]\n\t"
      "v_mfma_f32_16x16x16_bf16 v[68:71], v[146:147], v[16:17], v[68:71]\n\t"
      "v_pk_mul_f32 v[76:77], v[76:77], v[10:11] op_sel:[0,0] op_sel_hi:[1,0]\n\t"
      "buffer_load_dwordx4 v250, s[36:39], s69 offen offset:2048 sc1 lds\n\t"
      "v_pk_mul_f32 v[78:79], v[78:79], v[10:11] op_sel:[0,0] op_sel_hi:[1,0]\n\t"
      "v_mfma_f32_16x16x16_bf16 v[72:75], v[148:149], v[16:17], v[72:75]\n\t"
      "v_pk_mul_f32 v[80:81], v[80:81], v[10:11] op_sel:[0,0] op_sel_hi:[1,0]\n\t"
      "v_pk_mul_f32 v[82:83], v[82:83], v[10:11] op_sel:[0,0] op_sel_hi:[1,0]\n\t"
      "v_mfma_f32_16x16x16_bf16 v[76:79], v[150:151], v[16:17], v[76:79]\n\t"
      "buffer_load_dwordx4 v250, s[36:39], s69 offen offset:3072 sc1 lds\n\t"
      "v_pk_mul_f32 v[84:85], v[84:85], v[10:11] op_sel:[0,0] op_sel_hi:[1,0]\n\t"
      "v_pk_mul_f32 v[86:87], v[86:87], v[10:11] op_sel:[0,0] op_sel_hi:[1,0]\n\t"
      "v_mfma_f32_16x16x16_bf16 v[80:83], v[152:153], v[16:17], v[80:83]\n\t"
      "v_pk_mul_f32 v[88:89], v[88:89], v[10:11] op_sel:[0,0] op_sel_hi:[1,0]\n\t"
      "s_add_u32 s69, s50, 12288\n\t"
      "s_add_u32 m0, s61, 4096\n\t"
      "s_nop 0\n\t"
      "buffer_load_dwordx4 v250, s[36:39], s69 offen sc1 lds\n\t"
      "v_pk_mul_f32 v[90:91], v[90:91], v[10:11] op_sel:[0,0] op_sel_hi:[1,0]\n\t"
      "v_mfma_f32_16x16x16_bf16 v[84:87], v[154:155], v[16:17], v[84:87]\n\t"
      "v_pk_mul_f32 v[92:93], v[92:93], v[10:11] op_sel:[0,0] op_sel_hi:[1,0]\n\t"
      "v_pk_mul_f32 v[94:95], v[94:95], v[10:11] op_sel:[0,0] op_sel_hi:[1,0]\n\t"
      "v_cvt_pk_bf16_f32 v24, v64, v65\n\t"
      "buffer_load_dwordx4 v250, s[36:39], s69 offen offset:1024 sc1 lds\n\t"
      "v_cvt_pk_bf16_f32 v25, v66, v67\n\t"
      "v_cvt_pk_bf16_f32 v26, v68, v69\n\t"
      "v_cvt_pk_bf16_f32 v27, v70, v71\n\t"
      "ds_write_b128 v254, v[24:27]\n\t"
      "v_mfma_f32_16x16x16_bf16 v[88:91], v[156:157], v[16:17], v[88:91]\n\t"
      "buffer_load_dwordx4 v250, s[36:39], s69 offen offset:2048 sc1 lds\n\t"
      "v_pk_mul_f32 v[96:97], v[96:97], v[10:11] op_sel:[0,1] op_sel_hi:[1,1]\n\t"
      "v_pk_mul_f32 v[98:99], v[98:99], v[10:11] op_sel:[0,1] op_sel_hi:[1,1]\n\t"
      "v_mfma_f32_16x16x16_bf16 v[92:95], v[158:159], v[16:17], v[92:95]\n\t"
      "v_pk_mul_f32 v[100:101], v[100:101], v[10:11] op_sel:[0,1] op_sel_hi:[1,1]\n\t"
      "v_pk_mul_f32 v[102:103], v[102:103], v[10:11] op_sel:[0,1] op_sel_hi:[1,1]\n\t"
      "buffer_load_dwordx4 v250, s[36:39], s69 offen offset:3072 sc1 lds\n\t"
      "v_cvt_pk_bf16_f32 v28, v72, v73\n\t"
      "v_cvt_pk_bf16_f32 v29, v74, v75\n\t"
      "v_cvt_pk_bf16_f32 v30, v76, v77\n\t"
      "v_cvt_pk_bf16_f32 v31, v78, v79\n\t"
      "ds_write_b128 v254, v[28:31] offset:2048\n\t"
      "v_mfma_f32_16x16x16_bf16 v[96:99], v[144:145], v[18:19], v[96:99]\n\t"
      "s_add_u32 s69, s50, 16384\n\t"
      "buffer_load_dwordx4 a[160:163], v248, s[36:39], s69 offen  sc1\n\t"
      "v_pk_mul_f32 v[104:105], v[104:105], v[10:11] op_sel:[0,1] op_sel_hi:[1,1]\n\t"
      "v_pk_mul_f32 v[106:107], v[106:107], v[10:11] op_sel:[0,1] op_sel_hi:[1,1]\n\t"
      "v_mfma_f32_16x16x16_bf16 v[100:103], v[146:147], v[18:19], v[100:103]\n\t"
      "v_pk_mul_f32 v[108:109], v[108:109], v[10:11] op_sel:[0,1] op_sel_hi:[1,1]\n\t"
      "buffer_load_dwordx4 a[164:167], v248, s[36:39], s69 offen offset:1024  sc1\n\t"
      "v_pk_mul_f32 v[110:111], v[110:111], v[10:11] op_sel:[0,1] op_sel_hi:[1,1]\n\t"
      "v_cvt_pk_bf16_f32 v24, v80, v81\n\t"
      "v_cvt_pk_bf16_f32 v25, v82, v83\n\t"
      "v_cvt_pk_bf16_f32 v26, v84, v85\n\t"
      "v_cvt_pk_bf16_f32 v27, v86, v87\n\t"
      "ds_write_b128 v254, v[24:27] offset:4096\n\t"
      "buffer_load_dwordx4 a[168:171], v248, s[36:39], s69 offen offset:2048  sc1\n\t"
      "v_mfma_f32_16x16x16_bf16 v[104:107], v[148:149], v[18:19], v[104:107]\n\t"
      "v_pk_mul_f32 v[112:113], v[112:113], v[10:11] op_sel:[0,1] op_sel_hi:[1,1]\n\t"
      "v_pk_mul_f32 v[114:115], v[114:115], v[10:11] op_sel:[0,1] op_sel_hi:[1,1]\n\t"
      "v_mfma_f32_16x16x16_bf16 v[108:111], v[150:151], v[18:19], v[108:111]\n\t"
      "buffer_load_dwordx4 a[172:175], v248, s[36:39], s69 offen offset:3072  sc1\n\t"
      "v_pk_mul_f32 v[116:117], v[116:117], v[10:11] op_sel:[0,1] op_sel_hi:[1,1]\n\t"
      "v_pk_mul_f32 v[118:119], v[118:119], v[10:11] op_sel:[0,1] op_sel_hi:[1,1]\n\t"
      "v_cvt_pk_bf16_f32 v28, v88, v89\n\t"
      "v_cvt_pk_bf16_f32 v29, v90, v91\n\t"
      "v_cvt_pk_bf16_f32 v30, v92, v93\n\t"
      "v_cvt_pk_bf16_f32 v31, v94, v95\n\t"
      "s_add_u32 s69, s50, 20480\n\t"
      "buffer_load_dwordx4 a[176:179], v248, s[36:39], s69 offen  sc1\n\t"
      "ds_write_b128 v254, v[28:31] offset:6144\n\t"
      "v_mfma_f32_16x16x16_bf16 v[112:115], v[152:153], v[18:19], v[112:115]\n\t"
      "v_pk_mul_f32 v[120:121], v[120:121], v[10:11] op_sel:[0,1] op_sel_hi:[1,1]\n\t"
      "v_pk_mul_f32 v[122:123], v[122:123], v[10:11] op_sel:[0,1] op_sel_hi:[1,1]\n\t"
      "v_mfma_f32_16x16x16_bf16 v[116:119], v[154:155], v[18:19], v[116:119]\n\t"
      "buffer_load_dwordx4 a[180:183], v248, s[36:39], s69 offen offset:1024  sc1\n\t"
      "v_pk_mul_f32 v[124:125], v[124:125], v[10:11] op_sel:[0,1] op_sel_hi:[1,1]\n\t"
      "v_pk_mul_f32 v[126:127], v[126:127], v[10:11] op_sel:[0,1] op_sel_hi:[1,1]\n\t"
      "v_cvt_pk_bf16_f32 v24, v96, v97\n\t"
      "v_cvt_pk_bf16_f32 v25, v98, v99\n\t"
      "v_cvt_pk_bf16_f32 v26, v100, v101\n\t"
      "buffer_load_dwordx4 a[184:187], v248, s[36:39], s69 offen offset:2048  sc1\n\t"
      "v_cvt_pk_bf16_f32 v27, v102, v103\n\t"
      "ds_write_b128 v254, v[24:27] offset:1024\n\t"
      "v_mfma_f32_16x16x16_bf16 v[120:123], v[156:157], v[18:19], v[120:123]\n\t"
      "v_mfma_f32_16x16x16_bf16 v[124:127], v[158:159], v[18:19], v[124:127]\n\t"
      "buffer_load_dwordx4 a[188:191], v248, s[36:39], s69 offen offset:3072  sc1\n\t"
      "v_cvt_pk_bf16_f32 v28, v104, v105\n\t"
      "v_cvt_pk_bf16_f32 v29, v106, v107\n\t"
      "v_cvt_pk_bf16_f32 v30, v108, v109\n\t"
      "v_cvt_pk_bf16_f32 v31, v110, v111\n\t"
      "ds_write_b128 v254, v[28:31] offset:3072\n\t"
      "v_cvt_pk_bf16_f32 v24, v112, v113\n\t"
      "v_cvt_pk_bf16_f32 v25, v114, v115\n\t"
      "v_cvt_pk_bf16_f32 v26, v116, v117\n\t"
      "v_cvt_pk_bf16_f32 v27, v118, v119\n\t"
      "ds_write_b128 v254, v[24:27] offset:5120\n\t"
      "v_cvt_pk_bf16_f32 v28, v120, v121\n\t"
      "v_cvt_pk_bf16_f32 v29, v122, v123\n\t"
      "v_cvt_pk_bf16_f32 v30, v124, v125\n\t"
      "v_cvt_pk_bf16_f32 v31, v126, v127\n\t"
      "ds_write_b128 v254, v[28:31] offset:7168\n\t"
      "s_waitcnt vmcnt(40) lgkmcnt(0)\n\t"
      "s_barrier\n\t"
      "s_branch WO%=\n\t"
      ".p2align 5\n\t"
      "WB1%=:\n\t"
      "v_mfma_f32_16x16x32_bf16 v[0:3], v[128:131], a[160:163], 0\n\t"
      "v_mfma_f32_16x16x32_bf16 v[4:7], v[128:131], a[164:167], 0\n\t"
      "v_mfma_f32_16x16x32_bf16 v[0:3], v[132:135], a[168:171], v[0:3]\n\t"
      "v_mfma_f32_16x16x32_bf16 v[4:7], v[132:135], a[172:175], v[4:7]\n\t"
      "v_mfma_f32_16x16x32_bf16 v[0:3], v[136:139], a[176:179], v[0:3]\n\t"
      "v_mfma_f32_16x16x32_bf16 v[4:7], v[136:139], a[180:183], v[4:7]\n\t"
      "v_mfma_f32_16x16x32_bf16 v[0:3], v[140:143], a[184:187], v[0:3]\n\t"
      "v_mfma_f32_16x16x32_bf16 v[4:7], v[140:143], a[188:191], v[4:7]\n\t"
      "s_add_u32 s51, s51, 262144\n\t"
      "s_add_u32 s52, s52, 262144\n\t"
      "s_add_u32 s69, s50, 8192\n\t"
      "s_add_u32 m0, s61, 0\n\t"
      "s_nop 0\n\t"
      "buffer_load_dwordx4 v250, s[36:39], s69 offen sc1 lds\n\t"
      "s_nop 0\n\t"
      "v_max3_f32 v8, v0, v1, v2\n\t"
      "v_max3_f32 v8, v8, v3, v234\n\t"
      "v_max3_f32 v9, v4, v5, v6\n\t"
      "v_max3_f32 v9, v9, v7, v235\n\t"
      "s_nop 1\n\t"
      "v_permlane16_swap_b32 v8, v9\n\t"
      "v_max_f32 v8, v8, v9\n\t"
      "v_mov_b32 v9, v8\n\t"
      "s_nop 1\n\t"
      "v_permlane16_swap_b32 v8, v9\n\t"
      "v_pk_mul_f32 v[8:9], v[8:9], s[86:87]\n\t"
      "v_pk_fma_f32 v[10:11], v[234:235], s[86:87], v[8:9] neg_lo:[0,0,1] neg_hi:[0,0,1]\n\t"
      "buffer_load_dwordx4 v250, s[36:39], s69 offen offset:1024 sc1 lds\n\t"
      "v_pk_fma_f32 v[0:1], v[0:1], s[86:87], v[8:9] op_sel:[0,0,0] op_sel_hi:[1,1,0] neg_lo:[0,0,1] neg_hi:[0,0,1]\n\t"
      "v_pk_fma_f32 v[2:3], v[2:3], s[86:87], v[8:9] op_sel:[0,0,0] op_sel_hi:[1,1,0] neg_lo:[0,0,1] neg_hi:[0,0,1]\n\t"
      "v_pk_fma_f32 v[4:5], v[4:5], s[86:87], v[8:9] op_sel:[0,0,1] op_sel_hi:[1,1,1] neg_lo:[0,0,1] neg_hi:[0,0,1]\n\t"
      "v_pk_fma_f32 v[6:7], v[6:7], s[86:87], v[8:9] op_sel:[0,0,1] op_sel_hi:[1,1,1] neg_lo:[0,0,1] neg_hi:[0,0,1]\n\t"
      "v_exp_f32 v10, v10\n\t"
      "v_exp_f32 v11, v11\n\t"
      "v_exp_f32 v0, v0\n\t"
      "buffer_load_dwordx4 v250, s[36:39], s69 offen offset:2048 sc1 lds\n\t"
      "v_exp_f32 v1, v1\n\t"
      "v_exp_f32 v2, v2\n\t"
      "v_exp_f32 v3, v3\n\t"
      "v_exp_f32 v4, v4\n\t"
      "v_exp_f32 v5, v5\n\t"
      "buffer_load_dwordx4 v250, s[36:39], s69 offen offset:3072 sc1 lds\n\t"
      "v_exp_f32 v6, v6\n\t"
      "v_exp_f32 v7, v7\n\t"
      "v_pk_mul_f32 v[12:13], v[236:237], v[10:11]\n\t"
      "v_pk_add_f32 v[14:15], v[0:1], v[2:3]\n\t"
      "v_pk_add_f32 v[16:17], v[4:5], v[6:7]\n\t"
      "v_add_f32 v14, v14, v15\n\t"
      "v_add_f32 v15, v16, v17\n\t"
      "v_fma_f32 v12, v14, 0.5, v12\n\t"
      "s_add_u32 s69, s50, 12288\n\t"
      "s_add_u32 m0, s61, 4096\n\t"
      "s_nop 0\n\t"
      "buffer_load_dwordx4 v250, s[36:39], s69 offen sc1 lds\n\t"
      "v_fma_f32 v13, v15, 0.5, v13\n\t"
      "s_nop 1\n\t"
      "v_permlane16_swap_b32 v12, v13\n\t"
      "v_add_f32 v12, v12, v13\n\t"
      "v_mov_b32 v13, v12\n\t"
      "s_nop 1\n\t"
      "v_permlane32_swap_b32 v12, v13\n\t"
      "v_add_f32 v12, v12, v13\n\t"
      "v_mov_b32 v13, v12\n\t"
      "s_nop 1\n\t"
      "v_permlane16_swap_b32 v12, v13\n\t"
      "v_rcp_f32 v12, v12\n\t"
      "buffer_load_dwordx4 v250, s[36:39], s69 offen offset:1024 sc1 lds\n\t"
      "v_rcp_f32 v13, v13\n\t"
      "s_nop 0\n\t"
      "v_pk_mul_f32 v[10:11], v[10:11], v[12:13]\n\t"
      "v_pk_mul_f32 v[12:13], v[12:13], 0.5 op_sel_hi:[1,0]\n\t"
      "v_pk_mul_f32 v[0:1], v[0:1], v[12:13] op_sel:[0,0] op_sel_hi:[1,0]\n\t"
      "v_pk_mul_f32 v[2:3], v[2:3], v[12:13] op_sel:[0,0] op_sel_hi:[1,0]\n\t"
      "v_pk_mul_f32 v[4:5], v[4:5], v[12:13] op_sel:[0,1] op_sel_hi:[1,1]\n\t"
      "v_pk_mul_f32 v[6:7], v[6:7], v[12:13] op_sel:[0,1] op_sel_hi:[1,1]\n\t"
      "v_cvt_pk_bf16_f32 v16, v0, v1\n\t"
      "buffer_load_dwordx4 v250, s[36:39], s69 offen offset:2048 sc1 lds\n\t"
      "v_cvt_pk_bf16_f32 v17, v2, v3\n\t"
      "v_cvt_pk_bf16_f32 v18, v4, v5\n\t"
      "v_cvt_pk_bf16_f32 v19, v6, v7\n\t"
      "v_pk_mul_f32 v[64:65], v[64:65], v[10:11] op_sel:[0,0] op_sel_hi:[1,0]\n\t"
      "v_pk_mul_f32 v[66:67], v[66:67], v[10:11] op_sel:[0,0] op_sel_hi:[1,0]\n\t"
      "v_pk_mul_f32 v[68:69], v[68:69], v[10:11] op_sel:[0,0] op_sel_hi:[1,0]\n\t"
      "v_pk_mul_f32 v[70:71], v[70:71], v[10:11] op_sel:[0,0] op_sel_hi:[1,0]\n\t"
      "v_mfma_f32_16x16x16_bf16 v[64:67], v[144:145], v[16:17], v[64:67]\n\t"
      "v_pk_mul_f32 v[72:73], v[72:73], v[10:11] op_sel:[0,0] op_sel_hi:[1,0]\n\t"
      "buffer_load_dwordx4 v250, s[36:39], s69 offen offset:3072 sc1 lds\n\t"
      "v_pk_mul_f32 v[74:75], v[74:75], v[10:11] op_sel:[0,0] op_sel_hi:[1,0]\n\t"
      "v_mfma_f32_16x16x16_bf16 v[68:71], v[146:147], v[16:17], v[68:71]\n\t"
      "v_pk_mul_f32 v[76:77], v[76:77], v[10:11] op_sel:[0,0] op_sel_hi:[1,0]\n\t"
      "v_pk_mul_f32 v[78:79], v[78:79], v[10:11] op_sel:[0,0] op_sel_hi:[1,0]\n\t"
      "v_mfma_f32_16x16x16_bf16 v[72:75], v[148:149], v[16:17], v[72:75]\n\t"
      "v_pk_mul_f32 v[80:81], v[80:81], v[10:11] op_sel:[0,0] op_sel_hi:[1,0]\n\t"
      "v_pk_mul_f32 v[82:83], v[82:83], v[10:11] op_sel:[0,0] op_sel_hi:[1,0]\n\t"
      "v_mfma_f32_16x16x16_bf16 v[76:79], v[150:151], v[16:17], v[76:79]\n\t"
      "s_add_u32 s69, s50, 16384\n\t"
      "buffer_load_dwordx4 a[160:163], v248, s[36:39], s69 offen  sc1\n\t"
      "v_pk_mul_f32 v[84:85], v[84:85], v[10:11] op_sel:[0,0] op_sel_hi:[1,0]\n\t"
      "v_pk_mul_f32 v[86:87], v[86:87], v[10:11] op_sel:[0,0] op_sel_hi:[1,0]\n\t"
      "v_mfma_f32_16x16x16_bf16 v[80:83], v[152:153], v[16:17], v[80:83]\n\t"
      "v_pk_mul_f32 v[88:89], v[88:89], v[10:11] op_sel:[0,0] op_sel_hi:[1,0]\n\t"
      "v_pk_mul_f32 v[90:91], v[90:91], v[10:11] op_sel:[0,0] op_sel_hi:[1,0]\n\t"
      "v_mfma_f32_16x16x16_bf16 v[84:87], v[154:155], v[16:17], v[84:87]\n\t"
      "v_pk_mul_f32 v[92:93], v[92:93], v[10:11] op_sel:[0,0] op_sel_hi:[1,0]\n\t"
      "buffer_load_dwordx4 a[164:167], v248, s[36:39], s69 offen offset:1024  sc1\n\t"
      "v_pk_mul_f32 v[94:95], v[94:95], v[10:11] op_sel:[0,0] op_sel_hi:[1,0]\n\t"
      "v_cvt_pk_bf16_f32 v24, v64, v65\n\t"
      "v_cvt_pk_bf16_f32 v25, v66, v67\n\t"
      "v_cvt_pk_bf16_f32 v26, v68, v69\n\t"
      "v_cvt_pk_bf16_f32 v27, v70, v71\n\t"
      "ds_write_b128 v254, v[24:27]\n\t"
      "v_mfma_f32_16x16x16_bf16 v[88:91], v[156:157], v[16:17], v[88:91]\n\t"
      "v_pk_mul_f32 v[96:97], v[96:97], v[10:11] op_sel:[0,1] op_sel_hi:[1,1]\n\t"
      "buffer_load_dwordx4 a[168:171], v248, s[36:39], s69 offen offset:2048  sc1\n\t"
      "v_pk_mul_f32 v[98:99], v[98:99], v[10:11] op_sel:[0,1] op_sel_hi:[1,1]\n\t"
      "v_mfma_f32_16x16x16_bf16 v[92:95], v[158:159], v[16:17], v[92:95]\n\t"
      "v_pk_mul_f32 v[100:101], v[100:101], v[10:11] op_sel:[0,1] op_sel_hi:[1,1]\n\t"
      "v_pk_mul_f32 v[102:103], v[102:103], v[10:11] op_sel:[0,1] op_sel_hi:[1,1]\n\t"
      "v_cvt_pk_bf16_f32 v28, v72, v73\n\t"
      "v_cvt_pk_bf16_f32 v29, v74, v75\n\t"
      "v_cvt_pk_bf16_f32 v30, v76, v77\n\t"
      "v_cvt_pk_bf16_f32 v31, v78, v79\n\t"
      "ds_write_b128 v254, v[28:31] offset:2048\n\t"
      "buffer_load_dwordx4 a[172:175], v248, s[36:39], s69 offen offset:3072  sc1\n\t"
      "v_mfma_f32_16x16x16_bf16 v[96:99], v[144:145], v[18:19], v[96:99]\n\t"
      "v_pk_mul_f32 v[104:105], v[104:105], v[10:11] op_sel:[0,1] op_sel_hi:[1,1]\n\t"
      "v_pk_mul_f32 v[106:107], v[106:107], v[10:11] op_sel:[0,1] op_sel_hi:[1,1]\n\t"
      "v_mfma_f32_16x16x16_bf16 v[100:103], v[146:147], v[18:19], v[100:103]\n\t"
      "v_pk_mul_f32 v[108:109], v[108:109], v[10:11] op_sel:[0,1] op_sel_hi:[1,1]\n\t"
      "v_pk_mul_f32 v[110:111], v[110:111], v[10:11] op_sel:[0,1] op_sel_hi:[1,1]\n\t"
      "v_cvt_pk_bf16_f32 v24, v80, v81\n\t"
      "v_cvt_pk_bf16_f32 v25, v82, v83\n\t"
      "s_add_u32 s69, s50, 20480\n\t"
      "buffer_load_dwordx4 a[176:179], v248, s[36:39], s69 offen  sc1\n\t"
      "v_cvt_pk_bf16_f32 v26, v84, v85\n\t"
      "v_cvt_pk_bf16_f32 v27, v86, v87\n\t"
      "ds_write_b128 v254, v[24:27] offset:4096\n\t"
      "v_mfma_f32_16x16x16_bf16 v[104:107], v[148:149], v[18:19], v[104:107]\n\t"
      "v_pk_mul_f32 v[112:113], v[112:113], v[10:11] op_sel:[0,1] op_sel_hi:[1,1]\n\t"
      "v_pk_mul_f32 v[114:115], v[114:115], v[10:11] op_sel:[0,1] op_sel_hi:[1,1]\n\t"
      "v_mfma_f32_16x16x16_bf16 v[108:111], v[150:151], v[18:19], v[108:111]\n\t"
      "buffer_load_dwordx4 a[180:183], v248, s[36:39], s69 offen offset:1024  sc1\n\t"
      "v_pk_mul_f32 v[116:117], v[116:117], v[10:11] op_sel:[0,1] op_sel_hi:[1,1]\n\t"
      "v_pk_mul_f32 v[118:119], v[118:119], v[10:11] op_sel:[0,1] op_sel_hi:[1,1]\n\t"
      "v_cvt_pk_bf16_f32 v28, v88, v89\n\t"
      "v_cvt_pk_bf16_f32 v29, v90, v91\n\t"
      "v_cvt_pk_bf16_f32 v30, v92, v93\n\t"
      "v_cvt_pk_bf16_f32 v31, v94, v95\n\t"
      "ds_write_b128 v254, v[28:31] offset:6144\n\t"
      "v_mfma_f32_16x16x16_bf16 v[112:115], v[152:153], v[18:19], v[112:115]\n\t"
      "v_pk_mul_f32 v[120:121], v[120:121], v[10:11] op_sel:[0,1] op_sel_hi:[1,1]\n\t"
      "buffer_load_dwordx4 a[184:187], v248, s[36:39], s69 offen offset:2048  sc1\n\t"
      "v_pk_mul_f32 v[122:123], v[122:123], v[10:11] op_sel:[0,1] op_sel_hi:[1,1]\n\t"
      "v_mfma_f32_16x16x16_bf16 v[116:119], v[154:155], v[18:19], v[116:119]\n\t"
      "v_pk_mul_f32 v[124:125], v[124:125], v[10:11] op_sel:[0,1] op_sel_hi:[1,1]\n\t"
      "v_pk_mul_f32 v[126:127], v[126:127], v[10:11] op_sel:[0,1] op_sel_hi:[1,1]\n\t"
      "v_cvt_pk_bf16_f32 v24, v96, v97\n\t"
      "v_cvt_pk_bf16_f32 v25, v98, v99\n\t"
      "v_cvt_pk_bf16_f32 v26, v100, v101\n\t"
      "v_cvt_pk_bf16_f32 v27, v102, v103\n\t"
      "ds_write_b128 v254, v[24:27] offset:1024\n\t"
      "buffer_load_dwordx4 a[188:191], v248, s[36:39], s69 offen offset:3072  sc1\n\t"
      "v_mfma_f32_16x16x16_bf16 v[120:123], v[156:157], v[18:19], v[120:123]\n\t"
      "v_mfma_f32_16x16x16_bf16 v[124:127], v[158:159], v[18:19], v[124:127]\n\t"
      "v_cvt_pk_bf16_f32 v28, v104, v105\n\t"
      "v_cvt_pk_bf16_f32 v29, v106, v107\n\t"
      "v_cvt_pk_bf16_f32 v30, v108, v109\n\t"
      "v_cvt_pk_bf16_f32 v31, v110, v111\n\t"
      "ds_write_b128 v254, v[28:31] offset:3072\n\t"
      "v_cvt_pk_bf16_f32 v24, v112, v113\n\t"
      "v_cvt_pk_bf16_f32 v25, v114, v115\n\t"
      "v_cvt_pk_bf16_f32 v26, v116, v117\n\t"
      "v_cvt_pk_bf16_f32 v27, v118, v119\n\t"
      "ds_write_b128 v254, v[24:27] offset:5120\n\t"
      "v_cvt_pk_bf16_f32 v28, v120, v121\n\t"
      "v_cvt_pk_bf16_f32 v29, v122, v123\n\t"
      "v_cvt_pk_bf16_f32 v30, v124, v125\n\t"
      "v_cvt_pk_bf16_f32 v31, v126, v127\n\t"
      "ds_write_b128 v254, v[28:31] offset:7168\n\t"
      "s_waitcnt vmcnt(40) lgkmcnt(0)\n\t"
      "s_barrier\n\t"
      "s_branch WO%=\n\t"
      ".p2align 5\n\t"
      "WOL1%=:\n\t"
      "buffer_load_dwordx4 v[128:131], v252, s[36:39], s51 offen sc1\n\t"
      "s_waitcnt lgkmcnt(11)\n\t"
      "v_mfma_f32_16x16x32_bf16 v[0:3], a[0:3], v[64:67], 0\n\t"
      "v_mfma_f32_16x16x32_bf16 v[4:7], a[64:67], v[64:67], 0\n\t"
      "ds_read_b128 v[112:115], v255 offset:12288\n\t"
      "s_waitcnt lgkmcnt(11)\n\t"
      "v_mfma_f32_16x16x32_bf16 v[8:11], a[0:3], v[68:71], 0\n\t"
      "v_mfma_f32_16x16x32_bf16 v[12:15], a[64:67], v[68:71], 0\n\t"
      "ds_read_b128 v[116:119], v255 offset:13312\n\t"
      "s_waitcnt lgkmcnt(11)\n\t"
      "v_mfma_f32_16x16x32_bf16 v[0:3], a[4:7], v[72:75], v[0:3]\n\t"
      "v_mfma_f32_16x16x32_bf16 v[4:7], a[68:71], v[72:75], v[4:7]\n\t"
      "ds_read_b128 v[120:123], v255 offset:14336\n\t"
      "s_waitcnt lgkmcnt(11)\n\t"
      "v_mfma_f32_16x16x32_bf16 v[8:11], a[4:7], v[76:79], v[8:11]\n\t"
      "v_mfma_f32_16x16x32_bf16 v[12:15], a[68:71], v[76:79], v[12:15]\n\t"
      "buffer_load_dwordx4 v[132:135], v252, s[36:39], s51 offen offset:512 sc1\n\t"
      "ds_read_b128 v[124:127], v255 offset:15360\n\t"
      "s_waitcnt lgkmcnt(11)\n\t"
      "v_mfma_f32_16x16x32_bf16 v[0:3], a[8:11], v[80:83], v[0:3]\n\t"
      "v_mfma_f32_16x16x32_bf16 v[4:7], a[72:75], v[80:83], v[4:7]\n\t"
      "ds_read_b128 v[64:67], v255 offset:16384\n\t"
      "s_waitcnt lgkmcnt(11)\n\t"
      "v_mfma_f32_16x16x32_bf16 v[8:11], a[8:11], v[84:87], v[8:11]\n\t"
      "v_mfma_f32_16x16x32_bf16 v[12:15], a[72:75], v[84:87], v[12:15]\n\t"
      "ds_read_b128 v[68:71], v255 offset:17408\n\t"
      "s_waitcnt lgkmcnt(11)\n\t"
      "v_mfma_f32_16x16x32_bf16 v[0:3], a[12:15], v[88:91], v[0:3]\n\t"
      "v_mfma_f32_16x16x32_bf16 v[4:7], a[76:79], v[88:91], v[4:7]\n\t"
      "ds_read_b128 v[72:75], v255 offset:18432\n\t"
      "s_waitcnt lgkmcnt(11)\n\t"
      "v_mfma_f32_16x16x32_bf16 v[8:11], a[12:15], v[92:95], v[8:11]\n\t"
      "v_mfma_f32_16x16x32_bf16 v[12:15], a[76:79], v[92:95], v[12:15]\n\t"
      "buffer_load_dwordx4 v[136:139], v252, s[36:39], s51 offen offset:1024 sc1\n\t"
      "ds_read_b128 v[76:79], v255 offset:19456\n\t"
      "s_waitcnt lgkmcnt(11)\n\t"
      "v_mfma_f32_16x16x32_bf16 v[0:3], a[16:19], v[96:99], v[0:3]\n\t"
      "v_mfma_f32_16x16x32_bf16 v[4:7], a[80:83], v[96:99], v[4:7]\n\t"
      "ds_read_b128 v[80:83], v255 offset:20480\n\t"
      "s_waitcnt lgkmcnt(11)\n\t"
      "v_mfma_f32_16x16x32_bf16 v[8:11], a[16:19], v[100:103], v[8:11]\n\t"
      "v_mfma_f32_16x16x32_bf16 v[12:15], a[80:83], v[100:103], v[12:15]\n\t"
      "ds_read_b128 v[84:87], v255 offset:21504\n\t"
      "s_waitcnt lgkmcnt(11)\n\t"
      "v_mfma_f32_16x16x32_bf16 v[0:3], a[20:23], v[104:107], v[0:3]\n\t"
      "v_mfma_f32_16x16x32_bf16 v[4:7], a[84:87], v[104:107], v[4:7]\n\t"
      "ds_read_b128 v[88:91], v255 offset:22528\n\t"
      "s_waitcnt lgkmcnt(11)\n\t"
      "v_mfma_f32_16x16x32_bf16 v[8:11], a[20:23], v[108:111], v[8:11]\n\t"
      "v_mfma_f32_16x16x32_bf16 v[12:15], a[84:87], v[108:111], v[12:15]\n\t"
      "buffer_load_dwordx4 v[140:143], v252, s[36:39], s51 offen offset:1536 sc1\n\t"
      "ds_read_b128 v[92:95], v255 offset:23552\n\t"
      "s_waitcnt lgkmcnt(11)\n\t"
      "v_mfma_f32_16x16x32_bf16 v[0:3], a[24:27], v[112:115], v[0:3]\n\t"
      "v_mfma_f32_16x16x32_bf16 v[4:7], a[88:91], v[112:115], v[4:7]\n\t"
      "ds_read_b128 v[96:99], v255 offset:24576\n\t"
      "s_waitcnt lgkmcnt(11)\n\t"
      "v_mfma_f32_16x16x32_bf16 v[8:11], a[24:27], v[116:119], v[8:11]\n\t"
      "v_mfma_f32_16x16x32_bf16 v[12:15], a[88:91], v[116:119], v[12:15]\n\t"
      "ds_read_b128 v[100:103], v255 offset:25600\n\t"
      "s_waitcnt lgkmcnt(11)\n\t"
      "v_mfma_f32_16x16x32_bf16 v[0:3], a[28:31], v[120:123], v[0:3]\n\t"
      "v_mfma_f32_16x16x32_bf16 v[4:7], a[92:95], v[120:123], v[4:7]\n\t"
      "ds_read_b128 v[104:107], v255 offset:26624\n\t"
      "s_waitcnt lgkmcnt(11)\n\t"
      "v_mfma_f32_16x16x32_bf16 v[8:11], a[28:31], v[124:127], v[8:11]\n\t"
      "buffer_load_dwordx4 v[144:147], v253, s[36:39], s52 offen sc1\n\t"
      "v_mfma_f32_16x16x32_bf16 v[12:15], a[92:95], v[124:127], v[12:15]\n\t"
      "ds_read_b128 v[108:111], v255 offset:27648\n\t"
      "s_waitcnt lgkmcnt(11)\n\t"
      "v_mfma_f32_16x16x32_bf16 v[0:3], a[32:35], v[64:67], v[0:3]\n\t"
      "v_mfma_f32_16x16x32_bf16 v[4:7], a[96:99], v[64:67], v[4:7]\n\t"
      "ds_read_b128 v[112:115], v255 offset:28672\n\t"
      "s_waitcnt lgkmcnt(11)\n\t"
      "v_mfma_f32_16x16x32_bf16 v[8:11], a[32:35], v[68:71], v[8:11]\n\t"
      "v_mfma_f32_16x16x32_bf16 v[12:15], a[96:99], v[68:71], v[12:15]\n\t"
      "ds_read_b128 v[116:119], v255 offset:29696\n\t"
      "s_waitcnt lgkmcnt(11)\n\t"
      "v_mfma_f32_16x16x32_bf16 v[0:3], a[36:39], v[72:75], v[0:3]\n\t"
      "v_mfma_f32_16x16x32_bf16 v[4:7], a[100:103], v[72:75], v[4:7]\n\t"
      "ds_read_b128 v[120:123], v255 offset:30720\n\t"
      "s_waitcnt lgkmcnt(11)\n\t"
      "v_mfma_f32_16x16x32_bf16 v[8:11], a[36:39], v[76:79], v[8:11]\n\t"
      "buffer_load_dwordx4 v[148:151], v253, s[36:39], s52 offen offset:512 sc1\n\t"
      "v_mfma_f32_16x16x32_bf16 v[12:15], a[100:103], v[76:79], v[12:15]\n\t"
      "ds_read_b128 v[124:127], v255 offset:31744\n\t"
      "s_waitcnt lgkmcnt(11)\n\t"
      "v_mfma_f32_16x16x32_bf16 v[0:3], a[40:43], v[80:83], v[0:3]\n\t"
      "v_mfma_f32_16x16x32_bf16 v[4:7], a[104:107], v[80:83], v[4:7]\n\t"
      "s_waitcnt lgkmcnt(10)\n\t"
      "v_mfma_f32_16x16x32_bf16 v[8:11], a[40:43], v[84:87], v[8:11]\n\t"
      "v_mfma_f32_16x16x32_bf16 v[12:15], a[104:107], v[84:87], v[12:15]\n\t"
      "s_waitcnt lgkmcnt(9)\n\t"
      "v_mfma_f32_16x16x32_bf16 v[0:3], a[44:47], v[88:91], v[0:3]\n\t"
      "v_mfma_f32_16x16x32_bf16 v[4:7], a[108:111], v[88:91], v[4:7]\n\t"
      "s_waitcnt lgkmcnt(8)\n\t"
      "v_mfma_f32_16x16x32_bf16 v[8:11], a[44:47], v[92:95], v[8:11]\n\t"
      "buffer_load_dwordx4 v[152:155], v253, s[36:39], s52 offen offset:1024 sc1\n\t"
      "v_mfma_f32_16x16x32_bf16 v[12:15], a[108:111], v[92:95], v[12:15]\n\t"
      "s_waitcnt lgkmcnt(7)\n\t"
      "v_mfma_f32_16x16x32_bf16 v[0:3], a[48:51], v[96:99], v[0:3]\n\t"
      "v_mfma_f32_16x16x32_bf16 v[4:7], a[112:115], v[96:99], v[4:7]\n\t"
      "s_waitcnt lgkmcnt(6)\n\t"
      "v_mfma_f32_16x16x32_bf16 v[8:11], a[48:51], v[100:103], v[8:11]\n\t"
      "v_mfma_f32_16x16x32_bf16 v[12:15], a[112:115], v[100:103], v[12:15]\n\t"
      "s_waitcnt lgkmcnt(5)\n\t"
      "v_mfma_f32_16x16x32_bf16 v[0:3], a[52:55], v[104:107], v[0:3]\n\t"
      "v_mfma_f32_16x16x32_bf16 v[4:7], a[116:119], v[104:107], v[4:7]\n\t"
      "s_waitcnt lgkmcnt(4)\n\t"
      "v_mfma_f32_16x16x32_bf16 v[8:11], a[52:55], v[108:111], v[8:11]\n\t"
      "v_mfma_f32_16x16x32_bf16 v[12:15], a[116:119], v[108:111], v[12:15]\n\t"
      "buffer_load_dwordx4 v[156:159], v253, s[36:39], s52 offen offset:1536 sc1\n\t"
      "s_waitcnt lgkmcnt(3)\n\t"
      "v_mfma_f32_16x16x32_bf16 v[0:3], a[56:59], v[112:115], v[0:3]\n\t"
      "v_mfma_f32_16x16x32_bf16 v[4:7], a[120:123], v[112:115], v[4:7]\n\t"
      "s_waitcnt lgkmcnt(2)\n\t"
      "v_mfma_f32_16x16x32_bf16 v[8:11], a[56:59], v[116:119], v[8:11]\n\t"
      "v_mfma_f32_16x16x32_bf16 v[12:15], a[120:123], v[116:119], v[12:15]\n\t"
      "s_waitcnt lgkmcnt(1)\n\t"
      "v_mfma_f32_16x16x32_bf16 v[0:3], a[60:63], v[120:123], v[0:3]\n\t"
      "v_mfma_f32_16x16x32_bf16 v[4:7], a[124:127], v[120:123], v[4:7]\n\t"
      "s_waitcnt lgkmcnt(0)\n\t"
      "v_mfma_f32_16x16x32_bf16 v[8:11], a[60:63], v[124:127], v[8:11]\n\t"
      "v_mfma_f32_16x16x32_bf16 v[12:15], a[124:127], v[124:127], v[12:15]\n\t"
      "v_xor_b32 v254, 0x8000, v254\n\t"
      "v_xor_b32 v255, 0x8000, v255\n\t"
      "s_add_u32 s69, s53, 0x4000\n\t"
      "s_add_u32 s48, s48, 4\n\t"
      "s_waitcnt vmcnt(40)\n\t"
      "v_cvt_pk_bf16_f32 v0, v0, v1\n\t"
      "v_cvt_pk_bf16_f32 v1, v2, v3\n\t"
      "v_cvt_pk_bf16_f32 v2, v4, v5\n\t"
      "v_cvt_pk_bf16_f32 v3, v6, v7\n\t"
      "v_min_u32 v0, -2, v0\n\t"
      "v_min_u32 v1, -2, v1\n\t"
      "v_min_u32 v2, -2, v2\n\t"
      "v_min_u32 v3, -2, v3\n\t"
      "buffer_store_dwordx4 v[0:3], v238, s[36:39], s53 offen sc1\n\t"
      "v_cvt_pk_bf16_f32 v8, v8, v9\n\t"
      "v_cvt_pk_bf16_f32 v9, v10, v11\n\t"
      "v_cvt_pk_bf16_f32 v10, v12, v13\n\t"
      "v_cvt_pk_bf16_f32 v11, v14, v15\n\t"
      "v_min_u32 v8, -2, v8\n\t"
      "v_min_u32 v9, -2, v9\n\t"
      "v_min_u32 v10, -2, v10\n\t"
      "v_min_u32 v11, -2, v11\n\t"
      "buffer_store_dwordx4 v[8:11], v238, s[36:39], s69 offen sc1\n\t"
      "s_add_u32 s53, s53, 1048576\n\t"
      "s_nop 1\n\t"
      "s_branch WTOP%=\n\t"
      ".p2align 5\n\t"
      "WR2%=:\n\t"
      "v_cvt_pk_bf16_f32 v204, v204, v205\n\t"
      "v_and_b32 v205, s74, v204\n\t"
      "v_lshlrev_b32 v204, 16, v204\n\t"
      "v_add_f32 v205, 0x3727c5ac, v205\n\t"
      "v_pk_add_f32 v[40:41], v[192:193], v[204:205] op_sel_hi:[1,0] neg_lo:[0,1] neg_hi:[0,1]\n\t"
      "v_pk_add_f32 v[42:43], v[194:195], v[204:205] op_sel_hi:[1,0] neg_lo:[0,1] neg_hi:[0,1]\n\t"
      "v_pk_add_f32 v[44:45], v[196:197], v[204:205] op_sel_hi:[1,0] neg_lo:[0,1] neg_hi:[0,1]\n\t"
      "v_pk_add_f32 v[46:47], v[198:199], v[204:205] op_sel_hi:[1,0] neg_lo:[0,1] neg_hi:[0,1]\n\t"
      "v_cvt_pk_bf16_f32 v205, v205, v205\n\t"
      "v_and_b32 v205, s74, v205\n\t"
      "v_sqrt_f32 v205, v205\n\t"
      "v_cvt_pk_bf16_f32 v40, v40, v41\n\t"
      "v_cvt_pk_bf16_f32 v42, v42, v43\n\t"
      "v_cvt_pk_bf16_f32 v44, v44, v45\n\t"
      "v_cvt_pk_bf16_f32 v46, v46, v47\n\t"
      "v_cvt_pk_bf16_f32 v205, v205, v205\n\t"
      "v_and_b32 v205, s74, v205\n\t"
      "v_rcp_f32 v204, v205\n\t"
      "v_and_b32 v41, s74, v40\n\t"
      "v_lshlrev_b32 v40, 16, v40\n\t"
      "v_and_b32 v43, s74, v42\n\t"
      "v_lshlrev_b32 v42, 16, v42\n\t"
      "v_and_b32 v45, s74, v44\n\t"
      "v_lshlrev_b32 v44, 16, v44\n\t"
      "v_and_b32 v47, s74, v46\n\t"
      "v_lshlrev_b32 v46, 16, v46\n\t"
      "v_pk_mul_f32 v[40:41], v[40:41], v[204:205] op_sel_hi:[1,0]\n\t"
      "v_pk_mul_f32 v[42:43], v[42:43], v[204:205] op_sel_hi:[1,0]\n\t"
      "v_pk_mul_f32 v[44:45], v[44:45], v[204:205] op_sel_hi:[1,0]\n\t"
      "v_pk_mul_f32 v[46:47], v[46:47], v[204:205] op_sel_hi:[1,0]\n\t"
      "v_cvt_pk_bf16_f32 v52, v40, v41\n\t"
      "v_cvt_pk_bf16_f32 v53, v42, v43\n\t"
      "v_cvt_pk_bf16_f32 v54, v44, v45\n\t"
      "v_cvt_pk_bf16_f32 v55, v46, v47\n\t"
      "v_and_b32 v33, s74, v52\n\t"
      "v_lshlrev_b32 v32, 16, v52\n\t"
      "v_and_b32 v35, s74, v53\n\t"
      "v_lshlrev_b32 v34, 16, v53\n\t"
      "v_and_b32 v37, s74, v54\n\t"
      "v_lshlrev_b32 v36, 16, v54\n\t"
      "v_and_b32 v39, s74, v55\n\t"
      "v_lshlrev_b32 v38, 16, v55\n\t"
      "v_pk_mul_f32 v[40:41], v[32:33], v[240:241]\n\t"
      "v_pk_fma_f32 v[40:41], v[34:35], v[242:243], v[40:41]\n\t"
      "v_pk_fma_f32 v[40:41], v[36:37], v[244:245], v[40:41]\n\t"
      "v_pk_fma_f32 v[40:41], v[38:39], v[246:247], v[40:41]\n\t"
      "v_add_f32 v40, v40, v41\n\t"
      "s_lshl_b32 s73, s49, 5\n\t"
      "s_add_u32 s73, s73, s60\n\t"
      "s_lshl_b32 s69, s73, 2\n\t"
      "s_add_u32 s69, s69, s57\n\t"
      "s_lshl_b32 s70, s73, 7\n\t"
      "s_add_u32 s70, s70, s57\n\t"
      "s_sub_u32 s70, s70, 212992\n\t"
      "s_cmp_ge_u32 s49, 56\n\t"
      "s_cselect_b32 s70, s70, s69\n\t"
      "v_add_f32_dpp v40, v40, v40 quad_perm:[1,0,3,2] row_mask:0xf bank_mask:0xf\n\t"
      "s_nop 1\n\t"
      "v_add_f32_dpp v40, v40, v40 quad_perm:[2,3,0,1] row_mask:0xf bank_mask:0xf\n\t"
      "s_nop 1\n\t"
      "v_add_f32_dpp v40, v40, v40 row_ror:4 row_mask:0xf bank_mask:0xf\n\t"
      "s_nop 1\n\t"
      "v_add_f32_dpp v40, v40, v40 row_ror:8 row_mask:0xf bank_mask:0xf\n\t"
      "v_mov_b32 v41, v40\n\t"
      "s_nop 1\n\t"
      "v_permlane16_swap_b32 v40, v41\n\t"
      "v_add_f32 v40, v40, v41\n\t"
      "v_mov_b32 v41, v40\n\t"
      "s_nop 1\n\t"
      "v_permlane32_swap_b32 v40, v41\n\t"
      "v_add_f32 v40, v40, v41\n\t"
      "v_cvt_pk_bf16_f32 v40, v40, v40\n\t"
      "v_and_b32 v40, s74, v40\n\t"
      "s_mov_b64 exec, 1\n\t"
      "buffer_store_dword v40, off, s[36:39], s70 sc1\n\t"
      "s_mov_b64 exec, -1\n\t"
      "v_pk_add_f32 v[192:193], v[192:193], v[32:33]\n\t"
      "v_pk_add_f32 v[194:195], v[194:195], v[34:35]\n\t"
      "v_pk_add_f32 v[196:197], v[196:197], v[36:37]\n\t"
      "v_pk_add_f32 v[198:199], v[198:199], v[38:39]\n\t"
      "v_readfirstlane_b32 s68, v40\n\t"
      "v_cvt_pk_bf16_f32 v56, v192, v193\n\t"
      "v_cvt_pk_bf16_f32 v57, v194, v195\n\t"
      "v_cvt_pk_bf16_f32 v58, v196, v197\n\t"
      "v_cvt_pk_bf16_f32 v59, v198, v199\n\t"
      "s_add_u32 s67, s69, 8192\n\t"
      "s_lshl_b32 s73, s73, 10\n\t"
      "s_add_u32 s69, s73, s58\n\t"
      "s_add_u32 s70, s73, s59\n\t"
      "buffer_store_dwordx2 v[56:57], v249, s[44:47], s73 offen sc1\n\t"
      "buffer_store_dwordx2 v[52:53], v249, s[36:39], s69 offen sc1\n\t"
      "buffer_store_dwordx2 v[200:201], v249, s[36:39], s70 offen sc1\n\t"
      "buffer_store_dwordx2 v[58:59], v249, s[44:47], s73 offen offset:512 sc1\n\t"
      "buffer_store_dwordx2 v[54:55], v249, s[36:39], s69 offen offset:512 sc1\n\t"
      "buffer_store_dwordx2 v[202:203], v249, s[36:39], s70 offen offset:512 sc1\n\t"
      "s_mov_b32 s49, s89\n\t"
      "s_add_u32 s89, s89, 16\n\t"
      "s_lshr_b32 s69, s89, 4\n\t"
      "s_cmp_eq_u32 s69, 3\n\t"
      "s_cselect_b32 s89, s93, s89\n\t"
      "s_cmp_ge_u32 s49, 48\n\t"
      "s_cselect_b32 s60, s94, s60\n\t"
      "s_cselect_b32 s54, s95, s54\n\t"
      "s_mov_b32 s65, 0\n\t"
      "s_branch WGO2%=\n\t"
      ".p2align 5\n\t"
      "WOM1%=:\n\t"
      "s_mov_b32 s65, 2\n\t"
      "s_cmp_lg_u32 s96, 0\n\t"
      "s_cbranch_scc1 WOL3%=\n\t"
      "WOL2%=:\n\t"
      "s_waitcnt lgkmcnt(11)\n\t"
      "v_mfma_f32_16x16x32_bf16 v[0:3], a[0:3], v[64:67], 0\n\t"
      "v_mfma_f32_16x16x32_bf16 v[16:19], a[224:227], v[160:163], 0\n\t"
      "v_mfma_f32_16x16x32_bf16 v[4:7], a[64:67], v[64:67], 0\n\t"
      "v_mfma_f32_16x16x32_bf16 v[20:23], a[228:231], v[160:163], 0\n\t"
      "v_and_b32 v193, s74, v224\n\t"
      "v_lshlrev_b32 v192, 16, v224\n\t"
      "ds_read_b128 v[112:115], v255 offset:12288\n\t"
      "s_waitcnt lgkmcnt(11)\n\t"
      "v_mfma_f32_16x16x32_bf16 v[8:11], a[0:3], v[68:71], 0\n\t"
      "v_mfma_f32_16x16x32_bf16 v[16:19], a[224:227], v[164:167], v[16:19]\n\t"
      "v_mfma_f32_16x16x32_bf16 v[12:15], a[64:67], v[68:71], 0\n\t"
      "v_mfma_f32_16x16x32_bf16 v[20:23], a[228:231], v[164:167], v[20:23]\n\t"
      "v_and_b32 v195, s74, v225\n\t"
      "v_lshlrev_b32 v194, 16, v225\n\t"
      "ds_read_b128 v[116:119], v255 offset:13312\n\t"
      "s_waitcnt lgkmcnt(11)\n\t"
      "v_mfma_f32_16x16x32_bf16 v[0:3], a[4:7], v[72:75], v[0:3]\n\t"
      "v_mfma_f32_16x16x32_bf16 v[16:19], a[224:227], v[168:171], v[16:19]\n\t"
      "v_mfma_f32_16x16x32_bf16 v[4:7], a[68:71], v[72:75], v[4:7]\n\t"
      "v_mfma_f32_16x16x32_bf16 v[20:23], a[228:231], v[168:171], v[20:23]\n\t"
      "v_and_b32 v197, s74, v226\n\t"
      "v_lshlrev_b32 v196, 16, v226\n\t"
      "ds_read_b128 v[120:123], v255 offset:14336\n\t"
      "s_waitcnt lgkmcnt(11)\n\t"
      "v_mfma_f32_16x16x32_bf16 v[8:11], a[4:7], v[76:79], v[8:11]\n\t"
      "v_mfma_f32_16x16x32_bf16 v[16:19], a[224:227], v[172:175], v[16:19]\n\t"
      "v_mfma_f32_16x16x32_bf16 v[12:15], a[68:71], v[76:79], v[12:15]\n\t"
      "v_mfma_f32_16x16x32_bf16 v[20:23], a[228:231], v[172:175], v[20:23]\n\t"
      "v_and_b32 v199, s74, v227\n\t"
      "v_lshlrev_b32 v198, 16, v227\n\t"
      "ds_read_b128 v[124:127], v255 offset:15360\n\t"
      "s_waitcnt lgkmcnt(11)\n\t"
      "v_mfma_f32_16x16x32_bf16 v[0:3], a[8:11], v[80:83], v[0:3]\n\t"
      "v_mfma_f32_16x16x32_bf16 v[16:19], a[224:227], v[176:179], v[16:19]\n\t"
      "v_mfma_f32_16x16x32_bf16 v[4:7], a[72:75], v[80:83], v[4:7]\n\t"
      "v_mfma_f32_16x16x32_bf16 v[20:23], a[228:231], v[176:179], v[20:23]\n\t"
      "ds_read_b128 v[64:67], v255 offset:16384\n\t"
      "s_waitcnt lgkmcnt(11)\n\t"
      "v_mfma_f32_16x16x32_bf16 v[8:11], a[8:11], v[84:87], v[8:11]\n\t"
      "v_mfma_f32_16x16x32_bf16 v[16:19], a[224:227], v[180:183], v[16:19]\n\t"
      "v_mfma_f32_16x16x32_bf16 v[12:15], a[72:75], v[84:87], v[12:15]\n\t"
      "v_mfma_f32_16x16x32_bf16 v[20:23], a[228:231], v[180:183], v[20:23]\n\t"
      "ds_read_b128 v[68:71], v255 offset:17408\n\t"
      "s_waitcnt lgkmcnt(11)\n\t"
      "v_mfma_f32_16x16x32_bf16 v[0:3], a[12:15], v[88:91], v[0:3]\n\t"
      "v_mfma_f32_16x16x32_bf16 v[16:19], a[224:227], v[184:187], v[16:19]\n\t"
      "v_mfma_f32_16x16x32_bf16 v[4:7], a[76:79], v[88:91], v[4:7]\n\t"
      "v_mfma_f32_16x16x32_bf16 v[20:23], a[228:231], v[184:187], v[20:23]\n\t"
      "ds_read_b128 v[72:75], v255 offset:18432\n\t"
      "s_waitcnt lgkmcnt(11)\n\t"
      "v_mfma_f32_16x16x32_bf16 v[8:11], a[12:15], v[92:95], v[8:11]\n\t"
      "v_mfma_f32_16x16x32_bf16 v[16:19], a[224:227], v[188:191], v[16:19]\n\t"
      "v_mfma_f32_16x16x32_bf16 v[12:15], a[76:79], v[92:95], v[12:15]\n\t"
      "v_mfma_f32_16x16x32_bf16 v[20:23], a[228:231], v[188:191], v[20:23]\n\t"
      "ds_read_b128 v[76:79], v255 offset:19456\n\t"
      "s_waitcnt lgkmcnt(11)\n\t"
      "v_mfma_f32_16x16x32_bf16 v[0:3], a[16:19], v[96:99], v[0:3]\n\t"
      "v_cvt_pk_bf16_f32 v32, v16, v17\n\t"
      "v_cvt_pk_bf16_f32 v34, v18, v19\n\t"
      "v_mfma_f32_16x16x32_bf16 v[4:7], a[80:83], v[96:99], v[4:7]\n\t"
      "ds_read_b128 v[80:83], v255 offset:20480\n\t"
      "s_waitcnt lgkmcnt(11)\n\t"
      "v_mfma_f32_16x16x32_bf16 v[8:11], a[16:19], v[100:103], v[8:11]\n\t"
      "v_cvt_pk_bf16_f32 v36, v20, v21\n\t"
      "v_cvt_pk_bf16_f32 v38, v22, v23\n\t"
      "v_mfma_f32_16x16x32_bf16 v[12:15], a[80:83], v[100:103], v[12:15]\n\t"
      "v_and_b32 v33, s74, v32\n\t"
      "v_lshlrev_b32 v32, 16, v32\n\t"
      "ds_read_b128 v[84:87], v255 offset:21504\n\t"
      "s_waitcnt lgkmcnt(11)\n\t"
      "v_mfma_f32_16x16x32_bf16 v[0:3], a[20:23], v[104:107], v[0:3]\n\t"
      "v_and_b32 v35, s74, v34\n\t"
      "v_lshlrev_b32 v34, 16, v34\n\t"
      "v_mfma_f32_16x16x32_bf16 v[4:7], a[84:87], v[104:107], v[4:7]\n\t"
      "v_and_b32 v37, s74, v36\n\t"
      "v_lshlrev_b32 v36, 16, v36\n\t"
      "ds_read_b128 v[88:91], v255 offset:22528\n\t"
      "s_waitcnt lgkmcnt(11)\n\t"
      "v_mfma_f32_16x16x32_bf16 v[8:11], a[20:23], v[108:111], v[8:11]\n\t"
      "v_and_b32 v39, s74, v38\n\t"
      "v_lshlrev_b32 v38, 16, v38\n\t"
      "v_mfma_f32_16x16x32_bf16 v[12:15], a[84:87], v[108:111], v[12:15]\n\t"
      "v_add_f32 v192, v192, v32\n\t"
      "v_add_f32 v193, v193, v33\n\t"
      "ds_read_b128 v[92:95], v255 offset:23552\n\t"
      "s_waitcnt lgkmcnt(11)\n\t"
      "v_mfma_f32_16x16x32_bf16 v[0:3], a[24:27], v[112:115], v[0:3]\n\t"
      "v_add_f32 v194, v194, v34\n\t"
      "v_add_f32 v195, v195, v35\n\t"
      "v_mfma_f32_16x16x32_bf16 v[4:7], a[88:91], v[112:115], v[4:7]\n\t"
      "v_add_f32 v196, v196, v36\n\t"
      "v_add_f32 v197, v197, v37\n\t"
      "ds_read_b128 v[96:99], v255 offset:24576\n\t"
      "s_waitcnt lgkmcnt(11)\n\t"
      "v_mfma_f32_16x16x32_bf16 v[8:11], a[24:27], v[116:119], v[8:11]\n\t"
      "v_add_f32 v198, v198, v38\n\t"
      "v_add_f32 v199, v199, v39\n\t"
      "v_mfma_f32_16x16x32_bf16 v[12:15], a[88:91], v[116:119], v[12:15]\n\t"
      "v_cvt_pk_bf16_f32 v200, v192, v193\n\t"
      "v_cvt_pk_bf16_f32 v201, v194, v195\n\t"
      "ds_read_b128 v[100:103], v255 offset:25600\n\t"
      "s_waitcnt lgkmcnt(11)\n\t"
      "v_mfma_f32_16x16x32_bf16 v[0:3], a[28:31], v[120:123], v[0:3]\n\t"
      "v_cvt_pk_bf16_f32 v202, v196, v197\n\t"
      "v_cvt_pk_bf16_f32 v203, v198, v199\n\t"
      "v_mfma_f32_16x16x32_bf16 v[4:7], a[92:95], v[120:123], v[4:7]\n\t"
      "v_and_b32 v193, s74, v200\n\t"
      "v_lshlrev_b32 v192, 16, v200\n\t"
      "ds_read_b128 v[104:107], v255 offset:26624\n\t"
      "s_waitcnt lgkmcnt(11)\n\t"
      "v_mfma_f32_16x16x32_bf16 v[8:11], a[28:31], v[124:127], v[8:11]\n\t"
      "v_and_b32 v195, s74, v201\n\t"
      "v_lshlrev_b32 v194, 16, v201\n\t"
      "v_mfma_f32_16x16x32_bf16 v[12:15], a[92:95], v[124:127], v[12:15]\n\t"
      "v_and_b32 v197, s74, v202\n\t"
      "v_lshlrev_b32 v196, 16, v202\n\t"
      "ds_read_b128 v[108:111], v255 offset:27648\n\t"
      "s_waitcnt lgkmcnt(11)\n\t"
      "v_mfma_f32_16x16x32_bf16 v[0:3], a[32:35], v[64:67], v[0:3]\n\t"
      "v_and_b32 v199, s74, v203\n\t"
      "v_lshlrev_b32 v198, 16, v203\n\t"
      "v_mfma_f32_16x16x32_bf16 v[4:7], a[96:99], v[64:67], v[4:7]\n\t"
      "v_add_f32 v32, v192, v196\n\t"
      "v_add_f32 v33, v193, v197\n\t"
      "ds_read_b128 v[112:115], v255 offset:28672\n\t"
      "s_waitcnt lgkmcnt(11)\n\t"
      "v_mfma_f32_16x16x32_bf16 v[8:11], a[32:35], v[68:71], v[8:11]\n\t"
      "v_add_f32 v34, v194, v198\n\t"
      "v_add_f32 v35, v195, v199\n\t"
      "v_mfma_f32_16x16x32_bf16 v[12:15], a[96:99], v[68:71], v[12:15]\n\t"
      "v_add_f32 v32, v32, v34\n\t"
      "v_add_f32 v33, v33, v35\n\t"
      "ds_read_b128 v[116:119], v255 offset:29696\n\t"
      "s_waitcnt lgkmcnt(11)\n\t"
      "v_mfma_f32_16x16x32_bf16 v[0:3], a[36:39], v[72:75], v[0:3]\n\t"
      "v_add_f32 v32, v32, v33\n\t"
      "v_mfma_f32_16x16x32_bf16 v[4:7], a[100:103], v[72:75], v[4:7]\n\t"
      "ds_read_b128 v[120:123], v255 offset:30720\n\t"
      "s_waitcnt lgkmcnt(11)\n\t"
      "v_mfma_f32_16x16x32_bf16 v[8:11], a[36:39], v[76:79], v[8:11]\n\t"
      "v_add_f32_dpp v32, v32, v32 quad_perm:[1,0,3,2] row_mask:0xf bank_mask:0xf\n\t"
      "v_mfma_f32_16x16x32_bf16 v[12:15], a[100:103], v[76:79], v[12:15]\n\t"
      "ds_read_b128 v[124:127], v255 offset:31744\n\t"
      "s_waitcnt lgkmcnt(11)\n\t"
      "v_mfma_f32_16x16x32_bf16 v[0:3], a[40:43], v[80:83], v[0:3]\n\t"
      "v_add_f32_dpp v32, v32, v32 quad_perm:[2,3,0,1] row_mask:0xf bank_mask:0xf\n\t"
      "v_mfma_f32_16x16x32_bf16 v[4:7], a[104:107], v[80:83], v[4:7]\n\t"
      "s_waitcnt lgkmcnt(10)\n\t"
      "v_mfma_f32_16x16x32_bf16 v[8:11], a[40:43], v[84:87], v[8:11]\n\t"
      "v_add_f32_dpp v32, v32, v32 row_ror:4 row_mask:0xf bank_mask:0xf\n\t"
      "v_mfma_f32_16x16x32_bf16 v[12:15], a[104:107], v[84:87], v[12:15]\n\t"
      "s_waitcnt lgkmcnt(9)\n\t"
      "v_mfma_f32_16x16x32_bf16 v[0:3], a[44:47], v[88:91], v[0:3]\n\t"
      "v_add_f32_dpp v32, v32, v32 row_ror:8 row_mask:0xf bank_mask:0xf\n\t"
      "v_mov_b32 v33, v32\n\t"
      "v_mfma_f32_16x16x32_bf16 v[4:7], a[108:111], v[88:91], v[4:7]\n\t"
      "s_waitcnt lgkmcnt(8)\n\t"
      "v_mfma_f32_16x16x32_bf16 v[8:11], a[44:47], v[92:95], v[8:11]\n\t"
      "v_permlane16_swap_b32 v32, v33\n\t"
      "v_add_f32 v32, v32, v33\n\t"
      "v_mfma_f32_16x16x32_bf16 v[12:15], a[108:111], v[92:95], v[12:15]\n\t"
      "v_mov_b32 v33, v32\n\t"
      "s_waitcnt lgkmcnt(7)\n\t"
      "v_mfma_f32_16x16x32_bf16 v[0:3], a[48:51], v[96:99], v[0:3]\n\t"
      "v_permlane32_swap_b32 v32, v33\n\t"
      "v_add_f32 v32, v32, v33\n\t"
      "v_mfma_f32_16x16x32_bf16 v[4:7], a[112:115], v[96:99], v[4:7]\n\t"
      "v_mul_f32 v204, 0x3b000000, v32\n\t"
      "v_sub_f32 v40, v192, v204\n\t"
      "s_waitcnt lgkmcnt(6)\n\t"
      "v_mfma_f32_16x16x32_bf16 v[8:11], a[48:51], v[100:103], v[8:11]\n\t"
      "v_sub_f32 v41, v193, v204\n\t"
      "v_sub_f32 v42, v194, v204\n\t"
      "v_mfma_f32_16x16x32_bf16 v[12:15], a[112:115], v[100:103], v[12:15]\n\t"
      "v_sub_f32 v43, v195, v204\n\t"
      "v_sub_f32 v44, v196, v204\n\t"
      "s_waitcnt lgkmcnt(5)\n\t"
      "v_mfma_f32_16x16x32_bf16 v[0:3], a[52:55], v[104:107], v[0:3]\n\t"
      "v_sub_f32 v45, v197, v204\n\t"
      "v_sub_f32 v46, v198, v204\n\t"
      "v_mfma_f32_16x16x32_bf16 v[4:7], a[116:119], v[104:107], v[4:7]\n\t"
      "v_sub_f32 v47, v199, v204\n\t"
      "v_mul_f32 v32, v40, v40\n\t"
      "s_waitcnt lgkmcnt(4)\n\t"
      "v_mfma_f32_16x16x32_bf16 v[8:11], a[52:55], v[108:111], v[8:11]\n\t"
      "v_mul_f32 v33, v41, v41\n\t"
      "v_fma_f32 v32, v42, v42, v32\n\t"
      "v_mfma_f32_16x16x32_bf16 v[12:15], a[116:119], v[108:111], v[12:15]\n\t"
      "v_fma_f32 v33, v43, v43, v33\n\t"
      "v_fma_f32 v32, v44, v44, v32\n\t"
      "s_waitcnt lgkmcnt(3)\n\t"
      "v_mfma_f32_16x16x32_bf16 v[0:3], a[56:59], v[112:115], v[0:3]\n\t"
      "v_fma_f32 v33, v45, v45, v33\n\t"
      "v_fma_f32 v32, v46, v46, v32\n\t"
      "v_mfma_f32_16x16x32_bf16 v[4:7], a[120:123], v[112:115], v[4:7]\n\t"
      "v_fma_f32 v33, v47, v47, v33\n\t"
      "v_add_f32 v32, v32, v33\n\t"
      "s_waitcnt lgkmcnt(2)\n\t"
      "v_mfma_f32_16x16x32_bf16 v[8:11], a[56:59], v[116:119], v[8:11]\n\t"
      "v_add_f32_dpp v32, v32, v32 quad_perm:[1,0,3,2] row_mask:0xf bank_mask:0xf\n\t"
      "v_mfma_f32_16x16x32_bf16 v[12:15], a[120:123], v[116:119], v[12:15]\n\t"
      "s_waitcnt lgkmcnt(1)\n\t"
      "v_mfma_f32_16x16x32_bf16 v[0:3], a[60:63], v[120:123], v[0:3]\n\t"
      "v_add_f32_dpp v32, v32, v32 quad_perm:[2,3,0,1] row_mask:0xf bank_mask:0xf\n\t"
      "v_mfma_f32_16x16x32_bf16 v[4:7], a[124:127], v[120:123], v[4:7]\n\t"
      "s_waitcnt lgkmcnt(0)\n\t"
      "v_mfma_f32_16x16x32_bf16 v[8:11], a[60:63], v[124:127], v[8:11]\n\t"
      "v_add_f32_dpp v32, v32, v32 row_ror:4 row_mask:0xf bank_mask:0xf\n\t"
      "v_mfma_f32_16x16x32_bf16 v[12:15], a[124:127], v[124:127], v[12:15]\n\t"
      "s_nop 0\n\t"
      "v_add_f32_dpp v32, v32, v32 row_ror:8 row_mask:0xf bank_mask:0xf\n\t"
      "v_mov_b32 v33, v32\n\t"
      "s_nop 1\n\t"
      "v_permlane16_swap_b32 v32, v33\n\t"
      "v_add_f32 v32, v32, v33\n\t"
      "v_mov_b32 v33, v32\n\t"
      "s_nop 1\n\t"
      "v_permlane32_swap_b32 v32, v33\n\t"
      "v_add_f32 v32, v32, v33\n\t"
      "v_mul_f32 v205, 0x3b000000, v32\n\t"
      "v_xor_b32 v254, 0x8000, v254\n\t"
      "v_xor_b32 v255, 0x8000, v255\n\t"
      "s_add_u32 s69, s53, 0x4000\n\t"
      "s_add_u32 s48, s48, 4\n\t"
      "s_waitcnt vmcnt(40)\n\t"
      "v_cvt_pk_bf16_f32 v0, v0, v1\n\t"
      "v_cvt_pk_bf16_f32 v1, v2, v3\n\t"
      "v_cvt_pk_bf16_f32 v2, v4, v5\n\t"
      "v_cvt_pk_bf16_f32 v3, v6, v7\n\t"
      "v_min_u32 v0, -2, v0\n\t"
      "v_min_u32 v1, -2, v1\n\t"
      "v_min_u32 v2, -2, v2\n\t"
      "v_min_u32 v3, -2, v3\n\t"
      "buffer_store_dwordx4 v[0:3], v238, s[36:39], s53 offen sc1\n\t"
      "v_cvt_pk_bf16_f32 v8, v8, v9\n\t"
      "v_cvt_pk_bf16_f32 v9, v10, v11\n\t"
      "v_cvt_pk_bf16_f32 v10, v12, v13\n\t"
      "v_cvt_pk_bf16_f32 v11, v14, v15\n\t"
      "v_min_u32 v8, -2, v8\n\t"
      "v_min_u32 v9, -2, v9\n\t"
      "v_min_u32 v10, -2, v10\n\t"
      "v_min_u32 v11, -2, v11\n\t"
      "buffer_store_dwordx4 v[8:11], v238, s[36:39], s69 offen sc1\n\t"
      "s_add_u32 s53, s53, 1048576\n\t"
      "s_nop 1\n\t"
      "s_branch WTOP%=\n\t"
      ".p2align 5\n\t"
      "WOL3%=:\n\t"
      "buffer_load_dwordx4 v[128:131], v252, s[36:39], s51 offen sc1\n\t"
      "s_waitcnt lgkmcnt(11)\n\t"
      "v_mfma_f32_16x16x32_bf16 v[0:3], a[0:3], v[64:67], 0\n\t"
      "v_mfma_f32_16x16x32_bf16 v[16:19], a[224:227], v[160:163], 0\n\t"
      "v_mfma_f32_16x16x32_bf16 v[4:7], a[64:67], v[64:67], 0\n\t"
      "v_mfma_f32_16x16x32_bf16 v[20:23], a[228:231], v[160:163], 0\n\t"
      "v_and_b32 v193, s74, v224\n\t"
      "v_lshlrev_b32 v192, 16, v224\n\t"
      "ds_read_b128 v[112:115], v255 offset:12288\n\t"
      "s_waitcnt lgkmcnt(11)\n\t"
      "v_mfma_f32_16x16x32_bf16 v[8:11], a[0:3], v[68:71], 0\n\t"
      "v_mfma_f32_16x16x32_bf16 v[16:19], a[224:227], v[164:167], v[16:19]\n\t"
      "v_mfma_f32_16x16x32_bf16 v[12:15], a[64:67], v[68:71], 0\n\t"
      "v_mfma_f32_16x16x32_bf16 v[20:23], a[228:231], v[164:167], v[20:23]\n\t"
      "v_and_b32 v195, s74, v225\n\t"
      "v_lshlrev_b32 v194, 16, v225\n\t"
      "ds_read_b128 v[116:119], v255 offset:13312\n\t"
      "s_waitcnt lgkmcnt(11)\n\t"
      "v_mfma_f32_16x16x32_bf16 v[0:3], a[4:7], v[72:75], v[0:3]\n\t"
      "v_mfma_f32_16x16x32_bf16 v[16:19], a[224:227], v[168:171], v[16:19]\n\t"
      "v_mfma_f32_16x16x32_bf16 v[4:7], a[68:71], v[72:75], v[4:7]\n\t"
      "v_mfma_f32_16x16x32_bf16 v[20:23], a[228:231], v[168:171], v[20:23]\n\t"
      "v_and_b32 v197, s74, v226\n\t"
      "v_lshlrev_b32 v196, 16, v226\n\t"
      "ds_read_b128 v[120:123], v255 offset:14336\n\t"
      "s_waitcnt lgkmcnt(11)\n\t"
      "v_mfma_f32_16x16x32_bf16 v[8:11], a[4:7], v[76:79], v[8:11]\n\t"
      "v_mfma_f32_16x16x32_bf16 v[16:19], a[224:227], v[172:175], v[16:19]\n\t"
      "v_mfma_f32_16x16x32_bf16 v[12:15], a[68:71], v[76:79], v[12:15]\n\t"
      "buffer_load_dwordx4 v[132:135], v252, s[36:39], s51 offen offset:512 sc1\n\t"
      "ds_read_b128 v[124:127], v255 offset:15360\n\t"
      "s_waitcnt lgkmcnt(11)\n\t"
      "v_mfma_f32_16x16x32_bf16 v[0:3], a[8:11], v[80:83], v[0:3]\n\t"
      "v_mfma_f32_16x16x32_bf16 v[20:23], a[228:231], v[172:175], v[20:23]\n\t"
      "v_and_b32 v199, s74, v227\n\t"
      "v_lshlrev_b32 v198, 16, v227\n\t"
      "v_mfma_f32_16x16x32_bf16 v[4:7], a[72:75], v[80:83], v[4:7]\n\t"
      "v_mfma_f32_16x16x32_bf16 v[16:19], a[224:227], v[176:179], v[16:19]\n\t"
      "ds_read_b128 v[64:67], v255 offset:16384\n\t"
      "s_waitcnt lgkmcnt(11)\n\t"
      "v_mfma_f32_16x16x32_bf16 v[8:11], a[8:11], v[84:87], v[8:11]\n\t"
      "v_mfma_f32_16x16x32_bf16 v[20:23], a[228:231], v[176:179], v[20:23]\n\t"
      "v_mfma_f32_16x16x32_bf16 v[12:15], a[72:75], v[84:87], v[12:15]\n\t"
      "v_mfma_f32_16x16x32_bf16 v[16:19], a[224:227], v[180:183], v[16:19]\n\t"
      "ds_read_b128 v[68:71], v255 offset:17408\n\t"
      "s_waitcnt lgkmcnt(11)\n\t"
      "v_mfma_f32_16x16x32_bf16 v[0:3], a[12:15], v[88:91], v[0:3]\n\t"
      "v_mfma_f32_16x16x32_bf16 v[20:23], a[228:231], v[180:183], v[20:23]\n\t"
      "v_mfma_f32_16x16x32_bf16 v[4:7], a[76:79], v[88:91], v[4:7]\n\t"
      "v_mfma_f32_16x16x32_bf16 v[16:19], a[224:227], v[184:187], v[16:19]\n\t"
      "ds_read_b128 v[72:75], v255 offset:18432\n\t"
      "s_waitcnt lgkmcnt(11)\n\t"
      "v_mfma_f32_16x16x32_bf16 v[8:11], a[12:15], v[92:95], v[8:11]\n\t"
      "v_mfma_f32_16x16x32_bf16 v[20:23], a[228:231], v[184:187], v[20:23]\n\t"
      "v_mfma_f32_16x16x32_bf16 v[12:15], a[76:79], v[92:95], v[12:15]\n\t"
      "buffer_load_dwordx4 v[136:139], v252, s[36:39], s51 offen offset:1024 sc1\n\t"
      "ds_read_b128 v[76:79], v255 offset:19456\n\t"
      "s_waitcnt lgkmcnt(11)\n\t"
      "v_mfma_f32_16x16x32_bf16 v[0:3], a[16:19], v[96:99], v[0:3]\n\t"
      "v_mfma_f32_16x16x32_bf16 v[16:19], a[224:227], v[188:191], v[16:19]\n\t"
      "v_mfma_f32_16x16x32_bf16 v[4:7], a[80:83], v[96:99], v[4:7]\n\t"
      "v_mfma_f32_16x16x32_bf16 v[20:23], a[228:231], v[188:191], v[20:23]\n\t"
      "ds_read_b128 v[80:83], v255 offset:20480\n\t"
      "s_waitcnt lgkmcnt(11)\n\t"
      "v_mfma_f32_16x16x32_bf16 v[8:11], a[16:19], v[100:103], v[8:11]\n\t"
      "v_cvt_pk_bf16_f32 v32, v16, v17\n\t"
      "v_cvt_pk_bf16_f32 v34, v18, v19\n\t"
      "v_mfma_f32_16x16x32_bf16 v[12:15], a[80:83], v[100:103], v[12:15]\n\t"
      "ds_read_b128 v[84:87], v255 offset:21504\n\t"
      "s_waitcnt lgkmcnt(11)\n\t"
      "v_mfma_f32_16x16x32_bf16 v[0:3], a[20:23], v[104:107], v[0:3]\n\t"
      "v_cvt_pk_bf16_f32 v36, v20, v21\n\t"
      "v_cvt_pk_bf16_f32 v38, v22, v23\n\t"
      "v_mfma_f32_16x16x32_bf16 v[4:7], a[84:87], v[104:107], v[4:7]\n\t"
      "v_and_b32 v33, s74, v32\n\t"
      "v_lshlrev_b32 v32, 16, v32\n\t"
      "ds_read_b128 v[88:91], v255 offset:22528\n\t"
      "s_waitcnt lgkmcnt(11)\n\t"
      "v_mfma_f32_16x16x32_bf16 v[8:11], a[20:23], v[108:111], v[8:11]\n\t"
      "v_and_b32 v35, s74, v34\n\t"
      "v_lshlrev_b32 v34, 16, v34\n\t"
      "v_mfma_f32_16x16x32_bf16 v[12:15], a[84:87], v[108:111], v[12:15]\n\t"
      "buffer_load_dwordx4 v[140:143], v252, s[36:39], s51 offen offset:1536 sc1\n\t"
      "ds_read_b128 v[92:95], v255 offset:23552\n\t"
      "s_waitcnt lgkmcnt(11)\n\t"
      "v_mfma_f32_16x16x32_bf16 v[0:3], a[24:27], v[112:115], v[0:3]\n\t"
      "v_and_b32 v37, s74, v36\n\t"
      "v_lshlrev_b32 v36, 16, v36\n\t"
      "v_mfma_f32_16x16x32_bf16 v[4:7], a[88:91], v[112:115], v[4:7]\n\t"
      "v_and_b32 v39, s74, v38\n\t"
      "v_lshlrev_b32 v38, 16, v38\n\t"
      "ds_read_b128 v[96:99], v255 offset:24576\n\t"
      "s_waitcnt lgkmcnt(11)\n\t"
      "v_mfma_f32_16x16x32_bf16 v[8:11], a[24:27], v[116:119], v[8:11]\n\t"
      "v_add_f32 v192, v192, v32\n\t"
      "v_add_f32 v193, v193, v33\n\t"
      "v_mfma_f32_16x16x32_bf16 v[12:15], a[88:91], v[116:119], v[12:15]\n\t"
      "v_add_f32 v194, v194, v34\n\t"
      "v_add_f32 v195, v195, v35\n\t"
      "ds_read_b128 v[100:103], v255 offset:25600\n\t"
      "s_waitcnt lgkmcnt(11)\n\t"
      "v_mfma_f32_16x16x32_bf16 v[0:3], a[28:31], v[120:123], v[0:3]\n\t"
      "v_add_f32 v196, v196, v36\n\t"
      "v_add_f32 v197, v197, v37\n\t"
      "v_mfma_f32_16x16x32_bf16 v[4:7], a[92:95], v[120:123], v[4:7]\n\t"
      "v_add_f32 v198, v198, v38\n\t"
      "v_add_f32 v199, v199, v39\n\t"
      "ds_read_b128 v[104:107], v255 offset:26624\n\t"
      "s_waitcnt lgkmcnt(11)\n\t"
      "v_mfma_f32_16x16x32_bf16 v[8:11], a[28:31], v[124:127], v[8:11]\n\t"
      "buffer_load_dwordx4 v[144:147], v253, s[36:39], s52 offen sc1\n\t"
      "v_mfma_f32_16x16x32_bf16 v[12:15], a[92:95], v[124:127], v[12:15]\n\t"
      "v_cvt_pk_bf16_f32 v200, v192, v193\n\t"
      "v_cvt_pk_bf16_f32 v201, v194, v195\n\t"
      "ds_read_b128 v[108:111], v255 offset:27648\n\t"
      "s_waitcnt lgkmcnt(11)\n\t"
      "v_mfma_f32_16x16x32_bf16 v[0:3], a[32:35], v[64:67], v[0:3]\n\t"
      "v_cvt_pk_bf16_f32 v202, v196, v197\n\t"
      "v_cvt_pk_bf16_f32 v203, v198, v199\n\t"
      "v_mfma_f32_16x16x32_bf16 v[4:7], a[96:99], v[64:67], v[4:7]\n\t"
      "v_and_b32 v193, s74, v200\n\t"
      "v_lshlrev_b32 v192, 16, v200\n\t"
      "ds_read_b128 v[112:115], v255 offset:28672\n\t"
      "s_waitcnt lgkmcnt(11)\n\t"
      "v_mfma_f32_16x16x32_bf16 v[8:11], a[32:35], v[68:71], v[8:11]\n\t"
      "v_and_b32 v195, s74, v201\n\t"
      "v_lshlrev_b32 v194, 16, v201\n\t"
      "v_mfma_f32_16x16x32_bf16 v[12:15], a[96:99], v[68:71], v[12:15]\n\t"
      "v_and_b32 v197, s74, v202\n\t"
      "v_lshlrev_b32 v196, 16, v202\n\t"
      "ds_read_b128 v[116:119], v255 offset:29696\n\t"
      "s_waitcnt lgkmcnt(11)\n\t"
      "v_mfma_f32_16x16x32_bf16 v[0:3], a[36:39], v[72:75], v[0:3]\n\t"
      "v_and_b32 v199, s74, v203\n\t"
      "v_lshlrev_b32 v198, 16, v203\n\t"
      "v_mfma_f32_16x16x32_bf16 v[4:7], a[100:103], v[72:75], v[4:7]\n\t"
      "v_add_f32 v32, v192, v196\n\t"
      "v_add_f32 v33, v193, v197\n\t"
      "ds_read_b128 v[120:123], v255 offset:30720\n\t"
      "s_waitcnt lgkmcnt(11)\n\t"
      "v_mfma_f32_16x16x32_bf16 v[8:11], a[36:39], v[76:79], v[8:11]\n\t"
      "buffer_load_dwordx4 v[148:151], v253, s[36:39], s52 offen offset:512 sc1\n\t"
      "v_mfma_f32_16x16x32_bf16 v[12:15], a[100:103], v[76:79], v[12:15]\n\t"
      "v_add_f32 v34, v194, v198\n\t"
      "v_add_f32 v35, v195, v199\n\t"
      "ds_read_b128 v[124:127], v255 offset:31744\n\t"
      "s_waitcnt lgkmcnt(11)\n\t"
      "v_mfma_f32_16x16x32_bf16 v[0:3], a[40:43], v[80:83], v[0:3]\n\t"
      "v_add_f32 v32, v32, v34\n\t"
      "v_add_f32 v33, v33, v35\n\t"
      "v_mfma_f32_16x16x32_bf16 v[4:7], a[104:107], v[80:83], v[4:7]\n\t"
      "v_add_f32 v32, v32, v33\n\t"
      "s_waitcnt lgkmcnt(10)\n\t"
      "v_mfma_f32_16x16x32_bf16 v[8:11], a[40:43], v[84:87], v[8:11]\n\t"
      "v_add_f32_dpp v32, v32, v32 quad_perm:[1,0,3,2] row_mask:0xf bank_mask:0xf\n\t"
      "v_mfma_f32_16x16x32_bf16 v[12:15], a[104:107], v[84:87], v[12:15]\n\t"
      "s_waitcnt lgkmcnt(9)\n\t"
      "v_mfma_f32_16x16x32_bf16 v[0:3], a[44:47], v[88:91], v[0:3]\n\t"
      "v_add_f32_dpp v32, v32, v32 quad_perm:[2,3,0,1] row_mask:0xf bank_mask:0xf\n\t"
      "v_mfma_f32_16x16x32_bf16 v[4:7], a[108:111], v[88:91], v[4:7]\n\t"
      "s_waitcnt lgkmcnt(8)\n\t"
      "v_mfma_f32_16x16x32_bf16 v[8:11], a[44:47], v[92:95], v[8:11]\n\t"
      "buffer_load_dwordx4 v[152:155], v253, s[36:39], s52 offen offset:1024 sc1\n\t"
      "v_mfma_f32_16x16x32_bf16 v[12:15], a[108:111], v[92:95], v[12:15]\n\t"
      "v_add_f32_dpp v32, v32, v32 row_ror:4 row_mask:0xf bank_mask:0xf\n\t"
      "s_waitcnt lgkmcnt(7)\n\t"
      "v_mfma_f32_16x16x32_bf16 v[0:3], a[48:51], v[96:99], v[0:3]\n\t"
      "v_add_f32_dpp v32, v32, v32 row_ror:8 row_mask:0xf bank_mask:0xf\n\t"
      "v_mov_b32 v33, v32\n\t"
      "v_mfma_f32_16x16x32_bf16 v[4:7], a[112:115], v[96:99], v[4:7]\n\t"
      "s_waitcnt lgkmcnt(6)\n\t"
      "v_mfma_f32_16x16x32_bf16 v[8:11], a[48:51], v[100:103], v[8:11]\n\t"
      "v_permlane16_swap_b32 v32, v33\n\t"
      "v_add_f32 v32, v32, v33\n\t"
      "v_mfma_f32_16x16x32_bf16 v[12:15], a[112:115], v[100:103], v[12:15]\n\t"
      "v_mov_b32 v33, v32\n\t"
      "s_waitcnt lgkmcnt(5)\n\t"
      "v_mfma_f32_16x16x32_bf16 v[0:3], a[52:55], v[104:107], v[0:3]\n\t"
      "v_permlane32_swap_b32 v32, v33\n\t"
      "v_add_f32 v32, v32, v33\n\t"
      "v_mfma_f32_16x16x32_bf16 v[4:7], a[116:119], v[104:107], v[4:7]\n\t"
      "v_mul_f32 v204, 0x3b000000, v32\n\t"
      "v_sub_f32 v40, v192, v204\n\t"
      "s_waitcnt lgkmcnt(4)\n\t"
      "v_mfma_f32_16x16x32_bf16 v[8:11], a[52:55], v[108:111], v[8:11]\n\t"
      "v_sub_f32 v41, v193, v204\n\t"
      "v_sub_f32 v42, v194, v204\n\t"
      "v_mfma_f32_16x16x32_bf16 v[12:15], a[116:119], v[108:111], v[12:15]\n\t"
      "buffer_load_dwordx4 v[156:159], v253, s[36:39], s52 offen offset:1536 sc1\n\t"
      "s_waitcnt lgkmcnt(3)\n\t"
      "v_mfma_f32_16x16x32_bf16 v[0:3], a[56:59], v[112:115], v[0:3]\n\t"
      "v_sub_f32 v43, v195, v204\n\t"
      "v_sub_f32 v44, v196, v204\n\t"
      "v_mfma_f32_16x16x32_bf16 v[4:7], a[120:123], v[112:115], v[4:7]\n\t"
      "v_sub_f32 v45, v197, v204\n\t"
      "v_sub_f32 v46, v198, v204\n\t"
      "s_waitcnt lgkmcnt(2)\n\t"
      "v_mfma_f32_16x16x32_bf16 v[8:11], a[56:59], v[116:119], v[8:11]\n\t"
      "v_sub_f32 v47, v199, v204\n\t"
      "v_mul_f32 v32, v40, v40\n\t"
      "v_mfma_f32_16x16x32_bf16 v[12:15], a[120:123], v[116:119], v[12:15]\n\t"
      "v_mul_f32 v33, v41, v41\n\t"
      "v_fma_f32 v32, v42, v42, v32\n\t"
      "s_waitcnt lgkmcnt(1)\n\t"
      "v_mfma_f32_16x16x32_bf16 v[0:3], a[60:63], v[120:123], v[0:3]\n\t"
      "v_fma_f32 v33, v43, v43, v33\n\t"
      "v_fma_f32 v32, v44, v44, v32\n\t"
      "v_mfma_f32_16x16x32_bf16 v[4:7], a[124:127], v[120:123], v[4:7]\n\t"
      "v_fma_f32 v33, v45, v45, v33\n\t"
      "v_fma_f32 v32, v46, v46, v32\n\t"
      "s_waitcnt lgkmcnt(0)\n\t"
      "v_mfma_f32_16x16x32_bf16 v[8:11], a[60:63], v[124:127], v[8:11]\n\t"
      "v_fma_f32 v33, v47, v47, v33\n\t"
      "v_add_f32 v32, v32, v33\n\t"
      "v_mfma_f32_16x16x32_bf16 v[12:15], a[124:127], v[124:127], v[12:15]\n\t"
      "s_nop 0\n\t"
      "v_add_f32_dpp v32, v32, v32 quad_perm:[1,0,3,2] row_mask:0xf bank_mask:0xf\n\t"
      "s_nop 1\n\t"
      "v_add_f32_dpp v32, v32, v32 quad_perm:[2,3,0,1] row_mask:0xf bank_mask:0xf\n\t"
      "s_nop 1\n\t"
      "v_add_f32_dpp v32, v32, v32 row_ror:4 row_mask:0xf bank_mask:0xf\n\t"
      "s_nop 1\n\t"
      "v_add_f32_dpp v32, v32, v32 row_ror:8 row_mask:0xf bank_mask:0xf\n\t"
      "v_mov_b32 v33, v32\n\t"
      "s_nop 1\n\t"
      "v_permlane16_swap_b32 v32, v33\n\t"
      "v_add_f32 v32, v32, v33\n\t"
      "v_mov_b32 v33, v32\n\t"
      "s_nop 1\n\t"
      "v_permlane32_swap_b32 v32, v33\n\t"
      "v_add_f32 v32, v32, v33\n\t"
      "v_mul_f32 v205, 0x3b000000, v32\n\t"
      "v_xor_b32 v254, 0x8000, v254\n\t"
      "v_xor_b32 v255, 0x8000, v255\n\t"
      "s_add_u32 s69, s53, 0x4000\n\t"
      "s_add_u32 s48, s48, 4\n\t"
      "s_waitcnt vmcnt(40)\n\t"
      "v_cvt_pk_bf16_f32 v0, v0, v1\n\t"
      "v_cvt_pk_bf16_f32 v1, v2, v3\n\t"
      "v_cvt_pk_bf16_f32 v2, v4, v5\n\t"
      "v_cvt_pk_bf16_f32 v3, v6, v7\n\t"
      "v_min_u32 v0, -2, v0\n\t"
      "v_min_u32 v1, -2, v1\n\t"
      "v_min_u32 v2, -2, v2\n\t"
      "v_min_u32 v3, -2, v3\n\t"
      "buffer_store_dwordx4 v[0:3], v238, s[36:39], s53 offen sc1\n\t"
      "v_cvt_pk_bf16_f32 v8, v8, v9\n\t"
      "v_cvt_pk_bf16_f32 v9, v10, v11\n\t"
      "v_cvt_pk_bf16_f32 v10, v12, v13\n\t"
      "v_cvt_pk_bf16_f32 v11, v14, v15\n\t"
      "v_min_u32 v8, -2, v8\n\t"
      "v_min_u32 v9, -2, v9\n\t"
      "v_min_u32 v10, -2, v10\n\t"
      "v_min_u32 v11, -2, v11\n\t"
      "buffer_store_dwordx4 v[8:11], v238, s[36:39], s69 offen sc1\n\t"
      "s_add_u32 s53, s53, 1048576\n\t"
      "s_nop 1\n\t"
      "s_branch WTOP%=\n\t"
      ".p2align 5\n\t"
      "WOM2%=:\n\t"
      "s_mov_b32 s65, 0\n\t"
      "s_add_u32 s24, s96, 8\n\t"
      "s_cmp_lg_u32 s96, 0\n\t"
      "s_cbranch_scc1 WOL5%=\n\t"
      "WOL4%=:\n\t"
      "s_waitcnt lgkmcnt(11)\n\t"
      "v_mfma_f32_16x16x32_bf16 v[0:3], a[0:3], v[64:67], 0\n\t"
      "v_cvt_pk_bf16_f32 v204, v204, v205\n\t"
      "v_and_b32 v205, s74, v204\n\t"
      "v_mfma_f32_16x16x32_bf16 v[4:7], a[64:67], v[64:67], 0\n\t"
      "v_lshlrev_b32 v204, 16, v204\n\t"
      "v_add_f32 v205, 0x3727c5ac, v205\n\t"
      "ds_read_b128 v[112:115], v255 offset:12288\n\t"
      "s_waitcnt lgkmcnt(11)\n\t"
      "v_mfma_f32_16x16x32_bf16 v[8:11], a[0:3], v[68:71], 0\n\t"
      "v_sub_f32 v40, v192, v204\n\t"
      "v_sub_f32 v41, v193, v204\n\t"
      "v_mfma_f32_16x16x32_bf16 v[12:15], a[64:67], v[68:71], 0\n\t"
      "v_sub_f32 v42, v194, v204\n\t"
      "v_sub_f32 v43, v195, v204\n\t"
      "ds_read_b128 v[116:119], v255 offset:13312\n\t"
      "s_waitcnt lgkmcnt(11)\n\t"
      "v_mfma_f32_16x16x32_bf16 v[0:3], a[4:7], v[72:75], v[0:3]\n\t"
      "v_sub_f32 v44, v196, v204\n\t"
      "v_sub_f32 v45, v197, v204\n\t"
      "v_mfma_f32_16x16x32_bf16 v[4:7], a[68:71], v[72:75], v[4:7]\n\t"
      "v_sub_f32 v46, v198, v204\n\t"
      "v_sub_f32 v47, v199, v204\n\t"
      "ds_read_b128 v[120:123], v255 offset:14336\n\t"
      "s_waitcnt lgkmcnt(11)\n\t"
      "v_mfma_f32_16x16x32_bf16 v[8:11], a[4:7], v[76:79], v[8:11]\n\t"
      "v_cvt_pk_bf16_f32 v205, v205, v205\n\t"
      "v_and_b32 v205, s74, v205\n\t"
      "v_mfma_f32_16x16x32_bf16 v[12:15], a[68:71], v[76:79], v[12:15]\n\t"
      "v_sqrt_f32 v205, v205\n\t"
      "ds_read_b128 v[124:127], v255 offset:15360\n\t"
      "s_waitcnt lgkmcnt(11)\n\t"
      "v_mfma_f32_16x16x32_bf16 v[0:3], a[8:11], v[80:83], v[0:3]\n\t"
      "v_cvt_pk_bf16_f32 v40, v40, v41\n\t"
      "v_cvt_pk_bf16_f32 v42, v42, v43\n\t"
      "v_mfma_f32_16x16x32_bf16 v[4:7], a[72:75], v[80:83], v[4:7]\n\t"
      "v_cvt_pk_bf16_f32 v44, v44, v45\n\t"
      "v_cvt_pk_bf16_f32 v46, v46, v47\n\t"
      "ds_read_b128 v[64:67], v255 offset:16384\n\t"
      "s_waitcnt lgkmcnt(11)\n\t"
      "v_mfma_f32_16x16x32_bf16 v[8:11], a[8:11], v[84:87], v[8:11]\n\t"
      "v_cvt_pk_bf16_f32 v205, v205, v205\n\t"
      "v_and_b32 v205, s74, v205\n\t"
      "v_mfma_f32_16x16x32_bf16 v[12:15], a[72:75], v[84:87], v[12:15]\n\t"
      "v_rcp_f32 v204, v205\n\t"
      "ds_read_b128 v[68:71], v255 offset:17408\n\t"
      "s_waitcnt lgkmcnt(11)\n\t"
      "v_mfma_f32_16x16x32_bf16 v[0:3], a[12:15], v[88:91], v[0:3]\n\t"
      "v_and_b32 v41, s74, v40\n\t"
      "v_lshlrev_b32 v40, 16, v40\n\t"
      "v_mfma_f32_16x16x32_bf16 v[4:7], a[76:79], v[88:91], v[4:7]\n\t"
      "v_and_b32 v43, s74, v42\n\t"
      "v_lshlrev_b32 v42, 16, v42\n\t"
      "ds_read_b128 v[72:75], v255 offset:18432\n\t"
      "s_waitcnt lgkmcnt(11)\n\t"
      "v_mfma_f32_16x16x32_bf16 v[8:11], a[12:15], v[92:95], v[8:11]\n\t"
      "v_and_b32 v45, s74, v44\n\t"
      "v_lshlrev_b32 v44, 16, v44\n\t"
      "v_mfma_f32_16x16x32_bf16 v[12:15], a[76:79], v[92:95], v[12:15]\n\t"
      "v_and_b32 v47, s74, v46\n\t"
      "v_lshlrev_b32 v46, 16, v46\n\t"
      "ds_read_b128 v[76:79], v255 offset:19456\n\t"
      "s_waitcnt lgkmcnt(11)\n\t"
      "v_mfma_f32_16x16x32_bf16 v[0:3], a[16:19], v[96:99], v[0:3]\n\t"
      "v_mul_f32 v40, v40, v204\n\t"
      "v_mul_f32 v41, v41, v204\n\t"
      "v_mfma_f32_16x16x32_bf16 v[4:7], a[80:83], v[96:99], v[4:7]\n\t"
      "v_mul_f32 v42, v42, v204\n\t"
      "v_mul_f32 v43, v43, v204\n\t"
      "ds_read_b128 v[80:83], v255 offset:20480\n\t"
      "s_waitcnt lgkmcnt(11)\n\t"
      "v_mfma_f32_16x16x32_bf16 v[8:11], a[16:19], v[100:103], v[8:11]\n\t"
      "v_mul_f32 v44, v44, v204\n\t"
      "v_mul_f32 v45, v45, v204\n\t"
      "v_mfma_f32_16x16x32_bf16 v[12:15], a[80:83], v[100:103], v[12:15]\n\t"
      "v_mul_f32 v46, v46, v204\n\t"
      "v_mul_f32 v47, v47, v204\n\t"
      "ds_read_b128 v[84:87], v255 offset:21504\n\t"
      "s_waitcnt lgkmcnt(11)\n\t"
      "v_mfma_f32_16x16x32_bf16 v[0:3], a[20:23], v[104:107], v[0:3]\n\t"
      "v_cvt_pk_bf16_f32 v52, v40, v41\n\t"
      "v_cvt_pk_bf16_f32 v53, v42, v43\n\t"
      "v_mfma_f32_16x16x32_bf16 v[4:7], a[84:87], v[104:107], v[4:7]\n\t"
      "v_cvt_pk_bf16_f32 v54, v44, v45\n\t"
      "v_cvt_pk_bf16_f32 v55, v46, v47\n\t"
      "ds_read_b128 v[88:91], v255 offset:22528\n\t"
      "s_waitcnt lgkmcnt(11)\n\t"
      "v_mfma_f32_16x16x32_bf16 v[8:11], a[20:23], v[108:111], v[8:11]\n\t"
      "v_and_b32 v33, s74, v52\n\t"
      "v_lshlrev_b32 v32, 16, v52\n\t"
      "v_mfma_f32_16x16x32_bf16 v[12:15], a[84:87], v[108:111], v[12:15]\n\t"
      "v_and_b32 v35, s74, v53\n\t"
      "v_lshlrev_b32 v34, 16, v53\n\t"
      "ds_read_b128 v[92:95], v255 offset:23552\n\t"
      "s_waitcnt lgkmcnt(11)\n\t"
      "v_mfma_f32_16x16x32_bf16 v[0:3], a[24:27], v[112:115], v[0:3]\n\t"
      "v_and_b32 v37, s74, v54\n\t"
      "v_lshlrev_b32 v36, 16, v54\n\t"
      "v_mfma_f32_16x16x32_bf16 v[4:7], a[88:91], v[112:115], v[4:7]\n\t"
      "v_and_b32 v39, s74, v55\n\t"
      "v_lshlrev_b32 v38, 16, v55\n\t"
      "ds_read_b128 v[96:99], v255 offset:24576\n\t"
      "s_waitcnt lgkmcnt(11)\n\t"
      "v_mfma_f32_16x16x32_bf16 v[8:11], a[24:27], v[116:119], v[8:11]\n\t"
      "v_mul_f32 v40, v32, v240\n\t"
      "v_mul_f32 v41, v33, v241\n\t"
      "v_mfma_f32_16x16x32_bf16 v[12:15], a[88:91], v[116:119], v[12:15]\n\t"
      "v_fma_f32 v40, v34, v242, v40\n\t"
      "v_fma_f32 v41, v35, v243, v41\n\t"
      "ds_read_b128 v[100:103], v255 offset:25600\n\t"
      "s_waitcnt lgkmcnt(11)\n\t"
      "v_mfma_f32_16x16x32_bf16 v[0:3], a[28:31], v[120:123], v[0:3]\n\t"
      "v_fma_f32 v40, v36, v244, v40\n\t"
      "v_fma_f32 v41, v37, v245, v41\n\t"
      "v_mfma_f32_16x16x32_bf16 v[4:7], a[92:95], v[120:123], v[4:7]\n\t"
      "v_fma_f32 v40, v38, v246, v40\n\t"
      "v_fma_f32 v41, v39, v247, v41\n\t"
      "ds_read_b128 v[104:107], v255 offset:26624\n\t"
      "s_waitcnt lgkmcnt(11)\n\t"
      "v_mfma_f32_16x16x32_bf16 v[8:11], a[28:31], v[124:127], v[8:11]\n\t"
      "v_add_f32 v40, v40, v41\n\t"
      "s_lshl_b32 s73, s49, 5\n\t"
      "s_add_u32 s73, s73, s60\n\t"
      "s_lshl_b32 s69, s73, 2\n\t"
      "s_add_u32 s69, s69, s57\n\t"
      "s_lshl_b32 s70, s73, 7\n\t"
      "s_add_u32 s70, s70, s57\n\t"
      "s_sub_u32 s70, s70, 212992\n\t"
      "s_cmp_ge_u32 s49, 56\n\t"
      "s_cselect_b32 s70, s70, s69\n\t"
      "v_add_f32_dpp v40, v40, v40 quad_perm:[1,0,3,2] row_mask:0xf bank_mask:0xf\n\t"
      "v_mfma_f32_16x16x32_bf16 v[12:15], a[92:95], v[124:127], v[12:15]\n\t"
      "ds_read_b128 v[108:111], v255 offset:27648\n\t"
      "s_waitcnt lgkmcnt(11)\n\t"
      "v_mfma_f32_16x16x32_bf16 v[0:3], a[32:35], v[64:67], v[0:3]\n\t"
      "v_add_f32_dpp v40, v40, v40 quad_perm:[2,3,0,1] row_mask:0xf bank_mask:0xf\n\t"
      "v_mfma_f32_16x16x32_bf16 v[4:7], a[96:99], v[64:67], v[4:7]\n\t"
      "ds_read_b128 v[112:115], v255 offset:28672\n\t"
      "s_waitcnt lgkmcnt(11)\n\t"
      "v_mfma_f32_16x16x32_bf16 v[8:11], a[32:35], v[68:71], v[8:11]\n\t"
      "v_add_f32_dpp v40, v40, v40 row_ror:4 row_mask:0xf bank_mask:0xf\n\t"
      "v_mfma_f32_16x16x32_bf16 v[12:15], a[96:99], v[68:71], v[12:15]\n\t"
      "ds_read_b128 v[116:119], v255 offset:29696\n\t"
      "s_waitcnt lgkmcnt(11)\n\t"
      "v_mfma_f32_16x16x32_bf16 v[0:3], a[36:39], v[72:75], v[0:3]\n\t"
      "v_add_f32_dpp v40, v40, v40 row_ror:8 row_mask:0xf bank_mask:0xf\n\t"
      "v_mov_b32 v41, v40\n\t"
      "v_mfma_f32_16x16x32_bf16 v[4:7], a[100:103], v[72:75], v[4:7]\n\t"
      "ds_read_b128 v[120:123], v255 offset:30720\n\t"
      "s_waitcnt lgkmcnt(11)\n\t"
      "v_mfma_f32_16x16x32_bf16 v[8:11], a[36:39], v[76:79], v[8:11]\n\t"
      "v_permlane16_swap_b32 v40, v41\n\t"
      "v_add_f32 v40, v40, v41\n\t"
      "v_mfma_f32_16x16x32_bf16 v[12:15], a[100:103], v[76:79], v[12:15]\n\t"
      "v_mov_b32 v41, v40\n\t"
      "ds_read_b128 v[124:127], v255 offset:31744\n\t"
      "s_waitcnt lgkmcnt(11)\n\t"
      "v_mfma_f32_16x16x32_bf16 v[0:3], a[40:43], v[80:83], v[0:3]\n\t"
      "v_permlane32_swap_b32 v40, v41\n\t"
      "v_add_f32 v40, v40, v41\n\t"
      "v_mfma_f32_16x16x32_bf16 v[4:7], a[104:107], v[80:83], v[4:7]\n\t"
      "v_cvt_pk_bf16_f32 v40, v40, v40\n\t"
      "v_and_b32 v40, s74, v40\n\t"
      "s_waitcnt lgkmcnt(10)\n\t"
      "v_mfma_f32_16x16x32_bf16 v[8:11], a[40:43], v[84:87], v[8:11]\n\t"
      "s_mov_b64 exec, 1\n\t"
      "buffer_store_dword v40, off, s[36:39], s70 sc1\n\t"
      "s_mov_b64 exec, -1\n\t"
      "v_add_f32 v192, v192, v32\n\t"
      "v_add_f32 v193, v193, v33\n\t"
      "v_mfma_f32_16x16x32_bf16 v[12:15], a[104:107], v[84:87], v[12:15]\n\t"
      "v_add_f32 v194, v194, v34\n\t"
      "v_add_f32 v195, v195, v35\n\t"
      "s_waitcnt lgkmcnt(9)\n\t"
      "v_mfma_f32_16x16x32_bf16 v[0:3], a[44:47], v[88:91], v[0:3]\n\t"
      "v_add_f32 v196, v196, v36\n\t"
      "v_add_f32 v197, v197, v37\n\t"
      "v_mfma_f32_16x16x32_bf16 v[4:7], a[108:111], v[88:91], v[4:7]\n\t"
      "v_add_f32 v198, v198, v38\n\t"
      "v_add_f32 v199, v199, v39\n\t"
      "s_waitcnt lgkmcnt(8)\n\t"
      "v_mfma_f32_16x16x32_bf16 v[8:11], a[44:47], v[92:95], v[8:11]\n\t"
      "v_readfirstlane_b32 s68, v40\n\t"
      "v_cvt_pk_bf16_f32 v56, v192, v193\n\t"
      "v_mfma_f32_16x16x32_bf16 v[12:15], a[108:111], v[92:95], v[12:15]\n\t"
      "v_cvt_pk_bf16_f32 v57, v194, v195\n\t"
      "v_cvt_pk_bf16_f32 v58, v196, v197\n\t"
      "s_waitcnt lgkmcnt(7)\n\t"
      "v_mfma_f32_16x16x32_bf16 v[0:3], a[48:51], v[96:99], v[0:3]\n\t"
      "v_cvt_pk_bf16_f32 v59, v198, v199\n\t"
      "s_add_u32 s67, s69, 8192\n\t"
      "s_lshl_b32 s73, s73, 10\n\t"
      "s_add_u32 s69, s73, s58\n\t"
      "s_add_u32 s70, s73, s59\n\t"
      "buffer_store_dwordx2 v[56:57], v249, s[44:47], s73 offen sc1\n\t"
      "v_mfma_f32_16x16x32_bf16 v[4:7], a[112:115], v[96:99], v[4:7]\n\t"
      "buffer_store_dwordx2 v[52:53], v249, s[36:39], s69 offen sc1\n\t"
      "buffer_store_dwordx2 v[200:201], v249, s[36:39], s70 offen sc1\n\t"
      "s_waitcnt lgkmcnt(6)\n\t"
      "v_mfma_f32_16x16x32_bf16 v[8:11], a[48:51], v[100:103], v[8:11]\n\t"
      "buffer_store_dwordx2 v[58:59], v249, s[44:47], s73 offen offset:512 sc1\n\t"
      "buffer_store_dwordx2 v[54:55], v249, s[36:39], s69 offen offset:512 sc1\n\t"
      "v_mfma_f32_16x16x32_bf16 v[12:15], a[112:115], v[100:103], v[12:15]\n\t"
      "buffer_store_dwordx2 v[202:203], v249, s[36:39], s70 offen offset:512 sc1\n\t"
      "s_mov_b32 s49, s89\n\t"
      "s_add_u32 s89, s89, 16\n\t"
      "s_lshr_b32 s69, s89, 4\n\t"
      "s_cmp_eq_u32 s69, 3\n\t"
      "s_cselect_b32 s89, s93, s89\n\t"
      "s_cmp_ge_u32 s49, 48\n\t"
      "s_cselect_b32 s60, s94, s60\n\t"
      "s_cselect_b32 s54, s95, s54\n\t"
      "s_waitcnt lgkmcnt(5)\n\t"
      "v_mfma_f32_16x16x32_bf16 v[0:3], a[52:55], v[104:107], v[0:3]\n\t"
      "v_mfma_f32_16x16x32_bf16 v[4:7], a[116:119], v[104:107], v[4:7]\n\t"
      "s_waitcnt lgkmcnt(4)\n\t"
      "v_mfma_f32_16x16x32_bf16 v[8:11], a[52:55], v[108:111], v[8:11]\n\t"
      "v_mfma_f32_16x16x32_bf16 v[12:15], a[116:119], v[108:111], v[12:15]\n\t"
      "s_waitcnt lgkmcnt(3)\n\t"
      "v_mfma_f32_16x16x32_bf16 v[0:3], a[56:59], v[112:115], v[0:3]\n\t"
      "v_mfma_f32_16x16x32_bf16 v[4:7], a[120:123], v[112:115], v[4:7]\n\t"
      "s_waitcnt lgkmcnt(2)\n\t"
      "v_mfma_f32_16x16x32_bf16 v[8:11], a[56:59], v[116:119], v[8:11]\n\t"
      "v_mfma_f32_16x16x32_bf16 v[12:15], a[120:123], v[116:119], v[12:15]\n\t"
      "s_waitcnt lgkmcnt(1)\n\t"
      "v_mfma_f32_16x16x32_bf16 v[0:3], a[60:63], v[120:123], v[0:3]\n\t"
      "v_mfma_f32_16x16x32_bf16 v[4:7], a[124:127], v[120:123], v[4:7]\n\t"
      "s_waitcnt lgkmcnt(0)\n\t"
      "v_mfma_f32_16x16x32_bf16 v[8:11], a[60:63], v[124:127], v[8:11]\n\t"
      "v_mfma_f32_16x16x32_bf16 v[12:15], a[124:127], v[124:127], v[12:15]\n\t"
      "v_xor_b32 v254, 0x8000, v254\n\t"
      "v_xor_b32 v255, 0x8000, v255\n\t"
      "s_add_u32 s69, s53, 0x4000\n\t"
      "s_add_u32 s48, s48, 4\n\t"
      "s_waitcnt vmcnt(40)\n\t"
      "v_cvt_pk_bf16_f32 v0, v0, v1\n\t"
      "v_cvt_pk_bf16_f32 v1, v2, v3\n\t"
      "v_cvt_pk_bf16_f32 v2, v4, v5\n\t"
      "v_cvt_pk_bf16_f32 v3, v6, v7\n\t"
      "v_min_u32 v0, -2, v0\n\t"
      "v_min_u32 v1, -2, v1\n\t"
      "v_min_u32 v2, -2, v2\n\t"
      "v_min_u32 v3, -2, v3\n\t"
      "buffer_store_dwordx4 v[0:3], v238, s[36:39], s53 offen sc1\n\t"
      "v_cvt_pk_bf16_f32 v8, v8, v9\n\t"
      "v_cvt_pk_bf16_f32 v9, v10, v11\n\t"
      "v_cvt_pk_bf16_f32 v10, v12, v13\n\t"
      "v_cvt_pk_bf16_f32 v11, v14, v15\n\t"
      "v_min_u32 v8, -2, v8\n\t"
      "v_min_u32 v9, -2, v9\n\t"
      "v_min_u32 v10, -2, v10\n\t"
      "v_min_u32 v11, -2, v11\n\t"
      "buffer_store_dwordx4 v[8:11], v238, s[36:39], s69 offen sc1\n\t"
      "s_add_u32 s53, s53, 1048576\n\t"
      "s_nop 1\n\t"
      "s_branch WTOP%=\n\t"
      ".p2align 5\n\t"
      "WOL5%=:\n\t"
      "buffer_load_dwordx4 v[128:131], v252, s[36:39], s51 offen sc1\n\t"
      "s_waitcnt lgkmcnt(11)\n\t"
      "v_mfma_f32_16x16x32_bf16 v[0:3], a[0:3], v[64:67], 0\n\t"
      "v_cvt_pk_bf16_f32 v204, v204, v205\n\t"
      "v_and_b32 v205, s74, v204\n\t"
      "v_mfma_f32_16x16x32_bf16 v[4:7], a[64:67], v[64:67], 0\n\t"
      "v_lshlrev_b32 v204, 16, v204\n\t"
      "v_add_f32 v205, 0x3727c5ac, v205\n\t"
      "ds_read_b128 v[112:115], v255 offset:12288\n\t"
      "s_waitcnt lgkmcnt(11)\n\t"
      "v_mfma_f32_16x16x32_bf16 v[8:11], a[0:3], v[68:71], 0\n\t"
      "v_sub_f32 v40, v192, v204\n\t"
      "v_sub_f32 v41, v193, v204\n\t"
      "v_mfma_f32_16x16x32_bf16 v[12:15], a[64:67], v[68:71], 0\n\t"
      "v_sub_f32 v42, v194, v204\n\t"
      "v_sub_f32 v43, v195, v204\n\t"
      "ds_read_b128 v[116:119], v255 offset:13312\n\t"
      "s_waitcnt lgkmcnt(11)\n\t"
      "v_mfma_f32_16x16x32_bf16 v[0:3], a[4:7], v[72:75], v[0:3]\n\t"
      "v_sub_f32 v44, v196, v204\n\t"
      "v_sub_f32 v45, v197, v204\n\t"
      "v_mfma_f32_16x16x32_bf16 v[4:7], a[68:71], v[72:75], v[4:7]\n\t"
      "v_sub_f32 v46, v198, v204\n\t"
      "v_sub_f32 v47, v199, v204\n\t"
      "ds_read_b128 v[120:123], v255 offset:14336\n\t"
      "s_waitcnt lgkmcnt(11)\n\t"
      "v_mfma_f32_16x16x32_bf16 v[8:11], a[4:7], v[76:79], v[8:11]\n\t"
      "v_cvt_pk_bf16_f32 v205, v205, v205\n\t"
      "v_and_b32 v205, s74, v205\n\t"
      "v_mfma_f32_16x16x32_bf16 v[12:15], a[68:71], v[76:79], v[12:15]\n\t"
      "buffer_load_dwordx4 v[132:135], v252, s[36:39], s51 offen offset:512 sc1\n\t"
      "ds_read_b128 v[124:127], v255 offset:15360\n\t"
      "s_waitcnt lgkmcnt(11)\n\t"
      "v_mfma_f32_16x16x32_bf16 v[0:3], a[8:11], v[80:83], v[0:3]\n\t"
      "v_sqrt_f32 v205, v205\n\t"
      "v_mfma_f32_16x16x32_bf16 v[4:7], a[72:75], v[80:83], v[4:7]\n\t"
      "v_cvt_pk_bf16_f32 v40, v40, v41\n\t"
      "v_cvt_pk_bf16_f32 v42, v42, v43\n\t"
      "ds_read_b128 v[64:67], v255 offset:16384\n\t"
      "s_waitcnt lgkmcnt(11)\n\t"
      "v_mfma_f32_16x16x32_bf16 v[8:11], a[8:11], v[84:87], v[8:11]\n\t"
      "v_cvt_pk_bf16_f32 v44, v44, v45\n\t"
      "v_cvt_pk_bf16_f32 v46, v46, v47\n\t"
      "v_mfma_f32_16x16x32_bf16 v[12:15], a[72:75], v[84:87], v[12:15]\n\t"
      "v_cvt_pk_bf16_f32 v205, v205, v205\n\t"
      "v_and_b32 v205, s74, v205\n\t"
      "ds_read_b128 v[68:71], v255 offset:17408\n\t"
      "s_waitcnt lgkmcnt(11)\n\t"
      "v_mfma_f32_16x16x32_bf16 v[0:3], a[12:15], v[88:91], v[0:3]\n\t"
      "v_rcp_f32 v204, v205\n\t"
      "v_mfma_f32_16x16x32_bf16 v[4:7], a[76:79], v[88:91], v[4:7]\n\t"
      "v_and_b32 v41, s74, v40\n\t"
      "v_lshlrev_b32 v40, 16, v40\n\t"
      "ds_read_b128 v[72:75], v255 offset:18432\n\t"
      "s_waitcnt lgkmcnt(11)\n\t"
      "v_mfma_f32_16x16x32_bf16 v[8:11], a[12:15], v[92:95], v[8:11]\n\t"
      "v_and_b32 v43, s74, v42\n\t"
      "v_lshlrev_b32 v42, 16, v42\n\t"
      "v_mfma_f32_16x16x32_bf16 v[12:15], a[76:79], v[92:95], v[12:15]\n\t"
      "buffer_load_dwordx4 v[136:139], v252, s[36:39], s51 offen offset:1024 sc1\n\t"
      "ds_read_b128 v[76:79], v255 offset:19456\n\t"
      "s_waitcnt lgkmcnt(11)\n\t"
      "v_mfma_f32_16x16x32_bf16 v[0:3], a[16:19], v[96:99], v[0:3]\n\t"
      "v_and_b32 v45, s74, v44\n\t"
      "v_lshlrev_b32 v44, 16, v44\n\t"
      "v_mfma_f32_16x16x32_bf16 v[4:7], a[80:83], v[96:99], v[4:7]\n\t"
      "v_and_b32 v47, s74, v46\n\t"
      "v_lshlrev_b32 v46, 16, v46\n\t"
      "ds_read_b128 v[80:83], v255 offset:20480\n\t"
      "s_waitcnt lgkmcnt(11)\n\t"
      "v_mfma_f32_16x16x32_bf16 v[8:11], a[16:19], v[100:103], v[8:11]\n\t"
      "v_mul_f32 v40, v40, v204\n\t"
      "v_mul_f32 v41, v41, v204\n\t"
      "v_mfma_f32_16x16x32_bf16 v[12:15], a[80:83], v[100:103], v[12:15]\n\t"
      "v_mul_f32 v42, v42, v204\n\t"
      "v_mul_f32 v43, v43, v204\n\t"
      "ds_read_b128 v[84:87], v255 offset:21504\n\t"
      "s_waitcnt lgkmcnt(11)\n\t"
      "v_mfma_f32_16x16x32_bf16 v[0:3], a[20:23], v[104:107], v[0:3]\n\t"
      "v_mul_f32 v44, v44, v204\n\t"
      "v_mul_f32 v45, v45, v204\n\t"
      "v_mfma_f32_16x16x32_bf16 v[4:7], a[84:87], v[104:107], v[4:7]\n\t"
      "v_mul_f32 v46, v46, v204\n\t"
      "v_mul_f32 v47, v47, v204\n\t"
      "ds_read_b128 v[88:91], v255 offset:22528\n\t"
      "s_waitcnt lgkmcnt(11)\n\t"
      "v_mfma_f32_16x16x32_bf16 v[8:11], a[20:23], v[108:111], v[8:11]\n\t"
      "v_cvt_pk_bf16_f32 v52, v40, v41\n\t"
      "v_cvt_pk_bf16_f32 v53, v42, v43\n\t"
      "v_mfma_f32_16x16x32_bf16 v[12:15], a[84:87], v[108:111], v[12:15]\n\t"
      "buffer_load_dwordx4 v[140:143], v252, s[36:39], s51 offen offset:1536 sc1\n\t"
      "ds_read_b128 v[92:95], v255 offset:23552\n\t"
      "s_waitcnt lgkmcnt(11)\n\t"
      "v_mfma_f32_16x16x32_bf16 v[0:3], a[24:27], v[112:115], v[0:3]\n\t"
      "v_cvt_pk_bf16_f32 v54, v44, v45\n\t"
      "v_cvt_pk_bf16_f32 v55, v46, v47\n\t"
      "v_mfma_f32_16x16x32_bf16 v[4:7], a[88:91], v[112:115], v[4:7]\n\t"
      "v_and_b32 v33, s74, v52\n\t"
      "v_lshlrev_b32 v32, 16, v52\n\t"
      "ds_read_b128 v[96:99], v255 offset:24576\n\t"
      "s_waitcnt lgkmcnt(11)\n\t"
      "v_mfma_f32_16x16x32_bf16 v[8:11], a[24:27], v[116:119], v[8:11]\n\t"
      "v_and_b32 v35, s74, v53\n\t"
      "v_lshlrev_b32 v34, 16, v53\n\t"
      "v_mfma_f32_16x16x32_bf16 v[12:15], a[88:91], v[116:119], v[12:15]\n\t"
      "v_and_b32 v37, s74, v54\n\t"
      "v_lshlrev_b32 v36, 16, v54\n\t"
      "ds_read_b128 v[100:103], v255 offset:25600\n\t"
      "s_waitcnt lgkmcnt(11)\n\t"
      "v_mfma_f32_16x16x32_bf16 v[0:3], a[28:31], v[120:123], v[0:3]\n\t"
      "v_and_b32 v39, s74, v55\n\t"
      "v_lshlrev_b32 v38, 16, v55\n\t"
      "v_mfma_f32_16x16x32_bf16 v[4:7], a[92:95], v[120:123], v[4:7]\n\t"
      "v_mul_f32 v40, v32, v240\n\t"
      "v_mul_f32 v41, v33, v241\n\t"
      "ds_read_b128 v[104:107], v255 offset:26624\n\t"
      "s_waitcnt lgkmcnt(11)\n\t"
      "v_mfma_f32_16x16x32_bf16 v[8:11], a[28:31], v[124:127], v[8:11]\n\t"
      "buffer_load_dwordx4 v[144:147], v253, s[36:39], s52 offen sc1\n\t"
      "v_mfma_f32_16x16x32_bf16 v[12:15], a[92:95], v[124:127], v[12:15]\n\t"
      "v_fma_f32 v40, v34, v242, v40\n\t"
      "v_fma_f32 v41, v35, v243, v41\n\t"
      "ds_read_b128 v[108:111], v255 offset:27648\n\t"
      "s_waitcnt lgkmcnt(11)\n\t"
      "v_mfma_f32_16x16x32_bf16 v[0:3], a[32:35], v[64:67], v[0:3]\n\t"
      "v_fma_f32 v40, v36, v244, v40\n\t"
      "v_fma_f32 v41, v37, v245, v41\n\t"
      "v_mfma_f32_16x16x32_bf16 v[4:7], a[96:99], v[64:67], v[4:7]\n\t"
      "v_fma_f32 v40, v38, v246, v40\n\t"
      "v_fma_f32 v41, v39, v247, v41\n\t"
      "ds_read_b128 v[112:115], v255 offset:28672\n\t"
      "s_waitcnt lgkmcnt(11)\n\t"
      "v_mfma_f32_16x16x32_bf16 v[8:11], a[32:35], v[68:71], v[8:11]\n\t"
      "v_add_f32 v40, v40, v41\n\t"
      "s_lshl_b32 s73, s49, 5\n\t"
      "s_add_u32 s73, s73, s60\n\t"
      "s_lshl_b32 s69, s73, 2\n\t"
      "s_add_u32 s69, s69, s57\n\t"
      "s_lshl_b32 s70, s73, 7\n\t"
      "s_add_u32 s70, s70, s57\n\t"
      "s_sub_u32 s70, s70, 212992\n\t"
      "s_cmp_ge_u32 s49, 56\n\t"
      "s_cselect_b32 s70, s70, s69\n\t"
      "v_add_f32_dpp v40, v40, v40 quad_perm:[1,0,3,2] row_mask:0xf bank_mask:0xf\n\t"
      "v_mfma_f32_16x16x32_bf16 v[12:15], a[96:99], v[68:71], v[12:15]\n\t"
      "ds_read_b128 v[116:119], v255 offset:29696\n\t"
      "s_waitcnt lgkmcnt(11)\n\t"
      "v_mfma_f32_16x16x32_bf16 v[0:3], a[36:39], v[72:75], v[0:3]\n\t"
      "v_add_f32_dpp v40, v40, v40 quad_perm:[2,3,0,1] row_mask:0xf bank_mask:0xf\n\t"
      "v_mfma_f32_16x16x32_bf16 v[4:7], a[100:103], v[72:75], v[4:7]\n\t"
      "ds_read_b128 v[120:123], v255 offset:30720\n\t"
      "s_waitcnt lgkmcnt(11)\n\t"
      "v_mfma_f32_16x16x32_bf16 v[8:11], a[36:39], v[76:79], v[8:11]\n\t"
      "buffer_load_dwordx4 v[148:151], v253, s[36:39], s52 offen offset:512 sc1\n\t"
      "v_mfma_f32_16x16x32_bf16 v[12:15], a[100:103], v[76:79], v[12:15]\n\t"
      "v_add_f32_dpp v40, v40, v40 row_ror:4 row_mask:0xf bank_mask:0xf\n\t"
      "ds_read_b128 v[124:127], v255 offset:31744\n\t"
      "s_waitcnt lgkmcnt(11)\n\t"
      "v_mfma_f32_16x16x32_bf16 v[0:3], a[40:43], v[80:83], v[0:3]\n\t"
      "v_add_f32_dpp v40, v40, v40 row_ror:8 row_mask:0xf bank_mask:0xf\n\t"
      "v_mov_b32 v41, v40\n\t"
      "v_mfma_f32_16x16x32_bf16 v[4:7], a[104:107], v[80:83], v[4:7]\n\t"
      "s_waitcnt lgkmcnt(10)\n\t"
      "v_mfma_f32_16x16x32_bf16 v[8:11], a[40:43], v[84:87], v[8:11]\n\t"
      "v_permlane16_swap_b32 v40, v41\n\t"
      "v_add_f32 v40, v40, v41\n\t"
      "v_mfma_f32_16x16x32_bf16 v[12:15], a[104:107], v[84:87], v[12:15]\n\t"
      "v_mov_b32 v41, v40\n\t"
      "s_waitcnt lgkmcnt(9)\n\t"
      "v_mfma_f32_16x16x32_bf16 v[0:3], a[44:47], v[88:91], v[0:3]\n\t"
      "v_permlane32_swap_b32 v40, v41\n\t"
      "v_add_f32 v40, v40, v41\n\t"
      "v_mfma_f32_16x16x32_bf16 v[4:7], a[108:111], v[88:91], v[4:7]\n\t"
      "v_cvt_pk_bf16_f32 v40, v40, v40\n\t"
      "v_and_b32 v40, s74, v40\n\t"
      "s_waitcnt lgkmcnt(8)\n\t"
      "v_mfma_f32_16x16x32_bf16 v[8:11], a[44:47], v[92:95], v[8:11]\n\t"
      "buffer_load_dwordx4 v[152:155], v253, s[36:39], s52 offen offset:1024 sc1\n\t"
      "v_mfma_f32_16x16x32_bf16 v[12:15], a[108:111], v[92:95], v[12:15]\n\t"
      "s_mov_b64 exec, 1\n\t"
      "buffer_store_dword v40, off, s[36:39], s70 sc1\n\t"
      "s_mov_b64 exec, -1\n\t"
      "v_add_f32 v192, v192, v32\n\t"
      "v_add_f32 v193, v193, v33\n\t"
      "s_waitcnt lgkmcnt(7)\n\t"
      "v_mfma_f32_16x16x32_bf16 v[0:3], a[48:51], v[96:99], v[0:3]\n\t"
      "v_add_f32 v194, v194, v34\n\t"
      "v_add_f32 v195, v195, v35\n\t"
      "v_mfma_f32_16x16x32_bf16 v[4:7], a[112:115], v[96:99], v[4:7]\n\t"
      "v_add_f32 v196, v196, v36\n\t"
      "v_add_f32 v197, v197, v37\n\t"
      "s_waitcnt lgkmcnt(6)\n\t"
      "v_mfma_f32_16x16x32_bf16 v[8:11], a[48:51], v[100:103], v[8:11]\n\t"
      "v_add_f32 v198, v198, v38\n\t"
      "v_add_f32 v199, v199, v39\n\t"
      "v_mfma_f32_16x16x32_bf16 v[12:15], a[112:115], v[100:103], v[12:15]\n\t"
      "v_readfirstlane_b32 s68, v40\n\t"
      "v_cvt_pk_bf16_f32 v56, v192, v193\n\t"
      "s_waitcnt lgkmcnt(5)\n\t"
      "v_mfma_f32_16x16x32_bf16 v[0:3], a[52:55], v[104:107], v[0:3]\n\t"
      "v_cvt_pk_bf16_f32 v57, v194, v195\n\t"
      "v_cvt_pk_bf16_f32 v58, v196, v197\n\t"
      "v_mfma_f32_16x16x32_bf16 v[4:7], a[116:119], v[104:107], v[4:7]\n\t"
      "v_cvt_pk_bf16_f32 v59, v198, v199\n\t"
      "s_add_u32 s67, s69, 8192\n\t"
      "s_lshl_b32 s73, s73, 10\n\t"
      "s_add_u32 s69, s73, s58\n\t"
      "s_add_u32 s70, s73, s59\n\t"
      "buffer_store_dwordx2 v[56:57], v249, s[44:47], s73 offen sc1\n\t"
      "s_waitcnt lgkmcnt(4)\n\t"
      "v_mfma_f32_16x16x32_bf16 v[8:11], a[52:55], v[108:111], v[8:11]\n\t"
      "buffer_store_dwordx2 v[52:53], v249, s[36:39], s69 offen sc1\n\t"
      "buffer_store_dwordx2 v[200:201], v249, s[36:39], s70 offen sc1\n\t"
      "v_mfma_f32_16x16x32_bf16 v[12:15], a[116:119], v[108:111], v[12:15]\n\t"
      "buffer_load_dwordx4 v[156:159], v253, s[36:39], s52 offen offset:1536 sc1\n\t"
      "s_waitcnt lgkmcnt(3)\n\t"
      "v_mfma_f32_16x16x32_bf16 v[0:3], a[56:59], v[112:115], v[0:3]\n\t"
      "buffer_store_dwordx2 v[58:59], v249, s[44:47], s73 offen offset:512 sc1\n\t"
      "buffer_store_dwordx2 v[54:55], v249, s[36:39], s69 offen offset:512 sc1\n\t"
      "v_mfma_f32_16x16x32_bf16 v[4:7], a[120:123], v[112:115], v[4:7]\n\t"
      "buffer_store_dwordx2 v[202:203], v249, s[36:39], s70 offen offset:512 sc1\n\t"
      "s_mov_b32 s49, s89\n\t"
      "s_add_u32 s89, s89, 16\n\t"
      "s_lshr_b32 s69, s89, 4\n\t"
      "s_cmp_eq_u32 s69, 3\n\t"
      "s_cselect_b32 s89, s93, s89\n\t"
      "s_cmp_ge_u32 s49, 48\n\t"
      "s_cselect_b32 s60, s94, s60\n\t"
      "s_cselect_b32 s54, s95, s54\n\t"
      "s_waitcnt lgkmcnt(2)\n\t"
      "v_mfma_f32_16x16x32_bf16 v[8:11], a[56:59], v[116:119], v[8:11]\n\t"
      "v_mfma_f32_16x16x32_bf16 v[12:15], a[120:123], v[116:119], v[12:15]\n\t"
      "s_waitcnt lgkmcnt(1)\n\t"
      "v_mfma_f32_16x16x32_bf16 v[0:3], a[60:63], v[120:123], v[0:3]\n\t"
      "v_mfma_f32_16x16x32_bf16 v[4:7], a[124:127], v[120:123], v[4:7]\n\t"
      "s_waitcnt lgkmcnt(0)\n\t"
      "v_mfma_f32_16x16x32_bf16 v[8:11], a[60:63], v[124:127], v[8:11]\n\t"
      "v_mfma_f32_16x16x32_bf16 v[12:15], a[124:127], v[124:127], v[12:15]\n\t"
      "v_xor_b32 v254, 0x8000, v254\n\t"
      "v_xor_b32 v255, 0x8000, v255\n\t"
      "s_add_u32 s69, s53, 0x4000\n\t"
      "s_add_u32 s48, s48, 4\n\t"
      "s_waitcnt vmcnt(40)\n\t"
      "v_cvt_pk_bf16_f32 v0, v0, v1\n\t"
      "v_cvt_pk_bf16_f32 v1, v2, v3\n\t"
      "v_cvt_pk_bf16_f32 v2, v4, v5\n\t"
      "v_cvt_pk_bf16_f32 v3, v6, v7\n\t"
      "v_min_u32 v0, -2, v0\n\t"
      "v_min_u32 v1, -2, v1\n\t"
      "v_min_u32 v2, -2, v2\n\t"
      "v_min_u32 v3, -2, v3\n\t"
      "buffer_store_dwordx4 v[0:3], v238, s[36:39], s53 offen sc1\n\t"
      "v_cvt_pk_bf16_f32 v8, v8, v9\n\t"
      "v_cvt_pk_bf16_f32 v9, v10, v11\n\t"
      "v_cvt_pk_bf16_f32 v10, v12, v13\n\t"
      "v_cvt_pk_bf16_f32 v11, v14, v15\n\t"
      "v_min_u32 v8, -2, v8\n\t"
      "v_min_u32 v9, -2, v9\n\t"
      "v_min_u32 v10, -2, v10\n\t"
      "v_min_u32 v11, -2, v11\n\t"
      "buffer_store_dwordx4 v[8:11], v238, s[36:39], s69 offen sc1\n\t"
      "s_add_u32 s53, s53, 1048576\n\t"
      "s_nop 1\n\t"
      "s_branch WTOP%=\n\t"
      ".p2align 5\n\t"
      "WKF%=:\n\t"
      "s_cmp_eq_u32 s48, 63\n\t"
      "s_cselect_b32 s69, 0, 46\n\t"
      "s_add_u32 s88, s88, s69\n\t"
      "s_add_u32 s88, s88, -16\n\t"
      "WKT%=:\n\t"
      "s_memrealtime s[90:91]\n\t"
      "s_waitcnt lgkmcnt(0)\n\t"
      "s_sub_u32 s69, s88, s90\n\t"
      "s_and_b32 s69, s69, 0xffff\n\t"
      "s_cmp_lt_u32 s69, 400\n\t"
      "s_cbranch_scc1 WKT%=\n\t"
      "WKL%=:\n\t"
      "buffer_load_dwordx4 v[128:131], v252, s[36:39], s51 offen sc1\n\t"
      "buffer_load_dwordx4 v[132:135], v252, s[36:39], s51 offen offset:512 sc1\n\t"
      "buffer_load_dwordx4 v[136:139], v252, s[36:39], s51 offen offset:1024 sc1\n\t"
      "buffer_load_dwordx4 v[140:143], v252, s[36:39], s51 offen offset:1536 sc1\n\t"
      "buffer_load_dwordx4 v[144:147], v253, s[36:39], s52 offen sc1\n\t"
      "buffer_load_dwordx4 v[148:151], v253, s[36:39], s52 offen offset:512 sc1\n\t"
      "buffer_load_dwordx4 v[152:155], v253, s[36:39], s52 offen offset:1024 sc1\n\t"
      "buffer_load_dwordx4 v[156:159], v253, s[36:39], s52 offen offset:1536 sc1\n\t"
      "v_lshlrev_b32 v6, 4, v248\n\t"
      "v_mov_b64 v[4:5], 0\n\t"
      "s_cmp_ge_u32 s49, s48\n\t"
      "s_cbranch_scc1 WKL1%=\n\t"
      "buffer_load_dword v4, v6, s[36:39], s55 offen sc1\n\t"
      "buffer_load_dword v5, v6, s[36:39], s55 offen offset:128 sc1\n\t"
      "WKL1%=:\n\t"
      "s_waitcnt vmcnt(0)\n\t"
      "v_min_u32 v4, v4, v5\n\t"
      "v_cmp_ge_u32 vcc, s49, v4\n\t"
      "s_nop 0\n\t"
      "s_cmp_eq_u64 vcc, 0\n\t"
      "s_cselect_b32 s66, 1, 0\n\t"
      "s_cmp_lt_u32 s48, 40\n\t"
      "s_cbranch_scc1 WKV%=\n\t"
      "WKC%=:\n\t"
      "v_max3_u32 v0, v128, v129, v130\n\t"
      "v_max3_u32 v0, v0, v131, v132\n\t"
      "v_max3_u32 v0, v0, v133, v134\n\t"
      "v_max3_u32 v0, v0, v135, v136\n\t"
      "v_max3_u32 v0, v0, v137, v138\n\t"
      "v_max3_u32 v0, v0, v139, v140\n\t"
      "v_max3_u32 v0, v0, v141, v142\n\t"
      "v_max3_u32 v0, v0, v143, v144\n\t"
      "v_max3_u32 v0, v0, v145, v146\n\t"
      "v_max3_u32 v0, v0, v147, v148\n\t"
      "v_max3_u32 v0, v0, v149, v150\n\t"
      "v_max3_u32 v0, v0, v151, v152\n\t"
      "v_max3_u32 v0, v0, v153, v154\n\t"
      "v_max3_u32 v0, v0, v155, v156\n\t"
      "v_max3_u32 v0, v0, v157, v158\n\t"
      "v_max_u32 v0, v0, v159\n\t"
      "v_cmp_eq_u32 vcc, -1, v0\n\t"
      "s_nop 0\n\t"
      "s_cbranch_vccz WKV%=\n\t"
      "s_sleep 2\n\t"
      "s_sub_u32 s72, s72, 1\n\t"
      "s_cmp_gt_i32 s72, 0\n\t"
      "s_cbranch_scc1 WKL%=\n\t"
      "WKV%=:\n\t"
      "s_cmp_lt_u32 s48, 60\n\t"
      "s_cbranch_scc1 WKV1%=\n\t"
      "s_cmp_eq_u32 s66, 0\n\t"
      "s_cbranch_scc1 WB0%=\n\t"
      "s_lshl_b32 s70, s49, 5\n\t"
      "s_add_u32 s70, s70, s60\n\t"
      "s_lshl_b32 s70, s70, 10\n\t"
      "buffer_load_dwordx2 v[224:225], v249, s[40:43], s70 offen \n\t"
      "buffer_load_dwordx2 v[226:227], v249, s[40:43], s70 offen offset:512 \n\t"
      "s_lshl_b32 s69, s49, 18\n\t"
      "s_add_u32 s69, s69, s54\n\t"
      "buffer_load_dwordx4 v[160:163], v248, s[36:39], s69 offen sc1\n\t"
      "s_add_u32 s69, s69, 0x8000\n\t"
      "buffer_load_dwordx4 v[164:167], v248, s[36:39], s69 offen sc1\n\t"
      "s_add_u32 s69, s69, 0x8000\n\t"
      "buffer_load_dwordx4 v[168:171], v248, s[36:39], s69 offen sc1\n\t"
      "s_add_u32 s69, s69, 0x8000\n\t"
      "buffer_load_dwordx4 v[172:175], v248, s[36:39], s69 offen sc1\n\t"
      "s_add_u32 s69, s69, 0x8000\n\t"
      "buffer_load_dwordx4 v[176:179], v248, s[36:39], s69 offen sc1\n\t"
      "s_add_u32 s69, s69, 0x8000\n\t"
      "buffer_load_dwordx4 v[180:183], v248, s[36:39], s69 offen sc1\n\t"
      "s_add_u32 s69, s69, 0x8000\n\t"
      "buffer_load_dwordx4 v[184:187], v248, s[36:39], s69 offen sc1\n\t"
      "s_add_u32 s69, s69, 0x8000\n\t"
      "buffer_load_dwordx4 v[188:191], v248, s[36:39], s69 offen sc1\n\t"
      "s_mov_b32 s65, 1\n\t"
      "WB0%=:\n\t"
      "v_mfma_f32_16x16x32_bf16 v[0:3], v[128:131], a[160:163], 0\n\t"
      "v_mfma_f32_16x16x32_bf16 v[4:7], v[128:131], a[164:167], 0\n\t"
      "v_mfma_f32_16x16x32_bf16 v[0:3], v[132:135], a[168:171], v[0:3]\n\t"
      "v_mfma_f32_16x16x32_bf16 v[4:7], v[132:135], a[172:175], v[4:7]\n\t"
      "v_mfma_f32_16x16x32_bf16 v[0:3], v[136:139], a[176:179], v[0:3]\n\t"
      "v_mfma_f32_16x16x32_bf16 v[4:7], v[136:139], a[180:183], v[4:7]\n\t"
      "v_mfma_f32_16x16x32_bf16 v[0:3], v[140:143], a[184:187], v[0:3]\n\t"
      "v_mfma_f32_16x16x32_bf16 v[4:7], v[140:143], a[188:191], v[4:7]\n\t"
      "s_add_u32 s51, s51, 262144\n\t"
      "s_add_u32 s52, s52, 262144\n\t"
      "s_nop 4\n\t"
      "v_max3_f32 v8, v0, v1, v2\n\t"
      "v_max3_f32 v8, v8, v3, v234\n\t"
      "v_max3_f32 v9, v4, v5, v6\n\t"
      "v_max3_f32 v9, v9, v7, v235\n\t"
      "s_nop 1\n\t"
      "v_permlane16_swap_b32 v8, v9\n\t"
      "v_max_f32 v8, v8, v9\n\t"
      "v_mov_b32 v9, v8\n\t"
      "s_nop 1\n\t"
      "v_permlane16_swap_b32 v8, v9\n\t"
      "v_pk_mul_f32 v[8:9], v[8:9], s[86:87]\n\t"
      "v_pk_fma_f32 v[10:11], v[234:235], s[86:87], v[8:9] neg_lo:[0,0,1] neg_hi:[0,0,1]\n\t"
      "v_pk_fma_f32 v[0:1], v[0:1], s[86:87], v[8:9] op_sel:[0,0,0] op_sel_hi:[1,1,0] neg_lo:[0,0,1] neg_hi:[0,0,1]\n\t"
      "v_pk_fma_f32 v[2:3], v[2:3], s[86:87], v[8:9] op_sel:[0,0,0] op_sel_hi:[1,1,0] neg_lo:[0,0,1] neg_hi:[0,0,1]\n\t"
      "v_pk_fma_f32 v[4:5], v[4:5], s[86:87], v[8:9] op_sel:[0,0,1] op_sel_hi:[1,1,1] neg_lo:[0,0,1] neg_hi:[0,0,1]\n\t"
      "v_pk_fma_f32 v[6:7], v[6:7], s[86:87], v[8:9] op_sel:[0,0,1] op_sel_hi:[1,1,1] neg_lo:[0,0,1] neg_hi:[0,0,1]\n\t"
      "v_exp_f32 v10, v10\n\t"
      "v_exp_f32 v11, v11\n\t"
      "v_exp_f32 v0, v0\n\t"
      "v_exp_f32 v1, v1\n\t"
      "v_exp_f32 v2, v2\n\t"
      "v_exp_f32 v3, v3\n\t"
      "v_exp_f32 v4, v4\n\t"
      "v_exp_f32 v5, v5\n\t"
      "v_exp_f32 v6, v6\n\t"
      "v_exp_f32 v7, v7\n\t"
      "v_pk_mul_f32 v[12:13], v[236:237], v[10:11]\n\t"
      "v_pk_add_f32 v[14:15], v[0:1], v[2:3]\n\t"
      "v_pk_add_f32 v[16:17], v[4:5], v[6:7]\n\t"
      "v_add_f32 v14, v14, v15\n\t"
      "v_add_f32 v15, v16, v17\n\t"
      "v_fma_f32 v12, v14, 0.5, v12\n\t"
      "v_fma_f32 v13, v15, 0.5, v13\n\t"
      "s_nop 1\n\t"
      "v_permlane16_swap_b32 v12, v13\n\t"
      "v_add_f32 v12, v12, v13\n\t"
      "v_mov_b32 v13, v12\n\t"
      "s_nop 1\n\t"
      "v_permlane32_swap_b32 v12, v13\n\t"
      "v_add_f32 v12, v12, v13\n\t"
      "v_mov_b32 v13, v12\n\t"
      "s_nop 1\n\t"
      "v_permlane16_swap_b32 v12, v13\n\t"
      "v_rcp_f32 v12, v12\n\t"
      "v_rcp_f32 v13, v13\n\t"
      "s_nop 0\n\t"
      "v_pk_mul_f32 v[10:11], v[10:11], v[12:13]\n\t"
      "v_pk_mul_f32 v[12:13], v[12:13], 0.5 op_sel_hi:[1,0]\n\t"
      "v_pk_mul_f32 v[0:1], v[0:1], v[12:13] op_sel:[0,0] op_sel_hi:[1,0]\n\t"
      "v_pk_mul_f32 v[2:3], v[2:3], v[12:13] op_sel:[0,0] op_sel_hi:[1,0]\n\t"
      "v_pk_mul_f32 v[4:5], v[4:5], v[12:13] op_sel:[0,1] op_sel_hi:[1,1]\n\t"
      "v_pk_mul_f32 v[6:7], v[6:7], v[12:13] op_sel:[0,1] op_sel_hi:[1,1]\n\t"
      "v_cvt_pk_bf16_f32 v16, v0, v1\n\t"
      "v_cvt_pk_bf16_f32 v17, v2, v3\n\t"
      "v_cvt_pk_bf16_f32 v18, v4, v5\n\t"
      "v_cvt_pk_bf16_f32 v19, v6, v7\n\t"
      "v_pk_mul_f32 v[64:65], v[64:65], v[10:11] op_sel:[0,0] op_sel_hi:[1,0]\n\t"
      "v_pk_mul_f32 v[66:67], v[66:67], v[10:11] op_sel:[0,0] op_sel_hi:[1,0]\n\t"
      "v_pk_mul_f32 v[68:69], v[68:69], v[10:11] op_sel:[0,0] op_sel_hi:[1,0]\n\t"
      "v_pk_mul_f32 v[70:71], v[70:71], v[10:11] op_sel:[0,0] op_sel_hi:[1,0]\n\t"
      "v_mfma_f32_16x16x16_bf16 v[64:67], v[144:145], v[16:17], v[64:67]\n\t"
      "v_pk_mul_f32 v[72:73], v[72:73], v[10:11] op_sel:[0,0] op_sel_hi:[1,0]\n\t"
      "v_pk_mul_f32 v[74:75], v[74:75], v[10:11] op_sel:[0,0] op_sel_hi:[1,0]\n\t"
      "v_mfma_f32_16x16x16_bf16 v[68:71], v[146:147], v[16:17], v[68:71]\n\t"
      "v_pk_mul_f32 v[76:77], v[76:77], v[10:11] op_sel:[0,0] op_sel_hi:[1,0]\n\t"
      "v_pk_mul_f32 v[78:79], v[78:79], v[10:11] op_sel:[0,0] op_sel_hi:[1,0]\n\t"
      "v_mfma_f32_16x16x16_bf16 v[72:75], v[148:149], v[16:17], v[72:75]\n\t"
      "v_pk_mul_f32 v[80:81], v[80:81], v[10:11] op_sel:[0,0] op_sel_hi:[1,0]\n\t"
      "v_pk_mul_f32 v[82:83], v[82:83], v[10:11] op_sel:[0,0] op_sel_hi:[1,0]\n\t"
      "v_mfma_f32_16x16x16_bf16 v[76:79], v[150:151], v[16:17], v[76:79]\n\t"
      "v_pk_mul_f32 v[84:85], v[84:85], v[10:11] op_sel:[0,0] op_sel_hi:[1,0]\n\t"
      "v_pk_mul_f32 v[86:87], v[86:87], v[10:11] op_sel:[0,0] op_sel_hi:[1,0]\n\t"
      "v_mfma_f32_16x16x16_bf16 v[80:83], v[152:153], v[16:17], v[80:83]\n\t"
      "v_pk_mul_f32 v[88:89], v[88:89], v[10:11] op_sel:[0,0] op_sel_hi:[1,0]\n\t"
      "v_pk_mul_f32 v[90:91], v[90:91], v[10:11] op_sel:[0,0] op_sel_hi:[1,0]\n\t"
      "v_mfma_f32_16x16x16_bf16 v[84:87], v[154:155], v[16:17], v[84:87]\n\t"
      "v_pk_mul_f32 v[92:93], v[92:93], v[10:11] op_sel:[0,0] op_sel_hi:[1,0]\n\t"
      "v_pk_mul_f32 v[94:95], v[94:95], v[10:11] op_sel:[0,0] op_sel_hi:[1,0]\n\t"
      "v_cvt_pk_bf16_f32 v24, v64, v65\n\t"
      "v_cvt_pk_bf16_f32 v25, v66, v67\n\t"
      "v_cvt_pk_bf16_f32 v26, v68, v69\n\t"
      "v_cvt_pk_bf16_f32 v27, v70, v71\n\t"
      "ds_write_b128 v254, v[24:27]\n\t"
      "v_mfma_f32_16x16x16_bf16 v[88:91], v[156:157], v[16:17], v[88:91]\n\t"
      "v_pk_mul_f32 v[96:97], v[96:97], v[10:11] op_sel:[0,1] op_sel_hi:[1,1]\n\t"
      "v_pk_mul_f32 v[98:99], v[98:99], v[10:11] op_sel:[0,1] op_sel_hi:[1,1]\n\t"
      "v_mfma_f32_16x16x16_bf16 v[92:95], v[158:159], v[16:17], v[92:95]\n\t"
      "v_pk_mul_f32 v[100:101], v[100:101], v[10:11] op_sel:[0,1] op_sel_hi:[1,1]\n\t"
      "v_pk_mul_f32 v[102:103], v[102:103], v[10:11] op_sel:[0,1] op_sel_hi:[1,1]\n\t"
      "v_cvt_pk_bf16_f32 v28, v72, v73\n\t"
      "v_cvt_pk_bf16_f32 v29, v74, v75\n\t"
      "v_cvt_pk_bf16_f32 v30, v76, v77\n\t"
      "v_cvt_pk_bf16_f32 v31, v78, v79\n\t"
      "ds_write_b128 v254, v[28:31] offset:2048\n\t"
      "v_mfma_f32_16x16x16_bf16 v[96:99], v[144:145], v[18:19], v[96:99]\n\t"
      "v_pk_mul_f32 v[104:105], v[104:105], v[10:11] op_sel:[0,1] op_sel_hi:[1,1]\n\t"
      "v_pk_mul_f32 v[106:107], v[106:107], v[10:11] op_sel:[0,1] op_sel_hi:[1,1]\n\t"
      "v_mfma_f32_16x16x16_bf16 v[100:103], v[146:147], v[18:19], v[100:103]\n\t"
      "v_pk_mul_f32 v[108:109], v[108:109], v[10:11] op_sel:[0,1] op_sel_hi:[1,1]\n\t"
      "v_pk_mul_f32 v[110:111], v[110:111], v[10:11] op_sel:[0,1] op_sel_hi:[1,1]\n\t"
      "v_cvt_pk_bf16_f32 v24, v80, v81\n\t"
      "v_cvt_pk_bf16_f32 v25, v82, v83\n\t"
      "v_cvt_pk_bf16_f32 v26, v84, v85\n\t"
      "v_cvt_pk_bf16_f32 v27, v86, v87\n\t"
      "ds_write_b128 v254, v[24:27] offset:4096\n\t"
      "v_mfma_f32_16x16x16_bf16 v[104:107], v[148:149], v[18:19], v[104:107]\n\t"
      "v_pk_mul_f32 v[112:113], v[112:113], v[10:11] op_sel:[0,1] op_sel_hi:[1,1]\n\t"
      "v_pk_mul_f32 v[114:115], v[114:115], v[10:11] op_sel:[0,1] op_sel_hi:[1,1]\n\t"
      "v_mfma_f32_16x16x16_bf16 v[108:111], v[150:151], v[18:19], v[108:111]\n\t"
      "v_pk_mul_f32 v[116:117], v[116:117], v[10:11] op_sel:[0,1] op_sel_hi:[1,1]\n\t"
      "v_pk_mul_f32 v[118:119], v[118:119], v[10:11] op_sel:[0,1] op_sel_hi:[1,1]\n\t"
      "v_cvt_pk_bf16_f32 v28, v88, v89\n\t"
      "v_cvt_pk_bf16_f32 v29, v90, v91\n\t"
      "v_cvt_pk_bf16_f32 v30, v92, v93\n\t"
      "v_cvt_pk_bf16_f32 v31, v94, v95\n\t"
      "ds_write_b128 v254, v[28:31] offset:6144\n\t"
      "v_mfma_f32_16x16x16_bf16 v[112:115], v[152:153], v[18:19], v[112:115]\n\t"
      "v_pk_mul_f32 v[120:121], v[120:121], v[10:11] op_sel:[0,1] op_sel_hi:[1,1]\n\t"
      "v_pk_mul_f32 v[122:123], v[122:123], v[10:11] op_sel:[0,1] op_sel_hi:[1,1]\n\t"
      "v_mfma_f32_16x16x16_bf16 v[116:119], v[154:155], v[18:19], v[116:119]\n\t"
      "v_pk_mul_f32 v[124:125], v[124:125], v[10:11] op_sel:[0,1] op_sel_hi:[1,1]\n\t"
      "v_pk_mul_f32 v[126:127], v[126:127], v[10:11] op_sel:[0,1] op_sel_hi:[1,1]\n\t"
      "v_cvt_pk_bf16_f32 v24, v96, v97\n\t"
      "v_cvt_pk_bf16_f32 v25, v98, v99\n\t"
      "v_cvt_pk_bf16_f32 v26, v100, v101\n\t"
      "v_cvt_pk_bf16_f32 v27, v102, v103\n\t"
      "ds_write_b128 v254, v[24:27] offset:1024\n\t"
      "v_mfma_f32_16x16x16_bf16 v[120:123], v[156:157], v[18:19], v[120:123]\n\t"
      "v_mfma_f32_16x16x16_bf16 v[124:127], v[158:159], v[18:19], v[124:127]\n\t"
      "v_cvt_pk_bf16_f32 v28, v104, v105\n\t"
      "v_cvt_pk_bf16_f32 v29, v106, v107\n\t"
      "v_cvt_pk_bf16_f32 v30, v108, v109\n\t"
      "v_cvt_pk_bf16_f32 v31, v110, v111\n\t"
      "ds_write_b128 v254, v[28:31] offset:3072\n\t"
      "v_cvt_pk_bf16_f32 v24, v112, v113\n\t"
      "v_cvt_pk_bf16_f32 v25, v114, v115\n\t"
      "v_cvt_pk_bf16_f32 v26, v116, v117\n\t"
      "v_cvt_pk_bf16_f32 v27, v118, v119\n\t"
      "ds_write_b128 v254, v[24:27] offset:5120\n\t"
      "v_cvt_pk_bf16_f32 v28, v120, v121\n\t"
      "v_cvt_pk_bf16_f32 v29, v122, v123\n\t"
      "v_cvt_pk_bf16_f32 v30, v124, v125\n\t"
      "v_cvt_pk_bf16_f32 v31, v126, v127\n\t"
      "ds_write_b128 v254, v[28:31] offset:7168\n\t"
      "s_waitcnt vmcnt(40) lgkmcnt(0)\n\t"
      "s_barrier\n\t"
      "WO%=:\n\t"
      "s_waitcnt vmcnt(16)\n\t"
      "ds_read_b128 v[64:67], v255\n\t"
      "ds_read_b128 v[68:71], v255 offset:1024\n\t"
      "ds_read_b128 v[72:75], v255 offset:2048\n\t"
      "ds_read_b128 v[76:79], v255 offset:3072\n\t"
      "ds_read_b128 v[80:83], v255 offset:4096\n\t"
      "ds_read_b128 v[84:87], v255 offset:5120\n\t"
      "ds_read_b128 v[88:91], v255 offset:6144\n\t"
      "ds_read_b128 v[92:95], v255 offset:7168\n\t"
      "ds_read_b128 v[96:99], v255 offset:8192\n\t"
      "ds_read_b128 v[100:103], v255 offset:9216\n\t"
      "ds_read_b128 v[104:107], v255 offset:10240\n\t"
      "ds_read_b128 v[108:111], v255 offset:11264\n\t"
      "s_cmp_ge_u32 s48, 32\n\t"
      "s_cbranch_scc1 WO3%=\n\t"
      "v_mov_b64 v[20:21], -1\n\t"
      "v_mov_b64 v[22:23], -1\n\t"
      "buffer_store_dwordx4 v[20:23], v238, s[36:39], s98 offen sc1\n\t"
      "s_bitcmp0_b32 s48, 2\n\t"
      "s_cselect_b32 s69, 1, 63\n\t"
      "s_lshl_b32 s69, s69, 14\n\t"
      "s_add_u32 s98, s98, s69\n\t"
      "WO3%=:\n\t"
      "v_lshlrev_b32 v16, 4, v248\n\t"
      "s_cmp_ge_u32 s48, 60\n\t"
      "s_cbranch_scc1 WO1%=\n\t"
      "buffer_load_dwordx4 v[228:231], off, s[36:39], s56 sc1\n\t"
      "s_mov_b32 s97, s96\n\t"
      "v_mov_b64 v[232:233], 0\n\t"
      "s_cmp_lg_u32 s65, 0\n\t"
      "s_cselect_b32 s70, s89, s49\n\t"
      "s_cmp_ge_u32 s70, s48\n\t"
      "s_cbranch_scc1 WO1%=\n\t"
      "buffer_load_dword v232, v16, s[36:39], s55 offen sc1\n\t"
      "buffer_load_dword v233, v16, s[36:39], s55 offen offset:128 sc1\n\t"
      "WO1%=:\n\t"
      "s_cmp_lt_u32 s48, 56\n\t"
      "s_cselect_b32 s69, 3145728, 0\n\t"
      "s_add_u32 s50, s50, s69\n\t"
      "s_mov_b32 s24, 0\n\t"
      "s_cmp_eq_u32 s65, 2\n\t"
      "s_cbranch_scc1 WOM2%=\n\t"
      "s_cmp_lt_u32 s48, 60\n\t"
      "s_cselect_b32 s69, s65, 0\n\t"
      "s_cmp_lg_u32 s69, 0\n\t"
      "s_cbranch_scc1 WOM1%=\n\t"
      "s_cmp_lg_u32 s96, 0\n\t"
      "s_cbranch_scc1 WOL1%=\n\t"
      "WOL0%=:\n\t"
      "s_waitcnt lgkmcnt(11)\n\t"
      "v_mfma_f32_16x16x32_bf16 v[0:3], a[0:3], v[64:67], 0\n\t"
      "v_mfma_f32_16x16x32_bf16 v[4:7], a[64:67], v[64:67], 0\n\t"
      "ds_read_b128 v[112:115], v255 offset:12288\n\t"
      "s_waitcnt lgkmcnt(11)\n\t"
      "v_mfma_f32_16x16x32_bf16 v[8:11], a[0:3], v[68:71], 0\n\t"
      "v_mfma_f32_16x16x32_bf16 v[12:15], a[64:67], v[68:71], 0\n\t"
      "ds_read_b128 v[116:119], v255 offset:13312\n\t"
      "s_waitcnt lgkmcnt(11)\n\t"
      "v_mfma_f32_16x16x32_bf16 v[0:3], a[4:7], v[72:75], v[0:3]\n\t"
      "v_mfma_f32_16x16x32_bf16 v[4:7], a[68:71], v[72:75], v[4:7]\n\t"
      "ds_read_b128 v[120:123], v255 offset:14336\n\t"
      "s_waitcnt lgkmcnt(11)\n\t"
      "v_mfma_f32_16x16x32_bf16 v[8:11], a[4:7], v[76:79], v[8:11]\n\t"
      "v_mfma_f32_16x16x32_bf16 v[12:15], a[68:71], v[76:79], v[12:15]\n\t"
      "ds_read_b128 v[124:127], v255 offset:15360\n\t"
      "s_waitcnt lgkmcnt(11)\n\t"
      "v_mfma_f32_16x16x32_bf16 v[0:3], a[8:11], v[80:83], v[0:3]\n\t"
      "v_mfma_f32_16x16x32_bf16 v[4:7], a[72:75], v[80:83], v[4:7]\n\t"
      "ds_read_b128 v[64:67], v255 offset:16384\n\t"
      "s_waitcnt lgkmcnt(11)\n\t"
      "v_mfma_f32_16x16x32_bf16 v[8:11], a[8:11], v[84:87], v[8:11]\n\t"
      "v_mfma_f32_16x16x32_bf16 v[12:15], a[72:75], v[84:87], v[12:15]\n\t"
      "ds_read_b128 v[68:71], v255 offset:17408\n\t"
      "s_waitcnt lgkmcnt(11)\n\t"
      "v_mfma_f32_16x16x32_bf16 v[0:3], a[12:15], v[88:91], v[0:3]\n\t"
      "v_mfma_f32_16x16x32_bf16 v[4:7], a[76:79], v[88:91], v[4:7]\n\t"
      "ds_read_b128 v[72:75], v255 offset:18432\n\t"
      "s_waitcnt lgkmcnt(11)\n\t"
      "v_mfma_f32_16x16x32_bf16 v[8:11], a[12:15], v[92:95], v[8:11]\n\t"
      "v_mfma_f32_16x16x32_bf16 v[12:15], a[76:79], v[92:95], v[12:15]\n\t"
      "ds_read_b128 v[76:79], v255 offset:19456\n\t"
      "s_waitcnt lgkmcnt(11)\n\t"
      "v_mfma_f32_16x16x32_bf16 v[0:3], a[16:19], v[96:99], v[0:3]\n\t"
      "v_mfma_f32_16x16x32_bf16 v[4:7], a[80:83], v[96:99], v[4:7]\n\t"
      "ds_read_b128 v[80:83], v255 offset:20480\n\t"
      "s_waitcnt lgkmcnt(11)\n\t"
      "v_mfma_f32_16x16x32_bf16 v[8:11], a[16:19], v[100:103], v[8:11]\n\t"
      "v_mfma_f32_16x16x32_bf16 v[12:15], a[80:83], v[100:103], v[12:15]\n\t"
      "ds_read_b128 v[84:87], v255 offset:21504\n\t"
      "s_waitcnt lgkmcnt(11)\n\t"
      "v_mfma_f32_16x16x32_bf16 v[0:3], a[20:23], v[104:107], v[0:3]\n\t"
      "v_mfma_f32_16x16x32_bf16 v[4:7], a[84:87], v[104:107], v[4:7]\n\t"
      "ds_read_b128 v[88:91], v255 offset:22528\n\t"
      "s_waitcnt lgkmcnt(11)\n\t"
      "v_mfma_f32_16x16x32_bf16 v[8:11], a[20:23], v[108:111], v[8:11]\n\t"
      "v_mfma_f32_16x16x32_bf16 v[12:15], a[84:87], v[108:111], v[12:15]\n\t"
      "ds_read_b128 v[92:95], v255 offset:23552\n\t"
      "s_waitcnt lgkmcnt(11)\n\t"
      "v_mfma_f32_16x16x32_bf16 v[0:3], a[24:27], v[112:115], v[0:3]\n\t"
      "v_mfma_f32_16x16x32_bf16 v[4:7], a[88:91], v[112:115], v[4:7]\n\t"
      "ds_read_b128 v[96:99], v255 offset:24576\n\t"
      "s_waitcnt lgkmcnt(11)\n\t"
      "v_mfma_f32_16x16x32_bf16 v[8:11], a[24:27], v[116:119], v[8:11]\n\t"
      "v_mfma_f32_16x16x32_bf16 v[12:15], a[88:91], v[116:119], v[12:15]\n\t"
      "ds_read_b128 v[100:103], v255 offset:25600\n\t"
      "s_waitcnt lgkmcnt(11)\n\t"
      "v_mfma_f32_16x16x32_bf16 v[0:3], a[28:31], v[120:123], v[0:3]\n\t"
      "v_mfma_f32_16x16x32_bf16 v[4:7], a[92:95], v[120:123], v[4:7]\n\t"
      "ds_read_b128 v[104:107], v255 offset:26624\n\t"
      "s_waitcnt lgkmcnt(11)\n\t"
      "v_mfma_f32_16x16x32_bf16 v[8:11], a[28:31], v[124:127], v[8:11]\n\t"
      "v_mfma_f32_16x16x32_bf16 v[12:15], a[92:95], v[124:127], v[12:15]\n\t"
      "ds_read_b128 v[108:111], v255 offset:27648\n\t"
      "s_waitcnt lgkmcnt(11)\n\t"
      "v_mfma_f32_16x16x32_bf16 v[0:3], a[32:35], v[64:67], v[0:3]\n\t"
      "v_mfma_f32_16x16x32_bf16 v[4:7], a[96:99], v[64:67], v[4:7]\n\t"
      "ds_read_b128 v[112:115], v255 offset:28672\n\t"
      "s_waitcnt lgkmcnt(11)\n\t"
      "v_mfma_f32_16x16x32_bf16 v[8:11], a[32:35], v[68:71], v[8:11]\n\t"
      "v_mfma_f32_16x16x32_bf16 v[12:15], a[96:99], v[68:71], v[12:15]\n\t"
      "ds_read_b128 v[116:119], v255 offset:29696\n\t"
      "s_waitcnt lgkmcnt(11)\n\t"
      "v_mfma_f32_16x16x32_bf16 v[0:3], a[36:39], v[72:75], v[0:3]\n\t"
      "v_mfma_f32_16x16x32_bf16 v[4:7], a[100:103], v[72:75], v[4:7]\n\t"
      "ds_read_b128 v[120:123], v255 offset:30720\n\t"
      "s_waitcnt lgkmcnt(11)\n\t"
      "v_mfma_f32_16x16x32_bf16 v[8:11], a[36:39], v[76:79], v[8:11]\n\t"
      "v_mfma_f32_16x16x32_bf16 v[12:15], a[100:103], v[76:79], v[12:15]\n\t"
      "ds_read_b128 v[124:127], v255 offset:31744\n\t"
      "s_waitcnt lgkmcnt(11)\n\t"
      "v_mfma_f32_16x16x32_bf16 v[0:3], a[40:43], v[80:83], v[0:3]\n\t"
      "v_mfma_f32_16x16x32_bf16 v[4:7], a[104:107], v[80:83], v[4:7]\n\t"
      "s_waitcnt lgkmcnt(10)\n\t"
      "v_mfma_f32_16x16x32_bf16 v[8:11], a[40:43], v[84:87], v[8:11]\n\t"
      "v_mfma_f32_16x16x32_bf16 v[12:15], a[104:107], v[84:87], v[12:15]\n\t"
      "s_waitcnt lgkmcnt(9)\n\t"
      "v_mfma_f32_16x16x32_bf16 v[0:3], a[44:47], v[88:91], v[0:3]\n\t"
      "v_mfma_f32_16x16x32_bf16 v[4:7], a[108:111], v[88:91], v[4:7]\n\t"
      "s_waitcnt lgkmcnt(8)\n\t"
      "v_mfma_f32_16x16x32_bf16 v[8:11], a[44:47], v[92:95], v[8:11]\n\t"
      "v_mfma_f32_16x16x32_bf16 v[12:15], a[108:111], v[92:95], v[12:15]\n\t"
      "s_waitcnt lgkmcnt(7)\n\t"
      "v_mfma_f32_16x16x32_bf16 v[0:3], a[48:51], v[96:99], v[0:3]\n\t"
      "v_mfma_f32_16x16x32_bf16 v[4:7], a[112:115], v[96:99], v[4:7]\n\t"
      "s_waitcnt lgkmcnt(6)\n\t"
      "v_mfma_f32_16x16x32_bf16 v[8:11], a[48:51], v[100:103], v[8:11]\n\t"
      "v_mfma_f32_16x16x32_bf16 v[12:15], a[112:115], v[100:103], v[12:15]\n\t"
      "s_waitcnt lgkmcnt(5)\n\t"
      "v_mfma_f32_16x16x32_bf16 v[0:3], a[52:55], v[104:107], v[0:3]\n\t"
      "v_mfma_f32_16x16x32_bf16 v[4:7], a[116:119], v[104:107], v[4:7]\n\t"
      "s_waitcnt lgkmcnt(4)\n\t"
      "v_mfma_f32_16x16x32_bf16 v[8:11], a[52:55], v[108:111], v[8:11]\n\t"
      "v_mfma_f32_16x16x32_bf16 v[12:15], a[116:119], v[108:111], v[12:15]\n\t"
      "s_waitcnt lgkmcnt(3)\n\t"
      "v_mfma_f32_16x16x32_bf16 v[0:3], a[56:59], v[112:115], v[0:3]\n\t"
      "v_mfma_f32_16x16x32_bf16 v[4:7], a[120:123], v[112:115], v[4:7]\n\t"
      "s_waitcnt lgkmcnt(2)\n\t"
      "v_mfma_f32_16x16x32_bf16 v[8:11], a[56:59], v[116:119], v[8:11]\n\t"
      "v_mfma_f32_16x16x32_bf16 v[12:15], a[120:123], v[116:119], v[12:15]\n\t"
      "s_waitcnt lgkmcnt(1)\n\t"
      "v_mfma_f32_16x16x32_bf16 v[0:3], a[60:63], v[120:123], v[0:3]\n\t"
      "v_mfma_f32_16x16x32_bf16 v[4:7], a[124:127], v[120:123], v[4:7]\n\t"
      "s_waitcnt lgkmcnt(0)\n\t"
      "v_mfma_f32_16x16x32_bf16 v[8:11], a[60:63], v[124:127], v[8:11]\n\t"
      "v_mfma_f32_16x16x32_bf16 v[12:15], a[124:127], v[124:127], v[12:15]\n\t"
      "v_xor_b32 v254, 0x8000, v254\n\t"
      "v_xor_b32 v255, 0x8000, v255\n\t"
      "s_add_u32 s69, s53, 0x4000\n\t"
      "s_add_u32 s48, s48, 4\n\t"
      "s_waitcnt vmcnt(40)\n\t"
      "v_cvt_pk_bf16_f32 v0, v0, v1\n\t"
      "v_cvt_pk_bf16_f32 v1, v2, v3\n\t"
      "v_cvt_pk_bf16_f32 v2, v4, v5\n\t"
      "v_cvt_pk_bf16_f32 v3, v6, v7\n\t"
      "v_min_u32 v0, -2, v0\n\t"
      "v_min_u32 v1, -2, v1\n\t"
      "v_min_u32 v2, -2, v2\n\t"
      "v_min_u32 v3, -2, v3\n\t"
      "buffer_store_dwordx4 v[0:3], v238, s[36:39], s53 offen sc1\n\t"
      "v_cvt_pk_bf16_f32 v8, v8, v9\n\t"
      "v_cvt_pk_bf16_f32 v9, v10, v11\n\t"
      "v_cvt_pk_bf16_f32 v10, v12, v13\n\t"
      "v_cvt_pk_bf16_f32 v11, v14, v15\n\t"
      "v_min_u32 v8, -2, v8\n\t"
      "v_min_u32 v9, -2, v9\n\t"
      "v_min_u32 v10, -2, v10\n\t"
      "v_min_u32 v11, -2, v11\n\t"
      "buffer_store_dwordx4 v[8:11], v238, s[36:39], s69 offen sc1\n\t"
      "s_add_u32 s53, s53, 1048576\n\t"
      "s_nop 1\n\t"
      "s_branch WTOP%=\n\t"
      ".p2align 5\n\t"
      "WEND%=:\n\t"
      : "+{v[0:15]}"(c0), "+{v[16:31]}"(c1), "+{v[240:255]}"(c2), "+{v238}"(c3), "+{v[32:39]}"(c4), "+s"(w), "+s"(wv), "+s"(A),
        "+{a[0:31]}"(wo.a), "+{a[32:63]}"(wo.b), "+{a[64:95]}"(wo.c), "+{a[96:127]}"(wo.d)
      :
      : "memory", "vcc", "scc", "v40", "v41", "v42", "v43", "v44", "v45", "v46", "v47", "v48", "v49", "v50", "v51", "v52", "v53", "v54", "v55", "v56", "v57", "v58", "v59", "v60", "v61", "v62", "v63", "v64", "v65", "v66", "v67", "v68", "v69", "v70", "v71", "v72", "v73", "v74", "v75", "v76", "v77", "v78", "v79", "v80", "v81", "v82", "v83", "v84", "v85", "v86", "v87", "v88", "v89", "v90", "v91", "v92", "v93", "v94", "v95", "v96", "v97", "v98", "v99", "v100", "v101", "v102", "v103", "v104", "v105", "v106", "v107", "v108", "v109", "v110", "v111", "v112", "v113", "v114", "v115", "v116", "v117", "v118", "v119", "v120", "v121", "v122", "v123", "v124", "v125", "v126", "v127", "v128", "v129", "v130", "v131", "v132", "v133", "v134", "v135", "v136", "v137", "v138", "v139", "v140", "v141", "v142", "v143", "v144", "v145", "v146", "v147", "v148", "v149", "v150", "v151", "v152", "v153", "v154", "v155", "v156", "v157", "v158", "v159", "v160", "v161", "v162", "v163", "v164", "v165", "v166", "v167", "v168", "v169", "v170", "v171", "v172", "v173", "v174", "v175", "v176", "v177", "v178", "v179", "v180", "v181", "v182", "v183", "v184", "v185", "v186", "v187", "v188", "v189", "v190", "v191", "v192", "v193", "v194", "v195", "v196", "v197", "v198", "v199", "v200", "v201", "v202", "v203", "v204", "v205", "v206", "v207", "v208", "v209", "v210", "v211", "v212", "v213", "v214", "v215", "v216", "v217", "v218", "v219", "v220", "v221", "v222", "v223", "v224", "v225", "v226", "v227", "v228", "v229", "v230", "v231", "v232", "v233", "v234", "v235", "v236", "v237", "v239", "a128", "a129", "a130", "a131", "a132", "a133", "a134", "a135", "a136", "a137", "a138", "a139", "a140", "a141", "a142", "a143", "a144", "a145", "a146", "a147", "a148", "a149", "a150", "a151", "a152", "a153", "a154", "a155", "a156", "a157", "a158", "a159", "a160", "a161", "a162", "a163", "a164", "a165", "a166", "a167", "a168", "a169", "a170", "a171", "a172", "a173", "a174", "a175", "a176", "a177", "a178", "a179", "a180", "a181", "a182", "a183", "a184", "a185", "a186", "a187", "a188", "a189", "a190", "a191", "a192", "a193", "a194", "a195", "a196", "a197", "a198", "a199", "a200", "a201", "a202", "a203", "a204", "a205", "a206", "a207", "a208", "a209", "a210", "a211", "a212", "a213", "a214", "a215", "a216", "a217", "a218", "a219", "a220", "a221", "a222", "a223", "a224", "a225", "a226", "a227", "a228", "a229", "a230", "a231", "a232", "a233", "a234", "a235", "a236", "a237", "a238", "a239", "a240", "a241", "a242", "a243", "a244", "a245", "a246", "a247", "a248", "a249", "a250", "a251", "a252", "a253", "a254", "a255", "s24", "s36", "s37", "s38", "s39", "s40", "s41", "s42", "s43", "s44", "s45", "s46", "s47", "s48", "s49", "s50", "s51", "s52", "s53", "s54", "s55", "s56", "s57", "s58", "s59", "s60", "s61", "s62", "s63", "s64", "s65", "s66", "s67", "s68", "s69", "s70", "s71", "s72", "s73", "s74", "s75", "s76", "s77", "s78", "s79", "s80", "s81", "s82", "s83", "s84", "s85", "s86", "s87", "s88", "s89", "s90", "s91", "s92", "s93", "s94", "s95", "s96", "s97", "s98", "s99");
}

__global__ void __launch_bounds__(256, 1) mega_kernel(MegaParams P) {
  __shared__ __attribute__((aligned(16))) char lds[LDS_BYTES];
  int b, wv;
  KArgs A = (KArgs)__builtin_amdgcn_kernarg_segment_ptr();
  u32x16 ka; u32x8 kb;
  asm volatile("s_load_dwordx16 s[36:51], %4, 0\n\t"
               "s_load_dwordx8 s[52:59], %4, 64\n\t"
               "s_getpc_b64 s[60:61]\n"
               "KPC:\n\t"
               "s_lshr_b32 %0, %5, 3\n\t"
               "s_and_b32 %0, %0, 7\n\t"
               "s_lshl_b32 %0, %0, 14\n\t"
               "v_lshl_add_u32 v254, %6, 6, %0\n\t"
               "v_min_u32 v254, KENDW-KPC, v254\n\t"
               "global_load_dword v255, v254, s[60:61]\n\t"
               "v_readfirstlane_b32 %1, %6\n\t"
               "s_lshr_b32 %0, %5, 1\n\t"
               "s_andn2_b32 %0, %0, 3\n\t"
               "s_and_b32 s60, %5, 3\n\t"
               "s_or_b32 %0, %0, s60\n\t"
               "s_lshr_b32 %1, %1, 6\n\t"
               "s_bitcmp1_b32 %5, 2\n\t"
               "s_cbranch_scc1 GSTART\n\t"
               "s_waitcnt lgkmcnt(0)"
               : "=&{s34}"(b), "=&{s33}"(wv), "=&{s[36:51]}"(ka), "=&{s[52:59]}"(kb), "+{s[0:1]}"(A)
               : "s"(blockIdx.x), "v"(threadIdx.x) : "v252", "v253", "v254", "v255", "s60", "s61", "s62", "s63", "scc");
  static_assert(XS == 0, "");
  if (b >= NREC) {
    int w = b, wv2 = wv;
    WoRegs wo;
    const unsigned tag = worker_role(w, wv2, A, wo);
    worker_tasks(lds, w, wv2, A, tag, wo);
    asm volatile(".p2align 6");
    if (w == 0 && wv2 == 0) {
      const int lane = fresh_lane();
#pragma unroll 1
      for (unsigned spins = 0; spins < (1u << 16); ++spins) {
        const u32x2 sel = top2_wave((const float*)(A->ws + O_RBUF) + S_, lane, spins == 0, spins < 8);
        if (spins && sel[1]) {
          if (lane < 8) __hip_atomic_store((gu32*)(A->ws + O_CNT + lane * 64 + 8), (unsigned)__builtin_amdgcn_s_memrealtime(), RLX_AGENT);
          if (lane < 8) __hip_atomic_store((gu64*)(A->ws + O_CNT + lane * 64), (u64)sel[0] | ((u64)sel[1] << 32), RLX_AGENT);
          break;
        }
        __builtin_amdgcn_s_sleep(SSLEEP);
        if (spins == 0 && SWAIT) {
          unsigned f = 0;
#pragma unroll 1
          for (unsigned n = 0; n < 4096; ++n) {
            f = __builtin_amdgcn_readfirstlane(__hip_atomic_load((gu32*)(A->ws + O_KVSTEP), RLX_AGENT));
            if ((f >> 16) >= 61) break;
            __builtin_amdgcn_s_sleep(4);
          }
          f += SWAIT - ((f >> 16) - 61) * 156;
#pragma unroll 1
          while (((f - (unsigned)__builtin_amdgcn_s_memrealtime()) & 0xffffu) < 400u) __builtin_amdgcn_s_sleep(1);
        }
      }
    }
    __syncthreads();
    if ((w & 2) == 0) mlp2_role(A, lds, (w & 1) * 32 + (w >> 2), wv2 * 64 + fresh_lane());
    asm volatile("KENDW:");
    __builtin_amdgcn_endpgm();
    __builtin_unreachable();
  } else {
    KArgs kp = A;
    rec_role(lds, kp, b, wv, ka, kb);
    asm volatile(".p2align 6");
    const int tid = wv * 64 + fresh_lane();
    __syncthreads();
    if ((b & 31) < M1_WGS / 4) mlp1_role(kp, lds, (b & 31) * 4 + (b >> 5), tid);
    else if (SW2 && b == M1_WGS / 4 + 1) {
      if (wv == 0) {
        const unsigned t0 = (unsigned)__builtin_amdgcn_s_memrealtime();
#pragma unroll 1
        for (unsigned spins = 0; spins < (1u << 16); ++spins) {
          const u32x2 sel = top2_wave((const float*)(kp->ws + O_RBUF) + S_, tid, spins == 0, false);
          if (spins && sel[1]) {
            if (tid < 32) __hip_atomic_store((gu64*)(kp->ws + O_SEL2 + tid * 128), (u64)sel[0] | ((u64)sel[1] << 32), RLX_AGENT);
            break;
          }
#pragma unroll 1
          while ((unsigned)__builtin_amdgcn_s_memrealtime() - t0 < SW2) __builtin_amdgcn_s_sleep(1);
        }
      }
    }
    else if (b == M1_WGS / 4) {
      if (tid == 64) {
        gu32* e = (gu32*)(kp->ws + O_EPOCH);
        __hip_atomic_store(e, mktag(__hip_atomic_load(e, RLX_AGENT)), RLX_AGENT);
      }
    }
    __builtin_amdgcn_endpgm();
    __builtin_unreachable();
  }
}

extern "C" void kernel_launch(void* const* d_in, const int* in_sizes, int n_in, void* d_out, int out_size, void* d_ws, size_t ws_size,
                              hipStream_t stream) {
  MegaParams P;
  P.x = (const u16*)d_in[0]; P.mem0 = (const u16*)d_in[1];
  P.Wq = (const u16*)d_in[2]; P.Wk = (const u16*)d_in[3]; P.Wv = (const u16*)d_in[4]; P.Wo = (const u16*)d_in[5];
  P.w_r = (const u16*)d_in[8]; P.wg = (const u16*)d_in[9]; P.wu = (const u16*)d_in[10]; P.wd = (const u16*)d_in[11];
  P.out = (u16*)d_out; P.ws = (char*)d_ws;
  mega_kernel<<<256, 256, 0, stream>>>(P);
}
```

```cpp
#include <hip/hip_runtime.h>
#include <hip/hip_bf16.h>
#define SWAIT 0
#define SW2 0
#define ASLEEP 32
#define ASALL 0
#define PSLEEP 8
#define FSLEEP 6
#define RDLY 210
#define RDL2 100
#define ADLY 130
#define CPF 1
#define TCH 0
#define SNEED 0
#define MPF 1
#define RDO 100

typedef unsigned short u16;
typedef unsigned long long u64;
typedef __attribute__((ext_vector_type(8))) short bf16x8;
typedef __attribute__((ext_vector_type(4))) float f32x4;
typedef __attribute__((ext_vector_type(4))) unsigned u32x4;
#define GAS __attribute__((address_space(1)))
typedef GAS u64 gu64;
typedef GAS unsigned gu32;
#define RLX_AGENT __ATOMIC_RELAXED, __HIP_MEMORY_SCOPE_AGENT

constexpr int S_ = 2048, DM = 512, NH = 32, DH = 128, MM = 8, SEG = 32, NSEG = 64;
constexpr int HD = NH * DH;
constexpr int QKVN = 3 * HD;
constexpr int FF = 1792;

__device__ __forceinline__ float bf2f(u16 v) { return __builtin_bit_cast(float, (unsigned)v << 16); }
__device__ __forceinline__ u16 f2bf(float f) { unsigned u = __builtin_bit_cast(unsigned, f); return (u16)((u + 0x7fffu + ((u >> 16) & 1u)) >> 16); }
__device__ __forceinline__ float rbf(float f) { return bf2f(f2bf(f)); }

typedef __attribute__((ext_vector_type(2))) float f32x2;
typedef __attribute__((ext_vector_type(2))) __bf16 bf16x2_t;
typedef __attribute__((ext_vector_type(2))) unsigned u32x2;
__device__ __forceinline__ unsigned pk2(float a, float b) { return __builtin_bit_cast(unsigned, __builtin_convertvector((f32x2){a, b}, bf16x2_t)); }
__device__ __forceinline__ unsigned f2u(float f) { return __builtin_bit_cast(unsigned, f); }
__device__ __forceinline__ float u2f(unsigned u) { return __builtin_bit_cast(float, u); }
__device__ __forceinline__ float lo2f(unsigned u) { return __builtin_bit_cast(float, u << 16); }
__device__ __forceinline__ float hi2f(unsigned u) { return __builtin_bit_cast(float, u & 0xffff0000u); }
__device__ __forceinline__ void unpack8(const u32x4 v, float (&f)[8]) {
#pragma unroll
  for (int i = 0; i < 4; ++i) { f[2 * i] = lo2f(v[i]); f[2 * i + 1] = hi2f(v[i]); }
}

constexpr unsigned O_EPOCH = 0;
constexpr unsigned O_TFLAG = 256;
constexpr unsigned O_MFLAG = O_TFLAG + 16 * 32 * 64;
constexpr unsigned O_LFLAG = O_MFLAG + 32 * 128;
constexpr unsigned O_WM = O_LFLAG + 128 * 128;
constexpr unsigned O_KVSTEP = O_WM + 128 * 128;
constexpr unsigned O_CNT = O_KVSTEP + 2048;
constexpr unsigned O_PFLAG = O_CNT + 2048;
constexpr unsigned O_SEL2 = O_PFLAG + 4 * 128 * 128;
constexpr unsigned O_ACT = O_SEL2 + 32 * 128;
constexpr unsigned O_RBUF = O_ACT + 3 * 112 * 128;
constexpr unsigned O_INIT_END = O_RBUF + 2 * S_ * 4 + 256 * 128;
constexpr unsigned O_STAMPS = 196608 + 65 * 32768;
constexpr unsigned O_XN = O_STAMPS + (4 << 20);
constexpr unsigned O_QKV = O_XN + S_ * DM * 2;
constexpr unsigned O_KVM = O_QKV + S_ * QKVN * 2;
constexpr unsigned O_VTM = O_KVM + NSEG * 8 * HD * 2;
constexpr unsigned O_PART = O_VTM + NSEG * 8 * HD * 2;
constexpr unsigned O_HBUF = O_PART + S_ * 8 * DM * 4;
constexpr unsigned O_LNH = O_HBUF + S_ * DM * 2;
constexpr unsigned O_MFRAG = O_LNH + S_ * DM * 2;
constexpr unsigned O_SLOTS = (O_MFRAG + 2 * DM * FF * 2 + 0x1fffff) & ~0x1fffffu;
constexpr unsigned WS_BYTES = O_SLOTS + 65 * 32768;
#define QO(i) (((i) & 1) * 16384 + ((i) >> 1) * 8192)
#define CAL(b) (O_SLOTS + 128 + ((b) >> 3) * 16384 + ((b) & 7) * 1024)
static_assert(O_INIT_END <= O_STAMPS && (O_INIT_END - O_KVSTEP) / 16 <= 128 * 128, "layout");
static_assert(O_TFLAG == 256 && O_MFLAG == 33024 && O_LFLAG == 37120 && O_STAMPS == 2326528 && O_XN == 6520832 && O_QKV == 8617984 && O_KVM == 58949632 && O_VTM == 63143936 && O_PART == 67338240 && O_MFRAG == 105086976 && O_SLOTS == 109051904 && O_CNT + 1024 == 72960 && O_INIT_END <= 262144 && O_WM == 53504, "layout");
constexpr int L_BS = 520, L_QN = 49152, L_TB = 98304, L_VT = 32768;
struct MegaParams {
  const u16* x; const u16* mem0; const u16* Wq; const u16* Wk; const u16* Wv; const u16* Wo;
  const u16* w_r; const u16* wg; const u16* wu; const u16* wd;
  u16* out;
  char* ws;
};
__device__ __forceinline__ unsigned mktag(unsigned e) { return e * 0x9e3779b1u + 0x7f4a7c15u; }

typedef __attribute__((ext_vector_type(4))) short s16x4;
#define LAS __attribute__((address_space(3)))
constexpr int LDS_BYTES = 163840;
#define SB() __builtin_amdgcn_sched_barrier(0)
#define RARE(x) __builtin_expect_with_probability(!!(x), 0, 0.9999)
#define LDS_BARRIER() asm volatile("s_waitcnt lgkmcnt(0)\n\ts_barrier" ::: "memory")
#define VM_DRAIN() asm volatile("s_waitcnt vmcnt(0)" ::: "memory")
__device__ __forceinline__ float xmax16_32(float m) {
  u32x2 p = __builtin_amdgcn_permlane16_swap(f2u(m), f2u(m), false, false);
  m = fmaxf(u2f(p[0]), u2f(p[1]));
  p = __builtin_amdgcn_permlane32_swap(f2u(m), f2u(m), false, false);
  return fmaxf(u2f(p[0]), u2f(p[1]));
}
__device__ __forceinline__ float xsum16_32(float m) {
  u32x2 p = __builtin_amdgcn_permlane16_swap(f2u(m), f2u(m), false, false);
  m = u2f(p[0]) + u2f(p[1]);
  p = __builtin_amdgcn_permlane32_swap(f2u(m), f2u(m), false, false);
  return u2f(p[0]) + u2f(p[1]);
}
__device__ __forceinline__ unsigned wave_max_u32(unsigned v) {
  v = max(v, (unsigned)__builtin_amdgcn_mov_dpp(v, 0xb1, 0xf, 0xf, true));
  v = max(v, (unsigned)__builtin_amdgcn_mov_dpp(v, 0x4e, 0xf, 0xf, true));
  v = max(v, (unsigned)__builtin_amdgcn_mov_dpp(v, 0x124, 0xf, 0xf, true));
  v = max(v, (unsigned)__builtin_amdgcn_mov_dpp(v, 0x128, 0xf, 0xf, true));
  u32x2 p = __builtin_amdgcn_permlane16_swap(v, v, false, false);
  v = max(p[0], p[1]);
  p = __builtin_amdgcn_permlane32_swap(v, v, false, false);
  return max(p[0], p[1]);
}
__device__ __forceinline__ u32x4 ld_sc1(__amdgpu_buffer_rsrc_t rs, unsigned off) { return __builtin_amdgcn_raw_buffer_load_b128(rs, off, 0, 16); }
__device__ __forceinline__ u32x2 ld8_sc1(__amdgpu_buffer_rsrc_t rs, unsigned off) { return __builtin_amdgcn_raw_buffer_load_b64(rs, off, 0, 16); }

#define XS 0
#define VBX __builtin_amdgcn_readfirstlane((int)(((blockIdx.x & 4) << 5) + (blockIdx.x >> 3) * 4 + (blockIdx.x & 3)))
constexpr int NREC = 128, NWORK = 128;
typedef __attribute__((ext_vector_type(8))) unsigned u32x8;
typedef __attribute__((ext_vector_type(16))) unsigned u32x16;
typedef __attribute__((ext_vector_type(32))) unsigned u32x32;
#define RSRC(p, n) __builtin_amdgcn_make_buffer_rsrc((void*)(p), 0, (n), 0x00020000)

__device__ __forceinline__ int fresh_lane() {
  int l;
  asm volatile("v_mbcnt_lo_u32_b32 %0, -1, 0\n\tv_mbcnt_hi_u32_b32 %0, -1, %0" : "=v"(l));
  return l;
}
typedef const __attribute__((address_space(4))) MegaParams* KArgs;
#define ARGS() KArgs A = (KArgs)__builtin_amdgcn_kernarg_segment_ptr(); asm volatile("" : "+s"(A))
#define MS(k)
__device__ __forceinline__ void rec_role(char* lds, KArgs& A, int& b, int& wv, const u32x16 ka, const u32x8 kb) {
#define HMAP 0
  const int h = HMAP == 1 ? (b & 3) * 8 + ((b >> 2) & 7) : (HMAP == 2 ? (b & 1) * 16 + (b & 2) / 2 + ((b >> 2) & 7) * 2 : b & 31), part = b >> 5;
  const int lane = fresh_lane(), l15 = lane & 15, l4 = lane >> 4;
  const int b1 = (l15 >> 1) & 1, mrow = 2 * (l15 >> 2) + (l15 & 1);
  const int r4 = l15 >> 2, c4 = l15 & 3;
#define KA(v, i) ((u64)v[i] | (u64)v[(i) + 1] << 32)
  const u64 wsa = KA(kb, 6), woa = KA(ka, 10), wva = KA(ka, 8), wqa = KA(ka, 4), wka = KA(ka, 6), m0a = KA(ka, 2);
  const unsigned awv = (unsigned)((8 * wv + l4) * 8192 + h * 256 + ((l15 ^ (l4 | (wv << 2))) << 4)), rot = 1 ? part : 0;
  const int sig = 1 ? (h >> 2) & 1 : 0;
  const int sig2 = 1 ? (h >> 3) & 1 : 0;
  unsigned tch;
  asm volatile(
      "s_lshl_b32 s91, %5, 4\n\t"
      "s_add_u32 m0, %5, 0\n\t"
      "s_xor_b32 s90, s91, 0\n\t"
      "buffer_load_dwordx4 %1, %3, s90 offen lds\n\t"
      "s_add_u32 m0, %5, 1024\n\t"
      "s_xor_b32 s90, s91, 16384\n\t"
      "buffer_load_dwordx4 %1, %3, s90 offen lds\n\t"
      "s_add_u32 m0, %5, 8192\n\t"
      "s_xor_b32 s90, s91, 32768\n\t"
      "buffer_load_dwordx4 %1, %3, s90 offen lds\n\t"
      "s_add_u32 m0, %5, 9216\n\t"
      "s_xor_b32 s90, s91, 49152\n\t"
      "buffer_load_dwordx4 %1, %3, s90 offen lds\n\t"
      "s_add_u32 m0, %5, 16384\n\t"
      "s_xor_b32 s90, s91, 65536\n\t"
      "buffer_load_dwordx4 %1, %3, s90 offen lds\n\t"
      "s_add_u32 m0, %5, 17408\n\t"
      "s_xor_b32 s90, s91, 81920\n\t"
      "buffer_load_dwordx4 %1, %3, s90 offen lds\n\t"
      "s_add_u32 m0, %5, 24576\n\t"
      "s_xor_b32 s90, s91, 98304\n\t"
      "buffer_load_dwordx4 %1, %3, s90 offen lds\n\t"
      "s_add_u32 m0, %5, 25600\n\t"
      "s_xor_b32 s90, s91, 114688\n\t"
      "buffer_load_dwordx4 %1, %3, s90 offen lds\n\t"
      : "=&{s87}"(tch)
      : "v"((unsigned)((h * DH + (lane >> 2)) * DM + 128 * part + 32 * wv + 8 * (2 * (lane & 1) + (((lane >> 1) & 1) ^ sig))) * 2), "v"(awv), "s"(RSRC(woa, DM * HD * 2)), "s"(RSRC(wva, DM * HD * 2)),
        "s"(wv * 2048), "s"(rot << 19), "s"(wva), "s"(wqa), "s"(wka), "s"(wsa), "s"(m0a)
      : "memory", "scc", "s90", "s91", "s92");
  SB();
  const unsigned i0 = 1024 + b * 512 + wv * 64 + lane, o1 = O_KVSTEP + ((wv >> 1) * 8192 + b * 64 + lane) * 16;
  const unsigned oz = wv == 2 ? CAL(b ^ 1) + (lane >> 5) * 8192 + ((lane >> 3) & 3) * 256 + (lane & 7) * 16 : ((wv & 1) && o1 < O_INIT_END ? o1 : 0xffffff00u);
  const int sl = lane ^ ((l4 & 1) << 1), su = ((l15 >> 3) * 16 + c4 * 4 + l4) ^ ((l15 >> 3) << 1);
  const int jt = wv ^ rot;
  u32x8 cc = {oz, (unsigned)lane, (unsigned)lane,
              wv == 2 || o1 < O_ACT ? 0u : ~0u,
              (unsigned)(QO(jt >> 1) + (((jt & 1) * 2 + (l4 >> 1)) * 8 + (r4 & 1) * 2 + (l15 >> 3)) * 256 + ((l15 & 1) ^ sig2) * 64 + (2 * b1 + (l4 & 1)) * 16),
              (unsigned)(h * 2048 + (2 * (part & 1) + (l4 >> 1)) * 512 + (2 * b1 + (l4 & 1)) * 128 + mrow * 16),
              (unsigned)(h * 2048 + part * 512 + (l4 >> 1) * 256 + (8 * wv + 4 * (l15 >> 3) + c4) * 16 + (r4 & 1) * 8 + (l4 & 1) * 4),
              (unsigned)((2 * (wv >> 1) + (l4 >> 1)) * 2048 + ((2 * (wv & 1) + (l4 & 1)) * 16 + l15) * 16)};
  u32x8 c3 = {(unsigned)lane, O_SLOTS + (i0 >> 3) * 256 + (i0 & 7) * 16,
              (unsigned)(wv * 2048 + (8 * l4 + r4) * 64 + c4 * 16),
              (unsigned)(wv * 2048 + (r4 >> 1) * 1024 + (l4 * 16 + 8 * (c4 >> 1) + 4 * (r4 & 1) + (c4 & 1)) * 16),
              (unsigned)(L_VT + (8 * r4 + c4) * 256 + (((4 * wv + l4) ^ l15) << 4)),
              awv,
              (unsigned)(L_VT + ((wv & 1) * 32 + 8 * l4 + r4) * 256 + (((4 * part + 2 * (wv >> 1) + (c4 >> 1)) ^ (r4 | (l4 << 2))) << 4) + (c4 & 1) * 8),
              (unsigned)(QO(part) + h * 256 + (lane & 7) * 16)};
  const int L = (h & 7) * 8 + (lane & 7);
  const u64 mp = 1 ? (u64)((const u16*)m0a + (4 * (l15 >> 3) + 2 * ((l15 & 1) ^ sig2) + (r4 & 1)) * DM + 64 * jt + 16 * l4 + 2 * b1) :
                (u64)((const u16*)m0a + (2 * ((L >> 2) & 3) + ((L >> 4) & 1)) * DM + 128 * part + 32 * (h >> 3) + 8 * (L >> 5) + 16 * (L & 1) + (L & 2));
  u32x8 c0 = {(unsigned)((8 * wv + l4) * 8192 + h * 256 + ((l15 ^ (2 * l4 + 8 * (wv & 1))) << 4)),
              (unsigned)(L_TB + (8 * l4 + r4) * 256 + (((4 * wv + (c4 >> 1)) ^ (2 * r4 + 8 * (l4 & 1))) << 4) + (c4 & 1) * 8),
              (unsigned)mp, (unsigned)(mp >> 32), (unsigned)(CAL(b) + wv * 256 + ((wv ^ (b >> 2)) & 1) * 8192) & (unsigned)((b - 32) >> 31) & (unsigned)((1 + 1 - wv) >> 31), (unsigned)(QO(part) + sig * 1024 + sig2 * 512),
              b1 ? 0xEEEEu : 0x4444u, (unsigned)((wv * 8 + (lane >> 5)) * 256 + (lane & 31) * 4)};
  u32x4 c1 = {(unsigned)(sl * 8),
                    (unsigned)(L_QN + ((wv >> 1) * 2 + (r4 & 1)) * L_BS + ((wv & 1) * 32 + su) * 8),
                    (unsigned)lane, (unsigned)lane * 16};
  const unsigned k0 = O_QKV + h * 24576, v0 = O_QKV + 8192 + SEG * QKVN * 2 + h * 24576 + wv * 2048;
  const unsigned role = wv < 2 ? wv : (wv == 3 ? 4 : (part < 2 ? 2 + part : 5));
  u32x16 sc = {(unsigned)lane, (unsigned)wv, role, O_SLOTS, k0, O_MFRAG + (min(h, 27) * 4 + part) * 32768 + wv * 4096, v0, rot << 19, O_KVM, O_VTM,
                     O_TFLAG + h * 64, O_KVSTEP + h * 64 + part * 4, O_STAMPS + (b * 4 + wv) * 65 * 64,
                     O_EPOCH, O_MFLAG + h * 128, (unsigned)wv * 2048};
  u32x16 s2 = {(unsigned)woa, (unsigned)(woa >> 32) & 0xffffu, DM * HD * 2, 0x00020000u, (unsigned)wva, (unsigned)(wva >> 32) & 0xffffu, O_WM, O_WM + b * 128,
               (unsigned)wsa, (unsigned)(wsa >> 32) & 0xffffu, WS_BYTES, 0x00020000u,
               (unsigned)wqa, (unsigned)(wqa >> 32) & 0xffffu, (unsigned)wka, (unsigned)(wka >> 32) & 0xffffu};
  asm volatile(
      "s_nop 1\n\t"
      "v_readfirstlane_b32 s40, v0\n\t"
      "v_readfirstlane_b32 s41, v1\n\t"
      "v_readfirstlane_b32 s42, v2\n\t"
      "v_readfirstlane_b32 s43, v3\n\t"
      "v_readfirstlane_b32 s44, v4\n\t"
      "v_readfirstlane_b32 s45, v5\n\t"
      "v_readfirstlane_b32 s46, v6\n\t"
      "v_readfirstlane_b32 s47, v7\n\t"
      "v_readfirstlane_b32 s48, v8\n\t"
      "v_readfirstlane_b32 s49, v9\n\t"
      "v_readfirstlane_b32 s50, v10\n\t"
      "v_readfirstlane_b32 s51, v11\n\t"
      "v_readfirstlane_b32 s52, v12\n\t"
      "v_readfirstlane_b32 s53, v13\n\t"
      "v_readfirstlane_b32 s54, v14\n\t"
      "v_readfirstlane_b32 s55, v15\n\t"
      "v_readfirstlane_b32 s88, v16\n\t"
      "v_readfirstlane_b32 s89, v17\n\t"
      "v_readfirstlane_b32 s90, v18\n\t"
      "v_readfirstlane_b32 s91, v19\n\t"
      "v_readfirstlane_b32 s92, v20\n\t"
      "v_readfirstlane_b32 s93, v21\n\t"
      "v_readfirstlane_b32 s94, v22\n\t"
      "v_readfirstlane_b32 s95, v23\n\t"
      "v_readfirstlane_b32 s36, v24\n\t"
      "v_readfirstlane_b32 s37, v25\n\t"
      "v_readfirstlane_b32 s38, v26\n\t"
      "v_readfirstlane_b32 s39, v27\n\t"
      "v_readfirstlane_b32 s96, v28\n\t"
      "v_readfirstlane_b32 s97, v29\n\t"
      "v_readfirstlane_b32 s98, v30\n\t"
      "v_readfirstlane_b32 s99, v31\n\t"
      "s_nop 4\n\t"
      "s_memtime s[80:81]\n\t"
      "s_memrealtime s[82:83]\n\t"
      "v_readfirstlane_b32 s86, v120\n\t"
      "v_add_u32 v100, s55, v127\n\t"
      "s_mov_b64 s[88:89], s[92:93]\n\t"
      "s_add_u32 m0, s55, 32768\n\t"
      "s_xor_b32 s61, s47, 0\n\t"
      "buffer_load_dwordx4 v105, s[88:91], s61 offen lds\n\t"
      "s_add_u32 m0, s55, 33792\n\t"
      "s_xor_b32 s61, s47, 32768\n\t"
      "buffer_load_dwordx4 v105, s[88:91], s61 offen lds\n\t"
      "s_add_u32 m0, s55, 40960\n\t"
      "s_xor_b32 s61, s47, 262144\n\t"
      "buffer_load_dwordx4 v105, s[88:91], s61 offen lds\n\t"
      "s_add_u32 m0, s55, 41984\n\t"
      "s_xor_b32 s61, s47, 294912\n\t"
      "buffer_load_dwordx4 v105, s[88:91], s61 offen lds\n\t"
      "s_add_u32 m0, s55, 49152\n\t"
      "s_xor_b32 s61, s47, 524288\n\t"
      "buffer_load_dwordx4 v105, s[88:91], s61 offen lds\n\t"
      "s_add_u32 m0, s55, 50176\n\t"
      "s_xor_b32 s61, s47, 557056\n\t"
      "buffer_load_dwordx4 v105, s[88:91], s61 offen lds\n\t"
      "s_add_u32 m0, s55, 57344\n\t"
      "s_xor_b32 s61, s47, 786432\n\t"
      "buffer_load_dwordx4 v105, s[88:91], s61 offen lds\n\t"
      "s_add_u32 m0, s55, 58368\n\t"
      "s_xor_b32 s61, s47, 819200\n\t"
      "buffer_load_dwordx4 v105, s[88:91], s61 offen lds\n\t"
      "buffer_load_dword v126, off, s[36:39], s53 sc1\n\t"
      "global_load_dword a172, v[118:119], off offset:0\n\t"
      "global_load_dword a173, v[118:119], off offset:8\n\t"
      "global_load_dword a174, v[118:119], off offset:16\n\t"
      "global_load_dword a175, v[118:119], off offset:24\n\t"
      "global_load_dword a236, v[118:119], off offset:512\n\t"
      "global_load_dword a237, v[118:119], off offset:520\n\t"
      "global_load_dword a238, v[118:119], off offset:528\n\t"
      "global_load_dword a239, v[118:119], off offset:536\n\t"
      "s_xor_b32 s61, s86, 0x2000\n\t"
      "s_buffer_load_dword s87, s[36:39], s61\n\t"
      "s_waitcnt vmcnt(17)\n\t"
      "s_waitcnt lgkmcnt(0)\n\t"
      "ds_read_b64_tr_b16 v[64:65], v102\n\t"
      "ds_read_b64_tr_b16 v[66:67], v102 offset:256\n\t"
      "ds_read_b64_tr_b16 v[68:69], v102 offset:8\n\t"
      "ds_read_b64_tr_b16 v[70:71], v102 offset:264\n\t"
      "ds_read_b64_tr_b16 v[72:73], v102 offset:8192\n\t"
      "ds_read_b64_tr_b16 v[74:75], v102 offset:8448\n\t"
      "ds_read_b64_tr_b16 v[76:77], v102 offset:8200\n\t"
      "ds_read_b64_tr_b16 v[78:79], v102 offset:8456\n\t"
      "ds_read_b64_tr_b16 v[80:81], v102 offset:16384\n\t"
      "ds_read_b64_tr_b16 v[82:83], v102 offset:16640\n\t"
      "ds_read_b64_tr_b16 v[84:85], v102 offset:16392\n\t"
      "ds_read_b64_tr_b16 v[86:87], v102 offset:16648\n\t"
      "s_waitcnt lgkmcnt(4)\n\t"
      "ds_read_b64_tr_b16 v[88:89], v102 offset:24576\n\t"
      "ds_read_b64_tr_b16 v[90:91], v102 offset:24832\n\t"
      "ds_read_b64_tr_b16 v[92:93], v102 offset:24584\n\t"
      "ds_read_b64_tr_b16 v[94:95], v102 offset:24840\n\t"
      "s_mov_b64 s[88:89], s[96:97]\n\t"
      "s_add_u32 m0, s55, 98304\n\t"
      "s_xor_b32 s61, s47, 0\n\t"
      "buffer_load_dwordx4 v116, s[88:91], s61 offen lds\n\t"
      "s_add_u32 m0, s55, 99328\n\t"
      "s_xor_b32 s61, s47, 32768\n\t"
      "buffer_load_dwordx4 v116, s[88:91], s61 offen lds\n\t"
      "s_add_u32 m0, s55, 106496\n\t"
      "s_xor_b32 s61, s47, 262144\n\t"
      "buffer_load_dwordx4 v116, s[88:91], s61 offen lds\n\t"
      "s_add_u32 m0, s55, 107520\n\t"
      "s_xor_b32 s61, s47, 294912\n\t"
      "buffer_load_dwordx4 v116, s[88:91], s61 offen lds\n\t"
      "s_waitcnt lgkmcnt(0)\n\t"
      "ds_write_b128 v103, v[64:67]\n\t"
      "ds_write_b128 v103, v[68:71] offset:32\n\t"
      "ds_write_b128 v103, v[72:75] offset:8192\n\t"
      "ds_write_b128 v103, v[76:79] offset:8224\n\t"
      "ds_write_b128 v103, v[80:83] offset:16384\n\t"
      "ds_write_b128 v103, v[84:87] offset:16416\n\t"
      "ds_write_b128 v103, v[88:91] offset:24576\n\t"
      "ds_write_b128 v103, v[92:95] offset:24608\n\t"
      "s_waitcnt lgkmcnt(4)\n\t"
      "ds_read_b128 v[32:35], v100\n\t"
      "ds_read_b128 v[36:39], v100 offset:1024\n\t"
      "ds_read_b128 v[40:43], v100 offset:8192\n\t"
      "ds_read_b128 v[44:47], v100 offset:9216\n\t"
      "ds_read_b128 v[48:51], v100 offset:16384\n\t"
      "ds_read_b128 v[52:55], v100 offset:17408\n\t"
      "ds_read_b128 v[56:59], v100 offset:24576\n\t"
      "ds_read_b128 v[60:63], v100 offset:25600\n\t"
      "v_xor_b32 v118, 32, v117\n\t"
      "v_xor_b32 v119, 64, v104\n\t"
      "v_xor_b32 v102, 128, v104\n\t"
      "v_xor_b32 v103, 192, v104\n\t"
      "s_mov_b64 s[88:89], s[92:93]\n\t"
      "s_add_u32 m0, s55, 65536\n\t"
      "s_xor_b32 s61, s47, 1048576\n\t"
      "buffer_load_dwordx4 v105, s[88:91], s61 offen lds\n\t"
      "s_add_u32 m0, s55, 66560\n\t"
      "s_xor_b32 s61, s47, 1081344\n\t"
      "buffer_load_dwordx4 v105, s[88:91], s61 offen lds\n\t"
      "s_mov_b64 s[88:89], s[96:97]\n\t"
      "s_add_u32 m0, s55, 114688\n\t"
      "s_xor_b32 s61, s47, 524288\n\t"
      "buffer_load_dwordx4 v116, s[88:91], s61 offen lds\n\t"
      "s_add_u32 m0, s55, 115712\n\t"
      "s_xor_b32 s61, s47, 557056\n\t"
      "buffer_load_dwordx4 v116, s[88:91], s61 offen lds\n\t"
      "s_add_u32 m0, s55, 122880\n\t"
      "s_xor_b32 s61, s47, 786432\n\t"
      "buffer_load_dwordx4 v116, s[88:91], s61 offen lds\n\t"
      "s_add_u32 m0, s55, 123904\n\t"
      "s_xor_b32 s61, s47, 819200\n\t"
      "buffer_load_dwordx4 v116, s[88:91], s61 offen lds\n\t"
      "s_waitcnt lgkmcnt(0)\n\t"
      "s_waitcnt vmcnt(25)\n\t"
      "s_barrier\n\t"
      "ds_read_b128 v[0:3], v104\n\t"
      "ds_read_b128 v[4:7], v119\n\t"
      "ds_read_b128 v[8:11], v102\n\t"
      "ds_read_b128 v[12:15], v103\n\t"
      "ds_read_b128 v[64:67], v104 offset:1024\n\t"
      "ds_read_b128 v[68:71], v119 offset:1024\n\t"
      "ds_read_b128 v[72:75], v102 offset:1024\n\t"
      "ds_read_b128 v[76:79], v103 offset:1024\n\t"
      "s_mov_b64 s[88:89], s[92:93]\n\t"
      "s_add_u32 m0, s55, 73728\n\t"
      "s_xor_b32 s61, s47, 1310720\n\t"
      "buffer_load_dwordx4 v105, s[88:91], s61 offen lds\n\t"
      "s_add_u32 m0, s55, 74752\n\t"
      "s_xor_b32 s61, s47, 1343488\n\t"
      "buffer_load_dwordx4 v105, s[88:91], s61 offen lds\n\t"
      "s_mov_b64 s[88:89], s[96:97]\n\t"
      "s_add_u32 m0, s55, 131072\n\t"
      "s_xor_b32 s61, s47, 1048576\n\t"
      "buffer_load_dwordx4 v116, s[88:91], s61 offen lds\n\t"
      "s_add_u32 m0, s55, 132096\n\t"
      "s_xor_b32 s61, s47, 1081344\n\t"
      "buffer_load_dwordx4 v116, s[88:91], s61 offen lds\n\t"
      "s_add_u32 m0, s55, 139264\n\t"
      "s_xor_b32 s61, s47, 1310720\n\t"
      "buffer_load_dwordx4 v116, s[88:91], s61 offen lds\n\t"
      "s_add_u32 m0, s55, 140288\n\t"
      "s_xor_b32 s61, s47, 1343488\n\t"
      "buffer_load_dwordx4 v116, s[88:91], s61 offen lds\n\t"
      "s_mov_b64 s[88:89], s[92:93]\n\t"
      "s_add_u32 m0, s55, 81920\n\t"
      "s_xor_b32 s61, s47, 1572864\n\t"
      "buffer_load_dwordx4 v105, s[88:91], s61 offen lds\n\t"
      "s_add_u32 m0, s55, 82944\n\t"
      "s_xor_b32 s61, s47, 1605632\n\t"
      "buffer_load_dwordx4 v105, s[88:91], s61 offen lds\n\t"
      "s_waitcnt vmcnt(31)\n\t"
      "s_barrier\n\t"
      "s_waitcnt lgkmcnt(7)\n\t"
      "v_mfma_f32_16x16x32_bf16 v[16:19], v[0:3], v[32:35], 0\n\t"
      "ds_read_b128 v[80:83], v104 offset:8192\n\t"
      "v_mfma_f32_16x16x32_bf16 v[20:23], v[0:3], v[36:39], 0\n\t"
      "s_mov_b64 s[88:89], s[96:97]\n\t"
      "s_add_u32 m0, s55, 147456\n\t"
      "s_xor_b32 s61, s47, 1572864\n\t"
      "buffer_load_dwordx4 v116, s[88:91], s61 offen lds\n\t"
      "ds_read_b64_tr_b16 a[176:177], v106\n\t"
      "s_waitcnt lgkmcnt(8)\n\t"
      "v_mfma_f32_16x16x32_bf16 v[16:19], v[4:7], v[40:43], v[16:19]\n\t"
      "ds_read_b128 v[84:87], v119 offset:8192\n\t"
      "v_mfma_f32_16x16x32_bf16 v[20:23], v[4:7], v[44:47], v[20:23]\n\t"
      "s_add_u32 m0, s55, 148480\n\t"
      "s_xor_b32 s61, s47, 1605632\n\t"
      "buffer_load_dwordx4 v116, s[88:91], s61 offen lds\n\t"
      "ds_read_b64_tr_b16 a[178:179], v106 offset:1024\n\t"
      "s_waitcnt lgkmcnt(9)\n\t"
      "v_mfma_f32_16x16x32_bf16 v[16:19], v[8:11], v[48:51], v[16:19]\n\t"
      "ds_read_b128 v[88:91], v102 offset:8192\n\t"
      "v_mfma_f32_16x16x32_bf16 v[20:23], v[8:11], v[52:55], v[20:23]\n\t"
      "s_add_u32 m0, s55, 155648\n\t"
      "s_xor_b32 s61, s47, 1835008\n\t"
      "buffer_load_dwordx4 v116, s[88:91], s61 offen lds\n\t"
      "s_add_u32 m0, s55, 156672\n\t"
      "s_xor_b32 s61, s47, 1867776\n\t"
      "buffer_load_dwordx4 v116, s[88:91], s61 offen lds\n\t"
      "s_waitcnt lgkmcnt(9)\n\t"
      "v_mfma_f32_16x16x32_bf16 v[16:19], v[12:15], v[56:59], v[16:19]\n\t"
      "ds_read_b128 v[92:95], v103 offset:8192\n\t"
      "v_mfma_f32_16x16x32_bf16 v[20:23], v[12:15], v[60:63], v[20:23]\n\t"
      "s_mov_b64 s[88:89], s[92:93]\n\t"
      "s_add_u32 m0, s55, 90112\n\t"
      "s_xor_b32 s61, s47, 1835008\n\t"
      "buffer_load_dwordx4 v105, s[88:91], s61 offen lds\n\t"
      "s_add_u32 m0, s55, 91136\n\t"
      "s_xor_b32 s61, s47, 1867776\n\t"
      "buffer_load_dwordx4 v105, s[88:91], s61 offen lds\n\t"
      "s_waitcnt lgkmcnt(9)\n\t"
      "v_mfma_f32_16x16x32_bf16 v[24:27], v[64:67], v[32:35], 0\n\t"
      "ds_read_b128 v[0:3], v104 offset:9216\n\t"
      "v_mfma_f32_16x16x32_bf16 v[28:31], v[64:67], v[36:39], 0\n\t"
      "s_waitcnt lgkmcnt(9)\n\t"
      "v_mfma_f32_16x16x32_bf16 v[24:27], v[68:71], v[40:43], v[24:27]\n\t"
      "ds_read_b128 v[4:7], v119 offset:9216\n\t"
      "v_cvt_pk_bf16_f32 v128, v16, v17\n\t"
      "v_mfma_f32_16x16x32_bf16 v[28:31], v[68:71], v[44:47], v[28:31]\n\t"
      "v_cvt_pk_bf16_f32 v129, v18, v19\n\t"
      "s_waitcnt lgkmcnt(9)\n\t"
      "v_mfma_f32_16x16x32_bf16 v[24:27], v[72:75], v[48:51], v[24:27]\n\t"
      "ds_read_b128 v[8:11], v102 offset:9216\n\t"
      "v_cvt_pk_bf16_f32 v192, v20, v21\n\t"
      "v_mfma_f32_16x16x32_bf16 v[28:31], v[72:75], v[52:55], v[28:31]\n\t"
      "v_cvt_pk_bf16_f32 v193, v22, v23\n\t"
      "s_waitcnt lgkmcnt(9)\n\t"
      "v_mfma_f32_16x16x32_bf16 v[24:27], v[76:79], v[56:59], v[24:27]\n\t"
      "ds_read_b128 v[12:15], v103 offset:9216\n\t"
      "v_mfma_f32_16x16x32_bf16 v[28:31], v[76:79], v[60:63], v[28:31]\n\t"
      "s_waitcnt vmcnt(35)\n\t"
      "s_waitcnt lgkmcnt(6)\n\t"
      "s_barrier\n\t"
      "v_mfma_f32_16x16x32_bf16 v[16:19], v[80:83], v[32:35], 0\n\t"
      "ds_read_b128 v[64:67], v104 offset:16384\n\t"
      "v_mfma_f32_16x16x32_bf16 v[20:23], v[80:83], v[36:39], 0\n\t"
      "s_add_u32 m0, s55, 32768\n\t"
      "s_xor_b32 s61, s47, 2097152\n\t"
      "buffer_load_dwordx4 v105, s[88:91], s61 offen lds\n\t"
      "s_add_u32 m0, s55, 33792\n\t"
      "s_xor_b32 s61, s47, 2129920\n\t"
      "buffer_load_dwordx4 v105, s[88:91], s61 offen lds\n\t"
      "v_mfma_f32_16x16x32_bf16 v[16:19], v[84:87], v[40:43], v[16:19]\n\t"
      "ds_read_b128 v[68:71], v119 offset:16384\n\t"
      "v_cvt_pk_bf16_f32 v130, v24, v25\n\t"
      "v_mfma_f32_16x16x32_bf16 v[20:23], v[84:87], v[44:47], v[20:23]\n\t"
      "v_cvt_pk_bf16_f32 v131, v26, v27\n\t"
      "s_waitcnt lgkmcnt(7)\n\t"
      "v_mfma_f32_16x16x32_bf16 v[16:19], v[88:91], v[48:51], v[16:19]\n\t"
      "ds_read_b128 v[72:75], v102 offset:16384\n\t"
      "v_cvt_pk_bf16_f32 v194, v28, v29\n\t"
      "v_mfma_f32_16x16x32_bf16 v[20:23], v[88:91], v[52:55], v[20:23]\n\t"
      "v_cvt_pk_bf16_f32 v195, v30, v31\n\t"
      "s_waitcnt lgkmcnt(7)\n\t"
      "v_mfma_f32_16x16x32_bf16 v[16:19], v[92:95], v[56:59], v[16:19]\n\t"
      "ds_read_b128 v[76:79], v103 offset:16384\n\t"
      "v_mfma_f32_16x16x32_bf16 v[20:23], v[92:95], v[60:63], v[20:23]\n\t"
      "v_mfma_f32_16x16x32_bf16 a[168:171], v[128:131], v[128:131], 0\n\t"
      "s_waitcnt lgkmcnt(7)\n\t"
      "v_mfma_f32_16x16x32_bf16 v[24:27], v[0:3], v[32:35], 0\n\t"
      "ds_read_b128 v[80:83], v104 offset:17408\n\t"
      "v_mfma_f32_16x16x32_bf16 v[28:31], v[0:3], v[36:39], 0\n\t"
      "s_waitcnt lgkmcnt(7)\n\t"
      "v_mfma_f32_16x16x32_bf16 v[24:27], v[4:7], v[40:43], v[24:27]\n\t"
      "ds_read_b128 v[84:87], v119 offset:17408\n\t"
      "v_cvt_pk_bf16_f32 v132, v16, v17\n\t"
      "v_mfma_f32_16x16x32_bf16 v[28:31], v[4:7], v[44:47], v[28:31]\n\t"
      "v_cvt_pk_bf16_f32 v133, v18, v19\n\t"
      "s_waitcnt lgkmcnt(7)\n\t"
      "v_mfma_f32_16x16x32_bf16 v[24:27], v[8:11], v[48:51], v[24:27]\n\t"
      "ds_read_b128 v[88:91], v102 offset:17408\n\t"
      "v_cvt_pk_bf16_f32 v196, v20, v21\n\t"
      "v_mfma_f32_16x16x32_bf16 v[28:31], v[8:11], v[52:55], v[28:31]\n\t"
      "v_cvt_pk_bf16_f32 v197, v22, v23\n\t"
      "s_waitcnt lgkmcnt(7)\n\t"
      "v_mfma_f32_16x16x32_bf16 v[24:27], v[12:15], v[56:59], v[24:27]\n\t"
      "ds_read_b128 v[92:95], v103 offset:17408\n\t"
      "v_mfma_f32_16x16x32_bf16 v[28:31], v[12:15], v[60:63], v[28:31]\n\t"
      "v_mfma_f32_16x16x32_bf16 a[232:235], v[192:195], v[192:195], 0\n\t"
      "s_waitcnt vmcnt(35)\n\t"
      "s_waitcnt vmcnt(22)\n\t"
      "s_barrier\n\t"
      "s_waitcnt lgkmcnt(7)\n\t"
      "v_mfma_f32_16x16x32_bf16 v[16:19], v[64:67], v[32:35], 0\n\t"
      "ds_read_b128 v[0:3], v104 offset:24576\n\t"
      "v_mfma_f32_16x16x32_bf16 v[20:23], v[64:67], v[36:39], 0\n\t"
      "s_add_u32 m0, s55, 40960\n\t"
      "s_xor_b32 s61, s47, 2359296\n\t"
      "buffer_load_dwordx4 v105, s[88:91], s61 offen lds\n\t"
      "ds_read_b64_tr_b16 a[180:181], v106 offset:16384\n\t"
      "s_waitcnt lgkmcnt(8)\n\t"
      "v_mfma_f32_16x16x32_bf16 v[16:19], v[68:71], v[40:43], v[16:19]\n\t"
      "ds_read_b128 v[4:7], v119 offset:24576\n\t"
      "v_cvt_pk_bf16_f32 v134, v24, v25\n\t"
      "v_mfma_f32_16x16x32_bf16 v[20:23], v[68:71], v[44:47], v[20:23]\n\t"
      "s_add_u32 m0, s55, 41984\n\t"
      "s_xor_b32 s61, s47, 2392064\n\t"
      "buffer_load_dwordx4 v105, s[88:91], s61 offen lds\n\t"
      "ds_read_b64_tr_b16 a[182:183], v106 offset:17408\n\t"
      "v_cvt_pk_bf16_f32 v135, v26, v27\n\t"
      "s_waitcnt lgkmcnt(9)\n\t"
      "v_mfma_f32_16x16x32_bf16 v[16:19], v[72:75], v[48:51], v[16:19]\n\t"
      "ds_read_b128 v[8:11], v102 offset:24576\n\t"
      "v_cvt_pk_bf16_f32 v198, v28, v29\n\t"
      "v_mfma_f32_16x16x32_bf16 v[20:23], v[72:75], v[52:55], v[20:23]\n\t"
      "ds_read_b64_tr_b16 a[0:1], v117\n\t"
      "ds_read_b64_tr_b16 a[2:3], v117 offset:1024\n\t"
      "v_cvt_pk_bf16_f32 v199, v30, v31\n\t"
      "s_waitcnt lgkmcnt(11)\n\t"
      "v_mfma_f32_16x16x32_bf16 v[16:19], v[76:79], v[56:59], v[16:19]\n\t"
      "ds_read_b128 v[12:15], v103 offset:24576\n\t"
      "v_mfma_f32_16x16x32_bf16 v[20:23], v[76:79], v[60:63], v[20:23]\n\t"
      "ds_read_b64_tr_b16 a[64:65], v118\n\t"
      "ds_read_b64_tr_b16 a[66:67], v118 offset:1024\n\t"
      "v_mfma_f32_16x16x32_bf16 a[168:171], v[132:135], v[132:135], a[168:171]\n\t"
      "s_waitcnt lgkmcnt(13)\n\t"
      "v_mfma_f32_16x16x32_bf16 v[24:27], v[80:83], v[32:35], 0\n\t"
      "ds_read_b128 v[64:67], v104 offset:25600\n\t"
      "v_mfma_f32_16x16x32_bf16 v[28:31], v[80:83], v[36:39], 0\n\t"
      "ds_read_b64_tr_b16 a[4:5], v117 offset:8192\n\t"
      "s_waitcnt lgkmcnt(12)\n\t"
      "ds_read_b64_tr_b16 a[6:7], v117 offset:9216\n\t"
      "v_mfma_f32_16x16x32_bf16 v[24:27], v[84:87], v[40:43], v[24:27]\n\t"
      "ds_read_b128 v[68:71], v119 offset:25600\n\t"
      "v_cvt_pk_bf16_f32 v136, v16, v17\n\t"
      "v_mfma_f32_16x16x32_bf16 v[28:31], v[84:87], v[44:47], v[28:31]\n\t"
      "ds_read_b64_tr_b16 a[68:69], v118 offset:8192\n\t"
      "s_waitcnt lgkmcnt(12)\n\t"
      "ds_read_b64_tr_b16 a[70:71], v118 offset:9216\n\t"
      "v_cvt_pk_bf16_f32 v137, v18, v19\n\t"
      "v_mfma_f32_16x16x32_bf16 v[24:27], v[88:91], v[48:51], v[24:27]\n\t"
      "ds_read_b128 v[72:75], v102 offset:25600\n\t"
      "v_cvt_pk_bf16_f32 v200, v20, v21\n\t"
      "v_mfma_f32_16x16x32_bf16 v[28:31], v[88:91], v[52:55], v[28:31]\n\t"
      "v_cvt_pk_bf16_f32 v201, v22, v23\n\t"
      "v_mfma_f32_16x16x32_bf16 v[24:27], v[92:95], v[56:59], v[24:27]\n\t"
      "ds_read_b128 v[76:79], v103 offset:25600\n\t"
      "v_mfma_f32_16x16x32_bf16 v[28:31], v[92:95], v[60:63], v[28:31]\n\t"
      "v_mfma_f32_16x16x32_bf16 a[232:235], v[196:199], v[196:199], a[232:235]\n\t"
      "s_waitcnt vmcnt(22)\n\t"
      "s_waitcnt vmcnt(18)\n\t"
      "s_waitcnt lgkmcnt(2)\n\t"
      "s_barrier\n\t"
      "v_mfma_f32_16x16x32_bf16 v[16:19], v[0:3], v[32:35], 0\n\t"
      "ds_read_b128 v[80:83], v104 offset:32768\n\t"
      "v_mfma_f32_16x16x32_bf16 v[20:23], v[0:3], v[36:39], 0\n\t"
      "s_add_u32 m0, s55, 49152\n\t"
      "s_xor_b32 s61, s47, 2621440\n\t"
      "buffer_load_dwordx4 v105, s[88:91], s61 offen lds\n\t"
      "ds_read_b64_tr_b16 a[8:9], v117 offset:16384\n\t"
      "v_mfma_f32_16x16x32_bf16 v[16:19], v[4:7], v[40:43], v[16:19]\n\t"
      "ds_read_b128 v[84:87], v119 offset:32768\n\t"
      "v_cvt_pk_bf16_f32 v138, v24, v25\n\t"
      "v_mfma_f32_16x16x32_bf16 v[20:23], v[4:7], v[44:47], v[20:23]\n\t"
      "s_add_u32 m0, s55, 50176\n\t"
      "s_xor_b32 s61, s47, 2654208\n\t"
      "buffer_load_dwordx4 v105, s[88:91], s61 offen lds\n\t"
      "ds_read_b64_tr_b16 a[10:11], v117 offset:17408\n\t"
      "v_cvt_pk_bf16_f32 v139, v26, v27\n\t"
      "v_mfma_f32_16x16x32_bf16 v[16:19], v[8:11], v[48:51], v[16:19]\n\t"
      "ds_read_b128 v[88:91], v102 offset:32768\n\t"
      "v_cvt_pk_bf16_f32 v202, v28, v29\n\t"
      "v_mfma_f32_16x16x32_bf16 v[20:23], v[8:11], v[52:55], v[20:23]\n\t"
      "s_mov_b64 s[88:89], s[96:97]\n\t"
      "s_add_u32 m0, s55, 98304\n\t"
      "s_xor_b32 s61, s47, 2097152\n\t"
      "buffer_load_dwordx4 v116, s[88:91], s61 offen lds\n\t"
      "ds_read_b64_tr_b16 a[72:73], v118 offset:16384\n\t"
      "v_cvt_pk_bf16_f32 v203, v30, v31\n\t"
      "v_mfma_f32_16x16x32_bf16 v[16:19], v[12:15], v[56:59], v[16:19]\n\t"
      "ds_read_b128 v[92:95], v103 offset:32768\n\t"
      "v_mfma_f32_16x16x32_bf16 v[20:23], v[12:15], v[60:63], v[20:23]\n\t"
      "s_add_u32 m0, s55, 99328\n\t"
      "s_xor_b32 s61, s47, 2129920\n\t"
      "buffer_load_dwordx4 v116, s[88:91], s61 offen lds\n\t"
      "ds_read_b64_tr_b16 a[74:75], v118 offset:17408\n\t"
      "v_mfma_f32_16x16x32_bf16 a[168:171], v[136:139], v[136:139], a[168:171]\n\t"
      "v_mfma_f32_16x16x32_bf16 v[24:27], v[64:67], v[32:35], 0\n\t"
      "ds_read_b128 v[0:3], v104 offset:33792\n\t"
      "v_mfma_f32_16x16x32_bf16 v[28:31], v[64:67], v[36:39], 0\n\t"
      "s_add_u32 m0, s55, 106496\n\t"
      "s_xor_b32 s61, s47, 2359296\n\t"
      "buffer_load_dwordx4 v116, s[88:91], s61 offen lds\n\t"
      "ds_read_b64_tr_b16 a[12:13], v117 offset:24576\n\t"
      "v_mfma_f32_16x16x32_bf16 v[24:27], v[68:71], v[40:43], v[24:27]\n\t"
      "ds_read_b128 v[4:7], v119 offset:33792\n\t"
      "v_cvt_pk_bf16_f32 v140, v16, v17\n\t"
      "v_mfma_f32_16x16x32_bf16 v[28:31], v[68:71], v[44:47], v[28:31]\n\t"
      "s_add_u32 m0, s55, 107520\n\t"
      "s_xor_b32 s61, s47, 2392064\n\t"
      "buffer_load_dwordx4 v116, s[88:91], s61 offen lds\n\t"
      "ds_read_b64_tr_b16 a[14:15], v117 offset:25600\n\t"
      "v_cvt_pk_bf16_f32 v141, v18, v19\n\t"
      "s_waitcnt lgkmcnt(13)\n\t"
      "v_mfma_f32_16x16x32_bf16 v[24:27], v[72:75], v[48:51], v[24:27]\n\t"
      "ds_read_b128 v[8:11], v102 offset:33792\n\t"
      "v_cvt_pk_bf16_f32 v204, v20, v21\n\t"
      "v_mfma_f32_16x16x32_bf16 v[28:31], v[72:75], v[52:55], v[28:31]\n\t"
      "ds_read_b64_tr_b16 a[76:77], v118 offset:24576\n\t"
      "s_waitcnt lgkmcnt(12)\n\t"
      "ds_read_b64_tr_b16 a[78:79], v118 offset:25600\n\t"
      "v_cvt_pk_bf16_f32 v205, v22, v23\n\t"
      "v_mfma_f32_16x16x32_bf16 v[24:27], v[76:79], v[56:59], v[24:27]\n\t"
      "ds_read_b128 v[12:15], v103 offset:33792\n\t"
      "v_mfma_f32_16x16x32_bf16 v[28:31], v[76:79], v[60:63], v[28:31]\n\t"
      "v_mfma_f32_16x16x32_bf16 a[232:235], v[200:203], v[200:203], a[232:235]\n\t"
      "s_waitcnt vmcnt(22)\n\t"
      "s_waitcnt vmcnt(18)\n\t"
      "s_waitcnt lgkmcnt(1)\n\t"
      "s_barrier\n\t"
      "v_mfma_f32_16x16x32_bf16 v[16:19], v[80:83], v[32:35], 0\n\t"
      "ds_read_b128 v[64:67], v104 offset:40960\n\t"
      "v_mfma_f32_16x16x32_bf16 v[20:23], v[80:83], v[36:39], 0\n\t"
      "s_mov_b64 s[88:89], s[92:93]\n\t"
      "s_add_u32 m0, s55, 57344\n\t"
      "s_xor_b32 s61, s47, 2883584\n\t"
      "buffer_load_dwordx4 v105, s[88:91], s61 offen lds\n\t"
      "ds_read_b64_tr_b16 a[184:185], v106 offset:32768\n\t"
      "v_mfma_f32_16x16x32_bf16 v[16:19], v[84:87], v[40:43], v[16:19]\n\t"
      "ds_read_b128 v[68:71], v119 offset:40960\n\t"
      "v_cvt_pk_bf16_f32 v142, v24, v25\n\t"
      "v_mfma_f32_16x16x32_bf16 v[20:23], v[84:87], v[44:47], v[20:23]\n\t"
      "s_add_u32 m0, s55, 58368\n\t"
      "s_xor_b32 s61, s47, 2916352\n\t"
      "buffer_load_dwordx4 v105, s[88:91], s61 offen lds\n\t"
      "ds_read_b64_tr_b16 a[186:187], v106 offset:33792\n\t"
      "v_cvt_pk_bf16_f32 v143, v26, v27\n\t"
      "v_mfma_f32_16x16x32_bf16 v[16:19], v[88:91], v[48:51], v[16:19]\n\t"
      "ds_read_b128 v[72:75], v102 offset:40960\n\t"
      "v_cvt_pk_bf16_f32 v206, v28, v29\n\t"
      "v_mfma_f32_16x16x32_bf16 v[20:23], v[88:91], v[52:55], v[20:23]\n\t"
      "s_mov_b64 s[88:89], s[96:97]\n\t"
      "s_add_u32 m0, s55, 114688\n\t"
      "s_xor_b32 s61, s47, 2621440\n\t"
      "buffer_load_dwordx4 v116, s[88:91], s61 offen lds\n\t"
      "ds_read_b64_tr_b16 a[16:17], v117 offset:32768\n\t"
      "v_cvt_pk_bf16_f32 v207, v30, v31\n\t"
      "v_mfma_f32_16x16x32_bf16 v[16:19], v[92:95], v[56:59], v[16:19]\n\t"
      "ds_read_b128 v[76:79], v103 offset:40960\n\t"
      "v_mfma_f32_16x16x32_bf16 v[20:23], v[92:95], v[60:63], v[20:23]\n\t"
      "s_add_u32 m0, s55, 115712\n\t"
      "s_xor_b32 s61, s47, 2654208\n\t"
      "buffer_load_dwordx4 v116, s[88:91], s61 offen lds\n\t"
      "ds_read_b64_tr_b16 a[18:19], v117 offset:33792\n\t"
      "v_mfma_f32_16x16x32_bf16 a[168:171], v[140:143], v[140:143], a[168:171]\n\t"
      "v_mfma_f32_16x16x32_bf16 v[24:27], v[0:3], v[32:35], 0\n\t"
      "ds_read_b128 v[80:83], v104 offset:41984\n\t"
      "v_mfma_f32_16x16x32_bf16 v[28:31], v[0:3], v[36:39], 0\n\t"
      "s_add_u32 m0, s55, 122880\n\t"
      "s_xor_b32 s61, s47, 2883584\n\t"
      "buffer_load_dwordx4 v116, s[88:91], s61 offen lds\n\t"
      "ds_read_b64_tr_b16 a[80:81], v118 offset:32768\n\t"
      "v_mfma_f32_16x16x32_bf16 v[24:27], v[4:7], v[40:43], v[24:27]\n\t"
      "ds_read_b128 v[84:87], v119 offset:41984\n\t"
      "v_cvt_pk_bf16_f32 v144, v16, v17\n\t"
      "v_mfma_f32_16x16x32_bf16 v[28:31], v[4:7], v[44:47], v[28:31]\n\t"
      "s_add_u32 m0, s55, 123904\n\t"
      "s_xor_b32 s61, s47, 2916352\n\t"
      "buffer_load_dwordx4 v116, s[88:91], s61 offen lds\n\t"
      "ds_read_b64_tr_b16 a[82:83], v118 offset:33792\n\t"
      "v_cvt_pk_bf16_f32 v145, v18, v19\n\t"
      "v_mfma_f32_16x16x32_bf16 v[24:27], v[8:11], v[48:51], v[24:27]\n\t"
      "ds_read_b128 v[88:91], v102 offset:41984\n\t"
      "v_cvt_pk_bf16_f32 v208, v20, v21\n\t"
      "v_mfma_f32_16x16x32_bf16 v[28:31], v[8:11], v[52:55], v[28:31]\n\t"
      "ds_read_b64_tr_b16 a[20:21], v117 offset:40960\n\t"
      "s_waitcnt lgkmcnt(12)\n\t"
      "ds_read_b64_tr_b16 a[22:23], v117 offset:41984\n\t"
      "v_cvt_pk_bf16_f32 v209, v22, v23\n\t"
      "v_mfma_f32_16x16x32_bf16 v[24:27], v[12:15], v[56:59], v[24:27]\n\t"
      "ds_read_b128 v[92:95], v103 offset:41984\n\t"
      "v_mfma_f32_16x16x32_bf16 v[28:31], v[12:15], v[60:63], v[28:31]\n\t"
      "ds_read_b64_tr_b16 a[84:85], v118 offset:40960\n\t"
      "s_waitcnt lgkmcnt(12)\n\t"
      "ds_read_b64_tr_b16 a[86:87], v118 offset:41984\n\t"
      "v_mfma_f32_16x16x32_bf16 a[232:235], v[204:207], v[204:207], a[232:235]\n\t"
      "s_waitcnt vmcnt(22)\n\t"
      "s_waitcnt vmcnt(18)\n\t"
      "s_waitcnt lgkmcnt(0)\n\t"
      "s_barrier\n\t"
      "v_mfma_f32_16x16x32_bf16 v[16:19], v[64:67], v[32:35], 0\n\t"
      "ds_read_b128 v[0:3], v104 offset:49152\n\t"
      "v_mfma_f32_16x16x32_bf16 v[20:23], v[64:67], v[36:39], 0\n\t"
      "s_mov_b64 s[88:89], s[92:93]\n\t"
      "s_add_u32 m0, s55, 65536\n\t"
      "s_xor_b32 s61, s47, 3145728\n\t"
      "buffer_load_dwordx4 v105, s[88:91], s61 offen lds\n\t"
      "ds_read_b64_tr_b16 a[24:25], v117 offset:49152\n\t"
      "v_mfma_f32_16x16x32_bf16 v[16:19], v[68:71], v[40:43], v[16:19]\n\t"
      "ds_read_b128 v[4:7], v119 offset:49152\n\t"
      "v_cvt_pk_bf16_f32 v146, v24, v25\n\t"
      "v_mfma_f32_16x16x32_bf16 v[20:23], v[68:71], v[44:47], v[20:23]\n\t"
      "s_add_u32 m0, s55, 66560\n\t"
      "s_xor_b32 s61, s47, 3178496\n\t"
      "buffer_load_dwordx4 v105, s[88:91], s61 offen lds\n\t"
      "ds_read_b64_tr_b16 a[26:27], v117 offset:50176\n\t"
      "v_cvt_pk_bf16_f32 v147, v26, v27\n\t"
      "v_mfma_f32_16x16x32_bf16 v[16:19], v[72:75], v[48:51], v[16:19]\n\t"
      "ds_read_b128 v[8:11], v102 offset:49152\n\t"
      "v_cvt_pk_bf16_f32 v210, v28, v29\n\t"
      "v_mfma_f32_16x16x32_bf16 v[20:23], v[72:75], v[52:55], v[20:23]\n\t"
      "s_mov_b64 s[88:89], s[96:97]\n\t"
      "s_add_u32 m0, s55, 131072\n\t"
      "s_xor_b32 s61, s47, 3145728\n\t"
      "buffer_load_dwordx4 v116, s[88:91], s61 offen lds\n\t"
      "ds_read_b64_tr_b16 a[88:89], v118 offset:49152\n\t"
      "v_cvt_pk_bf16_f32 v211, v30, v31\n\t"
      "v_mfma_f32_16x16x32_bf16 v[16:19], v[76:79], v[56:59], v[16:19]\n\t"
      "ds_read_b128 v[12:15], v103 offset:49152\n\t"
      "v_mfma_f32_16x16x32_bf16 v[20:23], v[76:79], v[60:63], v[20:23]\n\t"
      "s_add_u32 m0, s55, 132096\n\t"
      "s_xor_b32 s61, s47, 3178496\n\t"
      "buffer_load_dwordx4 v116, s[88:91], s61 offen lds\n\t"
      "ds_read_b64_tr_b16 a[90:91], v118 offset:50176\n\t"
      "v_mfma_f32_16x16x32_bf16 a[168:171], v[144:147], v[144:147], a[168:171]\n\t"
      "v_mfma_f32_16x16x32_bf16 v[24:27], v[80:83], v[32:35], 0\n\t"
      "ds_read_b128 v[64:67], v104 offset:50176\n\t"
      "v_mfma_f32_16x16x32_bf16 v[28:31], v[80:83], v[36:39], 0\n\t"
      "s_add_u32 m0, s55, 139264\n\t"
      "s_xor_b32 s61, s47, 3407872\n\t"
      "buffer_load_dwordx4 v116, s[88:91], s61 offen lds\n\t"
      "ds_read_b64_tr_b16 a[28:29], v117 offset:57344\n\t"
      "v_mfma_f32_16x16x32_bf16 v[24:27], v[84:87], v[40:43], v[24:27]\n\t"
      "ds_read_b128 v[68:71], v119 offset:50176\n\t"
      "v_cvt_pk_bf16_f32 v148, v16, v17\n\t"
      "v_mfma_f32_16x16x32_bf16 v[28:31], v[84:87], v[44:47], v[28:31]\n\t"
      "s_add_u32 m0, s55, 140288\n\t"
      "s_xor_b32 s61, s47, 3440640\n\t"
      "buffer_load_dwordx4 v116, s[88:91], s61 offen lds\n\t"
      "ds_read_b64_tr_b16 a[30:31], v117 offset:58368\n\t"
      "v_cvt_pk_bf16_f32 v149, v18, v19\n\t"
      "v_mfma_f32_16x16x32_bf16 v[24:27], v[88:91], v[48:51], v[24:27]\n\t"
      "ds_read_b128 v[72:75], v102 offset:50176\n\t"
      "v_cvt_pk_bf16_f32 v212, v20, v21\n\t"
      "v_mfma_f32_16x16x32_bf16 v[28:31], v[88:91], v[52:55], v[28:31]\n\t"
      "ds_read_b64_tr_b16 a[92:93], v118 offset:57344\n\t"
      "ds_read_b64_tr_b16 a[94:95], v118 offset:58368\n\t"
      "v_cvt_pk_bf16_f32 v213, v22, v23\n\t"
      "v_mfma_f32_16x16x32_bf16 v[24:27], v[92:95], v[56:59], v[24:27]\n\t"
      "s_waitcnt lgkmcnt(12)\n\t"
      "ds_read_b128 v[76:79], v103 offset:50176\n\t"
      "v_mfma_f32_16x16x32_bf16 v[28:31], v[92:95], v[60:63], v[28:31]\n\t"
      "v_mfma_f32_16x16x32_bf16 a[232:235], v[208:211], v[208:211], a[232:235]\n\t"
      "s_waitcnt vmcnt(22)\n\t"
      "s_waitcnt vmcnt(12)\n\t"
      "s_waitcnt lgkmcnt(1)\n\t"
      "s_barrier\n\t"
      "v_mfma_f32_16x16x32_bf16 v[16:19], v[0:3], v[32:35], 0\n\t"
      "ds_read_b128 v[80:83], v104 offset:57344\n\t"
      "v_mfma_f32_16x16x32_bf16 v[20:23], v[0:3], v[36:39], 0\n\t"
      "s_mov_b64 s[88:89], s[92:93]\n\t"
      "s_add_u32 m0, s55, 73728\n\t"
      "s_xor_b32 s61, s47, 3407872\n\t"
      "buffer_load_dwordx4 v105, s[88:91], s61 offen lds\n\t"
      "ds_read_b64_tr_b16 a[188:189], v106 offset:49152\n\t"
      "v_mfma_f32_16x16x32_bf16 v[16:19], v[4:7], v[40:43], v[16:19]\n\t"
      "ds_read_b128 v[84:87], v119 offset:57344\n\t"
      "v_cvt_pk_bf16_f32 v150, v24, v25\n\t"
      "v_mfma_f32_16x16x32_bf16 v[20:23], v[4:7], v[44:47], v[20:23]\n\t"
      "s_add_u32 m0, s55, 74752\n\t"
      "s_xor_b32 s61, s47, 3440640\n\t"
      "buffer_load_dwordx4 v105, s[88:91], s61 offen lds\n\t"
      "ds_read_b64_tr_b16 a[190:191], v106 offset:50176\n\t"
      "v_cvt_pk_bf16_f32 v151, v26, v27\n\t"
      "v_mfma_f32_16x16x32_bf16 v[16:19], v[8:11], v[48:51], v[16:19]\n\t"
      "ds_read_b128 v[88:91], v102 offset:57344\n\t"
      "v_cvt_pk_bf16_f32 v214, v28, v29\n\t"
      "v_mfma_f32_16x16x32_bf16 v[20:23], v[8:11], v[52:55], v[20:23]\n\t"
      "s_mov_b64 s[88:89], s[96:97]\n\t"
      "s_add_u32 m0, s55, 147456\n\t"
      "s_xor_b32 s61, s47, 3670016\n\t"
      "buffer_load_dwordx4 v116, s[88:91], s61 offen lds\n\t"
      "ds_read_b64_tr_b16 a[32:33], v117\n\t"
      "v_cvt_pk_bf16_f32 v215, v30, v31\n\t"
      "v_mfma_f32_16x16x32_bf16 v[16:19], v[12:15], v[56:59], v[16:19]\n\t"
      "ds_read_b128 v[92:95], v103 offset:57344\n\t"
      "v_mfma_f32_16x16x32_bf16 v[20:23], v[12:15], v[60:63], v[20:23]\n\t"
      "s_add_u32 m0, s55, 148480\n\t"
      "s_xor_b32 s61, s47, 3702784\n\t"
      "buffer_load_dwordx4 v116, s[88:91], s61 offen lds\n\t"
      "ds_read_b64_tr_b16 a[34:35], v117 offset:1024\n\t"
      "v_mfma_f32_16x16x32_bf16 a[168:171], v[148:151], v[148:151], a[168:171]\n\t"
      "v_readfirstlane_b32 s53, v126\n\t"
      "s_nop 0\n\t"
      "s_mul_i32 s53, s53, 0x9e3779b1\n\t"
      "s_add_u32 s53, s53, 0x7f4a7c15\n\t"
      "v_mov_b32 v96, 0x20000000\n\t"
      "v_mov_b32 v97, 0x20000000\n\t"
      "v_mov_b32 v98, 0x20000000\n\t"
      "v_mov_b32 v99, 0x20000000\n\t"
      "s_mov_b32 s61, 8192\n\t"
      "buffer_store_dwordx4 v[96:99], v101, s[36:39], 0 offen sc1\n\t"
      "buffer_store_dwordx4 v[96:99], v101, s[36:39], s61 offen sc1\n\t"
      "s_nop 1\n\t"
      "v_mov_b32 v96, v111\n\t"
      "v_mov_b32 v97, v111\n\t"
      "v_mov_b32 v98, v111\n\t"
      "v_mov_b32 v99, v111\n\t"
      "buffer_store_dwordx4 v[96:99], v108, s[36:39], 0 offen sc1\n\t"
      "v_mfma_f32_16x16x32_bf16 v[24:27], v[64:67], v[32:35], 0\n\t"
      "ds_read_b128 v[0:3], v104 offset:58368\n\t"
      "v_mfma_f32_16x16x32_bf16 v[28:31], v[64:67], v[36:39], 0\n\t"
      "s_add_u32 m0, s55, 155648\n\t"
      "s_xor_b32 s61, s47, 3932160\n\t"
      "buffer_load_dwordx4 v116, s[88:91], s61 offen lds\n\t"
      "ds_read_b64_tr_b16 a[96:97], v118\n\t"
      "v_mfma_f32_16x16x32_bf16 v[24:27], v[68:71], v[40:43], v[24:27]\n\t"
      "ds_read_b128 v[4:7], v119 offset:58368\n\t"
      "v_cvt_pk_bf16_f32 v152, v16, v17\n\t"
      "v_mfma_f32_16x16x32_bf16 v[28:31], v[68:71], v[44:47], v[28:31]\n\t"
      "s_add_u32 m0, s55, 156672\n\t"
      "s_xor_b32 s61, s47, 3964928\n\t"
      "buffer_load_dwordx4 v116, s[88:91], s61 offen lds\n\t"
      "ds_read_b64_tr_b16 a[98:99], v118 offset:1024\n\t"
      "v_cvt_pk_bf16_f32 v153, v18, v19\n\t"
      "v_mfma_f32_16x16x32_bf16 v[24:27], v[72:75], v[48:51], v[24:27]\n\t"
      "ds_read_b128 v[8:11], v102 offset:58368\n\t"
      "v_cvt_pk_bf16_f32 v216, v20, v21\n\t"
      "v_mfma_f32_16x16x32_bf16 v[28:31], v[72:75], v[52:55], v[28:31]\n\t"
      "ds_read_b64_tr_b16 a[36:37], v117 offset:8192\n\t"
      "s_waitcnt lgkmcnt(12)\n\t"
      "ds_read_b64_tr_b16 a[38:39], v117 offset:9216\n\t"
      "v_cvt_pk_bf16_f32 v217, v22, v23\n\t"
      "v_mfma_f32_16x16x32_bf16 v[24:27], v[76:79], v[56:59], v[24:27]\n\t"
      "ds_read_b128 v[12:15], v103 offset:58368\n\t"
      "v_mfma_f32_16x16x32_bf16 v[28:31], v[76:79], v[60:63], v[28:31]\n\t"
      "ds_read_b64_tr_b16 a[100:101], v118 offset:8192\n\t"
      "s_waitcnt lgkmcnt(12)\n\t"
      "ds_read_b64_tr_b16 a[102:103], v118 offset:9216\n\t"
      "v_mfma_f32_16x16x32_bf16 a[232:235], v[212:215], v[212:215], a[232:235]\n\t"
      "s_waitcnt vmcnt(15)\n\t"
      "s_waitcnt lgkmcnt(0)\n\t"
      "s_barrier\n\t"
      "v_mfma_f32_16x16x32_bf16 v[16:19], v[80:83], v[32:35], 0\n\t"
      "ds_read_b128 v[64:67], v104\n\t"
      "v_mfma_f32_16x16x32_bf16 v[20:23], v[80:83], v[36:39], 0\n\t"
      "s_mov_b64 s[88:89], s[92:93]\n\t"
      "s_add_u32 m0, s55, 81920\n\t"
      "s_xor_b32 s61, s47, 3670016\n\t"
      "buffer_load_dwordx4 v105, s[88:91], s61 offen lds\n\t"
      "ds_read_b64_tr_b16 a[40:41], v117 offset:16384\n\t"
      "v_mfma_f32_16x16x32_bf16 v[16:19], v[84:87], v[40:43], v[16:19]\n\t"
      "ds_read_b128 v[68:71], v119\n\t"
      "v_cvt_pk_bf16_f32 v154, v24, v25\n\t"
      "v_mfma_f32_16x16x32_bf16 v[20:23], v[84:87], v[44:47], v[20:23]\n\t"
      "s_add_u32 m0, s55, 82944\n\t"
      "s_xor_b32 s61, s47, 3702784\n\t"
      "buffer_load_dwordx4 v105, s[88:91], s61 offen lds\n\t"
      "ds_read_b64_tr_b16 a[42:43], v117 offset:17408\n\t"
      "v_cvt_pk_bf16_f32 v155, v26, v27\n\t"
      "v_mfma_f32_16x16x32_bf16 v[16:19], v[88:91], v[48:51], v[16:19]\n\t"
      "ds_read_b128 v[72:75], v102\n\t"
      "v_cvt_pk_bf16_f32 v218, v28, v29\n\t"
      "v_mfma_f32_16x16x32_bf16 v[20:23], v[88:91], v[52:55], v[20:23]\n\t"
      "s_mov_b64 s[88:89], s[98:99]\n\t"
      "s_add_u32 m0, s55, 98304\n\t"
      "s_xor_b32 s61, s47, 0\n\t"
      "buffer_load_dwordx4 v116, s[88:91], s61 offen lds\n\t"
      "ds_read_b64_tr_b16 a[104:105], v118 offset:16384\n\t"
      "v_cvt_pk_bf16_f32 v219, v30, v31\n\t"
      "v_mfma_f32_16x16x32_bf16 v[16:19], v[92:95], v[56:59], v[16:19]\n\t"
      "ds_read_b128 v[76:79], v103\n\t"
      "v_mfma_f32_16x16x32_bf16 v[20:23], v[92:95], v[60:63], v[20:23]\n\t"
      "s_add_u32 m0, s55, 99328\n\t"
      "s_xor_b32 s61, s47, 32768\n\t"
      "buffer_load_dwordx4 v116, s[88:91], s61 offen lds\n\t"
      "ds_read_b64_tr_b16 a[106:107], v118 offset:17408\n\t"
      "v_mfma_f32_16x16x32_bf16 a[168:171], v[152:155], v[152:155], a[168:171]\n\t"
      "v_mfma_f32_16x16x32_bf16 v[24:27], v[0:3], v[32:35], 0\n\t"
      "ds_read_b128 v[80:83], v104 offset:1024\n\t"
      "v_mfma_f32_16x16x32_bf16 v[28:31], v[0:3], v[36:39], 0\n\t"
      "s_add_u32 m0, s55, 106496\n\t"
      "s_xor_b32 s61, s47, 262144\n\t"
      "buffer_load_dwordx4 v116, s[88:91], s61 offen lds\n\t"
      "ds_read_b64_tr_b16 a[44:45], v117 offset:24576\n\t"
      "v_mfma_f32_16x16x32_bf16 v[24:27], v[4:7], v[40:43], v[24:27]\n\t"
      "ds_read_b128 v[84:87], v119 offset:1024\n\t"
      "v_cvt_pk_bf16_f32 v156, v16, v17\n\t"
      "v_mfma_f32_16x16x32_bf16 v[28:31], v[4:7], v[44:47], v[28:31]\n\t"
      "s_add_u32 m0, s55, 107520\n\t"
      "s_xor_b32 s61, s47, 294912\n\t"
      "buffer_load_dwordx4 v116, s[88:91], s61 offen lds\n\t"
      "ds_read_b64_tr_b16 a[46:47], v117 offset:25600\n\t"
      "v_cvt_pk_bf16_f32 v157, v18, v19\n\t"
      "v_mfma_f32_16x16x32_bf16 v[24:27], v[8:11], v[48:51], v[24:27]\n\t"
      "ds_read_b128 v[88:91], v102 offset:1024\n\t"
      "v_cvt_pk_bf16_f32 v220, v20, v21\n\t"
      "v_mfma_f32_16x16x32_bf16 v[28:31], v[8:11], v[52:55], v[28:31]\n\t"
      "ds_read_b64_tr_b16 a[108:109], v118 offset:24576\n\t"
      "ds_read_b64_tr_b16 a[110:111], v118 offset:25600\n\t"
      "v_cvt_pk_bf16_f32 v221, v22, v23\n\t"
      "v_mfma_f32_16x16x32_bf16 v[24:27], v[12:15], v[56:59], v[24:27]\n\t"
      "s_waitcnt lgkmcnt(12)\n\t"
      "ds_read_b128 v[92:95], v103 offset:1024\n\t"
      "v_mfma_f32_16x16x32_bf16 v[28:31], v[12:15], v[60:63], v[28:31]\n\t"
      "v_mfma_f32_16x16x32_bf16 a[232:235], v[216:219], v[216:219], a[232:235]\n\t"
      "s_waitcnt vmcnt(15)\n\t"
      "s_waitcnt lgkmcnt(1)\n\t"
      "s_barrier\n\t"
      "v_mfma_f32_16x16x32_bf16 v[16:19], v[64:67], v[32:35], 0\n\t"
      "ds_read_b128 v[0:3], v104 offset:8192\n\t"
      "v_mfma_f32_16x16x32_bf16 v[20:23], v[64:67], v[36:39], 0\n\t"
      "s_mov_b64 s[88:89], s[92:93]\n\t"
      "s_add_u32 m0, s55, 90112\n\t"
      "s_xor_b32 s61, s47, 3932160\n\t"
      "buffer_load_dwordx4 v105, s[88:91], s61 offen lds\n\t"
      "ds_read_b64_tr_b16 a[240:241], v106\n\t"
      "v_mfma_f32_16x16x32_bf16 v[16:19], v[68:71], v[40:43], v[16:19]\n\t"
      "ds_read_b128 v[4:7], v119 offset:8192\n\t"
      "v_cvt_pk_bf16_f32 v158, v24, v25\n\t"
      "v_mfma_f32_16x16x32_bf16 v[20:23], v[68:71], v[44:47], v[20:23]\n\t"
      "s_add_u32 m0, s55, 91136\n\t"
      "s_xor_b32 s61, s47, 3964928\n\t"
      "buffer_load_dwordx4 v105, s[88:91], s61 offen lds\n\t"
      "ds_read_b64_tr_b16 a[242:243], v106 offset:1024\n\t"
      "v_cvt_pk_bf16_f32 v159, v26, v27\n\t"
      "v_mfma_f32_16x16x32_bf16 v[16:19], v[72:75], v[48:51], v[16:19]\n\t"
      "ds_read_b128 v[8:11], v102 offset:8192\n\t"
      "v_cvt_pk_bf16_f32 v222, v28, v29\n\t"
      "v_mfma_f32_16x16x32_bf16 v[20:23], v[72:75], v[52:55], v[20:23]\n\t"
      "s_mov_b64 s[88:89], s[98:99]\n\t"
      "s_add_u32 m0, s55, 114688\n\t"
      "s_xor_b32 s61, s47, 524288\n\t"
      "buffer_load_dwordx4 v116, s[88:91], s61 offen lds\n\t"
      "ds_read_b64_tr_b16 a[48:49], v117 offset:32768\n\t"
      "v_cvt_pk_bf16_f32 v223, v30, v31\n\t"
      "v_mfma_f32_16x16x32_bf16 v[16:19], v[76:79], v[56:59], v[16:19]\n\t"
      "ds_read_b128 v[12:15], v103 offset:8192\n\t"
      "v_mfma_f32_16x16x32_bf16 v[20:23], v[76:79], v[60:63], v[20:23]\n\t"
      "s_add_u32 m0, s55, 115712\n\t"
      "s_xor_b32 s61, s47, 557056\n\t"
      "buffer_load_dwordx4 v116, s[88:91], s61 offen lds\n\t"
      "ds_read_b64_tr_b16 a[50:51], v117 offset:33792\n\t"
      "v_mfma_f32_16x16x32_bf16 a[168:171], v[156:159], v[156:159], a[168:171]\n\t"
      "v_mfma_f32_16x16x32_bf16 v[24:27], v[80:83], v[32:35], 0\n\t"
      "ds_read_b128 v[64:67], v104 offset:9216\n\t"
      "v_mfma_f32_16x16x32_bf16 v[28:31], v[80:83], v[36:39], 0\n\t"
      "s_add_u32 m0, s55, 122880\n\t"
      "s_xor_b32 s61, s47, 786432\n\t"
      "buffer_load_dwordx4 v116, s[88:91], s61 offen lds\n\t"
      "ds_read_b64_tr_b16 a[112:113], v118 offset:32768\n\t"
      "v_mfma_f32_16x16x32_bf16 v[24:27], v[84:87], v[40:43], v[24:27]\n\t"
      "ds_read_b128 v[68:71], v119 offset:9216\n\t"
      "v_cvt_pk_bf16_f32 v160, v16, v17\n\t"
      "v_mfma_f32_16x16x32_bf16 v[28:31], v[84:87], v[44:47], v[28:31]\n\t"
      "s_add_u32 m0, s55, 123904\n\t"
      "s_xor_b32 s61, s47, 819200\n\t"
      "buffer_load_dwordx4 v116, s[88:91], s61 offen lds\n\t"
      "ds_read_b64_tr_b16 a[114:115], v118 offset:33792\n\t"
      "v_cvt_pk_bf16_f32 v161, v18, v19\n\t"
      "v_mfma_f32_16x16x32_bf16 v[24:27], v[88:91], v[48:51], v[24:27]\n\t"
      "ds_read_b128 v[72:75], v102 offset:9216\n\t"
      "v_cvt_pk_bf16_f32 v224, v20, v21\n\t"
      "v_mfma_f32_16x16x32_bf16 v[28:31], v[88:91], v[52:55], v[28:31]\n\t"
      "ds_read_b64_tr_b16 a[52:53], v117 offset:40960\n\t"
      "s_waitcnt lgkmcnt(12)\n\t"
      "ds_read_b64_tr_b16 a[54:55], v117 offset:41984\n\t"
      "v_cvt_pk_bf16_f32 v225, v22, v23\n\t"
      "v_mfma_f32_16x16x32_bf16 v[24:27], v[92:95], v[56:59], v[24:27]\n\t"
      "ds_read_b128 v[76:79], v103 offset:9216\n\t"
      "v_mfma_f32_16x16x32_bf16 v[28:31], v[92:95], v[60:63], v[28:31]\n\t"
      "ds_read_b64_tr_b16 a[116:117], v118 offset:40960\n\t"
      "s_waitcnt lgkmcnt(12)\n\t"
      "ds_read_b64_tr_b16 a[118:119], v118 offset:41984\n\t"
      "v_mfma_f32_16x16x32_bf16 a[232:235], v[220:223], v[220:223], a[232:235]\n\t"
      "s_waitcnt vmcnt(12)\n\t"
      "s_waitcnt lgkmcnt(0)\n\t"
      "s_barrier\n\t"
      "v_mfma_f32_16x16x32_bf16 v[16:19], v[0:3], v[32:35], 0\n\t"
      "ds_read_b128 v[80:83], v104 offset:16384\n\t"
      "v_mfma_f32_16x16x32_bf16 v[20:23], v[0:3], v[36:39], 0\n\t"
      "s_add_u32 m0, s55, 131072\n\t"
      "s_xor_b32 s61, s47, 1048576\n\t"
      "buffer_load_dwordx4 v116, s[88:91], s61 offen lds\n\t"
      "ds_read_b64_tr_b16 a[56:57], v117 offset:49152\n\t"
      "v_mfma_f32_16x16x32_bf16 v[16:19], v[4:7], v[40:43], v[16:19]\n\t"
      "ds_read_b128 v[84:87], v119 offset:16384\n\t"
      "v_cvt_pk_bf16_f32 v162, v24, v25\n\t"
      "v_mfma_f32_16x16x32_bf16 v[20:23], v[4:7], v[44:47], v[20:23]\n\t"
      "s_add_u32 m0, s55, 132096\n\t"
      "s_xor_b32 s61, s47, 1081344\n\t"
      "buffer_load_dwordx4 v116, s[88:91], s61 offen lds\n\t"
      "ds_read_b64_tr_b16 a[58:59], v117 offset:50176\n\t"
      "v_cvt_pk_bf16_f32 v163, v26, v27\n\t"
      "v_mfma_f32_16x16x32_bf16 v[16:19], v[8:11], v[48:51], v[16:19]\n\t"
      "ds_read_b128 v[88:91], v102 offset:16384\n\t"
      "v_cvt_pk_bf16_f32 v226, v28, v29\n\t"
      "v_mfma_f32_16x16x32_bf16 v[20:23], v[8:11], v[52:55], v[20:23]\n\t"
      "s_add_u32 m0, s55, 139264\n\t"
      "s_xor_b32 s61, s47, 1310720\n\t"
      "buffer_load_dwordx4 v116, s[88:91], s61 offen lds\n\t"
      "ds_read_b64_tr_b16 a[120:121], v118 offset:49152\n\t"
      "v_cvt_pk_bf16_f32 v227, v30, v31\n\t"
      "v_mfma_f32_16x16x32_bf16 v[16:19], v[12:15], v[56:59], v[16:19]\n\t"
      "ds_read_b128 v[92:95], v103 offset:16384\n\t"
      "v_mfma_f32_16x16x32_bf16 v[20:23], v[12:15], v[60:63], v[20:23]\n\t"
      "s_add_u32 m0, s55, 140288\n\t"
      "s_xor_b32 s61, s47, 1343488\n\t"
      "buffer_load_dwordx4 v116, s[88:91], s61 offen lds\n\t"
      "ds_read_b64_tr_b16 a[122:123], v118 offset:50176\n\t"
      "v_mfma_f32_16x16x32_bf16 a[168:171], v[160:163], v[160:163], a[168:171]\n\t"
      "v_mfma_f32_16x16x32_bf16 v[24:27], v[64:67], v[32:35], 0\n\t"
      "ds_read_b128 v[0:3], v104 offset:17408\n\t"
      "v_mfma_f32_16x16x32_bf16 v[28:31], v[64:67], v[36:39], 0\n\t"
      "ds_read_b64_tr_b16 a[60:61], v117 offset:57344\n\t"
      "ds_read_b64_tr_b16 a[62:63], v117 offset:58368\n\t"
      "v_mfma_f32_16x16x32_bf16 v[24:27], v[68:71], v[40:43], v[24:27]\n\t"
      "ds_read_b128 v[4:7], v119 offset:17408\n\t"
      "v_cvt_pk_bf16_f32 v164, v16, v17\n\t"
      "v_mfma_f32_16x16x32_bf16 v[28:31], v[68:71], v[44:47], v[28:31]\n\t"
      "ds_read_b64_tr_b16 a[124:125], v118 offset:57344\n\t"
      "ds_read_b64_tr_b16 a[126:127], v118 offset:58368\n\t"
      "v_cvt_pk_bf16_f32 v165, v18, v19\n\t"
      "v_mfma_f32_16x16x32_bf16 v[24:27], v[72:75], v[48:51], v[24:27]\n\t"
      "ds_read_b128 v[8:11], v102 offset:17408\n\t"
      "v_cvt_pk_bf16_f32 v228, v20, v21\n\t"
      "v_mfma_f32_16x16x32_bf16 v[28:31], v[72:75], v[52:55], v[28:31]\n\t"
      "v_cvt_pk_bf16_f32 v229, v22, v23\n\t"
      "v_mfma_f32_16x16x32_bf16 v[24:27], v[76:79], v[56:59], v[24:27]\n\t"
      "s_waitcnt lgkmcnt(12)\n\t"
      "ds_read_b128 v[12:15], v103 offset:17408\n\t"
      "v_mfma_f32_16x16x32_bf16 v[28:31], v[76:79], v[60:63], v[28:31]\n\t"
      "v_mfma_f32_16x16x32_bf16 a[232:235], v[224:227], v[224:227], a[232:235]\n\t"
      "s_waitcnt vmcnt(10)\n\t"
      "s_waitcnt lgkmcnt(2)\n\t"
      "s_barrier\n\t"
      "v_mfma_f32_16x16x32_bf16 v[16:19], v[80:83], v[32:35], 0\n\t"
      "ds_read_b128 v[64:67], v104 offset:24576\n\t"
      "v_mfma_f32_16x16x32_bf16 v[20:23], v[80:83], v[36:39], 0\n\t"
      "s_add_u32 m0, s55, 147456\n\t"
      "s_xor_b32 s61, s47, 1572864\n\t"
      "buffer_load_dwordx4 v116, s[88:91], s61 offen lds\n\t"
      "ds_read_b64_tr_b16 a[244:245], v106 offset:16384\n\t"
      "v_mfma_f32_16x16x32_bf16 v[16:19], v[84:87], v[40:43], v[16:19]\n\t"
      "ds_read_b128 v[68:71], v119 offset:24576\n\t"
      "v_cvt_pk_bf16_f32 v166, v24, v25\n\t"
      "v_mfma_f32_16x16x32_bf16 v[20:23], v[84:87], v[44:47], v[20:23]\n\t"
      "s_add_u32 m0, s55, 148480\n\t"
      "s_xor_b32 s61, s47, 1605632\n\t"
      "buffer_load_dwordx4 v116, s[88:91], s61 offen lds\n\t"
      "ds_read_b64_tr_b16 a[246:247], v106 offset:17408\n\t"
      "v_cvt_pk_bf16_f32 v167, v26, v27\n\t"
      "v_mfma_f32_16x16x32_bf16 v[16:19], v[88:91], v[48:51], v[16:19]\n\t"
      "ds_read_b128 v[72:75], v102 offset:24576\n\t"
      "v_cvt_pk_bf16_f32 v230, v28, v29\n\t"
      "v_mfma_f32_16x16x32_bf16 v[20:23], v[88:91], v[52:55], v[20:23]\n\t"
      "s_add_u32 m0, s55, 155648\n\t"
      "s_xor_b32 s61, s47, 1835008\n\t"
      "buffer_load_dwordx4 v116, s[88:91], s61 offen lds\n\t"
      "ds_read_b64_tr_b16 a[128:129], v117\n\t"
      "v_cvt_pk_bf16_f32 v231, v30, v31\n\t"
      "v_mfma_f32_16x16x32_bf16 v[16:19], v[92:95], v[56:59], v[16:19]\n\t"
      "ds_read_b128 v[76:79], v103 offset:24576\n\t"
      "v_mfma_f32_16x16x32_bf16 v[20:23], v[92:95], v[60:63], v[20:23]\n\t"
      "s_add_u32 m0, s55, 156672\n\t"
      "s_xor_b32 s61, s47, 1867776\n\t"
      "buffer_load_dwordx4 v116, s[88:91], s61 offen lds\n\t"
      "ds_read_b64_tr_b16 a[130:131], v117 offset:1024\n\t"
      "v_mfma_f32_16x16x32_bf16 a[168:171], v[164:167], v[164:167], a[168:171]\n\t"
      "v_mfma_f32_16x16x32_bf16 v[24:27], v[0:3], v[32:35], 0\n\t"
      "ds_read_b128 v[80:83], v104 offset:25600\n\t"
      "v_mfma_f32_16x16x32_bf16 v[28:31], v[0:3], v[36:39], 0\n\t"
      "ds_read_b64_tr_b16 a[192:193], v118\n\t"
      "ds_read_b64_tr_b16 a[194:195], v118 offset:1024\n\t"
      "v_mfma_f32_16x16x32_bf16 v[24:27], v[4:7], v[40:43], v[24:27]\n\t"
      "ds_read_b128 v[84:87], v119 offset:25600\n\t"
      "v_cvt_pk_bf16_f32 v168, v16, v17\n\t"
      "v_mfma_f32_16x16x32_bf16 v[28:31], v[4:7], v[44:47], v[28:31]\n\t"
      "ds_read_b64_tr_b16 a[132:133], v117 offset:8192\n\t"
      "s_waitcnt lgkmcnt(12)\n\t"
      "ds_read_b64_tr_b16 a[134:135], v117 offset:9216\n\t"
      "v_cvt_pk_bf16_f32 v169, v18, v19\n\t"
      "v_mfma_f32_16x16x32_bf16 v[24:27], v[8:11], v[48:51], v[24:27]\n\t"
      "ds_read_b128 v[88:91], v102 offset:25600\n\t"
      "v_cvt_pk_bf16_f32 v232, v20, v21\n\t"
      "v_mfma_f32_16x16x32_bf16 v[28:31], v[8:11], v[52:55], v[28:31]\n\t"
      "ds_read_b64_tr_b16 a[196:197], v118 offset:8192\n\t"
      "s_waitcnt lgkmcnt(12)\n\t"
      "ds_read_b64_tr_b16 a[198:199], v118 offset:9216\n\t"
      "v_cvt_pk_bf16_f32 v233, v22, v23\n\t"
      "v_mfma_f32_16x16x32_bf16 v[24:27], v[12:15], v[56:59], v[24:27]\n\t"
      "ds_read_b128 v[92:95], v103 offset:25600\n\t"
      "v_mfma_f32_16x16x32_bf16 v[28:31], v[12:15], v[60:63], v[28:31]\n\t"
      "v_mfma_f32_16x16x32_bf16 a[232:235], v[228:231], v[228:231], a[232:235]\n\t"
      "s_waitcnt vmcnt(8)\n\t"
      "s_waitcnt lgkmcnt(1)\n\t"
      "s_barrier\n\t"
      "v_mfma_f32_16x16x32_bf16 v[16:19], v[64:67], v[32:35], 0\n\t"
      "ds_read_b128 v[0:3], v104 offset:32768\n\t"
      "v_mfma_f32_16x16x32_bf16 v[20:23], v[64:67], v[36:39], 0\n\t"
      "s_add_u32 m0, s55, 98304\n\t"
      "s_xor_b32 s61, s47, 2097152\n\t"
      "buffer_load_dwordx4 v116, s[88:91], s61 offen lds\n\t"
      "ds_read_b64_tr_b16 a[136:137], v117 offset:16384\n\t"
      "v_mfma_f32_16x16x32_bf16 v[16:19], v[68:71], v[40:43], v[16:19]\n\t"
      "ds_read_b128 v[4:7], v119 offset:32768\n\t"
      "v_cvt_pk_bf16_f32 v170, v24, v25\n\t"
      "v_mfma_f32_16x16x32_bf16 v[20:23], v[68:71], v[44:47], v[20:23]\n\t"
      "s_add_u32 m0, s55, 99328\n\t"
      "s_xor_b32 s61, s47, 2129920\n\t"
      "buffer_load_dwordx4 v116, s[88:91], s61 offen lds\n\t"
      "ds_read_b64_tr_b16 a[138:139], v117 offset:17408\n\t"
      "v_cvt_pk_bf16_f32 v171, v26, v27\n\t"
      "v_mfma_f32_16x16x32_bf16 v[16:19], v[72:75], v[48:51], v[16:19]\n\t"
      "ds_read_b128 v[8:11], v102 offset:32768\n\t"
      "v_cvt_pk_bf16_f32 v234, v28, v29\n\t"
      "v_mfma_f32_16x16x32_bf16 v[20:23], v[72:75], v[52:55], v[20:23]\n\t"
      "s_add_u32 m0, s55, 106496\n\t"
      "s_xor_b32 s61, s47, 2359296\n\t"
      "buffer_load_dwordx4 v116, s[88:91], s61 offen lds\n\t"
      "ds_read_b64_tr_b16 a[200:201], v118 offset:16384\n\t"
      "v_cvt_pk_bf16_f32 v235, v30, v31\n\t"
      "v_mfma_f32_16x16x32_bf16 v[16:19], v[76:79], v[56:59], v[16:19]\n\t"
      "ds_read_b128 v[12:15], v103 offset:32768\n\t"
      "v_mfma_f32_16x16x32_bf16 v[20:23], v[76:79], v[60:63], v[20:23]\n\t"
      "s_add_u32 m0, s55, 107520\n\t"
      "s_xor_b32 s61, s47, 2392064\n\t"
      "buffer_load_dwordx4 v116, s[88:91], s61 offen lds\n\t"
      "ds_read_b64_tr_b16 a[202:203], v118 offset:17408\n\t"
      "v_mfma_f32_16x16x32_bf16 a[168:171], v[168:171], v[168:171], a[168:171]\n\t"
      "v_mfma_f32_16x16x32_bf16 v[24:27], v[80:83], v[32:35], 0\n\t"
      "ds_read_b128 v[64:67], v104 offset:33792\n\t"
      "v_mfma_f32_16x16x32_bf16 v[28:31], v[80:83], v[36:39], 0\n\t"
      "ds_read_b64_tr_b16 a[140:141], v117 offset:24576\n\t"
      "ds_read_b64_tr_b16 a[142:143], v117 offset:25600\n\t"
      "v_mfma_f32_16x16x32_bf16 v[24:27], v[84:87], v[40:43], v[24:27]\n\t"
      "ds_read_b128 v[68:71], v119 offset:33792\n\t"
      "v_cvt_pk_bf16_f32 v172, v16, v17\n\t"
      "v_mfma_f32_16x16x32_bf16 v[28:31], v[84:87], v[44:47], v[28:31]\n\t"
      "ds_read_b64_tr_b16 a[204:205], v118 offset:24576\n\t"
      "ds_read_b64_tr_b16 a[206:207], v118 offset:25600\n\t"
      "v_cvt_pk_bf16_f32 v173, v18, v19\n\t"
      "v_mfma_f32_16x16x32_bf16 v[24:27], v[88:91], v[48:51], v[24:27]\n\t"
      "s_waitcnt lgkmcnt(12)\n\t"
      "ds_read_b128 v[72:75], v102 offset:33792\n\t"
      "v_cvt_pk_bf16_f32 v236, v20, v21\n\t"
      "v_mfma_f32_16x16x32_bf16 v[28:31], v[88:91], v[52:55], v[28:31]\n\t"
      "v_cvt_pk_bf16_f32 v237, v22, v23\n\t"
      "v_mfma_f32_16x16x32_bf16 v[24:27], v[92:95], v[56:59], v[24:27]\n\t"
      "ds_read_b128 v[76:79], v103 offset:33792\n\t"
      "v_mfma_f32_16x16x32_bf16 v[28:31], v[92:95], v[60:63], v[28:31]\n\t"
      "v_mfma_f32_16x16x32_bf16 a[232:235], v[232:235], v[232:235], a[232:235]\n\t"
      "s_waitcnt vmcnt(8)\n\t"
      "s_waitcnt lgkmcnt(2)\n\t"
      "s_barrier\n\t"
      "v_mfma_f32_16x16x32_bf16 v[16:19], v[0:3], v[32:35], 0\n\t"
      "ds_read_b128 v[80:83], v104 offset:40960\n\t"
      "v_mfma_f32_16x16x32_bf16 v[20:23], v[0:3], v[36:39], 0\n\t"
      "s_add_u32 m0, s55, 114688\n\t"
      "s_xor_b32 s61, s47, 2621440\n\t"
      "buffer_load_dwordx4 v116, s[88:91], s61 offen lds\n\t"
      "ds_read_b64_tr_b16 a[248:249], v106 offset:32768\n\t"
      "v_mfma_f32_16x16x32_bf16 v[16:19], v[4:7], v[40:43], v[16:19]\n\t"
      "ds_read_b128 v[84:87], v119 offset:40960\n\t"
      "v_cvt_pk_bf16_f32 v174, v24, v25\n\t"
      "v_mfma_f32_16x16x32_bf16 v[20:23], v[4:7], v[44:47], v[20:23]\n\t"
      "s_add_u32 m0, s55, 115712\n\t"
      "s_xor_b32 s61, s47, 2654208\n\t"
      "buffer_load_dwordx4 v116, s[88:91], s61 offen lds\n\t"
      "ds_read_b64_tr_b16 a[250:251], v106 offset:33792\n\t"
      "v_cvt_pk_bf16_f32 v175, v26, v27\n\t"
      "v_mfma_f32_16x16x32_bf16 v[16:19], v[8:11], v[48:51], v[16:19]\n\t"
      "ds_read_b128 v[88:91], v102 offset:40960\n\t"
      "v_cvt_pk_bf16_f32 v238, v28, v29\n\t"
      "v_mfma_f32_16x16x32_bf16 v[20:23], v[8:11], v[52:55], v[20:23]\n\t"
      "s_add_u32 m0, s55, 122880\n\t"
      "s_xor_b32 s61, s47, 2883584\n\t"
      "buffer_load_dwordx4 v116, s[88:91], s61 offen lds\n\t"
      "ds_read_b64_tr_b16 a[144:145], v117 offset:32768\n\t"
      "v_cvt_pk_bf16_f32 v239, v30, v31\n\t"
      "v_mfma_f32_16x16x32_bf16 v[16:19], v[12:15], v[56:59], v[16:19]\n\t"
      "ds_read_b128 v[92:95], v103 offset:40960\n\t"
      "v_mfma_f32_16x16x32_bf16 v[20:23], v[12:15], v[60:63], v[20:23]\n\t"
      "s_add_u32 m0, s55, 123904\n\t"
      "s_xor_b32 s61, s47, 2916352\n\t"
      "buffer_load_dwordx4 v116, s[88:91], s61 offen lds\n\t"
      "ds_read_b64_tr_b16 a[146:147], v117 offset:33792\n\t"
      "v_mfma_f32_16x16x32_bf16 a[168:171], v[172:175], v[172:175], a[168:171]\n\t"
      "v_mfma_f32_16x16x32_bf16 v[24:27], v[64:67], v[32:35], 0\n\t"
      "ds_read_b128 v[0:3], v104 offset:41984\n\t"
      "v_mfma_f32_16x16x32_bf16 v[28:31], v[64:67], v[36:39], 0\n\t"
      "ds_read_b64_tr_b16 a[208:209], v118 offset:32768\n\t"
      "ds_read_b64_tr_b16 a[210:211], v118 offset:33792\n\t"
      "v_mfma_f32_16x16x32_bf16 v[24:27], v[68:71], v[40:43], v[24:27]\n\t"
      "ds_read_b128 v[4:7], v119 offset:41984\n\t"
      "v_cvt_pk_bf16_f32 v176, v16, v17\n\t"
      "v_mfma_f32_16x16x32_bf16 v[28:31], v[68:71], v[44:47], v[28:31]\n\t"
      "ds_read_b64_tr_b16 a[148:149], v117 offset:40960\n\t"
      "s_waitcnt lgkmcnt(12)\n\t"
      "ds_read_b64_tr_b16 a[150:151], v117 offset:41984\n\t"
      "v_cvt_pk_bf16_f32 v177, v18, v19\n\t"
      "v_mfma_f32_16x16x32_bf16 v[24:27], v[72:75], v[48:51], v[24:27]\n\t"
      "ds_read_b128 v[8:11], v102 offset:41984\n\t"
      "v_cvt_pk_bf16_f32 v240, v20, v21\n\t"
      "v_mfma_f32_16x16x32_bf16 v[28:31], v[72:75], v[52:55], v[28:31]\n\t"
      "ds_read_b64_tr_b16 a[212:213], v118 offset:40960\n\t"
      "s_waitcnt lgkmcnt(12)\n\t"
      "ds_read_b64_tr_b16 a[214:215], v118 offset:41984\n\t"
      "v_cvt_pk_bf16_f32 v241, v22, v23\n\t"
      "v_mfma_f32_16x16x32_bf16 v[24:27], v[76:79], v[56:59], v[24:27]\n\t"
      "ds_read_b128 v[12:15], v103 offset:41984\n\t"
      "v_mfma_f32_16x16x32_bf16 v[28:31], v[76:79], v[60:63], v[28:31]\n\t"
      "v_mfma_f32_16x16x32_bf16 a[232:235], v[236:239], v[236:239], a[232:235]\n\t"
      "s_waitcnt vmcnt(8)\n\t"
      "s_waitcnt lgkmcnt(1)\n\t"
      "s_barrier\n\t"
      "v_mfma_f32_16x16x32_bf16 v[16:19], v[80:83], v[32:35], 0\n\t"
      "ds_read_b128 v[64:67], v104 offset:49152\n\t"
      "v_mfma_f32_16x16x32_bf16 v[20:23], v[80:83], v[36:39], 0\n\t"
      "s_add_u32 m0, s55, 131072\n\t"
      "s_xor_b32 s61, s47, 3145728\n\t"
      "buffer_load_dwordx4 v116, s[88:91], s61 offen lds\n\t"
      "ds_read_b64_tr_b16 a[152:153], v117 offset:49152\n\t"
      "v_mfma_f32_16x16x32_bf16 v[16:19], v[84:87], v[40:43], v[16:19]\n\t"
      "ds_read_b128 v[68:71], v119 offset:49152\n\t"
      "v_cvt_pk_bf16_f32 v178, v24, v25\n\t"
      "v_mfma_f32_16x16x32_bf16 v[20:23], v[84:87], v[44:47], v[20:23]\n\t"
      "s_add_u32 m0, s55, 132096\n\t"
      "s_xor_b32 s61, s47, 3178496\n\t"
      "buffer_load_dwordx4 v116, s[88:91], s61 offen lds\n\t"
      "ds_read_b64_tr_b16 a[154:155], v117 offset:50176\n\t"
      "v_cvt_pk_bf16_f32 v179, v26, v27\n\t"
      "v_mfma_f32_16x16x32_bf16 v[16:19], v[88:91], v[48:51], v[16:19]\n\t"
      "ds_read_b128 v[72:75], v102 offset:49152\n\t"
      "v_cvt_pk_bf16_f32 v242, v28, v29\n\t"
      "v_mfma_f32_16x16x32_bf16 v[20:23], v[88:91], v[52:55], v[20:23]\n\t"
      "s_add_u32 m0, s55, 139264\n\t"
      "s_xor_b32 s61, s47, 3407872\n\t"
      "buffer_load_dwordx4 v116, s[88:91], s61 offen lds\n\t"
      "ds_read_b64_tr_b16 a[216:217], v118 offset:49152\n\t"
      "v_cvt_pk_bf16_f32 v243, v30, v31\n\t"
      "v_mfma_f32_16x16x32_bf16 v[16:19], v[92:95], v[56:59], v[16:19]\n\t"
      "ds_read_b128 v[76:79], v103 offset:49152\n\t"
      "v_mfma_f32_16x16x32_bf16 v[20:23], v[92:95], v[60:63], v[20:23]\n\t"
      "s_add_u32 m0, s55, 140288\n\t"
      "s_xor_b32 s61, s47, 3440640\n\t"
      "buffer_load_dwordx4 v116, s[88:91], s61 offen lds\n\t"
      "ds_read_b64_tr_b16 a[218:219], v118 offset:50176\n\t"
      "v_mfma_f32_16x16x32_bf16 a[168:171], v[176:179], v[176:179], a[168:171]\n\t"
      "v_mfma_f32_16x16x32_bf16 v[24:27], v[0:3], v[32:35], 0\n\t"
      "ds_read_b128 v[80:83], v104 offset:50176\n\t"
      "v_mfma_f32_16x16x32_bf16 v[28:31], v[0:3], v[36:39], 0\n\t"
      "ds_read_b64_tr_b16 a[156:157], v117 offset:57344\n\t"
      "ds_read_b64_tr_b16 a[158:159], v117 offset:58368\n\t"
      "v_mfma_f32_16x16x32_bf16 v[24:27], v[4:7], v[40:43], v[24:27]\n\t"
      "ds_read_b128 v[84:87], v119 offset:50176\n\t"
      "v_cvt_pk_bf16_f32 v180, v16, v17\n\t"
      "v_mfma_f32_16x16x32_bf16 v[28:31], v[4:7], v[44:47], v[28:31]\n\t"
      "ds_read_b64_tr_b16 a[220:221], v118 offset:57344\n\t"
      "ds_read_b64_tr_b16 a[222:223], v118 offset:58368\n\t"
      "v_cvt_pk_bf16_f32 v181, v18, v19\n\t"
      "v_mfma_f32_16x16x32_bf16 v[24:27], v[8:11], v[48:51], v[24:27]\n\t"
      "s_waitcnt lgkmcnt(12)\n\t"
      "ds_read_b128 v[88:91], v102 offset:50176\n\t"
      "v_cvt_pk_bf16_f32 v244, v20, v21\n\t"
      "v_mfma_f32_16x16x32_bf16 v[28:31], v[8:11], v[52:55], v[28:31]\n\t"
      "v_cvt_pk_bf16_f32 v245, v22, v23\n\t"
      "v_mfma_f32_16x16x32_bf16 v[24:27], v[12:15], v[56:59], v[24:27]\n\t"
      "ds_read_b128 v[92:95], v103 offset:50176\n\t"
      "v_mfma_f32_16x16x32_bf16 v[28:31], v[12:15], v[60:63], v[28:31]\n\t"
      "v_mfma_f32_16x16x32_bf16 a[232:235], v[240:243], v[240:243], a[232:235]\n\t"
      "s_waitcnt vmcnt(8)\n\t"
      "s_waitcnt lgkmcnt(2)\n\t"
      "s_barrier\n\t"
      "v_mfma_f32_16x16x32_bf16 v[16:19], v[64:67], v[32:35], 0\n\t"
      "ds_read_b128 v[0:3], v104 offset:57344\n\t"
      "v_mfma_f32_16x16x32_bf16 v[20:23], v[64:67], v[36:39], 0\n\t"
      "s_add_u32 m0, s55, 147456\n\t"
      "s_xor_b32 s61, s47, 3670016\n\t"
      "buffer_load_dwordx4 v116, s[88:91], s61 offen lds\n\t"
      "ds_read_b64_tr_b16 a[252:253], v106 offset:49152\n\t"
      "v_mfma_f32_16x16x32_bf16 v[16:19], v[68:71], v[40:43], v[16:19]\n\t"
      "ds_read_b128 v[4:7], v119 offset:57344\n\t"
      "v_cvt_pk_bf16_f32 v182, v24, v25\n\t"
      "v_mfma_f32_16x16x32_bf16 v[20:23], v[68:71], v[44:47], v[20:23]\n\t"
      "s_add_u32 m0, s55, 148480\n\t"
      "s_xor_b32 s61, s47, 3702784\n\t"
      "buffer_load_dwordx4 v116, s[88:91], s61 offen lds\n\t"
      "ds_read_b64_tr_b16 a[254:255], v106 offset:50176\n\t"
      "v_cvt_pk_bf16_f32 v183, v26, v27\n\t"
      "v_mfma_f32_16x16x32_bf16 v[16:19], v[72:75], v[48:51], v[16:19]\n\t"
      "ds_read_b128 v[8:11], v102 offset:57344\n\t"
      "v_cvt_pk_bf16_f32 v246, v28, v29\n\t"
      "v_mfma_f32_16x16x32_bf16 v[20:23], v[72:75], v[52:55], v[20:23]\n\t"
      "s_add_u32 m0, s55, 155648\n\t"
      "s_xor_b32 s61, s47, 3932160\n\t"
      "buffer_load_dwordx4 v116, s[88:91], s61 offen lds\n\t"
      "ds_read_b64_tr_b16 a[160:161], v117\n\t"
      "v_cvt_pk_bf16_f32 v247, v30, v31\n\t"
      "v_mfma_f32_16x16x32_bf16 v[16:19], v[76:79], v[56:59], v[16:19]\n\t"
      "ds_read_b128 v[12:15], v103 offset:57344\n\t"
      "v_mfma_f32_16x16x32_bf16 v[20:23], v[76:79], v[60:63], v[20:23]\n\t"
      "s_add_u32 m0, s55, 156672\n\t"
      "s_xor_b32 s61, s47, 3964928\n\t"
      "buffer_load_dwordx4 v116, s[88:91], s61 offen lds\n\t"
      "ds_read_b64_tr_b16 a[162:163], v117 offset:1024\n\t"
      "v_mfma_f32_16x16x32_bf16 a[168:171], v[180:183], v[180:183], a[168:171]\n\t"
      "v_mfma_f32_16x16x32_bf16 v[24:27], v[80:83], v[32:35], 0\n\t"
      "ds_read_b128 v[64:67], v104 offset:58368\n\t"
      "v_mfma_f32_16x16x32_bf16 v[28:31], v[80:83], v[36:39], 0\n\t"
      "ds_read_b64_tr_b16 a[224:225], v118\n\t"
      "ds_read_b64_tr_b16 a[226:227], v118 offset:1024\n\t"
      "v_mfma_f32_16x16x32_bf16 v[24:27], v[84:87], v[40:43], v[24:27]\n\t"
      "ds_read_b128 v[68:71], v119 offset:58368\n\t"
      "v_cvt_pk_bf16_f32 v184, v16, v17\n\t"
      "v_mfma_f32_16x16x32_bf16 v[28:31], v[84:87], v[44:47], v[28:31]\n\t"
      "ds_read_b64_tr_b16 a[164:165], v117 offset:8192\n\t"
      "s_waitcnt lgkmcnt(12)\n\t"
      "ds_read_b64_tr_b16 a[166:167], v117 offset:9216\n\t"
      "v_cvt_pk_bf16_f32 v185, v18, v19\n\t"
      "v_mfma_f32_16x16x32_bf16 v[24:27], v[88:91], v[48:51], v[24:27]\n\t"
      "ds_read_b128 v[72:75], v102 offset:58368\n\t"
      "v_cvt_pk_bf16_f32 v248, v20, v21\n\t"
      "v_mfma_f32_16x16x32_bf16 v[28:31], v[88:91], v[52:55], v[28:31]\n\t"
      "ds_read_b64_tr_b16 a[228:229], v118 offset:8192\n\t"
      "s_waitcnt lgkmcnt(12)\n\t"
      "ds_read_b64_tr_b16 a[230:231], v118 offset:9216\n\t"
      "v_cvt_pk_bf16_f32 v249, v22, v23\n\t"
      "v_mfma_f32_16x16x32_bf16 v[24:27], v[92:95], v[56:59], v[24:27]\n\t"
      "ds_read_b128 v[76:79], v103 offset:58368\n\t"
      "v_mfma_f32_16x16x32_bf16 v[28:31], v[92:95], v[60:63], v[28:31]\n\t"
      "v_mfma_f32_16x16x32_bf16 a[232:235], v[244:247], v[244:247], a[232:235]\n\t"
      "v_mfma_f32_16x16x32_bf16 v[16:19], v[0:3], v[32:35], 0\n\t"
      "v_mfma_f32_16x16x32_bf16 v[20:23], v[0:3], v[36:39], 0\n\t"
      "v_mfma_f32_16x16x32_bf16 v[16:19], v[4:7], v[40:43], v[16:19]\n\t"
      "v_cvt_pk_bf16_f32 v186, v24, v25\n\t"
      "v_mfma_f32_16x16x32_bf16 v[20:23], v[4:7], v[44:47], v[20:23]\n\t"
      "v_cvt_pk_bf16_f32 v187, v26, v27\n\t"
      "s_waitcnt lgkmcnt(13)\n\t"
      "v_mfma_f32_16x16x32_bf16 v[16:19], v[8:11], v[48:51], v[16:19]\n\t"
      "v_cvt_pk_bf16_f32 v250, v28, v29\n\t"
      "v_mfma_f32_16x16x32_bf16 v[20:23], v[8:11], v[52:55], v[20:23]\n\t"
      "v_cvt_pk_bf16_f32 v251, v30, v31\n\t"
      "s_waitcnt lgkmcnt(11)\n\t"
      "v_mfma_f32_16x16x32_bf16 v[16:19], v[12:15], v[56:59], v[16:19]\n\t"
      "v_mfma_f32_16x16x32_bf16 v[20:23], v[12:15], v[60:63], v[20:23]\n\t"
      "v_mfma_f32_16x16x32_bf16 a[168:171], v[184:187], v[184:187], a[168:171]\n\t"
      "buffer_load_dwordx4 v[96:99], off, s[36:39], s54 sc1\n\t"
      "s_waitcnt lgkmcnt(9)\n\t"
      "v_mfma_f32_16x16x32_bf16 v[24:27], v[64:67], v[32:35], 0\n\t"
      "v_mfma_f32_16x16x32_bf16 v[28:31], v[64:67], v[36:39], 0\n\t"
      "s_waitcnt lgkmcnt(6)\n\t"
      "v_mfma_f32_16x16x32_bf16 v[24:27], v[68:71], v[40:43], v[24:27]\n\t"
      "v_cvt_pk_bf16_f32 v188, v16, v17\n\t"
      "v_mfma_f32_16x16x32_bf16 v[28:31], v[68:71], v[44:47], v[28:31]\n\t"
      "v_cvt_pk_bf16_f32 v189, v18, v19\n\t"
      "s_waitcnt lgkmcnt(3)\n\t"
      "v_mfma_f32_16x16x32_bf16 v[24:27], v[72:75], v[48:51], v[24:27]\n\t"
      "v_cvt_pk_bf16_f32 v252, v20, v21\n\t"
      "v_mfma_f32_16x16x32_bf16 v[28:31], v[72:75], v[52:55], v[28:31]\n\t"
      "v_cvt_pk_bf16_f32 v253, v22, v23\n\t"
      "s_waitcnt lgkmcnt(0)\n\t"
      "v_mfma_f32_16x16x32_bf16 v[24:27], v[76:79], v[56:59], v[24:27]\n\t"
      "v_mfma_f32_16x16x32_bf16 v[28:31], v[76:79], v[60:63], v[28:31]\n\t"
      "v_mfma_f32_16x16x32_bf16 a[232:235], v[248:251], v[248:251], a[232:235]\n\t"
      "v_lshl_add_u32 v8, s41, 10, v127\n\t"
      "v_add_u32 v8, 65536, v8\n\t"
      "v_mov_b32 v12, 0\n\t"
      "v_mov_b32 v9, s41\n\t"
      "v_lshlrev_b32 v9, 2, v9\n\t"
      "v_cvt_pk_bf16_f32 v190, v24, v25\n\t"
      "v_cvt_pk_bf16_f32 v191, v26, v27\n\t"
      "v_cvt_pk_bf16_f32 v254, v28, v29\n\t"
      "v_cvt_pk_bf16_f32 v255, v30, v31\n\t"
      "s_nop 1\n\t"
      "v_mfma_f32_16x16x32_bf16 a[168:171], v[188:191], v[188:191], a[168:171]\n\t"
      "v_mfma_f32_16x16x32_bf16 a[232:235], v[252:255], v[252:255], a[232:235]\n\t"
      "s_sub_u32 s64, s46, 786432\n\t"
      "s_mov_b32 s71, 98304\n\t"
      "s_mov_b32 s63, 0x10000\n\t"
      "s_nop 4\n\t"
      "v_accvgpr_read_b32 v0, a168\n\t"
      "v_accvgpr_read_b32 v1, a169\n\t"
      "v_accvgpr_read_b32 v2, a170\n\t"
      "v_accvgpr_read_b32 v3, a171\n\t"
      "v_accvgpr_read_b32 v4, a232\n\t"
      "v_accvgpr_read_b32 v5, a233\n\t"
      "v_accvgpr_read_b32 v6, a234\n\t"
      "v_accvgpr_read_b32 v7, a235\n\t"
      "v_and_b32 v0, 0x7fffffff, v0\n\t"
      "v_and_b32 v1, 0x7fffffff, v1\n\t"
      "v_and_b32 v2, 0x7fffffff, v2\n\t"
      "v_and_b32 v3, 0x7fffffff, v3\n\t"
      "v_and_b32 v4, 0x7fffffff, v4\n\t"
      "v_and_b32 v5, 0x7fffffff, v5\n\t"
      "v_and_b32 v6, 0x7fffffff, v6\n\t"
      "v_and_b32 v7, 0x7fffffff, v7\n\t"
      "v_max3_u32 v0, v0, v1, v2\n\t"
      "v_max3_u32 v3, v3, v4, v5\n\t"
      "v_max3_u32 v0, v0, v6, v7\n\t"
      "v_max_u32 v0, v0, v3\n\t"
      "s_nop 1\n\t"
      "v_max_u32_dpp v0, v0, v0 quad_perm:[1,0,3,2] row_mask:0xf bank_mask:0xf\n\t"
      "s_nop 1\n\t"
      "v_max_u32_dpp v0, v0, v0 quad_perm:[2,3,0,1] row_mask:0xf bank_mask:0xf\n\t"
      "s_nop 1\n\t"
      "v_max_u32_dpp v0, v0, v0 row_ror:4 row_mask:0xf bank_mask:0xf\n\t"
      "s_nop 1\n\t"
      "v_max_u32_dpp v0, v0, v0 row_ror:8 row_mask:0xf bank_mask:0xf\n\t"
      "v_mov_b32 v1, v0\n\t"
      "s_nop 1\n\t"
      "v_permlane16_swap_b32 v0, v1\n\t"
      "v_max_u32 v0, v0, v1\n\t"
      "v_mov_b32 v1, v0\n\t"
      "s_nop 1\n\t"
      "v_permlane32_swap_b32 v0, v1\n\t"
      "v_max_u32 v0, v0, v1\n\t"
      "ds_write_b32 v9, v0 offset:32768\n\t"
      "s_waitcnt lgkmcnt(0)\n\t"
      "s_barrier\n\t"
      "ds_read_b128 v[4:7], v12 offset:32768\n\t"
      "s_waitcnt lgkmcnt(0)\n\t"
      "v_max_u32 v4, v4, v5\n\t"
      "v_max3_u32 v4, v4, v6, v7\n\t"
      "v_mov_b32 v5, s53\n\t"
      "s_memrealtime s[92:93]\n\t"
      "v_add_u32 v4, 0x10000, v4\n\t"
      "s_mov_b64 exec, 1\n\t"
      "s_mov_b32 s61, 0xffff\n\t"
      "s_waitcnt lgkmcnt(0)\n\t"
      "v_mov_b32 v6, s92\n\t"
      "v_bfi_b32 v4, s61, v6, v4\n\t"
      "buffer_store_dwordx2 v[4:5], off, s[36:39], s95 sc1\n\t"
      "s_mov_b64 exec, -1\n\t"
      "s_waitcnt vmcnt(1)\n\t"
      "v_bitop3_b32 v96, v96, v97, s53 bitop3:0x7e\n\t"
      "v_bitop3_b32 v98, v98, v99, s53 bitop3:0x7e\n\t"
      "v_or_b32 v96, v96, v98\n\t"
      "s_add_u32 m0, s55, 98304\n\t"
      "v_readfirstlane_b32 s56, v96\n\t"
      "s_nop 1\n\t"
      "s_cmp_eq_u32 s56, 0\n\t"
      "s_cbranch_scc0 LF2%=\n\t"
      "buffer_load_dwordx4 v127, s[36:39], s64 offen sc1 lds\n\t"
      "buffer_load_dwordx4 v127, s[36:39], s64 offen offset:1024 sc1 lds\n\t"
      "LF2%=:\n\t"
      "buffer_load_dwordx4 v[96:99], off, s[36:39], s54 sc1\n\t"
      "ds_write_b128 v8, a[176:179]\n\t"
      "ds_write_b128 v8, a[180:183] offset:4096\n\t"
      "ds_write_b128 v8, a[184:187] offset:8192\n\t"
      "ds_write_b128 v8, a[188:191] offset:12288\n\t"
      "ds_write_b128 v8, a[240:243] offset:16384\n\t"
      "ds_write_b128 v8, a[244:247] offset:20480\n\t"
      "ds_write_b128 v8, a[248:251] offset:24576\n\t"
      "ds_write_b128 v8, a[252:255] offset:28672\n\t"
      "ds_write_b128 v100, v[112:115] offset:57344\n\t"
      "s_cbranch_scc1 LF3%=\n\t"
      "LF0%=:\n\t"
      "s_waitcnt vmcnt(0)\n\t"
      "v_bitop3_b32 v96, v96, v97, s53 bitop3:0x7e\n\t"
      "v_bitop3_b32 v98, v98, v99, s53 bitop3:0x7e\n\t"
      "v_or_b32 v96, v96, v98\n\t"
      "s_add_u32 m0, s55, 98304\n\t"
      "v_readfirstlane_b32 s56, v96\n\t"
      "s_nop 1\n\t"
      "s_cmp_eq_u32 s56, 0\n\t"
      "s_cbranch_scc1 LF1%=\n\t"
      "buffer_load_dwordx4 v[96:99], off, s[36:39], s54 sc1\n\t"
      "s_sub_u32 s63, s63, 1\n\t"
      "s_cmp_lg_u32 s63, 0\n\t"
      "s_cbranch_scc1 LF0%=\n\t"
      "LF1%=:\n\t"
      "buffer_load_dwordx4 v127, s[36:39], s64 offen sc1 lds\n\t"
      "buffer_load_dwordx4 v127, s[36:39], s64 offen offset:1024 sc1 lds\n\t"
      "LF3%=:\n\t"
      "v_lshlrev_b32 v52, 4, v127\n\t"
      "s_mov_b32 s62, 0xffff0000\n\t"
      "v_accvgpr_read_b32 v85, a172\n\t"
      "v_accvgpr_read_b32 v87, a173\n\t"
      "v_accvgpr_read_b32 v89, a174\n\t"
      "v_accvgpr_read_b32 v91, a175\n\t"
      "v_accvgpr_read_b32 v93, a236\n\t"
      "v_accvgpr_read_b32 v95, a237\n\t"
      "v_lshl_or_b32 v84, v85, 16, 1\n\t"
      "v_and_or_b32 v85, v85, s62, 1\n\t"
      "v_lshl_or_b32 v86, v87, 16, 1\n\t"
      "v_and_or_b32 v87, v87, s62, 1\n\t"
      "v_lshl_or_b32 v88, v89, 16, 1\n\t"
      "v_and_or_b32 v89, v89, s62, 1\n\t"
      "v_lshl_or_b32 v90, v91, 16, 1\n\t"
      "v_and_or_b32 v91, v91, s62, 1\n\t"
      "v_lshl_or_b32 v92, v93, 16, 1\n\t"
      "v_and_or_b32 v93, v93, s62, 1\n\t"
      "v_lshl_or_b32 v94, v95, 16, 1\n\t"
      "v_and_or_b32 v95, v95, s62, 1\n\t"
      "s_cmp_eq_u32 s86, 0\n\t"
      "s_cbranch_scc1 LC9%=\n\t"
      "s_waitcnt lgkmcnt(0)\n\t"
      "s_memtime s[96:97]\n\t"
      "s_waitcnt lgkmcnt(0)\n\t"
      "s_buffer_load_dword s87, s[36:39], s86\n\t"
      "s_waitcnt lgkmcnt(0)\n\t"
      "s_memtime s[98:99]\n\t"
      "s_waitcnt lgkmcnt(0)\n\t"
      "LC9%=:\n\t"
      "s_sub_u32 s61, s98, s96\n\t"
      "s_lshr_b32 s61, s61, 2\n\t"
      "s_min_u32 s61, s61, 511\n\t"
      "s_sub_u32 s62, 0, s61\n\t"
      "s_bitcmp1_b32 s86, 13\n\t"
      "s_cselect_b32 s61, s62, s61\n\t"
      "s_cmp_eq_u32 s86, 0\n\t"
      "s_cselect_b32 s65, 0, s61\n\t"
      "v_readfirstlane_b32 s97, v121\n\t"
      "v_mov_b32 v65, v115\n\t"
      "v_mov_b32 v64, 0\n\t"
      "v_mov_b32 v66, 0\n\t"
      "v_mov_b32 v67, 0\n\t"
      "v_mov_b32 v109, 0\n\t"
      "v_mov_b32 v110, 0\n\t"
      "v_mov_b32 v111, 0\n\t"
      "s_mov_b32 s40, -1\n\t"
      "s_add_u32 s45, s55, 32768\n\t"
      "v_mov_b32 v126, 0\n\t"
      "s_mov_b32 s56, 0\n\t"
      "s_mov_b32 s58, 1\n\t"
      "s_mov_b32 s59, 0x3e0293ee\n\t"
      "s_mov_b32 s96, 0x40820000\n\t"
      "s_mov_b32 s57, 1.0\n\t"
      "s_add_u32 s68, s43, 8192\n\t"
      "s_add_u32 s97, s97, s43\n\t"
      "s_add_u32 s97, s97, 0x8000\n\t"
      "s_mov_b32 s99, 0\n\t"
      "s_mov_b32 s84, 3\n\t"
      "s_mov_b32 s85, 0x80400000\n\t"
      "s_mov_b32 s26, 2460\n\t"
      "s_mov_b32 s25, 0\n\t"
      "s_memtime s[78:79]\n\t"
      "s_waitcnt lgkmcnt(0)\n\t"
      "s_sub_u32 s78, s78, 4000\n\t"
      "s_mov_b32 s88, s78\n\t"
      "s_mov_b32 s27, s78\n\t"
      "s_mov_b32 s24, s26\n\t"
      "s_mov_b32 s23, s80\n\t"
      "s_mov_b32 s30, 0\n\t"
      "s_add_u32 s50, s50, 32\n\t"
      "s_mov_b32 s72, 0xcccccccc\n\t"
      "s_mov_b32 s73, 0xcccccccc\n\t"
      "s_waitcnt vmcnt(0) lgkmcnt(0)\n\t"
      "s_barrier\n\t"
      "s_mov_b32 m0, s45\n\t"
      "s_sub_u32 s61, 204800, s71\n\t"
      "buffer_load_dwordx4 v65, s[36:39], s44 offen sc1 lds\n\t"
      "buffer_load_dwordx4 v65, s[36:39], s44 offen offset:1024 sc1 lds\n\t"
      "s_add_u32 m0, s55, s61\n\t"
      "s_nop 0\n\t"
      "buffer_load_dwordx4 v127, s[36:39], s46 offen sc1 lds\n\t"
      "buffer_load_dwordx4 v127, s[36:39], s46 offen offset:1024 sc1 lds\n\t"
      "buffer_load_dwordx2 v[48:49], v52, s[36:39], s94 offen sc1\n\t"
      "buffer_load_dwordx2 v[50:51], v52, s[36:39], s94 offen offset:128 sc1\n\t"
      "s_mov_b32 s62, 0xffff0000\n\t"
      "v_accvgpr_read_b32 v97, a238\n\t"
      "v_accvgpr_read_b32 v99, a239\n\t"
      "v_lshl_or_b32 v96, v97, 16, 1\n\t"
      "v_and_or_b32 v97, v97, s62, 1\n\t"
      "v_lshl_or_b32 v98, v99, 16, 1\n\t"
      "v_and_or_b32 v99, v99, s62, 1\n\t"
      "ds_read_b64_tr_b16 a[168:169], v117 offset:16384\n\t"
      "ds_read_b64_tr_b16 a[170:171], v117 offset:17408\n\t"
      "ds_read_b64_tr_b16 a[232:233], v118 offset:16384\n\t"
      "ds_read_b64_tr_b16 a[234:235], v118 offset:17408\n\t"
      "ds_read_b64_tr_b16 a[172:173], v117 offset:24576\n\t"
      "ds_read_b64_tr_b16 a[174:175], v117 offset:25600\n\t"
      "ds_read_b64_tr_b16 a[236:237], v118 offset:24576\n\t"
      "ds_read_b64_tr_b16 a[238:239], v118 offset:25600\n\t"
      "ds_read_b64_tr_b16 a[176:177], v117 offset:32768\n\t"
      "ds_read_b64_tr_b16 a[178:179], v117 offset:33792\n\t"
      "ds_read_b64_tr_b16 a[240:241], v118 offset:32768\n\t"
      "ds_read_b64_tr_b16 a[242:243], v118 offset:33792\n\t"
      "ds_read_b64_tr_b16 a[180:181], v117 offset:40960\n\t"
      "ds_read_b64_tr_b16 a[182:183], v117 offset:41984\n\t"
      "ds_read_b64_tr_b16 a[244:245], v118 offset:40960\n\t"
      "s_waitcnt lgkmcnt(12)\n\t"
      "ds_read_b64_tr_b16 a[246:247], v118 offset:41984\n\t"
      "ds_read_b64_tr_b16 a[184:185], v117 offset:49152\n\t"
      "ds_read_b64_tr_b16 a[186:187], v117 offset:50176\n\t"
      "s_waitcnt lgkmcnt(12)\n\t"
      "ds_read_b64_tr_b16 a[248:249], v118 offset:49152\n\t"
      "ds_read_b64_tr_b16 a[250:251], v118 offset:50176\n\t"
      "ds_read_b64_tr_b16 a[188:189], v117 offset:57344\n\t"
      "s_waitcnt lgkmcnt(12)\n\t"
      "ds_read_b64_tr_b16 a[190:191], v117 offset:58368\n\t"
      "ds_read_b64_tr_b16 a[252:253], v118 offset:57344\n\t"
      "ds_read_b64_tr_b16 a[254:255], v118 offset:58368\n\t"
      "s_waitcnt lgkmcnt(6)\n\t"
      "s_cmp_eq_u32 s41, 1\n\t"
      "s_cbranch_scc1 LWIN_1%=\n\t"
      "s_cmp_eq_u32 s41, 2\n\t"
      "s_cbranch_scc1 LWIN_2%=\n\t"
      "s_cmp_eq_u32 s41, 3\n\t"
      "s_cbranch_scc1 LWIN_3%=\n\t"
      "s_branch LWIN_0%=\n\t"
      ".p2align 6\n\t.fill 16, 4, 0xbf800000\n\t"
      "LPOLL_0%=:\n\t"
      "buffer_load_dwordx4 v[84:87], v60, s[36:39], s43 offen sc1\n\t"
      "buffer_load_dwordx4 v[88:91], v60, s[36:39], s43 offen offset:1024 sc1\n\t"
      "s_waitcnt lgkmcnt(4)\n\t"
      "v_mfma_f32_16x16x32_bf16 v[32:35], v[0:3], v[8:11], v[32:35]\n\t"
      "v_mfma_f32_16x16x32_bf16 v[36:39], v[4:7], v[8:11], v[36:39]\n\t"
      "v_mfma_f32_16x16x32_bf16 v[40:43], v[0:3], v[12:15], v[40:43]\n\t"
      "v_mfma_f32_16x16x32_bf16 v[44:47], v[4:7], v[12:15], v[44:47]\n\t"
      "buffer_load_dwordx4 v[92:95], v60, s[36:39], s68 offen sc1\n\t"
      "buffer_load_dwordx4 v[96:99], v60, s[36:39], s68 offen offset:1024 sc1\n\t"
      "ds_read_b64 v[54:55], v124 offset:53312\n\t"
      "s_xor_b32 s69, s97, 1024\n\t"
      "s_xor_b32 s98, s97, 512\n\t"
      "s_xor_b32 s23, s97, 1536\n\t"
      "s_waitcnt lgkmcnt(1)\n\t"
      "v_mfma_f32_16x16x32_bf16 v[32:35], v[76:79], v[112:115], v[32:35]\n\t"
      "v_mfma_f32_16x16x32_bf16 v[36:39], v[80:83], v[112:115], v[36:39]\n\t"
      "v_mfma_f32_16x16x32_bf16 v[40:43], v[76:79], v[116:119], v[40:43]\n\t"
      "v_mfma_f32_16x16x32_bf16 v[44:47], v[80:83], v[116:119], v[44:47]\n\t"
      "s_waitcnt lgkmcnt(0)\n\t"
      "v_add_f32 v48, v48, v54\n\t"
      "v_add_f32_e64 v49, v49, v55\n\t"
      "v_cvt_pk_bf16_f32 v48, v48, v49\n\t"
      "buffer_store_dword v48, v62, s[36:39], s28 offen sc1\n\t"
      "s_mov_b64 exec, 1\n\t"
      "ds_write_b64 v109, v[110:111] offset:55392\n\t"
      "s_mov_b64 exec, -1\n\t"
      "s_nop 4\n\t"
      "v_cvt_pk_bf16_f32 v68, v32, v33\n\t"
      "v_cvt_pk_bf16_f32 v69, v34, v35\n\t"
      "v_cvt_pk_bf16_f32 v70, v36, v37\n\t"
      "v_cvt_pk_bf16_f32 v71, v38, v39\n\t"
      "v_cvt_pk_bf16_f32 v74, v40, v41\n\t"
      "v_cvt_pk_bf16_f32 v75, v42, v43\n\t"
      "v_cvt_pk_bf16_f32 v72, v44, v45\n\t"
      "v_cvt_pk_bf16_f32 v73, v46, v47\n\t"
      "v_mov_b64 v[32:33], 0\n\t"
      "v_mov_b64 v[34:35], 0\n\t"
      "v_mov_b64 v[36:37], 0\n\t"
      "v_mov_b64 v[38:39], 0\n\t"
      "v_mov_b64 v[40:41], 0\n\t"
      "v_mov_b64 v[42:43], 0\n\t"
      "v_mov_b64 v[44:45], 0\n\t"
      "v_mov_b64 v[46:47], 0\n\t"
      "v_mov_b64 v[48:49], 0\n\t"
      "v_mov_b64 v[50:51], 0\n\t"
      "v_mov_b64 v[52:53], 0\n\t"
      "v_mov_b64 v[54:55], 0\n\t"
      "LGO_0%=:\n\t"
      "s_nop 0\n\t"
      "s_nop 0\n\t"
      "s_nop 0\n\t"
      "s_nop 0\n\t"
      "s_waitcnt vmcnt(4)\n\t"
      "v_pk_fma_f32 v[6:7], v[86:87], s[56:57], v[126:127] op_sel:[0,1,0] op_sel_hi:[1,1,0]\n\t"
      "v_pk_fma_f32 v[4:5], v[84:85], s[56:57], v[126:127] op_sel:[0,1,0] op_sel_hi:[1,1,0]\n\t"
      "v_cvt_pk_bf16_f32 v0, v4, v5\n\t"
      "v_cvt_pk_bf16_f32 v1, v6, v7\n\t"
      "s_nop 0\n\t"
      "ds_write_b64 v127, v[0:1] offset:40960\n\t"
      "s_waitcnt vmcnt(3)\n\t"
      "v_pk_fma_f32 v[8:9], v[88:89], s[56:57], v[126:127] op_sel:[0,1,0] op_sel_hi:[1,1,0]\n\t"
      "v_pk_fma_f32 v[10:11], v[90:91], s[56:57], v[126:127] op_sel:[0,1,0] op_sel_hi:[1,1,0]\n\t"
      "v_cvt_pk_bf16_f32 v2, v8, v9\n\t"
      "v_cvt_pk_bf16_f32 v3, v10, v11\n\t"
      "ds_write_b64 v127, v[2:3] offset:40968\n\t"
      "v_bitop3_b32 v64, v84, v85, v86 bitop3:0x80\n\t"
      "v_mfma_f32_16x16x32_bf16 v[56:59], v[0:3], v[0:3], 0\n\t"
      "v_bitop3_b32 v64, v64, v87, v88 bitop3:0x80\n\t"
      "v_bitop3_b32 v64, v64, v89, v90 bitop3:0x80\n\t"
      "v_bitop3_b32 v64, v64, v91, s58 bitop3:0x80\n\t"
      "v_cmp_eq_u32_e64 vcc, 0, v64\n\t"
      "v_smfmac_f32_16x16x64_bf16 v[36:39], v[0:3], a[64:71], v122\n\t"
      "s_nop 0\n\t"
      "v_bitop3_b32 v120, v120, v121, s53 bitop3:0x7e\n\t"
      "v_smfmac_f32_16x16x64_bf16 v[44:47], v[0:3], a[192:199], v122\n\t"
      "s_cbranch_vccnz LR0_0%=\n\t"
      "LC0_0%=:\n\t"
      "s_waitcnt lgkmcnt(0)\n\t"
      "s_barrier\n\t"
      "ds_read_b128 v[4:7], v127 offset:41984\n\t"
      "v_smfmac_f32_16x16x64_bf16 v[52:55], v[0:3], v[192:199], v122\n\t"
      "ds_read_b128 v[8:11], v127 offset:43008\n\t"
      "v_smfmac_f32_16x16x64_bf16 v[48:51], v[0:3], v[128:135], v122\n\t"
      "ds_read_b128 v[12:15], v127 offset:44032\n\t"
      "v_smfmac_f32_16x16x64_bf16 v[32:35], v[0:3], a[0:7], v122\n\t"
      "ds_read_b128 v[84:87], v127 offset:36864\n\t"
      "v_smfmac_f32_16x16x64_bf16 v[40:43], v[0:3], a[128:135], v122\n\t"
      "s_waitcnt lgkmcnt(3)\n\t"
      "v_smfmac_f32_16x16x64_bf16 v[32:35], v[4:7], a[8:15], v122\n\t"
      "ds_read_b128 v[80:83], v127 offset:35840\n\t"
      "v_smfmac_f32_16x16x64_bf16 v[40:43], v[4:7], a[136:143], v122\n\t"
      "s_waitcnt vmcnt(2)\n\t"
      "v_fma_f32 v23, v95, s57, v126\n\t"
      "v_fma_f32 v20, v92, s57, v126\n\t"
      "v_smfmac_f32_16x16x64_bf16 v[36:39], v[4:7], a[72:79], v122\n\t"
      "v_fma_f32 v22, v94, s57, v126\n\t"
      "v_cvt_pk_bf16_f32 v17, v22, v23\n\t"
      "v_bitop3_b32 v64, v92, v93, v94 bitop3:0x80\n\t"
      "v_smfmac_f32_16x16x64_bf16 v[44:47], v[4:7], a[200:207], v122\n\t"
      "s_waitcnt lgkmcnt(2)\n\t"
      "v_fma_f32 v21, v93, s57, v126\n\t"
      "v_cvt_pk_bf16_f32 v16, v20, v21\n\t"
      "v_smfmac_f32_16x16x64_bf16 v[36:39], v[8:11], a[80:87], v122\n\t"
      "s_waitcnt vmcnt(1)\n\t"
      "ds_write_b64 v127, v[16:17] offset:45056\n\t"
      "v_fma_f32 v27, v99, s57, v126\n\t"
      "v_fma_f32 v26, v98, s57, v126\n\t"
      "v_cvt_pk_bf16_f32 v19, v26, v27\n\t"
      "v_fma_f32 v25, v97, s57, v126\n\t"
      "v_fma_f32 v24, v96, s57, v126\n\t"
      "v_cvt_pk_bf16_f32 v18, v24, v25\n\t"
      "v_smfmac_f32_16x16x64_bf16 v[44:47], v[8:11], a[208:215], v122\n\t"
      "ds_write_b64 v127, v[18:19] offset:45064\n\t"
      "v_smfmac_f32_16x16x64_bf16 v[40:43], v[8:11], a[144:151], v122\n\t"
      "ds_read_b128 v[88:91], v127 offset:39936\n\t"
      "v_bitop3_b32 v64, v64, v95, v96 bitop3:0x80\n\t"
      "v_bitop3_b32 v64, v64, v97, v98 bitop3:0x80\n\t"
      "v_smfmac_f32_16x16x64_bf16 v[40:43], v[12:15], a[152:159], v122\n\t"
      "v_bitop3_b32 v64, v64, v99, s60 bitop3:0x80\n\t"
      "v_smfmac_f32_16x16x64_bf16 v[36:39], v[12:15], a[88:95], v122\n\t"
      "v_cmp_eq_u32_e64 vcc, 0, v64\n\t"
      "v_smfmac_f32_16x16x64_bf16 v[44:47], v[12:15], a[216:223], v122\n\t"
      "s_waitcnt lgkmcnt(1)\n\t"
      "v_smfmac_f32_16x16x64_bf16 v[32:35], v[8:11], a[16:23], v122\n\t"
      "s_cbranch_vccnz LR1_0%=\n\t"
      "LC1_0%=:\n\t"
      "v_smfmac_f32_16x16x64_bf16 v[40:43], v[16:19], a[160:167], v122\n\t"
      "s_barrier\n\t"
      "v_mfma_f32_16x16x32_bf16 v[56:59], v[16:19], v[16:19], v[56:59]\n\t"
      "ds_read_b128 v[20:23], v127 offset:46080\n\t"
      "s_nop 0\n\t"
      "v_smfmac_f32_16x16x64_bf16 v[32:35], v[12:15], a[24:31], v122\n\t"
      "ds_read_b128 v[24:27], v127 offset:47104\n\t"
      "v_smfmac_f32_16x16x64_bf16 v[44:47], v[16:19], a[224:231], v122\n\t"
      "ds_read_b128 v[28:31], v127 offset:48128\n\t"
      "v_max3_f32 v56, v56, v57, v58\n\t"
      "v_smfmac_f32_16x16x64_bf16 v[36:39], v[16:19], a[96:103], v122\n\t"
      "ds_read_b128 v[76:79], v127 offset:32768\n\t"
      "v_max_f32_e64 v56, v56, v59\n\t"
      "v_smfmac_f32_16x16x64_bf16 v[32:35], v[16:19], a[32:39], v122\n\t"
      "s_waitcnt lgkmcnt(3)\n\t"
      "v_readfirstlane_b32 s56, v120\n\t"
      "v_smfmac_f32_16x16x64_bf16 v[32:35], v[20:23], a[40:47], v122\n\t"
      "s_waitcnt lgkmcnt(2)\n\t"
      "v_max_f32_dpp v56, v56, v56 quad_perm:[1,0,3,2] row_mask:0xf bank_mask:0xf\n\t"
      "v_smfmac_f32_16x16x64_bf16 v[32:35], v[24:27], a[48:55], v122\n\t"
      "s_waitcnt lgkmcnt(1)\n\t"
      "v_max_f32_dpp v56, v56, v56 quad_perm:[2,3,0,1] row_mask:0xf bank_mask:0xf\n\t"
      "v_smfmac_f32_16x16x64_bf16 v[32:35], v[28:31], a[56:63], v122\n\t"
      "s_mov_b32 exec_lo, 0x100001\n\t"
      "s_mov_b32 exec_hi, 0x10000100\n\t"
      "v_smfmac_f32_16x16x64_bf16 v[36:39], v[20:23], a[104:111], v122\n\t"
      "ds_max_i32 v109, v56 offset:55392\n\t"
      "v_smfmac_f32_16x16x64_bf16 v[40:43], v[20:23], a[168:175], v122\n\t"
      "s_mov_b64 exec, -1\n\t"
      "v_add_f32 v33, v33, v35\n\t"
      "v_smfmac_f32_16x16x64_bf16 v[36:39], v[24:27], a[112:119], v122\n\t"
      "ds_read_b128 v[60:63], v127 offset:37888\n\t"
      "v_mul_f32_e64 v33, s59, v33\n\t"
      "v_smfmac_f32_16x16x64_bf16 v[36:39], v[28:31], a[120:127], v122\n\t"
      "v_add_f32_e64 v32, v32, v34\n\t"
      "v_smfmac_f32_16x16x64_bf16 v[44:47], v[20:23], a[232:239], v122\n\t"
      "ds_read_b128 v[56:59], v127 offset:38912\n\t"
      "v_mul_f32_e64 v32, s59, v32\n\t"
      "v_smfmac_f32_16x16x64_bf16 v[44:47], v[24:27], a[240:247], v122\n\t"
      "v_add_f32 v36, v36, v38\n\t"
      "v_add_f32 v37, v37, v39\n\t"
      "v_mul_f32_e64 v37, s59, v37\n\t"
      "v_smfmac_f32_16x16x64_bf16 v[40:43], v[24:27], a[176:183], v122\n\t"
      "ds_read_b128 v[92:95], v127 offset:34816\n\t"
      "v_cvt_pk_bf16_f32 v112, v32, v33\n\t"
      "v_smfmac_f32_16x16x64_bf16 v[44:47], v[28:31], a[248:255], v122\n\t"
      "ds_read_b128 v[96:99], v127 offset:33792\n\t"
      "v_mul_f32_e64 v36, s59, v36\n\t"
      "v_cvt_pk_bf16_f32 v113, v36, v37\n\t"
      "v_smfmac_f32_16x16x64_bf16 v[40:43], v[28:31], a[184:191], v122\n\t"
      "ds_write_b64 v125, v[112:113]\n\t"
      "v_smfmac_f32_16x16x64_bf16 v[48:51], v[4:7], v[136:143], v122\n\t"
      "s_nop 0\n\t"
      "v_add_f32 v45, v45, v47\n\t"
      "v_add_f32_e64 v44, v44, v46\n\t"
      "v_cvt_pk_bf16_f32 v121, v44, v45\n\t"
      "v_add_f32 v41, v41, v43\n\t"
      "v_add_f32 v40, v40, v42\n\t"
      "v_cvt_pk_bf16_f32 v120, v40, v41\n\t"
      "v_smfmac_f32_16x16x64_bf16 v[48:51], v[8:11], v[144:151], v122\n\t"
      "s_mov_b32 s58, 0x10000000\n\t"
      "ds_write_b64 v125, v[120:121] offset:2080\n\t"
      "v_smfmac_f32_16x16x64_bf16 v[48:51], v[12:15], v[152:159], v122\n\t"
      "s_waitcnt lgkmcnt(1)\n\t"
      "s_barrier\n\t"
      "ds_read_b64_tr_b16 v[36:37], v124 offset:50192\n\t"
      "v_smfmac_f32_16x16x64_bf16 v[48:51], v[16:19], v[160:167], v122\n\t"
      "ds_read_b32 v108, v109 offset:55392\n\t"
      "ds_read_b64_tr_b16 v[32:33], v124 offset:49152\n\t"
      "v_smfmac_f32_16x16x64_bf16 v[52:55], v[4:7], v[200:207], v122\n\t"
      "ds_read_b64_tr_b16 v[34:35], v124 offset:49672\n\t"
      "s_waitcnt vmcnt(0) lgkmcnt(4)\n\t"
      "ds_read_b64_tr_b16 v[38:39], v124 offset:50712\n\t"
      "v_smfmac_f32_16x16x64_bf16 v[48:51], v[20:23], v[168:175], v122\n\t"
      "s_barrier\n\t"
      "ds_read_b64_tr_b16 v[46:47], v124 offset:52792\n\t"
      "v_smfmac_f32_16x16x64_bf16 v[48:51], v[24:27], v[176:183], v122\n\t"
      "s_waitcnt lgkmcnt(4)\n\t"
      "ds_read_b64_tr_b16 v[44:45], v124 offset:52272\n\t"
      "v_readfirstlane_b32 s61, v108\n\t"
      "v_smfmac_f32_16x16x64_bf16 v[48:51], v[28:31], v[184:191], v122\n\t"
      "s_waitcnt lgkmcnt(3)\n\t"
      "ds_read_b64_tr_b16 v[42:43], v124 offset:51752\n\t"
      "ds_read_b64_tr_b16 v[40:41], v124 offset:51232\n\t"
      "s_waitcnt lgkmcnt(4)\n\t"
      "v_mfma_f32_16x16x32_bf16 v[112:115], v[76:79], v[32:35], 0\n\t"
      "s_lshr_b32 s61, s61, 23\n\t"
      "s_add_u32 s61, s61, s67\n\t"
      "v_mov_b32_dpp v102, v34 quad_perm:[2,3,0,1] row_mask:0xf bank_mask:0xf\n\t"
      "v_mov_b32_dpp v106, v38 quad_perm:[2,3,0,1] row_mask:0xf bank_mask:0xf\n\t"
      "s_max_u32 s61, s61, s66\n\t"
      "v_mfma_f32_16x16x32_bf16 v[116:119], v[80:83], v[32:35], 0\n\t"
      "s_lshr_b32 s61, s61, 1\n\t"
      "v_add_f32_e64 v48, v48, v50\n\t"
      "v_mov_b32_dpp v100, v32 quad_perm:[2,3,0,1] row_mask:0xf bank_mask:0xf\n\t"
      "v_mfma_f32_16x16x32_bf16 v[116:119], v[88:91], v[36:39], v[116:119]\n\t"
      "s_min_u32 s61, s61, 200\n\t"
      "v_mov_b32_dpp v107, v39 quad_perm:[2,3,0,1] row_mask:0xf bank_mask:0xf\n\t"
      "s_waitcnt lgkmcnt(0)\n\t"
      "v_mfma_f32_16x16x32_bf16 v[76:79], v[40:43], v[32:35], 0\n\t"
      "buffer_load_dwordx4 v127, s[36:39], s46 offen sc1 lds\n\t"
      "s_lshl_b32 s61, s61, 23\n\t"
      "v_mfma_f32_16x16x32_bf16 v[76:79], v[44:47], v[36:39], v[76:79]\n\t"
      "s_add_u32 s65, s61, 33554432\n\t"
      "v_mov_b32_dpp v104, v36 quad_perm:[2,3,0,1] row_mask:0xf bank_mask:0xf\n\t"
      "v_mov_b32_dpp v103, v35 quad_perm:[2,3,0,1] row_mask:0xf bank_mask:0xf\n\t"
      "v_mov_b32_dpp v101, v33 quad_perm:[2,3,0,1] row_mask:0xf bank_mask:0xf\n\t"
      "v_mfma_f32_16x16x32_bf16 v[112:115], v[84:87], v[36:39], v[112:115]\n\t"
      "s_pack_hh_b32_b16 s75, s65, s65\n\t"
      "v_add_f32 v49, v49, v51\n\t"
      "v_cvt_pk_bf16_f32 v64, v48, v49\n\t"
      "v_cndmask_b32_e64 v120, v76, v78, s[72:73]\n\t"
      "v_cndmask_b32_e64 v121, v77, v79, s[72:73]\n\t"
      "v_mov_b32_dpp v105, v37 quad_perm:[2,3,0,1] row_mask:0xf bank_mask:0xf\n\t"
      "v_mfma_f32_16x16x32_bf16 v[112:115], v[92:95], v[100:103], v[112:115]\n\t"
      "buffer_load_dwordx4 v127, s[36:39], s46 offen offset:1024 sc1 lds\n\t"
      "v_mfma_f32_16x16x32_bf16 v[116:119], v[96:99], v[100:103], v[116:119]\n\t"
      "s_pack_hh_b32_b16 s74, s65, s65\n\t"
      "v_add_f32_dpp v121, v121, v121 quad_perm:[2,3,0,1] row_mask:0xf bank_mask:0xf\n\t"
      "v_add_f32_dpp v120, v120, v120 quad_perm:[2,3,0,1] row_mask:0xf bank_mask:0xf\n\t"
      "v_mfma_f32_16x16x32_bf16 v[112:115], v[56:59], v[104:107], v[112:115]\n\t"
      "v_exp_f32 v81, v121\n\t"
      "v_mfma_f32_16x16x32_bf16 v[116:119], v[60:63], v[104:107], v[116:119]\n\t"
      "s_mov_b32 m0, s45\n\t"
      "v_mov_b64 v[90:91], s[74:75]\n\t"
      "v_exp_f32 v80, v120\n\t"
      "v_mov_b64 v[88:89], s[74:75]\n\t"
      "v_cvt_pk_bf16_f32 v108, v80, v81\n\t"
      "v_smfmac_f32_16x16x64_bf16 v[52:55], v[8:11], v[208:215], v122\n\t"
      "buffer_load_dwordx4 v65, s[36:39], s44 offen offset:1024 sc1 lds\n\t"
      "v_mfma_f32_16x16x32_bf16 v[84:87], v[108:111], v[64:67], 0\n\t"
      "s_add_u32 s57, s61, 310378496\n\t"
      "v_cndmask_b32_e64 v112, v112, v116, s[72:73]\n\t"
      "v_exp_f32_e64 v56, v112\n\t"
      "v_mfma_f32_16x16x32_bf16 v[48:51], v[108:111], v[88:91], 0\n\t"
      "s_add_u32 s43, s43, 0x8000\n\t"
      "v_cndmask_b32_e64 v113, v113, v117, s[72:73]\n\t"
      "v_cndmask_b32_e64 v114, v114, v118, s[72:73]\n\t"
      "v_exp_f32_e64 v58, v114\n\t"
      "v_cndmask_b32_e64 v115, v115, v119, s[72:73]\n\t"
      "v_exp_f32 v57, v113\n\t"
      "v_exp_f32 v59, v115\n\t"
      "v_cvt_pk_bf16_f32 v96, v56, v57\n\t"
      "v_cvt_pk_bf16_f32 v97, v58, v59\n\t"
      "v_max3_f32 v82, |v112|, |v113|, |v114|\n\t"
      "v_mov_b32_dpp v98, v96 quad_perm:[2,3,0,1] row_mask:0xf bank_mask:0xf\n\t"
      "v_mov_b32_dpp v99, v97 quad_perm:[2,3,0,1] row_mask:0xf bank_mask:0xf\n\t"
      "v_smfmac_f32_16x16x64_bf16 v[52:55], v[12:15], v[216:223], v122\n\t"
      "v_max3_f32 v83, |v115|, |v120|, |v121|\n\t"
      "v_mfma_f32_16x16x32_bf16 v[48:51], v[96:99], v[88:91], v[48:51]\n\t"
      "v_max_f32_e64 v82, v82, v83\n\t"
      "v_cmp_nge_f32 vcc, 0x42000000, v82\n\t"
      "v_mfma_f32_16x16x32_bf16 v[84:87], v[96:99], v[68:71], v[84:87]\n\t"
      "buffer_load_dwordx4 v65, s[36:39], s44 offen sc1 lds\n\t"
      "v_smfmac_f32_16x16x64_bf16 v[52:55], v[16:19], v[224:231], v122\n\t"
      "s_add_u32 s68, s68, 0x8000\n\t"
      "s_cbranch_vccnz LMX_0%=\n\t"
      "v_rcp_f32 v49, v49\n\t"
      "v_rcp_f32_e64 v48, v48\n\t"
      "v_smfmac_f32_16x16x64_bf16 v[52:55], v[20:23], v[232:239], v122\n\t"
      "v_fma_f32 v85, v85, v49, s96\n\t"
      "v_fma_f32 v84, v84, v48, s96\n\t"
      "v_smfmac_f32_16x16x64_bf16 v[52:55], v[24:27], v[240:247], v122\n\t"
      "buffer_atomic_add v84, v123, s[36:39], s97 offen\n\t"
      "buffer_atomic_add v85, v123, s[36:39], s98 offen\n\t"
      "v_smfmac_f32_16x16x64_bf16 v[52:55], v[28:31], v[248:255], v122\n\t"
      "v_mfma_f32_16x16x32_bf16 v[92:95], v[96:99], v[72:75], 0\n\t"
      "s_nop 3\n\t"
      "v_add_f32 v53, v53, v55\n\t"
      "v_add_f32_e64 v52, v52, v54\n\t"
      "v_cvt_pk_bf16_f32 v66, v52, v53\n\t"
      "s_add_u32 s62, s61, 0x83400000\n\t"
      "v_mov_b32_e64 v126, s62\n\t"
      "v_mfma_f32_16x16x32_bf16 v[92:95], v[108:111], v[66:69], v[92:95]\n\t"
      "s_add_u32 s97, s97, 0x8000\n\t"
      "s_nop 6\n\t"
      "v_fma_f32 v95, v95, v49, s96\n\t"
      "buffer_atomic_add v95, v123, s[36:39], s23 offen\n\t"
      "v_fma_f32 v94, v94, v48, s96\n\t"
      "buffer_atomic_add v94, v123, s[36:39], s69 offen\n\t"
      "LWIN_0%=:\n\t"
      "v_add_u32 v100, s55, v127\n\t"
      "ds_read_b128 v[60:63], v100 offset:57344\n\t"
      "s_cmp_eq_u32 s56, 0\n\t"
      "s_cbranch_scc0 LSLOW_0%=\n\t"
      "LWIN1_0%=:\n\t"
      "buffer_load_dwordx2 v[120:121], off, s[36:39], s50 sc1\n\t"
      "s_mov_b32 s64, s50\n\t"
      "s_bitcmp1_b64 s[84:85], s40\n\t"
      "s_cbranch_scc1 LEARLY_0%=\n\t"
      "LROLE_0%=:\n\t"
      "v_add_u32 v52, 65536, v127\n\t"
      "ds_read_b128 v[88:91], v52\n\t"
      "ds_read_b128 v[92:95], v52 offset:1024\n\t"
      "ds_read_b128 v[32:35], v52 offset:4096\n\t"
      "ds_read_b128 v[36:39], v52 offset:5120\n\t"
      "ds_read_b128 v[40:43], v52 offset:8192\n\t"
      "ds_read_b128 v[44:47], v52 offset:9216\n\t"
      "v_mov_b64 v[48:49], 0\n\t"
      "v_mov_b64 v[50:51], 0\n\t"
      "v_add_u32 v104, s71, v100\n\t"
      "ds_read_b128 v[76:79], v104\n\t"
      "ds_read_b128 v[80:83], v104 offset:1024\n\t"
      "ds_read_b128 v[112:115], v100\n\t"
      "ds_read_b128 v[116:119], v100 offset:1024\n\t"
      "v_xor_b32 v101, 2048, v104\n\t"
      "v_xor_b32 v102, 4096, v104\n\t"
      "v_xor_b32 v103, 6144, v104\n\t"
      "s_waitcnt lgkmcnt(8)\n\t"
      "v_smfmac_f32_16x16x64_bf16 v[48:51], v[0:3], v[88:95], v122\n\t"
      "ds_read_b128 v[88:91], v52 offset:12288\n\t"
      "ds_read_b128 v[92:95], v52 offset:13312\n\t"
      "s_add_u32 m0, s55, s71\n\t"
      "s_sub_u32 s71, 204800, s71\n\t"
      "s_waitcnt lgkmcnt(8)\n\t"
      "v_smfmac_f32_16x16x64_bf16 v[48:51], v[4:7], v[32:39], v122\n\t"
      "ds_read_b128 v[16:19], v101\n\t"
      "ds_read_b128 v[20:23], v101 offset:1024\n\t"
      "s_add_u32 s40, s40, 1\n\t"
      "s_cmp_lt_u32 s40, 63\n\t"
      "s_waitcnt lgkmcnt(8)\n\t"
      "v_smfmac_f32_16x16x64_bf16 v[48:51], v[8:11], v[40:47], v122\n\t"
      "ds_read_b128 v[24:27], v100 offset:8192\n\t"
      "ds_read_b128 v[28:31], v100 offset:9216\n\t"
      "s_cselect_b32 s61, 786432, 0\n\t"
      "s_cselect_b32 s60, s58, 0\n\t"
      "s_waitcnt lgkmcnt(4)\n\t"
      "v_smfmac_f32_16x16x64_bf16 v[48:51], v[12:15], v[88:95], v122\n\t"
      "s_add_u32 s44, s44, s61\n\t"
      "s_cmp_lt_u32 s40, 62\n\t"
      "ds_read_b128 v[0:3], v102\n\t"
      "ds_read_b128 v[4:7], v102 offset:1024\n\t"
      "ds_read_b128 v[8:11], v100 offset:16384\n\t"
      "ds_read_b128 v[12:15], v100 offset:17408\n\t"
      "s_cselect_b32 s61, 786432, 0\n\t"
      "s_add_u32 s46, s46, s61\n\t"
      "s_add_u32 s61, s40, 3\n\t"
      "s_and_b32 s62, s61, 3\n\t"
      "s_cselect_b32 s62, 16, 2000\n\t"
      "s_cmp_lt_u32 s61, 64\n\t"
      "s_cselect_b32 s62, s62, 0\n\t"
      "s_add_u32 s50, s50, s62\n\t"
      "s_mov_b32 s28, s49\n\t"
      "s_add_u32 s49, s49, 65536\n\t"
      "v_add_f32 v48, v48, v50\n\t"
      "v_add_f32 v49, v49, v51\n\t"
      "v_mfma_f32_16x16x32_bf16 v[32:35], v[76:79], v[112:115], 0\n\t"
      "s_mov_b32 s63, 0x2000\n\t"
      "v_mfma_f32_16x16x32_bf16 v[36:39], v[80:83], v[112:115], 0\n\t"
      "v_mfma_f32_16x16x32_bf16 v[40:43], v[76:79], v[116:119], 0\n\t"
      "v_mfma_f32_16x16x32_bf16 v[44:47], v[80:83], v[116:119], 0\n\t"
      "ds_read_b128 v[76:79], v103\n\t"
      "ds_read_b128 v[80:83], v103 offset:1024\n\t"
      "ds_read_b128 v[112:115], v100 offset:24576\n\t"
      "ds_read_b128 v[116:119], v100 offset:25600\n\t"
      "s_waitcnt lgkmcnt(8)\n\t"
      "v_mfma_f32_16x16x32_bf16 v[32:35], v[16:19], v[24:27], v[32:35]\n\t"
      "v_mfma_f32_16x16x32_bf16 v[36:39], v[20:23], v[24:27], v[36:39]\n\t"
      "v_mfma_f32_16x16x32_bf16 v[40:43], v[16:19], v[28:31], v[40:43]\n\t"
      "v_mfma_f32_16x16x32_bf16 v[44:47], v[20:23], v[28:31], v[44:47]\n\t"
      "LGATE_0%=:\n\t"
      "LBAR_0%=:\n\t"
      "s_cmp_gt_u32 s40, 1\n\t"
      "s_barrier\n\t"
      "s_cbranch_scc1 LPOLL_0%=\n\t"
      "s_branch LFIRST_0%=\n\t"
      ".p2align 6\n\t.fill 16, 4, 0xbf800000\n\t"
      "LPOLL_1%=:\n\t"
      "buffer_load_dwordx4 v[84:87], v60, s[36:39], s43 offen sc1\n\t"
      "buffer_load_dwordx4 v[88:91], v60, s[36:39], s43 offen offset:1024 sc1\n\t"
      "s_waitcnt lgkmcnt(4)\n\t"
      "v_mfma_f32_16x16x32_bf16 v[32:35], v[0:3], v[8:11], v[32:35]\n\t"
      "v_mfma_f32_16x16x32_bf16 v[36:39], v[4:7], v[8:11], v[36:39]\n\t"
      "v_mfma_f32_16x16x32_bf16 v[40:43], v[0:3], v[12:15], v[40:43]\n\t"
      "v_mfma_f32_16x16x32_bf16 v[44:47], v[4:7], v[12:15], v[44:47]\n\t"
      "buffer_load_dwordx4 v[92:95], v60, s[36:39], s68 offen sc1\n\t"
      "buffer_load_dwordx4 v[96:99], v60, s[36:39], s68 offen offset:1024 sc1\n\t"
      "ds_read_b64 v[54:55], v124 offset:53832\n\t"
      "s_xor_b32 s69, s97, 1024\n\t"
      "s_xor_b32 s98, s97, 512\n\t"
      "s_xor_b32 s23, s97, 1536\n\t"
      "s_waitcnt lgkmcnt(1)\n\t"
      "v_mfma_f32_16x16x32_bf16 v[32:35], v[76:79], v[112:115], v[32:35]\n\t"
      "v_mfma_f32_16x16x32_bf16 v[36:39], v[80:83], v[112:115], v[36:39]\n\t"
      "v_mfma_f32_16x16x32_bf16 v[40:43], v[76:79], v[116:119], v[40:43]\n\t"
      "v_mfma_f32_16x16x32_bf16 v[44:47], v[80:83], v[116:119], v[44:47]\n\t"
      "s_waitcnt lgkmcnt(0)\n\t"
      "v_add_f32 v48, v48, v54\n\t"
      "v_add_f32_e64 v49, v49, v55\n\t"
      "v_cvt_pk_bf16_f32 v48, v48, v49\n\t"
      "buffer_store_dword v48, v62, s[36:39], s28 offen sc1\n\t"
      "s_mov_b64 exec, 1\n\t"
      "ds_write_b64 v109, v[110:111] offset:55392\n\t"
      "s_mov_b64 exec, -1\n\t"
      "s_nop 4\n\t"
      "v_cvt_pk_bf16_f32 v68, v32, v33\n\t"
      "v_cvt_pk_bf16_f32 v69, v34, v35\n\t"
      "v_cvt_pk_bf16_f32 v70, v36, v37\n\t"
      "v_cvt_pk_bf16_f32 v71, v38, v39\n\t"
      "v_cvt_pk_bf16_f32 v74, v40, v41\n\t"
      "v_cvt_pk_bf16_f32 v75, v42, v43\n\t"
      "v_cvt_pk_bf16_f32 v72, v44, v45\n\t"
      "v_cvt_pk_bf16_f32 v73, v46, v47\n\t"
      "v_mov_b64 v[32:33], 0\n\t"
      "v_mov_b64 v[34:35], 0\n\t"
      "v_mov_b64 v[36:37], 0\n\t"
      "v_mov_b64 v[38:39], 0\n\t"
      "v_mov_b64 v[40:41], 0\n\t"
      "v_mov_b64 v[42:43], 0\n\t"
      "v_mov_b64 v[44:45], 0\n\t"
      "v_mov_b64 v[46:47], 0\n\t"
      "v_mov_b64 v[48:49], 0\n\t"
      "v_mov_b64 v[50:51], 0\n\t"
      "v_mov_b64 v[52:53], 0\n\t"
      "v_mov_b64 v[54:55], 0\n\t"
      "LGO_1%=:\n\t"
      "s_nop 0\n\t"
      "s_nop 0\n\t"
      "s_nop 0\n\t"
      "s_nop 0\n\t"
      "s_waitcnt vmcnt(4)\n\t"
      "v_pk_fma_f32 v[2:3], v[86:87], s[56:57], v[126:127] op_sel:[0,1,0] op_sel_hi:[1,1,0]\n\t"
      "v_pk_fma_f32 v[0:1], v[84:85], s[56:57], v[126:127] op_sel:[0,1,0] op_sel_hi:[1,1,0]\n\t"
      "v_cvt_pk_bf16_f32 v4, v0, v1\n\t"
      "v_cvt_pk_bf16_f32 v5, v2, v3\n\t"
      "s_nop 0\n\t"
      "ds_write_b64 v127, v[4:5] offset:41984\n\t"
      "s_waitcnt vmcnt(3)\n\t"
      "v_pk_fma_f32 v[12:13], v[88:89], s[56:57], v[126:127] op_sel:[0,1,0] op_sel_hi:[1,1,0]\n\t"
      "v_pk_fma_f32 v[14:15], v[90:91], s[56:57], v[126:127] op_sel:[0,1,0] op_sel_hi:[1,1,0]\n\t"
      "v_cvt_pk_bf16_f32 v6, v12, v13\n\t"
      "v_cvt_pk_bf16_f32 v7, v14, v15\n\t"
      "ds_write_b64 v127, v[6:7] offset:41992\n\t"
      "v_bitop3_b32 v64, v84, v85, v86 bitop3:0x80\n\t"
      "v_mfma_f32_16x16x32_bf16 v[56:59], v[4:7], v[4:7], 0\n\t"
      "v_bitop3_b32 v64, v64, v87, v88 bitop3:0x80\n\t"
      "v_bitop3_b32 v64, v64, v89, v90 bitop3:0x80\n\t"
      "v_bitop3_b32 v64, v64, v91, s58 bitop3:0x80\n\t"
      "v_cmp_eq_u32_e64 vcc, 0, v64\n\t"
      "v_smfmac_f32_16x16x64_bf16 v[36:39], v[4:7], a[72:79], v122\n\t"
      "s_nop 0\n\t"
      "v_bitop3_b32 v120, v120, v121, s53 bitop3:0x7e\n\t"
      "v_smfmac_f32_16x16x64_bf16 v[44:47], v[4:7], a[200:207], v122\n\t"
      "s_cbranch_vccnz LR0_1%=\n\t"
      "LC0_1%=:\n\t"
      "s_waitcnt lgkmcnt(0)\n\t"
      "s_barrier\n\t"
      "ds_read_b128 v[0:3], v127 offset:40960\n\t"
      "v_smfmac_f32_16x16x64_bf16 v[52:55], v[4:7], v[200:207], v122\n\t"
      "ds_read_b128 v[12:15], v127 offset:44032\n\t"
      "v_smfmac_f32_16x16x64_bf16 v[48:51], v[4:7], v[136:143], v122\n\t"
      "ds_read_b128 v[8:11], v127 offset:43008\n\t"
      "v_smfmac_f32_16x16x64_bf16 v[32:35], v[4:7], a[8:15], v122\n\t"
      "ds_read_b128 v[84:87], v127 offset:36864\n\t"
      "v_smfmac_f32_16x16x64_bf16 v[40:43], v[4:7], a[136:143], v122\n\t"
      "s_waitcnt lgkmcnt(3)\n\t"
      "v_smfmac_f32_16x16x64_bf16 v[32:35], v[0:3], a[0:7], v122\n\t"
      "ds_read_b128 v[80:83], v127 offset:35840\n\t"
      "v_smfmac_f32_16x16x64_bf16 v[40:43], v[0:3], a[128:135], v122\n\t"
      "s_waitcnt vmcnt(2)\n\t"
      "v_fma_f32 v19, v95, s57, v126\n\t"
      "v_fma_f32 v16, v92, s57, v126\n\t"
      "v_smfmac_f32_16x16x64_bf16 v[36:39], v[0:3], a[64:71], v122\n\t"
      "v_fma_f32 v18, v94, s57, v126\n\t"
      "v_cvt_pk_bf16_f32 v21, v18, v19\n\t"
      "v_bitop3_b32 v64, v92, v93, v94 bitop3:0x80\n\t"
      "v_smfmac_f32_16x16x64_bf16 v[44:47], v[0:3], a[192:199], v122\n\t"
      "s_waitcnt lgkmcnt(2)\n\t"
      "v_fma_f32 v17, v93, s57, v126\n\t"
      "v_cvt_pk_bf16_f32 v20, v16, v17\n\t"
      "v_smfmac_f32_16x16x64_bf16 v[36:39], v[12:15], a[88:95], v122\n\t"
      "s_waitcnt vmcnt(1)\n\t"
      "ds_write_b64 v127, v[20:21] offset:46080\n\t"
      "v_fma_f32 v31, v99, s57, v126\n\t"
      "v_fma_f32 v30, v98, s57, v126\n\t"
      "v_cvt_pk_bf16_f32 v23, v30, v31\n\t"
      "v_fma_f32 v29, v97, s57, v126\n\t"
      "v_fma_f32 v28, v96, s57, v126\n\t"
      "v_cvt_pk_bf16_f32 v22, v28, v29\n\t"
      "v_smfmac_f32_16x16x64_bf16 v[44:47], v[12:15], a[216:223], v122\n\t"
      "ds_write_b64 v127, v[22:23] offset:46088\n\t"
      "v_smfmac_f32_16x16x64_bf16 v[40:43], v[12:15], a[152:159], v122\n\t"
      "ds_read_b128 v[88:91], v127 offset:39936\n\t"
      "v_bitop3_b32 v64, v64, v95, v96 bitop3:0x80\n\t"
      "v_bitop3_b32 v64, v64, v97, v98 bitop3:0x80\n\t"
      "v_smfmac_f32_16x16x64_bf16 v[40:43], v[8:11], a[144:151], v122\n\t"
      "v_bitop3_b32 v64, v64, v99, s60 bitop3:0x80\n\t"
      "v_smfmac_f32_16x16x64_bf16 v[36:39], v[8:11], a[80:87], v122\n\t"
      "v_cmp_eq_u32_e64 vcc, 0, v64\n\t"
      "v_smfmac_f32_16x16x64_bf16 v[44:47], v[8:11], a[208:215], v122\n\t"
      "s_waitcnt lgkmcnt(1)\n\t"
      "v_smfmac_f32_16x16x64_bf16 v[32:35], v[12:15], a[24:31], v122\n\t"
      "s_cbranch_vccnz LR1_1%=\n\t"
      "LC1_1%=:\n\t"
      "v_smfmac_f32_16x16x64_bf16 v[40:43], v[20:23], a[168:175], v122\n\t"
      "s_barrier\n\t"
      "v_mfma_f32_16x16x32_bf16 v[56:59], v[20:23], v[20:23], v[56:59]\n\t"
      "ds_read_b128 v[16:19], v127 offset:45056\n\t"
      "s_nop 0\n\t"
      "v_smfmac_f32_16x16x64_bf16 v[32:35], v[8:11], a[16:23], v122\n\t"
      "ds_read_b128 v[28:31], v127 offset:48128\n\t"
      "v_smfmac_f32_16x16x64_bf16 v[44:47], v[20:23], a[232:239], v122\n\t"
      "ds_read_b128 v[24:27], v127 offset:47104\n\t"
      "v_max3_f32 v56, v56, v57, v58\n\t"
      "v_smfmac_f32_16x16x64_bf16 v[36:39], v[20:23], a[104:111], v122\n\t"
      "ds_read_b128 v[76:79], v127 offset:32768\n\t"
      "v_max_f32_e64 v56, v56, v59\n\t"
      "v_smfmac_f32_16x16x64_bf16 v[32:35], v[20:23], a[40:47], v122\n\t"
      "s_waitcnt lgkmcnt(3)\n\t"
      "v_readfirstlane_b32 s56, v120\n\t"
      "v_smfmac_f32_16x16x64_bf16 v[32:35], v[16:19], a[32:39], v122\n\t"
      "s_waitcnt lgkmcnt(2)\n\t"
      "v_max_f32_dpp v56, v56, v56 quad_perm:[1,0,3,2] row_mask:0xf bank_mask:0xf\n\t"
      "v_smfmac_f32_16x16x64_bf16 v[32:35], v[28:31], a[56:63], v122\n\t"
      "s_waitcnt lgkmcnt(1)\n\t"
      "v_max_f32_dpp v56, v56, v56 quad_perm:[2,3,0,1] row_mask:0xf bank_mask:0xf\n\t"
      "v_smfmac_f32_16x16x64_bf16 v[32:35], v[24:27], a[48:55], v122\n\t"
      "s_mov_b32 exec_lo, 0x100001\n\t"
      "s_mov_b32 exec_hi, 0x10000100\n\t"
      "v_smfmac_f32_16x16x64_bf16 v[36:39], v[16:19], a[96:103], v122\n\t"
      "ds_max_i32 v109, v56 offset:55392\n\t"
      "v_smfmac_f32_16x16x64_bf16 v[40:43], v[16:19], a[160:167], v122\n\t"
      "s_mov_b64 exec, -1\n\t"
      "v_add_f32 v33, v33, v35\n\t"
      "v_smfmac_f32_16x16x64_bf16 v[36:39], v[28:31], a[120:127], v122\n\t"
      "ds_read_b128 v[60:63], v127 offset:37888\n\t"
      "v_mul_f32_e64 v33, s59, v33\n\t"
      "v_smfmac_f32_16x16x64_bf16 v[36:39], v[24:27], a[112:119], v122\n\t"
      "v_add_f32_e64 v32, v32, v34\n\t"
      "v_smfmac_f32_16x16x64_bf16 v[44:47], v[16:19], a[224:231], v122\n\t"
      "ds_read_b128 v[56:59], v127 offset:38912\n\t"
      "v_mul_f32_e64 v32, s59, v32\n\t"
      "v_smfmac_f32_16x16x64_bf16 v[44:47], v[28:31], a[248:255], v122\n\t"
      "v_add_f32 v36, v36, v38\n\t"
      "v_add_f32 v37, v37, v39\n\t"
      "v_mul_f32_e64 v37, s59, v37\n\t"
      "v_smfmac_f32_16x16x64_bf16 v[40:43], v[28:31], a[184:191], v122\n\t"
      "ds_read_b128 v[92:95], v127 offset:34816\n\t"
      "v_cvt_pk_bf16_f32 v112, v32, v33\n\t"
      "v_smfmac_f32_16x16x64_bf16 v[44:47], v[24:27], a[240:247], v122\n\t"
      "ds_read_b128 v[96:99], v127 offset:33792\n\t"
      "v_mul_f32_e64 v36, s59, v36\n\t"
      "v_cvt_pk_bf16_f32 v113, v36, v37\n\t"
      "v_smfmac_f32_16x16x64_bf16 v[40:43], v[24:27], a[176:183], v122\n\t"
      "ds_write_b64 v125, v[112:113]\n\t"
      "v_smfmac_f32_16x16x64_bf16 v[48:51], v[0:3], v[128:135], v122\n\t"
      "s_nop 0\n\t"
      "v_add_f32 v45, v45, v47\n\t"
      "v_add_f32_e64 v44, v44, v46\n\t"
      "v_cvt_pk_bf16_f32 v121, v44, v45\n\t"
      "v_add_f32 v41, v41, v43\n\t"
      "v_add_f32 v40, v40, v42\n\t"
      "v_cvt_pk_bf16_f32 v120, v40, v41\n\t"
      "v_smfmac_f32_16x16x64_bf16 v[48:51], v[12:15], v[152:159], v122\n\t"
      "s_mov_b32 s58, 0x10000000\n\t"
      "ds_write_b64 v125, v[120:121] offset:2080\n\t"
      "v_smfmac_f32_16x16x64_bf16 v[48:51], v[8:11], v[144:151], v122\n\t"
      "s_waitcnt lgkmcnt(1)\n\t"
      "s_barrier\n\t"
      "ds_read_b64_tr_b16 v[36:37], v124 offset:50192\n\t"
      "v_smfmac_f32_16x16x64_bf16 v[48:51], v[20:23], v[168:175], v122\n\t"
      "ds_read_b32 v108, v109 offset:55392\n\t"
      "ds_read_b64_tr_b16 v[32:33], v124 offset:49152\n\t"
      "v_smfmac_f32_16x16x64_bf16 v[52:55], v[0:3], v[192:199], v122\n\t"
      "ds_read_b64_tr_b16 v[34:35], v124 offset:49672\n\t"
      "s_waitcnt vmcnt(0) lgkmcnt(4)\n\t"
      "ds_read_b64_tr_b16 v[38:39], v124 offset:50712\n\t"
      "v_smfmac_f32_16x16x64_bf16 v[48:51], v[16:19], v[160:167], v122\n\t"
      "s_barrier\n\t"
      "ds_read_b64_tr_b16 v[46:47], v124 offset:52792\n\t"
      "v_smfmac_f32_16x16x64_bf16 v[48:51], v[28:31], v[184:191], v122\n\t"
      "s_waitcnt lgkmcnt(4)\n\t"
      "ds_read_b64_tr_b16 v[44:45], v124 offset:52272\n\t"
      "v_readfirstlane_b32 s61, v108\n\t"
      "v_smfmac_f32_16x16x64_bf16 v[48:51], v[24:27], v[176:183], v122\n\t"
      "s_waitcnt lgkmcnt(3)\n\t"
      "ds_read_b64_tr_b16 v[42:43], v124 offset:51752\n\t"
      "ds_read_b64_tr_b16 v[40:41], v124 offset:51232\n\t"
      "s_waitcnt lgkmcnt(4)\n\t"
      "v_mfma_f32_16x16x32_bf16 v[112:115], v[76:79], v[32:35], 0\n\t"
      "s_lshr_b32 s61, s61, 23\n\t"
      "s_add_u32 s61, s61, s67\n\t"
      "v_mov_b32_dpp v102, v34 quad_perm:[2,3,0,1] row_mask:0xf bank_mask:0xf\n\t"
      "v_mov_b32_dpp v106, v38 quad_perm:[2,3,0,1] row_mask:0xf bank_mask:0xf\n\t"
      "s_max_u32 s61, s61, s66\n\t"
      "v_mfma_f32_16x16x32_bf16 v[116:119], v[80:83], v[32:35], 0\n\t"
      "s_lshr_b32 s61, s61, 1\n\t"
      "v_add_f32_e64 v48, v48, v50\n\t"
      "v_mov_b32_dpp v100, v32 quad_perm:[2,3,0,1] row_mask:0xf bank_mask:0xf\n\t"
      "v_mfma_f32_16x16x32_bf16 v[116:119], v[88:91], v[36:39], v[116:119]\n\t"
      "s_min_u32 s61, s61, 200\n\t"
      "v_mov_b32_dpp v107, v39 quad_perm:[2,3,0,1] row_mask:0xf bank_mask:0xf\n\t"
      "s_waitcnt lgkmcnt(0)\n\t"
      "v_mfma_f32_16x16x32_bf16 v[76:79], v[40:43], v[32:35], 0\n\t"
      "buffer_load_dwordx4 v127, s[36:39], s46 offen sc1 lds\n\t"
      "s_lshl_b32 s61, s61, 23\n\t"
      "v_mfma_f32_16x16x32_bf16 v[76:79], v[44:47], v[36:39], v[76:79]\n\t"
      "s_add_u32 s65, s61, 33554432\n\t"
      "v_mov_b32_dpp v104, v36 quad_perm:[2,3,0,1] row_mask:0xf bank_mask:0xf\n\t"
      "v_mov_b32_dpp v103, v35 quad_perm:[2,3,0,1] row_mask:0xf bank_mask:0xf\n\t"
      "v_mov_b32_dpp v101, v33 quad_perm:[2,3,0,1] row_mask:0xf bank_mask:0xf\n\t"
      "v_mfma_f32_16x16x32_bf16 v[112:115], v[84:87], v[36:39], v[112:115]\n\t"
      "s_pack_hh_b32_b16 s75, s65, s65\n\t"
      "v_add_f32 v49, v49, v51\n\t"
      "v_cvt_pk_bf16_f32 v64, v48, v49\n\t"
      "v_cndmask_b32_e64 v120, v76, v78, s[72:73]\n\t"
      "v_cndmask_b32_e64 v121, v77, v79, s[72:73]\n\t"
      "v_mov_b32_dpp v105, v37 quad_perm:[2,3,0,1] row_mask:0xf bank_mask:0xf\n\t"
      "v_mfma_f32_16x16x32_bf16 v[112:115], v[92:95], v[100:103], v[112:115]\n\t"
      "buffer_load_dwordx4 v127, s[36:39], s46 offen offset:1024 sc1 lds\n\t"
      "v_mfma_f32_16x16x32_bf16 v[116:119], v[96:99], v[100:103], v[116:119]\n\t"
      "s_pack_hh_b32_b16 s74, s65, s65\n\t"
      "v_add_f32_dpp v121, v121, v121 quad_perm:[2,3,0,1] row_mask:0xf bank_mask:0xf\n\t"
      "v_add_f32_dpp v120, v120, v120 quad_perm:[2,3,0,1] row_mask:0xf bank_mask:0xf\n\t"
      "v_mfma_f32_16x16x32_bf16 v[112:115], v[56:59], v[104:107], v[112:115]\n\t"
      "v_exp_f32 v81, v121\n\t"
      "v_mfma_f32_16x16x32_bf16 v[116:119], v[60:63], v[104:107], v[116:119]\n\t"
      "s_mov_b32 m0, s45\n\t"
      "v_mov_b64 v[90:91], s[74:75]\n\t"
      "v_exp_f32 v80, v120\n\t"
      "v_mov_b64 v[88:89], s[74:75]\n\t"
      "v_cvt_pk_bf16_f32 v108, v80, v81\n\t"
      "v_smfmac_f32_16x16x64_bf16 v[52:55], v[12:15], v[216:223], v122\n\t"
      "buffer_load_dwordx4 v65, s[36:39], s44 offen offset:1024 sc1 lds\n\t"
      "v_mfma_f32_16x16x32_bf16 v[84:87], v[108:111], v[64:67], 0\n\t"
      "s_add_u32 s57, s61, 310378496\n\t"
      "v_cndmask_b32_e64 v112, v112, v116, s[72:73]\n\t"
      "v_exp_f32_e64 v56, v112\n\t"
      "v_mfma_f32_16x16x32_bf16 v[48:51], v[108:111], v[88:91], 0\n\t"
      "s_add_u32 s43, s43, 0x8000\n\t"
      "v_cndmask_b32_e64 v113, v113, v117, s[72:73]\n\t"
      "v_cndmask_b32_e64 v114, v114, v118, s[72:73]\n\t"
      "v_exp_f32_e64 v58, v114\n\t"
      "v_cndmask_b32_e64 v115, v115, v119, s[72:73]\n\t"
      "v_exp_f32 v57, v113\n\t"
      "v_exp_f32 v59, v115\n\t"
      "v_cvt_pk_bf16_f32 v96, v56, v57\n\t"
      "v_cvt_pk_bf16_f32 v97, v58, v59\n\t"
      "v_max3_f32 v82, |v112|, |v113|, |v114|\n\t"
      "v_mov_b32_dpp v98, v96 quad_perm:[2,3,0,1] row_mask:0xf bank_mask:0xf\n\t"
      "v_mov_b32_dpp v99, v97 quad_perm:[2,3,0,1] row_mask:0xf bank_mask:0xf\n\t"
      "v_smfmac_f32_16x16x64_bf16 v[52:55], v[8:11], v[208:215], v122\n\t"
      "v_max3_f32 v83, |v115|, |v120|, |v121|\n\t"
      "v_mfma_f32_16x16x32_bf16 v[48:51], v[96:99], v[88:91], v[48:51]\n\t"
      "v_max_f32_e64 v82, v82, v83\n\t"
      "v_cmp_nge_f32 vcc, 0x42000000, v82\n\t"
      "v_mfma_f32_16x16x32_bf16 v[84:87], v[96:99], v[68:71], v[84:87]\n\t"
      "buffer_load_dwordx4 v65, s[36:39], s44 offen sc1 lds\n\t"
      "v_smfmac_f32_16x16x64_bf16 v[52:55], v[20:23], v[232:239], v122\n\t"
      "s_add_u32 s68, s68, 0x8000\n\t"
      "s_cbranch_vccnz LMX_1%=\n\t"
      "v_rcp_f32 v49, v49\n\t"
      "v_rcp_f32_e64 v48, v48\n\t"
      "v_smfmac_f32_16x16x64_bf16 v[52:55], v[16:19], v[224:231], v122\n\t"
      "v_fma_f32 v85, v85, v49, s96\n\t"
      "v_fma_f32 v84, v84, v48, s96\n\t"
      "v_smfmac_f32_16x16x64_bf16 v[52:55], v[28:31], v[248:255], v122\n\t"
      "buffer_atomic_add v84, v123, s[36:39], s97 offen\n\t"
      "buffer_atomic_add v85, v123, s[36:39], s98 offen\n\t"
      "v_smfmac_f32_16x16x64_bf16 v[52:55], v[24:27], v[240:247], v122\n\t"
      "v_mfma_f32_16x16x32_bf16 v[92:95], v[96:99], v[72:75], 0\n\t"
      "s_nop 3\n\t"
      "v_add_f32 v53, v53, v55\n\t"
      "v_add_f32_e64 v52, v52, v54\n\t"
      "v_cvt_pk_bf16_f32 v66, v52, v53\n\t"
      "s_add_u32 s62, s61, 0x83400000\n\t"
      "v_mov_b32_e64 v126, s62\n\t"
      "v_mfma_f32_16x16x32_bf16 v[92:95], v[108:111], v[66:69], v[92:95]\n\t"
      "s_add_u32 s97, s97, 0x8000\n\t"
      "s_nop 6\n\t"
      "v_fma_f32 v95, v95, v49, s96\n\t"
      "buffer_atomic_add v95, v123, s[36:39], s23 offen\n\t"
      "v_fma_f32 v94, v94, v48, s96\n\t"
      "buffer_atomic_add v94, v123, s[36:39], s69 offen\n\t"
      "LWIN_1%=:\n\t"
      "v_add_u32 v100, s55, v127\n\t"
      "ds_read_b128 v[60:63], v100 offset:57344\n\t"
      "s_cmp_eq_u32 s56, 0\n\t"
      "s_cbranch_scc0 LSLOW_1%=\n\t"
      "LWIN1_1%=:\n\t"
      "buffer_load_dwordx2 v[120:121], off, s[36:39], s50 sc1\n\t"
      "s_mov_b32 s64, s50\n\t"
      "s_bitcmp1_b64 s[84:85], s40\n\t"
      "s_cbranch_scc1 LEARLY_1%=\n\t"
      "LROLE_1%=:\n\t"
      "v_add_u32 v52, 67584, v127\n\t"
      "ds_read_b128 v[88:91], v52\n\t"
      "ds_read_b128 v[92:95], v52 offset:1024\n\t"
      "ds_read_b128 v[32:35], v52 offset:4096\n\t"
      "ds_read_b128 v[36:39], v52 offset:5120\n\t"
      "ds_read_b128 v[40:43], v52 offset:8192\n\t"
      "ds_read_b128 v[44:47], v52 offset:9216\n\t"
      "v_mov_b64 v[48:49], 0\n\t"
      "v_mov_b64 v[50:51], 0\n\t"
      "v_add_u32 v104, s71, v100\n\t"
      "ds_read_b128 v[76:79], v104\n\t"
      "ds_read_b128 v[80:83], v104 offset:1024\n\t"
      "ds_read_b128 v[112:115], v100\n\t"
      "ds_read_b128 v[116:119], v100 offset:1024\n\t"
      "v_xor_b32 v101, 2048, v104\n\t"
      "v_xor_b32 v102, 4096, v104\n\t"
      "v_xor_b32 v103, 6144, v104\n\t"
      "s_waitcnt lgkmcnt(8)\n\t"
      "v_smfmac_f32_16x16x64_bf16 v[48:51], v[0:3], v[88:95], v122\n\t"
      "ds_read_b128 v[88:91], v52 offset:12288\n\t"
      "ds_read_b128 v[92:95], v52 offset:13312\n\t"
      "s_add_u32 m0, s55, s71\n\t"
      "s_sub_u32 s71, 204800, s71\n\t"
      "s_waitcnt lgkmcnt(8)\n\t"
      "v_smfmac_f32_16x16x64_bf16 v[48:51], v[4:7], v[32:39], v122\n\t"
      "ds_read_b128 v[16:19], v101\n\t"
      "ds_read_b128 v[20:23], v101 offset:1024\n\t"
      "s_add_u32 s40, s40, 1\n\t"
      "s_cmp_lt_u32 s40, 63\n\t"
      "s_waitcnt lgkmcnt(8)\n\t"
      "v_smfmac_f32_16x16x64_bf16 v[48:51], v[8:11], v[40:47], v122\n\t"
      "ds_read_b128 v[24:27], v100 offset:8192\n\t"
      "ds_read_b128 v[28:31], v100 offset:9216\n\t"
      "s_cselect_b32 s61, 786432, 0\n\t"
      "s_cselect_b32 s60, s58, 0\n\t"
      "s_waitcnt lgkmcnt(4)\n\t"
      "v_smfmac_f32_16x16x64_bf16 v[48:51], v[12:15], v[88:95], v122\n\t"
      "s_add_u32 s44, s44, s61\n\t"
      "s_cmp_lt_u32 s40, 62\n\t"
      "ds_read_b128 v[0:3], v102\n\t"
      "ds_read_b128 v[4:7], v102 offset:1024\n\t"
      "ds_read_b128 v[8:11], v100 offset:16384\n\t"
      "ds_read_b128 v[12:15], v100 offset:17408\n\t"
      "s_cselect_b32 s61, 786432, 0\n\t"
      "s_add_u32 s46, s46, s61\n\t"
      "s_add_u32 s61, s40, 3\n\t"
      "s_and_b32 s62, s61, 3\n\t"
      "s_cselect_b32 s62, 16, 2000\n\t"
      "s_cmp_lt_u32 s61, 64\n\t"
      "s_cselect_b32 s62, s62, 0\n\t"
      "s_add_u32 s50, s50, s62\n\t"
      "s_mov_b32 s28, s49\n\t"
      "s_add_u32 s49, s49, 65536\n\t"
      "v_add_f32 v48, v48, v50\n\t"
      "v_add_f32 v49, v49, v51\n\t"
      "v_mfma_f32_16x16x32_bf16 v[32:35], v[76:79], v[112:115], 0\n\t"
      "s_mov_b32 s63, 0x2000\n\t"
      "v_mfma_f32_16x16x32_bf16 v[36:39], v[80:83], v[112:115], 0\n\t"
      "v_mfma_f32_16x16x32_bf16 v[40:43], v[76:79], v[116:119], 0\n\t"
      "v_mfma_f32_16x16x32_bf16 v[44:47], v[80:83], v[116:119], 0\n\t"
      "ds_read_b128 v[76:79], v103\n\t"
      "ds_read_b128 v[80:83], v103 offset:1024\n\t"
      "ds_read_b128 v[112:115], v100 offset:24576\n\t"
      "ds_read_b128 v[116:119], v100 offset:25600\n\t"
      "s_waitcnt lgkmcnt(8)\n\t"
      "v_mfma_f32_16x16x32_bf16 v[32:35], v[16:19], v[24:27], v[32:35]\n\t"
      "v_mfma_f32_16x16x32_bf16 v[36:39], v[20:23], v[24:27], v[36:39]\n\t"
      "v_mfma_f32_16x16x32_bf16 v[40:43], v[16:19], v[28:31], v[40:43]\n\t"
      "v_mfma_f32_16x16x32_bf16 v[44:47], v[20:23], v[28:31], v[44:47]\n\t"
      "LGATE_1%=:\n\t"
      "LBAR_1%=:\n\t"
      "s_cmp_gt_u32 s40, 1\n\t"
      "s_barrier\n\t"
      "s_cbranch_scc1 LPOLL_1%=\n\t"
      "s_branch LFIRST_1%=\n\t"
      ".p2align 6\n\t.fill 16, 4, 0xbf800000\n\t"
      "LPOLL_2%=:\n\t"
      "buffer_load_dwordx4 v[84:87], v60, s[36:39], s43 offen sc1\n\t"
      "buffer_load_dwordx4 v[88:91], v60, s[36:39], s43 offen offset:1024 sc1\n\t"
      "s_waitcnt lgkmcnt(8)\n\t"
      "v_mfma_f32_16x16x32_bf16 v[32:35], v[0:3], v[8:11], v[32:35]\n\t"
      "v_mfma_f32_16x16x32_bf16 v[36:39], v[4:7], v[8:11], v[36:39]\n\t"
      "v_mfma_f32_16x16x32_bf16 v[40:43], v[0:3], v[12:15], v[40:43]\n\t"
      "v_mfma_f32_16x16x32_bf16 v[44:47], v[4:7], v[12:15], v[44:47]\n\t"
      "buffer_load_dwordx4 v[92:95], v60, s[36:39], s68 offen sc1\n\t"
      "buffer_load_dwordx4 v[96:99], v60, s[36:39], s68 offen offset:1024 sc1\n\t"
      "v_mov_b64 v[48:49], 0\n\t"
      "v_mov_b64 v[50:51], 0\n\t"
      "v_mov_b64 v[52:53], 0\n\t"
      "v_mov_b64 v[54:55], 0\n\t"
      "s_xor_b32 s69, s97, 1024\n\t"
      "s_xor_b32 s98, s97, 512\n\t"
      "s_xor_b32 s23, s97, 1536\n\t"
      "s_waitcnt lgkmcnt(4)\n\t"
      "v_mfma_f32_16x16x32_bf16 v[32:35], v[16:19], v[24:27], v[32:35]\n\t"
      "v_mfma_f32_16x16x32_bf16 v[36:39], v[20:23], v[24:27], v[36:39]\n\t"
      "v_mfma_f32_16x16x32_bf16 v[40:43], v[16:19], v[28:31], v[40:43]\n\t"
      "v_mfma_f32_16x16x32_bf16 v[44:47], v[20:23], v[28:31], v[44:47]\n\t"
      "s_waitcnt lgkmcnt(0)\n\t"
      "v_mfma_f32_16x16x32_bf16 v[32:35], v[76:79], v[112:115], v[32:35]\n\t"
      "v_mfma_f32_16x16x32_bf16 v[36:39], v[80:83], v[112:115], v[36:39]\n\t"
      "v_mfma_f32_16x16x32_bf16 v[40:43], v[76:79], v[116:119], v[40:43]\n\t"
      "v_mfma_f32_16x16x32_bf16 v[44:47], v[80:83], v[116:119], v[44:47]\n\t"
      "s_mov_b64 exec, 1\n\t"
      "ds_write_b64 v109, v[110:111] offset:55392\n\t"
      "s_mov_b64 exec, -1\n\t"
      "s_nop 4\n\t"
      "v_cvt_pk_bf16_f32 v68, v32, v33\n\t"
      "v_cvt_pk_bf16_f32 v69, v34, v35\n\t"
      "v_cvt_pk_bf16_f32 v70, v36, v37\n\t"
      "v_cvt_pk_bf16_f32 v71, v38, v39\n\t"
      "v_cvt_pk_bf16_f32 v74, v40, v41\n\t"
      "v_cvt_pk_bf16_f32 v75, v42, v43\n\t"
      "v_cvt_pk_bf16_f32 v72, v44, v45\n\t"
      "v_cvt_pk_bf16_f32 v73, v46, v47\n\t"
      "v_mov_b64 v[32:33], 0\n\t"
      "v_mov_b64 v[34:35], 0\n\t"
      "v_mov_b64 v[36:37], 0\n\t"
      "v_mov_b64 v[38:39], 0\n\t"
      "v_mov_b64 v[40:41], 0\n\t"
      "v_mov_b64 v[42:43], 0\n\t"
      "v_mov_b64 v[44:45], 0\n\t"
      "v_mov_b64 v[46:47], 0\n\t"
      "LGO_2%=:\n\t"
      "s_nop 0\n\t"
      "s_nop 0\n\t"
      "s_nop 0\n\t"
      "s_nop 0\n\t"
      "s_nop 0\n\t"
      "s_waitcnt vmcnt(3)\n\t"
      "v_pk_fma_f32 v[14:15], v[86:87], s[56:57], v[126:127] op_sel:[0,1,0] op_sel_hi:[1,1,0]\n\t"
      "v_pk_fma_f32 v[12:13], v[84:85], s[56:57], v[126:127] op_sel:[0,1,0] op_sel_hi:[1,1,0]\n\t"
      "v_cvt_pk_bf16_f32 v8, v12, v13\n\t"
      "v_cvt_pk_bf16_f32 v9, v14, v15\n\t"
      "s_nop 0\n\t"
      "ds_write_b64 v127, v[8:9] offset:43008\n\t"
      "s_waitcnt vmcnt(2)\n\t"
      "v_pk_fma_f32 v[0:1], v[88:89], s[56:57], v[126:127] op_sel:[0,1,0] op_sel_hi:[1,1,0]\n\t"
      "v_pk_fma_f32 v[2:3], v[90:91], s[56:57], v[126:127] op_sel:[0,1,0] op_sel_hi:[1,1,0]\n\t"
      "v_cvt_pk_bf16_f32 v10, v0, v1\n\t"
      "v_cvt_pk_bf16_f32 v11, v2, v3\n\t"
      "ds_write_b64 v127, v[10:11] offset:43016\n\t"
      "v_bitop3_b32 v64, v84, v85, v86 bitop3:0x80\n\t"
      "v_mfma_f32_16x16x32_bf16 v[56:59], v[8:11], v[8:11], 0\n\t"
      "v_bitop3_b32 v64, v64, v87, v88 bitop3:0x80\n\t"
      "v_bitop3_b32 v64, v64, v89, v90 bitop3:0x80\n\t"
      "v_bitop3_b32 v64, v64, v91, s58 bitop3:0x80\n\t"
      "v_cmp_eq_u32_e64 vcc, 0, v64\n\t"
      "v_smfmac_f32_16x16x64_bf16 v[36:39], v[8:11], a[80:87], v122\n\t"
      "s_nop 0\n\t"
      "v_bitop3_b32 v120, v120, v121, s53 bitop3:0x7e\n\t"
      "v_smfmac_f32_16x16x64_bf16 v[44:47], v[8:11], a[208:215], v122\n\t"
      "s_cbranch_vccnz LR0_2%=\n\t"
      "LC0_2%=:\n\t"
      "s_waitcnt lgkmcnt(0)\n\t"
      "s_barrier\n\t"
      "ds_read_b128 v[12:15], v127 offset:44032\n\t"
      "v_smfmac_f32_16x16x64_bf16 v[52:55], v[8:11], v[208:215], v122\n\t"
      "ds_read_b128 v[0:3], v127 offset:40960\n\t"
      "v_smfmac_f32_16x16x64_bf16 v[48:51], v[8:11], v[144:151], v122\n\t"
      "ds_read_b128 v[4:7], v127 offset:41984\n\t"
      "v_smfmac_f32_16x16x64_bf16 v[32:35], v[8:11], a[16:23], v122\n\t"
      "ds_read_b128 v[84:87], v127 offset:36864\n\t"
      "v_smfmac_f32_16x16x64_bf16 v[40:43], v[8:11], a[144:151], v122\n\t"
      "s_waitcnt lgkmcnt(3)\n\t"
      "v_smfmac_f32_16x16x64_bf16 v[32:35], v[12:15], a[24:31], v122\n\t"
      "ds_read_b128 v[80:83], v127 offset:35840\n\t"
      "v_smfmac_f32_16x16x64_bf16 v[40:43], v[12:15], a[152:159], v122\n\t"
      "s_waitcnt vmcnt(1)\n\t"
      "v_fma_f32 v31, v95, s57, v126\n\t"
      "v_fma_f32 v28, v92, s57, v126\n\t"
      "v_smfmac_f32_16x16x64_bf16 v[36:39], v[12:15], a[88:95], v122\n\t"
      "v_fma_f32 v30, v94, s57, v126\n\t"
      "v_cvt_pk_bf16_f32 v25, v30, v31\n\t"
      "v_bitop3_b32 v64, v92, v93, v94 bitop3:0x80\n\t"
      "v_smfmac_f32_16x16x64_bf16 v[44:47], v[12:15], a[216:223], v122\n\t"
      "s_waitcnt lgkmcnt(2)\n\t"
      "v_fma_f32 v29, v93, s57, v126\n\t"
      "v_cvt_pk_bf16_f32 v24, v28, v29\n\t"
      "v_smfmac_f32_16x16x64_bf16 v[36:39], v[0:3], a[64:71], v122\n\t"
      "s_waitcnt vmcnt(0)\n\t"
      "ds_write_b64 v127, v[24:25] offset:47104\n\t"
      "v_fma_f32 v19, v99, s57, v126\n\t"
      "v_fma_f32 v18, v98, s57, v126\n\t"
      "v_cvt_pk_bf16_f32 v27, v18, v19\n\t"
      "v_fma_f32 v17, v97, s57, v126\n\t"
      "v_fma_f32 v16, v96, s57, v126\n\t"
      "v_cvt_pk_bf16_f32 v26, v16, v17\n\t"
      "v_smfmac_f32_16x16x64_bf16 v[44:47], v[0:3], a[192:199], v122\n\t"
      "ds_write_b64 v127, v[26:27] offset:47112\n\t"
      "v_smfmac_f32_16x16x64_bf16 v[40:43], v[0:3], a[128:135], v122\n\t"
      "ds_read_b128 v[88:91], v127 offset:39936\n\t"
      "v_bitop3_b32 v64, v64, v95, v96 bitop3:0x80\n\t"
      "v_bitop3_b32 v64, v64, v97, v98 bitop3:0x80\n\t"
      "v_smfmac_f32_16x16x64_bf16 v[40:43], v[4:7], a[136:143], v122\n\t"
      "v_bitop3_b32 v64, v64, v99, s60 bitop3:0x80\n\t"
      "v_smfmac_f32_16x16x64_bf16 v[36:39], v[4:7], a[72:79], v122\n\t"
      "v_cmp_eq_u32_e64 vcc, 0, v64\n\t"
      "v_smfmac_f32_16x16x64_bf16 v[44:47], v[4:7], a[200:207], v122\n\t"
      "s_waitcnt lgkmcnt(1)\n\t"
      "v_smfmac_f32_16x16x64_bf16 v[32:35], v[0:3], a[0:7], v122\n\t"
      "s_cbranch_vccnz LR1_2%=\n\t"
      "LC1_2%=:\n\t"
      "v_smfmac_f32_16x16x64_bf16 v[40:43], v[24:27], a[176:183], v122\n\t"
      "s_barrier\n\t"
      "v_mfma_f32_16x16x32_bf16 v[56:59], v[24:27], v[24:27], v[56:59]\n\t"
      "ds_read_b128 v[28:31], v127 offset:48128\n\t"
      "s_nop 0\n\t"
      "v_smfmac_f32_16x16x64_bf16 v[32:35], v[4:7], a[8:15], v122\n\t"
      "ds_read_b128 v[16:19], v127 offset:45056\n\t"
      "v_smfmac_f32_16x16x64_bf16 v[44:47], v[24:27], a[240:247], v122\n\t"
      "ds_read_b128 v[20:23], v127 offset:46080\n\t"
      "v_max3_f32 v56, v56, v57, v58\n\t"
      "v_smfmac_f32_16x16x64_bf16 v[36:39], v[24:27], a[112:119], v122\n\t"
      "ds_read_b128 v[76:79], v127 offset:32768\n\t"
      "v_max_f32_e64 v56, v56, v59\n\t"
      "v_smfmac_f32_16x16x64_bf16 v[32:35], v[24:27], a[48:55], v122\n\t"
      "s_waitcnt lgkmcnt(3)\n\t"
      "v_readfirstlane_b32 s56, v120\n\t"
      "v_smfmac_f32_16x16x64_bf16 v[32:35], v[28:31], a[56:63], v122\n\t"
      "s_waitcnt lgkmcnt(2)\n\t"
      "v_max_f32_dpp v56, v56, v56 quad_perm:[1,0,3,2] row_mask:0xf bank_mask:0xf\n\t"
      "v_smfmac_f32_16x16x64_bf16 v[32:35], v[16:19], a[32:39], v122\n\t"
      "s_waitcnt lgkmcnt(1)\n\t"
      "v_max_f32_dpp v56, v56, v56 quad_perm:[2,3,0,1] row_mask:0xf bank_mask:0xf\n\t"
      "v_smfmac_f32_16x16x64_bf16 v[32:35], v[20:23], a[40:47], v122\n\t"
      "s_mov_b32 exec_lo, 0x100001\n\t"
      "s_mov_b32 exec_hi, 0x10000100\n\t"
      "v_smfmac_f32_16x16x64_bf16 v[36:39], v[28:31], a[120:127], v122\n\t"
      "ds_max_i32 v109, v56 offset:55392\n\t"
      "v_smfmac_f32_16x16x64_bf16 v[40:43], v[28:31], a[184:191], v122\n\t"
      "s_mov_b64 exec, -1\n\t"
      "v_add_f32 v33, v33, v35\n\t"
      "v_smfmac_f32_16x16x64_bf16 v[36:39], v[16:19], a[96:103], v122\n\t"
      "ds_read_b128 v[60:63], v127 offset:37888\n\t"
      "v_mul_f32_e64 v33, s59, v33\n\t"
      "v_smfmac_f32_16x16x64_bf16 v[36:39], v[20:23], a[104:111], v122\n\t"
      "v_add_f32_e64 v32, v32, v34\n\t"
      "v_smfmac_f32_16x16x64_bf16 v[44:47], v[28:31], a[248:255], v122\n\t"
      "ds_read_b128 v[56:59], v127 offset:38912\n\t"
      "v_mul_f32_e64 v32, s59, v32\n\t"
      "v_smfmac_f32_16x16x64_bf16 v[44:47], v[16:19], a[224:231], v122\n\t"
      "v_add_f32 v36, v36, v38\n\t"
      "v_add_f32 v37, v37, v39\n\t"
      "v_mul_f32_e64 v37, s59, v37\n\t"
      "v_smfmac_f32_16x16x64_bf16 v[40:43], v[16:19], a[160:167], v122\n\t"
      "ds_read_b128 v[92:95], v127 offset:34816\n\t"
      "v_cvt_pk_bf16_f32 v112, v32, v33\n\t"
      "v_smfmac_f32_16x16x64_bf16 v[44:47], v[20:23], a[232:239], v122\n\t"
      "ds_read_b128 v[96:99], v127 offset:33792\n\t"
      "v_mul_f32_e64 v36, s59, v36\n\t"
      "v_cvt_pk_bf16_f32 v113, v36, v37\n\t"
      "v_smfmac_f32_16x16x64_bf16 v[40:43], v[20:23], a[168:175], v122\n\t"
      "ds_write_b64 v125, v[112:113]\n\t"
      "v_smfmac_f32_16x16x64_bf16 v[48:51], v[12:15], v[152:159], v122\n\t"
      "s_nop 0\n\t"
      "v_add_f32 v45, v45, v47\n\t"
      "v_add_f32_e64 v44, v44, v46\n\t"
      "v_cvt_pk_bf16_f32 v121, v44, v45\n\t"
      "v_add_f32 v41, v41, v43\n\t"
      "v_add_f32 v40, v40, v42\n\t"
      "v_cvt_pk_bf16_f32 v120, v40, v41\n\t"
      "v_smfmac_f32_16x16x64_bf16 v[48:51], v[0:3], v[128:135], v122\n\t"
      "s_mov_b32 s58, 0x10000000\n\t"
      "ds_write_b64 v125, v[120:121] offset:2080\n\t"
      "v_smfmac_f32_16x16x64_bf16 v[48:51], v[4:7], v[136:143], v122\n\t"
      "s_waitcnt lgkmcnt(1)\n\t"
      "s_barrier\n\t"
      "ds_read_b64_tr_b16 v[36:37], v124 offset:50192\n\t"
      "v_smfmac_f32_16x16x64_bf16 v[48:51], v[24:27], v[176:183], v122\n\t"
      "ds_read_b32 v108, v109 offset:55392\n\t"
      "ds_read_b64_tr_b16 v[32:33], v124 offset:49152\n\t"
      "v_smfmac_f32_16x16x64_bf16 v[52:55], v[12:15], v[216:223], v122\n\t"
      "ds_read_b64_tr_b16 v[34:35], v124 offset:49672\n\t"
      "s_waitcnt lgkmcnt(4)\n\t"
      "ds_read_b64_tr_b16 v[38:39], v124 offset:50712\n\t"
      "v_smfmac_f32_16x16x64_bf16 v[48:51], v[28:31], v[184:191], v122\n\t"
      "s_barrier\n\t"
      "ds_read_b64_tr_b16 v[46:47], v124 offset:52792\n\t"
      "v_smfmac_f32_16x16x64_bf16 v[48:51], v[16:19], v[160:167], v122\n\t"
      "s_waitcnt lgkmcnt(4)\n\t"
      "ds_read_b64_tr_b16 v[44:45], v124 offset:52272\n\t"
      "v_readfirstlane_b32 s61, v108\n\t"
      "v_smfmac_f32_16x16x64_bf16 v[48:51], v[20:23], v[168:175], v122\n\t"
      "s_waitcnt lgkmcnt(3)\n\t"
      "ds_read_b64_tr_b16 v[42:43], v124 offset:51752\n\t"
      "ds_read_b64_tr_b16 v[40:41], v124 offset:51232\n\t"
      "s_waitcnt lgkmcnt(4)\n\t"
      "v_mfma_f32_16x16x32_bf16 v[112:115], v[76:79], v[32:35], 0\n\t"
      "s_lshr_b32 s61, s61, 23\n\t"
      "s_add_u32 s61, s61, s67\n\t"
      "v_mov_b32_dpp v102, v34 quad_perm:[2,3,0,1] row_mask:0xf bank_mask:0xf\n\t"
      "v_mov_b32_dpp v106, v38 quad_perm:[2,3,0,1] row_mask:0xf bank_mask:0xf\n\t"
      "s_max_u32 s61, s61, s66\n\t"
      "v_mfma_f32_16x16x32_bf16 v[116:119], v[80:83], v[32:35], 0\n\t"
      "s_lshr_b32 s61, s61, 1\n\t"
      "v_add_f32_e64 v48, v48, v50\n\t"
      "v_mov_b32_dpp v100, v32 quad_perm:[2,3,0,1] row_mask:0xf bank_mask:0xf\n\t"
      "v_mfma_f32_16x16x32_bf16 v[116:119], v[88:91], v[36:39], v[116:119]\n\t"
      "s_min_u32 s61, s61, 200\n\t"
      "v_mov_b32_dpp v107, v39 quad_perm:[2,3,0,1] row_mask:0xf bank_mask:0xf\n\t"
      "s_waitcnt lgkmcnt(0)\n\t"
      "v_mfma_f32_16x16x32_bf16 v[76:79], v[40:43], v[32:35], 0\n\t"
      "buffer_load_dwordx4 v127, s[36:39], s46 offen sc1 lds\n\t"
      "s_lshl_b32 s61, s61, 23\n\t"
      "v_mfma_f32_16x16x32_bf16 v[76:79], v[44:47], v[36:39], v[76:79]\n\t"
      "s_add_u32 s65, s61, 33554432\n\t"
      "v_mov_b32_dpp v104, v36 quad_perm:[2,3,0,1] row_mask:0xf bank_mask:0xf\n\t"
      "v_mov_b32_dpp v103, v35 quad_perm:[2,3,0,1] row_mask:0xf bank_mask:0xf\n\t"
      "v_mov_b32_dpp v101, v33 quad_perm:[2,3,0,1] row_mask:0xf bank_mask:0xf\n\t"
      "v_mfma_f32_16x16x32_bf16 v[112:115], v[84:87], v[36:39], v[112:115]\n\t"
      "s_pack_hh_b32_b16 s75, s65, s65\n\t"
      "v_add_f32 v49, v49, v51\n\t"
      "v_cvt_pk_bf16_f32 v64, v48, v49\n\t"
      "v_cndmask_b32_e64 v120, v76, v78, s[72:73]\n\t"
      "v_cndmask_b32_e64 v121, v77, v79, s[72:73]\n\t"
      "v_mov_b32_dpp v105, v37 quad_perm:[2,3,0,1] row_mask:0xf bank_mask:0xf\n\t"
      "v_mfma_f32_16x16x32_bf16 v[112:115], v[92:95], v[100:103], v[112:115]\n\t"
      "buffer_load_dwordx4 v127, s[36:39], s46 offen offset:1024 sc1 lds\n\t"
      "v_mfma_f32_16x16x32_bf16 v[116:119], v[96:99], v[100:103], v[116:119]\n\t"
      "s_pack_hh_b32_b16 s74, s65, s65\n\t"
      "v_add_f32_dpp v121, v121, v121 quad_perm:[2,3,0,1] row_mask:0xf bank_mask:0xf\n\t"
      "v_add_f32_dpp v120, v120, v120 quad_perm:[2,3,0,1] row_mask:0xf bank_mask:0xf\n\t"
      "v_mfma_f32_16x16x32_bf16 v[112:115], v[56:59], v[104:107], v[112:115]\n\t"
      "v_exp_f32 v81, v121\n\t"
      "v_mfma_f32_16x16x32_bf16 v[116:119], v[60:63], v[104:107], v[116:119]\n\t"
      "s_mov_b32 m0, s45\n\t"
      "v_mov_b64 v[90:91], s[74:75]\n\t"
      "v_exp_f32 v80, v120\n\t"
      "v_mov_b64 v[88:89], s[74:75]\n\t"
      "v_cvt_pk_bf16_f32 v108, v80, v81\n\t"
      "v_smfmac_f32_16x16x64_bf16 v[52:55], v[0:3], v[192:199], v122\n\t"
      "buffer_load_dwordx4 v65, s[36:39], s44 offen offset:1024 sc1 lds\n\t"
      "v_mfma_f32_16x16x32_bf16 v[84:87], v[108:111], v[64:67], 0\n\t"
      "s_add_u32 s57, s61, 310378496\n\t"
      "v_cndmask_b32_e64 v112, v112, v116, s[72:73]\n\t"
      "v_exp_f32_e64 v56, v112\n\t"
      "v_mfma_f32_16x16x32_bf16 v[48:51], v[108:111], v[88:91], 0\n\t"
      "s_add_u32 s43, s43, 0x8000\n\t"
      "v_cndmask_b32_e64 v113, v113, v117, s[72:73]\n\t"
      "v_cndmask_b32_e64 v114, v114, v118, s[72:73]\n\t"
      "v_exp_f32_e64 v58, v114\n\t"
      "v_cndmask_b32_e64 v115, v115, v119, s[72:73]\n\t"
      "v_exp_f32 v57, v113\n\t"
      "v_exp_f32 v59, v115\n\t"
      "v_cvt_pk_bf16_f32 v96, v56, v57\n\t"
      "v_cvt_pk_bf16_f32 v97, v58, v59\n\t"
      "v_max3_f32 v82, |v112|, |v113|, |v114|\n\t"
      "v_mov_b32_dpp v98, v96 quad_perm:[2,3,0,1] row_mask:0xf bank_mask:0xf\n\t"
      "v_mov_b32_dpp v99, v97 quad_perm:[2,3,0,1] row_mask:0xf bank_mask:0xf\n\t"
      "v_smfmac_f32_16x16x64_bf16 v[52:55], v[4:7], v[200:207], v122\n\t"
      "v_max3_f32 v83, |v115|, |v120|, |v121|\n\t"
      "v_mfma_f32_16x16x32_bf16 v[48:51], v[96:99], v[88:91], v[48:51]\n\t"
      "v_max_f32_e64 v82, v82, v83\n\t"
      "v_cmp_nge_f32 vcc, 0x42000000, v82\n\t"
      "v_mfma_f32_16x16x32_bf16 v[84:87], v[96:99], v[68:71], v[84:87]\n\t"
      "buffer_load_dwordx4 v65, s[36:39], s44 offen sc1 lds\n\t"
      "v_smfmac_f32_16x16x64_bf16 v[52:55], v[24:27], v[240:247], v122\n\t"
      "s_add_u32 s68, s68, 0x8000\n\t"
      "s_cbranch_vccnz LMX_2%=\n\t"
      "v_rcp_f32 v49, v49\n\t"
      "v_rcp_f32_e64 v48, v48\n\t"
      "v_smfmac_f32_16x16x64_bf16 v[52:55], v[28:31], v[248:255], v122\n\t"
      "v_fma_f32 v85, v85, v49, s96\n\t"
      "v_fma_f32 v84, v84, v48, s96\n\t"
      "v_smfmac_f32_16x16x64_bf16 v[52:55], v[16:19], v[224:231], v122\n\t"
      "buffer_atomic_add v84, v123, s[36:39], s97 offen\n\t"
      "buffer_atomic_add v85, v123, s[36:39], s98 offen\n\t"
      "v_smfmac_f32_16x16x64_bf16 v[52:55], v[20:23], v[232:239], v122\n\t"
      "v_mfma_f32_16x16x32_bf16 v[92:95], v[96:99], v[72:75], 0\n\t"
      "s_nop 3\n\t"
      "v_add_f32 v53, v53, v55\n\t"
      "v_add_f32_e64 v52, v52, v54\n\t"
      "v_cvt_pk_bf16_f32 v66, v52, v53\n\t"
      "s_add_u32 s62, s61, 0x83400000\n\t"
      "v_mov_b32_e64 v126, s62\n\t"
      "v_mfma_f32_16x16x32_bf16 v[92:95], v[108:111], v[66:69], v[92:95]\n\t"
      "s_add_u32 s97, s97, 0x8000\n\t"
      "s_nop 6\n\t"
      "v_fma_f32 v95, v95, v49, s96\n\t"
      "buffer_atomic_add v95, v123, s[36:39], s23 offen\n\t"
      "v_fma_f32 v94, v94, v48, s96\n\t"
      "buffer_atomic_add v94, v123, s[36:39], s69 offen\n\t"
      "LWIN_2%=:\n\t"
      "v_add_u32 v100, s55, v127\n\t"
      "ds_read_b128 v[60:63], v100 offset:57344\n\t"
      "s_cmp_eq_u32 s56, 0\n\t"
      "s_cbranch_scc0 LSLOW_2%=\n\t"
      "LWIN1_2%=:\n\t"
      "buffer_load_dwordx2 v[120:121], off, s[36:39], s50 sc1\n\t"
      "s_mov_b32 s64, s50\n\t"
      "s_bitcmp1_b64 s[84:85], s40\n\t"
      "s_cbranch_scc1 LEARLY_2%=\n\t"
      "LROLE_2%=:\n\t"
      "v_add_u32 v52, 65536, v127\n\t"
      "ds_read_b128 v[88:91], v52 offset:16384\n\t"
      "ds_read_b128 v[92:95], v52 offset:17408\n\t"
      "ds_read_b128 v[32:35], v52 offset:20480\n\t"
      "ds_read_b128 v[36:39], v52 offset:21504\n\t"
      "s_cmp_eq_u32 s42, 3\n\t"
      "s_waitcnt lgkmcnt(4)\n\t"
      "s_cbranch_scc1 LWK1_2%=\n\t"
      "s_cmp_eq_u32 s42, 2\n\t"
      "s_cbranch_scc0 LW4_2%=\n\t"
      "buffer_store_dwordx4 v[40:43], v61, s[36:39], s48 offen sc1\n\t"
      "LW4_2%=:\n\t"
      "s_add_u32 s48, s48, 65536\n\t"
      "s_nop 0\n\t"
      "ds_read_b128 v[40:43], v52 offset:24576\n\t"
      "ds_read_b128 v[44:47], v52 offset:25600\n\t"
      "v_mov_b64 v[48:49], 0\n\t"
      "v_mov_b64 v[50:51], 0\n\t"
      "s_memrealtime s[92:93]\n\t"
      "s_waitcnt lgkmcnt(0)\n\t"
      "s_sub_u32 s61, s92, s95\n\t"
      "s_mov_b32 s95, s92\n\t"
      "s_min_u32 s61, s61, 190\n\t"
      "s_add_u32 s61, s61, s92\n\t"
      "s_pack_ll_b32_b16 s61, s61, s40\n\t"
      "v_mov_b32 v63, s61\n\t"
      "s_mov_b64 exec, 1\n\t"
      "buffer_store_dword v63, off, s[36:39], s51 sc1\n\t"
      "s_mov_b64 exec, -1\n\t"
      "v_add_u32 v104, s71, v100\n\t"
      "ds_read_b128 v[76:79], v104\n\t"
      "ds_read_b128 v[80:83], v104 offset:1024\n\t"
      "ds_read_b128 v[112:115], v100\n\t"
      "ds_read_b128 v[116:119], v100 offset:1024\n\t"
      "v_xor_b32 v101, 2048, v104\n\t"
      "v_xor_b32 v102, 4096, v104\n\t"
      "v_xor_b32 v103, 6144, v104\n\t"
      "v_smfmac_f32_16x16x64_bf16 v[48:51], v[16:19], v[88:95], v122\n\t"
      "ds_read_b128 v[88:91], v52 offset:28672\n\t"
      "ds_read_b128 v[92:95], v52 offset:29696\n\t"
      "s_add_u32 m0, s55, s71\n\t"
      "s_sub_u32 s71, 204800, s71\n\t"
      "v_smfmac_f32_16x16x64_bf16 v[48:51], v[20:23], v[32:39], v122\n\t"
      "ds_read_b128 v[0:3], v101\n\t"
      "ds_read_b128 v[4:7], v101 offset:1024\n\t"
      "s_add_u32 s40, s40, 1\n\t"
      "s_cmp_lt_u32 s40, 63\n\t"
      "v_smfmac_f32_16x16x64_bf16 v[48:51], v[24:27], v[40:47], v122\n\t"
      "ds_read_b128 v[8:11], v100 offset:8192\n\t"
      "ds_read_b128 v[12:15], v100 offset:9216\n\t"
      "s_cselect_b32 s61, 786432, 0\n\t"
      "s_cselect_b32 s60, s58, 0\n\t"
      "s_waitcnt lgkmcnt(4)\n\t"
      "v_smfmac_f32_16x16x64_bf16 v[48:51], v[28:31], v[88:95], v122\n\t"
      "s_add_u32 s44, s44, s61\n\t"
      "s_cmp_lt_u32 s40, 62\n\t"
      "ds_read_b128 v[16:19], v102\n\t"
      "ds_read_b128 v[20:23], v102 offset:1024\n\t"
      "ds_read_b128 v[24:27], v100 offset:16384\n\t"
      "ds_read_b128 v[28:31], v100 offset:17408\n\t"
      "s_cselect_b32 s61, 786432, 0\n\t"
      "s_add_u32 s46, s46, s61\n\t"
      "s_add_u32 s61, s40, 3\n\t"
      "s_and_b32 s62, s61, 3\n\t"
      "s_cselect_b32 s62, 16, 2000\n\t"
      "s_cmp_lt_u32 s61, 64\n\t"
      "s_cselect_b32 s62, s62, 0\n\t"
      "s_add_u32 s50, s50, s62\n\t"
      "v_add_f32 v48, v48, v50\n\t"
      "v_add_f32 v49, v49, v51\n\t"
      "ds_write_b64 v124, v[48:49] offset:53312\n\t"
      "v_mfma_f32_16x16x32_bf16 v[32:35], v[76:79], v[112:115], 0\n\t"
      "s_mov_b32 s63, 0x2000\n\t"
      "v_mfma_f32_16x16x32_bf16 v[36:39], v[80:83], v[112:115], 0\n\t"
      "v_mfma_f32_16x16x32_bf16 v[40:43], v[76:79], v[116:119], 0\n\t"
      "v_mfma_f32_16x16x32_bf16 v[44:47], v[80:83], v[116:119], 0\n\t"
      "ds_read_b128 v[76:79], v103\n\t"
      "ds_read_b128 v[80:83], v103 offset:1024\n\t"
      "ds_read_b128 v[112:115], v100 offset:24576\n\t"
      "ds_read_b128 v[116:119], v100 offset:25600\n\t"
      "LGATE_2%=:\n\t"
      "LBAR_2%=:\n\t"
      "s_cmp_gt_u32 s40, 1\n\t"
      "s_waitcnt lgkmcnt(0)\n\t"
      "s_barrier\n\t"
      "s_cbranch_scc1 LPOLL_2%=\n\t"
      "s_branch LFIRST_2%=\n\t"
      ".p2align 6\n\t.fill 16, 4, 0xbf800000\n\t"
      "LPOLL_3%=:\n\t"
      "s_memtime s[78:79]\n\t"
      "buffer_load_dwordx4 v[84:87], v60, s[36:39], s43 offen sc1\n\t"
      "buffer_load_dwordx4 v[88:91], v60, s[36:39], s43 offen offset:1024 sc1\n\t"
      "s_waitcnt lgkmcnt(8)\n\t"
      "v_mfma_f32_16x16x32_bf16 v[32:35], v[0:3], v[8:11], v[32:35]\n\t"
      "v_mfma_f32_16x16x32_bf16 v[36:39], v[4:7], v[8:11], v[36:39]\n\t"
      "v_mfma_f32_16x16x32_bf16 v[40:43], v[0:3], v[12:15], v[40:43]\n\t"
      "v_mfma_f32_16x16x32_bf16 v[44:47], v[4:7], v[12:15], v[44:47]\n\t"
      "buffer_load_dwordx4 v[92:95], v60, s[36:39], s68 offen sc1\n\t"
      "buffer_load_dwordx4 v[96:99], v60, s[36:39], s68 offen offset:1024 sc1\n\t"
      "v_mov_b64 v[48:49], 0\n\t"
      "v_mov_b64 v[50:51], 0\n\t"
      "v_mov_b64 v[52:53], 0\n\t"
      "v_mov_b64 v[54:55], 0\n\t"
      "s_xor_b32 s69, s97, 1024\n\t"
      "s_xor_b32 s98, s97, 512\n\t"
      "s_xor_b32 s23, s97, 1536\n\t"
      "s_waitcnt lgkmcnt(4)\n\t"
      "v_mfma_f32_16x16x32_bf16 v[32:35], v[16:19], v[24:27], v[32:35]\n\t"
      "v_mfma_f32_16x16x32_bf16 v[36:39], v[20:23], v[24:27], v[36:39]\n\t"
      "v_mfma_f32_16x16x32_bf16 v[40:43], v[16:19], v[28:31], v[40:43]\n\t"
      "v_mfma_f32_16x16x32_bf16 v[44:47], v[20:23], v[28:31], v[44:47]\n\t"
      "s_waitcnt lgkmcnt(0)\n\t"
      "v_mfma_f32_16x16x32_bf16 v[32:35], v[76:79], v[112:115], v[32:35]\n\t"
      "v_mfma_f32_16x16x32_bf16 v[36:39], v[80:83], v[112:115], v[36:39]\n\t"
      "v_mfma_f32_16x16x32_bf16 v[40:43], v[76:79], v[116:119], v[40:43]\n\t"
      "v_mfma_f32_16x16x32_bf16 v[44:47], v[80:83], v[116:119], v[44:47]\n\t"
      "s_mov_b64 exec, 1\n\t"
      "ds_write_b64 v109, v[110:111] offset:55392\n\t"
      "s_mov_b64 exec, -1\n\t"
      "s_nop 4\n\t"
      "v_cvt_pk_bf16_f32 v68, v32, v33\n\t"
      "v_cvt_pk_bf16_f32 v69, v34, v35\n\t"
      "v_cvt_pk_bf16_f32 v70, v36, v37\n\t"
      "v_cvt_pk_bf16_f32 v71, v38, v39\n\t"
      "v_cvt_pk_bf16_f32 v74, v40, v41\n\t"
      "v_cvt_pk_bf16_f32 v75, v42, v43\n\t"
      "v_cvt_pk_bf16_f32 v72, v44, v45\n\t"
      "v_cvt_pk_bf16_f32 v73, v46, v47\n\t"
      "v_mov_b64 v[32:33], 0\n\t"
      "v_mov_b64 v[34:35], 0\n\t"
      "v_mov_b64 v[36:37], 0\n\t"
      "v_mov_b64 v[38:39], 0\n\t"
      "v_mov_b64 v[40:41], 0\n\t"
      "v_mov_b64 v[42:43], 0\n\t"
      "v_mov_b64 v[44:45], 0\n\t"
      "v_mov_b64 v[46:47], 0\n\t"
      "LGO_3%=:\n\t"
      "s_nop 0\n\t"
      "s_waitcnt vmcnt(3)\n\t"
      "s_memtime s[88:89]\n\t"
      "v_pk_fma_f32 v[10:11], v[86:87], s[56:57], v[126:127] op_sel:[0,1,0] op_sel_hi:[1,1,0]\n\t"
      "v_pk_fma_f32 v[8:9], v[84:85], s[56:57], v[126:127] op_sel:[0,1,0] op_sel_hi:[1,1,0]\n\t"
      "v_cvt_pk_bf16_f32 v12, v8, v9\n\t"
      "v_cvt_pk_bf16_f32 v13, v10, v11\n\t"
      "s_nop 0\n\t"
      "ds_write_b64 v127, v[12:13] offset:44032\n\t"
      "s_waitcnt vmcnt(2)\n\t"
      "v_pk_fma_f32 v[4:5], v[88:89], s[56:57], v[126:127] op_sel:[0,1,0] op_sel_hi:[1,1,0]\n\t"
      "v_pk_fma_f32 v[6:7], v[90:91], s[56:57], v[126:127] op_sel:[0,1,0] op_sel_hi:[1,1,0]\n\t"
      "v_cvt_pk_bf16_f32 v14, v4, v5\n\t"
      "v_cvt_pk_bf16_f32 v15, v6, v7\n\t"
      "ds_write_b64 v127, v[14:15] offset:44040\n\t"
      "v_bitop3_b32 v64, v84, v85, v86 bitop3:0x80\n\t"
      "v_mfma_f32_16x16x32_bf16 v[56:59], v[12:15], v[12:15], 0\n\t"
      "v_bitop3_b32 v64, v64, v87, v88 bitop3:0x80\n\t"
      "v_bitop3_b32 v64, v64, v89, v90 bitop3:0x80\n\t"
      "v_bitop3_b32 v64, v64, v91, s58 bitop3:0x80\n\t"
      "v_cmp_eq_u32_e64 vcc, 0, v64\n\t"
      "v_smfmac_f32_16x16x64_bf16 v[36:39], v[12:15], a[88:95], v122\n\t"
      "s_nop 0\n\t"
      "v_bitop3_b32 v120, v120, v121, s53 bitop3:0x7e\n\t"
      "v_smfmac_f32_16x16x64_bf16 v[44:47], v[12:15], a[216:223], v122\n\t"
      "s_cbranch_vccnz LR0_3%=\n\t"
      "LC0_3%=:\n\t"
      "s_waitcnt lgkmcnt(0)\n\t"
      "s_barrier\n\t"
      "ds_read_b128 v[8:11], v127 offset:43008\n\t"
      "v_smfmac_f32_16x16x64_bf16 v[52:55], v[12:15], v[216:223], v122\n\t"
      "ds_read_b128 v[4:7], v127 offset:41984\n\t"
      "v_smfmac_f32_16x16x64_bf16 v[48:51], v[12:15], v[152:159], v122\n\t"
      "ds_read_b128 v[0:3], v127 offset:40960\n\t"
      "v_smfmac_f32_16x16x64_bf16 v[32:35], v[12:15], a[24:31], v122\n\t"
      "ds_read_b128 v[84:87], v127 offset:36864\n\t"
      "v_smfmac_f32_16x16x64_bf16 v[40:43], v[12:15], a[152:159], v122\n\t"
      "s_waitcnt lgkmcnt(3)\n\t"
      "v_smfmac_f32_16x16x64_bf16 v[32:35], v[8:11], a[16:23], v122\n\t"
      "ds_read_b128 v[80:83], v127 offset:35840\n\t"
      "v_smfmac_f32_16x16x64_bf16 v[40:43], v[8:11], a[144:151], v122\n\t"
      "s_waitcnt vmcnt(1)\n\t"
      "v_fma_f32 v27, v95, s57, v126\n\t"
      "v_fma_f32 v24, v92, s57, v126\n\t"
      "v_smfmac_f32_16x16x64_bf16 v[36:39], v[8:11], a[80:87], v122\n\t"
      "v_fma_f32 v26, v94, s57, v126\n\t"
      "v_cvt_pk_bf16_f32 v29, v26, v27\n\t"
      "v_bitop3_b32 v64, v92, v93, v94 bitop3:0x80\n\t"
      "v_smfmac_f32_16x16x64_bf16 v[44:47], v[8:11], a[208:215], v122\n\t"
      "s_waitcnt lgkmcnt(2)\n\t"
      "v_fma_f32 v25, v93, s57, v126\n\t"
      "v_cvt_pk_bf16_f32 v28, v24, v25\n\t"
      "v_smfmac_f32_16x16x64_bf16 v[36:39], v[4:7], a[72:79], v122\n\t"
      "s_waitcnt vmcnt(0)\n\t"
      "ds_write_b64 v127, v[28:29] offset:48128\n\t"
      "v_fma_f32 v23, v99, s57, v126\n\t"
      "v_fma_f32 v22, v98, s57, v126\n\t"
      "v_cvt_pk_bf16_f32 v31, v22, v23\n\t"
      "v_fma_f32 v21, v97, s57, v126\n\t"
      "v_fma_f32 v20, v96, s57, v126\n\t"
      "v_cvt_pk_bf16_f32 v30, v20, v21\n\t"
      "v_smfmac_f32_16x16x64_bf16 v[44:47], v[4:7], a[200:207], v122\n\t"
      "ds_write_b64 v127, v[30:31] offset:48136\n\t"
      "v_smfmac_f32_16x16x64_bf16 v[40:43], v[4:7], a[136:143], v122\n\t"
      "ds_read_b128 v[88:91], v127 offset:39936\n\t"
      "v_bitop3_b32 v64, v64, v95, v96 bitop3:0x80\n\t"
      "v_bitop3_b32 v64, v64, v97, v98 bitop3:0x80\n\t"
      "v_smfmac_f32_16x16x64_bf16 v[40:43], v[0:3], a[128:135], v122\n\t"
      "v_bitop3_b32 v64, v64, v99, s60 bitop3:0x80\n\t"
      "v_smfmac_f32_16x16x64_bf16 v[36:39], v[0:3], a[64:71], v122\n\t"
      "v_cmp_eq_u32_e64 vcc, 0, v64\n\t"
      "v_smfmac_f32_16x16x64_bf16 v[44:47], v[0:3], a[192:199], v122\n\t"
      "s_waitcnt lgkmcnt(1)\n\t"
      "v_smfmac_f32_16x16x64_bf16 v[32:35], v[4:7], a[8:15], v122\n\t"
      "s_cbranch_vccnz LR1_3%=\n\t"
      "LC1_3%=:\n\t"
      "v_smfmac_f32_16x16x64_bf16 v[40:43], v[28:31], a[184:191], v122\n\t"
      "s_barrier\n\t"
      "v_mfma_f32_16x16x32_bf16 v[56:59], v[28:31], v[28:31], v[56:59]\n\t"
      "ds_read_b128 v[24:27], v127 offset:47104\n\t"
      "s_nop 0\n\t"
      "v_smfmac_f32_16x16x64_bf16 v[32:35], v[0:3], a[0:7], v122\n\t"
      "ds_read_b128 v[20:23], v127 offset:46080\n\t"
      "v_smfmac_f32_16x16x64_bf16 v[44:47], v[28:31], a[248:255], v122\n\t"
      "ds_read_b128 v[16:19], v127 offset:45056\n\t"
      "v_max3_f32 v56, v56, v57, v58\n\t"
      "v_smfmac_f32_16x16x64_bf16 v[36:39], v[28:31], a[120:127], v122\n\t"
      "ds_read_b128 v[76:79], v127 offset:32768\n\t"
      "v_max_f32_e64 v56, v56, v59\n\t"
      "v_smfmac_f32_16x16x64_bf16 v[32:35], v[28:31], a[56:63], v122\n\t"
      "s_waitcnt lgkmcnt(3)\n\t"
      "v_readfirstlane_b32 s56, v120\n\t"
      "v_smfmac_f32_16x16x64_bf16 v[32:35], v[24:27], a[48:55], v122\n\t"
      "s_waitcnt lgkmcnt(2)\n\t"
      "v_max_f32_dpp v56, v56, v56 quad_perm:[1,0,3,2] row_mask:0xf bank_mask:0xf\n\t"
      "v_smfmac_f32_16x16x64_bf16 v[32:35], v[20:23], a[40:47], v122\n\t"
      "s_waitcnt lgkmcnt(1)\n\t"
      "v_max_f32_dpp v56, v56, v56 quad_perm:[2,3,0,1] row_mask:0xf bank_mask:0xf\n\t"
      "v_smfmac_f32_16x16x64_bf16 v[32:35], v[16:19], a[32:39], v122\n\t"
      "s_mov_b32 exec_lo, 0x100001\n\t"
      "s_mov_b32 exec_hi, 0x10000100\n\t"
      "v_smfmac_f32_16x16x64_bf16 v[36:39], v[24:27], a[112:119], v122\n\t"
      "ds_max_i32 v109, v56 offset:55392\n\t"
      "v_smfmac_f32_16x16x64_bf16 v[40:43], v[24:27], a[176:183], v122\n\t"
      "s_mov_b64 exec, -1\n\t"
      "v_add_f32 v33, v33, v35\n\t"
      "v_smfmac_f32_16x16x64_bf16 v[36:39], v[20:23], a[104:111], v122\n\t"
      "ds_read_b128 v[60:63], v127 offset:37888\n\t"
      "v_mul_f32_e64 v33, s59, v33\n\t"
      "v_smfmac_f32_16x16x64_bf16 v[36:39], v[16:19], a[96:103], v122\n\t"
      "v_add_f32_e64 v32, v32, v34\n\t"
      "v_smfmac_f32_16x16x64_bf16 v[44:47], v[24:27], a[240:247], v122\n\t"
      "ds_read_b128 v[56:59], v127 offset:38912\n\t"
      "v_mul_f32_e64 v32, s59, v32\n\t"
      "v_smfmac_f32_16x16x64_bf16 v[44:47], v[20:23], a[232:239], v122\n\t"
      "v_add_f32 v36, v36, v38\n\t"
      "v_add_f32 v37, v37, v39\n\t"
      "v_mul_f32_e64 v37, s59, v37\n\t"
      "v_smfmac_f32_16x16x64_bf16 v[40:43], v[20:23], a[168:175], v122\n\t"
      "ds_read_b128 v[92:95], v127 offset:34816\n\t"
      "v_cvt_pk_bf16_f32 v112, v32, v33\n\t"
      "v_smfmac_f32_16x16x64_bf16 v[44:47], v[16:19], a[224:231], v122\n\t"
      "ds_read_b128 v[96:99], v127 offset:33792\n\t"
      "v_mul_f32_e64 v36, s59, v36\n\t"
      "v_cvt_pk_bf16_f32 v113, v36, v37\n\t"
      "v_smfmac_f32_16x16x64_bf16 v[40:43], v[16:19], a[160:167], v122\n\t"
      "ds_write_b64 v125, v[112:113]\n\t"
      "v_smfmac_f32_16x16x64_bf16 v[48:51], v[8:11], v[144:151], v122\n\t"
      "s_nop 0\n\t"
      "v_add_f32 v45, v45, v47\n\t"
      "v_add_f32_e64 v44, v44, v46\n\t"
      "v_cvt_pk_bf16_f32 v121, v44, v45\n\t"
      "v_add_f32 v41, v41, v43\n\t"
      "v_add_f32 v40, v40, v42\n\t"
      "v_cvt_pk_bf16_f32 v120, v40, v41\n\t"
      "v_smfmac_f32_16x16x64_bf16 v[48:51], v[4:7], v[136:143], v122\n\t"
      "s_mov_b32 s58, 0x10000000\n\t"
      "ds_write_b64 v125, v[120:121] offset:2080\n\t"
      "v_smfmac_f32_16x16x64_bf16 v[48:51], v[0:3], v[128:135], v122\n\t"
      "s_waitcnt lgkmcnt(1)\n\t"
      "s_barrier\n\t"
      "ds_read_b64_tr_b16 v[36:37], v124 offset:50192\n\t"
      "v_smfmac_f32_16x16x64_bf16 v[48:51], v[28:31], v[184:191], v122\n\t"
      "ds_read_b32 v108, v109 offset:55392\n\t"
      "ds_read_b64_tr_b16 v[32:33], v124 offset:49152\n\t"
      "v_smfmac_f32_16x16x64_bf16 v[52:55], v[8:11], v[208:215], v122\n\t"
      "ds_read_b64_tr_b16 v[34:35], v124 offset:49672\n\t"
      "s_waitcnt lgkmcnt(4)\n\t"
      "ds_read_b64_tr_b16 v[38:39], v124 offset:50712\n\t"
      "v_smfmac_f32_16x16x64_bf16 v[48:51], v[24:27], v[176:183], v122\n\t"
      "s_barrier\n\t"
      "ds_read_b64_tr_b16 v[46:47], v124 offset:52792\n\t"
      "v_smfmac_f32_16x16x64_bf16 v[48:51], v[20:23], v[168:175], v122\n\t"
      "s_waitcnt lgkmcnt(4)\n\t"
      "ds_read_b64_tr_b16 v[44:45], v124 offset:52272\n\t"
      "v_readfirstlane_b32 s61, v108\n\t"
      "v_smfmac_f32_16x16x64_bf16 v[48:51], v[16:19], v[160:167], v122\n\t"
      "s_waitcnt lgkmcnt(3)\n\t"
      "ds_read_b64_tr_b16 v[42:43], v124 offset:51752\n\t"
      "ds_read_b64_tr_b16 v[40:41], v124 offset:51232\n\t"
      "s_waitcnt lgkmcnt(4)\n\t"
      "v_mfma_f32_16x16x32_bf16 v[112:115], v[76:79], v[32:35], 0\n\t"
      "s_lshr_b32 s61, s61, 23\n\t"
      "s_add_u32 s61, s61, s67\n\t"
      "v_mov_b32_dpp v102, v34 quad_perm:[2,3,0,1] row_mask:0xf bank_mask:0xf\n\t"
      "v_mov_b32_dpp v106, v38 quad_perm:[2,3,0,1] row_mask:0xf bank_mask:0xf\n\t"
      "s_max_u32 s61, s61, s66\n\t"
      "v_mfma_f32_16x16x32_bf16 v[116:119], v[80:83], v[32:35], 0\n\t"
      "s_lshr_b32 s61, s61, 1\n\t"
      "v_add_f32_e64 v48, v48, v50\n\t"
      "v_mov_b32_dpp v100, v32 quad_perm:[2,3,0,1] row_mask:0xf bank_mask:0xf\n\t"
      "v_mfma_f32_16x16x32_bf16 v[116:119], v[88:91], v[36:39], v[116:119]\n\t"
      "s_min_u32 s61, s61, 200\n\t"
      "v_mov_b32_dpp v107, v39 quad_perm:[2,3,0,1] row_mask:0xf bank_mask:0xf\n\t"
      "s_waitcnt lgkmcnt(0)\n\t"
      "v_mfma_f32_16x16x32_bf16 v[76:79], v[40:43], v[32:35], 0\n\t"
      "buffer_load_dwordx4 v127, s[36:39], s46 offen sc1 lds\n\t"
      "s_lshl_b32 s61, s61, 23\n\t"
      "v_mfma_f32_16x16x32_bf16 v[76:79], v[44:47], v[36:39], v[76:79]\n\t"
      "s_add_u32 s65, s61, 33554432\n\t"
      "v_mov_b32_dpp v104, v36 quad_perm:[2,3,0,1] row_mask:0xf bank_mask:0xf\n\t"
      "v_mov_b32_dpp v103, v35 quad_perm:[2,3,0,1] row_mask:0xf bank_mask:0xf\n\t"
      "v_mov_b32_dpp v101, v33 quad_perm:[2,3,0,1] row_mask:0xf bank_mask:0xf\n\t"
      "v_mfma_f32_16x16x32_bf16 v[112:115], v[84:87], v[36:39], v[112:115]\n\t"
      "s_pack_hh_b32_b16 s75, s65, s65\n\t"
      "v_add_f32 v49, v49, v51\n\t"
      "v_cvt_pk_bf16_f32 v64, v48, v49\n\t"
      "v_cndmask_b32_e64 v120, v76, v78, s[72:73]\n\t"
      "v_cndmask_b32_e64 v121, v77, v79, s[72:73]\n\t"
      "v_mov_b32_dpp v105, v37 quad_perm:[2,3,0,1] row_mask:0xf bank_mask:0xf\n\t"
      "v_mfma_f32_16x16x32_bf16 v[112:115], v[92:95], v[100:103], v[112:115]\n\t"
      "buffer_load_dwordx4 v127, s[36:39], s46 offen offset:1024 sc1 lds\n\t"
      "v_mfma_f32_16x16x32_bf16 v[116:119], v[96:99], v[100:103], v[116:119]\n\t"
      "s_pack_hh_b32_b16 s74, s65, s65\n\t"
      "v_add_f32_dpp v121, v121, v121 quad_perm:[2,3,0,1] row_mask:0xf bank_mask:0xf\n\t"
      "v_add_f32_dpp v120, v120, v120 quad_perm:[2,3,0,1] row_mask:0xf bank_mask:0xf\n\t"
      "v_mfma_f32_16x16x32_bf16 v[112:115], v[56:59], v[104:107], v[112:115]\n\t"
      "v_exp_f32 v81, v121\n\t"
      "v_mfma_f32_16x16x32_bf16 v[116:119], v[60:63], v[104:107], v[116:119]\n\t"
      "s_mov_b32 m0, s45\n\t"
      "v_mov_b64 v[90:91], s[74:75]\n\t"
      "v_exp_f32 v80, v120\n\t"
      "v_mov_b64 v[88:89], s[74:75]\n\t"
      "v_cvt_pk_bf16_f32 v108, v80, v81\n\t"
      "v_smfmac_f32_16x16x64_bf16 v[52:55], v[4:7], v[200:207], v122\n\t"
      "buffer_load_dwordx4 v65, s[36:39], s44 offen offset:1024 sc1 lds\n\t"
      "v_mfma_f32_16x16x32_bf16 v[84:87], v[108:111], v[64:67], 0\n\t"
      "s_add_u32 s57, s61, 310378496\n\t"
      "v_cndmask_b32_e64 v112, v112, v116, s[72:73]\n\t"
      "v_exp_f32_e64 v56, v112\n\t"
      "v_mfma_f32_16x16x32_bf16 v[48:51], v[108:111], v[88:91], 0\n\t"
      "s_add_u32 s43, s43, 0x8000\n\t"
      "v_cndmask_b32_e64 v113, v113, v117, s[72:73]\n\t"
      "v_cndmask_b32_e64 v114, v114, v118, s[72:73]\n\t"
      "v_exp_f32_e64 v58, v114\n\t"
      "v_cndmask_b32_e64 v115, v115, v119, s[72:73]\n\t"
      "v_exp_f32 v57, v113\n\t"
      "v_exp_f32 v59, v115\n\t"
      "v_cvt_pk_bf16_f32 v96, v56, v57\n\t"
      "v_cvt_pk_bf16_f32 v97, v58, v59\n\t"
      "v_max3_f32 v82, |v112|, |v113|, |v114|\n\t"
      "v_mov_b32_dpp v98, v96 quad_perm:[2,3,0,1] row_mask:0xf bank_mask:0xf\n\t"
      "v_mov_b32_dpp v99, v97 quad_perm:[2,3,0,1] row_mask:0xf bank_mask:0xf\n\t"
      "v_smfmac_f32_16x16x64_bf16 v[52:55], v[0:3], v[192:199], v122\n\t"
      "v_max3_f32 v83, |v115|, |v120|, |v121|\n\t"
      "v_mfma_f32_16x16x32_bf16 v[48:51], v[96:99], v[88:91], v[48:51]\n\t"
      "v_max_f32_e64 v82, v82, v83\n\t"
      "v_cmp_nge_f32 vcc, 0x42000000, v82\n\t"
      "v_mfma_f32_16x16x32_bf16 v[84:87], v[96:99], v[68:71], v[84:87]\n\t"
      "buffer_load_dwordx4 v65, s[36:39], s44 offen sc1 lds\n\t"
      "v_smfmac_f32_16x16x64_bf16 v[52:55], v[28:31], v[248:255], v122\n\t"
      "s_add_u32 s68, s68, 0x8000\n\t"
      "s_cbranch_vccnz LMX_3%=\n\t"
      "v_rcp_f32 v49, v49\n\t"
      "v_rcp_f32_e64 v48, v48\n\t"
      "v_smfmac_f32_16x16x64_bf16 v[52:55], v[24:27], v[240:247], v122\n\t"
      "v_fma_f32 v85, v85, v49, s96\n\t"
      "v_fma_f32 v84, v84, v48, s96\n\t"
      "v_smfmac_f32_16x16x64_bf16 v[52:55], v[20:23], v[232:239], v122\n\t"
      "buffer_atomic_add v84, v123, s[36:39], s97 offen\n\t"
      "buffer_atomic_add v85, v123, s[36:39], s98 offen\n\t"
      "v_smfmac_f32_16x16x64_bf16 v[52:55], v[16:19], v[224:231], v122\n\t"
      "v_mfma_f32_16x16x32_bf16 v[92:95], v[96:99], v[72:75], 0\n\t"
      "s_nop 3\n\t"
      "v_add_f32 v53, v53, v55\n\t"
      "v_add_f32_e64 v52, v52, v54\n\t"
      "v_cvt_pk_bf16_f32 v66, v52, v53\n\t"
      "s_add_u32 s62, s61, 0x83400000\n\t"
      "v_mov_b32_e64 v126, s62\n\t"
      "v_mfma_f32_16x16x32_bf16 v[92:95], v[108:111], v[66:69], v[92:95]\n\t"
      "s_add_u32 s97, s97, 0x8000\n\t"
      "s_nop 6\n\t"
      "v_fma_f32 v95, v95, v49, s96\n\t"
      "buffer_atomic_add v95, v123, s[36:39], s23 offen\n\t"
      "v_fma_f32 v94, v94, v48, s96\n\t"
      "buffer_atomic_add v94, v123, s[36:39], s69 offen\n\t"
      "LWIN_3%=:\n\t"
      "s_memtime s[76:77]\n\t"
      "s_memrealtime s[80:81]\n\t"
      "v_add_u32 v100, s55, v127\n\t"
      "ds_read_b128 v[60:63], v100 offset:57344\n\t"
      "s_cmp_eq_u32 s56, 0\n\t"
      "s_cbranch_scc0 LSLOW_3%=\n\t"
      "LWIN1_3%=:\n\t"
      "buffer_load_dwordx2 v[120:121], off, s[36:39], s50 sc1\n\t"
      "s_mov_b32 s64, s50\n\t"
      "s_bitcmp1_b64 s[84:85], s40\n\t"
      "s_cbranch_scc1 LEARLY_3%=\n\t"
      "LROLE_3%=:\n\t"
      "v_add_u32 v52, 67584, v127\n\t"
      "ds_read_b128 v[88:91], v52 offset:16384\n\t"
      "ds_read_b128 v[92:95], v52 offset:17408\n\t"
      "ds_read_b128 v[32:35], v52 offset:20480\n\t"
      "ds_read_b128 v[36:39], v52 offset:21504\n\t"
      "ds_read_b128 v[40:43], v52 offset:24576\n\t"
      "ds_read_b128 v[44:47], v52 offset:25600\n\t"
      "v_mov_b64 v[48:49], 0\n\t"
      "v_mov_b64 v[50:51], 0\n\t"
      "v_add_u32 v104, s71, v100\n\t"
      "ds_read_b128 v[76:79], v104\n\t"
      "ds_read_b128 v[80:83], v104 offset:1024\n\t"
      "ds_read_b128 v[112:115], v100\n\t"
      "ds_read_b128 v[116:119], v100 offset:1024\n\t"
      "v_xor_b32 v101, 2048, v104\n\t"
      "v_xor_b32 v102, 4096, v104\n\t"
      "v_xor_b32 v103, 6144, v104\n\t"
      "s_waitcnt lgkmcnt(8)\n\t"
      "v_smfmac_f32_16x16x64_bf16 v[48:51], v[16:19], v[88:95], v122\n\t"
      "ds_read_b128 v[88:91], v52 offset:28672\n\t"
      "ds_read_b128 v[92:95], v52 offset:29696\n\t"
      "s_add_u32 m0, s55, s71\n\t"
      "s_sub_u32 s71, 204800, s71\n\t"
      "s_waitcnt lgkmcnt(8)\n\t"
      "v_smfmac_f32_16x16x64_bf16 v[48:51], v[20:23], v[32:39], v122\n\t"
      "ds_read_b128 v[0:3], v101\n\t"
      "ds_read_b128 v[4:7], v101 offset:1024\n\t"
      "s_add_u32 s40, s40, 1\n\t"
      "s_cmp_lt_u32 s40, 63\n\t"
      "s_waitcnt lgkmcnt(8)\n\t"
      "v_smfmac_f32_16x16x64_bf16 v[48:51], v[24:27], v[40:47], v122\n\t"
      "ds_read_b128 v[8:11], v100 offset:8192\n\t"
      "ds_read_b128 v[12:15], v100 offset:9216\n\t"
      "s_cselect_b32 s61, 786432, 0\n\t"
      "s_cselect_b32 s60, s58, 0\n\t"
      "s_waitcnt lgkmcnt(4)\n\t"
      "v_smfmac_f32_16x16x64_bf16 v[48:51], v[28:31], v[88:95], v122\n\t"
      "s_add_u32 s44, s44, s61\n\t"
      "s_cmp_lt_u32 s40, 62\n\t"
      "ds_read_b128 v[16:19], v102\n\t"
      "ds_read_b128 v[20:23], v102 offset:1024\n\t"
      "ds_read_b128 v[24:27], v100 offset:16384\n\t"
      "ds_read_b128 v[28:31], v100 offset:17408\n\t"
      "s_cselect_b32 s61, 786432, 0\n\t"
      "s_add_u32 s46, s46, s61\n\t"
      "s_add_u32 s61, s40, 3\n\t"
      "s_and_b32 s62, s61, 3\n\t"
      "s_cselect_b32 s62, 16, 2000\n\t"
      "s_cmp_lt_u32 s61, 64\n\t"
      "s_cselect_b32 s62, s62, 0\n\t"
      "s_add_u32 s50, s50, s62\n\t"
      "v_add_f32 v48, v48, v50\n\t"
      "v_add_f32 v49, v49, v51\n\t"
      "ds_write_b64 v124, v[48:49] offset:53832\n\t"
      "v_mfma_f32_16x16x32_bf16 v[32:35], v[76:79], v[112:115], 0\n\t"
      "s_sub_u32 s61, s88, s78\n\t"
      "s_sub_i32 s61, s61, 560\n\t"
      "s_lshr_b32 s62, s25, 3\n\t"
      "v_mfma_f32_16x16x32_bf16 v[36:39], v[80:83], v[112:115], 0\n\t"
      "s_sub_u32 s25, s25, s62\n\t"
      "s_sub_i32 s25, s25, 24\n\t"
      "s_max_i32 s25, s25, 0\n\t"
      "v_mfma_f32_16x16x32_bf16 v[40:43], v[76:79], v[116:119], 0\n\t"
      "s_sub_i32 s62, s61, 96\n\t"
      "s_max_i32 s62, s62, 0\n\t"
      "s_min_i32 s62, s62, 320\n\t"
      "v_mfma_f32_16x16x32_bf16 v[44:47], v[80:83], v[116:119], 0\n\t"
      "s_lshr_b32 s62, s62, 2\n\t"
      "s_add_u32 s25, s25, s62\n\t"
      "s_min_i32 s61, s61, 96\n\t"
      "ds_read_b128 v[76:79], v103\n\t"
      "ds_read_b128 v[80:83], v103 offset:1024\n\t"
      "ds_read_b128 v[112:115], v100 offset:24576\n\t"
      "ds_read_b128 v[116:119], v100 offset:25600\n\t"
      "s_max_i32 s61, s61, -96\n\t"
      "s_ashr_i32 s61, s61, 3\n\t"
      "s_add_u32 s26, s26, s61\n\t"
      "s_max_u32 s26, s26, 1500\n\t"
      "s_min_u32 s26, s26, 2300\n\t"
      "s_sub_u32 s62, s88, s27\n\t"
      "s_mov_b32 s27, s88\n\t"
      "s_sub_i32 s62, s62, s24\n\t"
      "s_cmp_gt_i32 s62, 1150\n\t"
      "s_cselect_b32 s62, 200, 0\n\t"
      "s_max_u32 s25, s25, s62\n\t"
      "s_sub_u32 s61, s76, s28\n\t"
      "s_sub_u32 s62, s80, s82\n\t"
      "s_mov_b32 s28, s76\n\t"
      "s_mov_b32 s82, s80\n\t"
      "s_lshl_b32 s61, s61, 8\n\t"
      "s_mul_i32 s62, s62, s83\n\t"
      "s_sub_i32 s61, s61, s62\n\t"
      "s_ashr_i32 s61, s61, 10\n\t"
      "s_min_i32 s61, s61, 100\n\t"
      "s_max_i32 s61, s61, -100\n\t"
      "s_add_u32 s83, s83, s61\n\t"
      "s_mul_i32 s24, s26, s83\n\t"
      "s_lshr_b32 s24, s24, 12\n\t"
      "s_add_u32 s24, s24, s25\n\t"
      "s_min_u32 s61, s30, 300\n\t"
      "s_sub_u32 s30, s30, s61\n\t"
      "s_sub_u32 s24, s24, s61\n\t"
      "s_mov_b32 s63, 0x2000\n\t"
      "LGATE_3%=:\n\t"
      "s_memtime s[92:93]\n\t"
      "s_waitcnt lgkmcnt(0)\n\t"
      "s_sub_u32 s61, s92, s88\n\t"
      "s_sub_i32 s61, s24, s61\n\t"
      "s_sub_i32 s61, s61, 70\n\t"
      "s_min_i32 s61, s61, 3000\n\t"
      "s_getpc_b64 s[92:93]\n\t"
      "LDPC_3%=:\n\t"
      "s_max_i32 s61, s61, 0\n\t"
      "s_andn2_b32 s62, 28, s61\n\t"
      "s_add_u32 s62, s62, LDSL_3%=-LDPC_3%=\n\t"
      "s_add_u32 s92, s92, s62\n\t"
      "s_addc_u32 s93, s93, 0\n\t"
      "LDLY_3%=:\n\t"
      "s_sub_i32 s61, s61, 32\n\t"
      "s_cmp_ge_i32 s61, 0\n\t"
      "s_cbranch_scc1 LDLY_3%=\n\t"
      "s_setpc_b64 s[92:93]\n\t"
      "LDSL_3%=:\n\t"
      "s_nop 0\n\t"
      "s_nop 0\n\t"
      "s_nop 0\n\t"
      "s_nop 0\n\t"
      "s_nop 0\n\t"
      "s_nop 0\n\t"
      "s_nop 0\n\t"
      "LBAR_3%=:\n\t"
      "s_cmp_gt_u32 s40, 1\n\t"
      "s_barrier\n\t"
      "s_cbranch_scc1 LPOLL_3%=\n\t"
      "s_branch LFIRST_3%=\n\t"
      "LUTOK_0%=:\n\t"
      "v_add_u32 v104, s71, v100\n\t"
      "ds_read_b128 v[76:79], v104\n\t"
      "ds_read_b128 v[80:83], v104 offset:1024\n\t"
      "ds_read_b128 v[112:115], v100\n\t"
      "ds_read_b128 v[116:119], v100 offset:1024\n\t"
      "v_xor_b32 v101, 2048, v104\n\t"
      "v_xor_b32 v102, 4096, v104\n\t"
      "v_xor_b32 v103, 6144, v104\n\t"
      "ds_read_b128 v[16:19], v101\n\t"
      "ds_read_b128 v[20:23], v101 offset:1024\n\t"
      "ds_read_b128 v[24:27], v100 offset:8192\n\t"
      "ds_read_b128 v[28:31], v100 offset:9216\n\t"
      "ds_read_b128 v[0:3], v102\n\t"
      "ds_read_b128 v[4:7], v102 offset:1024\n\t"
      "ds_read_b128 v[8:11], v100 offset:16384\n\t"
      "ds_read_b128 v[12:15], v100 offset:17408\n\t"
      "s_mov_b32 s28, s52\n\t"
      "s_add_u32 m0, s55, s71\n\t"
      "s_sub_u32 s71, 204800, s71\n\t"
      "s_add_u32 s40, s40, 1\n\t"
      "s_cmp_lt_u32 s40, 63\n\t"
      "s_cselect_b32 s61, 786432, 0\n\t"
      "s_cselect_b32 s60, s58, 0\n\t"
      "s_add_u32 s44, s44, s61\n\t"
      "s_cmp_lt_u32 s40, 62\n\t"
      "s_cselect_b32 s61, 786432, 0\n\t"
      "s_add_u32 s46, s46, s61\n\t"
      "s_add_u32 s61, s40, 3\n\t"
      "s_and_b32 s62, s61, 3\n\t"
      "s_cselect_b32 s62, 16, 2000\n\t"
      "s_cmp_lt_u32 s61, 64\n\t"
      "s_cselect_b32 s62, s62, 0\n\t"
      "s_add_u32 s50, s50, s62\n\t"
      "s_mov_b32 s63, 0x2000\n\t"
      "s_waitcnt lgkmcnt(8)\n\t"
      "v_mfma_f32_16x16x32_bf16 v[32:35], v[76:79], v[112:115], 0\n\t"
      "v_mfma_f32_16x16x32_bf16 v[36:39], v[80:83], v[112:115], 0\n\t"
      "v_mfma_f32_16x16x32_bf16 v[40:43], v[76:79], v[116:119], 0\n\t"
      "v_mfma_f32_16x16x32_bf16 v[44:47], v[80:83], v[116:119], 0\n\t"
      "ds_read_b128 v[76:79], v103\n\t"
      "ds_read_b128 v[80:83], v103 offset:1024\n\t"
      "ds_read_b128 v[112:115], v100 offset:24576\n\t"
      "ds_read_b128 v[116:119], v100 offset:25600\n\t"
      "s_waitcnt lgkmcnt(8)\n\t"
      "v_mfma_f32_16x16x32_bf16 v[32:35], v[16:19], v[24:27], v[32:35]\n\t"
      "v_mfma_f32_16x16x32_bf16 v[36:39], v[20:23], v[24:27], v[36:39]\n\t"
      "v_mfma_f32_16x16x32_bf16 v[40:43], v[16:19], v[28:31], v[40:43]\n\t"
      "v_mfma_f32_16x16x32_bf16 v[44:47], v[20:23], v[28:31], v[44:47]\n\t"
      "s_branch LGATE_0%=\n\t"
      "LFIRST_0%=:\n\t"
      "s_cmp_eq_u32 s40, 0\n\t"
      "s_cbranch_scc1 LWM0_0%=\n\t"
      "s_barrier\n\t"
      "ds_read_b32 v99, v109 offset:55392\n\t"
      "s_waitcnt lgkmcnt(0)\n\t"
      "v_readfirstlane_b32 s61, v99\n\t"
      "s_nop 1\n\t"
      "s_cmp_gt_i32 s61, 0\n\t"
      "s_cselect_b32 s99, 0x2000, 0\n\t"
      "s_xor_b32 s97, s97, s99\n\t"
      "s_barrier\n\t"
      "s_mov_b32 s63, 0x2000\n\t"
      "s_branch LPOLL_0%=\n\t"
      "LWM0_0%=:\n\t"
      "s_mov_b32 s63, 0x10000\n\t"
      "LWM_0%=:\n\t"
      "s_waitcnt vmcnt(0) lgkmcnt(0)\n\t"
      "v_bitop3_b32 v49, v49, v51, s53 bitop3:0x7e\n\t"
      "v_max_u32 v48, v48, v50\n\t"
      "v_cmp_ne_u32 vcc, 0, v49\n\t"
      "s_nop 1\n\t"
      "v_max_u32_dpp v48, v48, v48 quad_perm:[1,0,3,2] row_mask:0xf bank_mask:0xf\n\t"
      "s_nop 1\n\t"
      "v_max_u32_dpp v48, v48, v48 quad_perm:[2,3,0,1] row_mask:0xf bank_mask:0xf\n\t"
      "s_nop 1\n\t"
      "v_max_u32_dpp v48, v48, v48 row_ror:4 row_mask:0xf bank_mask:0xf\n\t"
      "s_nop 1\n\t"
      "v_max_u32_dpp v48, v48, v48 row_ror:8 row_mask:0xf bank_mask:0xf\n\t"
      "v_mov_b32 v49, v48\n\t"
      "s_nop 1\n\t"
      "v_permlane16_swap_b32 v48, v49\n\t"
      "v_max_u32 v48, v48, v49\n\t"
      "v_mov_b32 v49, v48\n\t"
      "s_nop 1\n\t"
      "v_permlane32_swap_b32 v48, v49\n\t"
      "v_max_u32 v48, v48, v49\n\t"
      "v_mul_f32 v49, 0x40a00000, v48\n\t"
      "s_nop 0\n\t"
      "v_readfirstlane_b32 s67, v49\n\t"
      "v_mul_f32 v49, 0x43030000, v49\n\t"
      "s_nop 0\n\t"
      "v_readfirstlane_b32 s66, v49\n\t"
      "s_cbranch_vccz LWM1_0%=\n\t"
      "s_sleep 4\n\t"
      "buffer_load_dwordx2 v[48:49], v52, s[36:39], s94 offen sc1\n\t"
      "buffer_load_dwordx2 v[50:51], v52, s[36:39], s94 offen offset:128 sc1\n\t"
      "s_sub_u32 s63, s63, 1\n\t"
      "s_cmp_lg_u32 s63, 0\n\t"
      "s_cbranch_scc1 LWM_0%=\n\t"
      "s_waitcnt vmcnt(0)\n\t"
      "LWM1_0%=:\n\t"
      "buffer_load_dwordx2 v[120:121], off, s[36:39], s64 sc1\n\t"
      "s_lshr_b32 s67, s67, 23\n\t"
      "s_add_u32 s67, s67, 5\n\t"
      "s_lshr_b32 s66, s66, 23\n\t"
      "s_add_u32 s66, s66, 129\n\t"
      "s_max_u32 s66, s66, 80\n\t"
      "s_mov_b32 s63, 0x2000\n\t"
      "LPOL0_0%=:\n\t"
      "s_waitcnt lgkmcnt(4)\n\t"
      "v_mfma_f32_16x16x32_bf16 v[32:35], v[0:3], v[8:11], v[32:35]\n\t"
      "v_mfma_f32_16x16x32_bf16 v[36:39], v[4:7], v[8:11], v[36:39]\n\t"
      "v_mfma_f32_16x16x32_bf16 v[40:43], v[0:3], v[12:15], v[40:43]\n\t"
      "v_mfma_f32_16x16x32_bf16 v[44:47], v[4:7], v[12:15], v[44:47]\n\t"
      "v_mov_b64 v[48:49], 0\n\t"
      "v_mov_b64 v[50:51], 0\n\t"
      "v_mov_b64 v[52:53], 0\n\t"
      "v_mov_b64 v[54:55], 0\n\t"
      "s_xor_b32 s69, s97, 1024\n\t"
      "s_xor_b32 s98, s97, 512\n\t"
      "s_xor_b32 s23, s97, 1536\n\t"
      "s_waitcnt lgkmcnt(0)\n\t"
      "v_mfma_f32_16x16x32_bf16 v[32:35], v[76:79], v[112:115], v[32:35]\n\t"
      "v_mfma_f32_16x16x32_bf16 v[36:39], v[80:83], v[112:115], v[36:39]\n\t"
      "v_mfma_f32_16x16x32_bf16 v[40:43], v[76:79], v[116:119], v[40:43]\n\t"
      "v_mfma_f32_16x16x32_bf16 v[44:47], v[80:83], v[116:119], v[44:47]\n\t"
      "s_mov_b64 exec, 1\n\t"
      "ds_write_b64 v109, v[110:111] offset:55392\n\t"
      "s_mov_b64 exec, -1\n\t"
      "s_nop 4\n\t"
      "v_cvt_pk_bf16_f32 v68, v32, v33\n\t"
      "v_cvt_pk_bf16_f32 v69, v34, v35\n\t"
      "v_cvt_pk_bf16_f32 v70, v36, v37\n\t"
      "v_cvt_pk_bf16_f32 v71, v38, v39\n\t"
      "v_cvt_pk_bf16_f32 v74, v40, v41\n\t"
      "v_cvt_pk_bf16_f32 v75, v42, v43\n\t"
      "v_cvt_pk_bf16_f32 v72, v44, v45\n\t"
      "v_cvt_pk_bf16_f32 v73, v46, v47\n\t"
      "v_mov_b64 v[32:33], 0\n\t"
      "v_mov_b64 v[34:35], 0\n\t"
      "v_mov_b64 v[36:37], 0\n\t"
      "v_mov_b64 v[38:39], 0\n\t"
      "v_mov_b64 v[40:41], 0\n\t"
      "v_mov_b64 v[42:43], 0\n\t"
      "v_mov_b64 v[44:45], 0\n\t"
      "v_mov_b64 v[46:47], 0\n\t"
      "s_waitcnt vmcnt(0)\n\t"
      "s_branch LGO_0%=\n\t"
      "LEARLY_0%=:\n\t"
      "s_cmp_eq_u32 s40, 54\n\t"
      "s_cbranch_scc1 LPW_0%=\n\t"
      "s_cmp_lt_i32 s40, 0\n\t"
      "s_waitcnt lgkmcnt(0)\n\t"
      "s_cbranch_scc1 LUTOK_0%=\n\t"
      "s_xor_b32 s43, s43, s99\n\t"
      "s_xor_b32 s68, s68, s99\n\t"
      "s_branch LROLE_0%=\n\t"
      "LPW_0%=:\n\t"
      "s_and_b32 s61, s51, 0x70c\n\t"
      "s_cmp_lg_u32 s61, 0x100\n\t"
      "s_cbranch_scc1 LROLE_0%=\n\t"
      "s_getpc_b64 s[92:93]\n\t"
      "LPX_0%=:\n\t"
      "s_add_u32 s92, s92, LCOLD%=-LPX_0%=+0\n\t"
      "s_addc_u32 s93, s93, (LCOLD%=-LPX_0%=+0)>>32\n\t"
      "v_lshlrev_b32 v99, 2, v127\n\t"
      "global_load_dword v98, v99, s[92:93]\n\t"
      "v_add_u32 v99, 4096, v99\n\t"
      "global_load_dword v98, v99, s[92:93]\n\t"
      "v_add_u32 v99, 4096, v99\n\t"
      "s_branch LROLE_0%=\n\t"
      "LSLOW_0%=:\n\t"
      "s_mov_b32 s63, 0x10000\n\t"
      "LSLOW1_0%=:\n\t"
      "buffer_load_dwordx2 v[92:93], off, s[36:39], s64 sc1\n\t"
      "s_waitcnt vmcnt(0)\n\t"
      "v_bitop3_b32 v92, v92, v93, s53 bitop3:0x7e\n\t"
      "s_nop 0\n\t"
      "v_readfirstlane_b32 s56, v92\n\t"
      "s_nop 1\n\t"
      "s_cmp_eq_u32 s56, 0\n\t"
      "s_cbranch_scc1 LSLOW2_0%=\n\t"
      "s_sleep 8\n\t"
      "s_sub_u32 s63, s63, 1\n\t"
      "s_cmp_lg_u32 s63, 0\n\t"
      "s_cbranch_scc1 LSLOW1_0%=\n\t"
      "LSLOW2_0%=:\n\t"
      "s_mov_b32 m0, s45\n\t"
      "s_sub_u32 s61, 204800, s71\n\t"
      "buffer_load_dwordx4 v65, s[36:39], s44 offen sc1 lds\n\t"
      "buffer_load_dwordx4 v65, s[36:39], s44 offen offset:1024 sc1 lds\n\t"
      "s_add_u32 m0, s55, s61\n\t"
      "s_nop 0\n\t"
      "buffer_load_dwordx4 v127, s[36:39], s46 offen sc1 lds\n\t"
      "buffer_load_dwordx4 v127, s[36:39], s46 offen offset:1024 sc1 lds\n\t"
      "s_branch LWIN1_0%=\n\t"
      "LMX_0%=:\n\t"
      "s_waitcnt lgkmcnt(0)\n\t"
      "s_nop 7\n\t"
      "v_smfmac_f32_16x16x64_bf16 v[52:55], v[20:23], v[232:239], v122\n\t"
      "v_smfmac_f32_16x16x64_bf16 v[52:55], v[24:27], v[240:247], v122\n\t"
      "v_smfmac_f32_16x16x64_bf16 v[52:55], v[28:31], v[248:255], v122\n\t"
      "s_nop 7\n\t"
      "v_add_f32 v53, v53, v55\n\t"
      "v_add_f32_e64 v52, v52, v54\n\t"
      "v_cvt_pk_bf16_f32 v66, v52, v53\n\t"
      "s_add_u32 s62, s61, 0x83400000\n\t"
      "v_mov_b32_e64 v126, s62\n\t"
      "v_max3_f32 v82, v112, v113, v114\n\t"
      "v_max_f32 v82, v82, v115\n\t"
      "s_nop 1\n\t"
      "v_max_f32_dpp v82, v82, v82 quad_perm:[2,3,0,1] row_mask:0xf bank_mask:0xf\n\t"
      "v_max3_f32 v83, v82, v120, v121\n\t"
      "v_max3_f32 v60, v82, v120, v121\n\t"
      "s_nop 1\n\t"
      "v_permlane16_swap_b32 v83, v60\n\t"
      "v_max_f32 v61, v83, v60\n\t"
      "v_max_f32 v60, v83, v60\n\t"
      "s_nop 1\n\t"
      "v_permlane32_swap_b32 v61, v60\n\t"
      "v_max_f32 v83, v61, v60\n\t"
      "v_sub_f32 v112, v112, v83\n\t"
      "v_sub_f32 v113, v113, v83\n\t"
      "v_sub_f32 v114, v114, v83\n\t"
      "v_sub_f32 v115, v115, v83\n\t"
      "v_sub_f32 v120, v120, v83\n\t"
      "v_sub_f32 v121, v121, v83\n\t"
      "v_exp_f32_e64 v56, v112\n\t"
      "v_exp_f32 v57, v113\n\t"
      "v_exp_f32_e64 v58, v114\n\t"
      "v_exp_f32 v59, v115\n\t"
      "v_exp_f32 v80, v120\n\t"
      "v_exp_f32 v81, v121\n\t"
      "v_cvt_pk_bf16_f32 v96, v56, v57\n\t"
      "v_cvt_pk_bf16_f32 v97, v58, v59\n\t"
      "v_cvt_pk_bf16_f32 v108, v80, v81\n\t"
      "v_mov_b32_dpp v98, v96 quad_perm:[2,3,0,1] row_mask:0xf bank_mask:0xf\n\t"
      "v_mov_b32_dpp v99, v97 quad_perm:[2,3,0,1] row_mask:0xf bank_mask:0xf\n\t"
      "v_mfma_f32_16x16x32_bf16 v[48:51], v[108:111], v[88:91], 0\n\t"
      "s_nop 0\n\t"
      "v_mfma_f32_16x16x32_bf16 v[48:51], v[96:99], v[88:91], v[48:51]\n\t"
      "v_mfma_f32_16x16x32_bf16 v[84:87], v[108:111], v[64:67], 0\n\t"
      "v_mfma_f32_16x16x32_bf16 v[84:87], v[96:99], v[68:71], v[84:87]\n\t"
      "v_mfma_f32_16x16x32_bf16 v[92:95], v[96:99], v[72:75], 0\n\t"
      "v_mfma_f32_16x16x32_bf16 v[92:95], v[108:111], v[66:69], v[92:95]\n\t"
      "s_nop 3\n\t"
      "v_rcp_f32_e64 v48, v48\n\t"
      "v_rcp_f32 v49, v49\n\t"
      "v_fma_f32 v84, v84, v48, s96\n\t"
      "buffer_atomic_add v84, v123, s[36:39], s97 offen\n\t"
      "v_fma_f32 v85, v85, v49, s96\n\t"
      "buffer_atomic_add v85, v123, s[36:39], s98 offen\n\t"
      "v_fma_f32 v94, v94, v48, s96\n\t"
      "buffer_atomic_add v94, v123, s[36:39], s69 offen\n\t"
      "v_fma_f32 v95, v95, v49, s96\n\t"
      "buffer_atomic_add v95, v123, s[36:39], s23 offen\n\t"
      "s_add_u32 s97, s97, 0x8000\n\t"
      "s_branch LWIN_0%=\n\t"
      "LR0_0%=:\n\t"
      "buffer_load_dwordx4 v[84:87], v60, s[36:39], s43 offen sc1\n\t"
      "buffer_load_dwordx4 v[88:91], v60, s[36:39], s43 offen offset:1024 sc1\n\t"
      "s_waitcnt vmcnt(2)\n\t"
      "buffer_load_dwordx4 v[92:95], v60, s[36:39], s68 offen sc1\n\t"
      "buffer_load_dwordx4 v[96:99], v60, s[36:39], s68 offen offset:1024 sc1\n\t"
      "s_sub_u32 s63, s63, 1\n\t"
      "s_mov_b64 exec, 1\n\t"
      "buffer_store_dword v109, off, s[36:39], s52 sc1\n\t"
      "s_mov_b64 exec, -1\n\t"
      "s_waitcnt vmcnt(3) lgkmcnt(0)\n\t"
      "v_bitop3_b32 v64, v84, v85, v86 bitop3:0x80\n\t"
      "v_bitop3_b32 v64, v64, v87, v88 bitop3:0x80\n\t"
      "v_bitop3_b32 v64, v64, v89, v90 bitop3:0x80\n\t"
      "v_bitop3_b32 v64, v64, v91, s58 bitop3:0x80\n\t"
      "v_cmp_eq_u32 vcc, 0, v64\n\t"
      "v_pk_fma_f32 v[4:5], v[84:85], s[56:57], v[126:127] op_sel:[0,1,0] op_sel_hi:[1,1,0]\n\t"
      "v_pk_fma_f32 v[6:7], v[86:87], s[56:57], v[126:127] op_sel:[0,1,0] op_sel_hi:[1,1,0]\n\t"
      "v_cvt_pk_bf16_f32 v0, v4, v5\n\t"
      "v_cvt_pk_bf16_f32 v1, v6, v7\n\t"
      "ds_write_b64 v127, v[0:1] offset:40960\n\t"
      "v_pk_fma_f32 v[8:9], v[88:89], s[56:57], v[126:127] op_sel:[0,1,0] op_sel_hi:[1,1,0]\n\t"
      "v_pk_fma_f32 v[10:11], v[90:91], s[56:57], v[126:127] op_sel:[0,1,0] op_sel_hi:[1,1,0]\n\t"
      "v_cvt_pk_bf16_f32 v2, v8, v9\n\t"
      "v_cvt_pk_bf16_f32 v3, v10, v11\n\t"
      "ds_write_b64 v127, v[2:3] offset:40968\n\t"
      "s_cmp_le_i32 s63, 0\n\t"
      "s_waitcnt lgkmcnt(0)\n\t"
      "s_cbranch_scc1 LRZ_0%=\n\t"
      "s_cbranch_vccnz LR0_0%=\n\t"
      "LRZ_0%=:\n\t"
      "v_mov_b64 v[32:33], 0\n\t"
      "v_mov_b64 v[34:35], 0\n\t"
      "v_mov_b64 v[36:37], 0\n\t"
      "v_mov_b64 v[38:39], 0\n\t"
      "v_mov_b64 v[40:41], 0\n\t"
      "v_mov_b64 v[42:43], 0\n\t"
      "v_mov_b64 v[44:45], 0\n\t"
      "v_mov_b64 v[46:47], 0\n\t"
      "v_mov_b64 v[48:49], 0\n\t"
      "v_mov_b64 v[50:51], 0\n\t"
      "v_mov_b64 v[52:53], 0\n\t"
      "v_mov_b64 v[54:55], 0\n\t"
      "v_mfma_f32_16x16x32_bf16 v[56:59], v[0:3], v[0:3], 0\n\t"
      "v_smfmac_f32_16x16x64_bf16 v[36:39], v[0:3], a[64:71], v122\n\t"
      "v_smfmac_f32_16x16x64_bf16 v[44:47], v[0:3], a[192:199], v122\n\t"
      "s_branch LC0_0%=\n\t"
      "LUTOK_1%=:\n\t"
      "v_add_u32 v104, s71, v100\n\t"
      "ds_read_b128 v[76:79], v104\n\t"
      "ds_read_b128 v[80:83], v104 offset:1024\n\t"
      "ds_read_b128 v[112:115], v100\n\t"
      "ds_read_b128 v[116:119], v100 offset:1024\n\t"
      "v_xor_b32 v101, 2048, v104\n\t"
      "v_xor_b32 v102, 4096, v104\n\t"
      "v_xor_b32 v103, 6144, v104\n\t"
      "ds_read_b128 v[16:19], v101\n\t"
      "ds_read_b128 v[20:23], v101 offset:1024\n\t"
      "ds_read_b128 v[24:27], v100 offset:8192\n\t"
      "ds_read_b128 v[28:31], v100 offset:9216\n\t"
      "ds_read_b128 v[0:3], v102\n\t"
      "ds_read_b128 v[4:7], v102 offset:1024\n\t"
      "ds_read_b128 v[8:11], v100 offset:16384\n\t"
      "ds_read_b128 v[12:15], v100 offset:17408\n\t"
      "s_mov_b32 s28, s52\n\t"
      "s_add_u32 m0, s55, s71\n\t"
      "s_sub_u32 s71, 204800, s71\n\t"
      "s_add_u32 s40, s40, 1\n\t"
      "s_cmp_lt_u32 s40, 63\n\t"
      "s_cselect_b32 s61, 786432, 0\n\t"
      "s_cselect_b32 s60, s58, 0\n\t"
      "s_add_u32 s44, s44, s61\n\t"
      "s_cmp_lt_u32 s40, 62\n\t"
      "s_cselect_b32 s61, 786432, 0\n\t"
      "s_add_u32 s46, s46, s61\n\t"
      "s_add_u32 s61, s40, 3\n\t"
      "s_and_b32 s62, s61, 3\n\t"
      "s_cselect_b32 s62, 16, 2000\n\t"
      "s_cmp_lt_u32 s61, 64\n\t"
      "s_cselect_b32 s62, s62, 0\n\t"
      "s_add_u32 s50, s50, s62\n\t"
      "s_mov_b32 s63, 0x2000\n\t"
      "s_waitcnt lgkmcnt(8)\n\t"
      "v_mfma_f32_16x16x32_bf16 v[32:35], v[76:79], v[112:115], 0\n\t"
      "v_mfma_f32_16x16x32_bf16 v[36:39], v[80:83], v[112:115], 0\n\t"
      "v_mfma_f32_16x16x32_bf16 v[40:43], v[76:79], v[116:119], 0\n\t"
      "v_mfma_f32_16x16x32_bf16 v[44:47], v[80:83], v[116:119], 0\n\t"
      "ds_read_b128 v[76:79], v103\n\t"
      "ds_read_b128 v[80:83], v103 offset:1024\n\t"
      "ds_read_b128 v[112:115], v100 offset:24576\n\t"
      "ds_read_b128 v[116:119], v100 offset:25600\n\t"
      "s_waitcnt lgkmcnt(8)\n\t"
      "v_mfma_f32_16x16x32_bf16 v[32:35], v[16:19], v[24:27], v[32:35]\n\t"
      "v_mfma_f32_16x16x32_bf16 v[36:39], v[20:23], v[24:27], v[36:39]\n\t"
      "v_mfma_f32_16x16x32_bf16 v[40:43], v[16:19], v[28:31], v[40:43]\n\t"
      "v_mfma_f32_16x16x32_bf16 v[44:47], v[20:23], v[28:31], v[44:47]\n\t"
      "s_branch LGATE_1%=\n\t"
      "LFIRST_1%=:\n\t"
      "s_cmp_eq_u32 s40, 0\n\t"
      "s_cbranch_scc1 LWM0_1%=\n\t"
      "s_barrier\n\t"
      "ds_read_b32 v99, v109 offset:55392\n\t"
      "s_waitcnt lgkmcnt(0)\n\t"
      "v_readfirstlane_b32 s61, v99\n\t"
      "s_nop 1\n\t"
      "s_cmp_gt_i32 s61, 0\n\t"
      "s_cselect_b32 s99, 0x2000, 0\n\t"
      "s_xor_b32 s97, s97, s99\n\t"
      "s_barrier\n\t"
      "s_mov_b32 s63, 0x2000\n\t"
      "s_branch LPOLL_1%=\n\t"
      "LWM0_1%=:\n\t"
      "s_mov_b32 s63, 0x10000\n\t"
      "LWM_1%=:\n\t"
      "s_waitcnt vmcnt(0) lgkmcnt(0)\n\t"
      "v_bitop3_b32 v49, v49, v51, s53 bitop3:0x7e\n\t"
      "v_max_u32 v48, v48, v50\n\t"
      "v_cmp_ne_u32 vcc, 0, v49\n\t"
      "s_nop 1\n\t"
      "v_max_u32_dpp v48, v48, v48 quad_perm:[1,0,3,2] row_mask:0xf bank_mask:0xf\n\t"
      "s_nop 1\n\t"
      "v_max_u32_dpp v48, v48, v48 quad_perm:[2,3,0,1] row_mask:0xf bank_mask:0xf\n\t"
      "s_nop 1\n\t"
      "v_max_u32_dpp v48, v48, v48 row_ror:4 row_mask:0xf bank_mask:0xf\n\t"
      "s_nop 1\n\t"
      "v_max_u32_dpp v48, v48, v48 row_ror:8 row_mask:0xf bank_mask:0xf\n\t"
      "v_mov_b32 v49, v48\n\t"
      "s_nop 1\n\t"
      "v_permlane16_swap_b32 v48, v49\n\t"
      "v_max_u32 v48, v48, v49\n\t"
      "v_mov_b32 v49, v48\n\t"
      "s_nop 1\n\t"
      "v_permlane32_swap_b32 v48, v49\n\t"
      "v_max_u32 v48, v48, v49\n\t"
      "v_mul_f32 v49, 0x40a00000, v48\n\t"
      "s_nop 0\n\t"
      "v_readfirstlane_b32 s67, v49\n\t"
      "v_mul_f32 v49, 0x43030000, v49\n\t"
      "s_nop 0\n\t"
      "v_readfirstlane_b32 s66, v49\n\t"
      "s_cbranch_vccz LWM1_1%=\n\t"
      "s_sleep 4\n\t"
      "buffer_load_dwordx2 v[48:49], v52, s[36:39], s94 offen sc1\n\t"
      "buffer_load_dwordx2 v[50:51], v52, s[36:39], s94 offen offset:128 sc1\n\t"
      "s_sub_u32 s63, s63, 1\n\t"
      "s_cmp_lg_u32 s63, 0\n\t"
      "s_cbranch_scc1 LWM_1%=\n\t"
      "s_waitcnt vmcnt(0)\n\t"
      "LWM1_1%=:\n\t"
      "buffer_load_dwordx2 v[120:121], off, s[36:39], s64 sc1\n\t"
      "s_lshr_b32 s67, s67, 23\n\t"
      "s_add_u32 s67, s67, 5\n\t"
      "s_lshr_b32 s66, s66, 23\n\t"
      "s_add_u32 s66, s66, 129\n\t"
      "s_max_u32 s66, s66, 80\n\t"
      "s_mov_b32 s63, 0x2000\n\t"
      "LPOL0_1%=:\n\t"
      "s_waitcnt lgkmcnt(4)\n\t"
      "v_mfma_f32_16x16x32_bf16 v[32:35], v[0:3], v[8:11], v[32:35]\n\t"
      "v_mfma_f32_16x16x32_bf16 v[36:39], v[4:7], v[8:11], v[36:39]\n\t"
      "v_mfma_f32_16x16x32_bf16 v[40:43], v[0:3], v[12:15], v[40:43]\n\t"
      "v_mfma_f32_16x16x32_bf16 v[44:47], v[4:7], v[12:15], v[44:47]\n\t"
      "v_mov_b64 v[48:49], 0\n\t"
      "v_mov_b64 v[50:51], 0\n\t"
      "v_mov_b64 v[52:53], 0\n\t"
      "v_mov_b64 v[54:55], 0\n\t"
      "s_xor_b32 s69, s97, 1024\n\t"
      "s_xor_b32 s98, s97, 512\n\t"
      "s_xor_b32 s23, s97, 1536\n\t"
      "s_waitcnt lgkmcnt(0)\n\t"
      "v_mfma_f32_16x16x32_bf16 v[32:35], v[76:79], v[112:115], v[32:35]\n\t"
      "v_mfma_f32_16x16x32_bf16 v[36:39], v[80:83], v[112:115], v[36:39]\n\t"
      "v_mfma_f32_16x16x32_bf16 v[40:43], v[76:79], v[116:119], v[40:43]\n\t"
      "v_mfma_f32_16x16x32_bf16 v[44:47], v[80:83], v[116:119], v[44:47]\n\t"
      "s_mov_b64 exec, 1\n\t"
      "ds_write_b64 v109, v[110:111] offset:55392\n\t"
      "s_mov_b64 exec, -1\n\t"
      "s_nop 4\n\t"
      "v_cvt_pk_bf16_f32 v68, v32, v33\n\t"
      "v_cvt_pk_bf16_f32 v69, v34, v35\n\t"
      "v_cvt_pk_bf16_f32 v70, v36, v37\n\t"
      "v_cvt_pk_bf16_f32 v71, v38, v39\n\t"
      "v_cvt_pk_bf16_f32 v74, v40, v41\n\t"
      "v_cvt_pk_bf16_f32 v75, v42, v43\n\t"
      "v_cvt_pk_bf16_f32 v72, v44, v45\n\t"
      "v_cvt_pk_bf16_f32 v73, v46, v47\n\t"
      "v_mov_b64 v[32:33], 0\n\t"
      "v_mov_b64 v[34:35], 0\n\t"
      "v_mov_b64 v[36:37], 0\n\t"
      "v_mov_b64 v[38:39], 0\n\t"
      "v_mov_b64 v[40:41], 0\n\t"
      "v_mov_b64 v[42:43], 0\n\t"
      "v_mov_b64 v[44:45], 0\n\t"
      "v_mov_b64 v[46:47], 0\n\t"
      "s_waitcnt vmcnt(0)\n\t"
      "s_branch LGO_1%=\n\t"
      "LEARLY_1%=:\n\t"
      "s_cmp_eq_u32 s40, 54\n\t"
      "s_cbranch_scc1 LROLE_1%=\n\t"
      "s_cmp_lt_i32 s40, 0\n\t"
      "s_waitcnt lgkmcnt(0)\n\t"
      "s_cbranch_scc1 LUTOK_1%=\n\t"
      "s_xor_b32 s43, s43, s99\n\t"
      "s_xor_b32 s68, s68, s99\n\t"
      "s_branch LROLE_1%=\n\t"
      "LSLOW_1%=:\n\t"
      "s_mov_b32 s63, 0x10000\n\t"
      "LSLOW1_1%=:\n\t"
      "buffer_load_dwordx2 v[92:93], off, s[36:39], s64 sc1\n\t"
      "s_waitcnt vmcnt(0)\n\t"
      "v_bitop3_b32 v92, v92, v93, s53 bitop3:0x7e\n\t"
      "s_nop 0\n\t"
      "v_readfirstlane_b32 s56, v92\n\t"
      "s_nop 1\n\t"
      "s_cmp_eq_u32 s56, 0\n\t"
      "s_cbranch_scc1 LSLOW2_1%=\n\t"
      "s_sleep 8\n\t"
      "s_sub_u32 s63, s63, 1\n\t"
      "s_cmp_lg_u32 s63, 0\n\t"
      "s_cbranch_scc1 LSLOW1_1%=\n\t"
      "LSLOW2_1%=:\n\t"
      "s_mov_b32 m0, s45\n\t"
      "s_sub_u32 s61, 204800, s71\n\t"
      "buffer_load_dwordx4 v65, s[36:39], s44 offen sc1 lds\n\t"
      "buffer_load_dwordx4 v65, s[36:39], s44 offen offset:1024 sc1 lds\n\t"
      "s_add_u32 m0, s55, s61\n\t"
      "s_nop 0\n\t"
      "buffer_load_dwordx4 v127, s[36:39], s46 offen sc1 lds\n\t"
      "buffer_load_dwordx4 v127, s[36:39], s46 offen offset:1024 sc1 lds\n\t"
      "s_branch LWIN1_1%=\n\t"
      "LMX_1%=:\n\t"
      "s_waitcnt lgkmcnt(0)\n\t"
      "s_nop 7\n\t"
      "v_smfmac_f32_16x16x64_bf16 v[52:55], v[16:19], v[224:231], v122\n\t"
      "v_smfmac_f32_16x16x64_bf16 v[52:55], v[28:31], v[248:255], v122\n\t"
      "v_smfmac_f32_16x16x64_bf16 v[52:55], v[24:27], v[240:247], v122\n\t"
      "s_nop 7\n\t"
      "v_add_f32 v53, v53, v55\n\t"
      "v_add_f32_e64 v52, v52, v54\n\t"
      "v_cvt_pk_bf16_f32 v66, v52, v53\n\t"
      "s_add_u32 s62, s61, 0x83400000\n\t"
      "v_mov_b32_e64 v126, s62\n\t"
      "v_max3_f32 v82, v112, v113, v114\n\t"
      "v_max_f32 v82, v82, v115\n\t"
      "s_nop 1\n\t"
      "v_max_f32_dpp v82, v82, v82 quad_perm:[2,3,0,1] row_mask:0xf bank_mask:0xf\n\t"
      "v_max3_f32 v83, v82, v120, v121\n\t"
      "v_max3_f32 v60, v82, v120, v121\n\t"
      "s_nop 1\n\t"
      "v_permlane16_swap_b32 v83, v60\n\t"
      "v_max_f32 v61, v83, v60\n\t"
      "v_max_f32 v60, v83, v60\n\t"
      "s_nop 1\n\t"
      "v_permlane32_swap_b32 v61, v60\n\t"
      "v_max_f32 v83, v61, v60\n\t"
      "v_sub_f32 v112, v112, v83\n\t"
      "v_sub_f32 v113, v113, v83\n\t"
      "v_sub_f32 v114, v114, v83\n\t"
      "v_sub_f32 v115, v115, v83\n\t"
      "v_sub_f32 v120, v120, v83\n\t"
      "v_sub_f32 v121, v121, v83\n\t"
      "v_exp_f32_e64 v56, v112\n\t"
      "v_exp_f32 v57, v113\n\t"
      "v_exp_f32_e64 v58, v114\n\t"
      "v_exp_f32 v59, v115\n\t"
      "v_exp_f32 v80, v120\n\t"
      "v_exp_f32 v81, v121\n\t"
      "v_cvt_pk_bf16_f32 v96, v56, v57\n\t"
      "v_cvt_pk_bf16_f32 v97, v58, v59\n\t"
      "v_cvt_pk_bf16_f32 v108, v80, v81\n\t"
      "v_mov_b32_dpp v98, v96 quad_perm:[2,3,0,1] row_mask:0xf bank_mask:0xf\n\t"
      "v_mov_b32_dpp v99, v97 quad_perm:[2,3,0,1] row_mask:0xf bank_mask:0xf\n\t"
      "v_mfma_f32_16x16x32_bf16 v[48:51], v[108:111], v[88:91], 0\n\t"
      "s_nop 0\n\t"
      "v_mfma_f32_16x16x32_bf16 v[48:51], v[96:99], v[88:91], v[48:51]\n\t"
      "v_mfma_f32_16x16x32_bf16 v[84:87], v[108:111], v[64:67], 0\n\t"
      "v_mfma_f32_16x16x32_bf16 v[84:87], v[96:99], v[68:71], v[84:87]\n\t"
      "v_mfma_f32_16x16x32_bf16 v[92:95], v[96:99], v[72:75], 0\n\t"
      "v_mfma_f32_16x16x32_bf16 v[92:95], v[108:111], v[66:69], v[92:95]\n\t"
      "s_nop 3\n\t"
      "v_rcp_f32_e64 v48, v48\n\t"
      "v_rcp_f32 v49, v49\n\t"
      "v_fma_f32 v84, v84, v48, s96\n\t"
      "buffer_atomic_add v84, v123, s[36:39], s97 offen\n\t"
      "v_fma_f32 v85, v85, v49, s96\n\t"
      "buffer_atomic_add v85, v123, s[36:39], s98 offen\n\t"
      "v_fma_f32 v94, v94, v48, s96\n\t"
      "buffer_atomic_add v94, v123, s[36:39], s69 offen\n\t"
      "v_fma_f32 v95, v95, v49, s96\n\t"
      "buffer_atomic_add v95, v123, s[36:39], s23 offen\n\t"
      "s_add_u32 s97, s97, 0x8000\n\t"
      "s_branch LWIN_1%=\n\t"
      "LR0_1%=:\n\t"
      "buffer_load_dwordx4 v[84:87], v60, s[36:39], s43 offen sc1\n\t"
      "buffer_load_dwordx4 v[88:91], v60, s[36:39], s43 offen offset:1024 sc1\n\t"
      "s_waitcnt vmcnt(2)\n\t"
      "buffer_load_dwordx4 v[92:95], v60, s[36:39], s68 offen sc1\n\t"
      "buffer_load_dwordx4 v[96:99], v60, s[36:39], s68 offen offset:1024 sc1\n\t"
      "s_sub_u32 s63, s63, 1\n\t"
      "s_mov_b64 exec, 1\n\t"
      "buffer_store_dword v109, off, s[36:39], s52 sc1\n\t"
      "s_mov_b64 exec, -1\n\t"
      "s_waitcnt vmcnt(3) lgkmcnt(0)\n\t"
      "v_bitop3_b32 v64, v84, v85, v86 bitop3:0x80\n\t"
      "v_bitop3_b32 v64, v64, v87, v88 bitop3:0x80\n\t"
      "v_bitop3_b32 v64, v64, v89, v90 bitop3:0x80\n\t"
      "v_bitop3_b32 v64, v64, v91, s58 bitop3:0x80\n\t"
      "v_cmp_eq_u32 vcc, 0, v64\n\t"
      "v_pk_fma_f32 v[0:1], v[84:85], s[56:57], v[126:127] op_sel:[0,1,0] op_sel_hi:[1,1,0]\n\t"
      "v_pk_fma_f32 v[2:3], v[86:87], s[56:57], v[126:127] op_sel:[0,1,0] op_sel_hi:[1,1,0]\n\t"
      "v_cvt_pk_bf16_f32 v4, v0, v1\n\t"
      "v_cvt_pk_bf16_f32 v5, v2, v3\n\t"
      "ds_write_b64 v127, v[4:5] offset:41984\n\t"
      "v_pk_fma_f32 v[12:13], v[88:89], s[56:57], v[126:127] op_sel:[0,1,0] op_sel_hi:[1,1,0]\n\t"
      "v_pk_fma_f32 v[14:15], v[90:91], s[56:57], v[126:127] op_sel:[0,1,0] op_sel_hi:[1,1,0]\n\t"
      "v_cvt_pk_bf16_f32 v6, v12, v13\n\t"
      "v_cvt_pk_bf16_f32 v7, v14, v15\n\t"
      "ds_write_b64 v127, v[6:7] offset:41992\n\t"
      "s_cmp_le_i32 s63, 0\n\t"
      "s_waitcnt lgkmcnt(0)\n\t"
      "s_cbranch_scc1 LRZ_1%=\n\t"
      "s_cbranch_vccnz LR0_1%=\n\t"
      "LRZ_1%=:\n\t"
      "v_mov_b64 v[32:33], 0\n\t"
      "v_mov_b64 v[34:35], 0\n\t"
      "v_mov_b64 v[36:37], 0\n\t"
      "v_mov_b64 v[38:39], 0\n\t"
      "v_mov_b64 v[40:41], 0\n\t"
      "v_mov_b64 v[42:43], 0\n\t"
      "v_mov_b64 v[44:45], 0\n\t"
      "v_mov_b64 v[46:47], 0\n\t"
      "v_mov_b64 v[48:49], 0\n\t"
      "v_mov_b64 v[50:51], 0\n\t"
      "v_mov_b64 v[52:53], 0\n\t"
      "v_mov_b64 v[54:55], 0\n\t"
      "v_mfma_f32_16x16x32_bf16 v[56:59], v[4:7], v[4:7], 0\n\t"
      "v_smfmac_f32_16x16x64_bf16 v[36:39], v[4:7], a[72:79], v122\n\t"
      "v_smfmac_f32_16x16x64_bf16 v[44:47], v[4:7], a[200:207], v122\n\t"
      "s_branch LC0_1%=\n\t"
      "LUTOK_2%=:\n\t"
      "v_add_u32 v104, s71, v100\n\t"
      "ds_read_b128 v[76:79], v104\n\t"
      "ds_read_b128 v[80:83], v104 offset:1024\n\t"
      "ds_read_b128 v[112:115], v100\n\t"
      "ds_read_b128 v[116:119], v100 offset:1024\n\t"
      "v_xor_b32 v101, 2048, v104\n\t"
      "v_xor_b32 v102, 4096, v104\n\t"
      "v_xor_b32 v103, 6144, v104\n\t"
      "ds_read_b128 v[0:3], v101\n\t"
      "ds_read_b128 v[4:7], v101 offset:1024\n\t"
      "ds_read_b128 v[8:11], v100 offset:8192\n\t"
      "ds_read_b128 v[12:15], v100 offset:9216\n\t"
      "ds_read_b128 v[16:19], v102\n\t"
      "ds_read_b128 v[20:23], v102 offset:1024\n\t"
      "ds_read_b128 v[24:27], v100 offset:16384\n\t"
      "ds_read_b128 v[28:31], v100 offset:17408\n\t"
      "s_add_u32 m0, s55, s71\n\t"
      "s_sub_u32 s71, 204800, s71\n\t"
      "s_add_u32 s40, s40, 1\n\t"
      "s_cmp_lt_u32 s40, 63\n\t"
      "s_cselect_b32 s61, 786432, 0\n\t"
      "s_cselect_b32 s60, s58, 0\n\t"
      "s_add_u32 s44, s44, s61\n\t"
      "s_cmp_lt_u32 s40, 62\n\t"
      "s_cselect_b32 s61, 786432, 0\n\t"
      "s_add_u32 s46, s46, s61\n\t"
      "s_add_u32 s61, s40, 3\n\t"
      "s_and_b32 s62, s61, 3\n\t"
      "s_cselect_b32 s62, 16, 2000\n\t"
      "s_cmp_lt_u32 s61, 64\n\t"
      "s_cselect_b32 s62, s62, 0\n\t"
      "s_add_u32 s50, s50, s62\n\t"
      "s_mov_b32 s63, 0x2000\n\t"
      "s_waitcnt lgkmcnt(8)\n\t"
      "v_mfma_f32_16x16x32_bf16 v[32:35], v[76:79], v[112:115], 0\n\t"
      "v_mfma_f32_16x16x32_bf16 v[36:39], v[80:83], v[112:115], 0\n\t"
      "v_mfma_f32_16x16x32_bf16 v[40:43], v[76:79], v[116:119], 0\n\t"
      "v_mfma_f32_16x16x32_bf16 v[44:47], v[80:83], v[116:119], 0\n\t"
      "ds_read_b128 v[76:79], v103\n\t"
      "ds_read_b128 v[80:83], v103 offset:1024\n\t"
      "ds_read_b128 v[112:115], v100 offset:24576\n\t"
      "ds_read_b128 v[116:119], v100 offset:25600\n\t"
      "s_branch LGATE_2%=\n\t"
      "LFIRST_2%=:\n\t"
      "s_cmp_eq_u32 s40, 0\n\t"
      "s_cbranch_scc1 LWM0_2%=\n\t"
      "s_barrier\n\t"
      "ds_read_b32 v99, v109 offset:55392\n\t"
      "s_waitcnt lgkmcnt(0)\n\t"
      "v_readfirstlane_b32 s61, v99\n\t"
      "s_nop 1\n\t"
      "s_cmp_gt_i32 s61, 0\n\t"
      "s_cselect_b32 s99, 0x2000, 0\n\t"
      "s_xor_b32 s97, s97, s99\n\t"
      "s_barrier\n\t"
      "s_mov_b32 s63, 0x2000\n\t"
      "s_branch LPOLL_2%=\n\t"
      "LWM0_2%=:\n\t"
      "s_mov_b32 s63, 0x10000\n\t"
      "LWM_2%=:\n\t"
      "s_waitcnt vmcnt(0) lgkmcnt(0)\n\t"
      "v_bitop3_b32 v49, v49, v51, s53 bitop3:0x7e\n\t"
      "v_max_u32 v48, v48, v50\n\t"
      "v_cmp_ne_u32 vcc, 0, v49\n\t"
      "s_nop 1\n\t"
      "v_max_u32_dpp v48, v48, v48 quad_perm:[1,0,3,2] row_mask:0xf bank_mask:0xf\n\t"
      "s_nop 1\n\t"
      "v_max_u32_dpp v48, v48, v48 quad_perm:[2,3,0,1] row_mask:0xf bank_mask:0xf\n\t"
      "s_nop 1\n\t"
      "v_max_u32_dpp v48, v48, v48 row_ror:4 row_mask:0xf bank_mask:0xf\n\t"
      "s_nop 1\n\t"
      "v_max_u32_dpp v48, v48, v48 row_ror:8 row_mask:0xf bank_mask:0xf\n\t"
      "v_mov_b32 v49, v48\n\t"
      "s_nop 1\n\t"
      "v_permlane16_swap_b32 v48, v49\n\t"
      "v_max_u32 v48, v48, v49\n\t"
      "v_mov_b32 v49, v48\n\t"
      "s_nop 1\n\t"
      "v_permlane32_swap_b32 v48, v49\n\t"
      "v_max_u32 v48, v48, v49\n\t"
      "v_mul_f32 v49, 0x40a00000, v48\n\t"
      "s_nop 0\n\t"
      "v_readfirstlane_b32 s67, v49\n\t"
      "v_mul_f32 v49, 0x43030000, v49\n\t"
      "s_nop 0\n\t"
      "v_readfirstlane_b32 s66, v49\n\t"
      "s_cbranch_vccz LWM1_2%=\n\t"
      "s_sleep 4\n\t"
      "buffer_load_dwordx2 v[48:49], v52, s[36:39], s94 offen sc1\n\t"
      "buffer_load_dwordx2 v[50:51], v52, s[36:39], s94 offen offset:128 sc1\n\t"
      "s_sub_u32 s63, s63, 1\n\t"
      "s_cmp_lg_u32 s63, 0\n\t"
      "s_cbranch_scc1 LWM_2%=\n\t"
      "s_waitcnt vmcnt(0)\n\t"
      "LWM1_2%=:\n\t"
      "buffer_load_dwordx2 v[120:121], off, s[36:39], s64 sc1\n\t"
      "s_lshr_b32 s67, s67, 23\n\t"
      "s_add_u32 s67, s67, 5\n\t"
      "s_lshr_b32 s66, s66, 23\n\t"
      "s_add_u32 s66, s66, 129\n\t"
      "s_max_u32 s66, s66, 80\n\t"
      "s_mov_b32 s63, 0x2000\n\t"
      "LPOL0_2%=:\n\t"
      "s_waitcnt lgkmcnt(8)\n\t"
      "v_mfma_f32_16x16x32_bf16 v[32:35], v[0:3], v[8:11], v[32:35]\n\t"
      "v_mfma_f32_16x16x32_bf16 v[36:39], v[4:7], v[8:11], v[36:39]\n\t"
      "v_mfma_f32_16x16x32_bf16 v[40:43], v[0:3], v[12:15], v[40:43]\n\t"
      "v_mfma_f32_16x16x32_bf16 v[44:47], v[4:7], v[12:15], v[44:47]\n\t"
      "v_mov_b64 v[48:49], 0\n\t"
      "v_mov_b64 v[50:51], 0\n\t"
      "v_mov_b64 v[52:53], 0\n\t"
      "v_mov_b64 v[54:55], 0\n\t"
      "s_xor_b32 s69, s97, 1024\n\t"
      "s_xor_b32 s98, s97, 512\n\t"
      "s_xor_b32 s23, s97, 1536\n\t"
      "s_waitcnt lgkmcnt(4)\n\t"
      "v_mfma_f32_16x16x32_bf16 v[32:35], v[16:19], v[24:27], v[32:35]\n\t"
      "v_mfma_f32_16x16x32_bf16 v[36:39], v[20:23], v[24:27], v[36:39]\n\t"
      "v_mfma_f32_16x16x32_bf16 v[40:43], v[16:19], v[28:31], v[40:43]\n\t"
      "v_mfma_f32_16x16x32_bf16 v[44:47], v[20:23], v[28:31], v[44:47]\n\t"
      "s_waitcnt lgkmcnt(0)\n\t"
      "v_mfma_f32_16x16x32_bf16 v[32:35], v[76:79], v[112:115], v[32:35]\n\t"
      "v_mfma_f32_16x16x32_bf16 v[36:39], v[80:83], v[112:115], v[36:39]\n\t"
      "v_mfma_f32_16x16x32_bf16 v[40:43], v[76:79], v[116:119], v[40:43]\n\t"
      "v_mfma_f32_16x16x32_bf16 v[44:47], v[80:83], v[116:119], v[44:47]\n\t"
      "s_mov_b64 exec, 1\n\t"
      "ds_write_b64 v109, v[110:111] offset:55392\n\t"
      "s_mov_b64 exec, -1\n\t"
      "s_nop 4\n\t"
      "v_cvt_pk_bf16_f32 v68, v32, v33\n\t"
      "v_cvt_pk_bf16_f32 v69, v34, v35\n\t"
      "v_cvt_pk_bf16_f32 v70, v36, v37\n\t"
      "v_cvt_pk_bf16_f32 v71, v38, v39\n\t"
      "v_cvt_pk_bf16_f32 v74, v40, v41\n\t"
      "v_cvt_pk_bf16_f32 v75, v42, v43\n\t"
      "v_cvt_pk_bf16_f32 v72, v44, v45\n\t"
      "v_cvt_pk_bf16_f32 v73, v46, v47\n\t"
      "v_mov_b64 v[32:33], 0\n\t"
      "v_mov_b64 v[34:35], 0\n\t"
      "v_mov_b64 v[36:37], 0\n\t"
      "v_mov_b64 v[38:39], 0\n\t"
      "v_mov_b64 v[40:41], 0\n\t"
      "v_mov_b64 v[42:43], 0\n\t"
      "v_mov_b64 v[44:45], 0\n\t"
      "v_mov_b64 v[46:47], 0\n\t"
      "s_waitcnt vmcnt(0)\n\t"
      "s_branch LGO_2%=\n\t"
      "LEARLY_2%=:\n\t"
      "s_cmp_eq_u32 s40, 54\n\t"
      "s_cbranch_scc1 LROLE_2%=\n\t"
      "s_cmp_lt_i32 s40, 0\n\t"
      "s_waitcnt lgkmcnt(0)\n\t"
      "s_cbranch_scc1 LUTOK_2%=\n\t"
      "s_xor_b32 s43, s43, s99\n\t"
      "s_xor_b32 s68, s68, s99\n\t"
      "s_branch LROLE_2%=\n\t"
      "LSLOW_2%=:\n\t"
      "s_mov_b32 s63, 0x10000\n\t"
      "LSLOW1_2%=:\n\t"
      "buffer_load_dwordx2 v[92:93], off, s[36:39], s64 sc1\n\t"
      "s_waitcnt vmcnt(0)\n\t"
      "v_bitop3_b32 v92, v92, v93, s53 bitop3:0x7e\n\t"
      "s_nop 0\n\t"
      "v_readfirstlane_b32 s56, v92\n\t"
      "s_nop 1\n\t"
      "s_cmp_eq_u32 s56, 0\n\t"
      "s_cbranch_scc1 LSLOW2_2%=\n\t"
      "s_sleep 8\n\t"
      "s_sub_u32 s63, s63, 1\n\t"
      "s_cmp_lg_u32 s63, 0\n\t"
      "s_cbranch_scc1 LSLOW1_2%=\n\t"
      "LSLOW2_2%=:\n\t"
      "s_mov_b32 m0, s45\n\t"
      "s_sub_u32 s61, 204800, s71\n\t"
      "buffer_load_dwordx4 v65, s[36:39], s44 offen sc1 lds\n\t"
      "buffer_load_dwordx4 v65, s[36:39], s44 offen offset:1024 sc1 lds\n\t"
      "s_add_u32 m0, s55, s61\n\t"
      "s_nop 0\n\t"
      "buffer_load_dwordx4 v127, s[36:39], s46 offen sc1 lds\n\t"
      "buffer_load_dwordx4 v127, s[36:39], s46 offen offset:1024 sc1 lds\n\t"
      "s_branch LWIN1_2%=\n\t"
      "LMX_2%=:\n\t"
      "s_waitcnt lgkmcnt(0)\n\t"
      "s_nop 7\n\t"
      "v_smfmac_f32_16x16x64_bf16 v[52:55], v[28:31], v[248:255], v122\n\t"
      "v_smfmac_f32_16x16x64_bf16 v[52:55], v[16:19], v[224:231], v122\n\t"
      "v_smfmac_f32_16x16x64_bf16 v[52:55], v[20:23], v[232:239], v122\n\t"
      "s_nop 7\n\t"
      "v_add_f32 v53, v53, v55\n\t"
      "v_add_f32_e64 v52, v52, v54\n\t"
      "v_cvt_pk_bf16_f32 v66, v52, v53\n\t"
      "s_add_u32 s62, s61, 0x83400000\n\t"
      "v_mov_b32_e64 v126, s62\n\t"
      "v_max3_f32 v82, v112, v113, v114\n\t"
      "v_max_f32 v82, v82, v115\n\t"
      "s_nop 1\n\t"
      "v_max_f32_dpp v82, v82, v82 quad_perm:[2,3,0,1] row_mask:0xf bank_mask:0xf\n\t"
      "v_max3_f32 v83, v82, v120, v121\n\t"
      "v_max3_f32 v60, v82, v120, v121\n\t"
      "s_nop 1\n\t"
      "v_permlane16_swap_b32 v83, v60\n\t"
      "v_max_f32 v61, v83, v60\n\t"
      "v_max_f32 v60, v83, v60\n\t"
      "s_nop 1\n\t"
      "v_permlane32_swap_b32 v61, v60\n\t"
      "v_max_f32 v83, v61, v60\n\t"
      "v_sub_f32 v112, v112, v83\n\t"
      "v_sub_f32 v113, v113, v83\n\t"
      "v_sub_f32 v114, v114, v83\n\t"
      "v_sub_f32 v115, v115, v83\n\t"
      "v_sub_f32 v120, v120, v83\n\t"
      "v_sub_f32 v121, v121, v83\n\t"
      "v_exp_f32_e64 v56, v112\n\t"
      "v_exp_f32 v57, v113\n\t"
      "v_exp_f32_e64 v58, v114\n\t"
      "v_exp_f32 v59, v115\n\t"
      "v_exp_f32 v80, v120\n\t"
      "v_exp_f32 v81, v121\n\t"
      "v_cvt_pk_bf16_f32 v96, v56, v57\n\t"
      "v_cvt_pk_bf16_f32 v97, v58, v59\n\t"
      "v_cvt_pk_bf16_f32 v108, v80, v81\n\t"
      "v_mov_b32_dpp v98, v96 quad_perm:[2,3,0,1] row_mask:0xf bank_mask:0xf\n\t"
      "v_mov_b32_dpp v99, v97 quad_perm:[2,3,0,1] row_mask:0xf bank_mask:0xf\n\t"
      "v_mfma_f32_16x16x32_bf16 v[48:51], v[108:111], v[88:91], 0\n\t"
      "s_nop 0\n\t"
      "v_mfma_f32_16x16x32_bf16 v[48:51], v[96:99], v[88:91], v[48:51]\n\t"
      "v_mfma_f32_16x16x32_bf16 v[84:87], v[108:111], v[64:67], 0\n\t"
      "v_mfma_f32_16x16x32_bf16 v[84:87], v[96:99], v[68:71], v[84:87]\n\t"
      "v_mfma_f32_16x16x32_bf16 v[92:95], v[96:99], v[72:75], 0\n\t"
      "v_mfma_f32_16x16x32_bf16 v[92:95], v[108:111], v[66:69], v[92:95]\n\t"
      "s_nop 3\n\t"
      "v_rcp_f32_e64 v48, v48\n\t"
      "v_rcp_f32 v49, v49\n\t"
      "v_fma_f32 v84, v84, v48, s96\n\t"
      "buffer_atomic_add v84, v123, s[36:39], s97 offen\n\t"
      "v_fma_f32 v85, v85, v49, s96\n\t"
      "buffer_atomic_add v85, v123, s[36:39], s98 offen\n\t"
      "v_fma_f32 v94, v94, v48, s96\n\t"
      "buffer_atomic_add v94, v123, s[36:39], s69 offen\n\t"
      "v_fma_f32 v95, v95, v49, s96\n\t"
      "buffer_atomic_add v95, v123, s[36:39], s23 offen\n\t"
      "s_add_u32 s97, s97, 0x8000\n\t"
      "s_branch LWIN_2%=\n\t"
      "LR0_2%=:\n\t"
      "buffer_load_dwordx4 v[84:87], v60, s[36:39], s43 offen sc1\n\t"
      "buffer_load_dwordx4 v[88:91], v60, s[36:39], s43 offen offset:1024 sc1\n\t"
      "s_waitcnt vmcnt(2)\n\t"
      "buffer_load_dwordx4 v[92:95], v60, s[36:39], s68 offen sc1\n\t"
      "buffer_load_dwordx4 v[96:99], v60, s[36:39], s68 offen offset:1024 sc1\n\t"
      "s_sub_u32 s63, s63, 1\n\t"
      "s_waitcnt vmcnt(2) lgkmcnt(0)\n\t"
      "v_bitop3_b32 v64, v84, v85, v86 bitop3:0x80\n\t"
      "v_bitop3_b32 v64, v64, v87, v88 bitop3:0x80\n\t"
      "v_bitop3_b32 v64, v64, v89, v90 bitop3:0x80\n\t"
      "v_bitop3_b32 v64, v64, v91, s58 bitop3:0x80\n\t"
      "v_cmp_eq_u32 vcc, 0, v64\n\t"
      "v_pk_fma_f32 v[12:13], v[84:85], s[56:57], v[126:127] op_sel:[0,1,0] op_sel_hi:[1,1,0]\n\t"
      "v_pk_fma_f32 v[14:15], v[86:87], s[56:57], v[126:127] op_sel:[0,1,0] op_sel_hi:[1,1,0]\n\t"
      "v_cvt_pk_bf16_f32 v8, v12, v13\n\t"
      "v_cvt_pk_bf16_f32 v9, v14, v15\n\t"
      "ds_write_b64 v127, v[8:9] offset:43008\n\t"
      "v_pk_fma_f32 v[0:1], v[88:89], s[56:57], v[126:127] op_sel:[0,1,0] op_sel_hi:[1,1,0]\n\t"
      "v_pk_fma_f32 v[2:3], v[90:91], s[56:57], v[126:127] op_sel:[0,1,0] op_sel_hi:[1,1,0]\n\t"
      "v_cvt_pk_bf16_f32 v10, v0, v1\n\t"
      "v_cvt_pk_bf16_f32 v11, v2, v3\n\t"
      "ds_write_b64 v127, v[10:11] offset:43016\n\t"
      "s_cmp_le_i32 s63, 0\n\t"
      "s_waitcnt lgkmcnt(0)\n\t"
      "s_cbranch_scc1 LRZ_2%=\n\t"
      "s_cbranch_vccnz LR0_2%=\n\t"
      "LRZ_2%=:\n\t"
      "v_mov_b64 v[32:33], 0\n\t"
      "v_mov_b64 v[34:35], 0\n\t"
      "v_mov_b64 v[36:37], 0\n\t"
      "v_mov_b64 v[38:39], 0\n\t"
      "v_mov_b64 v[40:41], 0\n\t"
      "v_mov_b64 v[42:43], 0\n\t"
      "v_mov_b64 v[44:45], 0\n\t"
      "v_mov_b64 v[46:47], 0\n\t"
      "v_mov_b64 v[48:49], 0\n\t"
      "v_mov_b64 v[50:51], 0\n\t"
      "v_mov_b64 v[52:53], 0\n\t"
      "v_mov_b64 v[54:55], 0\n\t"
      "v_mfma_f32_16x16x32_bf16 v[56:59], v[8:11], v[8:11], 0\n\t"
      "v_smfmac_f32_16x16x64_bf16 v[36:39], v[8:11], a[80:87], v122\n\t"
      "v_smfmac_f32_16x16x64_bf16 v[44:47], v[8:11], a[208:215], v122\n\t"
      "s_branch LC0_2%=\n\t"
      "LUTOK_3%=:\n\t"
      "v_add_u32 v104, s71, v100\n\t"
      "ds_read_b128 v[76:79], v104\n\t"
      "ds_read_b128 v[80:83], v104 offset:1024\n\t"
      "ds_read_b128 v[112:115], v100\n\t"
      "ds_read_b128 v[116:119], v100 offset:1024\n\t"
      "v_xor_b32 v101, 2048, v104\n\t"
      "v_xor_b32 v102, 4096, v104\n\t"
      "v_xor_b32 v103, 6144, v104\n\t"
      "ds_read_b128 v[0:3], v101\n\t"
      "ds_read_b128 v[4:7], v101 offset:1024\n\t"
      "ds_read_b128 v[8:11], v100 offset:8192\n\t"
      "ds_read_b128 v[12:15], v100 offset:9216\n\t"
      "ds_read_b128 v[16:19], v102\n\t"
      "ds_read_b128 v[20:23], v102 offset:1024\n\t"
      "ds_read_b128 v[24:27], v100 offset:16384\n\t"
      "ds_read_b128 v[28:31], v100 offset:17408\n\t"
      "s_sub_u32 s61, s88, s78\n\t"
      "s_sub_i32 s61, s61, 560\n\t"
      "s_lshr_b32 s62, s25, 3\n\t"
      "s_sub_u32 s25, s25, s62\n\t"
      "s_sub_i32 s25, s25, 24\n\t"
      "s_max_i32 s25, s25, 0\n\t"
      "s_sub_i32 s62, s61, 96\n\t"
      "s_max_i32 s62, s62, 0\n\t"
      "s_min_i32 s62, s62, 320\n\t"
      "s_lshr_b32 s62, s62, 2\n\t"
      "s_add_u32 s25, s25, s62\n\t"
      "s_min_i32 s61, s61, 96\n\t"
      "s_max_i32 s61, s61, -96\n\t"
      "s_ashr_i32 s61, s61, 3\n\t"
      "s_add_u32 s26, s26, s61\n\t"
      "s_max_u32 s26, s26, 1500\n\t"
      "s_min_u32 s26, s26, 2300\n\t"
      "s_sub_u32 s62, s88, s27\n\t"
      "s_mov_b32 s27, s88\n\t"
      "s_sub_i32 s62, s62, s24\n\t"
      "s_cmp_gt_i32 s62, 1150\n\t"
      "s_cselect_b32 s62, 200, 0\n\t"
      "s_max_u32 s25, s25, s62\n\t"
      "s_sub_u32 s61, s76, s28\n\t"
      "s_sub_u32 s62, s80, s82\n\t"
      "s_mov_b32 s28, s76\n\t"
      "s_mov_b32 s82, s80\n\t"
      "s_lshl_b32 s61, s61, 8\n\t"
      "s_mul_i32 s62, s62, s83\n\t"
      "s_sub_i32 s61, s61, s62\n\t"
      "s_ashr_i32 s61, s61, 10\n\t"
      "s_min_i32 s61, s61, 100\n\t"
      "s_max_i32 s61, s61, -100\n\t"
      "s_add_u32 s83, s83, s61\n\t"
      "s_mul_i32 s24, s26, s83\n\t"
      "s_lshr_b32 s24, s24, 12\n\t"
      "s_add_u32 s24, s24, s25\n\t"
      "s_min_u32 s61, s30, 300\n\t"
      "s_sub_u32 s30, s30, s61\n\t"
      "s_sub_u32 s24, s24, s61\n\t"
      "s_add_u32 m0, s55, s71\n\t"
      "s_sub_u32 s71, 204800, s71\n\t"
      "s_add_u32 s40, s40, 1\n\t"
      "s_cmp_lt_u32 s40, 63\n\t"
      "s_cselect_b32 s61, 786432, 0\n\t"
      "s_cselect_b32 s60, s58, 0\n\t"
      "s_add_u32 s44, s44, s61\n\t"
      "s_cmp_lt_u32 s40, 62\n\t"
      "s_cselect_b32 s61, 786432, 0\n\t"
      "s_add_u32 s46, s46, s61\n\t"
      "s_add_u32 s61, s40, 3\n\t"
      "s_and_b32 s62, s61, 3\n\t"
      "s_cselect_b32 s62, 16, 2000\n\t"
      "s_cmp_lt_u32 s61, 64\n\t"
      "s_cselect_b32 s62, s62, 0\n\t"
      "s_add_u32 s50, s50, s62\n\t"
      "s_mov_b32 s63, 0x2000\n\t"
      "s_waitcnt lgkmcnt(8)\n\t"
      "v_mfma_f32_16x16x32_bf16 v[32:35], v[76:79], v[112:115], 0\n\t"
      "v_mfma_f32_16x16x32_bf16 v[36:39], v[80:83], v[112:115], 0\n\t"
      "v_mfma_f32_16x16x32_bf16 v[40:43], v[76:79], v[116:119], 0\n\t"
      "v_mfma_f32_16x16x32_bf16 v[44:47], v[80:83], v[116:119], 0\n\t"
      "ds_read_b128 v[76:79], v103\n\t"
      "ds_read_b128 v[80:83], v103 offset:1024\n\t"
      "ds_read_b128 v[112:115], v100 offset:24576\n\t"
      "ds_read_b128 v[116:119], v100 offset:25600\n\t"
      "s_branch LGATE_3%=\n\t"
      "LFIRST_3%=:\n\t"
      "s_cmp_eq_u32 s40, 0\n\t"
      "s_cbranch_scc1 LWM0_3%=\n\t"
      "s_mov_b32 s63, 0x10000\n\t"
      "LST0_3%=:\n\t"
      "s_waitcnt vmcnt(0)\n\t"
      "v_bitop3_b32 v97, v57, v85, s53 bitop3:0x7e\n\t"
      "v_cmp_ne_u32 vcc, 0, v97\n\t"
      "v_add_u32 v99, v58, v86\n\t"
      "v_subrev_u32 v97, s87, v56\n\t"
      "v_subrev_u32 v98, s87, v84\n\t"
      "v_max_i32 v97, v97, v98\n\t"
      "s_cbranch_vccz LST3_3%=\n\t"
      "buffer_load_dwordx4 v[56:59], v96, s[36:39], s86 offen sc1\n\t"
      "buffer_load_dwordx4 v[84:87], v96, s[36:39], s86 offen offset:128 sc1\n\t"
      "s_sub_u32 s63, s63, 1\n\t"
      "s_cmp_eq_u32 s63, 0\n\t"
      "s_cbranch_scc0 LST0_3%=\n\t"
      "s_waitcnt vmcnt(0)\n\t"
      "LST3_3%=:\n\t"
      "s_nop 1\n\t"
      "v_add_u32_dpp v99, v99, v99 quad_perm:[1,0,3,2] row_mask:0xf bank_mask:0xf\n\t"
      "s_nop 1\n\t"
      "v_add_u32_dpp v99, v99, v99 quad_perm:[2,3,0,1] row_mask:0xf bank_mask:0xf\n\t"
      "s_nop 1\n\t"
      "v_add_u32_dpp v99, v99, v99 row_ror:4 row_mask:0xf bank_mask:0xf\n\t"
      "s_nop 1\n\t"
      "v_add_u32_dpp v99, v99, v99 row_ror:8 row_mask:0xf bank_mask:0xf\n\t"
      "v_mov_b32 v98, v99\n\t"
      "s_nop 1\n\t"
      "v_permlane16_swap_b32 v99, v98\n\t"
      "v_add_u32 v99, v99, v98\n\t"
      "v_mov_b32 v98, v99\n\t"
      "s_nop 1\n\t"
      "v_permlane32_swap_b32 v99, v98\n\t"
      "v_add_u32 v99, v99, v98\n\t"
      "ds_write_b32 v109, v99 offset:55392\n\t"
      "s_nop 1\n\t"
      "v_max_i32_dpp v97, v97, v97 quad_perm:[1,0,3,2] row_mask:0xf bank_mask:0xf\n\t"
      "s_nop 1\n\t"
      "v_max_i32_dpp v97, v97, v97 quad_perm:[2,3,0,1] row_mask:0xf bank_mask:0xf\n\t"
      "s_nop 1\n\t"
      "v_max_i32_dpp v97, v97, v97 row_ror:4 row_mask:0xf bank_mask:0xf\n\t"
      "s_nop 1\n\t"
      "v_max_i32_dpp v97, v97, v97 row_ror:8 row_mask:0xf bank_mask:0xf\n\t"
      "v_mov_b32 v98, v97\n\t"
      "s_nop 1\n\t"
      "v_permlane16_swap_b32 v97, v98\n\t"
      "v_max_i32 v97, v97, v98\n\t"
      "v_mov_b32 v98, v97\n\t"
      "s_nop 1\n\t"
      "v_permlane32_swap_b32 v97, v98\n\t"
      "v_max_i32 v97, v97, v98\n\t"
      "v_max_u32 v99, v59, v87\n\t"
      "s_nop 1\n\t"
      "v_max_u32_dpp v99, v99, v99 quad_perm:[1,0,3,2] row_mask:0xf bank_mask:0xf\n\t"
      "s_nop 1\n\t"
      "v_max_u32_dpp v99, v99, v99 quad_perm:[2,3,0,1] row_mask:0xf bank_mask:0xf\n\t"
      "s_nop 1\n\t"
      "v_max_u32_dpp v99, v99, v99 row_ror:4 row_mask:0xf bank_mask:0xf\n\t"
      "s_nop 1\n\t"
      "v_max_u32_dpp v99, v99, v99 row_ror:8 row_mask:0xf bank_mask:0xf\n\t"
      "v_mov_b32 v98, v99\n\t"
      "s_nop 1\n\t"
      "v_permlane16_swap_b32 v99, v98\n\t"
      "v_max_u32 v99, v99, v98\n\t"
      "v_mov_b32 v98, v99\n\t"
      "s_nop 1\n\t"
      "v_permlane32_swap_b32 v99, v98\n\t"
      "v_max_u32 v99, v99, v98\n\t"
      "v_rcp_f32 v99, v99\n\t"
      "s_nop 0\n\t"
      "v_mul_f32 v99, 0x41800000, v99\n\t"
      "s_mov_b32 s25, 0\n\t"
      "v_mul_f32 v99, 0x4519c000, v99\n\t"
      "v_cvt_u32_f32 v99, v99\n\t"
      "s_nop 0\n\t"
      "v_readfirstlane_b32 s26, v99\n\t"
      "s_waitcnt lgkmcnt(0)\n\t"
      "s_barrier\n\t"
      "ds_read_b32 v99, v109 offset:55392\n\t"
      "v_readfirstlane_b32 s62, v97\n\t"
      "s_waitcnt lgkmcnt(0)\n\t"
      "v_readfirstlane_b32 s61, v99\n\t"
      "s_nop 1\n\t"
      "s_cmp_gt_i32 s61, 0\n\t"
      "s_cselect_b32 s99, 0x2000, 0\n\t"
      "s_xor_b32 s97, s97, s99\n\t"
      "s_min_u32 s62, s62, 500\n\t"
      "s_add_u32 s62, s62, 200\n\t"
      "LST1_3%=:\n\t"
      "s_memrealtime s[92:93]\n\t"
      "s_waitcnt lgkmcnt(0)\n\t"
      "s_sub_u32 s61, s92, s87\n\t"
      "s_cmp_lt_u32 s61, s62\n\t"
      "s_cbranch_scc1 LST1_3%=\n\t"
      "s_sub_u32 s61, s61, s62\n\t"
      "s_mul_i32 s30, s61, 21\n\t"
      "s_barrier\n\t"
      "s_mov_b32 s63, 0x2000\n\t"
      "s_branch LPOLL_3%=\n\t"
      "LWM0_3%=:\n\t"
      "s_mov_b32 s63, 0x10000\n\t"
      "LWM_3%=:\n\t"
      "s_waitcnt vmcnt(0) lgkmcnt(0)\n\t"
      "v_bitop3_b32 v49, v49, v51, s53 bitop3:0x7e\n\t"
      "v_max_u32 v48, v48, v50\n\t"
      "v_cmp_ne_u32 vcc, 0, v49\n\t"
      "s_nop 1\n\t"
      "v_max_u32_dpp v48, v48, v48 quad_perm:[1,0,3,2] row_mask:0xf bank_mask:0xf\n\t"
      "s_nop 1\n\t"
      "v_max_u32_dpp v48, v48, v48 quad_perm:[2,3,0,1] row_mask:0xf bank_mask:0xf\n\t"
      "s_nop 1\n\t"
      "v_max_u32_dpp v48, v48, v48 row_ror:4 row_mask:0xf bank_mask:0xf\n\t"
      "s_nop 1\n\t"
      "v_max_u32_dpp v48, v48, v48 row_ror:8 row_mask:0xf bank_mask:0xf\n\t"
      "v_mov_b32 v49, v48\n\t"
      "s_nop 1\n\t"
      "v_permlane16_swap_b32 v48, v49\n\t"
      "v_max_u32 v48, v48, v49\n\t"
      "v_mov_b32 v49, v48\n\t"
      "s_nop 1\n\t"
      "v_permlane32_swap_b32 v48, v49\n\t"
      "v_max_u32 v48, v48, v49\n\t"
      "v_mul_f32 v49, 0x40a00000, v48\n\t"
      "s_nop 0\n\t"
      "v_readfirstlane_b32 s67, v49\n\t"
      "v_mul_f32 v49, 0x43030000, v49\n\t"
      "s_nop 0\n\t"
      "v_readfirstlane_b32 s66, v49\n\t"
      "s_cbranch_vccz LWM1_3%=\n\t"
      "s_sleep 4\n\t"
      "buffer_load_dwordx2 v[48:49], v52, s[36:39], s94 offen sc1\n\t"
      "buffer_load_dwordx2 v[50:51], v52, s[36:39], s94 offen offset:128 sc1\n\t"
      "s_sub_u32 s63, s63, 1\n\t"
      "s_cmp_lg_u32 s63, 0\n\t"
      "s_cbranch_scc1 LWM_3%=\n\t"
      "s_waitcnt vmcnt(0)\n\t"
      "LWM1_3%=:\n\t"
      "buffer_load_dwordx2 v[120:121], off, s[36:39], s64 sc1\n\t"
      "s_lshr_b32 s67, s67, 23\n\t"
      "s_add_u32 s67, s67, 5\n\t"
      "s_lshr_b32 s66, s66, 23\n\t"
      "s_add_u32 s66, s66, 129\n\t"
      "s_max_u32 s66, s66, 80\n\t"
      "s_mov_b32 s63, 0x2000\n\t"
      "LPOL0_3%=:\n\t"
      "s_memtime s[78:79]\n\t"
      "s_waitcnt lgkmcnt(8)\n\t"
      "v_mfma_f32_16x16x32_bf16 v[32:35], v[0:3], v[8:11], v[32:35]\n\t"
      "v_mfma_f32_16x16x32_bf16 v[36:39], v[4:7], v[8:11], v[36:39]\n\t"
      "v_mfma_f32_16x16x32_bf16 v[40:43], v[0:3], v[12:15], v[40:43]\n\t"
      "v_mfma_f32_16x16x32_bf16 v[44:47], v[4:7], v[12:15], v[44:47]\n\t"
      "v_mov_b64 v[48:49], 0\n\t"
      "v_mov_b64 v[50:51], 0\n\t"
      "v_mov_b64 v[52:53], 0\n\t"
      "v_mov_b64 v[54:55], 0\n\t"
      "s_xor_b32 s69, s97, 1024\n\t"
      "s_xor_b32 s98, s97, 512\n\t"
      "s_xor_b32 s23, s97, 1536\n\t"
      "s_waitcnt lgkmcnt(4)\n\t"
      "v_mfma_f32_16x16x32_bf16 v[32:35], v[16:19], v[24:27], v[32:35]\n\t"
      "v_mfma_f32_16x16x32_bf16 v[36:39], v[20:23], v[24:27], v[36:39]\n\t"
      "v_mfma_f32_16x16x32_bf16 v[40:43], v[16:19], v[28:31], v[40:43]\n\t"
      "v_mfma_f32_16x16x32_bf16 v[44:47], v[20:23], v[28:31], v[44:47]\n\t"
      "s_waitcnt lgkmcnt(0)\n\t"
      "v_mfma_f32_16x16x32_bf16 v[32:35], v[76:79], v[112:115], v[32:35]\n\t"
      "v_mfma_f32_16x16x32_bf16 v[36:39], v[80:83], v[112:115], v[36:39]\n\t"
      "v_mfma_f32_16x16x32_bf16 v[40:43], v[76:79], v[116:119], v[40:43]\n\t"
      "v_mfma_f32_16x16x32_bf16 v[44:47], v[80:83], v[116:119], v[44:47]\n\t"
      "s_mov_b64 exec, 1\n\t"
      "ds_write_b64 v109, v[110:111] offset:55392\n\t"
      "s_mov_b64 exec, -1\n\t"
      "s_nop 4\n\t"
      "v_cvt_pk_bf16_f32 v68, v32, v33\n\t"
      "v_cvt_pk_bf16_f32 v69, v34, v35\n\t"
      "v_cvt_pk_bf16_f32 v70, v36, v37\n\t"
      "v_cvt_pk_bf16_f32 v71, v38, v39\n\t"
      "v_cvt_pk_bf16_f32 v74, v40, v41\n\t"
      "v_cvt_pk_bf16_f32 v75, v42, v43\n\t"
      "v_cvt_pk_bf16_f32 v72, v44, v45\n\t"
      "v_cvt_pk_bf16_f32 v73, v46, v47\n\t"
      "v_mov_b64 v[32:33], 0\n\t"
      "v_mov_b64 v[34:35], 0\n\t"
      "v_mov_b64 v[36:37], 0\n\t"
      "v_mov_b64 v[38:39], 0\n\t"
      "v_mov_b64 v[40:41], 0\n\t"
      "v_mov_b64 v[42:43], 0\n\t"
      "v_mov_b64 v[44:45], 0\n\t"
      "v_mov_b64 v[46:47], 0\n\t"
      "s_waitcnt vmcnt(0)\n\t"
      "s_branch LGO_3%=\n\t"
      "LEARLY_3%=:\n\t"
      "s_cmp_eq_u32 s40, 54\n\t"
      "s_cbranch_scc1 LROLE_3%=\n\t"
      "s_cmp_lt_i32 s40, 0\n\t"
      "s_waitcnt lgkmcnt(0)\n\t"
      "s_cbranch_scc1 LSTART_3%=\n\t"
      "s_xor_b32 s43, s43, s99\n\t"
      "s_xor_b32 s68, s68, s99\n\t"
      "s_cmp_lg_u32 s40, 0\n\t"
      "s_cbranch_scc1 LROLE_3%=\n\t"
      "v_lshlrev_b32 v96, 4, v127\n\t"
      "buffer_load_dwordx4 v[56:59], v96, s[36:39], s86 offen sc1\n\t"
      "buffer_load_dwordx4 v[84:87], v96, s[36:39], s86 offen offset:128 sc1\n\t"
      "s_branch LROLE_3%=\n\t"
      "LSTART_3%=:\n\t"
      "s_memrealtime s[92:93]\n\t"
      "s_memtime s[90:91]\n\t"
      "s_mov_b64 exec, 1\n\t"
      "v_mov_b32 v57, s53\n\t"
      "v_mov_b32 v58, s65\n\t"
      "s_add_u32 s86, s94, 208640\n\t"
      "s_add_u32 s61, s95, 208640\n\t"
      "s_waitcnt lgkmcnt(0)\n\t"
      "v_mov_b32 v56, s92\n\t"
      "s_mov_b32 s87, s92\n\t"
      "s_sub_u32 s80, s90, s23\n\t"
      "s_sub_u32 s82, s92, s82\n\t"
      "v_cvt_f32_u32 v59, s82\n\t"
      "v_cvt_f32_u32 v63, s80\n\t"
      "v_rcp_f32 v59, v59\n\t"
      "s_nop 0\n\t"
      "v_mul_f32 v59, v59, v63\n\t"
      "v_mul_f32 v63, 0x43800000, v59\n\t"
      "v_cvt_u32_f32 v63, v63\n\t"
      "s_nop 0\n\t"
      "v_readfirstlane_b32 s83, v63\n\t"
      "s_mov_b32 s76, s90\n\t"
      "s_mov_b32 s80, s92\n\t"
      "s_sub_u32 s28, s90, 2100\n\t"
      "s_sub_u32 s82, s92, 100\n\t"
      "s_mov_b32 s26, 1800\n\t"
      "buffer_store_dwordx4 v[56:59], off, s[36:39], s61 sc1\n\t"
      "s_mov_b64 exec, -1\n\t"
      "s_branch LUTOK_3%=\n\t"
      "LSLOW_3%=:\n\t"
      "s_mov_b32 s63, 0x10000\n\t"
      "LSLOW1_3%=:\n\t"
      "buffer_load_dwordx2 v[92:93], off, s[36:39], s64 sc1\n\t"
      "s_waitcnt vmcnt(0)\n\t"
      "v_bitop3_b32 v92, v92, v93, s53 bitop3:0x7e\n\t"
      "s_nop 0\n\t"
      "v_readfirstlane_b32 s56, v92\n\t"
      "s_nop 1\n\t"
      "s_cmp_eq_u32 s56, 0\n\t"
      "s_cbranch_scc1 LSLOW2_3%=\n\t"
      "s_sleep 8\n\t"
      "s_sub_u32 s63, s63, 1\n\t"
      "s_cmp_lg_u32 s63, 0\n\t"
      "s_cbranch_scc1 LSLOW1_3%=\n\t"
      "LSLOW2_3%=:\n\t"
      "s_mov_b32 m0, s45\n\t"
      "s_sub_u32 s61, 204800, s71\n\t"
      "buffer_load_dwordx4 v65, s[36:39], s44 offen sc1 lds\n\t"
      "buffer_load_dwordx4 v65, s[36:39], s44 offen offset:1024 sc1 lds\n\t"
      "s_add_u32 m0, s55, s61\n\t"
      "s_nop 0\n\t"
      "buffer_load_dwordx4 v127, s[36:39], s46 offen sc1 lds\n\t"
      "buffer_load_dwordx4 v127, s[36:39], s46 offen offset:1024 sc1 lds\n\t"
      "s_branch LWIN1_3%=\n\t"
      "LMX_3%=:\n\t"
      "s_waitcnt lgkmcnt(0)\n\t"
      "s_nop 7\n\t"
      "v_smfmac_f32_16x16x64_bf16 v[52:55], v[24:27], v[240:247], v122\n\t"
      "v_smfmac_f32_16x16x64_bf16 v[52:55], v[20:23], v[232:239], v122\n\t"
      "v_smfmac_f32_16x16x64_bf16 v[52:55], v[16:19], v[224:231], v122\n\t"
      "s_nop 7\n\t"
      "v_add_f32 v53, v53, v55\n\t"
      "v_add_f32_e64 v52, v52, v54\n\t"
      "v_cvt_pk_bf16_f32 v66, v52, v53\n\t"
      "s_add_u32 s62, s61, 0x83400000\n\t"
      "v_mov_b32_e64 v126, s62\n\t"
      "v_max3_f32 v82, v112, v113, v114\n\t"
      "v_max_f32 v82, v82, v115\n\t"
      "s_nop 1\n\t"
      "v_max_f32_dpp v82, v82, v82 quad_perm:[2,3,0,1] row_mask:0xf bank_mask:0xf\n\t"
      "v_max3_f32 v83, v82, v120, v121\n\t"
      "v_max3_f32 v60, v82, v120, v121\n\t"
      "s_nop 1\n\t"
      "v_permlane16_swap_b32 v83, v60\n\t"
      "v_max_f32 v61, v83, v60\n\t"
      "v_max_f32 v60, v83, v60\n\t"
      "s_nop 1\n\t"
      "v_permlane32_swap_b32 v61, v60\n\t"
      "v_max_f32 v83, v61, v60\n\t"
      "v_sub_f32 v112, v112, v83\n\t"
      "v_sub_f32 v113, v113, v83\n\t"
      "v_sub_f32 v114, v114, v83\n\t"
      "v_sub_f32 v115, v115, v83\n\t"
      "v_sub_f32 v120, v120, v83\n\t"
      "v_sub_f32 v121, v121, v83\n\t"
      "v_exp_f32_e64 v56, v112\n\t"
      "v_exp_f32 v57, v113\n\t"
      "v_exp_f32_e64 v58, v114\n\t"
      "v_exp_f32 v59, v115\n\t"
      "v_exp_f32 v80, v120\n\t"
      "v_exp_f32 v81, v121\n\t"
      "v_cvt_pk_bf16_f32 v96, v56, v57\n\t"
      "v_cvt_pk_bf16_f32 v97, v58, v59\n\t"
      "v_cvt_pk_bf16_f32 v108, v80, v81\n\t"
      "v_mov_b32_dpp v98, v96 quad_perm:[2,3,0,1] row_mask:0xf bank_mask:0xf\n\t"
      "v_mov_b32_dpp v99, v97 quad_perm:[2,3,0,1] row_mask:0xf bank_mask:0xf\n\t"
      "v_mfma_f32_16x16x32_bf16 v[48:51], v[108:111], v[88:91], 0\n\t"
      "s_nop 0\n\t"
      "v_mfma_f32_16x16x32_bf16 v[48:51], v[96:99], v[88:91], v[48:51]\n\t"
      "v_mfma_f32_16x16x32_bf16 v[84:87], v[108:111], v[64:67], 0\n\t"
      "v_mfma_f32_16x16x32_bf16 v[84:87], v[96:99], v[68:71], v[84:87]\n\t"
      "v_mfma_f32_16x16x32_bf16 v[92:95], v[96:99], v[72:75], 0\n\t"
      "v_mfma_f32_16x16x32_bf16 v[92:95], v[108:111], v[66:69], v[92:95]\n\t"
      "s_nop 3\n\t"
      "v_rcp_f32_e64 v48, v48\n\t"
      "v_rcp_f32 v49, v49\n\t"
      "v_fma_f32 v84, v84, v48, s96\n\t"
      "buffer_atomic_add v84, v123, s[36:39], s97 offen\n\t"
      "v_fma_f32 v85, v85, v49, s96\n\t"
      "buffer_atomic_add v85, v123, s[36:39], s98 offen\n\t"
      "v_fma_f32 v94, v94, v48, s96\n\t"
      "buffer_atomic_add v94, v123, s[36:39], s69 offen\n\t"
      "v_fma_f32 v95, v95, v49, s96\n\t"
      "buffer_atomic_add v95, v123, s[36:39], s23 offen\n\t"
      "s_add_u32 s97, s97, 0x8000\n\t"
      "s_branch LWIN_3%=\n\t"
      "LR0_3%=:\n\t"
      "buffer_load_dwordx4 v[84:87], v60, s[36:39], s43 offen sc1\n\t"
      "buffer_load_dwordx4 v[88:91], v60, s[36:39], s43 offen offset:1024 sc1\n\t"
      "s_waitcnt vmcnt(2)\n\t"
      "buffer_load_dwordx4 v[92:95], v60, s[36:39], s68 offen sc1\n\t"
      "buffer_load_dwordx4 v[96:99], v60, s[36:39], s68 offen offset:1024 sc1\n\t"
      "s_sub_u32 s63, s63, 1\n\t"
      "s_waitcnt vmcnt(2) lgkmcnt(0)\n\t"
      "v_bitop3_b32 v64, v84, v85, v86 bitop3:0x80\n\t"
      "v_bitop3_b32 v64, v64, v87, v88 bitop3:0x80\n\t"
      "v_bitop3_b32 v64, v64, v89, v90 bitop3:0x80\n\t"
      "v_bitop3_b32 v64, v64, v91, s58 bitop3:0x80\n\t"
      "v_cmp_eq_u32 vcc, 0, v64\n\t"
      "v_pk_fma_f32 v[8:9], v[84:85], s[56:57], v[126:127] op_sel:[0,1,0] op_sel_hi:[1,1,0]\n\t"
      "v_pk_fma_f32 v[10:11], v[86:87], s[56:57], v[126:127] op_sel:[0,1,0] op_sel_hi:[1,1,0]\n\t"
      "v_cvt_pk_bf16_f32 v12, v8, v9\n\t"
      "v_cvt_pk_bf16_f32 v13, v10, v11\n\t"
      "ds_write_b64 v127, v[12:13] offset:44032\n\t"
      "v_pk_fma_f32 v[4:5], v[88:89], s[56:57], v[126:127] op_sel:[0,1,0] op_sel_hi:[1,1,0]\n\t"
      "v_pk_fma_f32 v[6:7], v[90:91], s[56:57], v[126:127] op_sel:[0,1,0] op_sel_hi:[1,1,0]\n\t"
      "v_cvt_pk_bf16_f32 v14, v4, v5\n\t"
      "v_cvt_pk_bf16_f32 v15, v6, v7\n\t"
      "ds_write_b64 v127, v[14:15] offset:44040\n\t"
      "s_memtime s[88:89]\n\t"
      "s_cmp_le_i32 s63, 0\n\t"
      "s_waitcnt lgkmcnt(0)\n\t"
      "s_cbranch_scc1 LRZ_3%=\n\t"
      "s_cbranch_vccnz LR0_3%=\n\t"
      "LRZ_3%=:\n\t"
      "s_sub_u32 s88, s88, 0\n\t"
      "v_mov_b64 v[32:33], 0\n\t"
      "v_mov_b64 v[34:35], 0\n\t"
      "v_mov_b64 v[36:37], 0\n\t"
      "v_mov_b64 v[38:39], 0\n\t"
      "v_mov_b64 v[40:41], 0\n\t"
      "v_mov_b64 v[42:43], 0\n\t"
      "v_mov_b64 v[44:45], 0\n\t"
      "v_mov_b64 v[46:47], 0\n\t"
      "v_mov_b64 v[48:49], 0\n\t"
      "v_mov_b64 v[50:51], 0\n\t"
      "v_mov_b64 v[52:53], 0\n\t"
      "v_mov_b64 v[54:55], 0\n\t"
      "v_mfma_f32_16x16x32_bf16 v[56:59], v[12:15], v[12:15], 0\n\t"
      "v_smfmac_f32_16x16x64_bf16 v[36:39], v[12:15], a[88:95], v122\n\t"
      "v_smfmac_f32_16x16x64_bf16 v[44:47], v[12:15], a[216:223], v122\n\t"
      "s_branch LC0_3%=\n\t"
      "LCOLD%=:\n\t"
      "LR1_0%=:\n\t"
      "v_bitop3_b32 v64, v92, v93, v94 bitop3:0x80\n\t"
      "v_bitop3_b32 v64, v64, v95, v96 bitop3:0x80\n\t"
      "v_bitop3_b32 v64, v64, v97, v98 bitop3:0x80\n\t"
      "v_bitop3_b32 v64, v64, v99, s58 bitop3:0x80\n\t"
      "v_cmp_eq_u32 vcc, 0, v64\n\t"
      "s_nop 0\n\t"
      "s_cbranch_vccz LRX_0%=\n\t"
      "LRY_0%=:\n\t"
      "buffer_load_dwordx4 v[92:95], v60, s[36:39], s68 offen sc1\n\t"
      "buffer_load_dwordx4 v[96:99], v60, s[36:39], s68 offen offset:1024 sc1\n\t"
      "s_sub_u32 s63, s63, 1\n\t"
      "s_waitcnt vmcnt(0) lgkmcnt(0)\n\t"
      "v_bitop3_b32 v64, v92, v93, v94 bitop3:0x80\n\t"
      "v_bitop3_b32 v64, v64, v95, v96 bitop3:0x80\n\t"
      "v_bitop3_b32 v64, v64, v97, v98 bitop3:0x80\n\t"
      "v_bitop3_b32 v64, v64, v99, s58 bitop3:0x80\n\t"
      "v_cmp_eq_u32 vcc, 0, v64\n\t"
      "v_pk_fma_f32 v[20:21], v[92:93], s[56:57], v[126:127] op_sel:[0,1,0] op_sel_hi:[1,1,0]\n\t"
      "v_pk_fma_f32 v[22:23], v[94:95], s[56:57], v[126:127] op_sel:[0,1,0] op_sel_hi:[1,1,0]\n\t"
      "v_cvt_pk_bf16_f32 v16, v20, v21\n\t"
      "v_cvt_pk_bf16_f32 v17, v22, v23\n\t"
      "ds_write_b64 v127, v[16:17] offset:45056\n\t"
      "v_pk_fma_f32 v[24:25], v[96:97], s[56:57], v[126:127] op_sel:[0,1,0] op_sel_hi:[1,1,0]\n\t"
      "v_pk_fma_f32 v[26:27], v[98:99], s[56:57], v[126:127] op_sel:[0,1,0] op_sel_hi:[1,1,0]\n\t"
      "v_cvt_pk_bf16_f32 v18, v24, v25\n\t"
      "v_cvt_pk_bf16_f32 v19, v26, v27\n\t"
      "ds_write_b64 v127, v[18:19] offset:45064\n\t"
      "s_cmp_le_i32 s63, 0\n\t"
      "s_waitcnt lgkmcnt(0)\n\t"
      "s_cbranch_scc1 LRX_0%=\n\t"
      "s_cbranch_vccnz LRY_0%=\n\t"
      "LRX_0%=:\n\t"
      "s_cmp_eq_u32 s40, 63\n\t"
      "s_cbranch_scc1 LLAST_0%=\n\t"
      "s_branch LC1_0%=\n\t"
      "LLAST_0%=:\n\t"
      "s_waitcnt vmcnt(0) lgkmcnt(0)\n\t"
      "LLAS2_0%=:\n\t"
      "v_add_u32 v100, s55, v127\n\t"
      "v_add_u32 v52, 65536, v127\n\t"
      "s_barrier\n\t"
      "ds_read_b128 v[20:23], v127 offset:46080\n\t"
      "ds_read_b128 v[24:27], v127 offset:47104\n\t"
      "ds_read_b128 v[28:31], v127 offset:48128\n\t"
      "ds_read_b128 v[60:63], v100 offset:57344\n\t"
      "ds_read_b128 v[88:91], v52\n\t"
      "ds_read_b128 v[92:95], v52 offset:1024\n\t"
      "ds_read_b128 v[32:35], v52 offset:4096\n\t"
      "ds_read_b128 v[36:39], v52 offset:5120\n\t"
      "ds_read_b128 v[76:79], v52 offset:8192\n\t"
      "ds_read_b128 v[80:83], v52 offset:9216\n\t"
      "ds_read_b128 v[112:115], v52 offset:12288\n\t"
      "ds_read_b128 v[116:119], v52 offset:13312\n\t"
      "v_mov_b64 v[48:49], 0\n\t"
      "v_mov_b64 v[50:51], 0\n\t"
      "v_smfmac_f32_16x16x64_bf16 v[40:43], v[16:19], a[160:167], v122\n\t"
      "v_smfmac_f32_16x16x64_bf16 v[44:47], v[16:19], a[224:231], v122\n\t"
      "s_waitcnt lgkmcnt(11)\n\t"
      "v_smfmac_f32_16x16x64_bf16 v[40:43], v[20:23], a[168:175], v122\n\t"
      "v_smfmac_f32_16x16x64_bf16 v[44:47], v[20:23], a[232:239], v122\n\t"
      "s_waitcnt lgkmcnt(10)\n\t"
      "v_smfmac_f32_16x16x64_bf16 v[44:47], v[24:27], a[240:247], v122\n\t"
      "v_smfmac_f32_16x16x64_bf16 v[40:43], v[24:27], a[176:183], v122\n\t"
      "s_waitcnt lgkmcnt(9)\n\t"
      "v_smfmac_f32_16x16x64_bf16 v[44:47], v[28:31], a[248:255], v122\n\t"
      "v_smfmac_f32_16x16x64_bf16 v[40:43], v[28:31], a[184:191], v122\n\t"
      "s_waitcnt lgkmcnt(0)\n\t"
      "v_smfmac_f32_16x16x64_bf16 v[48:51], v[0:3], v[88:95], v122\n\t"
      "v_smfmac_f32_16x16x64_bf16 v[48:51], v[4:7], v[32:39], v122\n\t"
      "v_smfmac_f32_16x16x64_bf16 v[48:51], v[8:11], v[76:83], v122\n\t"
      "v_smfmac_f32_16x16x64_bf16 v[48:51], v[12:15], v[112:119], v122\n\t"
      "v_add_f32 v40, v40, v42\n\t"
      "v_add_f32 v41, v41, v43\n\t"
      "v_cvt_pk_bf16_f32 v120, v40, v41\n\t"
      "v_add_f32 v44, v44, v46\n\t"
      "v_add_f32 v45, v45, v47\n\t"
      "v_cvt_pk_bf16_f32 v121, v44, v45\n\t"
      "ds_write_b64 v125, v[120:121] offset:2080\n\t"
      "s_nop 0\n\t"
      "v_add_f32 v48, v48, v50\n\t"
      "v_add_f32 v49, v49, v51\n\t"
      "s_mov_b32 s28, s49\n\t"
      "s_waitcnt lgkmcnt(0)\n\t"
      "s_barrier\n\t"
      "ds_read_b64 v[54:55], v124 offset:53312\n\t"
      "s_waitcnt lgkmcnt(0)\n\t"
      "v_add_f32 v48, v48, v54\n\t"
      "v_add_f32 v49, v49, v55\n\t"
      "v_cvt_pk_bf16_f32 v48, v48, v49\n\t"
      "buffer_store_dword v48, v62, s[36:39], s28 offen sc1\n\t"
      "s_branch LEND%=\n\t"
      "LR1_1%=:\n\t"
      "v_bitop3_b32 v64, v92, v93, v94 bitop3:0x80\n\t"
      "v_bitop3_b32 v64, v64, v95, v96 bitop3:0x80\n\t"
      "v_bitop3_b32 v64, v64, v97, v98 bitop3:0x80\n\t"
      "v_bitop3_b32 v64, v64, v99, s58 bitop3:0x80\n\t"
      "v_cmp_eq_u32 vcc, 0, v64\n\t"
      "s_nop 0\n\t"
      "s_cbranch_vccz LRX_1%=\n\t"
      "LRY_1%=:\n\t"
      "buffer_load_dwordx4 v[92:95], v60, s[36:39], s68 offen sc1\n\t"
      "buffer_load_dwordx4 v[96:99], v60, s[36:39], s68 offen offset:1024 sc1\n\t"
      "s_sub_u32 s63, s63, 1\n\t"
      "s_waitcnt vmcnt(0) lgkmcnt(0)\n\t"
      "v_bitop3_b32 v64, v92, v93, v94 bitop3:0x80\n\t"
      "v_bitop3_b32 v64, v64, v95, v96 bitop3:0x80\n\t"
      "v_bitop3_b32 v64, v64, v97, v98 bitop3:0x80\n\t"
      "v_bitop3_b32 v64, v64, v99, s58 bitop3:0x80\n\t"
      "v_cmp_eq_u32 vcc, 0, v64\n\t"
      "v_pk_fma_f32 v[16:17], v[92:93], s[56:57], v[126:127] op_sel:[0,1,0] op_sel_hi:[1,1,0]\n\t"
      "v_pk_fma_f32 v[18:19], v[94:95], s[56:57], v[126:127] op_sel:[0,1,0] op_sel_hi:[1,1,0]\n\t"
      "v_cvt_pk_bf16_f32 v20, v16, v17\n\t"
      "v_cvt_pk_bf16_f32 v21, v18, v19\n\t"
      "ds_write_b64 v127, v[20:21] offset:46080\n\t"
      "v_pk_fma_f32 v[28:29], v[96:97], s[56:57], v[126:127] op_sel:[0,1,0] op_sel_hi:[1,1,0]\n\t"
      "v_pk_fma_f32 v[30:31], v[98:99], s[56:57], v[126:127] op_sel:[0,1,0] op_sel_hi:[1,1,0]\n\t"
      "v_cvt_pk_bf16_f32 v22, v28, v29\n\t"
      "v_cvt_pk_bf16_f32 v23, v30, v31\n\t"
      "ds_write_b64 v127, v[22:23] offset:46088\n\t"
      "s_cmp_le_i32 s63, 0\n\t"
      "s_waitcnt lgkmcnt(0)\n\t"
      "s_cbranch_scc1 LRX_1%=\n\t"
      "s_cbranch_vccnz LRY_1%=\n\t"
      "LRX_1%=:\n\t"
      "s_cmp_eq_u32 s40, 63\n\t"
      "s_cbranch_scc1 LLAST_1%=\n\t"
      "s_branch LC1_1%=\n\t"
      "LLAST_1%=:\n\t"
      "s_waitcnt vmcnt(0) lgkmcnt(0)\n\t"
      "LLAS2_1%=:\n\t"
      "v_add_u32 v100, s55, v127\n\t"
      "v_add_u32 v52, 67584, v127\n\t"
      "s_barrier\n\t"
      "ds_read_b128 v[16:19], v127 offset:45056\n\t"
      "ds_read_b128 v[28:31], v127 offset:48128\n\t"
      "ds_read_b128 v[24:27], v127 offset:47104\n\t"
      "ds_read_b128 v[60:63], v100 offset:57344\n\t"
      "ds_read_b128 v[88:91], v52\n\t"
      "ds_read_b128 v[92:95], v52 offset:1024\n\t"
      "ds_read_b128 v[32:35], v52 offset:4096\n\t"
      "ds_read_b128 v[36:39], v52 offset:5120\n\t"
      "ds_read_b128 v[76:79], v52 offset:8192\n\t"
      "ds_read_b128 v[80:83], v52 offset:9216\n\t"
      "ds_read_b128 v[112:115], v52 offset:12288\n\t"
      "ds_read_b128 v[116:119], v52 offset:13312\n\t"
      "v_mov_b64 v[48:49], 0\n\t"
      "v_mov_b64 v[50:51], 0\n\t"
      "v_smfmac_f32_16x16x64_bf16 v[40:43], v[20:23], a[168:175], v122\n\t"
      "v_smfmac_f32_16x16x64_bf16 v[44:47], v[20:23], a[232:239], v122\n\t"
      "s_waitcnt lgkmcnt(11)\n\t"
      "v_smfmac_f32_16x16x64_bf16 v[40:43], v[16:19], a[160:167], v122\n\t"
      "v_smfmac_f32_16x16x64_bf16 v[44:47], v[16:19], a[224:231], v122\n\t"
      "s_waitcnt lgkmcnt(10)\n\t"
      "v_smfmac_f32_16x16x64_bf16 v[44:47], v[28:31], a[248:255], v122\n\t"
      "v_smfmac_f32_16x16x64_bf16 v[40:43], v[28:31], a[184:191], v122\n\t"
      "s_waitcnt lgkmcnt(9)\n\t"
      "v_smfmac_f32_16x16x64_bf16 v[44:47], v[24:27], a[240:247], v122\n\t"
      "v_smfmac_f32_16x16x64_bf16 v[40:43], v[24:27], a[176:183], v122\n\t"
      "s_waitcnt lgkmcnt(0)\n\t"
      "v_smfmac_f32_16x16x64_bf16 v[48:51], v[0:3], v[88:95], v122\n\t"
      "v_smfmac_f32_16x16x64_bf16 v[48:51], v[4:7], v[32:39], v122\n\t"
      "v_smfmac_f32_16x16x64_bf16 v[48:51], v[8:11], v[76:83], v122\n\t"
      "v_smfmac_f32_16x16x64_bf16 v[48:51], v[12:15], v[112:119], v122\n\t"
      "v_add_f32 v40, v40, v42\n\t"
      "v_add_f32 v41, v41, v43\n\t"
      "v_cvt_pk_bf16_f32 v120, v40, v41\n\t"
      "v_add_f32 v44, v44, v46\n\t"
      "v_add_f32 v45, v45, v47\n\t"
      "v_cvt_pk_bf16_f32 v121, v44, v45\n\t"
      "ds_write_b64 v125, v[120:121] offset:2080\n\t"
      "s_nop 0\n\t"
      "v_add_f32 v48, v48, v50\n\t"
      "v_add_f32 v49, v49, v51\n\t"
      "s_mov_b32 s28, s49\n\t"
      "s_waitcnt lgkmcnt(0)\n\t"
      "s_barrier\n\t"
      "ds_read_b64 v[54:55], v124 offset:53832\n\t"
      "s_waitcnt lgkmcnt(0)\n\t"
      "v_add_f32 v48, v48, v54\n\t"
      "v_add_f32 v49, v49, v55\n\t"
      "v_cvt_pk_bf16_f32 v48, v48, v49\n\t"
      "buffer_store_dword v48, v62, s[36:39], s28 offen sc1\n\t"
      "s_branch LEND%=\n\t"
      "LWK1_2%=:\n\t"
      "buffer_store_dwordx4 v[44:47], v61, s[36:39], s48 offen sc1\n\t"
      "s_branch LW4_2%=\n\t"
      "LR1_2%=:\n\t"
      "v_bitop3_b32 v64, v92, v93, v94 bitop3:0x80\n\t"
      "v_bitop3_b32 v64, v64, v95, v96 bitop3:0x80\n\t"
      "v_bitop3_b32 v64, v64, v97, v98 bitop3:0x80\n\t"
      "v_bitop3_b32 v64, v64, v99, s58 bitop3:0x80\n\t"
      "v_cmp_eq_u32 vcc, 0, v64\n\t"
      "s_nop 0\n\t"
      "s_cbranch_vccz LRX_2%=\n\t"
      "LRY_2%=:\n\t"
      "buffer_load_dwordx4 v[92:95], v60, s[36:39], s68 offen sc1\n\t"
      "buffer_load_dwordx4 v[96:99], v60, s[36:39], s68 offen offset:1024 sc1\n\t"
      "s_sub_u32 s63, s63, 1\n\t"
      "s_waitcnt vmcnt(0) lgkmcnt(0)\n\t"
      "v_bitop3_b32 v64, v92, v93, v94 bitop3:0x80\n\t"
      "v_bitop3_b32 v64, v64, v95, v96 bitop3:0x80\n\t"
      "v_bitop3_b32 v64, v64, v97, v98 bitop3:0x80\n\t"
      "v_bitop3_b32 v64, v64, v99, s58 bitop3:0x80\n\t"
      "v_cmp_eq_u32 vcc, 0, v64\n\t"
      "v_pk_fma_f32 v[28:29], v[92:93], s[56:57], v[126:127] op_sel:[0,1,0] op_sel_hi:[1,1,0]\n\t"
      "v_pk_fma_f32 v[30:31], v[94:95], s[56:57], v[126:127] op_sel:[0,1,0] op_sel_hi:[1,1,0]\n\t"
      "v_cvt_pk_bf16_f32 v24, v28, v29\n\t"
      "v_cvt_pk_bf16_f32 v25, v30, v31\n\t"
      "ds_write_b64 v127, v[24:25] offset:47104\n\t"
      "v_pk_fma_f32 v[16:17], v[96:97], s[56:57], v[126:127] op_sel:[0,1,0] op_sel_hi:[1,1,0]\n\t"
      "v_pk_fma_f32 v[18:19], v[98:99], s[56:57], v[126:127] op_sel:[0,1,0] op_sel_hi:[1,1,0]\n\t"
      "v_cvt_pk_bf16_f32 v26, v16, v17\n\t"
      "v_cvt_pk_bf16_f32 v27, v18, v19\n\t"
      "ds_write_b64 v127, v[26:27] offset:47112\n\t"
      "s_cmp_le_i32 s63, 0\n\t"
      "s_waitcnt lgkmcnt(0)\n\t"
      "s_cbranch_scc1 LRX_2%=\n\t"
      "s_cbranch_vccnz LRY_2%=\n\t"
      "LRX_2%=:\n\t"
      "s_cmp_eq_u32 s40, 63\n\t"
      "s_cbranch_scc1 LLAST_2%=\n\t"
      "s_branch LC1_2%=\n\t"
      "LLAST_2%=:\n\t"
      "s_waitcnt vmcnt(0) lgkmcnt(0)\n\t"
      "LLAS2_2%=:\n\t"
      "v_add_u32 v100, s55, v127\n\t"
      "v_add_u32 v52, 65536, v127\n\t"
      "s_barrier\n\t"
      "ds_read_b128 v[28:31], v127 offset:48128\n\t"
      "ds_read_b128 v[16:19], v127 offset:45056\n\t"
      "ds_read_b128 v[20:23], v127 offset:46080\n\t"
      "ds_read_b128 v[60:63], v100 offset:57344\n\t"
      "ds_read_b128 v[88:91], v52 offset:16384\n\t"
      "ds_read_b128 v[92:95], v52 offset:17408\n\t"
      "ds_read_b128 v[32:35], v52 offset:20480\n\t"
      "ds_read_b128 v[36:39], v52 offset:21504\n\t"
      "ds_read_b128 v[76:79], v52 offset:24576\n\t"
      "ds_read_b128 v[80:83], v52 offset:25600\n\t"
      "ds_read_b128 v[112:115], v52 offset:28672\n\t"
      "ds_read_b128 v[116:119], v52 offset:29696\n\t"
      "v_mov_b64 v[48:49], 0\n\t"
      "v_mov_b64 v[50:51], 0\n\t"
      "v_smfmac_f32_16x16x64_bf16 v[40:43], v[24:27], a[176:183], v122\n\t"
      "v_smfmac_f32_16x16x64_bf16 v[44:47], v[24:27], a[240:247], v122\n\t"
      "s_waitcnt lgkmcnt(11)\n\t"
      "v_smfmac_f32_16x16x64_bf16 v[40:43], v[28:31], a[184:191], v122\n\t"
      "v_smfmac_f32_16x16x64_bf16 v[44:47], v[28:31], a[248:255], v122\n\t"
      "s_waitcnt lgkmcnt(10)\n\t"
      "v_smfmac_f32_16x16x64_bf16 v[44:47], v[16:19], a[224:231], v122\n\t"
      "v_smfmac_f32_16x16x64_bf16 v[40:43], v[16:19], a[160:167], v122\n\t"
      "s_waitcnt lgkmcnt(9)\n\t"
      "v_smfmac_f32_16x16x64_bf16 v[44:47], v[20:23], a[232:239], v122\n\t"
      "v_smfmac_f32_16x16x64_bf16 v[40:43], v[20:23], a[168:175], v122\n\t"
      "s_waitcnt lgkmcnt(0)\n\t"
      "v_smfmac_f32_16x16x64_bf16 v[48:51], v[16:19], v[88:95], v122\n\t"
      "v_smfmac_f32_16x16x64_bf16 v[48:51], v[20:23], v[32:39], v122\n\t"
      "v_smfmac_f32_16x16x64_bf16 v[48:51], v[24:27], v[76:83], v122\n\t"
      "v_smfmac_f32_16x16x64_bf16 v[48:51], v[28:31], v[112:119], v122\n\t"
      "v_add_f32 v40, v40, v42\n\t"
      "v_add_f32 v41, v41, v43\n\t"
      "v_cvt_pk_bf16_f32 v120, v40, v41\n\t"
      "v_add_f32 v44, v44, v46\n\t"
      "v_add_f32 v45, v45, v47\n\t"
      "v_cvt_pk_bf16_f32 v121, v44, v45\n\t"
      "ds_write_b64 v125, v[120:121] offset:2080\n\t"
      "s_nop 0\n\t"
      "v_add_f32 v48, v48, v50\n\t"
      "v_add_f32 v49, v49, v51\n\t"
      "ds_write_b64 v124, v[48:49] offset:53312\n\t"
      "s_waitcnt lgkmcnt(0)\n\t"
      "s_barrier\n\t"
      "s_cmp_eq_u32 s42, 3\n\t"
      "s_cselect_b32 s61, 1040, 0\n\t"
      "v_add_u32 v64, s61, v124\n\t"
      "ds_read_b64_tr_b16 v[40:41], v64 offset:51232\n\t"
      "ds_read_b64_tr_b16 v[42:43], v64 offset:51752\n\t"
      "s_cmp_lt_u32 s42, 4\n\t"
      "s_cselect_b64 exec, -1, 0\n\t"
      "s_waitcnt lgkmcnt(0)\n\t"
      "buffer_store_dwordx4 v[40:43], v61, s[36:39], s48 offen sc1\n\t"
      "s_mov_b64 exec, -1\n\t"
      "s_branch LEND%=\n\t"
      "LR1_3%=:\n\t"
      "v_bitop3_b32 v64, v92, v93, v94 bitop3:0x80\n\t"
      "v_bitop3_b32 v64, v64, v95, v96 bitop3:0x80\n\t"
      "v_bitop3_b32 v64, v64, v97, v98 bitop3:0x80\n\t"
      "v_bitop3_b32 v64, v64, v99, s58 bitop3:0x80\n\t"
      "v_cmp_eq_u32 vcc, 0, v64\n\t"
      "s_nop 0\n\t"
      "s_cbranch_vccz LRX_3%=\n\t"
      "LRY_3%=:\n\t"
      "buffer_load_dwordx4 v[92:95], v60, s[36:39], s68 offen sc1\n\t"
      "buffer_load_dwordx4 v[96:99], v60, s[36:39], s68 offen offset:1024 sc1\n\t"
      "s_sub_u32 s63, s63, 1\n\t"
      "s_waitcnt vmcnt(0) lgkmcnt(0)\n\t"
      "v_bitop3_b32 v64, v92, v93, v94 bitop3:0x80\n\t"
      "v_bitop3_b32 v64, v64, v95, v96 bitop3:0x80\n\t"
      "v_bitop3_b32 v64, v64, v97, v98 bitop3:0x80\n\t"
      "v_bitop3_b32 v64, v64, v99, s58 bitop3:0x80\n\t"
      "v_cmp_eq_u32 vcc, 0, v64\n\t"
      "v_pk_fma_f32 v[24:25], v[92:93], s[56:57], v[126:127] op_sel:[0,1,0] op_sel_hi:[1,1,0]\n\t"
      "v_pk_fma_f32 v[26:27], v[94:95], s[56:57], v[126:127] op_sel:[0,1,0] op_sel_hi:[1,1,0]\n\t"
      "v_cvt_pk_bf16_f32 v28, v24, v25\n\t"
      "v_cvt_pk_bf16_f32 v29, v26, v27\n\t"
      "ds_write_b64 v127, v[28:29] offset:48128\n\t"
      "v_pk_fma_f32 v[20:21], v[96:97], s[56:57], v[126:127] op_sel:[0,1,0] op_sel_hi:[1,1,0]\n\t"
      "v_pk_fma_f32 v[22:23], v[98:99], s[56:57], v[126:127] op_sel:[0,1,0] op_sel_hi:[1,1,0]\n\t"
      "v_cvt_pk_bf16_f32 v30, v20, v21\n\t"
      "v_cvt_pk_bf16_f32 v31, v22, v23\n\t"
      "ds_write_b64 v127, v[30:31] offset:48136\n\t"
      "s_memtime s[88:89]\n\t"
      "s_cmp_le_i32 s63, 0\n\t"
      "s_waitcnt lgkmcnt(0)\n\t"
      "s_cbranch_scc1 LRX_3%=\n\t"
      "s_cbranch_vccnz LRY_3%=\n\t"
      "s_sub_u32 s88, s88, 336\n\t"
      "LRX_3%=:\n\t"
      "s_cmp_eq_u32 s40, 63\n\t"
      "s_cbranch_scc1 LLAST_3%=\n\t"
      "s_branch LC1_3%=\n\t"
      "LLAST_3%=:\n\t"
      "s_waitcnt vmcnt(0) lgkmcnt(0)\n\t"
      "LLAS2_3%=:\n\t"
      "v_add_u32 v100, s55, v127\n\t"
      "v_add_u32 v52, 67584, v127\n\t"
      "s_barrier\n\t"
      "ds_read_b128 v[24:27], v127 offset:47104\n\t"
      "ds_read_b128 v[20:23], v127 offset:46080\n\t"
      "ds_read_b128 v[16:19], v127 offset:45056\n\t"
      "ds_read_b128 v[60:63], v100 offset:57344\n\t"
      "ds_read_b128 v[88:91], v52 offset:16384\n\t"
      "ds_read_b128 v[92:95], v52 offset:17408\n\t"
      "ds_read_b128 v[32:35], v52 offset:20480\n\t"
      "ds_read_b128 v[36:39], v52 offset:21504\n\t"
      "ds_read_b128 v[76:79], v52 offset:24576\n\t"
      "ds_read_b128 v[80:83], v52 offset:25600\n\t"
      "ds_read_b128 v[112:115], v52 offset:28672\n\t"
      "ds_read_b128 v[116:119], v52 offset:29696\n\t"
      "v_mov_b64 v[48:49], 0\n\t"
      "v_mov_b64 v[50:51], 0\n\t"
      "v_smfmac_f32_16x16x64_bf16 v[40:43], v[28:31], a[184:191], v122\n\t"
      "v_smfmac_f32_16x16x64_bf16 v[44:47], v[28:31], a[248:255], v122\n\t"
      "s_waitcnt lgkmcnt(11)\n\t"
      "v_smfmac_f32_16x16x64_bf16 v[40:43], v[24:27], a[176:183], v122\n\t"
      "v_smfmac_f32_16x16x64_bf16 v[44:47], v[24:27], a[240:247], v122\n\t"
      "s_waitcnt lgkmcnt(10)\n\t"
      "v_smfmac_f32_16x16x64_bf16 v[44:47], v[20:23], a[232:239], v122\n\t"
      "v_smfmac_f32_16x16x64_bf16 v[40:43], v[20:23], a[168:175], v122\n\t"
      "s_waitcnt lgkmcnt(9)\n\t"
      "v_smfmac_f32_16x16x64_bf16 v[44:47], v[16:19], a[224:231], v122\n\t"
      "v_smfmac_f32_16x16x64_bf16 v[40:43], v[16:19], a[160:167], v122\n\t"
      "s_waitcnt lgkmcnt(0)\n\t"
      "v_smfmac_f32_16x16x64_bf16 v[48:51], v[16:19], v[88:95], v122\n\t"
      "v_smfmac_f32_16x16x64_bf16 v[48:51], v[20:23], v[32:39], v122\n\t"
      "v_smfmac_f32_16x16x64_bf16 v[48:51], v[24:27], v[76:83], v122\n\t"
      "v_smfmac_f32_16x16x64_bf16 v[48:51], v[28:31], v[112:119], v122\n\t"
      "v_add_f32 v40, v40, v42\n\t"
      "v_add_f32 v41, v41, v43\n\t"
      "v_cvt_pk_bf16_f32 v120, v40, v41\n\t"
      "v_add_f32 v44, v44, v46\n\t"
      "v_add_f32 v45, v45, v47\n\t"
      "v_cvt_pk_bf16_f32 v121, v44, v45\n\t"
      "ds_write_b64 v125, v[120:121] offset:2080\n\t"
      "s_nop 0\n\t"
      "v_add_f32 v48, v48, v50\n\t"
      "v_add_f32 v49, v49, v51\n\t"
      "ds_write_b64 v124, v[48:49] offset:53832\n\t"
      "s_waitcnt lgkmcnt(0)\n\t"
      "s_barrier\n\t"
      "s_memrealtime s[92:93]\n\t"
      "s_waitcnt lgkmcnt(0)\n\t"
      "s_sub_u32 s61, s92, s95\n\t"
      "s_mov_b32 s95, s92\n\t"
      "s_min_u32 s61, s61, 190\n\t"
      "s_add_u32 s61, s61, s92\n\t"
      "s_pack_ll_b32_b16 s61, s61, s40\n\t"
      "v_mov_b32 v63, s61\n\t"
      "s_mov_b64 exec, 1\n\t"
      "buffer_store_dword v63, off, s[36:39], s51 sc1\n\t"
      "s_mov_b64 exec, -1\n\t"
      "s_branch LEND%=\n\t"
      "LEND%=:\n\t"
      "s_waitcnt vmcnt(2) lgkmcnt(0)\n\t"
      "s_cmp_lg_u32 s42, 9\n\t"
      "s_cbranch_scc1 LOUT%=\n\t"
      "s_waitcnt vmcnt(0)\n\t"
      "LOUT%=:\n\t"
      : "+{v[116:123]}"(c0), "+{v[124:127]}"(c1), "+{v[0:15]}"(sc), "+{v[16:31]}"(s2), "+{v[100:107]}"(c3), "+{v[108:115]}"(cc), "+s"(A), "+s"(b), "+s"(wv), "+{s87}"(tch)
      :
      : "memory", "vcc", "scc", "v32", "v33", "v34", "v35", "v36", "v37", "v38", "v39", "v40", "v41", "v42", "v43", "v44", "v45", "v46", "v47", "v48", "v49", "v50", "v51", "v52", "v53", "v54", "v55", "v56", "v57", "v58", "v59", "v60", "v61", "v62", "v63", "v64", "v65", "v66", "v67", "v68", "v69", "v70", "v71", "v72", "v73", "v74", "v75", "v76", "v77", "v78", "v79", "v80", "v81", "v82", "v83", "v84", "v85", "v86", "v87", "v88", "v89", "v90", "v91", "v92", "v93", "v94", "v95", "v96", "v97", "v98", "v99", "v128", "v129", "v130", "v131", "v132", "v133", "v134", "v135", "v136", "v137", "v138", "v139", "v140", "v141", "v142", "v143", "v144", "v145", "v146", "v147", "v148", "v149", "v150", "v151", "v152", "v153", "v154", "v155", "v156", "v157", "v158", "v159", "v160", "v161", "v162", "v163", "v164", "v165", "v166", "v167", "v168", "v169", "v170", "v171", "v172", "v173", "v174", "v175", "v176", "v177", "v178", "v179", "v180", "v181", "v182", "v183", "v184", "v185", "v186", "v187", "v188", "v189", "v190", "v191", "v192", "v193", "v194", "v195", "v196", "v197", "v198", "v199", "v200", "v201", "v202", "v203", "v204", "v205", "v206", "v207", "v208", "v209", "v210", "v211", "v212", "v213", "v214", "v215", "v216", "v217", "v218", "v219", "v220", "v221", "v222", "v223", "v224", "v225", "v226", "v227", "v228", "v229", "v230", "v231", "v232", "v233", "v234", "v235", "v236", "v237", "v238", "v239", "v240", "v241", "v242", "v243", "v244", "v245", "v246", "v247", "v248", "v249", "v250", "v251", "v252", "v253", "v254", "v255", "a0", "a1", "a2", "a3", "a4", "a5", "a6", "a7", "a8", "a9", "a10", "a11", "a12", "a13", "a14", "a15", "a16", "a17", "a18", "a19", "a20", "a21", "a22", "a23", "a24", "a25", "a26", "a27", "a28", "a29", "a30", "a31", "a32", "a33", "a34", "a35", "a36", "a37", "a38", "a39", "a40", "a41", "a42", "a43", "a44", "a45", "a46", "a47", "a48", "a49", "a50", "a51", "a52", "a53", "a54", "a55", "a56", "a57", "a58", "a59", "a60", "a61", "a62", "a63", "a64", "a65", "a66", "a67", "a68", "a69", "a70", "a71", "a72", "a73", "a74", "a75", "a76", "a77", "a78", "a79", "a80", "a81", "a82", "a83", "a84", "a85", "a86", "a87", "a88", "a89", "a90", "a91", "a92", "a93", "a94", "a95", "a96", "a97", "a98", "a99", "a100", "a101", "a102", "a103", "a104", "a105", "a106", "a107", "a108", "a109", "a110", "a111", "a112", "a113", "a114", "a115", "a116", "a117", "a118", "a119", "a120", "a121", "a122", "a123", "a124", "a125", "a126", "a127", "a128", "a129", "a130", "a131", "a132", "a133", "a134", "a135", "a136", "a137", "a138", "a139", "a140", "a141", "a142", "a143", "a144", "a145", "a146", "a147", "a148", "a149", "a150", "a151", "a152", "a153", "a154", "a155", "a156", "a157", "a158", "a159", "a160", "a161", "a162", "a163", "a164", "a165", "a166", "a167", "a168", "a169", "a170", "a171", "a172", "a173", "a174", "a175", "a176", "a177", "a178", "a179", "a180", "a181", "a182", "a183", "a184", "a185", "a186", "a187", "a188", "a189", "a190", "a191", "a192", "a193", "a194", "a195", "a196", "a197", "a198", "a199", "a200", "a201", "a202", "a203", "a204", "a205", "a206", "a207", "a208", "a209", "a210", "a211", "a212", "a213", "a214", "a215", "a216", "a217", "a218", "a219", "a220", "a221", "a222", "a223", "a224", "a225", "a226", "a227", "a228", "a229", "a230", "a231", "a232", "a233", "a234", "a235", "a236", "a237", "a238", "a239", "a240", "a241", "a242", "a243", "a244", "a245", "a246", "a247", "a248", "a249", "a250", "a251", "a252", "a253", "a254", "a255", "s22", "s23", "s24", "s25", "s26", "s27", "s28", "s29", "s30", "s31", "s36", "s37", "s38", "s39", "s40", "s41", "s42", "s43", "s44", "s45", "s46", "s47", "s48", "s49", "s50", "s51", "s52", "s53", "s54", "s55", "s56", "s57", "s58", "s59", "s60", "s61", "s62", "s63", "s64", "s65", "s66", "s67", "s68", "s69", "s70", "s71", "s72", "s73", "s74", "s75", "s76", "s77", "s78", "s79", "s80", "s81", "s82", "s83", "s84", "s85", "s86", "s88", "s89", "s90", "s91", "s92", "s93", "s94", "s95", "s96", "s97", "s98", "s99");
}

#define SSLEEP 1
#define TSLEEP 4
__device__ __forceinline__ unsigned mkkey(unsigned w, int idx) {
  unsigned u = w >> 16;
  u = (u & 0x7fffu) == 0 ? 0u : u;
  unsigned o = (u & 0x8000u) ? (~u & 0xffffu) : (u | 0x8000u);
  o = (u & 0x7fffu) > 0x7f80u ? 0u : o;
  return (o << 16) | (0xffffu - (unsigned)idx);
}
template <bool spread>
__device__ __forceinline__ u32x2 top2(const float* rbuf, int tid, int need, unsigned* sk) {
  unsigned key[9];
  const __amdgpu_buffer_rsrc_t rrs = __builtin_amdgcn_make_buffer_rsrc((void*)rbuf, 0, spread ? 2 * S_ * 4 + 256 * 128 : S_ * 4, 0x00020000);
  const int t8 = spread && tid >= 224 ? 0 : tid * 8;
#pragma unroll 1
  for (unsigned spins = 0; spins < (1u << 20); ++spins) {
    asm volatile("" ::: "memory");
    const u32x4 a = ld_sc1(rrs, t8 * 4), b = ld_sc1(rrs, t8 * 4 + 16);
    key[8] = spread ? __builtin_amdgcn_raw_buffer_load_b32(rrs, 2 * S_ * 4 + tid * 128, 0, 16) : a[0];
    unsigned mx = key[8];
#pragma unroll
    for (int k = 0; k < 4; ++k) { key[k] = a[k]; key[4 + k] = b[k]; mx = max(mx, max(a[k], b[k])); }
    if (mx != ~0u || tid * 8 >= need) break;
    __builtin_amdgcn_s_sleep(TSLEEP);
  }
  unsigned m = 0;
#pragma unroll
  for (int k = 0; k < 9; ++k) {
    key[k] = mkkey(key[k], k < 8 ? t8 + k : (spread ? S_ - 256 + tid : t8));
    m = max(m, key[k]);
  }
  m = wave_max_u32(m);
  __syncthreads();
  if ((tid & 63) == 0) sk[tid >> 6] = m;
  __syncthreads();
  const unsigned k0 = max(max(sk[0], sk[1]), max(sk[2], sk[3]));
  m = 0;
#pragma unroll
  for (int k = 0; k < 9; ++k) m = max(m, key[k] == k0 ? 0u : key[k]);
  m = wave_max_u32(m);
  if ((tid & 63) == 0) sk[4 + (tid >> 6)] = m;
  __syncthreads();
  const unsigned k1 = max(max(sk[4], sk[5]), max(sk[6], sk[7]));
  return (u32x2){(unsigned)__builtin_amdgcn_readfirstlane(k0), (unsigned)__builtin_amdgcn_readfirstlane(k1)};
}
__device__ __forceinline__ u32x2 top2_wave(const float* rbuf, int lane, bool dry, bool strict) {
  const __amdgpu_buffer_rsrc_t rrs = __builtin_amdgcn_make_buffer_rsrc((void*)rbuf, 0, S_ * 4, 0x00020000);
  asm volatile("" ::: "memory");
  u32x4 a[8];
#pragma unroll
  for (int i = 0; i < 8; ++i) a[i] = (u32x4){0u, 0u, 0u, 0u};
  if (!dry) {
#pragma unroll
    for (int i = 0; i < 8; ++i) a[i] = ld_sc1(rrs, (i * 64 + lane) * 16);
  }
  if (SNEED) {
    unsigned mx = 0;
#pragma unroll
    for (int i = 0; i < 8; ++i) {
      const unsigned x = max(max(a[i][0], a[i][1]), max(a[i][2], a[i][3]));
      mx = max(mx, i * 8 + (lane >> 3) < SNEED ? x : 0u);
    }
    if (strict && __builtin_amdgcn_ballot_w64(mx == ~0u)) return (u32x2){0u, 0u};
  }
  unsigned m1 = 0, m2 = 0;
#pragma unroll
  for (int i = 0; i < 32; ++i) {
    const unsigned w = a[i >> 2][i & 3], o = w ^ ((unsigned)((int)w >> 31) | 0x80000000u);
    const unsigned k = (o & 0xffff0000u) | ((0xffffu - (unsigned)(((i >> 2) * 64 + lane) * 4 + (i & 3))) & ~w);
    m2 = max(min(m1, k), m2);
    m1 = max(m1, k);
  }
  unsigned k[2];
  k[0] = wave_max_u32(m1);
  k[1] = wave_max_u32(m1 == k[0] ? m2 : m1);
  if (!k[1]) return (u32x2){0u, 0u};
#pragma unroll
  for (int i = 0; i < 2; ++i) k[i] = mkkey((k[i] >> 31) ? k[i] & 0x7fffffffu : ~k[i], 0xffff - (k[i] & 0xffff));
  return (u32x2){max(k[0], k[1]), min(k[0], k[1])};
}
__device__ __forceinline__ bool other_eval(const u32x4 a, const u32x4 b, int t8, unsigned k0, unsigned k1) {
  const unsigned o = k1 >> 16;
  const float v1 = u2f(((o & 0x8000u) ? o & 0x7fffu : ~o & 0xffffu) << 16);
  int n = 0;
#pragma unroll
  for (int k = 0; k < 4; ++k) n += (u2f(a[k]) >= v1) + (u2f(b[k]) >= v1);
  n -= (((0xffff - (k0 & 0xffff)) >> 3) * 8 == t8) + (((0xffff - (k1 & 0xffff)) >> 3) * 8 == t8);
  if (!RARE(!o || __builtin_amdgcn_ballot_w64(n != 0) != 0)) return false;
  bool bad = false;
  u32x4 x = a, y = b;
#pragma unroll 1
  for (int k = 0; k < 8; ++k) {
    const unsigned ka = mkkey(x[0], t8 + k);
    bad |= ka > k1 && ka != k0;
    x = (u32x4){x[1], x[2], x[3], y[0]}; y = (u32x4){y[1], y[2], y[3], 0u};
  }
  return __builtin_amdgcn_ballot_w64(bad) != 0;
}
__device__ __forceinline__ bool other_top2a(const float* rbuf, int tid, unsigned k0, unsigned k1, u32x4 a, u32x4 b) {
  const __amdgpu_buffer_rsrc_t rrs = __builtin_amdgcn_make_buffer_rsrc((void*)rbuf, 0, S_ * 4, 0x00020000);
  const int t8 = tid >= 224 ? 0 : tid * 8;
  if (RARE(__builtin_amdgcn_ballot_w64(max(max(max(a[0], a[1]), max(a[2], a[3])), max(max(b[0], b[1]), max(b[2], b[3]))) == ~0u) != 0)) {
#pragma unroll 1
    for (unsigned spins = 0; spins < (1u << 20); ++spins) {
      asm volatile("" ::: "memory");
      a = ld_sc1(rrs, t8 * 4); b = ld_sc1(rrs, t8 * 4 + 16);
      if (__builtin_amdgcn_ballot_w64(max(max(max(a[0], a[1]), max(a[2], a[3])), max(max(b[0], b[1]), max(b[2], b[3]))) == ~0u) == 0) break;
      __builtin_amdgcn_s_sleep(TSLEEP);
    }
  }
  return other_eval(a, b, t8, k0, k1);
}
__device__ __forceinline__ int other_top2b(const float* rbuf, int tid, u32x2& sel, bool bad, unsigned* sk, unsigned& tmp, unsigned f, unsigned w = ~0u) {
  tmp = 0;
  if (__builtin_amdgcn_ballot_w64(w == ~0u)) {
  if (RDLY && (f >> 16) == 63) {
    const unsigned t = f + RDLY - (3 - (tid >> 6)) * RDL2;
#pragma unroll 1
    while (((t - (unsigned)__builtin_amdgcn_s_memrealtime()) & 0xffffu) < 400u) __builtin_amdgcn_s_sleep(1);
  }
  const __amdgpu_buffer_rsrc_t rrs = __builtin_amdgcn_make_buffer_rsrc((void*)rbuf, 0, 2 * S_ * 4 + 256 * 128, 0x00020000);
  unsigned n;
  w = RDLY ? __builtin_amdgcn_raw_buffer_load_b32(rrs, 2 * S_ * 4 + tid * 128, 0, 16) : ~0u;
  if (RARE(!RDLY || __builtin_amdgcn_ballot_w64(w == ~0u))) {
  asm volatile("buffer_load_dword %1, %3, %4, 0 offen sc1\n\ts_sleep %5\n\t"
               "buffer_load_dword %1, %3, %4, 0 offen sc1\n\ts_sleep %5\n\t"
               "s_mov_b32 %2, 0x100000\n"
               "TP%=:\n\t"
               "buffer_load_dword %1, %3, %4, 0 offen sc1\n\t"
               "s_sub_u32 %2, %2, 1\n\t"
               "s_waitcnt vmcnt(2)\n\t"
               "v_mov_b32 %0, %1\n\t"
               "v_cmp_eq_u32 vcc, -1, %0\n\t"
               "s_cbranch_scc1 TQ%=\n\t"
               "s_cbranch_vccnz TP%=\n"
               "TQ%=:"
               : "=&v"(w), "=&v"(tmp), "=&s"(n) : "v"(2 * S_ * 4 + tid * 128), "s"(rrs), "n"(PSLEEP) : "memory", "vcc", "scc");
  }
  }
  unsigned k = mkkey(w, S_ - 256 + tid);
  k = k > sel[1] && k != sel[0] ? k : 0u;
  u32x2 m = {bad ? ~0u : 0u, 0u};
  if (RARE(!bad && __builtin_amdgcn_ballot_w64(k != 0))) { m[0] = wave_max_u32(k); m[1] = wave_max_u32(k == m[0] ? 0u : k); }
  if ((tid & 63) == 0) *(u32x2*)(sk + (tid >> 6) * 2) = m;
  LDS_BARRIER();
  const u32x4 v = *(const u32x4*)sk, x = *(const u32x4*)(sk + 4);
  const unsigned top = __builtin_amdgcn_readfirstlane(max(max(v[0], v[2]), max(x[0], x[2])));
  asm volatile("s_cmp_lg_u32 %0, 0\n\ts_cbranch_scc1 1f\n\ts_endpgm\n1:" :: "s"(top) : "scc");
  if (!RARE(top != 0)) return 0;
  if (top == ~0u) return 2;
  unsigned a = sel[0], b = sel[1];
#pragma unroll
  for (int i = 0; i < 8; ++i) { const unsigned c = i < 4 ? v[i & 3] : x[i & 3]; b = max(b, min(a, c)); a = max(a, c); }
  sel = (u32x2){(unsigned)__builtin_amdgcn_readfirstlane(a), (unsigned)__builtin_amdgcn_readfirstlane(b)};
  return 1;
}

constexpr int M1_WGS = FF / 16;
constexpr int L_WD = 131072;
__device__ __forceinline__ void wait_set(gu32* f) {
#pragma unroll 1
  for (unsigned spins = 0; spins < (1u << 18); ++spins) {
    if (__hip_atomic_load(f, RLX_AGENT) != ~0u) return;
    __builtin_amdgcn_s_sleep(4);
  }
}
__device__ __forceinline__ u32x2 wait_selt(char* ws, int b, unsigned& tm) {
  const __amdgpu_buffer_rsrc_t rs = __builtin_amdgcn_make_buffer_rsrc((void*)(ws + O_CNT), 0, 1024, 0x00020000);
  u32x4 v;
#pragma unroll 1
  for (unsigned spins = 0; spins < (1u << 18); ++spins) {
    asm volatile("" ::: "memory");
    v = ld_sc1(rs, (b & 7) * 64);
    if (v[1]) break;
    __builtin_amdgcn_s_sleep(FSLEEP);
  }
  tm = __builtin_amdgcn_readfirstlane(v[2]);
  return (u32x2){(unsigned)__builtin_amdgcn_readfirstlane(v[0]), (unsigned)__builtin_amdgcn_readfirstlane(v[1])};
}
__device__ __forceinline__ u32x2 wait_sel(gu32* f, u64 v = 0) {
#pragma unroll 1
  for (unsigned spins = 0; spins < (1u << 18) && !v; ++spins) {
    v = __hip_atomic_load((gu64*)f, RLX_AGENT);
    if (v) break;
    __builtin_amdgcn_s_sleep(FSLEEP);
  }
  return (u32x2){(unsigned)__builtin_amdgcn_readfirstlane((unsigned)v), (unsigned)__builtin_amdgcn_readfirstlane((unsigned)(v >> 32))};
}
__device__ __forceinline__ unsigned code_ahead(int tid) {
  unsigned d = 0;
  if (MPF) { u64 pc; asm volatile("s_getpc_b64 %1\n\tglobal_load_dword %0, %2, %1" : "=v"(d), "=&s"(pc) : "v"((tid & 127) * 64) : "memory"); }
  return d;
}
__device__ __forceinline__ bf16x8 as_frag(u32x4 v) { return __builtin_bit_cast(bf16x8, v); }
__device__ __forceinline__ u32x4 tr_frag(char* p0, char* p1) {
  const s16x4 a = __builtin_amdgcn_ds_read_tr16_b64_v4i16((LAS s16x4*)p0), b = __builtin_amdgcn_ds_read_tr16_b64_v4i16((LAS s16x4*)p1);
  const u32x2 x = __builtin_bit_cast(u32x2, a), y = __builtin_bit_cast(u32x2, b);
  return (u32x4){x[0], x[1], y[0], y[1]};
}
__device__ __forceinline__ void mlp1_role(KArgs P, char* lds, int b, int tid) {
  float* part = (float*)lds;
  unsigned* si = (unsigned*)(lds + 4096);
  const int lane = tid & 63, wv = tid >> 6, l15 = lane & 15, l4 = lane >> 4;
  const __amdgpu_buffer_rsrc_t lrs = __builtin_amdgcn_make_buffer_rsrc((void*)(P->ws + O_LNH), 0, S_ * DM * 2, 0x00020000);
  const __amdgpu_buffer_rsrc_t mrs = __builtin_amdgcn_make_buffer_rsrc((void*)(P->ws + O_MFRAG), 0, 2 * DM * FF * 2, 0x00020000);
  u32x4 aw[2][4];
  MS(0);
  unsigned ca = code_ahead(tid);
  const u64 s0 = __hip_atomic_load((gu64*)(P->ws + O_CNT + (b & 7) * 64), RLX_AGENT);
#pragma unroll
  for (int i = 0; i < 8; ++i) aw[i >> 2][i & 3] = ld_sc1(mrs, (unsigned)(b * 32768 + (i >> 2) * 16384 + (wv * 4 + (i & 3)) * 1024 + lane * 16));
  u32x2 sel = wait_sel((gu32*)(P->ws + O_CNT) + (b & 7) * 16, s0);
  const unsigned fl = __hip_atomic_load((gu32*)(P->ws + O_KVSTEP + (b >> 2) * 64 + (b & 3) * 4), RLX_AGENT);
  asm volatile("" :: "v"(ca));
  MS(1);
  int round = 0;
#pragma unroll 1
  for (;;) {
    const int idx = 0xffff - (((l15 & 1) ? sel[1] : sel[0]) & 0xffff);
    if (RARE(round == 2)) wait_set((gu32*)(P->ws + O_RBUF) + S_ + idx);
    u32x4 bx[4];
#pragma unroll
    for (int i = 0; i < 4; ++i) bx[i] = ld_sc1(lrs, (unsigned)(idx * 1024 + (wv * 4 + i) * 64 + l4 * 16));
    f32x4 ag = {0.f, 0.f, 0.f, 0.f}, au = {0.f, 0.f, 0.f, 0.f};
#pragma unroll
    for (int i = 0; i < 4; ++i) {
      ag = __builtin_amdgcn_mfma_f32_16x16x32_bf16(as_frag(aw[0][i]), as_frag(bx[i]), ag, 0, 0, 0);
      au = __builtin_amdgcn_mfma_f32_16x16x32_bf16(as_frag(aw[1][i]), as_frag(bx[i]), au, 0, 0, 0);
    }
    if (l15 < 2) {
      *(f32x4*)(part + wv * 64 + l15 * 16 + l4 * 4) = ag;
      *(f32x4*)(part + wv * 64 + 32 + l15 * 16 + l4 * 4) = au;
    }
    LDS_BARRIER();
    MS(2);
    if (tid < 16) {
      float a[2];
#pragma unroll
      for (int e = 0; e < 2; ++e) {
        const float* q = part + tid * 2 + e;
        const float g = rbf((q[0] + q[64]) + (q[128] + q[192])), u = rbf((q[32] + q[96]) + (q[160] + q[224]));
        a[e] = rbf(g / (1.0f + __expf(-g))) * u;
      }
      unsigned av = pk2(a[0], a[1]);
      av = av == ~0u ? 0x7fc07fc0u : av;
      __hip_atomic_store((gu32*)(P->ws + O_ACT) + (round * M1_WGS + b) * 32 + tid, av, RLX_AGENT);
    }
    MS(4);
    if (RARE(round == 2)) break;
    if (SW2 && !round) {
      const u32x2 s2 = wait_sel((gu32*)(P->ws + O_SEL2) + (b & 31) * 32);
      if (s2[0] != sel[0] || s2[1] != sel[1]) { sel = s2; round = 1; __syncthreads(); continue; }
    }
    unsigned tmp;
    const __amdgpu_buffer_rsrc_t rrs = __builtin_amdgcn_make_buffer_rsrc((void*)(P->ws + O_RBUF), 0, S_ * 4, 0x00020000);
    const int t8 = tid >= 224 ? 0 : tid * 8;
    const int again = other_top2b((const float*)(P->ws + O_RBUF), tid, sel, other_top2a((const float*)(P->ws + O_RBUF), tid, sel[0], sel[1], ld_sc1(rrs, t8 * 4), ld_sc1(rrs, t8 * 4 + 16)), si, tmp, __builtin_amdgcn_readfirstlane(fl));
    MS(5);
    if (!RARE(again)) break;
    asm volatile("s_waitcnt vmcnt(0)" :: "v"(tmp) : "memory");
    if (again == 2) sel = top2<true>((const float*)(P->ws + O_RBUF), tid, S_, si + 8);
    round = 2;
  }
}
__device__ __forceinline__ void mlp2_role(KArgs P, char* lds, int b, int tid) {
  float* part = (float*)lds;
  unsigned* si = (unsigned*)(lds + 4096);
  const int lane = tid & 63, wv = tid >> 6, l15 = lane & 15, l4 = lane >> 4;
  const __amdgpu_buffer_rsrc_t hrs = __builtin_amdgcn_make_buffer_rsrc((void*)(P->ws + O_HBUF), 0, S_ * DM * 2, 0x00020000);
  const __amdgpu_buffer_rsrc_t lrs = __builtin_amdgcn_make_buffer_rsrc((void*)(P->ws + O_LNH), 0, S_ * DM * 2, 0x00020000);
  const __amdgpu_buffer_rsrc_t ors = __builtin_amdgcn_make_buffer_rsrc((void*)P->out, 0, S_ * DM * 2, 0x00020000);
  const __amdgpu_buffer_rsrc_t ars = __builtin_amdgcn_make_buffer_rsrc((void*)(P->ws + O_ACT), 0, 3 * M1_WGS * 128, 0x00020000);
  u32x4 aw[7];
  const int c0 = b * 8;
  MS(0);
  unsigned ca = code_ahead(tid);
  {
    char* p = lds + L_WD + (((l15 >> 1) & 1) * 896 + wv * 224 + l4 * 8 + (l15 >> 2)) * 16 + (l15 & 1) * 8;
#pragma unroll
    for (int i = 0; i < 7; ++i) aw[i] = tr_frag(p + i * 512, p + i * 512 + 64);
  }
  unsigned tsel;
  u32x2 sel = wait_selt(P->ws, b, tsel);
  asm volatile("" :: "v"(ca));
  const unsigned fl = __hip_atomic_load((gu32*)(P->ws + O_KVSTEP + (c0 >> 4) * 64), RLX_AGENT);
  MS(1);
  int round = 0;
#pragma unroll 1
  for (;;) {
    const int idx = 0xffff - (((tid & 8) ? sel[1] : sel[0]) & 0xffff), col = c0 + (tid & 7);
    if (RARE(round == 2)) wait_set((gu32*)(P->ws + O_RBUF) + S_ + idx);
    if (ADLY) {
#pragma unroll 1
      while (tsel + ADLY - (unsigned)__builtin_amdgcn_s_memrealtime() < 400u) __builtin_amdgcn_s_sleep(1);
    }
    const float rr = __hip_atomic_load((const GAS float*)(P->ws + O_RBUF) + S_ + idx, RLX_AGENT);
    const u16 lw = __builtin_amdgcn_raw_buffer_load_b16(lrs, (unsigned)(idx * DM + col) * 2, 0, 16);
    const u16 hw = __builtin_amdgcn_raw_buffer_load_b16(hrs, (unsigned)(idx * DM + col) * 2, 0, 16);
    const __amdgpu_buffer_rsrc_t rrs = __builtin_amdgcn_make_buffer_rsrc((void*)(P->ws + O_RBUF), 0, 2 * S_ * 4 + 256 * 128, 0x00020000);
    const int t8 = tid >= 224 ? 0 : tid * 8;
    const u32x4 pa = ld_sc1(rrs, t8 * 4), pb = ld_sc1(rrs, t8 * 4 + 16);
    unsigned pw = ~0u;
    if (wv == 0) pw = __builtin_amdgcn_raw_buffer_load_b32(rrs, 2 * S_ * 4 + tid * 128, 0, 16);
    u32x4 av[7];
    u64 v2 = 0;
#pragma unroll 1
    for (unsigned spins = 0; spins < (1u << 20); ++spins) {
      asm volatile("" ::: "memory");
      unsigned mx = 0;
      if (SW2) v2 = __hip_atomic_load((gu64*)(P->ws + O_SEL2 + (b & 31) * 128), RLX_AGENT);
#pragma unroll
      for (int i = 0; i < 7; ++i) {
        av[i] = ld_sc1(ars, (unsigned)((round * M1_WGS + ((l15 >> 1) & 1) * 56 + (wv * 7 + i) * 2 + (l4 >> 1)) * 128 + (l15 & 1) * 32 + (l4 & 1) * 16));
        mx = max(max(mx, max(av[i][0], av[i][1])), max(av[i][2], av[i][3]));
      }
      if (!RARE(__builtin_amdgcn_ballot_w64(mx == ~0u) != 0)) break;
      __builtin_amdgcn_s_sleep(4);
    }
    MS(2);
    if (SW2 && !round) {
      const u32x2 s2 = wait_sel((gu32*)(P->ws + O_SEL2) + (b & 31) * 32, v2);
      if (s2[0] != sel[0] || s2[1] != sel[1]) { sel = s2; round = 1; continue; }
    }
    const float lv = bf2f(lw), hv = bf2f(hw), sg = rbf(1.0f / (1.0f + __expf(-rr)));
    f32x4 a0 = {0.f, 0.f, 0.f, 0.f}, a1 = {0.f, 0.f, 0.f, 0.f};
#pragma unroll
    for (int i = 0; i < 7; ++i) {
      if (i & 1) a1 = __builtin_amdgcn_mfma_f32_16x16x32_bf16(as_frag(aw[i]), as_frag(av[i]), a1, 0, 0, 0);
      else a0 = __builtin_amdgcn_mfma_f32_16x16x32_bf16(as_frag(aw[i]), as_frag(av[i]), a0, 0, 0, 0);
    }
    if ((l15 >> 1) == (l4 >> 1)) *(f32x4*)(part + wv * 32 + l15 * 8 + (l4 & 1) * 4) = a0 + a1;
    LDS_BARRIER();
    const float* q = part + (tid & 15);
    const float y = rbf(((q[0] + q[16]) + (q[32] + q[48])) + ((q[64] + q[80]) + (q[96] + q[112])));
    const float upd = rbf(y * sg);
    const float bb = rbf(lv + upd);
    const u16 res = f2bf(hv + bb);
    MS(4);
    if (RARE(round == 2)) VM_DRAIN();
    if (tid < 16) __builtin_amdgcn_raw_buffer_store_b16(res, ors, (unsigned)(idx * DM + col) * 2, 0, 16);
    if (RARE(round == 2)) break;
    unsigned tmp;
    const bool bad = other_top2a((const float*)(P->ws + O_RBUF), tid, sel[0], sel[1], pa, pb);
    const int again = other_top2b((const float*)(P->ws + O_RBUF), tid, sel, bad, si, tmp, __builtin_amdgcn_readfirstlane(fl), pw);
    MS(5);
    if (!RARE(again)) break;
    asm volatile("s_waitcnt vmcnt(0)" :: "v"(tmp) : "memory");
    if (tid < 16) __builtin_amdgcn_raw_buffer_store_b16(f2bf(hv + lv), ors, (unsigned)(idx * DM + col) * 2, 0, 16);
    if (again == 2) sel = top2<true>((const float*)(P->ws + O_RBUF), tid, S_, si + 8);
    tsel = (unsigned)__builtin_amdgcn_s_memrealtime() + RDO - ADLY;
    round = 2;
  }
}

struct WoRegs { u32x32 a, b, c, d; };
__device__ __forceinline__ unsigned worker_role(int& w, int& wv, KArgs& A, WoRegs& wo) {
  unsigned tag;
  asm volatile(
      "GSTART:\n\t"
      "v_mbcnt_lo_u32_b32 v96, -1, 0\n\t"
      "v_mbcnt_hi_u32_b32 v96, -1, v96\n\t"
      "s_and_b32 s82, %0, 31\n\t"
      "s_mov_b32 s66, 4194304\n\t"
      "s_mov_b32 s67, 0x20000\n\t"
      "s_mov_b32 s62, 111181824\n\t"
      "s_mov_b32 s63, 0x20000\n\t"
      "v_and_b32 v104, 3, v96\n\t"
      "v_lshrrev_b32 v105, 2, v96\n\t"
      "s_lshl_b32 s78, %0, 14\n\t"
      "s_lshl_b32 s79, %1, 12\n\t"
      "s_add_u32 s79, s79, s78\n\t"
      "v_lshlrev_b32 v106, 10, v104\n\t"
      "v_lshl_add_u32 v106, v105, 4, v106\n\t"
      "v_lshlrev_b32 v99, 13, v105\n\t"
      "v_lshl_add_u32 v99, v104, 4, v99\n\t"
      "s_lshl_b32 s80, s82, 8\n\t"
      "s_lshl_b32 s72, %1, 6\n\t"
      "s_add_u32 s80, s80, s72\n\t"
      "v_add_u32 v99, s80, v99\n\t"
      "s_bitcmp1_b32 %0, 5\n\t"
      "s_waitcnt lgkmcnt(0)\n\t"
      "s_mov_b64 s[64:65], s[36:37]\n\t"
      "s_mov_b64 s[60:61], s[58:59]\n\t"
      "buffer_load_dwordx4 v[64:67], v106, s[64:67], s79 offen \n\t"
      "buffer_load_dwordx4 v[68:71], v106, s[64:67], s79 offen offset:256 \n\t"
      "buffer_load_dwordx4 v[72:75], v106, s[64:67], s79 offen offset:512 \n\t"
      "buffer_load_dwordx4 v[76:79], v106, s[64:67], s79 offen offset:768 \n\t"
      "s_cselect_b64 s[38:39], s[44:45], s[42:43]\n\t"
      "s_mov_b32 s78, 0\n\t"
      "s_load_dword s87, s[38:39], s78\n\t"
      "s_mov_b32 s78, 4194300\n\t"
      "s_load_dword s87, s[38:39], s78\n\t"
      "s_mov_b32 s78, 128\n\t"
      "s_load_dword s87, s[60:61], s78\n\t"
      "s_mov_b32 s78, 6520704\n\t"
      "s_load_dword s87, s[60:61], s78\n\t"
      "v_mov_b32 v118, v106\n\t"
      "s_mov_b64 s[64:65], s[36:37]\n\t"
      "s_lshr_b32 s78, %0, 6\n\t"
      "s_lshl_b32 s78, s78, 15\n\t"
      "s_lshl_b32 s79, %1, 12\n\t"
      "s_add_u32 s79, s79, s78\n\t"
      "buffer_load_dwordx4 v[128:131], v118, s[64:67], s79 offen \n\t"
      "buffer_load_dwordx4 v[132:135], v118, s[64:67], s79 offen offset:256 \n\t"
      "buffer_load_dwordx4 v[136:139], v118, s[64:67], s79 offen offset:512 \n\t"
      "buffer_load_dwordx4 v[140:143], v118, s[64:67], s79 offen offset:768 \n\t"
      "s_add_u32 s79, s79, 16384\n\t"
      "buffer_load_dwordx4 v[144:147], v118, s[64:67], s79 offen \n\t"
      "buffer_load_dwordx4 v[148:151], v118, s[64:67], s79 offen offset:256 \n\t"
      "buffer_load_dwordx4 v[152:155], v118, s[64:67], s79 offen offset:512 \n\t"
      "buffer_load_dwordx4 v[156:159], v118, s[64:67], s79 offen offset:768 \n\t"
      "s_mov_b64 s[64:65], s[38:39]\n\t"
      "s_mov_b64 s[64:65], s[38:39]\n\t"
      "buffer_load_dword v107, off, s[60:63], 0 sc1\n\t"
      "v_lshrrev_b32 v101, 4, v96\n\t"
      "v_and_b32 v102, 15, v96\n\t"
      "v_lshlrev_b32 v103, 2, v101\n\t"
      "v_xor_b32 v102, v102, v103\n\t"
      "v_lshlrev_b32 v108, 13, v101\n\t"
      "v_lshl_add_u32 v108, v102, 4, v108\n\t"
      "s_lshl_b32 s78, s82, 8\n\t"
      "s_lshl_b32 s79, %1, 16\n\t"
      "s_add_u32 s78, s78, s79\n\t"
      "v_add_u32 v108, s78, v108\n\t"
      "s_lshl_b32 s76, %1, 11\n\t"
      "s_mov_b32 s77, 0\n\t"
      "s_add_u32 m0, s76, 0\n\t"
      "s_add_u32 s68, s77, 0\n\t"
      "buffer_load_dwordx4 v108, s[64:67], s68 offen lds\n\t"
      "s_add_u32 m0, s76, 1024\n\t"
      "s_add_u32 s68, s77, 32768\n\t"
      "s_add_u32 s76, s76, 8192\n\t"
      "s_add_u32 s77, s77, 0x40000\n\t"
      "buffer_load_dwordx4 v108, s[64:67], s68 offen lds\n\t"
      "s_mov_b32 s84, 0xffff0000\n\t"
      "s_mov_b32 s86, 0\n\t"
      "s_lshl_b32 s85, %1, 13\n\t"
      "v_lshlrev_b32 v97, 4, v96\n\t"
      "v_lshrrev_b32 v101, 4, v96\n\t"
      "v_and_b32 v102, 3, v105\n\t"
      "v_lshlrev_b32 v100, 9, v101\n\t"
      "v_lshl_add_u32 v100, v102, 6, v100\n\t"
      "v_lshl_add_u32 v100, v104, 4, v100\n\t"
      "v_lshlrev_b32 v106, 10, v101\n\t"
      "v_lshl_add_u32 v106, v102, 8, v106\n\t"
      "v_lshl_add_u32 v106, v104, 4, v106\n\t"
      "s_lshl_b32 s74, %0, 14\n\t"
      "s_add_u32 s74, s74, s72\n\t"
      "s_add_u32 s74, s74, 6520832\n\t"
      "s_lshr_b32 s78, %0, 6\n\t"
      "v_min_u32 v104, 31, v96\n\t"
      "v_lshl_add_u32 v104, v104, 1, s78\n\t"
      "s_lshr_b32 s79, %0, 5\n\t"
      "s_and_b32 s83, s79, 1\n\t"
      "s_add_u32 s80, s85, 6520832\n\t"
      "v_lshl_add_u32 v101, v104, 15, s80\n\t"
      "s_mul_i32 s80, s82, 24576\n\t"
      "s_lshl_b32 s72, %1, 11\n\t"
      "s_add_u32 s80, s80, s72\n\t"
      "s_add_u32 s80, s80, 8617984\n\t"
      "s_lshl_b32 s72, s83, 13\n\t"
      "s_add_u32 s80, s80, s72\n\t"
      "v_mov_b32 v102, s80\n\t"
      "s_mov_b32 s72, 786432\n\t"
      "v_mad_u32_u24 v102, v104, s72, v102\n\t"
      "s_lshl_b32 s80, s82, 6\n\t"
      "s_lshl_b32 s72, s83, 2\n\t"
      "s_add_u32 s80, s80, s72\n\t"
      "s_add_u32 s80, s80, 256\n\t"
      "v_lshrrev_b32 v105, 2, v104\n\t"
      "v_lshl_add_u32 v103, v105, 11, s80\n\t"
      "v_and_b32 v104, 3, v104\n\t"
      "v_lshl_add_u32 v103, v104, 4, v103\n\t"
      "s_lshl_b32 s80, s82, 7\n\t"
      "s_lshl_b32 s79, s79, 2\n\t"
      "s_add_u32 s80, s80, s79\n\t"
      "s_add_u32 s80, s80, 33024\n\t"
      "v_writelane_b32 v103, s80, 0\n\t"
      "v_writelane_b32 v103, 0, 63\n\t"
      "s_cmp_eq_u32 %1, 0\n\t"
      "s_cselect_b64 s[90:91], 1, 0\n\t"
      "v_lshrrev_b32 v104, 4, v96\n\t"
      "v_bfe_u32 v105, v96, 2, 2\n\t"
      "v_and_b32 v110, 3, v96\n\t"
      "s_lshl_b32 s78, %1, 2\n\t"
      "v_lshl_add_u32 v109, v104, 3, v105\n\t"
      "v_lshlrev_b32 v109, 8, v109\n\t"
      "v_or_b32 v110, s78, v110\n\t"
      "v_lshlrev_b32 v105, 2, v105\n\t"
      "v_xor_b32 v110, v110, v105\n\t"
      "v_lshl_add_u32 v109, v110, 4, v109\n\t"
      "s_lshl_b32 s78, %1, 6\n\t"
      "s_add_u32 s78, s78, 131072\n\t"
      "v_add_u32 v110, s78, v106\n\t"
      "s_bfe_u32 s80, %0, 0x10005\n\t"
      "s_and_b32 s79, %1, 1\n\t"
      "v_bfe_u32 v105, v96, 4, 1\n\t"
      "v_lshlrev_b32 v105, 1, v105\n\t"
      "v_and_b32 v116, 7, v96\n\t"
      "v_xor_b32 v116, v116, v105\n\t"
      "s_lshl_b32 s78, s79, 2\n\t"
      "v_xor_b32 v116, s78, v116\n\t"
      "v_lshlrev_b32 v116, 4, v116\n\t"
      "v_lshrrev_b32 v105, 3, v96\n\t"
      "v_lshl_add_u32 v116, v105, 13, v116\n\t"
      "s_lshl_b32 s78, s82, 8\n\t"
      "s_lshl_b32 s72, s80, 7\n\t"
      "s_add_u32 s78, s78, s72\n\t"
      "s_lshl_b32 s72, %1, 16\n\t"
      "s_add_u32 s78, s78, s72\n\t"
      "v_add_u32 v116, s78, v116\n\t"
      "v_bfe_u32 v105, v96, 2, 2\n\t"
      "v_lshl_add_u32 v117, v104, 3, v105\n\t"
      "v_lshlrev_b32 v117, 7, v117\n\t"
      "v_bfe_u32 v105, v96, 3, 2\n\t"
      "v_xor_b32 v105, %1, v105\n\t"
      "v_lshl_add_u32 v117, v105, 5, v117\n\t"
      "v_and_b32 v105, 3, v96\n\t"
      "v_lshl_add_u32 v117, v105, 3, v117\n\t"
      "v_and_b32 v111, 15, v96\n\t"
      "v_lshlrev_b32 v111, 4, v111\n\t"
      "v_and_b32 v105, 1, v104\n\t"
      "v_lshl_add_u32 v111, v105, 10, v111\n\t"
      "v_lshrrev_b32 v105, 1, v104\n\t"
      "v_lshl_add_u32 v111, v105, 8, v111\n\t"
      "s_lshl_b32 s78, s79, 9\n\t"
      "s_add_u32 s78, s78, 16384\n\t"
      "s_lshl_b32 s80, s80, 12\n\t"
      "s_sub_u32 s78, s78, s80\n\t"
      "s_add_u32 s80, %1, 1\n\t"
      "s_lshr_b32 s80, s80, 1\n\t"
      "s_lshl_b32 s80, s80, 11\n\t"
      "s_sub_u32 s78, s78, s80\n\t"
      "v_add_u32 v111, s78, v111\n\t"
      "s_mov_b32 s69, 0\n\t"
      "s_add_u32 m0, s76, 0\n\t"
      "s_add_u32 s68, s77, 0\n\t"
      "buffer_load_dwordx4 v108, s[64:67], s68 offen lds\n\t"
      "s_add_u32 m0, s76, 1024\n\t"
      "s_add_u32 s68, s77, 32768\n\t"
      "s_add_u32 s76, s76, 8192\n\t"
      "s_add_u32 s77, s77, 0x40000\n\t"
      "buffer_load_dwordx4 v108, s[64:67], s68 offen lds\n\t"
      "s_add_u32 m0, s76, 0\n\t"
      "s_add_u32 s68, s77, 0\n\t"
      "buffer_load_dwordx4 v108, s[64:67], s68 offen lds\n\t"
      "s_add_u32 m0, s76, 1024\n\t"
      "s_add_u32 s68, s77, 32768\n\t"
      "s_add_u32 s76, s76, 8192\n\t"
      "s_add_u32 s77, s77, 0x40000\n\t"
      "buffer_load_dwordx4 v108, s[64:67], s68 offen lds\n\t"
      "s_add_u32 m0, s76, 0\n\t"
      "s_add_u32 s68, s77, 0\n\t"
      "buffer_load_dwordx4 v108, s[64:67], s68 offen lds\n\t"
      "s_add_u32 m0, s76, 1024\n\t"
      "s_add_u32 s68, s77, 32768\n\t"
      "s_add_u32 s76, s76, 8192\n\t"
      "s_add_u32 s77, s77, 0x40000\n\t"
      "buffer_load_dwordx4 v108, s[64:67], s68 offen lds\n\t"
      "s_mov_b64 s[88:89], 0\n\t"
      "s_waitcnt vmcnt(8)\n\t"
      "GLN%=:\n\t"
      "v_and_b32 v1, s84, v64\n\t"
      "v_lshlrev_b32 v0, 16, v64\n\t"
      "v_and_b32 v3, s84, v65\n\t"
      "v_lshlrev_b32 v2, 16, v65\n\t"
      "v_and_b32 v5, s84, v66\n\t"
      "v_lshlrev_b32 v4, 16, v66\n\t"
      "v_and_b32 v7, s84, v67\n\t"
      "v_lshlrev_b32 v6, 16, v67\n\t"
      "v_and_b32 v9, s84, v68\n\t"
      "v_lshlrev_b32 v8, 16, v68\n\t"
      "v_and_b32 v11, s84, v69\n\t"
      "v_lshlrev_b32 v10, 16, v69\n\t"
      "v_and_b32 v13, s84, v70\n\t"
      "v_lshlrev_b32 v12, 16, v70\n\t"
      "v_and_b32 v15, s84, v71\n\t"
      "v_lshlrev_b32 v14, 16, v71\n\t"
      "s_mov_b64 exec, s[88:89]\n\t"
      "s_add_u32 m0, s76, 0\n\t"
      "s_add_u32 s68, s77, 0\n\t"
      "buffer_load_dwordx4 v108, s[64:67], s68 offen lds\n\t"
      "s_mov_b64 exec, -1\n\t"
      "v_and_b32 v17, s84, v72\n\t"
      "v_lshlrev_b32 v16, 16, v72\n\t"
      "v_and_b32 v19, s84, v73\n\t"
      "v_lshlrev_b32 v18, 16, v73\n\t"
      "v_and_b32 v21, s84, v74\n\t"
      "v_lshlrev_b32 v20, 16, v74\n\t"
      "v_and_b32 v23, s84, v75\n\t"
      "v_lshlrev_b32 v22, 16, v75\n\t"
      "v_and_b32 v25, s84, v76\n\t"
      "v_lshlrev_b32 v24, 16, v76\n\t"
      "v_and_b32 v27, s84, v77\n\t"
      "v_lshlrev_b32 v26, 16, v77\n\t"
      "v_and_b32 v29, s84, v78\n\t"
      "v_lshlrev_b32 v28, 16, v78\n\t"
      "v_and_b32 v31, s84, v79\n\t"
      "v_lshlrev_b32 v30, 16, v79\n\t"
      "s_mov_b64 exec, s[88:89]\n\t"
      "s_add_u32 m0, s76, 1024\n\t"
      "s_add_u32 s68, s77, 32768\n\t"
      "s_add_u32 s76, s76, 8192\n\t"
      "s_add_u32 s77, s77, 0x40000\n\t"
      "buffer_load_dwordx4 v108, s[64:67], s68 offen lds\n\t"
      "s_mov_b64 exec, -1\n\t"
      "v_pk_add_f32 v[32:33], v[0:1], v[2:3]\n\t"
      "v_pk_add_f32 v[34:35], v[4:5], v[6:7]\n\t"
      "v_pk_add_f32 v[36:37], v[8:9], v[10:11]\n\t"
      "v_pk_add_f32 v[38:39], v[12:13], v[14:15]\n\t"
      "v_pk_add_f32 v[40:41], v[16:17], v[18:19]\n\t"
      "v_pk_add_f32 v[42:43], v[20:21], v[22:23]\n\t"
      "v_pk_add_f32 v[44:45], v[24:25], v[26:27]\n\t"
      "v_pk_add_f32 v[46:47], v[28:29], v[30:31]\n\t"
      "v_pk_add_f32 v[32:33], v[32:33], v[40:41]\n\t"
      "v_pk_add_f32 v[34:35], v[34:35], v[42:43]\n\t"
      "v_pk_add_f32 v[36:37], v[36:37], v[44:45]\n\t"
      "v_pk_add_f32 v[38:39], v[38:39], v[46:47]\n\t"
      "v_pk_add_f32 v[32:33], v[32:33], v[36:37]\n\t"
      "v_pk_add_f32 v[34:35], v[34:35], v[38:39]\n\t"
      "v_pk_add_f32 v[32:33], v[32:33], v[34:35]\n\t"
      "v_add_f32 v32, v32, v33\n\t"
      "s_mov_b64 exec, s[88:89]\n\t"
      "s_add_u32 m0, s76, 0\n\t"
      "s_add_u32 s68, s77, 0\n\t"
      "buffer_load_dwordx4 v108, s[64:67], s68 offen lds\n\t"
      "s_mov_b64 exec, -1\n\t"
      "v_add_f32_dpp v32, v32, v32 row_ror:4 row_mask:0xf bank_mask:0xf\n\t"
      "s_nop 1\n\t"
      "v_add_f32_dpp v32, v32, v32 row_ror:8 row_mask:0xf bank_mask:0xf\n\t"
      "v_mov_b32 v33, v32\n\t"
      "s_nop 1\n\t"
      "v_permlane16_swap_b32 v32, v33\n\t"
      "v_add_f32 v32, v32, v33\n\t"
      "v_mov_b32 v33, v32\n\t"
      "s_nop 1\n\t"
      "v_permlane32_swap_b32 v32, v33\n\t"
      "v_add_f32 v32, v32, v33\n\t"
      "v_mul_f32 v80, 0x3b000000, v32\n\t"
      "v_pk_add_f32 v[32:33], v[0:1], v[80:81] op_sel_hi:[1,0] neg_lo:[0,1] neg_hi:[0,1]\n\t"
      "v_pk_add_f32 v[34:35], v[2:3], v[80:81] op_sel_hi:[1,0] neg_lo:[0,1] neg_hi:[0,1]\n\t"
      "v_pk_add_f32 v[36:37], v[4:5], v[80:81] op_sel_hi:[1,0] neg_lo:[0,1] neg_hi:[0,1]\n\t"
      "v_pk_add_f32 v[38:39], v[6:7], v[80:81] op_sel_hi:[1,0] neg_lo:[0,1] neg_hi:[0,1]\n\t"
      "v_pk_add_f32 v[40:41], v[8:9], v[80:81] op_sel_hi:[1,0] neg_lo:[0,1] neg_hi:[0,1]\n\t"
      "v_pk_add_f32 v[42:43], v[10:11], v[80:81] op_sel_hi:[1,0] neg_lo:[0,1] neg_hi:[0,1]\n\t"
      "v_pk_add_f32 v[44:45], v[12:13], v[80:81] op_sel_hi:[1,0] neg_lo:[0,1] neg_hi:[0,1]\n\t"
      "s_mov_b64 exec, s[88:89]\n\t"
      "s_add_u32 m0, s76, 1024\n\t"
      "s_add_u32 s68, s77, 32768\n\t"
      "s_add_u32 s76, s76, 8192\n\t"
      "s_add_u32 s77, s77, 0x40000\n\t"
      "buffer_load_dwordx4 v108, s[64:67], s68 offen lds\n\t"
      "s_mov_b64 exec, -1\n\t"
      "v_pk_add_f32 v[46:47], v[14:15], v[80:81] op_sel_hi:[1,0] neg_lo:[0,1] neg_hi:[0,1]\n\t"
      "v_pk_add_f32 v[48:49], v[16:17], v[80:81] op_sel_hi:[1,0] neg_lo:[0,1] neg_hi:[0,1]\n\t"
      "v_pk_add_f32 v[50:51], v[18:19], v[80:81] op_sel_hi:[1,0] neg_lo:[0,1] neg_hi:[0,1]\n\t"
      "v_pk_add_f32 v[52:53], v[20:21], v[80:81] op_sel_hi:[1,0] neg_lo:[0,1] neg_hi:[0,1]\n\t"
      "v_pk_add_f32 v[54:55], v[22:23], v[80:81] op_sel_hi:[1,0] neg_lo:[0,1] neg_hi:[0,1]\n\t"
      "v_pk_add_f32 v[56:57], v[24:25], v[80:81] op_sel_hi:[1,0] neg_lo:[0,1] neg_hi:[0,1]\n\t"
      "v_pk_add_f32 v[58:59], v[26:27], v[80:81] op_sel_hi:[1,0] neg_lo:[0,1] neg_hi:[0,1]\n\t"
      "v_pk_add_f32 v[60:61], v[28:29], v[80:81] op_sel_hi:[1,0] neg_lo:[0,1] neg_hi:[0,1]\n\t"
      "v_pk_add_f32 v[62:63], v[30:31], v[80:81] op_sel_hi:[1,0] neg_lo:[0,1] neg_hi:[0,1]\n\t"
      "v_pk_mul_f32 v[64:65], v[32:33], v[32:33]\n\t"
      "v_pk_mul_f32 v[66:67], v[34:35], v[34:35]\n\t"
      "v_pk_mul_f32 v[68:69], v[36:37], v[36:37]\n\t"
      "v_pk_mul_f32 v[70:71], v[38:39], v[38:39]\n\t"
      "v_pk_fma_f32 v[64:65], v[40:41], v[40:41], v[64:65]\n\t"
      "v_pk_fma_f32 v[66:67], v[42:43], v[42:43], v[66:67]\n\t"
      "v_pk_fma_f32 v[68:69], v[44:45], v[44:45], v[68:69]\n\t"
      "s_mov_b64 exec, s[88:89]\n\t"
      "s_add_u32 m0, s76, 0\n\t"
      "s_add_u32 s68, s77, 0\n\t"
      "buffer_load_dwordx4 v108, s[64:67], s68 offen lds\n\t"
      "s_mov_b64 exec, -1\n\t"
      "v_pk_fma_f32 v[70:71], v[46:47], v[46:47], v[70:71]\n\t"
      "v_pk_fma_f32 v[64:65], v[48:49], v[48:49], v[64:65]\n\t"
      "v_pk_fma_f32 v[66:67], v[50:51], v[50:51], v[66:67]\n\t"
      "v_pk_fma_f32 v[68:69], v[52:53], v[52:53], v[68:69]\n\t"
      "v_pk_fma_f32 v[70:71], v[54:55], v[54:55], v[70:71]\n\t"
      "v_pk_fma_f32 v[64:65], v[56:57], v[56:57], v[64:65]\n\t"
      "v_pk_fma_f32 v[66:67], v[58:59], v[58:59], v[66:67]\n\t"
      "v_pk_fma_f32 v[68:69], v[60:61], v[60:61], v[68:69]\n\t"
      "v_pk_fma_f32 v[70:71], v[62:63], v[62:63], v[70:71]\n\t"
      "v_pk_add_f32 v[64:65], v[64:65], v[66:67]\n\t"
      "v_pk_add_f32 v[68:69], v[68:69], v[70:71]\n\t"
      "v_pk_add_f32 v[64:65], v[64:65], v[68:69]\n\t"
      "v_add_f32 v64, v64, v65\n\t"
      "s_nop 1\n\t"
      "v_add_f32_dpp v64, v64, v64 row_ror:4 row_mask:0xf bank_mask:0xf\n\t"
      "s_nop 1\n\t"
      "v_add_f32_dpp v64, v64, v64 row_ror:8 row_mask:0xf bank_mask:0xf\n\t"
      "v_mov_b32 v65, v64\n\t"
      "s_mov_b64 exec, s[88:89]\n\t"
      "s_add_u32 m0, s76, 1024\n\t"
      "s_add_u32 s68, s77, 32768\n\t"
      "s_add_u32 s76, s76, 8192\n\t"
      "s_add_u32 s77, s77, 0x40000\n\t"
      "buffer_load_dwordx4 v108, s[64:67], s68 offen lds\n\t"
      "s_mov_b64 exec, -1\n\t"
      "v_permlane16_swap_b32 v64, v65\n\t"
      "v_add_f32 v64, v64, v65\n\t"
      "v_mov_b32 v65, v64\n\t"
      "s_nop 1\n\t"
      "v_permlane32_swap_b32 v64, v65\n\t"
      "v_add_f32 v64, v64, v65\n\t"
      "v_mul_f32 v81, 0x3b000000, v64\n\t"
      "v_cvt_pk_bf16_f32 v80, v80, v81\n\t"
      "v_and_b32 v81, s84, v80\n\t"
      "v_lshlrev_b32 v80, 16, v80\n\t"
      "v_add_f32 v81, 0x3727c5ac, v81\n\t"
      "v_pk_add_f32 v[32:33], v[0:1], v[80:81] op_sel_hi:[1,0] neg_lo:[0,1] neg_hi:[0,1]\n\t"
      "v_pk_add_f32 v[34:35], v[2:3], v[80:81] op_sel_hi:[1,0] neg_lo:[0,1] neg_hi:[0,1]\n\t"
      "v_pk_add_f32 v[36:37], v[4:5], v[80:81] op_sel_hi:[1,0] neg_lo:[0,1] neg_hi:[0,1]\n\t"
      "v_pk_add_f32 v[38:39], v[6:7], v[80:81] op_sel_hi:[1,0] neg_lo:[0,1] neg_hi:[0,1]\n\t"
      "v_pk_add_f32 v[40:41], v[8:9], v[80:81] op_sel_hi:[1,0] neg_lo:[0,1] neg_hi:[0,1]\n\t"
      "v_pk_add_f32 v[42:43], v[10:11], v[80:81] op_sel_hi:[1,0] neg_lo:[0,1] neg_hi:[0,1]\n\t"
      "s_mov_b64 exec, s[88:89]\n\t"
      "s_add_u32 m0, s76, 0\n\t"
      "s_add_u32 s68, s77, 0\n\t"
      "buffer_load_dwordx4 v108, s[64:67], s68 offen lds\n\t"
      "s_mov_b64 exec, -1\n\t"
      "v_pk_add_f32 v[44:45], v[12:13], v[80:81] op_sel_hi:[1,0] neg_lo:[0,1] neg_hi:[0,1]\n\t"
      "v_pk_add_f32 v[46:47], v[14:15], v[80:81] op_sel_hi:[1,0] neg_lo:[0,1] neg_hi:[0,1]\n\t"
      "v_pk_add_f32 v[48:49], v[16:17], v[80:81] op_sel_hi:[1,0] neg_lo:[0,1] neg_hi:[0,1]\n\t"
      "v_pk_add_f32 v[50:51], v[18:19], v[80:81] op_sel_hi:[1,0] neg_lo:[0,1] neg_hi:[0,1]\n\t"
      "v_pk_add_f32 v[52:53], v[20:21], v[80:81] op_sel_hi:[1,0] neg_lo:[0,1] neg_hi:[0,1]\n\t"
      "v_pk_add_f32 v[54:55], v[22:23], v[80:81] op_sel_hi:[1,0] neg_lo:[0,1] neg_hi:[0,1]\n\t"
      "v_pk_add_f32 v[56:57], v[24:25], v[80:81] op_sel_hi:[1,0] neg_lo:[0,1] neg_hi:[0,1]\n\t"
      "v_pk_add_f32 v[58:59], v[26:27], v[80:81] op_sel_hi:[1,0] neg_lo:[0,1] neg_hi:[0,1]\n\t"
      "v_pk_add_f32 v[60:61], v[28:29], v[80:81] op_sel_hi:[1,0] neg_lo:[0,1] neg_hi:[0,1]\n\t"
      "v_pk_add_f32 v[62:63], v[30:31], v[80:81] op_sel_hi:[1,0] neg_lo:[0,1] neg_hi:[0,1]\n\t"
      "v_cvt_pk_bf16_f32 v81, v81, v81\n\t"
      "v_and_b32 v81, s84, v81\n\t"
      "v_sqrt_f32 v81, v81\n\t"
      "v_cvt_pk_bf16_f32 v32, v32, v33\n\t"
      "v_cvt_pk_bf16_f32 v34, v34, v35\n\t"
      "v_cvt_pk_bf16_f32 v36, v36, v37\n\t"
      "s_mov_b64 exec, s[88:89]\n\t"
      "s_add_u32 m0, s76, 1024\n\t"
      "s_add_u32 s68, s77, 32768\n\t"
      "s_add_u32 s76, s76, 8192\n\t"
      "s_add_u32 s77, s77, 0x40000\n\t"
      "buffer_load_dwordx4 v108, s[64:67], s68 offen lds\n\t"
      "s_mov_b64 exec, -1\n\t"
      "v_cvt_pk_bf16_f32 v38, v38, v39\n\t"
      "v_cvt_pk_bf16_f32 v40, v40, v41\n\t"
      "v_cvt_pk_bf16_f32 v42, v42, v43\n\t"
      "v_cvt_pk_bf16_f32 v44, v44, v45\n\t"
      "v_cvt_pk_bf16_f32 v46, v46, v47\n\t"
      "v_cvt_pk_bf16_f32 v48, v48, v49\n\t"
      "v_cvt_pk_bf16_f32 v50, v50, v51\n\t"
      "v_cvt_pk_bf16_f32 v52, v52, v53\n\t"
      "v_cvt_pk_bf16_f32 v54, v54, v55\n\t"
      "v_cvt_pk_bf16_f32 v56, v56, v57\n\t"
      "v_cvt_pk_bf16_f32 v58, v58, v59\n\t"
      "v_cvt_pk_bf16_f32 v60, v60, v61\n\t"
      "v_cvt_pk_bf16_f32 v62, v62, v63\n\t"
      "v_cvt_pk_bf16_f32 v81, v81, v81\n\t"
      "v_and_b32 v81, s84, v81\n\t"
      "v_rcp_f32 v80, v81\n\t"
      "s_mov_b64 exec, s[88:89]\n\t"
      "s_add_u32 m0, s76, 0\n\t"
      "s_add_u32 s68, s77, 0\n\t"
      "buffer_load_dwordx4 v108, s[64:67], s68 offen lds\n\t"
      "s_mov_b64 exec, -1\n\t"
      "v_and_b32 v33, s84, v32\n\t"
      "v_lshlrev_b32 v32, 16, v32\n\t"
      "v_and_b32 v35, s84, v34\n\t"
      "v_lshlrev_b32 v34, 16, v34\n\t"
      "v_and_b32 v37, s84, v36\n\t"
      "v_lshlrev_b32 v36, 16, v36\n\t"
      "v_and_b32 v39, s84, v38\n\t"
      "v_lshlrev_b32 v38, 16, v38\n\t"
      "v_pk_mul_f32 v[32:33], v[32:33], v[80:81] op_sel_hi:[1,0]\n\t"
      "v_pk_mul_f32 v[34:35], v[34:35], v[80:81] op_sel_hi:[1,0]\n\t"
      "v_pk_mul_f32 v[36:37], v[36:37], v[80:81] op_sel_hi:[1,0]\n\t"
      "v_pk_mul_f32 v[38:39], v[38:39], v[80:81] op_sel_hi:[1,0]\n\t"
      "v_cvt_pk_bf16_f32 v64, v32, v33\n\t"
      "v_cvt_pk_bf16_f32 v65, v34, v35\n\t"
      "v_cvt_pk_bf16_f32 v66, v36, v37\n\t"
      "v_cvt_pk_bf16_f32 v67, v38, v39\n\t"
      "s_mov_b64 exec, s[88:89]\n\t"
      "s_add_u32 m0, s76, 1024\n\t"
      "s_add_u32 s68, s77, 32768\n\t"
      "s_add_u32 s76, s76, 8192\n\t"
      "s_add_u32 s77, s77, 0x40000\n\t"
      "buffer_load_dwordx4 v108, s[64:67], s68 offen lds\n\t"
      "s_mov_b64 exec, -1\n\t"
      "v_and_b32 v41, s84, v40\n\t"
      "v_lshlrev_b32 v40, 16, v40\n\t"
      "v_and_b32 v43, s84, v42\n\t"
      "v_lshlrev_b32 v42, 16, v42\n\t"
      "v_and_b32 v45, s84, v44\n\t"
      "v_lshlrev_b32 v44, 16, v44\n\t"
      "v_and_b32 v47, s84, v46\n\t"
      "v_lshlrev_b32 v46, 16, v46\n\t"
      "v_pk_mul_f32 v[40:41], v[40:41], v[80:81] op_sel_hi:[1,0]\n\t"
      "v_pk_mul_f32 v[42:43], v[42:43], v[80:81] op_sel_hi:[1,0]\n\t"
      "v_pk_mul_f32 v[44:45], v[44:45], v[80:81] op_sel_hi:[1,0]\n\t"
      "v_pk_mul_f32 v[46:47], v[46:47], v[80:81] op_sel_hi:[1,0]\n\t"
      "v_cvt_pk_bf16_f32 v68, v40, v41\n\t"
      "v_cvt_pk_bf16_f32 v69, v42, v43\n\t"
      "v_cvt_pk_bf16_f32 v70, v44, v45\n\t"
      "v_cvt_pk_bf16_f32 v71, v46, v47\n\t"
      "s_mov_b64 exec, s[88:89]\n\t"
      "s_add_u32 m0, s76, 0\n\t"
      "s_add_u32 s68, s77, 0\n\t"
      "buffer_load_dwordx4 v108, s[64:67], s68 offen lds\n\t"
      "s_mov_b64 exec, -1\n\t"
      "v_and_b32 v49, s84, v48\n\t"
      "v_lshlrev_b32 v48, 16, v48\n\t"
      "v_and_b32 v51, s84, v50\n\t"
      "v_lshlrev_b32 v50, 16, v50\n\t"
      "v_and_b32 v53, s84, v52\n\t"
      "v_lshlrev_b32 v52, 16, v52\n\t"
      "v_and_b32 v55, s84, v54\n\t"
      "v_lshlrev_b32 v54, 16, v54\n\t"
      "v_pk_mul_f32 v[48:49], v[48:49], v[80:81] op_sel_hi:[1,0]\n\t"
      "v_pk_mul_f32 v[50:51], v[50:51], v[80:81] op_sel_hi:[1,0]\n\t"
      "v_pk_mul_f32 v[52:53], v[52:53], v[80:81] op_sel_hi:[1,0]\n\t"
      "v_pk_mul_f32 v[54:55], v[54:55], v[80:81] op_sel_hi:[1,0]\n\t"
      "v_cvt_pk_bf16_f32 v72, v48, v49\n\t"
      "v_cvt_pk_bf16_f32 v73, v50, v51\n\t"
      "v_cvt_pk_bf16_f32 v74, v52, v53\n\t"
      "v_cvt_pk_bf16_f32 v75, v54, v55\n\t"
      "s_mov_b64 exec, s[88:89]\n\t"
      "s_add_u32 m0, s76, 1024\n\t"
      "s_add_u32 s68, s77, 32768\n\t"
      "s_add_u32 s76, s76, 8192\n\t"
      "s_add_u32 s77, s77, 0x40000\n\t"
      "buffer_load_dwordx4 v108, s[64:67], s68 offen lds\n\t"
      "s_mov_b64 exec, -1\n\t"
      "v_and_b32 v57, s84, v56\n\t"
      "v_lshlrev_b32 v56, 16, v56\n\t"
      "v_and_b32 v59, s84, v58\n\t"
      "v_lshlrev_b32 v58, 16, v58\n\t"
      "v_and_b32 v61, s84, v60\n\t"
      "v_lshlrev_b32 v60, 16, v60\n\t"
      "v_and_b32 v63, s84, v62\n\t"
      "v_lshlrev_b32 v62, 16, v62\n\t"
      "v_pk_mul_f32 v[56:57], v[56:57], v[80:81] op_sel_hi:[1,0]\n\t"
      "v_pk_mul_f32 v[58:59], v[58:59], v[80:81] op_sel_hi:[1,0]\n\t"
      "v_pk_mul_f32 v[60:61], v[60:61], v[80:81] op_sel_hi:[1,0]\n\t"
      "v_pk_mul_f32 v[62:63], v[62:63], v[80:81] op_sel_hi:[1,0]\n\t"
      "v_cvt_pk_bf16_f32 v76, v56, v57\n\t"
      "v_cvt_pk_bf16_f32 v77, v58, v59\n\t"
      "v_cvt_pk_bf16_f32 v78, v60, v61\n\t"
      "v_cvt_pk_bf16_f32 v79, v62, v63\n\t"
      "s_cmp_lg_u32 s69, 0\n\t"
      "s_cbranch_scc1 GLN1%=\n\t"
      "buffer_store_dwordx4 v[64:67], v106, s[60:63], s74 offen sc1\n\t"
      "s_add_u32 s74, s74, 4096\n\t"
      "buffer_store_dwordx4 v[68:71], v106, s[60:63], s74 offen sc1\n\t"
      "s_add_u32 s74, s74, 4096\n\t"
      "buffer_store_dwordx4 v[72:75], v106, s[60:63], s74 offen sc1\n\t"
      "s_add_u32 s74, s74, 4096\n\t"
      "buffer_store_dwordx4 v[76:79], v106, s[60:63], s74 offen sc1\n\t"
      "s_add_u32 s74, s74, 4096\n\t"
      "s_sub_u32 s76, s76, 49152\n\t"
      "s_sub_u32 s77, s77, 1572864\n\t"
      "s_mov_b64 s[88:89], -1\n\t"
      "v_readfirstlane_b32 %3, v107\n\t"
      "v_mov_b64 v[64:65], v[128:129]\n\t"
      "v_mov_b64 v[66:67], v[130:131]\n\t"
      "v_mov_b64 v[68:69], v[132:133]\n\t"
      "v_mov_b64 v[70:71], v[134:135]\n\t"
      "v_mov_b64 v[72:73], v[136:137]\n\t"
      "v_mov_b64 v[74:75], v[138:139]\n\t"
      "v_mov_b64 v[76:77], v[140:141]\n\t"
      "v_mov_b64 v[78:79], v[142:143]\n\t"
      "s_mul_i32 %3, %3, 0x9e3779b1\n\t"
      "s_add_u32 %3, %3, 0x7f4a7c15\n\t"
      "s_mov_b32 s69, 1\n\t"
      "s_branch GLN%=\n\t"
      "GLN1%=:\n\t"
      "ds_write_b128 v110, v[64:67]\n\t"
      "ds_write_b128 v110, v[68:71] offset:4096\n\t"
      "ds_write_b128 v110, v[72:75] offset:8192\n\t"
      "ds_write_b128 v110, v[76:79] offset:12288\n\t"
      "s_cmp_eq_u32 s69, 2\n\t"
      "s_cbranch_scc1 GLN2%=\n\t"
      "v_mov_b32 v107, %3\n\t"
      "v_add_u32 v110, 0x4000, v110\n\t"
      "s_lshl_b32 s79, %0, 7\n\t"
      "s_add_u32 s79, s79, 37120\n\t"
      "s_waitcnt vmcnt(12) lgkmcnt(0)\n\t"
      "s_barrier\n\t"
      "s_mov_b64 exec, s[90:91]\n\t"
      "buffer_store_dword v107, off, s[60:63], s79 sc1\n\t"
      "s_mov_b64 exec, -1\n\t"
      "v_mov_b64 v[64:65], v[144:145]\n\t"
      "v_mov_b64 v[66:67], v[146:147]\n\t"
      "v_mov_b64 v[68:69], v[148:149]\n\t"
      "v_mov_b64 v[70:71], v[150:151]\n\t"
      "v_mov_b64 v[72:73], v[152:153]\n\t"
      "v_mov_b64 v[74:75], v[154:155]\n\t"
      "v_mov_b64 v[76:77], v[156:157]\n\t"
      "v_mov_b64 v[78:79], v[158:159]\n\t"
      "s_mov_b32 s69, 2\n\t"
      "s_branch GLN%=\n\t"
      "GLN2%=:\n\t"
      "s_waitcnt lgkmcnt(10)\n\t"
      "ds_read_b64_tr_b16 a[0:1], v109\n\t"
      "ds_read_b64_tr_b16 a[2:3], v109 offset:1024\n\t"
      "ds_read_b64_tr_b16 a[4:5], v109 offset:8\n\t"
      "ds_read_b64_tr_b16 a[6:7], v109 offset:1032\n\t"
      "s_waitcnt lgkmcnt(10)\n\t"
      "ds_read_b64_tr_b16 a[8:9], v109 offset:8192\n\t"
      "ds_read_b64_tr_b16 a[10:11], v109 offset:9216\n\t"
      "ds_read_b64_tr_b16 a[12:13], v109 offset:8200\n\t"
      "ds_read_b64_tr_b16 a[14:15], v109 offset:9224\n\t"
      "s_waitcnt lgkmcnt(10)\n\t"
      "ds_read_b64_tr_b16 a[16:17], v109 offset:16384\n\t"
      "ds_read_b64_tr_b16 a[18:19], v109 offset:17408\n\t"
      "ds_read_b64_tr_b16 a[20:21], v109 offset:16392\n\t"
      "ds_read_b64_tr_b16 a[22:23], v109 offset:17416\n\t"
      "s_waitcnt lgkmcnt(10)\n\t"
      "ds_read_b64_tr_b16 a[24:25], v109 offset:24576\n\t"
      "ds_read_b64_tr_b16 a[26:27], v109 offset:25600\n\t"
      "ds_read_b64_tr_b16 a[28:29], v109 offset:24584\n\t"
      "ds_read_b64_tr_b16 a[30:31], v109 offset:25608\n\t"
      "v_lshlrev_b32 v106, 8, v96\n\t"
      "s_mov_b32 s79, 37120\n\t"
      "s_mov_b32 s81, 0x40000\n\t"
      "v_add_u32 v98, 131072, v97\n\t"
      "v_readlane_b32 s73, v102, 0\n\t"
      "v_readlane_b32 s75, v103, 0\n\t"
      "s_mov_b64 s[64:65], s[40:41]\n\t"
      "s_lshl_b32 s74, %1, 10\n\t"
      "v_readlane_b32 s72, v101, 1\n\t"
      "s_add_u32 s76, s85, 0\n\t"
      "s_waitcnt vmcnt(12)\n\t"
      "s_barrier\n\t"
      "s_waitcnt lgkmcnt(10)\n\t"
      "ds_read_b64_tr_b16 a[32:33], v109 offset:32768\n\t"
      "ds_read_b64_tr_b16 a[34:35], v109 offset:33792\n\t"
      "ds_read_b64_tr_b16 a[36:37], v109 offset:32776\n\t"
      "ds_read_b64_tr_b16 a[38:39], v109 offset:33800\n\t"
      "s_waitcnt lgkmcnt(10)\n\t"
      "ds_read_b64_tr_b16 a[40:41], v109 offset:40960\n\t"
      "ds_read_b64_tr_b16 a[42:43], v109 offset:41984\n\t"
      "ds_read_b64_tr_b16 a[44:45], v109 offset:40968\n\t"
      "ds_read_b64_tr_b16 a[46:47], v109 offset:41992\n\t"
      "s_waitcnt lgkmcnt(10)\n\t"
      "ds_read_b64_tr_b16 a[48:49], v109 offset:49152\n\t"
      "ds_read_b64_tr_b16 a[50:51], v109 offset:50176\n\t"
      "ds_read_b64_tr_b16 a[52:53], v109 offset:49160\n\t"
      "ds_read_b64_tr_b16 a[54:55], v109 offset:50184\n\t"
      "s_waitcnt lgkmcnt(10)\n\t"
      "ds_read_b64_tr_b16 a[56:57], v109 offset:57344\n\t"
      "ds_read_b64_tr_b16 a[58:59], v109 offset:58368\n\t"
      "ds_read_b64_tr_b16 a[60:61], v109 offset:57352\n\t"
      "ds_read_b64_tr_b16 a[62:63], v109 offset:58376\n\t"
      "v_add_u32 v109, 0x10000, v109\n\t"
      "s_waitcnt lgkmcnt(10)\n\t"
      "ds_read_b64_tr_b16 a[64:65], v109\n\t"
      "ds_read_b64_tr_b16 a[66:67], v109 offset:1024\n\t"
      "ds_read_b64_tr_b16 a[68:69], v109 offset:8\n\t"
      "ds_read_b64_tr_b16 a[70:71], v109 offset:1032\n\t"
      "s_waitcnt lgkmcnt(10)\n\t"
      "ds_read_b64_tr_b16 a[72:73], v109 offset:8192\n\t"
      "ds_read_b64_tr_b16 a[74:75], v109 offset:9216\n\t"
      "ds_read_b64_tr_b16 a[76:77], v109 offset:8200\n\t"
      "ds_read_b64_tr_b16 a[78:79], v109 offset:9224\n\t"
      "s_waitcnt vmcnt(0)\n\t"
      "s_barrier\n\t"
      "buffer_load_dword v128, v106, s[60:63], s79 offen sc1\n\t"
      "buffer_load_dword v129, v106, s[60:63], s79 offen offset:128 sc1\n\t"
      "s_waitcnt lgkmcnt(10)\n\t"
      "ds_read_b64_tr_b16 a[80:81], v109 offset:16384\n\t"
      "ds_read_b64_tr_b16 a[82:83], v109 offset:17408\n\t"
      "ds_read_b64_tr_b16 a[84:85], v109 offset:16392\n\t"
      "ds_read_b64_tr_b16 a[86:87], v109 offset:17416\n\t"
      "s_waitcnt lgkmcnt(10)\n\t"
      "ds_read_b64_tr_b16 a[88:89], v109 offset:24576\n\t"
      "ds_read_b64_tr_b16 a[90:91], v109 offset:25600\n\t"
      "ds_read_b64_tr_b16 a[92:93], v109 offset:24584\n\t"
      "ds_read_b64_tr_b16 a[94:95], v109 offset:25608\n\t"
      "s_waitcnt lgkmcnt(10)\n\t"
      "ds_read_b64_tr_b16 a[96:97], v109 offset:32768\n\t"
      "ds_read_b64_tr_b16 a[98:99], v109 offset:33792\n\t"
      "ds_read_b64_tr_b16 a[100:101], v109 offset:32776\n\t"
      "ds_read_b64_tr_b16 a[102:103], v109 offset:33800\n\t"
      "s_waitcnt lgkmcnt(10)\n\t"
      "ds_read_b64_tr_b16 a[104:105], v109 offset:40960\n\t"
      "ds_read_b64_tr_b16 a[106:107], v109 offset:41984\n\t"
      "ds_read_b64_tr_b16 a[108:109], v109 offset:40968\n\t"
      "ds_read_b64_tr_b16 a[110:111], v109 offset:41992\n\t"
      "s_waitcnt lgkmcnt(10)\n\t"
      "ds_read_b64_tr_b16 a[112:113], v109 offset:49152\n\t"
      "ds_read_b64_tr_b16 a[114:115], v109 offset:50176\n\t"
      "ds_read_b64_tr_b16 a[116:117], v109 offset:49160\n\t"
      "ds_read_b64_tr_b16 a[118:119], v109 offset:50184\n\t"
      "s_waitcnt lgkmcnt(10)\n\t"
      "ds_read_b64_tr_b16 a[120:121], v109 offset:57344\n\t"
      "ds_read_b64_tr_b16 a[122:123], v109 offset:58368\n\t"
      "ds_read_b64_tr_b16 a[124:125], v109 offset:57352\n\t"
      "ds_read_b64_tr_b16 a[126:127], v109 offset:58376\n\t"
      "s_mov_b64 s[88:89], 0\n\t"
      "s_mov_b64 s[38:39], 0\n\t"
      "s_waitcnt lgkmcnt(4)\n\t"
      "s_mov_b32 s71, 0\n\t"
      "GKV%=:\n\t"
      "ds_read_b128 v[0:3], v98\n\t"
      "ds_read_b128 v[4:7], v98 offset:1024\n\t"
      "ds_read_b128 v[8:11], v98 offset:2048\n\t"
      "ds_read_b128 v[12:15], v98 offset:3072\n\t"
      "ds_read_b128 v[16:19], v98 offset:4096\n\t"
      "ds_read_b128 v[20:23], v98 offset:5120\n\t"
      "ds_read_b128 v[24:27], v98 offset:6144\n\t"
      "ds_read_b128 v[28:31], v98 offset:7168\n\t"
      "ds_read_b128 v[32:35], v98 offset:8192\n\t"
      "ds_read_b128 v[36:39], v98 offset:9216\n\t"
      "s_waitcnt lgkmcnt(9)\n\t"
      "v_mfma_f32_16x16x32_bf16 v[64:67], a[0:3], v[0:3], 0\n\t"
      "ds_read_b128 v[40:43], v98 offset:10240\n\t"
      "v_mfma_f32_16x16x32_bf16 v[68:71], a[4:7], v[0:3], 0\n\t"
      "s_waitcnt lgkmcnt(9)\n\t"
      "v_mfma_f32_16x16x32_bf16 v[64:67], a[8:11], v[4:7], v[64:67]\n\t"
      "ds_read_b128 v[44:47], v98 offset:11264\n\t"
      "v_mfma_f32_16x16x32_bf16 v[68:71], a[12:15], v[4:7], v[68:71]\n\t"
      "s_waitcnt lgkmcnt(9)\n\t"
      "v_mfma_f32_16x16x32_bf16 v[64:67], a[16:19], v[8:11], v[64:67]\n\t"
      "ds_read_b128 v[48:51], v98 offset:12288\n\t"
      "v_mfma_f32_16x16x32_bf16 v[68:71], a[20:23], v[8:11], v[68:71]\n\t"
      "s_waitcnt lgkmcnt(9)\n\t"
      "v_mfma_f32_16x16x32_bf16 v[64:67], a[24:27], v[12:15], v[64:67]\n\t"
      "ds_read_b128 v[52:55], v98 offset:13312\n\t"
      "v_mfma_f32_16x16x32_bf16 v[68:71], a[28:31], v[12:15], v[68:71]\n\t"
      "s_waitcnt lgkmcnt(9)\n\t"
      "v_mfma_f32_16x16x32_bf16 v[64:67], a[32:35], v[16:19], v[64:67]\n\t"
      "ds_read_b128 v[56:59], v98 offset:14336\n\t"
      "v_mfma_f32_16x16x32_bf16 v[68:71], a[36:39], v[16:19], v[68:71]\n\t"
      "s_waitcnt lgkmcnt(9)\n\t"
      "v_mfma_f32_16x16x32_bf16 v[64:67], a[40:43], v[20:23], v[64:67]\n\t"
      "ds_read_b128 v[60:63], v98 offset:15360\n\t"
      "v_mfma_f32_16x16x32_bf16 v[68:71], a[44:47], v[20:23], v[68:71]\n\t"
      "s_waitcnt lgkmcnt(9)\n\t"
      "v_mfma_f32_16x16x32_bf16 v[64:67], a[48:51], v[24:27], v[64:67]\n\t"
      "ds_read_b128 v[0:3], v98 offset:16384\n\t"
      "v_mfma_f32_16x16x32_bf16 v[68:71], a[52:55], v[24:27], v[68:71]\n\t"
      "s_waitcnt lgkmcnt(9)\n\t"
      "v_mfma_f32_16x16x32_bf16 v[64:67], a[56:59], v[28:31], v[64:67]\n\t"
      "ds_read_b128 v[4:7], v98 offset:17408\n\t"
      "v_mfma_f32_16x16x32_bf16 v[68:71], a[60:63], v[28:31], v[68:71]\n\t"
      "s_waitcnt lgkmcnt(9)\n\t"
      "v_mfma_f32_16x16x32_bf16 v[64:67], a[64:67], v[32:35], v[64:67]\n\t"
      "ds_read_b128 v[8:11], v98 offset:18432\n\t"
      "v_mfma_f32_16x16x32_bf16 v[68:71], a[68:71], v[32:35], v[68:71]\n\t"
      "s_waitcnt lgkmcnt(9)\n\t"
      "v_mfma_f32_16x16x32_bf16 v[64:67], a[72:75], v[36:39], v[64:67]\n\t"
      "ds_read_b128 v[12:15], v98 offset:19456\n\t"
      "v_mfma_f32_16x16x32_bf16 v[68:71], a[76:79], v[36:39], v[68:71]\n\t"
      "s_waitcnt lgkmcnt(9)\n\t"
      "v_mfma_f32_16x16x32_bf16 v[64:67], a[80:83], v[40:43], v[64:67]\n\t"
      "ds_read_b128 v[16:19], v98 offset:20480\n\t"
      "v_mfma_f32_16x16x32_bf16 v[68:71], a[84:87], v[40:43], v[68:71]\n\t"
      "s_waitcnt lgkmcnt(9)\n\t"
      "v_mfma_f32_16x16x32_bf16 v[64:67], a[88:91], v[44:47], v[64:67]\n\t"
      "ds_read_b128 v[20:23], v98 offset:21504\n\t"
      "v_mfma_f32_16x16x32_bf16 v[68:71], a[92:95], v[44:47], v[68:71]\n\t"
      "s_waitcnt lgkmcnt(9)\n\t"
      "v_mfma_f32_16x16x32_bf16 v[64:67], a[96:99], v[48:51], v[64:67]\n\t"
      "ds_read_b128 v[24:27], v98 offset:22528\n\t"
      "v_mfma_f32_16x16x32_bf16 v[68:71], a[100:103], v[48:51], v[68:71]\n\t"
      "s_cmp_lg_u32 s71, 0\n\t"
      "s_barrier\n\t"
      "s_cbranch_scc1 GKV0%=\n\t"
      "s_waitcnt vmcnt(0)\n\t"
      "v_bitop3_b32 v128, v128, v129, %3 bitop3:0x7e\n\t"
      "v_cmp_ne_u32 vcc, 0, v128\n\t"
      "s_nop 0\n\t"
      "s_cmp_eq_u64 vcc, 0\n\t"
      "s_cselect_b64 s[36:37], -1, 0\n\t"
      "GKV0%=:\n\t"
      "s_waitcnt lgkmcnt(9)\n\t"
      "v_mfma_f32_16x16x32_bf16 v[64:67], a[104:107], v[52:55], v[64:67]\n\t"
      "ds_read_b128 v[28:31], v98 offset:23552\n\t"
      "v_mfma_f32_16x16x32_bf16 v[68:71], a[108:111], v[52:55], v[68:71]\n\t"
      "s_add_u32 m0, s76, 0\n\t"
      "s_mov_b64 exec, s[36:37]\n\t"
      "buffer_load_dwordx4 v97, s[60:63], s72 offen sc1 lds\n\t"
      "s_mov_b64 exec, -1\n\t"
      "s_waitcnt lgkmcnt(9)\n\t"
      "v_mfma_f32_16x16x32_bf16 v[64:67], a[112:115], v[56:59], v[64:67]\n\t"
      "ds_read_b128 v[32:35], v98 offset:24576\n\t"
      "v_mfma_f32_16x16x32_bf16 v[68:71], a[116:119], v[56:59], v[68:71]\n\t"
      "s_waitcnt lgkmcnt(9)\n\t"
      "v_mfma_f32_16x16x32_bf16 v[64:67], a[120:123], v[60:63], v[64:67]\n\t"
      "ds_read_b128 v[36:39], v98 offset:25600\n\t"
      "v_mfma_f32_16x16x32_bf16 v[68:71], a[124:127], v[60:63], v[68:71]\n\t"
      "s_add_u32 m0, s76, 0\n\t"
      "s_mov_b64 exec, s[36:37]\n\t"
      "buffer_load_dwordx4 v97, s[60:63], s72 offen offset:1024 sc1 lds\n\t"
      "s_mov_b64 exec, -1\n\t"
      "s_waitcnt lgkmcnt(9)\n\t"
      "v_mfma_f32_16x16x32_bf16 v[72:75], a[0:3], v[0:3], 0\n\t"
      "ds_read_b128 v[40:43], v98 offset:26624\n\t"
      "v_mfma_f32_16x16x32_bf16 v[76:79], a[4:7], v[0:3], 0\n\t"
      "s_waitcnt lgkmcnt(9)\n\t"
      "v_mfma_f32_16x16x32_bf16 v[72:75], a[8:11], v[4:7], v[72:75]\n\t"
      "ds_read_b128 v[44:47], v98 offset:27648\n\t"
      "v_mfma_f32_16x16x32_bf16 v[76:79], a[12:15], v[4:7], v[76:79]\n\t"
      "s_add_u32 m0, s76, 0\n\t"
      "s_mov_b64 exec, s[36:37]\n\t"
      "buffer_load_dwordx4 v97, s[60:63], s72 offen offset:2048 sc1 lds\n\t"
      "s_mov_b64 exec, -1\n\t"
      "s_cmp_eq_u32 s71, 1\n\t"
      "s_cbranch_scc0 GKV2%=\n\t"
      "s_waitcnt vmcnt(3)\n\t"
      "s_barrier\n\t"
      "s_mov_b64 exec, s[38:39]\n\t"
      "buffer_store_dword v107, off, s[60:63], s75 sc1\n\t"
      "s_mov_b64 exec, -1\n\t"
      "GKV2%=:\n\t"
      "s_waitcnt lgkmcnt(9)\n\t"
      "v_mfma_f32_16x16x32_bf16 v[72:75], a[16:19], v[8:11], v[72:75]\n\t"
      "ds_read_b128 v[48:51], v98 offset:28672\n\t"
      "v_mfma_f32_16x16x32_bf16 v[76:79], a[20:23], v[8:11], v[76:79]\n\t"
      "s_add_u32 m0, s74, 65536\n\t"
      "s_mov_b32 s78, 0\n\t"
      "s_mov_b64 exec, s[88:89]\n\t"
      "buffer_load_dwordx4 v116, s[64:67], s78 offen lds\n\t"
      "s_mov_b64 exec, -1\n\t"
      "s_waitcnt lgkmcnt(9)\n\t"
      "v_mfma_f32_16x16x32_bf16 v[72:75], a[24:27], v[12:15], v[72:75]\n\t"
      "ds_read_b128 v[52:55], v98 offset:29696\n\t"
      "v_mfma_f32_16x16x32_bf16 v[76:79], a[28:31], v[12:15], v[76:79]\n\t"
      "s_add_u32 m0, s76, 0\n\t"
      "s_mov_b64 exec, s[36:37]\n\t"
      "buffer_load_dwordx4 v97, s[60:63], s72 offen offset:3072 sc1 lds\n\t"
      "s_mov_b64 exec, -1\n\t"
      "s_waitcnt lgkmcnt(9)\n\t"
      "v_mfma_f32_16x16x32_bf16 v[72:75], a[32:35], v[16:19], v[72:75]\n\t"
      "ds_read_b128 v[56:59], v98 offset:30720\n\t"
      "v_mfma_f32_16x16x32_bf16 v[76:79], a[36:39], v[16:19], v[76:79]\n\t"
      "s_add_u32 m0, s74, 69632\n\t"
      "s_mov_b32 s78, 262144\n\t"
      "s_mov_b64 exec, s[88:89]\n\t"
      "buffer_load_dwordx4 v116, s[64:67], s78 offen lds\n\t"
      "s_mov_b64 exec, -1\n\t"
      "s_waitcnt lgkmcnt(9)\n\t"
      "v_mfma_f32_16x16x32_bf16 v[72:75], a[40:43], v[20:23], v[72:75]\n\t"
      "ds_read_b128 v[60:63], v98 offset:31744\n\t"
      "v_mfma_f32_16x16x32_bf16 v[76:79], a[44:47], v[20:23], v[76:79]\n\t"
      "s_add_u32 m0, s76, 4096\n\t"
      "s_add_u32 s72, s72, 4096\n\t"
      "s_mov_b64 exec, s[36:37]\n\t"
      "buffer_load_dwordx4 v97, s[60:63], s72 offen sc1 lds\n\t"
      "s_mov_b64 exec, -1\n\t"
      "v_cvt_pk_bf16_f32 v80, v64, v65\n\t"
      "v_cvt_pk_bf16_f32 v81, v66, v67\n\t"
      "v_cvt_pk_bf16_f32 v82, v68, v69\n\t"
      "v_cvt_pk_bf16_f32 v83, v70, v71\n\t"
      "buffer_store_dwordx4 v[80:83], v97, s[60:63], s73 offen sc1\n\t"
      "s_waitcnt lgkmcnt(9)\n\t"
      "v_mfma_f32_16x16x32_bf16 v[72:75], a[48:51], v[24:27], v[72:75]\n\t"
      "v_mfma_f32_16x16x32_bf16 v[76:79], a[52:55], v[24:27], v[76:79]\n\t"
      "s_add_u32 m0, s74, 73728\n\t"
      "s_mov_b32 s78, 524288\n\t"
      "s_mov_b64 exec, s[88:89]\n\t"
      "buffer_load_dwordx4 v116, s[64:67], s78 offen lds\n\t"
      "s_mov_b64 exec, -1\n\t"
      "s_waitcnt lgkmcnt(8)\n\t"
      "v_mfma_f32_16x16x32_bf16 v[72:75], a[56:59], v[28:31], v[72:75]\n\t"
      "v_mfma_f32_16x16x32_bf16 v[76:79], a[60:63], v[28:31], v[76:79]\n\t"
      "s_add_u32 m0, s76, 4096\n\t"
      "s_mov_b64 exec, s[36:37]\n\t"
      "buffer_load_dwordx4 v97, s[60:63], s72 offen offset:1024 sc1 lds\n\t"
      "s_mov_b64 exec, -1\n\t"
      "s_waitcnt lgkmcnt(7)\n\t"
      "v_mfma_f32_16x16x32_bf16 v[72:75], a[64:67], v[32:35], v[72:75]\n\t"
      "v_mfma_f32_16x16x32_bf16 v[76:79], a[68:71], v[32:35], v[76:79]\n\t"
      "s_add_u32 m0, s74, 77824\n\t"
      "s_mov_b32 s78, 786432\n\t"
      "s_mov_b64 exec, s[88:89]\n\t"
      "buffer_load_dwordx4 v116, s[64:67], s78 offen lds\n\t"
      "s_mov_b64 exec, -1\n\t"
      "s_waitcnt lgkmcnt(6)\n\t"
      "v_mfma_f32_16x16x32_bf16 v[72:75], a[72:75], v[36:39], v[72:75]\n\t"
      "v_mfma_f32_16x16x32_bf16 v[76:79], a[76:79], v[36:39], v[76:79]\n\t"
      "s_add_u32 m0, s76, 4096\n\t"
      "s_mov_b64 exec, s[36:37]\n\t"
      "buffer_load_dwordx4 v97, s[60:63], s72 offen offset:2048 sc1 lds\n\t"
      "s_mov_b64 exec, -1\n\t"
      "s_waitcnt lgkmcnt(5)\n\t"
      "v_mfma_f32_16x16x32_bf16 v[72:75], a[80:83], v[40:43], v[72:75]\n\t"
      "v_mfma_f32_16x16x32_bf16 v[76:79], a[84:87], v[40:43], v[76:79]\n\t"
      "s_add_u32 m0, s74, 81920\n\t"
      "s_mov_b32 s78, 1048576\n\t"
      "s_mov_b64 exec, s[88:89]\n\t"
      "buffer_load_dwordx4 v116, s[64:67], s78 offen lds\n\t"
      "s_mov_b64 exec, -1\n\t"
      "s_waitcnt lgkmcnt(4)\n\t"
      "v_mfma_f32_16x16x32_bf16 v[72:75], a[88:91], v[44:47], v[72:75]\n\t"
      "v_mfma_f32_16x16x32_bf16 v[76:79], a[92:95], v[44:47], v[76:79]\n\t"
      "s_add_u32 m0, s76, 4096\n\t"
      "s_mov_b64 exec, s[36:37]\n\t"
      "buffer_load_dwordx4 v97, s[60:63], s72 offen offset:3072 sc1 lds\n\t"
      "s_mov_b64 exec, -1\n\t"
      "s_waitcnt lgkmcnt(3)\n\t"
      "v_mfma_f32_16x16x32_bf16 v[72:75], a[96:99], v[48:51], v[72:75]\n\t"
      "v_mfma_f32_16x16x32_bf16 v[76:79], a[100:103], v[48:51], v[76:79]\n\t"
      "s_add_u32 m0, s74, 86016\n\t"
      "s_mov_b32 s78, 1310720\n\t"
      "s_mov_b64 exec, s[88:89]\n\t"
      "buffer_load_dwordx4 v116, s[64:67], s78 offen lds\n\t"
      "s_mov_b64 exec, -1\n\t"
      "s_waitcnt lgkmcnt(2)\n\t"
      "v_mfma_f32_16x16x32_bf16 v[72:75], a[104:107], v[52:55], v[72:75]\n\t"
      "v_mfma_f32_16x16x32_bf16 v[76:79], a[108:111], v[52:55], v[76:79]\n\t"
      "s_waitcnt lgkmcnt(1)\n\t"
      "v_mfma_f32_16x16x32_bf16 v[72:75], a[112:115], v[56:59], v[72:75]\n\t"
      "v_mfma_f32_16x16x32_bf16 v[76:79], a[116:119], v[56:59], v[76:79]\n\t"
      "s_add_u32 m0, s74, 90112\n\t"
      "s_mov_b32 s78, 1572864\n\t"
      "s_mov_b64 exec, s[88:89]\n\t"
      "buffer_load_dwordx4 v116, s[64:67], s78 offen lds\n\t"
      "s_mov_b64 exec, -1\n\t"
      "s_waitcnt lgkmcnt(0)\n\t"
      "v_mfma_f32_16x16x32_bf16 v[72:75], a[120:123], v[60:63], v[72:75]\n\t"
      "v_mfma_f32_16x16x32_bf16 v[76:79], a[124:127], v[60:63], v[76:79]\n\t"
      "s_nop 6\n\t"
      "v_cvt_pk_bf16_f32 v84, v72, v73\n\t"
      "v_cvt_pk_bf16_f32 v85, v74, v75\n\t"
      "v_cvt_pk_bf16_f32 v86, v76, v77\n\t"
      "v_cvt_pk_bf16_f32 v87, v78, v79\n\t"
      "buffer_store_dwordx4 v[84:87], v97, s[60:63], s73 offen offset:1024 sc1\n\t"
      "s_cmp_eq_u32 s71, 2\n\t"
      "s_cbranch_scc1 GKV3%=\n\t"
      "s_cmp_lg_u32 s71, 0\n\t"
      "s_cbranch_scc1 GKV1%=\n\t"
      "s_cmp_lg_u64 s[36:37], 0\n\t"
      "s_cbranch_scc1 GL2%=\n\t"
      "GL0%=:\n\t"
      "buffer_load_dword v128, v106, s[60:63], s79 offen sc1\n\t"
      "buffer_load_dword v129, v106, s[60:63], s79 offen offset:128 sc1\n\t"
      "s_sub_u32 s81, s81, 1\n\t"
      "s_waitcnt vmcnt(0)\n\t"
      "v_bitop3_b32 v128, v128, v129, %3 bitop3:0x7e\n\t"
      "v_cmp_ne_u32 vcc, 0, v128\n\t"
      "s_cmp_eq_u32 s81, 0\n\t"
      "s_cbranch_scc1 GL1%=\n\t"
      "s_cbranch_vccnz GL0%=\n\t"
      "GL1%=:\n\t"
      "v_readlane_b32 s72, v101, 1\n\t"
      "s_add_u32 s76, s85, 0\n\t"
      "s_add_u32 m0, s76, 0\n\t"
      "s_nop 2\n\t"
      "buffer_load_dwordx4 v97, s[60:63], s72 offen sc1 lds\n\t"
      "buffer_load_dwordx4 v97, s[60:63], s72 offen offset:1024 sc1 lds\n\t"
      "buffer_load_dwordx4 v97, s[60:63], s72 offen offset:2048 sc1 lds\n\t"
      "buffer_load_dwordx4 v97, s[60:63], s72 offen offset:3072 sc1 lds\n\t"
      "s_add_u32 m0, s76, 4096\n\t"
      "s_add_u32 s72, s72, 4096\n\t"
      "buffer_load_dwordx4 v97, s[60:63], s72 offen sc1 lds\n\t"
      "buffer_load_dwordx4 v97, s[60:63], s72 offen offset:1024 sc1 lds\n\t"
      "buffer_load_dwordx4 v97, s[60:63], s72 offen offset:2048 sc1 lds\n\t"
      "buffer_load_dwordx4 v97, s[60:63], s72 offen offset:3072 sc1 lds\n\t"
      "s_waitcnt vmcnt(0)\n\t"
      "GL2%=:\n\t"
      "s_mov_b32 s77, s73\n\t"
      "v_readlane_b32 s73, v102, 1\n\t"
      "v_mov_b32 v98, v97\n\t"
      "s_mov_b64 s[88:89], -1\n\t"
      "s_mov_b64 s[38:39], s[90:91]\n\t"
      "s_mov_b64 s[36:37], -1\n\t"
      "s_mov_b32 s71, 1\n\t"
      "v_readlane_b32 s72, v101, 2\n\t"
      "s_add_u32 s76, s85, 32768\n\t"
      "s_waitcnt vmcnt(1)\n\t"
      "s_barrier\n\t"
      "s_branch GKV%=\n\t"
      "GKV1%=:\n\t"
      "v_readlane_b32 s75, v103, 1\n\t"
      "v_add_u32 v117, 0x10000, v117\n\t"
      "v_add_u32 v98, 131072, v97\n\t"
      "s_mov_b32 s68, 0\n\t"
      "s_add_u32 m0, s74, 94208\n\t"
      "s_mov_b32 s78, 1835008\n\t"
      "buffer_load_dwordx4 v116, s[64:67], s78 offen lds\n\t"
      "s_add_u32 m0, s74, 98304\n\t"
      "s_mov_b32 s78, 2097152\n\t"
      "buffer_load_dwordx4 v116, s[64:67], s78 offen lds\n\t"
      "s_add_u32 m0, s74, 102400\n\t"
      "s_mov_b32 s78, 2359296\n\t"
      "buffer_load_dwordx4 v116, s[64:67], s78 offen lds\n\t"
      "s_add_u32 m0, s74, 106496\n\t"
      "s_mov_b32 s78, 2621440\n\t"
      "buffer_load_dwordx4 v116, s[64:67], s78 offen lds\n\t"
      "s_add_u32 m0, s74, 110592\n\t"
      "s_mov_b32 s78, 2883584\n\t"
      "buffer_load_dwordx4 v116, s[64:67], s78 offen lds\n\t"
      "s_add_u32 m0, s74, 114688\n\t"
      "s_mov_b32 s78, 3145728\n\t"
      "buffer_load_dwordx4 v116, s[64:67], s78 offen lds\n\t"
      "s_add_u32 m0, s74, 118784\n\t"
      "s_mov_b32 s78, 3407872\n\t"
      "buffer_load_dwordx4 v116, s[64:67], s78 offen lds\n\t"
      "s_add_u32 m0, s74, 122880\n\t"
      "s_mov_b32 s78, 3670016\n\t"
      "buffer_load_dwordx4 v116, s[64:67], s78 offen lds\n\t"
      "s_add_u32 m0, s74, 126976\n\t"
      "s_mov_b32 s78, 3932160\n\t"
      "buffer_load_dwordx4 v116, s[64:67], s78 offen lds\n\t"
      "v_add_u32 v98, 0x8000, v97\n\t"
      "v_readlane_b32 s73, v102, 2\n\t"
      "s_mov_b64 s[88:89], 0\n\t"
      "s_mov_b64 s[36:37], 0\n\t"
      "s_mov_b32 s71, 2\n\t"
      "s_waitcnt vmcnt(9)\n\t"
      "s_barrier\n\t"
      "s_mov_b64 exec, s[90:91]\n\t"
      "buffer_store_dword v107, off, s[60:63], s75 sc1\n\t"
      "s_mov_b64 exec, -1\n\t"
      "s_branch GKV%=\n\t"
      "GKV3%=:\n\t"
      "v_readlane_b32 s75, v103, 2\n\t"
      "v_add_u32 v98, 131072, v97\n\t"
      "s_waitcnt vmcnt(0)\n\t"
      "s_barrier\n\t"
      "s_mov_b64 exec, s[90:91]\n\t"
      "s_nop 0\n\t"
      "buffer_store_dword v107, off, s[60:63], s75 sc1\n\t"
      "s_mov_b64 exec, -1\n\t"
      "s_waitcnt lgkmcnt(12)\n\t"
      "ds_read_b64_tr_b16 a[128:129], v117\n\t"
      "ds_read_b64_tr_b16 a[130:131], v117 offset:512\n\t"
      "s_waitcnt lgkmcnt(12)\n\t"
      "ds_read_b64_tr_b16 a[132:133], v117 offset:4096\n\t"
      "ds_read_b64_tr_b16 a[134:135], v117 offset:4608\n\t"
      "s_waitcnt lgkmcnt(12)\n\t"
      "ds_read_b64_tr_b16 a[136:137], v117 offset:8192\n\t"
      "ds_read_b64_tr_b16 a[138:139], v117 offset:8704\n\t"
      "s_waitcnt lgkmcnt(12)\n\t"
      "ds_read_b64_tr_b16 a[140:141], v117 offset:12288\n\t"
      "ds_read_b64_tr_b16 a[142:143], v117 offset:12800\n\t"
      "s_waitcnt lgkmcnt(12)\n\t"
      "ds_read_b64_tr_b16 a[144:145], v117 offset:16384\n\t"
      "ds_read_b64_tr_b16 a[146:147], v117 offset:16896\n\t"
      "s_waitcnt lgkmcnt(12)\n\t"
      "ds_read_b64_tr_b16 a[148:149], v117 offset:20480\n\t"
      "ds_read_b64_tr_b16 a[150:151], v117 offset:20992\n\t"
      "s_waitcnt lgkmcnt(12)\n\t"
      "ds_read_b64_tr_b16 a[152:153], v117 offset:24576\n\t"
      "ds_read_b64_tr_b16 a[154:155], v117 offset:25088\n\t"
      "s_waitcnt lgkmcnt(12)\n\t"
      "ds_read_b64_tr_b16 a[156:157], v117 offset:28672\n\t"
      "ds_read_b64_tr_b16 a[158:159], v117 offset:29184\n\t"
      "s_waitcnt lgkmcnt(12)\n\t"
      "ds_read_b64_tr_b16 a[160:161], v117 offset:32768\n\t"
      "ds_read_b64_tr_b16 a[162:163], v117 offset:33280\n\t"
      "s_waitcnt lgkmcnt(12)\n\t"
      "ds_read_b64_tr_b16 a[164:165], v117 offset:36864\n\t"
      "ds_read_b64_tr_b16 a[166:167], v117 offset:37376\n\t"
      "s_waitcnt lgkmcnt(12)\n\t"
      "ds_read_b64_tr_b16 a[168:169], v117 offset:40960\n\t"
      "ds_read_b64_tr_b16 a[170:171], v117 offset:41472\n\t"
      "s_waitcnt lgkmcnt(12)\n\t"
      "ds_read_b64_tr_b16 a[172:173], v117 offset:45056\n\t"
      "ds_read_b64_tr_b16 a[174:175], v117 offset:45568\n\t"
      "s_waitcnt lgkmcnt(12)\n\t"
      "ds_read_b64_tr_b16 a[176:177], v117 offset:49152\n\t"
      "ds_read_b64_tr_b16 a[178:179], v117 offset:49664\n\t"
      "s_waitcnt lgkmcnt(12)\n\t"
      "ds_read_b64_tr_b16 a[180:181], v117 offset:53248\n\t"
      "ds_read_b64_tr_b16 a[182:183], v117 offset:53760\n\t"
      "s_waitcnt lgkmcnt(12)\n\t"
      "ds_read_b64_tr_b16 a[184:185], v117 offset:57344\n\t"
      "ds_read_b64_tr_b16 a[186:187], v117 offset:57856\n\t"
      "s_waitcnt lgkmcnt(12)\n\t"
      "ds_read_b64_tr_b16 a[188:189], v117 offset:61440\n\t"
      "ds_read_b64_tr_b16 a[190:191], v117 offset:61952\n\t"
      "s_waitcnt lgkmcnt(4)\n\t"
      "GQ%=:\n\t"
      "ds_read_b128 v[0:3], v98\n\t"
      "ds_read_b128 v[4:7], v98 offset:1024\n\t"
      "ds_read_b128 v[8:11], v98 offset:2048\n\t"
      "ds_read_b128 v[12:15], v98 offset:3072\n\t"
      "ds_read_b128 v[16:19], v98 offset:4096\n\t"
      "ds_read_b128 v[20:23], v98 offset:5120\n\t"
      "ds_read_b128 v[24:27], v98 offset:6144\n\t"
      "ds_read_b128 v[28:31], v98 offset:7168\n\t"
      "ds_read_b128 v[32:35], v98 offset:8192\n\t"
      "ds_read_b128 v[36:39], v98 offset:9216\n\t"
      "s_waitcnt lgkmcnt(9)\n\t"
      "v_mfma_f32_16x16x32_bf16 v[88:91], a[128:131], v[0:3], 0\n\t"
      "ds_read_b128 v[40:43], v98 offset:10240\n\t"
      "s_waitcnt lgkmcnt(9)\n\t"
      "v_mfma_f32_16x16x32_bf16 v[88:91], a[132:135], v[4:7], v[88:91]\n\t"
      "ds_read_b128 v[44:47], v98 offset:11264\n\t"
      "s_waitcnt lgkmcnt(9)\n\t"
      "v_mfma_f32_16x16x32_bf16 v[88:91], a[136:139], v[8:11], v[88:91]\n\t"
      "ds_read_b128 v[48:51], v98 offset:12288\n\t"
      "s_waitcnt lgkmcnt(9)\n\t"
      "v_mfma_f32_16x16x32_bf16 v[88:91], a[140:143], v[12:15], v[88:91]\n\t"
      "ds_read_b128 v[52:55], v98 offset:13312\n\t"
      "s_waitcnt lgkmcnt(9)\n\t"
      "v_mfma_f32_16x16x32_bf16 v[88:91], a[144:147], v[16:19], v[88:91]\n\t"
      "ds_read_b128 v[56:59], v98 offset:14336\n\t"
      "s_waitcnt lgkmcnt(9)\n\t"
      "v_mfma_f32_16x16x32_bf16 v[88:91], a[148:151], v[20:23], v[88:91]\n\t"
      "ds_read_b128 v[60:63], v98 offset:15360\n\t"
      "s_waitcnt lgkmcnt(9)\n\t"
      "v_mfma_f32_16x16x32_bf16 v[88:91], a[152:155], v[24:27], v[88:91]\n\t"
      "ds_read_b128 v[0:3], v98 offset:16384\n\t"
      "s_waitcnt lgkmcnt(9)\n\t"
      "v_mfma_f32_16x16x32_bf16 v[88:91], a[156:159], v[28:31], v[88:91]\n\t"
      "ds_read_b128 v[4:7], v98 offset:17408\n\t"
      "s_waitcnt lgkmcnt(9)\n\t"
      "v_mfma_f32_16x16x32_bf16 v[88:91], a[160:163], v[32:35], v[88:91]\n\t"
      "ds_read_b128 v[8:11], v98 offset:18432\n\t"
      "s_waitcnt lgkmcnt(9)\n\t"
      "v_mfma_f32_16x16x32_bf16 v[88:91], a[164:167], v[36:39], v[88:91]\n\t"
      "ds_read_b128 v[12:15], v98 offset:19456\n\t"
      "s_waitcnt lgkmcnt(9)\n\t"
      "v_mfma_f32_16x16x32_bf16 v[88:91], a[168:171], v[40:43], v[88:91]\n\t"
      "ds_read_b128 v[16:19], v98 offset:20480\n\t"
      "s_waitcnt lgkmcnt(9)\n\t"
      "v_mfma_f32_16x16x32_bf16 v[88:91], a[172:175], v[44:47], v[88:91]\n\t"
      "ds_read_b128 v[20:23], v98 offset:21504\n\t"
      "s_waitcnt lgkmcnt(9)\n\t"
      "v_mfma_f32_16x16x32_bf16 v[88:91], a[176:179], v[48:51], v[88:91]\n\t"
      "ds_read_b128 v[24:27], v98 offset:22528\n\t"
      "s_waitcnt lgkmcnt(9)\n\t"
      "v_mfma_f32_16x16x32_bf16 v[88:91], a[180:183], v[52:55], v[88:91]\n\t"
      "ds_read_b128 v[28:31], v98 offset:23552\n\t"
      "s_waitcnt lgkmcnt(9)\n\t"
      "v_mfma_f32_16x16x32_bf16 v[88:91], a[184:187], v[56:59], v[88:91]\n\t"
      "ds_read_b128 v[32:35], v98 offset:24576\n\t"
      "s_waitcnt lgkmcnt(9)\n\t"
      "v_mfma_f32_16x16x32_bf16 v[88:91], a[188:191], v[60:63], v[88:91]\n\t"
      "ds_read_b128 v[36:39], v98 offset:25600\n\t"
      "s_waitcnt lgkmcnt(9)\n\t"
      "v_mfma_f32_16x16x32_bf16 v[92:95], a[128:131], v[0:3], 0\n\t"
      "ds_read_b128 v[40:43], v98 offset:26624\n\t"
      "s_waitcnt lgkmcnt(9)\n\t"
      "v_mfma_f32_16x16x32_bf16 v[92:95], a[132:135], v[4:7], v[92:95]\n\t"
      "ds_read_b128 v[44:47], v98 offset:27648\n\t"
      "s_waitcnt lgkmcnt(9)\n\t"
      "v_mfma_f32_16x16x32_bf16 v[92:95], a[136:139], v[8:11], v[92:95]\n\t"
      "ds_read_b128 v[48:51], v98 offset:28672\n\t"
      "s_waitcnt lgkmcnt(9)\n\t"
      "v_mfma_f32_16x16x32_bf16 v[92:95], a[140:143], v[12:15], v[92:95]\n\t"
      "ds_read_b128 v[52:55], v98 offset:29696\n\t"
      "s_waitcnt lgkmcnt(9)\n\t"
      "v_mfma_f32_16x16x32_bf16 v[92:95], a[144:147], v[16:19], v[92:95]\n\t"
      "ds_read_b128 v[56:59], v98 offset:30720\n\t"
      "s_waitcnt lgkmcnt(9)\n\t"
      "v_mfma_f32_16x16x32_bf16 v[92:95], a[148:151], v[20:23], v[92:95]\n\t"
      "ds_read_b128 v[60:63], v98 offset:31744\n\t"
      "s_waitcnt lgkmcnt(9)\n\t"
      "v_mfma_f32_16x16x32_bf16 v[92:95], a[152:155], v[24:27], v[92:95]\n\t"
      "s_waitcnt lgkmcnt(8)\n\t"
      "v_mfma_f32_16x16x32_bf16 v[92:95], a[156:159], v[28:31], v[92:95]\n\t"
      "s_waitcnt lgkmcnt(7)\n\t"
      "v_mfma_f32_16x16x32_bf16 v[92:95], a[160:163], v[32:35], v[92:95]\n\t"
      "s_waitcnt lgkmcnt(6)\n\t"
      "v_mfma_f32_16x16x32_bf16 v[92:95], a[164:167], v[36:39], v[92:95]\n\t"
      "s_waitcnt lgkmcnt(5)\n\t"
      "v_mfma_f32_16x16x32_bf16 v[92:95], a[168:171], v[40:43], v[92:95]\n\t"
      "s_waitcnt lgkmcnt(4)\n\t"
      "v_mfma_f32_16x16x32_bf16 v[92:95], a[172:175], v[44:47], v[92:95]\n\t"
      "s_waitcnt lgkmcnt(3)\n\t"
      "v_mfma_f32_16x16x32_bf16 v[92:95], a[176:179], v[48:51], v[92:95]\n\t"
      "s_waitcnt lgkmcnt(2)\n\t"
      "v_mfma_f32_16x16x32_bf16 v[92:95], a[180:183], v[52:55], v[92:95]\n\t"
      "s_waitcnt lgkmcnt(1)\n\t"
      "v_mfma_f32_16x16x32_bf16 v[92:95], a[184:187], v[56:59], v[92:95]\n\t"
      "s_waitcnt lgkmcnt(0)\n\t"
      "v_mfma_f32_16x16x32_bf16 v[92:95], a[188:191], v[60:63], v[92:95]\n\t"
      "v_cvt_pk_bf16_f32 v112, v88, v89\n\t"
      "v_cvt_pk_bf16_f32 v113, v90, v91\n\t"
      "s_cmp_eq_u32 s68, 3\n\t"
      "s_waitcnt vmcnt(17)\n\t"
      "s_barrier\n\t"
      "s_cbranch_scc1 GQ1%=\n\t"
      "s_nop 3\n\t"
      "v_cvt_pk_bf16_f32 v114, v92, v93\n\t"
      "v_cvt_pk_bf16_f32 v115, v94, v95\n\t"
      "s_nop 0\n\t"
      "v_permlane16_swap_b32 v112, v114\n\t"
      "v_permlane16_swap_b32 v113, v115\n\t"
      "buffer_store_dwordx4 v[112:115], v111, s[60:63], s77 offen sc1\n\t"
      "s_cmp_lg_u32 s68, 0\n\t"
      "s_cbranch_scc1 GQ2%=\n\t"
      "v_readlane_b32 s72, v101, 3\n\t"
      "s_add_u32 s76, s85, 65536\n\t"
      "s_add_u32 m0, s76, 0\n\t"
      "s_nop 2\n\t"
      "buffer_load_dwordx4 v97, s[60:63], s72 offen sc1 lds\n\t"
      "buffer_load_dwordx4 v97, s[60:63], s72 offen offset:1024 sc1 lds\n\t"
      "buffer_load_dwordx4 v97, s[60:63], s72 offen offset:2048 sc1 lds\n\t"
      "buffer_load_dwordx4 v97, s[60:63], s72 offen offset:3072 sc1 lds\n\t"
      "s_add_u32 m0, s76, 4096\n\t"
      "s_add_u32 s72, s72, 4096\n\t"
      "buffer_load_dwordx4 v97, s[60:63], s72 offen sc1 lds\n\t"
      "buffer_load_dwordx4 v97, s[60:63], s72 offen offset:1024 sc1 lds\n\t"
      "buffer_load_dwordx4 v97, s[60:63], s72 offen offset:2048 sc1 lds\n\t"
      "buffer_load_dwordx4 v97, s[60:63], s72 offen offset:3072 sc1 lds\n\t"
      "v_readlane_b32 s72, v101, 4\n\t"
      "s_add_u32 s76, s85, 98304\n\t"
      "s_add_u32 m0, s76, 0\n\t"
      "s_nop 2\n\t"
      "buffer_load_dwordx4 v97, s[60:63], s72 offen sc1 lds\n\t"
      "buffer_load_dwordx4 v97, s[60:63], s72 offen offset:1024 sc1 lds\n\t"
      "buffer_load_dwordx4 v97, s[60:63], s72 offen offset:2048 sc1 lds\n\t"
      "buffer_load_dwordx4 v97, s[60:63], s72 offen offset:3072 sc1 lds\n\t"
      "s_add_u32 m0, s76, 4096\n\t"
      "s_add_u32 s72, s72, 4096\n\t"
      "buffer_load_dwordx4 v97, s[60:63], s72 offen sc1 lds\n\t"
      "buffer_load_dwordx4 v97, s[60:63], s72 offen offset:1024 sc1 lds\n\t"
      "buffer_load_dwordx4 v97, s[60:63], s72 offen offset:2048 sc1 lds\n\t"
      "buffer_load_dwordx4 v97, s[60:63], s72 offen offset:3072 sc1 lds\n\t"
      "v_mov_b32 v98, v97\n\t"
      "v_readlane_b32 s77, v102, 1\n\t"
      "s_mov_b32 s68, 1\n\t"
      "s_branch GQ%=\n\t"
      "GQ2%=:\n\t"
      "v_readlane_b32 s72, v101, 5\n\t"
      "s_add_u32 s76, s85, 0\n\t"
      "s_add_u32 m0, s76, 0\n\t"
      "s_nop 2\n\t"
      "buffer_load_dwordx4 v97, s[60:63], s72 offen sc1 lds\n\t"
      "buffer_load_dwordx4 v97, s[60:63], s72 offen offset:1024 sc1 lds\n\t"
      "buffer_load_dwordx4 v97, s[60:63], s72 offen offset:2048 sc1 lds\n\t"
      "buffer_load_dwordx4 v97, s[60:63], s72 offen offset:3072 sc1 lds\n\t"
      "s_add_u32 m0, s76, 4096\n\t"
      "s_add_u32 s72, s72, 4096\n\t"
      "buffer_load_dwordx4 v97, s[60:63], s72 offen sc1 lds\n\t"
      "buffer_load_dwordx4 v97, s[60:63], s72 offen offset:1024 sc1 lds\n\t"
      "buffer_load_dwordx4 v97, s[60:63], s72 offen offset:2048 sc1 lds\n\t"
      "buffer_load_dwordx4 v97, s[60:63], s72 offen offset:3072 sc1 lds\n\t"
      "v_add_u32 v98, 0x8000, v97\n\t"
      "s_mov_b32 s68, 3\n\t"
      "s_branch GQ%=\n\t"
      "GQ1%=:\n\t"
      "s_mov_b32 s69, 6\n\t"
      "s_mov_b32 s70, 63\n\t"
      "s_waitcnt vmcnt(9)\n\t"
      "s_mov_b32 s77, 0x10000\n\t"
      "s_mov_b32 s71, 5\n\t"
      "s_getpc_b64 s[88:89]\n\t"
      "GPC%=:\n\t"
      "s_add_u32 s88, s88, GJOBS%=-GPC%=\n\t"
      "s_addc_u32 s89, s89, 0\n\t"
      "v_add_u32 v98, s77, v97\n\t"
      "s_add_u32 s76, s85, 0x8000\n\t"
      "ds_read_b128 v[0:3], v98\n\t"
      "ds_read_b128 v[4:7], v98 offset:1024\n\t"
      "ds_read_b128 v[8:11], v98 offset:2048\n\t"
      "ds_read_b128 v[12:15], v98 offset:3072\n\t"
      "ds_read_b128 v[16:19], v98 offset:4096\n\t"
      "ds_read_b128 v[20:23], v98 offset:5120\n\t"
      "ds_read_b128 v[24:27], v98 offset:6144\n\t"
      "ds_read_b128 v[28:31], v98 offset:7168\n\t"
      "ds_read_b128 v[32:35], v98 offset:8192\n\t"
      "ds_read_b128 v[36:39], v98 offset:9216\n\t"
      ".p2align 6\n\t"
      "GU%=:\n\t"
      "s_cmp_eq_u32 s68, s71\n\t"
      "s_cbranch_scc1 GCALL%=\n\t"
      "GBACK%=:\n\t"
      "s_waitcnt lgkmcnt(9)\n\t"
      "v_mfma_f32_16x16x32_bf16 v[64:67], a[0:3], v[0:3], 0\n\t"
      "ds_read_b128 v[40:43], v98 offset:10240\n\t"
      "v_mfma_f32_16x16x32_bf16 v[68:71], a[4:7], v[0:3], 0\n\t"
      "v_readlane_b32 s72, v101, s69\n\t"
      "v_mfma_f32_16x16x32_bf16 v[88:91], a[128:131], v[0:3], 0\n\t"
      "s_waitcnt lgkmcnt(9)\n\t"
      "v_mfma_f32_16x16x32_bf16 v[64:67], a[8:11], v[4:7], v[64:67]\n\t"
      "ds_read_b128 v[44:47], v98 offset:11264\n\t"
      "v_mfma_f32_16x16x32_bf16 v[68:71], a[12:15], v[4:7], v[68:71]\n\t"
      "v_readlane_b32 s75, v103, s70\n\t"
      "v_mfma_f32_16x16x32_bf16 v[88:91], a[132:135], v[4:7], v[88:91]\n\t"
      "s_waitcnt lgkmcnt(9)\n\t"
      "v_mfma_f32_16x16x32_bf16 v[64:67], a[16:19], v[8:11], v[64:67]\n\t"
      "ds_read_b128 v[48:51], v98 offset:12288\n\t"
      "v_mfma_f32_16x16x32_bf16 v[68:71], a[20:23], v[8:11], v[68:71]\n\t"
      "v_cvt_pk_bf16_f32 v84, v72, v73\n\t"
      "v_cvt_pk_bf16_f32 v85, v74, v75\n\t"
      "v_mfma_f32_16x16x32_bf16 v[88:91], a[136:139], v[8:11], v[88:91]\n\t"
      "v_cvt_pk_bf16_f32 v114, v92, v93\n\t"
      "v_cvt_pk_bf16_f32 v115, v94, v95\n\t"
      "s_waitcnt lgkmcnt(9)\n\t"
      "v_mfma_f32_16x16x32_bf16 v[64:67], a[24:27], v[12:15], v[64:67]\n\t"
      "ds_read_b128 v[52:55], v98 offset:13312\n\t"
      "v_mfma_f32_16x16x32_bf16 v[68:71], a[28:31], v[12:15], v[68:71]\n\t"
      "v_cvt_pk_bf16_f32 v86, v76, v77\n\t"
      "v_cvt_pk_bf16_f32 v87, v78, v79\n\t"
      "v_mfma_f32_16x16x32_bf16 v[88:91], a[140:143], v[12:15], v[88:91]\n\t"
      "v_permlane16_swap_b32 v112, v114\n\t"
      "v_permlane16_swap_b32 v113, v115\n\t"
      "s_waitcnt lgkmcnt(9)\n\t"
      "v_mfma_f32_16x16x32_bf16 v[64:67], a[32:35], v[16:19], v[64:67]\n\t"
      "ds_read_b128 v[56:59], v98 offset:14336\n\t"
      "v_mfma_f32_16x16x32_bf16 v[68:71], a[36:39], v[16:19], v[68:71]\n\t"
      "buffer_store_dwordx4 v[84:87], v97, s[60:63], s73 offen offset:1024 sc1\n\t"
      "buffer_store_dwordx4 v[112:115], v111, s[60:63], s73 offen sc1\n\t"
      "v_mfma_f32_16x16x32_bf16 v[88:91], a[144:147], v[16:19], v[88:91]\n\t"
      "s_add_u32 m0, s76, 0\n\t"
      "buffer_load_dwordx4 v97, s[60:63], s72 offen sc1 lds\n\t"
      "s_waitcnt lgkmcnt(9)\n\t"
      "v_mfma_f32_16x16x32_bf16 v[64:67], a[40:43], v[20:23], v[64:67]\n\t"
      "ds_read_b128 v[60:63], v98 offset:15360\n\t"
      "v_mfma_f32_16x16x32_bf16 v[68:71], a[44:47], v[20:23], v[68:71]\n\t"
      "v_mfma_f32_16x16x32_bf16 v[88:91], a[148:151], v[20:23], v[88:91]\n\t"
      "s_waitcnt lgkmcnt(9)\n\t"
      "v_mfma_f32_16x16x32_bf16 v[64:67], a[48:51], v[24:27], v[64:67]\n\t"
      "ds_read_b128 v[0:3], v98 offset:16384\n\t"
      "v_mfma_f32_16x16x32_bf16 v[68:71], a[52:55], v[24:27], v[68:71]\n\t"
      "v_mfma_f32_16x16x32_bf16 v[88:91], a[152:155], v[24:27], v[88:91]\n\t"
      "s_waitcnt lgkmcnt(9)\n\t"
      "v_mfma_f32_16x16x32_bf16 v[64:67], a[56:59], v[28:31], v[64:67]\n\t"
      "ds_read_b128 v[4:7], v98 offset:17408\n\t"
      "v_mfma_f32_16x16x32_bf16 v[68:71], a[60:63], v[28:31], v[68:71]\n\t"
      "buffer_load_dwordx4 v97, s[60:63], s72 offen offset:1024 sc1 lds\n\t"
      "v_mfma_f32_16x16x32_bf16 v[88:91], a[156:159], v[28:31], v[88:91]\n\t"
      "s_waitcnt lgkmcnt(9)\n\t"
      "v_mfma_f32_16x16x32_bf16 v[64:67], a[64:67], v[32:35], v[64:67]\n\t"
      "ds_read_b128 v[8:11], v98 offset:18432\n\t"
      "v_mfma_f32_16x16x32_bf16 v[68:71], a[68:71], v[32:35], v[68:71]\n\t"
      "v_mfma_f32_16x16x32_bf16 v[88:91], a[160:163], v[32:35], v[88:91]\n\t"
      "s_waitcnt lgkmcnt(9)\n\t"
      "v_mfma_f32_16x16x32_bf16 v[64:67], a[72:75], v[36:39], v[64:67]\n\t"
      "ds_read_b128 v[12:15], v98 offset:19456\n\t"
      "v_mfma_f32_16x16x32_bf16 v[68:71], a[76:79], v[36:39], v[68:71]\n\t"
      "v_readlane_b32 s73, v102, s68\n\t"
      "v_mfma_f32_16x16x32_bf16 v[88:91], a[164:167], v[36:39], v[88:91]\n\t"
      "s_waitcnt lgkmcnt(9)\n\t"
      "v_mfma_f32_16x16x32_bf16 v[64:67], a[80:83], v[40:43], v[64:67]\n\t"
      "ds_read_b128 v[16:19], v98 offset:20480\n\t"
      "v_mfma_f32_16x16x32_bf16 v[68:71], a[84:87], v[40:43], v[68:71]\n\t"
      "buffer_load_dwordx4 v97, s[60:63], s72 offen offset:2048 sc1 lds\n\t"
      "v_mfma_f32_16x16x32_bf16 v[88:91], a[168:171], v[40:43], v[88:91]\n\t"
      "s_waitcnt lgkmcnt(9)\n\t"
      "v_mfma_f32_16x16x32_bf16 v[64:67], a[88:91], v[44:47], v[64:67]\n\t"
      "ds_read_b128 v[20:23], v98 offset:21504\n\t"
      "v_mfma_f32_16x16x32_bf16 v[68:71], a[92:95], v[44:47], v[68:71]\n\t"
      "v_mfma_f32_16x16x32_bf16 v[88:91], a[172:175], v[44:47], v[88:91]\n\t"
      "s_waitcnt lgkmcnt(9)\n\t"
      "v_mfma_f32_16x16x32_bf16 v[64:67], a[96:99], v[48:51], v[64:67]\n\t"
      "ds_read_b128 v[24:27], v98 offset:22528\n\t"
      "v_mfma_f32_16x16x32_bf16 v[68:71], a[100:103], v[48:51], v[68:71]\n\t"
      "v_mfma_f32_16x16x32_bf16 v[88:91], a[176:179], v[48:51], v[88:91]\n\t"
      "s_waitcnt lgkmcnt(9)\n\t"
      "v_mfma_f32_16x16x32_bf16 v[64:67], a[104:107], v[52:55], v[64:67]\n\t"
      "ds_read_b128 v[28:31], v98 offset:23552\n\t"
      "v_mfma_f32_16x16x32_bf16 v[68:71], a[108:111], v[52:55], v[68:71]\n\t"
      "buffer_load_dwordx4 v97, s[60:63], s72 offen offset:3072 sc1 lds\n\t"
      "v_mfma_f32_16x16x32_bf16 v[88:91], a[180:183], v[52:55], v[88:91]\n\t"
      "s_waitcnt lgkmcnt(9)\n\t"
      "v_mfma_f32_16x16x32_bf16 v[64:67], a[112:115], v[56:59], v[64:67]\n\t"
      "ds_read_b128 v[32:35], v98 offset:24576\n\t"
      "v_mfma_f32_16x16x32_bf16 v[68:71], a[116:119], v[56:59], v[68:71]\n\t"
      "v_mfma_f32_16x16x32_bf16 v[88:91], a[184:187], v[56:59], v[88:91]\n\t"
      "s_waitcnt lgkmcnt(9)\n\t"
      "v_mfma_f32_16x16x32_bf16 v[64:67], a[120:123], v[60:63], v[64:67]\n\t"
      "ds_read_b128 v[36:39], v98 offset:25600\n\t"
      "v_mfma_f32_16x16x32_bf16 v[68:71], a[124:127], v[60:63], v[68:71]\n\t"
      "s_add_u32 s68, s68, 1\n\t"
      "v_mfma_f32_16x16x32_bf16 v[88:91], a[188:191], v[60:63], v[88:91]\n\t"
      "s_waitcnt lgkmcnt(9)\n\t"
      "v_mfma_f32_16x16x32_bf16 v[72:75], a[0:3], v[0:3], 0\n\t"
      "ds_read_b128 v[40:43], v98 offset:26624\n\t"
      "v_mfma_f32_16x16x32_bf16 v[76:79], a[4:7], v[0:3], 0\n\t"
      "s_add_u32 m0, s76, 4096\n\t"
      "s_add_u32 s72, s72, 4096\n\t"
      "buffer_load_dwordx4 v97, s[60:63], s72 offen sc1 lds\n\t"
      "v_mfma_f32_16x16x32_bf16 v[92:95], a[128:131], v[0:3], 0\n\t"
      "s_add_u32 s69, s69, 1\n\t"
      "s_add_u32 s70, s70, 1\n\t"
      "s_waitcnt lgkmcnt(9)\n\t"
      "v_mfma_f32_16x16x32_bf16 v[72:75], a[8:11], v[4:7], v[72:75]\n\t"
      "ds_read_b128 v[44:47], v98 offset:27648\n\t"
      "v_mfma_f32_16x16x32_bf16 v[76:79], a[12:15], v[4:7], v[76:79]\n\t"
      "s_cmp_lt_u32 s68, 7\n\t"
      "s_cselect_b32 s70, 1, 3\n\t"
      "v_mfma_f32_16x16x32_bf16 v[92:95], a[132:135], v[4:7], v[92:95]\n\t"
      "s_sub_u32 s70, s68, s70\n\t"
      "s_add_u32 s76, s77, s85\n\t"
      "s_add_u32 s77, s77, 0x8000\n\t"
      "s_and_b32 s77, s77, 0x1ffff\n\t"
      "s_waitcnt lgkmcnt(9)\n\t"
      "v_mfma_f32_16x16x32_bf16 v[72:75], a[16:19], v[8:11], v[72:75]\n\t"
      "ds_read_b128 v[48:51], v98 offset:28672\n\t"
      "v_mfma_f32_16x16x32_bf16 v[76:79], a[20:23], v[8:11], v[76:79]\n\t"
      "v_cvt_pk_bf16_f32 v80, v64, v65\n\t"
      "v_cvt_pk_bf16_f32 v81, v66, v67\n\t"
      "v_mfma_f32_16x16x32_bf16 v[92:95], a[136:139], v[8:11], v[92:95]\n\t"
      "v_cvt_pk_bf16_f32 v112, v88, v89\n\t"
      "s_waitcnt lgkmcnt(9)\n\t"
      "v_mfma_f32_16x16x32_bf16 v[72:75], a[24:27], v[12:15], v[72:75]\n\t"
      "ds_read_b128 v[52:55], v98 offset:29696\n\t"
      "v_mfma_f32_16x16x32_bf16 v[76:79], a[28:31], v[12:15], v[76:79]\n\t"
      "buffer_load_dwordx4 v97, s[60:63], s72 offen offset:1024 sc1 lds\n\t"
      "v_cvt_pk_bf16_f32 v82, v68, v69\n\t"
      "v_mfma_f32_16x16x32_bf16 v[92:95], a[140:143], v[12:15], v[92:95]\n\t"
      "v_cvt_pk_bf16_f32 v83, v70, v71\n\t"
      "v_cvt_pk_bf16_f32 v113, v90, v91\n\t"
      "s_waitcnt lgkmcnt(9)\n\t"
      "v_mfma_f32_16x16x32_bf16 v[72:75], a[32:35], v[16:19], v[72:75]\n\t"
      "ds_read_b128 v[56:59], v98 offset:30720\n\t"
      "v_mfma_f32_16x16x32_bf16 v[76:79], a[36:39], v[16:19], v[76:79]\n\t"
      "buffer_store_dwordx4 v[80:83], v97, s[60:63], s73 offen sc1\n\t"
      "v_mfma_f32_16x16x32_bf16 v[92:95], a[144:147], v[16:19], v[92:95]\n\t"
      "s_waitcnt lgkmcnt(9)\n\t"
      "v_mfma_f32_16x16x32_bf16 v[72:75], a[40:43], v[20:23], v[72:75]\n\t"
      "ds_read_b128 v[60:63], v98 offset:31744\n\t"
      "v_mfma_f32_16x16x32_bf16 v[76:79], a[44:47], v[20:23], v[76:79]\n\t"
      "v_mfma_f32_16x16x32_bf16 v[92:95], a[148:151], v[20:23], v[92:95]\n\t"
      "s_waitcnt lgkmcnt(9)\n\t"
      "v_mfma_f32_16x16x32_bf16 v[72:75], a[48:51], v[24:27], v[72:75]\n\t"
      "v_mfma_f32_16x16x32_bf16 v[76:79], a[52:55], v[24:27], v[76:79]\n\t"
      "buffer_load_dwordx4 v97, s[60:63], s72 offen offset:2048 sc1 lds\n\t"
      "v_add_u32 v98, s77, v97\n\t"
      "v_mfma_f32_16x16x32_bf16 v[92:95], a[152:155], v[24:27], v[92:95]\n\t"
      "s_cmp_lg_u32 s75, 0\n\t"
      "s_waitcnt lgkmcnt(8)\n\t"
      "v_mfma_f32_16x16x32_bf16 v[72:75], a[56:59], v[28:31], v[72:75]\n\t"
      "v_mfma_f32_16x16x32_bf16 v[76:79], a[60:63], v[28:31], v[76:79]\n\t"
      "s_cselect_b64 vcc, s[90:91], 0\n\t"
      "v_mfma_f32_16x16x32_bf16 v[92:95], a[156:159], v[28:31], v[92:95]\n\t"
      "s_waitcnt lgkmcnt(7)\n\t"
      "v_mfma_f32_16x16x32_bf16 v[72:75], a[64:67], v[32:35], v[72:75]\n\t"
      "v_mfma_f32_16x16x32_bf16 v[76:79], a[68:71], v[32:35], v[76:79]\n\t"
      "buffer_load_dwordx4 v97, s[60:63], s72 offen offset:3072 sc1 lds\n\t"
      "v_mfma_f32_16x16x32_bf16 v[92:95], a[160:163], v[32:35], v[92:95]\n\t"
      "s_cmp_gt_u32 s68, 7\n\t"
      "s_waitcnt lgkmcnt(6)\n\t"
      "v_mfma_f32_16x16x32_bf16 v[72:75], a[72:75], v[36:39], v[72:75]\n\t"
      "v_mfma_f32_16x16x32_bf16 v[76:79], a[76:79], v[36:39], v[76:79]\n\t"
      "v_mfma_f32_16x16x32_bf16 v[92:95], a[164:167], v[36:39], v[92:95]\n\t"
      "s_waitcnt vmcnt(22) lgkmcnt(0)\n\t"
      "s_cbranch_scc1 GEF%=\n\t"
      "s_waitcnt vmcnt(9)\n\t"
      "GEF%=:\n\t"
      "s_barrier\n\t"
      "v_mfma_f32_16x16x32_bf16 v[72:75], a[80:83], v[40:43], v[72:75]\n\t"
      "ds_read_b128 v[0:3], v98\n\t"
      "v_mfma_f32_16x16x32_bf16 v[76:79], a[84:87], v[40:43], v[76:79]\n\t"
      "ds_read_b128 v[4:7], v98 offset:1024\n\t"
      "v_mfma_f32_16x16x32_bf16 v[92:95], a[168:171], v[40:43], v[92:95]\n\t"
      "v_mfma_f32_16x16x32_bf16 v[72:75], a[88:91], v[44:47], v[72:75]\n\t"
      "ds_read_b128 v[8:11], v98 offset:2048\n\t"
      "v_mfma_f32_16x16x32_bf16 v[76:79], a[92:95], v[44:47], v[76:79]\n\t"
      "ds_read_b128 v[12:15], v98 offset:3072\n\t"
      "v_mfma_f32_16x16x32_bf16 v[92:95], a[172:175], v[44:47], v[92:95]\n\t"
      "v_mfma_f32_16x16x32_bf16 v[72:75], a[96:99], v[48:51], v[72:75]\n\t"
      "ds_read_b128 v[16:19], v98 offset:4096\n\t"
      "v_mfma_f32_16x16x32_bf16 v[76:79], a[100:103], v[48:51], v[76:79]\n\t"
      "ds_read_b128 v[20:23], v98 offset:5120\n\t"
      "v_mfma_f32_16x16x32_bf16 v[92:95], a[176:179], v[48:51], v[92:95]\n\t"
      "v_mfma_f32_16x16x32_bf16 v[72:75], a[104:107], v[52:55], v[72:75]\n\t"
      "ds_read_b128 v[24:27], v98 offset:6144\n\t"
      "v_mfma_f32_16x16x32_bf16 v[76:79], a[108:111], v[52:55], v[76:79]\n\t"
      "ds_read_b128 v[28:31], v98 offset:7168\n\t"
      "v_mfma_f32_16x16x32_bf16 v[92:95], a[180:183], v[52:55], v[92:95]\n\t"
      "v_mfma_f32_16x16x32_bf16 v[72:75], a[112:115], v[56:59], v[72:75]\n\t"
      "ds_read_b128 v[32:35], v98 offset:8192\n\t"
      "v_mfma_f32_16x16x32_bf16 v[76:79], a[116:119], v[56:59], v[76:79]\n\t"
      "ds_read_b128 v[36:39], v98 offset:9216\n\t"
      "v_mfma_f32_16x16x32_bf16 v[92:95], a[184:187], v[56:59], v[92:95]\n\t"
      "s_cbranch_vccnz GFLAG%=\n\t"
      "GFB%=:\n\t"
      "v_mfma_f32_16x16x32_bf16 v[72:75], a[120:123], v[60:63], v[72:75]\n\t"
      "v_mfma_f32_16x16x32_bf16 v[76:79], a[124:127], v[60:63], v[76:79]\n\t"
      "v_mfma_f32_16x16x32_bf16 v[92:95], a[188:191], v[60:63], v[92:95]\n\t"
      "s_branch GU%=\n\t"
      "GFLAG%=:\n\t"
      "s_mov_b64 exec, 1\n\t"
      "buffer_store_dword v107, off, s[60:63], s75 sc1\n\t"
      "s_mov_b64 exec, -1\n\t"
      "s_branch GFB%=\n\t"
      "GCALL%=:\n\t"
      "s_setpc_b64 s[88:89]\n\t"
      "GJOBS%=:\n\t"
      "s_nop 7\n\t"
      "v_readlane_b32 s75, v103, 3\n\t"
      "s_waitcnt vmcnt(9)\n\t"
      "s_barrier\n\t"
      "s_mov_b64 exec, s[90:91]\n\t"
      "s_nop 1\n\t"
      "buffer_store_dword v107, off, s[60:63], s75 sc1\n\t"
      "s_mov_b64 exec, -1\n\t"
      "s_mov_b32 s71, 6\n\t"
      "s_getpc_b64 s[88:89]\n\t"
      "s_add_u32 s88, s88, 12\n\t"
      "s_addc_u32 s89, s89, 0\n\t"
      "s_branch GBACK%=\n\t"
      "s_bitcmp0_b32 %0, 1\n\t"
      "s_cselect_b64 s[36:37], -1, 0\n\t"
      "s_cmp_lt_u32 s82, 28\n\t"
      "s_cselect_b64 s[38:39], -1, 0\n\t"
      "s_min_u32 s74, s82, 27\n\t"
      "v_lshrrev_b32 v104, 3, v96\n\t"
      "v_and_b32 v105, 7, v96\n\t"
      "s_mov_b32 s78, 3584\n\t"
      "v_mul_u32_u24 v104, s78, v104\n\t"
      "v_lshl_add_u32 v104, v105, 4, v104\n\t"
      "s_lshr_b32 s78, %0, 5\n\t"
      "s_lshl_b32 s78, s78, 2\n\t"
      "s_add_u32 s78, s78, %1\n\t"
      "s_mul_i32 s78, s78, 114688\n\t"
      "s_lshl_b32 s80, s74, 7\n\t"
      "s_add_u32 s80, s80, s78\n\t"
      "s_mov_b64 s[64:65], s[50:51]\n\t"
      "s_add_u32 s78, s80, 0\n\t"
      "buffer_load_dwordx4 v[128:131], v104, s[64:67], s78 offen \n\t"
      "s_add_u32 s78, s80, 28672\n\t"
      "buffer_load_dwordx4 v[132:135], v104, s[64:67], s78 offen \n\t"
      "s_add_u32 s78, s80, 57344\n\t"
      "buffer_load_dwordx4 v[136:139], v104, s[64:67], s78 offen \n\t"
      "s_add_u32 s71, s71, 1\n\t"
      "s_getpc_b64 s[88:89]\n\t"
      "s_add_u32 s88, s88, 12\n\t"
      "s_addc_u32 s89, s89, 0\n\t"
      "s_branch GBACK%=\n\t"
      "s_add_u32 s78, s80, 86016\n\t"
      "buffer_load_dwordx4 v[140:143], v104, s[64:67], s78 offen \n\t"
      "s_mov_b64 s[64:65], s[52:53]\n\t"
      "s_add_u32 s78, s80, 0\n\t"
      "buffer_load_dwordx4 v[144:147], v104, s[64:67], s78 offen \n\t"
      "s_add_u32 s78, s80, 28672\n\t"
      "buffer_load_dwordx4 v[148:151], v104, s[64:67], s78 offen \n\t"
      "s_add_u32 s71, s71, 1\n\t"
      "s_getpc_b64 s[88:89]\n\t"
      "s_add_u32 s88, s88, 12\n\t"
      "s_addc_u32 s89, s89, 0\n\t"
      "s_branch GBACK%=\n\t"
      "s_add_u32 s78, s80, 57344\n\t"
      "buffer_load_dwordx4 v[152:155], v104, s[64:67], s78 offen \n\t"
      "s_add_u32 s78, s80, 86016\n\t"
      "buffer_load_dwordx4 v[156:159], v104, s[64:67], s78 offen \n\t"
      "v_mov_b32 v120, -1\n\t"
      "v_mov_b32 v121, -1\n\t"
      "v_mov_b32 v122, -1\n\t"
      "v_mov_b32 v123, -1\n\t"
      "s_lshr_b32 s78, %0, 5\n\t"
      "s_lshl_b32 s78, s78, 2\n\t"
      "s_add_u32 s78, s78, %1\n\t"
      "s_lshr_b32 s79, s78, 1\n\t"
      "s_lshl_b32 s79, s79, 16\n\t"
      "s_lshl_b32 s80, s82, 11\n\t"
      "s_add_u32 s79, s79, s80\n\t"
      "s_and_b32 s78, s78, 1\n\t"
      "s_lshl_b32 s78, s78, 10\n\t"
      "s_add_u32 s79, s79, s78\n\t"
      "s_add_u32 s79, s79, 66813952\n\t"
      "buffer_store_dwordx4 v[120:123], v97, s[60:63], s79 offen sc1\n\t"
      "s_add_u32 s71, s71, 1\n\t"
      "s_getpc_b64 s[88:89]\n\t"
      "s_add_u32 s88, s88, 12\n\t"
      "s_addc_u32 s89, s89, 0\n\t"
      "s_branch GBACK%=\n\t"
      "s_sub_u32 s79, s79, 4194304\n\t"
      "buffer_store_dwordx4 v[120:123], v97, s[60:63], s79 offen sc1\n\t"
      "s_add_u32 s79, s79, 3670016\n\t"
      "buffer_store_dwordx4 v[120:123], v97, s[60:63], s79 offen sc1\n\t"
      "s_sub_u32 s79, s79, 4194304\n\t"
      "buffer_store_dwordx4 v[120:123], v97, s[60:63], s79 offen sc1\n\t"
      "s_add_u32 s71, s71, 1\n\t"
      "s_getpc_b64 s[88:89]\n\t"
      "s_add_u32 s88, s88, 12\n\t"
      "s_addc_u32 s89, s89, 0\n\t"
      "s_branch GBACK%=\n\t"
      "s_add_u32 s79, s79, 3670016\n\t"
      "buffer_store_dwordx4 v[120:123], v97, s[60:63], s79 offen sc1\n\t"
      "s_sub_u32 s79, s79, 4194304\n\t"
      "buffer_store_dwordx4 v[120:123], v97, s[60:63], s79 offen sc1\n\t"
      "s_mov_b64 s[64:65], s[54:55]\n\t"
      "v_lshrrev_b32 v104, 5, v96\n\t"
      "v_and_b32 v105, 31, v96\n\t"
      "v_lshlrev_b32 v104, 10, v104\n\t"
      "v_lshl_add_u32 v104, v105, 4, v104\n\t"
      "s_andn2_b32 s78, %0, 3\n\t"
      "s_add_u32 s78, s78, %1\n\t"
      "s_mul_i32 s78, s78, 14336\n\t"
      "s_and_b32 s79, %0, 1\n\t"
      "s_lshl_b32 s79, s79, 9\n\t"
      "s_add_u32 s78, s78, s79\n\t"
      "s_mov_b64 exec, s[36:37]\n\t"
      "buffer_load_dwordx4 v[160:163], v104, s[64:67], s78 offen \n\t"
      "s_mov_b64 exec, -1\n\t"
      "s_add_u32 s71, s71, 1\n\t"
      "s_getpc_b64 s[88:89]\n\t"
      "s_add_u32 s88, s88, 12\n\t"
      "s_addc_u32 s89, s89, 0\n\t"
      "s_branch GBACK%=\n\t"
      "s_mov_b64 exec, s[36:37]\n\t"
      "buffer_load_dwordx4 v[164:167], v104, s[64:67], s78 offen offset:2048 \n\t"
      "s_add_u32 s78, s78, 4096\n\t"
      "buffer_load_dwordx4 v[168:171], v104, s[64:67], s78 offen \n\t"
      "buffer_load_dwordx4 v[172:175], v104, s[64:67], s78 offen offset:2048 \n\t"
      "s_mov_b64 exec, -1\n\t"
      "s_add_u32 s71, s71, 1\n\t"
      "s_getpc_b64 s[88:89]\n\t"
      "s_add_u32 s88, s88, 12\n\t"
      "s_addc_u32 s89, s89, 0\n\t"
      "s_branch GBACK%=\n\t"
      "s_mov_b64 exec, s[36:37]\n\t"
      "s_add_u32 s78, s78, 4096\n\t"
      "buffer_load_dwordx4 v[176:179], v104, s[64:67], s78 offen \n\t"
      "buffer_load_dwordx4 v[180:183], v104, s[64:67], s78 offen offset:2048 \n\t"
      "s_add_u32 s78, s78, 4096\n\t"
      "buffer_load_dwordx4 v[184:187], v104, s[64:67], s78 offen \n\t"
      "s_mov_b64 exec, -1\n\t"
      "s_add_u32 s71, s71, 1\n\t"
      "s_getpc_b64 s[88:89]\n\t"
      "s_add_u32 s88, s88, 12\n\t"
      "s_addc_u32 s89, s89, 0\n\t"
      "s_branch GBACK%=\n\t"
      "s_mov_b64 exec, s[36:37]\n\t"
      "s_mov_b64 exec, -1\n\t"
      "v_and_b32 v106, 3, v96\n\t"
      "v_mul_u32_u24 v106, 24, v106\n\t"
      "v_lshlrev_b32 v104, 1, v100\n\t"
      "v_sub_u32 v106, v104, v106\n\t"
      "v_add_u32 v104, s76, v97\n\t"
      "v_add_u32 v105, s76, v106\n\t"
      "s_waitcnt vmcnt(30) lgkmcnt(0)\n\t"
      "ds_write_b128 v104, v[128:131]\n\t"
      "ds_write_b128 v104, v[132:135] offset:1024\n\t"
      "ds_write_b128 v104, v[136:139] offset:2048\n\t"
      "ds_write_b128 v104, v[140:143] offset:3072\n\t"
      "ds_write_b128 v104, v[144:147] offset:4096\n\t"
      "ds_write_b128 v104, v[148:151] offset:5120\n\t"
      "ds_write_b128 v104, v[152:155] offset:6144\n\t"
      "ds_write_b128 v104, v[156:159] offset:7168\n\t"
      "s_lshr_b32 s78, %0, 5\n\t"
      "s_lshl_b32 s78, s78, 2\n\t"
      "s_add_u32 s78, s78, %1\n\t"
      "s_lshl_b32 s78, s78, 10\n\t"
      "s_lshl_b32 s79, s74, 17\n\t"
      "s_add_u32 s78, s78, s79\n\t"
      "s_add_u32 s78, s78, 105086976\n\t"
      "s_waitcnt lgkmcnt(6)\n\t"
      "ds_read_b64_tr_b16 v[128:129], v105\n\t"
      "ds_read_b64_tr_b16 v[130:131], v105 offset:512\n\t"
      "ds_read_b64_tr_b16 v[132:133], v105 offset:4096\n\t"
      "ds_read_b64_tr_b16 v[134:135], v105 offset:4608\n\t"
      "ds_read_b64_tr_b16 v[136:137], v105 offset:32\n\t"
      "ds_read_b64_tr_b16 v[138:139], v105 offset:544\n\t"
      "ds_read_b64_tr_b16 v[140:141], v105 offset:4128\n\t"
      "ds_read_b64_tr_b16 v[142:143], v105 offset:4640\n\t"
      "s_waitcnt lgkmcnt(6)\n\t"
      "ds_read_b64_tr_b16 v[144:145], v105 offset:64\n\t"
      "ds_read_b64_tr_b16 v[146:147], v105 offset:576\n\t"
      "ds_read_b64_tr_b16 v[148:149], v105 offset:4160\n\t"
      "ds_read_b64_tr_b16 v[150:151], v105 offset:4672\n\t"
      "ds_read_b64_tr_b16 v[152:153], v105 offset:96\n\t"
      "ds_read_b64_tr_b16 v[154:155], v105 offset:608\n\t"
      "ds_read_b64_tr_b16 v[156:157], v105 offset:4192\n\t"
      "ds_read_b64_tr_b16 v[158:159], v105 offset:4704\n\t"
      "s_waitcnt lgkmcnt(0)\n\t"
      "s_mov_b64 exec, s[38:39]\n\t"
      "buffer_store_dwordx4 v[128:131], v97, s[60:63], s78 offen sc1\n\t"
      "s_add_u32 s78, s78, 16384\n\t"
      "buffer_store_dwordx4 v[132:135], v97, s[60:63], s78 offen sc1\n\t"
      "s_add_u32 s78, s78, 16384\n\t"
      "buffer_store_dwordx4 v[136:139], v97, s[60:63], s78 offen sc1\n\t"
      "s_mov_b64 exec, -1\n\t"
      "s_add_u32 s71, s71, 1\n\t"
      "s_getpc_b64 s[88:89]\n\t"
      "s_add_u32 s88, s88, 12\n\t"
      "s_addc_u32 s89, s89, 0\n\t"
      "s_branch GBACK%=\n\t"
      "s_mov_b64 exec, s[38:39]\n\t"
      "s_add_u32 s78, s78, 16384\n\t"
      "buffer_store_dwordx4 v[140:143], v97, s[60:63], s78 offen sc1\n\t"
      "s_add_u32 s78, s78, 16384\n\t"
      "buffer_store_dwordx4 v[144:147], v97, s[60:63], s78 offen sc1\n\t"
      "s_add_u32 s78, s78, 16384\n\t"
      "buffer_store_dwordx4 v[148:151], v97, s[60:63], s78 offen sc1\n\t"
      "s_mov_b64 exec, -1\n\t"
      "s_add_u32 s71, s71, 1\n\t"
      "s_getpc_b64 s[88:89]\n\t"
      "s_add_u32 s88, s88, 12\n\t"
      "s_addc_u32 s89, s89, 0\n\t"
      "s_branch GBACK%=\n\t"
      "s_mov_b64 exec, s[38:39]\n\t"
      "s_add_u32 s78, s78, 16384\n\t"
      "buffer_store_dwordx4 v[152:155], v97, s[60:63], s78 offen sc1\n\t"
      "s_add_u32 s78, s78, 16384\n\t"
      "buffer_store_dwordx4 v[156:159], v97, s[60:63], s78 offen sc1\n\t"
      "s_add_u32 s78, s78, 16384\n\t"
      "s_mov_b64 exec, -1\n\t"
      "v_lshlrev_b32 v104, 10, v96\n\t"
      "s_and_b32 s78, %0, 1\n\t"
      "s_lshl_b32 s78, s78, 5\n\t"
      "s_lshr_b32 s79, %0, 2\n\t"
      "s_add_u32 s78, s78, s79\n\t"
      "s_lshl_b32 s78, s78, 4\n\t"
      "s_lshl_b32 s79, %1, 16\n\t"
      "s_add_u32 s78, s78, s79\n\t"
      "s_lshl_b32 s79, %1, 10\n\t"
      "s_mov_b64 exec, s[36:37]\n\t"
      "s_add_u32 m0, s79, 131072\n\t"
      "s_nop 0\n\t"
      "buffer_load_dwordx4 v104, s[64:67], s78 offen lds\n\t"
      "s_mov_b64 exec, -1\n\t"
      "s_add_u32 s71, s71, 1\n\t"
      "s_getpc_b64 s[88:89]\n\t"
      "s_add_u32 s88, s88, 12\n\t"
      "s_addc_u32 s89, s89, 0\n\t"
      "s_branch GBACK%=\n\t"
      "s_mov_b64 exec, s[36:37]\n\t"
      "s_add_u32 s78, s78, 0x40000\n\t"
      "s_add_u32 m0, s79, 135168\n\t"
      "s_nop 0\n\t"
      "buffer_load_dwordx4 v104, s[64:67], s78 offen lds\n\t"
      "s_add_u32 s78, s78, 0x40000\n\t"
      "s_add_u32 m0, s79, 139264\n\t"
      "s_nop 0\n\t"
      "buffer_load_dwordx4 v104, s[64:67], s78 offen lds\n\t"
      "s_add_u32 s78, s78, 0x40000\n\t"
      "s_add_u32 m0, s79, 143360\n\t"
      "s_nop 0\n\t"
      "buffer_load_dwordx4 v104, s[64:67], s78 offen lds\n\t"
      "s_mov_b64 exec, -1\n\t"
      "s_add_u32 s71, s71, 1\n\t"
      "s_getpc_b64 s[88:89]\n\t"
      "s_add_u32 s88, s88, 12\n\t"
      "s_addc_u32 s89, s89, 0\n\t"
      "s_branch GBACK%=\n\t"
      "s_mov_b64 exec, s[36:37]\n\t"
      "s_add_u32 s78, s78, 0x40000\n\t"
      "s_add_u32 m0, s79, 147456\n\t"
      "s_nop 0\n\t"
      "buffer_load_dwordx4 v104, s[64:67], s78 offen lds\n\t"
      "s_add_u32 s78, s78, 0x40000\n\t"
      "s_add_u32 m0, s79, 151552\n\t"
      "s_nop 0\n\t"
      "buffer_load_dwordx4 v104, s[64:67], s78 offen lds\n\t"
      "s_add_u32 s78, s78, 0x40000\n\t"
      "s_add_u32 m0, s79, 155648\n\t"
      "s_nop 0\n\t"
      "buffer_load_dwordx4 v104, s[64:67], s78 offen lds\n\t"
      "s_mov_b64 exec, -1\n\t"
      "s_add_u32 s71, s71, 1\n\t"
      "s_getpc_b64 s[88:89]\n\t"
      "s_add_u32 s88, s88, 12\n\t"
      "s_addc_u32 s89, s89, 0\n\t"
      "s_branch GBACK%=\n\t"
      "s_mov_b64 exec, s[36:37]\n\t"
      "s_add_u32 s78, s78, 0x40000\n\t"
      "s_mov_b64 exec, -1\n\t"
      "s_mov_b64 s[64:65], s[46:47]\n\t"
      "v_lshrrev_b32 v99, 2, v96\n\t"
      "v_and_b32 v106, 3, v96\n\t"
      "v_lshlrev_b32 v99, 10, v99\n\t"
      "v_lshl_add_u32 v99, v106, 4, v99\n\t"
      "s_lshr_b32 s78, %0, 4\n\t"
      "s_lshl_b32 s78, s78, 19\n\t"
      "s_bfe_u32 s79, %0, 0x20002\n\t"
      "s_lshl_b32 s79, s79, 8\n\t"
      "s_add_u32 s78, s78, s79\n\t"
      "s_lshl_b32 s79, %1, 6\n\t"
      "s_add_u32 s78, s78, s79\n\t"
      "v_add_u32 v99, s78, v99\n\t"
      "s_mov_b32 s78, 262144\n\t"
      "buffer_load_dwordx4 v[192:195], v99, s[64:67], s78 offen \n\t"
      "s_mov_b32 s78, 278528\n\t"
      "buffer_load_dwordx4 v[196:199], v99, s[64:67], s78 offen \n\t"
      "s_mov_b32 s78, 294912\n\t"
      "buffer_load_dwordx4 v[200:203], v99, s[64:67], s78 offen \n\t"
      "s_add_u32 s71, s71, 1\n\t"
      "s_getpc_b64 s[88:89]\n\t"
      "s_add_u32 s88, s88, 12\n\t"
      "s_addc_u32 s89, s89, 0\n\t"
      "s_branch GBACK%=\n\t"
      "s_mov_b32 s78, 311296\n\t"
      "buffer_load_dwordx4 v[204:207], v99, s[64:67], s78 offen \n\t"
      "s_mov_b32 s78, 327680\n\t"
      "buffer_load_dwordx4 v[208:211], v99, s[64:67], s78 offen \n\t"
      "s_mov_b32 s78, 344064\n\t"
      "buffer_load_dwordx4 v[212:215], v99, s[64:67], s78 offen \n\t"
      "s_add_u32 s71, s71, 1\n\t"
      "s_getpc_b64 s[88:89]\n\t"
      "s_add_u32 s88, s88, 12\n\t"
      "s_addc_u32 s89, s89, 0\n\t"
      "s_branch GBACK%=\n\t"
      "s_mov_b32 s78, 360448\n\t"
      "buffer_load_dwordx4 v[216:219], v99, s[64:67], s78 offen \n\t"
      "s_mov_b32 s78, 376832\n\t"
      "buffer_load_dwordx4 v[220:223], v99, s[64:67], s78 offen \n\t"
      "s_mov_b32 s78, 393216\n\t"
      "buffer_load_dwordx4 v[224:227], v99, s[64:67], s78 offen \n\t"
      "s_add_u32 s71, s71, 1\n\t"
      "s_getpc_b64 s[88:89]\n\t"
      "s_add_u32 s88, s88, 12\n\t"
      "s_addc_u32 s89, s89, 0\n\t"
      "s_branch GBACK%=\n\t"
      "s_mov_b32 s78, 409600\n\t"
      "buffer_load_dwordx4 v[228:231], v99, s[64:67], s78 offen \n\t"
      "s_mov_b32 s78, 425984\n\t"
      "buffer_load_dwordx4 v[232:235], v99, s[64:67], s78 offen \n\t"
      "s_mov_b32 s78, 442368\n\t"
      "buffer_load_dwordx4 v[236:239], v99, s[64:67], s78 offen \n\t"
      "s_add_u32 s71, s71, 1\n\t"
      "s_getpc_b64 s[88:89]\n\t"
      "s_add_u32 s88, s88, 12\n\t"
      "s_addc_u32 s89, s89, 0\n\t"
      "s_branch GBACK%=\n\t"
      "s_mov_b32 s78, 458752\n\t"
      "buffer_load_dwordx4 v[240:243], v99, s[64:67], s78 offen \n\t"
      "s_mov_b32 s78, 475136\n\t"
      "buffer_load_dwordx4 v[244:247], v99, s[64:67], s78 offen \n\t"
      "s_mov_b32 s78, 491520\n\t"
      "buffer_load_dwordx4 v[248:251], v99, s[64:67], s78 offen \n\t"
      "s_add_u32 s71, s71, 1\n\t"
      "s_getpc_b64 s[88:89]\n\t"
      "s_add_u32 s88, s88, 12\n\t"
      "s_addc_u32 s89, s89, 0\n\t"
      "s_branch GBACK%=\n\t"
      "s_mov_b32 s78, 507904\n\t"
      "buffer_load_dwordx4 v[252:255], v99, s[64:67], s78 offen \n\t"
      "s_mov_b32 s78, 0\n\t"
      "buffer_load_dwordx4 v[128:131], v99, s[64:67], s78 offen \n\t"
      "s_mov_b32 s78, 16384\n\t"
      "buffer_load_dwordx4 v[132:135], v99, s[64:67], s78 offen \n\t"
      "s_add_u32 s71, s71, 1\n\t"
      "s_getpc_b64 s[88:89]\n\t"
      "s_add_u32 s88, s88, 12\n\t"
      "s_addc_u32 s89, s89, 0\n\t"
      "s_branch GBACK%=\n\t"
      "s_mov_b32 s78, 32768\n\t"
      "buffer_load_dwordx4 v[136:139], v99, s[64:67], s78 offen \n\t"
      "s_mov_b32 s78, 49152\n\t"
      "buffer_load_dwordx4 v[140:143], v99, s[64:67], s78 offen \n\t"
      "s_mov_b32 s78, 65536\n\t"
      "buffer_load_dwordx4 v[144:147], v99, s[64:67], s78 offen \n\t"
      "s_add_u32 s71, s71, 1\n\t"
      "s_getpc_b64 s[88:89]\n\t"
      "s_add_u32 s88, s88, 12\n\t"
      "s_addc_u32 s89, s89, 0\n\t"
      "s_branch GBACK%=\n\t"
      "s_mov_b32 s78, 81920\n\t"
      "buffer_load_dwordx4 v[148:151], v99, s[64:67], s78 offen \n\t"
      "s_mov_b32 s78, 98304\n\t"
      "buffer_load_dwordx4 v[152:155], v99, s[64:67], s78 offen \n\t"
      "s_mov_b32 s78, 114688\n\t"
      "buffer_load_dwordx4 v[156:159], v99, s[64:67], s78 offen \n\t"
      "s_add_u32 s71, s71, 1\n\t"
      "s_getpc_b64 s[88:89]\n\t"
      "s_add_u32 s88, s88, 12\n\t"
      "s_addc_u32 s89, s89, 0\n\t"
      "s_branch GBACK%=\n\t"
      "s_mov_b32 s78, 131072\n\t"
      "buffer_load_dwordx4 v[160:163], v99, s[64:67], s78 offen \n\t"
      "s_mov_b32 s78, 147456\n\t"
      "buffer_load_dwordx4 v[164:167], v99, s[64:67], s78 offen \n\t"
      "s_mov_b32 s78, 163840\n\t"
      "buffer_load_dwordx4 v[168:171], v99, s[64:67], s78 offen \n\t"
      "s_add_u32 s71, s71, 1\n\t"
      "s_getpc_b64 s[88:89]\n\t"
      "s_add_u32 s88, s88, 12\n\t"
      "s_addc_u32 s89, s89, 0\n\t"
      "s_branch GBACK%=\n\t"
      "s_mov_b32 s78, 180224\n\t"
      "buffer_load_dwordx4 v[172:175], v99, s[64:67], s78 offen \n\t"
      "s_mov_b32 s78, 196608\n\t"
      "buffer_load_dwordx4 v[176:179], v99, s[64:67], s78 offen \n\t"
      "s_mov_b32 s78, 212992\n\t"
      "buffer_load_dwordx4 v[180:183], v99, s[64:67], s78 offen \n\t"
      "s_add_u32 s71, s71, 1\n\t"
      "s_getpc_b64 s[88:89]\n\t"
      "s_add_u32 s88, s88, 12\n\t"
      "s_addc_u32 s89, s89, 0\n\t"
      "s_branch GBACK%=\n\t"
      "s_mov_b32 s78, 229376\n\t"
      "buffer_load_dwordx4 v[184:187], v99, s[64:67], s78 offen \n\t"
      "s_mov_b32 s78, 245760\n\t"
      "buffer_load_dwordx4 v[188:191], v99, s[64:67], s78 offen \n\t"
      "s_mov_b32 s71, 32\n\t"
      "s_getpc_b64 s[88:89]\n\t"
      "s_add_u32 s88, s88, 12\n\t"
      "s_addc_u32 s89, s89, 0\n\t"
      "s_branch GBACK%=\n\t"
      "s_nop 7\n\t"
      "v_add_u32 v104, s76, v97\n\t"
      "v_add_u32 v105, s76, v100\n\t"
      "s_sub_u32 s68, s68, 1\n\t"
      "s_waitcnt vmcnt(11) lgkmcnt(0)\n\t"
      "v_cvt_pk_bf16_f32 v84, v72, v73\n\t"
      "v_cvt_pk_bf16_f32 v85, v74, v75\n\t"
      "v_cvt_pk_bf16_f32 v86, v76, v77\n\t"
      "v_cvt_pk_bf16_f32 v87, v78, v79\n\t"
      "buffer_store_dwordx4 v[84:87], v97, s[60:63], s73 offen offset:1024 sc1\n\t"
      "v_cvt_pk_bf16_f32 v114, v92, v93\n\t"
      "v_cvt_pk_bf16_f32 v115, v94, v95\n\t"
      "s_nop 0\n\t"
      "v_permlane16_swap_b32 v112, v114\n\t"
      "v_permlane16_swap_b32 v113, v115\n\t"
      "buffer_store_dwordx4 v[112:115], v111, s[60:63], s73 offen sc1\n\t"
      "v_readlane_b32 s75, v103, s68\n\t"
      "s_waitcnt lgkmcnt(6)\n\t"
      "ds_write_b128 v104, v[128:131]\n\t"
      "ds_write_b128 v104, v[132:135] offset:1024\n\t"
      "ds_read_b64_tr_b16 a[0:1], v105\n\t"
      "ds_read_b64_tr_b16 a[2:3], v105 offset:256\n\t"
      "ds_read_b64_tr_b16 a[64:65], v105 offset:8\n\t"
      "ds_read_b64_tr_b16 a[66:67], v105 offset:264\n\t"
      "s_waitcnt lgkmcnt(6)\n\t"
      "ds_write_b128 v104, v[136:139] offset:2048\n\t"
      "ds_write_b128 v104, v[140:143] offset:3072\n\t"
      "ds_read_b64_tr_b16 a[4:5], v105 offset:2048\n\t"
      "ds_read_b64_tr_b16 a[6:7], v105 offset:2304\n\t"
      "ds_read_b64_tr_b16 a[68:69], v105 offset:2056\n\t"
      "ds_read_b64_tr_b16 a[70:71], v105 offset:2312\n\t"
      "s_waitcnt lgkmcnt(6)\n\t"
      "ds_write_b128 v104, v[144:147] offset:4096\n\t"
      "ds_write_b128 v104, v[148:151] offset:5120\n\t"
      "ds_read_b64_tr_b16 a[8:9], v105 offset:4096\n\t"
      "ds_read_b64_tr_b16 a[10:11], v105 offset:4352\n\t"
      "ds_read_b64_tr_b16 a[72:73], v105 offset:4104\n\t"
      "ds_read_b64_tr_b16 a[74:75], v105 offset:4360\n\t"
      "s_waitcnt lgkmcnt(6)\n\t"
      "ds_write_b128 v104, v[152:155] offset:6144\n\t"
      "ds_write_b128 v104, v[156:159] offset:7168\n\t"
      "ds_read_b64_tr_b16 a[12:13], v105 offset:6144\n\t"
      "ds_read_b64_tr_b16 a[14:15], v105 offset:6400\n\t"
      "ds_read_b64_tr_b16 a[76:77], v105 offset:6152\n\t"
      "ds_read_b64_tr_b16 a[78:79], v105 offset:6408\n\t"
      "s_waitcnt lgkmcnt(6)\n\t"
      "ds_write_b128 v104, v[160:163]\n\t"
      "ds_write_b128 v104, v[164:167] offset:1024\n\t"
      "ds_read_b64_tr_b16 a[16:17], v105\n\t"
      "ds_read_b64_tr_b16 a[18:19], v105 offset:256\n\t"
      "ds_read_b64_tr_b16 a[80:81], v105 offset:8\n\t"
      "ds_read_b64_tr_b16 a[82:83], v105 offset:264\n\t"
      "s_waitcnt lgkmcnt(6)\n\t"
      "ds_write_b128 v104, v[168:171] offset:2048\n\t"
      "ds_write_b128 v104, v[172:175] offset:3072\n\t"
      "ds_read_b64_tr_b16 a[20:21], v105 offset:2048\n\t"
      "ds_read_b64_tr_b16 a[22:23], v105 offset:2304\n\t"
      "ds_read_b64_tr_b16 a[84:85], v105 offset:2056\n\t"
      "ds_read_b64_tr_b16 a[86:87], v105 offset:2312\n\t"
      "s_waitcnt lgkmcnt(6)\n\t"
      "ds_write_b128 v104, v[176:179] offset:4096\n\t"
      "ds_write_b128 v104, v[180:183] offset:5120\n\t"
      "ds_read_b64_tr_b16 a[24:25], v105 offset:4096\n\t"
      "ds_read_b64_tr_b16 a[26:27], v105 offset:4352\n\t"
      "ds_read_b64_tr_b16 a[88:89], v105 offset:4104\n\t"
      "ds_read_b64_tr_b16 a[90:91], v105 offset:4360\n\t"
      "s_waitcnt lgkmcnt(6)\n\t"
      "ds_write_b128 v104, v[184:187] offset:6144\n\t"
      "ds_write_b128 v104, v[188:191] offset:7168\n\t"
      "ds_read_b64_tr_b16 a[28:29], v105 offset:6144\n\t"
      "ds_read_b64_tr_b16 a[30:31], v105 offset:6400\n\t"
      "ds_read_b64_tr_b16 a[92:93], v105 offset:6152\n\t"
      "ds_read_b64_tr_b16 a[94:95], v105 offset:6408\n\t"
      "s_waitcnt lgkmcnt(6)\n\t"
      "ds_write_b128 v104, v[192:195]\n\t"
      "ds_write_b128 v104, v[196:199] offset:1024\n\t"
      "ds_read_b64_tr_b16 a[32:33], v105\n\t"
      "ds_read_b64_tr_b16 a[34:35], v105 offset:256\n\t"
      "ds_read_b64_tr_b16 a[96:97], v105 offset:8\n\t"
      "ds_read_b64_tr_b16 a[98:99], v105 offset:264\n\t"
      "s_waitcnt lgkmcnt(6)\n\t"
      "ds_write_b128 v104, v[200:203] offset:2048\n\t"
      "ds_write_b128 v104, v[204:207] offset:3072\n\t"
      "ds_read_b64_tr_b16 a[36:37], v105 offset:2048\n\t"
      "ds_read_b64_tr_b16 a[38:39], v105 offset:2304\n\t"
      "ds_read_b64_tr_b16 a[100:101], v105 offset:2056\n\t"
      "ds_read_b64_tr_b16 a[102:103], v105 offset:2312\n\t"
      "s_waitcnt lgkmcnt(6)\n\t"
      "ds_write_b128 v104, v[208:211] offset:4096\n\t"
      "ds_write_b128 v104, v[212:215] offset:5120\n\t"
      "ds_read_b64_tr_b16 a[40:41], v105 offset:4096\n\t"
      "ds_read_b64_tr_b16 a[42:43], v105 offset:4352\n\t"
      "ds_read_b64_tr_b16 a[104:105], v105 offset:4104\n\t"
      "ds_read_b64_tr_b16 a[106:107], v105 offset:4360\n\t"
      "s_waitcnt lgkmcnt(6)\n\t"
      "ds_write_b128 v104, v[216:219] offset:6144\n\t"
      "ds_write_b128 v104, v[220:223] offset:7168\n\t"
      "ds_read_b64_tr_b16 a[44:45], v105 offset:6144\n\t"
      "ds_read_b64_tr_b16 a[46:47], v105 offset:6400\n\t"
      "ds_read_b64_tr_b16 a[108:109], v105 offset:6152\n\t"
      "ds_read_b64_tr_b16 a[110:111], v105 offset:6408\n\t"
      "s_waitcnt lgkmcnt(6)\n\t"
      "ds_write_b128 v104, v[224:227]\n\t"
      "ds_write_b128 v104, v[228:231] offset:1024\n\t"
      "ds_read_b64_tr_b16 a[48:49], v105\n\t"
      "ds_read_b64_tr_b16 a[50:51], v105 offset:256\n\t"
      "ds_read_b64_tr_b16 a[112:113], v105 offset:8\n\t"
      "ds_read_b64_tr_b16 a[114:115], v105 offset:264\n\t"
      "s_waitcnt lgkmcnt(6)\n\t"
      "ds_write_b128 v104, v[232:235] offset:2048\n\t"
      "ds_write_b128 v104, v[236:239] offset:3072\n\t"
      "ds_read_b64_tr_b16 a[52:53], v105 offset:2048\n\t"
      "ds_read_b64_tr_b16 a[54:55], v105 offset:2304\n\t"
      "ds_read_b64_tr_b16 a[116:117], v105 offset:2056\n\t"
      "ds_read_b64_tr_b16 a[118:119], v105 offset:2312\n\t"
      "s_waitcnt lgkmcnt(6)\n\t"
      "ds_write_b128 v104, v[240:243] offset:4096\n\t"
      "ds_write_b128 v104, v[244:247] offset:5120\n\t"
      "ds_read_b64_tr_b16 a[56:57], v105 offset:4096\n\t"
      "ds_read_b64_tr_b16 a[58:59], v105 offset:4352\n\t"
      "ds_read_b64_tr_b16 a[120:121], v105 offset:4104\n\t"
      "ds_read_b64_tr_b16 a[122:123], v105 offset:4360\n\t"
      "s_waitcnt lgkmcnt(6)\n\t"
      "ds_write_b128 v104, v[248:251] offset:6144\n\t"
      "ds_write_b128 v104, v[252:255] offset:7168\n\t"
      "ds_read_b64_tr_b16 a[60:61], v105 offset:6144\n\t"
      "ds_read_b64_tr_b16 a[62:63], v105 offset:6400\n\t"
      "ds_read_b64_tr_b16 a[124:125], v105 offset:6152\n\t"
      "ds_read_b64_tr_b16 a[126:127], v105 offset:6408\n\t"
      "s_waitcnt vmcnt(0) lgkmcnt(0)\n\t"
      "s_barrier\n\t"
      "s_mov_b64 exec, s[90:91]\n\t"
      "buffer_store_dword v107, off, s[60:63], s75 sc1\n\t"
      "s_sub_u32 s75, s75, 32\n\t"
      "buffer_store_dword v107, off, s[60:63], s75 sc1\n\t"
      "s_sub_u32 s75, s75, 2016\n\t"
      "buffer_store_dword v107, off, s[60:63], s75 sc1\n\t"
      "s_mov_b64 exec, -1\n\t"
      : "+{s34}"(w), "+{s33}"(wv), "+{s[0:1]}"(A), "=&s"(tag), "={a[0:31]}"(wo.a), "={a[32:63]}"(wo.b), "={a[64:95]}"(wo.c), "={a[96:127]}"(wo.d)
      :
      : "memory", "vcc", "scc", "v0", "v1", "v2", "v3", "v4", "v5", "v6", "v7", "v8", "v9", "v10", "v11", "v12", "v13", "v14", "v15", "v16", "v17", "v18", "v19", "v20", "v21", "v22", "v23", "v24", "v25", "v26", "v27", "v28", "v29", "v30", "v31", "v32", "v33", "v34", "v35", "v36", "v37", "v38", "v39", "v40", "v41", "v42", "v43", "v44", "v45", "v46", "v47", "v48", "v49", "v50", "v51", "v52", "v53", "v54", "v55", "v56", "v57", "v58", "v59", "v60", "v61", "v62", "v63", "v64", "v65", "v66", "v67", "v68", "v69", "v70", "v71", "v72", "v73", "v74", "v75", "v76", "v77", "v78", "v79", "v80", "v81", "v82", "v83", "v84", "v85", "v86", "v87", "v88", "v89", "v90", "v91", "v92", "v93", "v94", "v95", "v96", "v97", "v98", "v99", "v100", "v101", "v102", "v103", "v104", "v105", "v106", "v107", "v108", "v109", "v110", "v111", "v112", "v113", "v114", "v115", "v116", "v117", "v118", "v119", "v120", "v121", "v122", "v123", "v124", "v125", "v126", "v127", "v128", "v129", "v130", "v131", "v132", "v133", "v134", "v135", "v136", "v137", "v138", "v139", "v140", "v141", "v142", "v143", "v144", "v145", "v146", "v147", "v148", "v149", "v150", "v151", "v152", "v153", "v154", "v155", "v156", "v157", "v158", "v159", "v160", "v161", "v162", "v163", "v164", "v165", "v166", "v167", "v168", "v169", "v170", "v171", "v172", "v173", "v174", "v175", "v176", "v177", "v178", "v179", "v180", "v181", "v182", "v183", "v184", "v185", "v186", "v187", "v188", "v189", "v190", "v191", "v192", "v193", "v194", "v195", "v196", "v197", "v198", "v199", "v200", "v201", "v202", "v203", "v204", "v205", "v206", "v207", "v208", "v209", "v210", "v211", "v212", "v213", "v214", "v215", "v216", "v217", "v218", "v219", "v220", "v221", "v222", "v223", "v224", "v225", "v226", "v227", "v228", "v229", "v230", "v231", "v232", "v233", "v234", "v235", "v236", "v237", "v238", "v239", "v240", "v241", "v242", "v243", "v244", "v245", "v246", "v247", "v248", "v249", "v250", "v251", "v252", "v253", "v254", "v255", "a128", "a129", "a130", "a131", "a132", "a133", "a134", "a135", "a136", "a137", "a138", "a139", "a140", "a141", "a142", "a143", "a144", "a145", "a146", "a147", "a148", "a149", "a150", "a151", "a152", "a153", "a154", "a155", "a156", "a157", "a158", "a159", "a160", "a161", "a162", "a163", "a164", "a165", "a166", "a167", "a168", "a169", "a170", "a171", "a172", "a173", "a174", "a175", "a176", "a177", "a178", "a179", "a180", "a181", "a182", "a183", "a184", "a185", "a186", "a187", "a188", "a189", "a190", "a191", "a192", "a193", "a194", "a195", "a196", "a197", "a198", "a199", "a200", "a201", "a202", "a203", "a204", "a205", "a206", "a207", "a208", "a209", "a210", "a211", "a212", "a213", "a214", "a215", "a216", "a217", "a218", "a219", "a220", "a221", "a222", "a223", "a224", "a225", "a226", "a227", "a228", "a229", "a230", "a231", "a232", "a233", "a234", "a235", "a236", "a237", "a238", "a239", "a240", "a241", "a242", "a243", "a244", "a245", "a246", "a247", "a248", "a249", "a250", "a251", "a252", "a253", "a254", "a255", "s36", "s37", "s38", "s39", "s40", "s41", "s42", "s43", "s44", "s45", "s46", "s47", "s48", "s49", "s50", "s51", "s52", "s53", "s54", "s55", "s56", "s57", "s58", "s59", "s60", "s61", "s62", "s63", "s64", "s65", "s66", "s67", "s68", "s69", "s70", "s71", "s72", "s73", "s74", "s75", "s76", "s77", "s78", "s79", "s80", "s81", "s82", "s83", "s84", "s85", "s86", "s87", "s88", "s89", "s90", "s91", "s92", "s93", "s94", "s95");
  return tag;
}

__device__ __forceinline__ void worker_tasks(char* lds, int& w, int& wv, KArgs& A, unsigned tag, WoRegs& wo) {
  asm volatile("" : "+s"(w), "+s"(wv), "+s"(A));
  const int lane = fresh_lane(), l15 = lane & 15, l4 = lane >> 4;
  const int team = w & 3, j = w >> 2, g = j >> 2, c = j & 3, h = g * 4 + wv;
  const u64 wsa = (u64)A->ws, xa = (u64)A->x, oa = (u64)A->out;
  u32x16 c0 = {(unsigned)wsa, (unsigned)(wsa >> 32) & 0xffffu, WS_BYTES, 0x00020000u, (unsigned)xa, (unsigned)(xa >> 32) & 0xffffu, S_ * DM * 2, 0x00020000u,
               (unsigned)oa, (unsigned)(oa >> 32) & 0xffffu, S_ * DM * 2, 0x00020000u, O_STAMPS + (310000 + j * 4 + wv) * 8, (unsigned)(l15 - l4 * 4),
               (unsigned)(team + 4 * wv + 48), (unsigned)j};
  const int r0 = 0 ? h : j;
  u32x16 c1 = {(unsigned)team, (unsigned)(team + 4 * (0 ? c : wv)), O_QKV + (team * 32 + h) * 24576, O_KVM + (team * 32 + h) * 2048, O_VTM + (team * 32 + h) * 2048,
               O_PART + (team * 8 + g) * 32768, O_PART + r0 * 1024, O_PFLAG + team * 16384, O_KVSTEP + h * 64, O_RBUF, O_LNH, O_HBUF, (unsigned)r0,
               (unsigned)wv * 8192, tag, O_PFLAG + team * 16384 + (j * 4 + wv) * 128};
  u32x8 c4 = {(unsigned)lane, (unsigned)lane, (unsigned)lane, (unsigned)lane, O_TFLAG + (6 * 32 + h) * 64 + 32 + (l15 & 1) * 16, O_TFLAG + (15 * 32 + h) * 64 + 32, O_WM + lane * 256 + 4, (unsigned)lane};
  const int rc = 8 * (l15 + 16 * (l4 >> 1)) + 4 * (l4 & 1);
  const u32x2 w0 = *(const u32x2*)(A->w_r + rc), w1 = *(const u32x2*)(A->w_r + 256 + rc);
  u32x16 c2 = {w0[0] << 16, w0[0] & 0xffff0000u, w0[1] << 16, w0[1] & 0xffff0000u, w1[0] << 16, w1[0] & 0xffff0000u, w1[1] << 16, w1[1] & 0xffff0000u,
               (unsigned)lane * 16, (unsigned)rc * 2, (unsigned)(lane ^ ((l4 & 1) * 8)) * 16,
               (unsigned)(wv * 8192 + (l15 & 3) * 256 + (((l4 * 4 + (l15 >> 2)) ^ ((l15 & 1) * 8)) << 4)),
               (unsigned)(l4 * 128 + (l15 & 7) * 16), (unsigned)(lane & 31) * 16,
               (unsigned)(65536 + wv * 8192 + lane * 16), (unsigned)(65536 + lane * 16)};
  unsigned c3 = (unsigned)(l15 * DM + c * 128 + wv * 32 + l4 * 8) * 2;
  asm volatile(
      ".p2align 5\n\t"
      "s_nop 1\n\t"
      "v_readfirstlane_b32 s36, v0\n\t"
      "v_readfirstlane_b32 s37, v1\n\t"
      "v_readfirstlane_b32 s38, v2\n\t"
      "v_readfirstlane_b32 s39, v3\n\t"
      "v_readfirstlane_b32 s40, v4\n\t"
      "v_readfirstlane_b32 s41, v5\n\t"
      "v_readfirstlane_b32 s42, v6\n\t"
      "v_readfirstlane_b32 s43, v7\n\t"
      "v_readfirstlane_b32 s44, v8\n\t"
      "v_readfirstlane_b32 s45, v9\n\t"
      "v_readfirstlane_b32 s46, v10\n\t"
      "v_readfirstlane_b32 s47, v11\n\t"
      "v_readfirstlane_b32 s48, v16\n\t"
      "v_readfirstlane_b32 s49, v17\n\t"
      "v_readfirstlane_b32 s50, v18\n\t"
      "v_readfirstlane_b32 s51, v19\n\t"
      "v_readfirstlane_b32 s52, v20\n\t"
      "v_readfirstlane_b32 s53, v21\n\t"
      "v_readfirstlane_b32 s54, v22\n\t"
      "v_readfirstlane_b32 s55, v23\n\t"
      "v_readfirstlane_b32 s56, v24\n\t"
      "v_readfirstlane_b32 s57, v25\n\t"
      "v_readfirstlane_b32 s58, v26\n\t"
      "v_readfirstlane_b32 s59, v27\n\t"
      "v_readfirstlane_b32 s60, v28\n\t"
      "v_readfirstlane_b32 s61, v29\n\t"
      "v_readfirstlane_b32 s62, v30\n\t"
      "v_readfirstlane_b32 s63, v31\n\t"
      "v_readfirstlane_b32 s75, v12\n\t"
      "v_readfirstlane_b32 s93, v14\n\t"
      "v_readfirstlane_b32 s94, v15\n\t"
      "v_cmp_ge_i32 s[76:77], v13, 0\n\t"
      "v_cmp_ge_i32 s[78:79], v13, 1\n\t"
      "v_cmp_ge_i32 s[80:81], v13, 2\n\t"
      "v_cmp_ge_i32 s[82:83], v13, 3\n\t"
      "s_nop 4\n\t"
      "v_readfirstlane_b32 s99, v37\n\t"
      "s_add_u32 s98, s53, 12582912\n\t"
      "v_lshrrev_b32 v40, 4, v248\n\t"
      "v_and_b32 v41, 15, v40\n\t"
      "v_lshrrev_b32 v40, 1, v40\n\t"
      "v_and_b32 v40, 24, v40\n\t"
      "v_sub_u32 v41, v41, v40\n\t"
      "s_mov_b32 s69, 0x3f80\n\t"
      "s_mov_b32 s70, 0x3f800000\n\t"
      "v_mov_b32 v42, 0\n\t"
      "v_cmp_eq_i32 vcc, 0, v41\n\t"
      "v_mov_b32 v43, s69\n\t"
      "s_nop 0\n\t"
      "v_cndmask_b32 v42, v42, v43\n\t"
      "v_cmp_eq_i32 vcc, 1, v41\n\t"
      "v_mov_b32 v43, s70\n\t"
      "s_nop 0\n\t"
      "v_cndmask_b32 v42, v42, v43\n\t"
      "v_accvgpr_write_b32 a224, v42\n\t"
      "v_mov_b32 v42, 0\n\t"
      "v_cmp_eq_i32 vcc, 2, v41\n\t"
      "v_mov_b32 v43, s69\n\t"
      "s_nop 0\n\t"
      "v_cndmask_b32 v42, v42, v43\n\t"
      "v_cmp_eq_i32 vcc, 3, v41\n\t"
      "v_mov_b32 v43, s70\n\t"
      "s_nop 0\n\t"
      "v_cndmask_b32 v42, v42, v43\n\t"
      "v_accvgpr_write_b32 a225, v42\n\t"
      "v_mov_b32 v42, 0\n\t"
      "v_cmp_eq_i32 vcc, 4, v41\n\t"
      "v_mov_b32 v43, s69\n\t"
      "s_nop 0\n\t"
      "v_cndmask_b32 v42, v42, v43\n\t"
      "v_cmp_eq_i32 vcc, 5, v41\n\t"
      "v_mov_b32 v43, s70\n\t"
      "s_nop 0\n\t"
      "v_cndmask_b32 v42, v42, v43\n\t"
      "v_accvgpr_write_b32 a226, v42\n\t"
      "v_mov_b32 v42, 0\n\t"
      "v_cmp_eq_i32 vcc, 6, v41\n\t"
      "v_mov_b32 v43, s69\n\t"
      "s_nop 0\n\t"
      "v_cndmask_b32 v42, v42, v43\n\t"
      "v_cmp_eq_i32 vcc, 7, v41\n\t"
      "v_mov_b32 v43, s70\n\t"
      "s_nop 0\n\t"
      "v_cndmask_b32 v42, v42, v43\n\t"
      "v_accvgpr_write_b32 a227, v42\n\t"
      "v_mov_b32 v42, 0\n\t"
      "v_cmp_eq_i32 vcc, -16, v41\n\t"
      "v_mov_b32 v43, s69\n\t"
      "s_nop 0\n\t"
      "v_cndmask_b32 v42, v42, v43\n\t"
      "v_cmp_eq_i32 vcc, -15, v41\n\t"
      "v_mov_b32 v43, s70\n\t"
      "s_nop 0\n\t"
      "v_cndmask_b32 v42, v42, v43\n\t"
      "v_accvgpr_write_b32 a228, v42\n\t"
      "v_mov_b32 v42, 0\n\t"
      "v_cmp_eq_i32 vcc, -14, v41\n\t"
      "v_mov_b32 v43, s69\n\t"
      "s_nop 0\n\t"
      "v_cndmask_b32 v42, v42, v43\n\t"
      "v_cmp_eq_i32 vcc, -13, v41\n\t"
      "v_mov_b32 v43, s70\n\t"
      "s_nop 0\n\t"
      "v_cndmask_b32 v42, v42, v43\n\t"
      "v_accvgpr_write_b32 a229, v42\n\t"
      "v_mov_b32 v42, 0\n\t"
      "v_cmp_eq_i32 vcc, -12, v41\n\t"
      "v_mov_b32 v43, s69\n\t"
      "s_nop 0\n\t"
      "v_cndmask_b32 v42, v42, v43\n\t"
      "v_cmp_eq_i32 vcc, -11, v41\n\t"
      "v_mov_b32 v43, s70\n\t"
      "s_nop 0\n\t"
      "v_cndmask_b32 v42, v42, v43\n\t"
      "v_accvgpr_write_b32 a230, v42\n\t"
      "v_mov_b32 v42, 0\n\t"
      "v_cmp_eq_i32 vcc, -10, v41\n\t"
      "v_mov_b32 v43, s69\n\t"
      "s_nop 0\n\t"
      "v_cndmask_b32 v42, v42, v43\n\t"
      "v_cmp_eq_i32 vcc, -9, v41\n\t"
      "v_mov_b32 v43, s70\n\t"
      "s_nop 0\n\t"
      "v_cndmask_b32 v42, v42, v43\n\t"
      "v_accvgpr_write_b32 a231, v42\n\t"
      "s_mov_b32 s72, 0x40000\n\t"
      "WE0%=:\n\t"
      "buffer_load_dwordx4 v[40:43], v36, s[36:39], 0 offen sc1\n\t"
      "buffer_load_dword v48, v38, s[36:39], 0 offen sc1\n\t"
      "buffer_load_dword v49, v38, s[36:39], 0 offen offset:128 sc1\n\t"
      "s_waitcnt vmcnt(0)\n\t"
      "v_bitop3_b32 v40, v40, v41, s62 bitop3:0x7e\n\t"
      "v_bitop3_b32 v48, v48, v49, s62 bitop3:0x7e\n\t"
      "v_or_b32 v40, v40, v48\n\t"
      "v_cmp_ne_u32 vcc, 0, v40\n\t"
      "s_nop 0\n\t"
      "s_cbranch_vccz WE1%=\n\t"
      "s_sleep 8\n\t"
      "s_sub_u32 s72, s72, 1\n\t"
      "s_cmp_lg_u32 s72, 0\n\t"
      "s_cbranch_scc1 WE0%=\n\t"
      "WE1%=:\n\t"
      "buffer_load_dwordx4 a[128:131], v248, s[36:39], s50 offen  sc1\n\t"
      "buffer_load_dwordx4 a[132:135], v248, s[36:39], s50 offen offset:1024  sc1\n\t"
      "buffer_load_dwordx4 a[136:139], v248, s[36:39], s50 offen offset:2048  sc1\n\t"
      "buffer_load_dwordx4 a[140:143], v248, s[36:39], s50 offen offset:3072  sc1\n\t"
      "s_add_u32 s69, s50, 4096\n\t"
      "buffer_load_dwordx4 a[144:147], v248, s[36:39], s69 offen  sc1\n\t"
      "buffer_load_dwordx4 a[148:151], v248, s[36:39], s69 offen offset:1024  sc1\n\t"
      "buffer_load_dwordx4 a[152:155], v248, s[36:39], s69 offen offset:2048  sc1\n\t"
      "buffer_load_dwordx4 a[156:159], v248, s[36:39], s69 offen offset:3072  sc1\n\t"
      "s_add_u32 s69, s50, 8192\n\t"
      "s_add_u32 m0, s61, 0\n\t"
      "s_nop 0\n\t"
      "buffer_load_dwordx4 v250, s[36:39], s69 offen sc1 lds\n\t"
      "buffer_load_dwordx4 v250, s[36:39], s69 offen offset:1024 sc1 lds\n\t"
      "buffer_load_dwordx4 v250, s[36:39], s69 offen offset:2048 sc1 lds\n\t"
      "buffer_load_dwordx4 v250, s[36:39], s69 offen offset:3072 sc1 lds\n\t"
      "s_add_u32 s69, s50, 12288\n\t"
      "s_add_u32 m0, s61, 4096\n\t"
      "s_nop 0\n\t"
      "buffer_load_dwordx4 v250, s[36:39], s69 offen sc1 lds\n\t"
      "buffer_load_dwordx4 v250, s[36:39], s69 offen offset:1024 sc1 lds\n\t"
      "buffer_load_dwordx4 v250, s[36:39], s69 offen offset:2048 sc1 lds\n\t"
      "buffer_load_dwordx4 v250, s[36:39], s69 offen offset:3072 sc1 lds\n\t"
      "s_add_u32 s69, s50, 16384\n\t"
      "buffer_load_dwordx4 a[160:163], v248, s[36:39], s69 offen  sc1\n\t"
      "buffer_load_dwordx4 a[164:167], v248, s[36:39], s69 offen offset:1024  sc1\n\t"
      "buffer_load_dwordx4 a[168:171], v248, s[36:39], s69 offen offset:2048  sc1\n\t"
      "buffer_load_dwordx4 a[172:175], v248, s[36:39], s69 offen offset:3072  sc1\n\t"
      "s_add_u32 s69, s50, 20480\n\t"
      "buffer_load_dwordx4 a[176:179], v248, s[36:39], s69 offen  sc1\n\t"
      "buffer_load_dwordx4 a[180:183], v248, s[36:39], s69 offen offset:1024  sc1\n\t"
      "buffer_load_dwordx4 a[184:187], v248, s[36:39], s69 offen offset:2048  sc1\n\t"
      "buffer_load_dwordx4 a[188:191], v248, s[36:39], s69 offen offset:3072  sc1\n\t"
      "s_add_u32 s50, s50, 3145728\n\t"
      "s_mov_b32 s64, 0\n\t"
      "s_mov_b32 s65, 0\n\t"
      "s_mov_b32 s66, 0\n\t"
      "s_mov_b32 s96, 0\n\t"
      "s_mov_b32 s97, 0\n\t"
      "s_add_u32 s89, s49, 16\n\t"
      "s_sub_u32 s95, s94, s60\n\t"
      "s_lshl_b32 s95, s95, 10\n\t"
      "s_add_u32 s95, s95, s54\n\t"
      "s_mov_b32 s67, -1\n\t"
      "s_mov_b32 s74, 0xffff0000\n\t"
      "s_mov_b32 s84, 0x3db504f3\n\t"
      "s_mov_b32 s86, 0x3e0293ee\n\t"
      "s_mov_b32 s85, 0x3db504f3\n\t"
      "s_mov_b32 s87, 0x3e0293ee\n\t"
      "v_mov_b32 v239, 0xff61b1e6\n\t"
      "s_waitcnt vmcnt(0) lgkmcnt(0)\n\t"
      "s_branch WA%=\n\t"
      ".p2align 5\n\t"
      "WTOP%=:\n\t"
      "s_cmp_ge_u32 s48, 64\n\t"
      "s_cbranch_scc1 WFIN%=\n\t"
      "s_lshr_b32 s69, s48, 2\n\t"
      "s_cmp_eq_u32 s69, 5\n\t"
      "s_cbranch_scc1 WH2%=\n\t"
      "WT0%=:\n\t"
      "s_waitcnt vmcnt(17)\n\t"
      "s_cmp_eq_u32 s24, 9\n\t"
      "s_cbranch_scc1 WT1%=\n\t"
      "s_waitcnt vmcnt(10)\n\t"
      "s_cmp_lg_u32 s97, 0\n\t"
      "s_cbranch_scc1 WT1%=\n\t"
      "s_waitcnt vmcnt(9)\n\t"
      "s_cmp_eq_u32 s24, 8\n\t"
      "s_cbranch_scc1 WT1%=\n\t"
      "s_waitcnt vmcnt(2)\n\t"
      "WT1%=:\n\t"
      "v_min_u32 v228, v228, v229\n\t"
      "v_min3_u32 v228, v228, v230, v231\n\t"
      "v_min_u32 v232, v232, v233\n\t"
      "s_cmp_lg_u32 s65, 0\n\t"
      "s_cselect_b32 s70, s89, s49\n\t"
      "v_readfirstlane_b32 s88, v228\n\t"
      "s_memrealtime s[90:91]\n\t"
      "v_cmp_ge_u32 vcc, s70, v232\n\t"
      "s_lshr_b32 s69, s88, 16\n\t"
      "s_cmp_gt_u32 s69, s48\n\t"
      "s_cselect_b32 s64, 1, 0\n\t"
      "s_cmp_eq_u64 vcc, 0\n\t"
      "s_cselect_b32 s66, 1, 0\n\t"
      "s_cmp_eq_u32 s65, 2\n\t"
      "s_cselect_b32 s66, 0, s66\n\t"
      "s_add_u32 s70, s48, 4\n\t"
      "s_cmp_gt_u32 s69, s70\n\t"
      "s_cselect_b32 s96, 1, 0\n\t"
      "s_or_b32 s64, s64, s97\n\t"
      "s_cmp_lg_u32 s97, 0\n\t"
      "s_cbranch_scc1 WA%=\n\t"
      "s_cmp_lg_u32 s64, 0\n\t"
      "s_cbranch_scc1 WT2%=\n\t"
      "s_sub_u32 s69, s48, s69\n\t"
      "s_mul_i32 s69, s69, 170\n\t"
      "s_waitcnt lgkmcnt(0)\n\t"
      "s_sub_u32 s70, s90, s88\n\t"
      "s_add_u32 s70, s70, 200\n\t"
      "s_and_b32 s70, s70, 0xffff\n\t"
      "s_add_u32 s69, s69, 60\n\t"
      "s_cmp_ge_u32 s70, s69\n\t"
      "s_cselect_b32 s69, s48, 0\n\t"
      "s_cmp_ge_u32 s69, 40\n\t"
      "s_cselect_b32 s64, 2, 0\n\t"
      "s_cmp_eq_u32 s64, 0\n\t"
      "s_cbranch_scc1 WA%=\n\t"
      "WT2%=:\n\t"
      "buffer_load_dwordx4 v[128:131], v252, s[36:39], s51 offen sc1\n\t"
      "buffer_load_dwordx4 v[132:135], v252, s[36:39], s51 offen offset:512 sc1\n\t"
      "buffer_load_dwordx4 v[136:139], v252, s[36:39], s51 offen offset:1024 sc1\n\t"
      "buffer_load_dwordx4 v[140:143], v252, s[36:39], s51 offen offset:1536 sc1\n\t"
      "buffer_load_dwordx4 v[144:147], v253, s[36:39], s52 offen sc1\n\t"
      "buffer_load_dwordx4 v[148:151], v253, s[36:39], s52 offen offset:512 sc1\n\t"
      "buffer_load_dwordx4 v[152:155], v253, s[36:39], s52 offen offset:1024 sc1\n\t"
      "buffer_load_dwordx4 v[156:159], v253, s[36:39], s52 offen offset:1536 sc1\n\t"
      "WA%=:\n\t"
      "v_mfma_f32_16x16x32_bf16 v[0:3], a[128:131], a[160:163], 0\n\t"
      "ds_read_b64_tr_b16 a[192:193], v251\n\t"
      "ds_read_b64_tr_b16 a[194:195], v251 offset:1024\n\t"
      "v_mfma_f32_16x16x32_bf16 v[4:7], a[128:131], a[164:167], 0\n\t"
      "ds_read_b64_tr_b16 a[196:197], v251 offset:8\n\t"
      "ds_read_b64_tr_b16 a[198:199], v251 offset:1032\n\t"
      "v_mfma_f32_16x16x32_bf16 v[8:11], a[132:135], a[164:167], 0\n\t"
      "ds_read_b64_tr_b16 a[200:201], v251 offset:2048\n\t"
      "ds_read_b64_tr_b16 a[202:203], v251 offset:3072\n\t"
      "v_mfma_f32_16x16x32_bf16 v[0:3], a[136:139], a[168:171], v[0:3]\n\t"
      "ds_read_b64_tr_b16 a[204:205], v251 offset:2056\n\t"
      "ds_read_b64_tr_b16 a[206:207], v251 offset:3080\n\t"
      "v_mfma_f32_16x16x32_bf16 v[4:7], a[136:139], a[172:175], v[4:7]\n\t"
      "v_mfma_f32_16x16x32_bf16 v[8:11], a[140:143], a[172:175], v[8:11]\n\t"
      "s_waitcnt lgkmcnt(0)\n\t"
      "ds_read_b64_tr_b16 a[208:209], v251 offset:4096\n\t"
      "ds_read_b64_tr_b16 a[210:211], v251 offset:5120\n\t"
      "ds_read_b64_tr_b16 a[212:213], v251 offset:4104\n\t"
      "ds_read_b64_tr_b16 a[214:215], v251 offset:5128\n\t"
      "ds_read_b64_tr_b16 a[216:217], v251 offset:6144\n\t"
      "ds_read_b64_tr_b16 a[218:219], v251 offset:7168\n\t"
      "ds_read_b64_tr_b16 a[220:221], v251 offset:6152\n\t"
      "ds_read_b64_tr_b16 a[222:223], v251 offset:7176\n\t"
      "v_mfma_f32_16x16x32_bf16 v[0:3], a[144:147], a[176:179], v[0:3]\n\t"
      "v_mfma_f32_16x16x32_bf16 v[4:7], a[144:147], a[180:183], v[4:7]\n\t"
      "v_mfma_f32_16x16x32_bf16 v[8:11], a[148:151], a[180:183], v[8:11]\n\t"
      "v_mfma_f32_16x16x32_bf16 v[0:3], a[152:155], a[184:187], v[0:3]\n\t"
      "v_mfma_f32_16x16x32_bf16 v[4:7], a[152:155], a[188:191], v[4:7]\n\t"
      "v_mfma_f32_16x16x32_bf16 v[8:11], a[156:159], a[188:191], v[8:11]\n\t"
      "s_waitcnt lgkmcnt(0)\n\t"
      "buffer_load_dwordx4 a[128:131], v248, s[36:39], s50 offen  sc1\n\t"
      "s_nop 3\n\t"
      "v_cndmask_b32_e64 v0, v239, v0, s[76:77]\n\t"
      "s_nop 0\n\t"
      "v_cndmask_b32_e64 v8, v239, v8, s[76:77]\n\t"
      "v_cndmask_b32_e64 v1, v239, v1, s[78:79]\n\t"
      "v_cndmask_b32_e64 v9, v239, v9, s[78:79]\n\t"
      "v_cndmask_b32_e64 v2, v239, v2, s[80:81]\n\t"
      "v_cndmask_b32_e64 v10, v239, v10, s[80:81]\n\t"
      "v_cndmask_b32_e64 v3, v239, v3, s[82:83]\n\t"
      "v_cndmask_b32_e64 v11, v239, v11, s[82:83]\n\t"
      "v_max3_f32 v234, v0, v1, v2\n\t"
      "v_max3_f32 v235, v4, v5, v6\n\t"
      "v_max_f32 v234, v234, v3\n\t"
      "v_max3_f32 v235, v235, v7, v8\n\t"
      "v_max3_f32 v235, v235, v9, v10\n\t"
      "v_max_f32 v235, v235, v11\n\t"
      "s_nop 1\n\t"
      "v_permlane16_swap_b32 v234, v235\n\t"
      "v_max_f32 v234, v234, v235\n\t"
      "buffer_load_dwordx4 a[132:135], v248, s[36:39], s50 offen offset:1024  sc1\n\t"
      "v_mov_b32 v235, v234\n\t"
      "s_nop 1\n\t"
      "v_permlane32_swap_b32 v234, v235\n\t"
      "v_max_f32 v234, v234, v235\n\t"
      "v_mov_b32 v235, v234\n\t"
      "s_nop 1\n\t"
      "v_permlane16_swap_b32 v234, v235\n\t"
      "v_pk_mul_f32 v[12:13], v[234:235], s[86:87]\n\t"
      "v_pk_fma_f32 v[0:1], v[0:1], s[86:87], v[12:13] op_sel:[0,0,0] op_sel_hi:[1,1,0] neg_lo:[0,0,1] neg_hi:[0,0,1]\n\t"
      "v_pk_fma_f32 v[2:3], v[2:3], s[86:87], v[12:13] op_sel:[0,0,0] op_sel_hi:[1,1,0] neg_lo:[0,0,1] neg_hi:[0,0,1]\n\t"
      "v_pk_fma_f32 v[4:5], v[4:5], s[86:87], v[12:13] op_sel:[0,0,1] op_sel_hi:[1,1,1] neg_lo:[0,0,1] neg_hi:[0,0,1]\n\t"
      "v_pk_fma_f32 v[6:7], v[6:7], s[86:87], v[12:13] op_sel:[0,0,1] op_sel_hi:[1,1,1] neg_lo:[0,0,1] neg_hi:[0,0,1]\n\t"
      "v_pk_fma_f32 v[8:9], v[8:9], s[86:87], v[12:13] op_sel:[0,0,1] op_sel_hi:[1,1,1] neg_lo:[0,0,1] neg_hi:[0,0,1]\n\t"
      "v_pk_fma_f32 v[10:11], v[10:11], s[86:87], v[12:13] op_sel:[0,0,1] op_sel_hi:[1,1,1] neg_lo:[0,0,1] neg_hi:[0,0,1]\n\t"
      "v_exp_f32 v0, v0\n\t"
      "v_exp_f32 v1, v1\n\t"
      "buffer_load_dwordx4 a[136:139], v248, s[36:39], s50 offen offset:2048  sc1\n\t"
      "v_exp_f32 v2, v2\n\t"
      "v_exp_f32 v3, v3\n\t"
      "v_exp_f32 v4, v4\n\t"
      "v_exp_f32 v5, v5\n\t"
      "v_exp_f32 v6, v6\n\t"
      "v_exp_f32 v7, v7\n\t"
      "v_exp_f32 v8, v8\n\t"
      "v_exp_f32 v9, v9\n\t"
      "buffer_load_dwordx4 a[140:143], v248, s[36:39], s50 offen offset:3072  sc1\n\t"
      "v_exp_f32 v10, v10\n\t"
      "v_exp_f32 v11, v11\n\t"
      "v_mov_b64 v[18:19], 0\n\t"
      "v_cvt_pk_bf16_f32 v16, v0, v1\n\t"
      "v_cvt_pk_bf16_f32 v17, v2, v3\n\t"
      "v_cvt_pk_bf16_f32 v20, v4, v5\n\t"
      "v_cvt_pk_bf16_f32 v21, v6, v7\n\t"
      "v_cvt_pk_bf16_f32 v22, v8, v9\n\t"
      "v_cvt_pk_bf16_f32 v23, v10, v11\n\t"
      "v_mfma_f32_16x16x32_bf16 v[64:67], a[192:195], v[16:19], 0\n\t"
      "v_pk_add_f32 v[0:1], v[0:1], v[2:3]\n\t"
      "s_add_u32 s69, s50, 4096\n\t"
      "buffer_load_dwordx4 a[144:147], v248, s[36:39], s69 offen  sc1\n\t"
      "v_mfma_f32_16x16x32_bf16 v[96:99], a[192:195], v[20:23], 0\n\t"
      "v_pk_add_f32 v[4:5], v[4:5], v[6:7]\n\t"
      "v_mfma_f32_16x16x32_bf16 v[68:71], a[196:199], v[16:19], 0\n\t"
      "v_pk_add_f32 v[8:9], v[8:9], v[10:11]\n\t"
      "v_mfma_f32_16x16x32_bf16 v[100:103], a[196:199], v[20:23], 0\n\t"
      "v_add_f32 v236, v0, v1\n\t"
      "v_mfma_f32_16x16x32_bf16 v[72:75], a[200:203], v[16:19], 0\n\t"
      "buffer_load_dwordx4 a[148:151], v248, s[36:39], s69 offen offset:1024  sc1\n\t"
      "v_pk_add_f32 v[4:5], v[4:5], v[8:9]\n\t"
      "v_mfma_f32_16x16x32_bf16 v[104:107], a[200:203], v[20:23], 0\n\t"
      "v_add_f32 v237, v4, v5\n\t"
      "v_mfma_f32_16x16x32_bf16 v[76:79], a[204:207], v[16:19], 0\n\t"
      "v_mfma_f32_16x16x32_bf16 v[108:111], a[204:207], v[20:23], 0\n\t"
      "buffer_load_dwordx4 a[152:155], v248, s[36:39], s69 offen offset:2048  sc1\n\t"
      "v_mfma_f32_16x16x32_bf16 v[80:83], a[208:211], v[16:19], 0\n\t"
      "v_mfma_f32_16x16x32_bf16 v[112:115], a[208:211], v[20:23], 0\n\t"
      "v_mfma_f32_16x16x32_bf16 v[84:87], a[212:215], v[16:19], 0\n\t"
      "v_mfma_f32_16x16x32_bf16 v[116:119], a[212:215], v[20:23], 0\n\t"
      "buffer_load_dwordx4 a[156:159], v248, s[36:39], s69 offen offset:3072  sc1\n\t"
      "v_mfma_f32_16x16x32_bf16 v[88:91], a[216:219], v[16:19], 0\n\t"
      "v_mfma_f32_16x16x32_bf16 v[120:123], a[216:219], v[20:23], 0\n\t"
      "v_mfma_f32_16x16x32_bf16 v[92:95], a[220:223], v[16:19], 0\n\t"
      "v_mfma_f32_16x16x32_bf16 v[124:127], a[220:223], v[20:23], 0\n\t"
      "s_waitcnt vmcnt(8)\n\t"
      "s_cmp_lt_u32 s48, 4\n\t"
      "s_cbranch_scc1 WGO%=\n\t"
      "v_mov_b32 v0, s48\n\t"
      "s_mov_b64 exec, 1\n\t"
      "buffer_store_dword v0, off, s[36:39], s63 sc1\n\t"
      "s_mov_b64 exec, -1\n\t"
      "s_cmp_lt_i32 s67, 0\n\t"
      "s_cbranch_scc1 WPB1%=\n\t"
      "v_mov_b32 v8, s68\n\t"
      "s_mov_b64 exec, 1\n\t"
      "buffer_store_dword v8, off, s[36:39], s67 sc1\n\t"
      "s_mov_b64 exec, -1\n\t"
      "s_mov_b32 s67, -1\n\t"
      "WPB1%=:\n\t"
      "WGO%=:\n\t"
      "s_cmp_eq_u32 s64, 1\n\t"
      "s_cbranch_scc1 WKV%=\n\t"
      "s_cmp_lg_u32 s65, 0\n\t"
      "s_cbranch_scc1 WR2%=\n\t"
      "WGO2%=:\n\t"
      "s_cmp_lt_u32 s48, 40\n\t"
      "s_cselect_b32 s71, 1, -1\n\t"
      "s_add_u32 s71, s71, s48\n\t"
      "s_mov_b32 s72, 0x40000\n\t"
      "WLOOP%=:\n\t"
      "s_cmp_ge_u32 s49, 48\n\t"
      "s_cselect_b32 s69, s49, 99\n\t"
      "s_cmp_lt_u32 s69, s48\n\t"
      "s_cbranch_scc1 WRW%=\n\t"
      "s_cmp_eq_u32 s64, 2\n\t"
      "s_cbranch_scc1 WKC%=\n\t"
      "v_lshlrev_b32 v6, 4, v248\n\t"
      "v_mov_b64 v[4:5], 0\n\t"
      "buffer_load_dwordx4 v[0:3], off, s[36:39], s56 sc1\n\t"
      "s_cmp_ge_u32 s49, s48\n\t"
      "s_cbranch_scc1 WL1%=\n\t"
      "buffer_load_dword v4, v6, s[36:39], s55 offen sc1\n\t"
      "buffer_load_dword v5, v6, s[36:39], s55 offen offset:128 sc1\n\t"
      "WL1%=:\n\t"
      "s_waitcnt vmcnt(0)\n\t"
      "s_cmp_lt_i32 s67, 0\n\t"
      "s_cbranch_scc1 WL2%=\n\t"
      "v_mov_b32 v8, s68\n\t"
      "s_mov_b64 exec, 1\n\t"
      "buffer_store_dword v8, off, s[36:39], s67 sc1\n\t"
      "s_mov_b64 exec, -1\n\t"
      "s_mov_b32 s67, -1\n\t"
      "WL2%=:\n\t"
      "v_min_u32 v0, v0, v1\n\t"
      "v_min3_u32 v0, v0, v2, v3\n\t"
      "v_min_u32 v4, v4, v5\n\t"
      "v_readfirstlane_b32 s88, v0\n\t"
      "v_cmp_ge_u32 vcc, s49, v4\n\t"
      "s_lshr_b32 s69, s88, 16\n\t"
      "s_max_u32 s70, s71, s48\n\t"
      "s_cmp_ge_u32 s69, s70\n\t"
      "s_cbranch_scc1 WKL%=\n\t"
      "s_cmp_ge_u32 s69, s71\n\t"
      "s_cbranch_scc1 WKF%=\n\t"
      "s_cbranch_vccz WRW%=\n\t"
      "s_sleep 6\n\t"
      "s_cmp_lt_u32 s49, s48\n\t"
      "s_cbranch_scc1 WKM%=\n\t"
      "s_sub_u32 s69, s71, s69\n\t"
      "s_min_u32 s69, s69, 9\n\t"
      "WKN%=:\n\t"
      "s_sub_u32 s69, s69, 1\n\t"
      "s_cmp_eq_u32 s69, 0\n\t"
      "s_cbranch_scc1 WKM%=\n\t"
      "s_sleep 48\n\t"
      "s_branch WKN%=\n\t"
      "WKM%=:\n\t"
      "s_sub_u32 s72, s72, 1\n\t"
      "s_cmp_gt_i32 s72, 0\n\t"
      "s_cbranch_scc1 WLOOP%=\n\t"
      "s_branch WKL%=\n\t"
      ".p2align 5\n\t"
      "WFIN%=:\n\t"
      "s_mov_b32 s64, 0\n\t"
      "s_mov_b32 s72, 0x40000\n\t"
      "s_waitcnt vmcnt(2)\n\t"
      "WFIN1%=:\n\t"
      "s_cmp_lt_i32 s67, 0\n\t"
      "s_cbranch_scc1 WF1%=\n\t"
      "v_mov_b32 v8, s68\n\t"
      "s_mov_b64 exec, 1\n\t"
      "buffer_store_dword v8, off, s[36:39], s67 sc1\n\t"
      "s_mov_b64 exec, -1\n\t"
      "s_mov_b32 s67, -1\n\t"
      "WF1%=:\n\t"
      "s_cmp_lg_u32 s65, 0\n\t"
      "s_cbranch_scc1 WRC%=\n\t"
      "s_cmp_ge_u32 s49, 64\n\t"
      "s_cbranch_scc1 WEND%=\n\t"
      "s_cmp_ge_u32 s49, 48\n\t"
      "s_cbranch_scc1 WRS%=\n\t"
      "v_lshlrev_b32 v6, 4, v248\n\t"
      "WRF%=:\n\t"
      "buffer_load_dword v4, v6, s[36:39], s55 offen sc1\n\t"
      "buffer_load_dword v5, v6, s[36:39], s55 offen offset:128 sc1\n\t"
      "s_waitcnt vmcnt(0)\n\t"
      "v_min_u32 v4, v4, v5\n\t"
      "v_cmp_ge_u32 vcc, s49, v4\n\t"
      "s_nop 0\n\t"
      "s_cbranch_vccz WRW%=\n\t"
      "s_sleep 8\n\t"
      "s_sub_u32 s72, s72, 1\n\t"
      "s_cmp_gt_i32 s72, 0\n\t"
      "s_cbranch_scc1 WRF%=\n\t"
      "WRS%=:\n\t"
      "s_waitcnt vmcnt(0)\n\t"
      "WRW%=:\n\t"
      "s_lshl_b32 s70, s49, 5\n\t"
      "s_add_u32 s70, s70, s60\n\t"
      "s_lshl_b32 s70, s70, 10\n\t"
      "buffer_load_dwordx2 v[224:225], v249, s[40:43], s70 offen \n\t"
      "buffer_load_dwordx2 v[226:227], v249, s[40:43], s70 offen offset:512 \n\t"
      "WRL%=:\n\t"
      "s_lshl_b32 s69, s49, 18\n\t"
      "s_add_u32 s69, s69, s54\n\t"
      "buffer_load_dwordx4 v[160:163], v248, s[36:39], s69 offen sc1\n\t"
      "s_add_u32 s69, s69, 0x8000\n\t"
      "buffer_load_dwordx4 v[164:167], v248, s[36:39], s69 offen sc1\n\t"
      "s_add_u32 s69, s69, 0x8000\n\t"
      "buffer_load_dwordx4 v[168:171], v248, s[36:39], s69 offen sc1\n\t"
      "s_add_u32 s69, s69, 0x8000\n\t"
      "buffer_load_dwordx4 v[172:175], v248, s[36:39], s69 offen sc1\n\t"
      "s_add_u32 s69, s69, 0x8000\n\t"
      "buffer_load_dwordx4 v[176:179], v248, s[36:39], s69 offen sc1\n\t"
      "s_add_u32 s69, s69, 0x8000\n\t"
      "buffer_load_dwordx4 v[180:183], v248, s[36:39], s69 offen sc1\n\t"
      "s_add_u32 s69, s69, 0x8000\n\t"
      "buffer_load_dwordx4 v[184:187], v248, s[36:39], s69 offen sc1\n\t"
      "s_add_u32 s69, s69, 0x8000\n\t"
      "buffer_load_dwordx4 v[188:191], v248, s[36:39], s69 offen sc1\n\t"
      "s_waitcnt vmcnt(0)\n\t"
      "s_cmp_lt_i32 s67, 0\n\t"
      "s_cbranch_scc1 WF2%=\n\t"
      "v_mov_b32 v8, s68\n\t"
      "s_mov_b64 exec, 1\n\t"
      "buffer_store_dword v8, off, s[36:39], s67 sc1\n\t"
      "s_mov_b64 exec, -1\n\t"
      "s_mov_b32 s67, -1\n\t"
      "WF2%=:\n\t"
      "WRC%=:\n\t"
      "v_mfma_f32_16x16x32_bf16 v[16:19], a[224:227], v[160:163], 0\n\t"
      "v_mfma_f32_16x16x32_bf16 v[20:23], a[228:231], v[160:163], 0\n\t"
      "v_and_b32 v193, s74, v224\n\t"
      "v_lshlrev_b32 v192, 16, v224\n\t"
      "v_mfma_f32_16x16x32_bf16 v[16:19], a[224:227], v[164:167], v[16:19]\n\t"
      "v_mfma_f32_16x16x32_bf16 v[20:23], a[228:231], v[164:167], v[20:23]\n\t"
      "v_and_b32 v195, s74, v225\n\t"
      "v_lshlrev_b32 v194, 16, v225\n\t"
      "v_mfma_f32_16x16x32_bf16 v[16:19], a[224:227], v[168:171], v[16:19]\n\t"
      "v_mfma_f32_16x16x32_bf16 v[20:23], a[228:231], v[168:171], v[20:23]\n\t"
      "v_and_b32 v197, s74, v226\n\t"
      "v_lshlrev_b32 v196, 16, v226\n\t"
      "v_mfma_f32_16x16x32_bf16 v[16:19], a[224:227], v[172:175], v[16:19]\n\t"
      "v_mfma_f32_16x16x32_bf16 v[20:23], a[228:231], v[172:175], v[20:23]\n\t"
      "v_and_b32 v199, s74, v227\n\t"
      "v_lshlrev_b32 v198, 16, v227\n\t"
      "v_mfma_f32_16x16x32_bf16 v[16:19], a[224:227], v[176:179], v[16:19]\n\t"
      "v_mfma_f32_16x16x32_bf16 v[20:23], a[228:231], v[176:179], v[20:23]\n\t"
      "v_mfma_f32_16x16x32_bf16 v[16:19], a[224:227], v[180:183], v[16:19]\n\t"
      "v_mfma_f32_16x16x32_bf16 v[20:23], a[228:231], v[180:183], v[20:23]\n\t"
      "v_mfma_f32_16x16x32_bf16 v[16:19], a[224:227], v[184:187], v[16:19]\n\t"
      "v_mfma_f32_16x16x32_bf16 v[20:23], a[228:231], v[184:187], v[20:23]\n\t"
      "v_mfma_f32_16x16x32_bf16 v[16:19], a[224:227], v[188:191], v[16:19]\n\t"
      "v_mfma_f32_16x16x32_bf16 v[20:23], a[228:231], v[188:191], v[20:23]\n\t"
      "s_nop 6\n\t"
      "v_cvt_pk_bf16_f32 v32, v16, v17\n\t"
      "v_cvt_pk_bf16_f32 v34, v18, v19\n\t"
      "v_cvt_pk_bf16_f32 v36, v20, v21\n\t"
      "v_cvt_pk_bf16_f32 v38, v22, v23\n\t"
      "v_and_b32 v33, s74, v32\n\t"
      "v_lshlrev_b32 v32, 16, v32\n\t"
      "v_and_b32 v35, s74, v34\n\t"
      "v_lshlrev_b32 v34, 16, v34\n\t"
      "v_and_b32 v37, s74, v36\n\t"
      "v_lshlrev_b32 v36, 16, v36\n\t"
      "v_and_b32 v39, s74, v38\n\t"
      "v_lshlrev_b32 v38, 16, v38\n\t"
      "v_pk_add_f32 v[192:193], v[192:193], v[32:33]\n\t"
      "v_pk_add_f32 v[194:195], v[194:195], v[34:35]\n\t"
      "v_pk_add_f32 v[196:197], v[196:197], v[36:37]\n\t"
      "v_pk_add_f32 v[198:199], v[198:199], v[38:39]\n\t"
      "v_cvt_pk_bf16_f32 v200, v192, v193\n\t"
      "v_cvt_pk_bf16_f32 v201, v194, v195\n\t"
      "v_cvt_pk_bf16_f32 v202, v196, v197\n\t"
      "v_cvt_pk_bf16_f32 v203, v198, v199\n\t"
      "v_and_b32 v193, s74, v200\n\t"
      "v_lshlrev_b32 v192, 16, v200\n\t"
      "v_and_b32 v195, s74, v201\n\t"
      "v_lshlrev_b32 v194, 16, v201\n\t"
      "v_and_b32 v197, s74, v202\n\t"
      "v_lshlrev_b32 v196, 16, v202\n\t"
      "v_and_b32 v199, s74, v203\n\t"
      "v_lshlrev_b32 v198, 16, v203\n\t"
      "v_pk_add_f32 v[32:33], v[192:193], v[196:197]\n\t"
      "v_pk_add_f32 v[34:35], v[194:195], v[198:199]\n\t"
      "v_pk_add_f32 v[32:33], v[32:33], v[34:35]\n\t"
      "v_add_f32 v32, v32, v33\n\t"
      "s_nop 1\n\t"
      "v_add_f32_dpp v32, v32, v32 quad_perm:[1,0,3,2] row_mask:0xf bank_mask:0xf\n\t"
      "s_nop 1\n\t"
      "v_add_f32_dpp v32, v32, v32 quad_perm:[2,3,0,1] row_mask:0xf bank_mask:0xf\n\t"
      "s_nop 1\n\t"
      "v_add_f32_dpp v32, v32, v32 row_ror:4 row_mask:0xf bank_mask:0xf\n\t"
      "s_nop 1\n\t"
      "v_add_f32_dpp v32, v32, v32 row_ror:8 row_mask:0xf bank_mask:0xf\n\t"
      "v_mov_b32 v33, v32\n\t"
      "s_nop 1\n\t"
      "v_permlane16_swap_b32 v32, v33\n\t"
      "v_add_f32 v32, v32, v33\n\t"
      "v_mov_b32 v33, v32\n\t"
      "s_nop 1\n\t"
      "v_permlane32_swap_b32 v32, v33\n\t"
      "v_add_f32 v32, v32, v33\n\t"
      "v_cmp_u_f32 vcc, v32, v32\n\t"
      "v_mul_f32 v204, 0x3b000000, v32\n\t"
      "s_cbranch_vccnz WRN%=\n\t"
      "WRD%=:\n\t"
      "v_pk_add_f32 v[40:41], v[192:193], v[204:205] op_sel_hi:[1,0] neg_lo:[0,1] neg_hi:[0,1]\n\t"
      "v_pk_add_f32 v[42:43], v[194:195], v[204:205] op_sel_hi:[1,0] neg_lo:[0,1] neg_hi:[0,1]\n\t"
      "v_pk_add_f32 v[44:45], v[196:197], v[204:205] op_sel_hi:[1,0] neg_lo:[0,1] neg_hi:[0,1]\n\t"
      "v_pk_add_f32 v[46:47], v[198:199], v[204:205] op_sel_hi:[1,0] neg_lo:[0,1] neg_hi:[0,1]\n\t"
      "v_pk_mul_f32 v[32:33], v[40:41], v[40:41]\n\t"
      "v_pk_fma_f32 v[32:33], v[42:43], v[42:43], v[32:33]\n\t"
      "v_pk_fma_f32 v[32:33], v[44:45], v[44:45], v[32:33]\n\t"
      "v_pk_fma_f32 v[32:33], v[46:47], v[46:47], v[32:33]\n\t"
      "v_add_f32 v32, v32, v33\n\t"
      "s_nop 1\n\t"
      "v_add_f32_dpp v32, v32, v32 quad_perm:[1,0,3,2] row_mask:0xf bank_mask:0xf\n\t"
      "s_nop 1\n\t"
      "v_add_f32_dpp v32, v32, v32 quad_perm:[2,3,0,1] row_mask:0xf bank_mask:0xf\n\t"
      "s_nop 1\n\t"
      "v_add_f32_dpp v32, v32, v32 row_ror:4 row_mask:0xf bank_mask:0xf\n\t"
      "s_nop 1\n\t"
      "v_add_f32_dpp v32, v32, v32 row_ror:8 row_mask:0xf bank_mask:0xf\n\t"
      "v_mov_b32 v33, v32\n\t"
      "s_nop 1\n\t"
      "v_permlane16_swap_b32 v32, v33\n\t"
      "v_add_f32 v32, v32, v33\n\t"
      "v_mov_b32 v33, v32\n\t"
      "s_nop 1\n\t"
      "v_permlane32_swap_b32 v32, v33\n\t"
      "v_add_f32 v32, v32, v33\n\t"
      "v_mul_f32 v205, 0x3b000000, v32\n\t"
      "v_cvt_pk_bf16_f32 v204, v204, v205\n\t"
      "v_and_b32 v205, s74, v204\n\t"
      "v_lshlrev_b32 v204, 16, v204\n\t"
      "v_add_f32 v205, 0x3727c5ac, v205\n\t"
      "v_pk_add_f32 v[40:41], v[192:193], v[204:205] op_sel_hi:[1,0] neg_lo:[0,1] neg_hi:[0,1]\n\t"
      "v_pk_add_f32 v[42:43], v[194:195], v[204:205] op_sel_hi:[1,0] neg_lo:[0,1] neg_hi:[0,1]\n\t"
      "v_pk_add_f32 v[44:45], v[196:197], v[204:205] op_sel_hi:[1,0] neg_lo:[0,1] neg_hi:[0,1]\n\t"
      "v_pk_add_f32 v[46:47], v[198:199], v[204:205] op_sel_hi:[1,0] neg_lo:[0,1] neg_hi:[0,1]\n\t"
      "v_cvt_pk_bf16_f32 v205, v205, v205\n\t"
      "v_and_b32 v205, s74, v205\n\t"
      "v_sqrt_f32 v205, v205\n\t"
      "v_cvt_pk_bf16_f32 v40, v40, v41\n\t"
      "v_cvt_pk_bf16_f32 v42, v42, v43\n\t"
      "v_cvt_pk_bf16_f32 v44, v44, v45\n\t"
      "v_cvt_pk_bf16_f32 v46, v46, v47\n\t"
      "v_cvt_pk_bf16_f32 v205, v205, v205\n\t"
      "v_and_b32 v205, s74, v205\n\t"
      "v_rcp_f32 v204, v205\n\t"
      "v_and_b32 v41, s74, v40\n\t"
      "v_lshlrev_b32 v40, 16, v40\n\t"
      "v_and_b32 v43, s74, v42\n\t"
      "v_lshlrev_b32 v42, 16, v42\n\t"
      "v_and_b32 v45, s74, v44\n\t"
      "v_lshlrev_b32 v44, 16, v44\n\t"
      "v_and_b32 v47, s74, v46\n\t"
      "v_lshlrev_b32 v46, 16, v46\n\t"
      "v_pk_mul_f32 v[40:41], v[40:41], v[204:205] op_sel_hi:[1,0]\n\t"
      "v_pk_mul_f32 v[42:43], v[42:43], v[204:205] op_sel_hi:[1,0]\n\t"
      "v_pk_mul_f32 v[44:45], v[44:45], v[204:205] op_sel_hi:[1,0]\n\t"
      "v_pk_mul_f32 v[46:47], v[46:47], v[204:205] op_sel_hi:[1,0]\n\t"
      "v_cvt_pk_bf16_f32 v52, v40, v41\n\t"
      "v_cvt_pk_bf16_f32 v53, v42, v43\n\t"
      "v_cvt_pk_bf16_f32 v54, v44, v45\n\t"
      "v_cvt_pk_bf16_f32 v55, v46, v47\n\t"
      "v_and_b32 v33, s74, v52\n\t"
      "v_lshlrev_b32 v32, 16, v52\n\t"
      "v_and_b32 v35, s74, v53\n\t"
      "v_lshlrev_b32 v34, 16, v53\n\t"
      "v_and_b32 v37, s74, v54\n\t"
      "v_lshlrev_b32 v36, 16, v54\n\t"
      "v_and_b32 v39, s74, v55\n\t"
      "v_lshlrev_b32 v38, 16, v55\n\t"
      "v_pk_mul_f32 v[40:41], v[32:33], v[240:241]\n\t"
      "v_pk_fma_f32 v[40:41], v[34:35], v[242:243], v[40:41]\n\t"
      "v_pk_fma_f32 v[40:41], v[36:37], v[244:245], v[40:41]\n\t"
      "v_pk_fma_f32 v[40:41], v[38:39], v[246:247], v[40:41]\n\t"
      "v_add_f32 v40, v40, v41\n\t"
      "s_lshl_b32 s73, s49, 5\n\t"
      "s_add_u32 s73, s73, s60\n\t"
      "s_lshl_b32 s69, s73, 2\n\t"
      "s_add_u32 s69, s69, s57\n\t"
      "s_lshl_b32 s70, s73, 7\n\t"
      "s_add_u32 s70, s70, s57\n\t"
      "s_sub_u32 s70, s70, 212992\n\t"
      "s_cmp_ge_u32 s49, 56\n\t"
      "s_cselect_b32 s70, s70, s69\n\t"
      "v_add_f32_dpp v40, v40, v40 quad_perm:[1,0,3,2] row_mask:0xf bank_mask:0xf\n\t"
      "s_nop 1\n\t"
      "v_add_f32_dpp v40, v40, v40 quad_perm:[2,3,0,1] row_mask:0xf bank_mask:0xf\n\t"
      "s_nop 1\n\t"
      "v_add_f32_dpp v40, v40, v40 row_ror:4 row_mask:0xf bank_mask:0xf\n\t"
      "s_nop 1\n\t"
      "v_add_f32_dpp v40, v40, v40 row_ror:8 row_mask:0xf bank_mask:0xf\n\t"
      "v_mov_b32 v41, v40\n\t"
      "s_nop 1\n\t"
      "v_permlane16_swap_b32 v40, v41\n\t"
      "v_add_f32 v40, v40, v41\n\t"
      "v_mov_b32 v41, v40\n\t"
      "s_nop 1\n\t"
      "v_permlane32_swap_b32 v40, v41\n\t"
      "v_add_f32 v40, v40, v41\n\t"
      "v_cvt_pk_bf16_f32 v40, v40, v40\n\t"
      "v_and_b32 v40, s74, v40\n\t"
      "s_mov_b64 exec, 1\n\t"
      "buffer_store_dword v40, off, s[36:39], s70 sc1\n\t"
      "s_mov_b64 exec, -1\n\t"
      "v_pk_add_f32 v[192:193], v[192:193], v[32:33]\n\t"
      "v_pk_add_f32 v[194:195], v[194:195], v[34:35]\n\t"
      "v_pk_add_f32 v[196:197], v[196:197], v[36:37]\n\t"
      "v_pk_add_f32 v[198:199], v[198:199], v[38:39]\n\t"
      "v_readfirstlane_b32 s68, v40\n\t"
      "v_cvt_pk_bf16_f32 v56, v192, v193\n\t"
      "v_cvt_pk_bf16_f32 v57, v194, v195\n\t"
      "v_cvt_pk_bf16_f32 v58, v196, v197\n\t"
      "v_cvt_pk_bf16_f32 v59, v198, v199\n\t"
      "s_add_u32 s67, s69, 8192\n\t"
      "s_lshl_b32 s73, s73, 10\n\t"
      "s_add_u32 s69, s73, s58\n\t"
      "s_add_u32 s70, s73, s59\n\t"
      "buffer_store_dwordx2 v[56:57], v249, s[44:47], s73 offen sc1\n\t"
      "buffer_store_dwordx2 v[52:53], v249, s[36:39], s69 offen sc1\n\t"
      "buffer_store_dwordx2 v[200:201], v249, s[36:39], s70 offen sc1\n\t"
      "buffer_store_dwordx2 v[58:59], v249, s[44:47], s73 offen offset:512 sc1\n\t"
      "buffer_store_dwordx2 v[54:55], v249, s[36:39], s69 offen offset:512 sc1\n\t"
      "buffer_store_dwordx2 v[202:203], v249, s[36:39], s70 offen offset:512 sc1\n\t"
      "s_mov_b32 s49, s89\n\t"
      "s_add_u32 s89, s89, 16\n\t"
      "s_lshr_b32 s69, s89, 4\n\t"
      "s_cmp_eq_u32 s69, 3\n\t"
      "s_cselect_b32 s89, s93, s89\n\t"
      "s_cmp_ge_u32 s49, 48\n\t"
      "s_cselect_b32 s60, s94, s60\n\t"
      "s_cselect_b32 s54, s95, s54\n\t"
      "s_cmp_ge_u32 s48, 64\n\t"
      "s_cbranch_scc1 WRX%=\n\t"
      "s_cmp_lg_u32 s65, 0\n\t"
      "s_mov_b32 s65, 0\n\t"
      "s_cbranch_scc1 WGO%=\n\t"
      "s_mov_b32 s66, 0\n\t"
      "s_branch WLOOP%=\n\t"
      ".p2align 5\n\t"
      "WRX%=:\n\t"
      "s_mov_b32 s65, 0\n\t"
      "s_waitcnt vmcnt(0)\n\t"
      "s_branch WFIN1%=\n\t"
      ".p2align 5\n\t"
      "WRN%=:\n\t"
      "v_max3_u32 v40, v160, v161, v162\n\t"
      "v_max3_u32 v40, v40, v163, v164\n\t"
      "v_max3_u32 v40, v40, v165, v166\n\t"
      "v_max3_u32 v40, v40, v167, v168\n\t"
      "v_max3_u32 v40, v40, v169, v170\n\t"
      "v_max3_u32 v40, v40, v171, v172\n\t"
      "v_max3_u32 v40, v40, v173, v174\n\t"
      "v_max3_u32 v40, v40, v175, v176\n\t"
      "v_max3_u32 v40, v40, v177, v178\n\t"
      "v_max3_u32 v40, v40, v179, v180\n\t"
      "v_max3_u32 v40, v40, v181, v182\n\t"
      "v_max3_u32 v40, v40, v183, v184\n\t"
      "v_max3_u32 v40, v40, v185, v186\n\t"
      "v_max3_u32 v40, v40, v187, v188\n\t"
      "v_max3_u32 v40, v40, v189, v190\n\t"
      "v_max_u32 v40, v40, v191\n\t"
      "v_cmp_eq_u32 vcc, -1, v40\n\t"
      "s_nop 0\n\t"
      "s_cbranch_vccz WRD%=\n\t"
      "s_cmp_lg_u32 s65, 0\n\t"
      "s_cbranch_scc1 WRD%=\n\t"
      "s_sub_u32 s72, s72, 1\n\t"
      "s_cmp_gt_i32 s72, 0\n\t"
      "s_cbranch_scc1 WRL%=\n\t"
      "s_branch WRD%=\n\t"
      ".p2align 5\n\t"
      "WH2%=:\n\t"
      "s_mov_b32 s72, 0x40000\n\t"
      "WH3%=:\n\t"
      "buffer_load_dwordx2 v[0:1], off, s[36:39], s99 sc1\n\t"
      "buffer_load_dwordx2 v[2:3], off, s[36:39], s99 offset:16 sc1\n\t"
      "s_sub_u32 s72, s72, 1\n\t"
      "s_waitcnt vmcnt(0)\n\t"
      "v_bitop3_b32 v0, v0, v1, s62 bitop3:0x7e\n\t"
      "v_bitop3_b32 v2, v2, v3, s62 bitop3:0x7e\n\t"
      "v_or_b32 v0, v0, v2\n\t"
      "v_cmp_ne_u32 vcc, 0, v0\n\t"
      "s_cmp_eq_u32 s72, 0\n\t"
      "s_cbranch_scc1 WT0%=\n\t"
      "s_cbranch_vccnz WH3%=\n\t"
      "s_branch WT0%=\n\t"
      ".p2align 5\n\t"
      "WKV1%=:\n\t"
      "s_cmp_eq_u32 s66, 0\n\t"
      "s_cbranch_scc1 WB1%=\n\t"
      "WB2%=:\n\t"
      "s_mov_b32 s65, 1\n\t"
      "v_mfma_f32_16x16x32_bf16 v[0:3], v[128:131], a[160:163], 0\n\t"
      "v_mfma_f32_16x16x32_bf16 v[4:7], v[128:131], a[164:167], 0\n\t"
      "v_mfma_f32_16x16x32_bf16 v[0:3], v[132:135], a[168:171], v[0:3]\n\t"
      "v_mfma_f32_16x16x32_bf16 v[4:7], v[132:135], a[172:175], v[4:7]\n\t"
      "v_mfma_f32_16x16x32_bf16 v[0:3], v[136:139], a[176:179], v[0:3]\n\t"
      "v_mfma_f32_16x16x32_bf16 v[4:7], v[136:139], a[180:183], v[4:7]\n\t"
      "v_mfma_f32_16x16x32_bf16 v[0:3], v[140:143], a[184:187], v[0:3]\n\t"
      "v_mfma_f32_16x16x32_bf16 v[4:7], v[140:143], a[188:191], v[4:7]\n\t"
      "s_add_u32 s51, s51, 262144\n\t"
      "s_add_u32 s52, s52, 262144\n\t"
      "s_lshl_b32 s70, s49, 5\n\t"
      "s_add_u32 s70, s70, s60\n\t"
      "s_lshl_b32 s70, s70, 10\n\t"
      "buffer_load_dwordx2 v[224:225], v249, s[40:43], s70 offen \n\t"
      "s_nop 0\n\t"
      "v_max3_f32 v8, v0, v1, v2\n\t"
      "v_max3_f32 v8, v8, v3, v234\n\t"
      "v_max3_f32 v9, v4, v5, v6\n\t"
      "v_max3_f32 v9, v9, v7, v235\n\t"
      "s_nop 1\n\t"
      "v_permlane16_swap_b32 v8, v9\n\t"
      "v_max_f32 v8, v8, v9\n\t"
      "buffer_load_dwordx2 v[226:227], v249, s[40:43], s70 offen offset:512 \n\t"
      "v_mov_b32 v9, v8\n\t"
      "s_nop 1\n\t"
      "v_permlane16_swap_b32 v8, v9\n\t"
      "v_pk_mul_f32 v[8:9], v[8:9], s[86:87]\n\t"
      "v_pk_fma_f32 v[10:11], v[234:235], s[86:87], v[8:9] neg_lo:[0,0,1] neg_hi:[0,0,1]\n\t"
      "v_pk_fma_f32 v[0:1], v[0:1], s[86:87], v[8:9] op_sel:[0,0,0] op_sel_hi:[1,1,0] neg_lo:[0,0,1] neg_hi:[0,0,1]\n\t"
      "v_pk_fma_f32 v[2:3], v[2:3], s[86:87], v[8:9] op_sel:[0,0,0] op_sel_hi:[1,1,0] neg_lo:[0,0,1] neg_hi:[0,0,1]\n\t"
      "s_lshl_b32 s69, s49, 18\n\t"
      "s_add_u32 s69, s69, s54\n\t"
      "buffer_load_dwordx4 v[160:163], v248, s[36:39], s69 offen sc1\n\t"
      "v_pk_fma_f32 v[4:5], v[4:5], s[86:87], v[8:9] op_sel:[0,0,1] op_sel_hi:[1,1,1] neg_lo:[0,0,1] neg_hi:[0,0,1]\n\t"
      "v_pk_fma_f32 v[6:7], v[6:7], s[86:87], v[8:9] op_sel:[0,0,1] op_sel_hi:[1,1,1] neg_lo:[0,0,1] neg_hi:[0,0,1]\n\t"
      "v_exp_f32 v10, v10\n\t"
      "v_exp_f32 v11, v11\n\t"
      "s_add_u32 s69, s69, 0x8000\n\t"
      "buffer_load_dwordx4 v[164:167], v248, s[36:39], s69 offen sc1\n\t"
      "v_exp_f32 v0, v0\n\t"
      "v_exp_f32 v1, v1\n\t"
      "v_exp_f32 v2, v2\n\t"
      "v_exp_f32 v3, v3\n\t"
      "s_add_u32 s69, s69, 0x8000\n\t"
      "buffer_load_dwordx4 v[168:171], v248, s[36:39], s69 offen sc1\n\t"
      "v_exp_f32 v4, v4\n\t"
      "v_exp_f32 v5, v5\n\t"
      "v_exp_f32 v6, v6\n\t"
      "s_add_u32 s69, s69, 0x8000\n\t"
      "buffer_load_dwordx4 v[172:175], v248, s[36:39], s69 offen sc1\n\t"
      "v_exp_f32 v7, v7\n\t"
      "v_pk_mul_f32 v[12:13], v[236:237], v[10:11]\n\t"
      "v_pk_add_f32 v[14:15], v[0:1], v[2:3]\n\t"
      "v_pk_add_f32 v[16:17], v[4:5], v[6:7]\n\t"
      "s_add_u32 s69, s69, 0x8000\n\t"
      "buffer_load_dwordx4 v[176:179], v248, s[36:39], s69 offen sc1\n\t"
      "v_add_f32 v14, v14, v15\n\t"
      "v_add_f32 v15, v16, v17\n\t"
      "v_fma_f32 v12, v14, 0.5, v12\n\t"
      "v_fma_f32 v13, v15, 0.5, v13\n\t"
      "s_nop 1\n\t"
      "v_permlane16_swap_b32 v12, v13\n\t"
      "v_add_f32 v12, v12, v13\n\t"
      "s_add_u32 s69, s69, 0x8000\n\t"
      "buffer_load_dwordx4 v[180:183], v248, s[36:39], s69 offen sc1\n\t"
      "v_mov_b32 v13, v12\n\t"
      "s_nop 1\n\t"
      "v_permlane32_swap_b32 v12, v13\n\t"
      "v_add_f32 v12, v12, v13\n\t"
      "v_mov_b32 v13, v12\n\t"
      "s_nop 1\n\t"
      "v_permlane16_swap_b32 v12, v13\n\t"
      "v_rcp_f32 v12, v12\n\t"
      "s_add_u32 s69, s69, 0x8000\n\t"
      "buffer_load_dwordx4 v[184:187], v248, s[36:39], s69 offen sc1\n\t"
      "v_rcp_f32 v13, v13\n\t"
      "s_nop 0\n\t"
      "v_pk_mul_f32 v[10:11], v[10:11], v[12:13]\n\t"
      "v_pk_mul_f32 v[12:13], v[12:13], 0.5 op_sel_hi:[1,0]\n\t"
      "v_pk_mul_f32 v[0:1], v[0:1], v[12:13] op_sel:[0,0] op_sel_hi:[1,0]\n\t"
      "s_add_u32 s69, s69, 0x8000\n\t"
      "buffer_load_dwordx4 v[188:191], v248, s[36:39], s69 offen sc1\n\t"
      "v_pk_mul_f32 v[2:3], v[2:3], v[12:13] op_sel:[0,0] op_sel_hi:[1,0]\n\t"
      "v_pk_mul_f32 v[4:5], v[4:5], v[12:13] op_sel:[0,1] op_sel_hi:[1,1]\n\t"
      "v_pk_mul_f32 v[6:7], v[6:7], v[12:13] op_sel:[0,1] op_sel_hi:[1,1]\n\t"
      "v_cvt_pk_bf16_f32 v16, v0, v1\n\t"
      "v_cvt_pk_bf16_f32 v17, v2, v3\n\t"
      "v_cvt_pk_bf16_f32 v18, v4, v5\n\t"
      "s_add_u32 s69, s50, 8192\n\t"
      "s_add_u32 m0, s61, 0\n\t"
      "s_nop 0\n\t"
      "buffer_load_dwordx4 v250, s[36:39], s69 offen sc1 lds\n\t"
      "v_cvt_pk_bf16_f32 v19, v6, v7\n\t"
      "v_pk_mul_f32 v[64:65], v[64:65], v[10:11] op_sel:[0,0] op_sel_hi:[1,0]\n\t"
      "v_pk_mul_f32 v[66:67], v[66:67], v[10:11] op_sel:[0,0] op_sel_hi:[1,0]\n\t"
      "v_pk_mul_f32 v[68:69], v[68:69], v[10:11] op_sel:[0,0] op_sel_hi:[1,0]\n\t"
      "v_pk_mul_f32 v[70:71], v[70:71], v[10:11] op_sel:[0,0] op_sel_hi:[1,0]\n\t"
      "v_mfma_f32_16x16x16_bf16 v[64:67], v[144:145], v[16:17], v[64:67]\n\t"
      "buffer_load_dwordx4 v250, s[36:39], s69 offen offset:1024 sc1 lds\n\t"
      "v_pk_mul_f32 v[72:73], v[72:73], v[10:11] op_sel:[0,0] op_sel_hi:[1,0]\n\t"
      "v_pk_mul_f32 v[74:75], v[74:75], v[10:11] op_sel:[0,0] op_sel_hi:[1,0]\n\t"
      "v_mfma_f32_16x16x16_bf16 v[68:71], v[146:147], v[16:17], v[68:71]\n\t"
      "v_pk_mul_f32 v[76:77], v[76:77], v[10:11] op_sel:[0,0] op_sel_hi:[1,0]\n\t"
      "buffer_load_dwordx4 v250, s[36:39], s69 offen offset:2048 sc1 lds\n\t"
      "v_pk_mul_f32 v[78:79], v[78:79], v[10:11] op_sel:[0,0] op_sel_hi:[1,0]\n\t"
      "v_mfma_f32_16x16x16_bf16 v[72:75], v[148:149], v[16:17], v[72:75]\n\t"
      "v_pk_mul_f32 v[80:81], v[80:81], v[10:11] op_sel:[0,0] op_sel_hi:[1,0]\n\t"
      "v_pk_mul_f32 v[82:83], v[82:83], v[10:11] op_sel:[0,0] op_sel_hi:[1,0]\n\t"
      "v_mfma_f32_16x16x16_bf16 v[76:79], v[150:151], v[16:17], v[76:79]\n\t"
      "buffer_load_dwordx4 v250, s[36:39], s69 offen offset:3072 sc1 lds\n\t"
      "v_pk_mul_f32 v[84:85], v[84:85], v[10:11] op_sel:[0,0] op_sel_hi:[1,0]\n\t"
      "v_pk_mul_f32 v[86:87], v[86:87], v[10:11] op_sel:[0,0] op_sel_hi:[1,0]\n\t"
      "v_mfma_f32_16x16x16_bf16 v[80:83], v[152:153], v[16:17], v[80:83]\n\t"
      "v_pk_mul_f32 v[88:89], v[88:89], v[10:11] op_sel:[0,0] op_sel_hi:[1,0]\n\t"
      "s_add_u32 s69, s50, 12288\n\t"
      "s_add_u32 m0, s61, 4096\n\t"
      "s_nop 0\n\t"
      "buffer_load_dwordx4 v250, s[36:39], s69 offen sc1 lds\n\t"
      "v_pk_mul_f32 v[90:91], v[90:91], v[10:11] op_sel:[0,0] op_sel_hi:[1,0]\n\t"
      "v_mfma_f32_16x16x16_bf16 v[84:87], v[154:155], v[16:17], v[84:87]\n\t"
      "v_pk_mul_f32 v[92:93], v[92:93], v[10:11] op_sel:[0,0] op_sel_hi:[1,0]\n\t"
      "v_pk_mul_f32 v[94:95], v[94:95], v[10:11] op_sel:[0,0] op_sel_hi:[1,0]\n\t"
      "v_cvt_pk_bf16_f32 v24, v64, v65\n\t"
      "buffer_load_dwordx4 v250, s[36:39], s69 offen offset:1024 sc1 lds\n\t"
      "v_cvt_pk_bf16_f32 v25, v66, v67\n\t"
      "v_cvt_pk_bf16_f32 v26, v68, v69\n\t"
      "v_cvt_pk_bf16_f32 v27, v70, v71\n\t"
      "ds_write_b128 v254, v[24:27]\n\t"
      "v_mfma_f32_16x16x16_bf16 v[88:91], v[156:157], v[16:17], v[88:91]\n\t"
      "buffer_load_dwordx4 v250, s[36:39], s69 offen offset:2048 sc1 lds\n\t"
      "v_pk_mul_f32 v[96:97], v[96:97], v[10:11] op_sel:[0,1] op_sel_hi:[1,1]\n\t"
      "v_pk_mul_f32 v[98:99], v[98:99], v[10:11] op_sel:[0,1] op_sel_hi:[1,1]\n\t"
      "v_mfma_f32_16x16x16_bf16 v[92:95], v[158:159], v[16:17], v[92:95]\n\t"
      "v_pk_mul_f32 v[100:101], v[100:101], v[10:11] op_sel:[0,1] op_sel_hi:[1,1]\n\t"
      "v_pk_mul_f32 v[102:103], v[102:103], v[10:11] op_sel:[0,1] op_sel_hi:[1,1]\n\t"
      "buffer_load_dwordx4 v250, s[36:39], s69 offen offset:3072 sc1 lds\n\t"
      "v_cvt_pk_bf16_f32 v28, v72, v73\n\t"
      "v_cvt_pk_bf16_f32 v29, v74, v75\n\t"
      "v_cvt_pk_bf16_f32 v30, v76, v77\n\t"
      "v_cvt_pk_bf16_f32 v31, v78, v79\n\t"
      "ds_write_b128 v254, v[28:31] offset:2048\n\t"
      "v_mfma_f32_16x16x16_bf16 v[96:99], v[144:145], v[18:19], v[96:99]\n\t"
      "s_add_u32 s69, s50, 16384\n\t"
      "buffer_load_dwordx4 a[160:163], v248, s[36:39], s69 offen  sc1\n\t"
      "v_pk_mul_f32 v[104:105], v[104:105], v[10:11] op_sel:[0,1] op_sel_hi:[1,1]\n\t"
      "v_pk_mul_f32 v[106:107], v[106:107], v[10:11] op_sel:[0,1] op_sel_hi:[1,1]\n\t"
      "v_mfma_f32_16x16x16_bf16 v[100:103], v[146:147], v[18:19], v[100:103]\n\t"
      "v_pk_mul_f32 v[108:109], v[108:109], v[10:11] op_sel:[0,1] op_sel_hi:[1,1]\n\t"
      "buffer_load_dwordx4 a[164:167], v248, s[36:39], s69 offen offset:1024  sc1\n\t"
      "v_pk_mul_f32 v[110:111], v[110:111], v[10:11] op_sel:[0,1] op_sel_hi:[1,1]\n\t"
      "v_cvt_pk_bf16_f32 v24, v80, v81\n\t"
      "v_cvt_pk_bf16_f32 v25, v82, v83\n\t"
      "v_cvt_pk_bf16_f32 v26, v84, v85\n\t"
      "v_cvt_pk_bf16_f32 v27, v86, v87\n\t"
      "ds_write_b128 v254, v[24:27] offset:4096\n\t"
      "buffer_load_dwordx4 a[168:171], v248, s[36:39], s69 offen offset:2048  sc1\n\t"
      "v_mfma_f32_16x16x16_bf16 v[104:107], v[148:149], v[18:19], v[104:107]\n\t"
      "v_pk_mul_f32 v[112:113], v[112:113], v[10:11] op_sel:[0,1] op_sel_hi:[1,1]\n\t"
      "v_pk_mul_f32 v[114:115], v[114:115], v[10:11] op_sel:[0,1] op_sel_hi:[1,1]\n\t"
      "v_mfma_f32_16x16x16_bf16 v[108:111], v[150:151], v[18:19], v[108:111]\n\t"
      "buffer_load_dwordx4 a[172:175], v248, s[36:39], s69 offen offset:3072  sc1\n\t"
      "v_pk_mul_f32 v[116:117], v[116:117], v[10:11] op_sel:[0,1] op_sel_hi:[1,1]\n\t"
      "v_pk_mul_f32 v[118:119], v[118:119], v[10:11] op_sel:[0,1] op_sel_hi:[1,1]\n\t"
      "v_cvt_pk_bf16_f32 v28, v88, v89\n\t"
      "v_cvt_pk_bf16_f32 v29, v90, v91\n\t"
      "v_cvt_pk_bf16_f32 v30, v92, v93\n\t"
      "v_cvt_pk_bf16_f32 v31, v94, v95\n\t"
      "s_add_u32 s69, s50, 20480\n\t"
      "buffer_load_dwordx4 a[176:179], v248, s[36:39], s69 offen  sc1\n\t"
      "ds_write_b128 v254, v[28:31] offset:6144\n\t"
      "v_mfma_f32_16x16x16_bf16 v[112:115], v[152:153], v[18:19], v[112:115]\n\t"
      "v_pk_mul_f32 v[120:121], v[120:121], v[10:11] op_sel:[0,1] op_sel_hi:[1,1]\n\t"
      "v_pk_mul_f32 v[122:123], v[122:123], v[10:11] op_sel:[0,1] op_sel_hi:[1,1]\n\t"
      "v_mfma_f32_16x16x16_bf16 v[116:119], v[154:155], v[18:19], v[116:119]\n\t"
      "buffer_load_dwordx4 a[180:183], v248, s[36:39], s69 offen offset:1024  sc1\n\t"
      "v_pk_mul_f32 v[124:125], v[124:125], v[10:11] op_sel:[0,1] op_sel_hi:[1,1]\n\t"
      "v_pk_mul_f32 v[126:127], v[126:127], v[10:11] op_sel:[0,1] op_sel_hi:[1,1]\n\t"
      "v_cvt_pk_bf16_f32 v24, v96, v97\n\t"
      "v_cvt_pk_bf16_f32 v25, v98, v99\n\t"
      "v_cvt_pk_bf16_f32 v26, v100, v101\n\t"
      "buffer_load_dwordx4 a[184:187], v248, s[36:39], s69 offen offset:2048  sc1\n\t"
      "v_cvt_pk_bf16_f32 v27, v102, v103\n\t"
      "ds_write_b128 v254, v[24:27] offset:1024\n\t"
      "v_mfma_f32_16x16x16_bf16 v[120:123], v[156:157], v[18:19], v[120:123]\n\t"
      "v_mfma_f32_16x16x16_bf16 v[124:127], v[158:159], v[18:19], v[124:127]\n\t"
      "buffer_load_dwordx4 a[188:191], v248, s[36:39], s69 offen offset:3072  sc1\n\t"
      "v_cvt_pk_bf16_f32 v28, v104, v105\n\t"
      "v_cvt_pk_bf16_f32 v29, v106, v107\n\t"
      "v_cvt_pk_bf16_f32 v30, v108, v109\n\t"
      "v_cvt_pk_bf16_f32 v31, v110, v111\n\t"
      "ds_write_b128 v254, v[28:31] offset:3072\n\t"
      "v_cvt_pk_bf16_f32 v24, v112, v113\n\t"
      "v_cvt_pk_bf16_f32 v25, v114, v115\n\t"
      "v_cvt_pk_bf16_f32 v26, v116, v117\n\t"
      "v_cvt_pk_bf16_f32 v27, v118, v119\n\t"
      "ds_write_b128 v254, v[24:27] offset:5120\n\t"
      "v_cvt_pk_bf16_f32 v28, v120, v121\n\t"
      "v_cvt_pk_bf16_f32 v29, v122, v123\n\t"
      "v_cvt_pk_bf16_f32 v30, v124, v125\n\t"
      "v_cvt_pk_bf16_f32 v31, v126, v127\n\t"
      "ds_write_b128 v254, v[28:31] offset:7168\n\t"
      "s_waitcnt vmcnt(40) lgkmcnt(0)\n\t"
      "s_barrier\n\t"
      "s_branch WO%=\n\t"
      ".p2align 5\n\t"
      "WB1%=:\n\t"
      "v_mfma_f32_16x16x32_bf16 v[0:3], v[128:131], a[160:163], 0\n\t"
      "v_mfma_f32_16x16x32_bf16 v[4:7], v[128:131], a[164:167], 0\n\t"
      "v_mfma_f32_16x16x32_bf16 v[0:3], v[132:135], a[168:171], v[0:3]\n\t"
      "v_mfma_f32_16x16x32_bf16 v[4:7], v[132:135], a[172:175], v[4:7]\n\t"
      "v_mfma_f32_16x16x32_bf16 v[0:3], v[136:139], a[176:179], v[0:3]\n\t"
      "v_mfma_f32_16x16x32_bf16 v[4:7], v[136:139], a[180:183], v[4:7]\n\t"
      "v_mfma_f32_16x16x32_bf16 v[0:3], v[140:143], a[184:187], v[0:3]\n\t"
      "v_mfma_f32_16x16x32_bf16 v[4:7], v[140:143], a[188:191], v[4:7]\n\t"
      "s_add_u32 s51, s51, 262144\n\t"
      "s_add_u32 s52, s52, 262144\n\t"
      "s_add_u32 s69, s50, 8192\n\t"
      "s_add_u32 m0, s61, 0\n\t"
      "s_nop 0\n\t"
      "buffer_load_dwordx4 v250, s[36:39], s69 offen sc1 lds\n\t"
      "s_nop 0\n\t"
      "v_max3_f32 v8, v0, v1, v2\n\t"
      "v_max3_f32 v8, v8, v3, v234\n\t"
      "v_max3_f32 v9, v4, v5, v6\n\t"
      "v_max3_f32 v9, v9, v7, v235\n\t"
      "s_nop 1\n\t"
      "v_permlane16_swap_b32 v8, v9\n\t"
      "v_max_f32 v8, v8, v9\n\t"
      "v_mov_b32 v9, v8\n\t"
      "s_nop 1\n\t"
      "v_permlane16_swap_b32 v8, v9\n\t"
      "v_pk_mul_f32 v[8:9], v[8:9], s[86:87]\n\t"
      "v_pk_fma_f32 v[10:11], v[234:235], s[86:87], v[8:9] neg_lo:[0,0,1] neg_hi:[0,0,1]\n\t"
      "buffer_load_dwordx4 v250, s[36:39], s69 offen offset:1024 sc1 lds\n\t"
      "v_pk_fma_f32 v[0:1], v[0:1], s[86:87], v[8:9] op_sel:[0,0,0] op_sel_hi:[1,1,0] neg_lo:[0,0,1] neg_hi:[0,0,1]\n\t"
      "v_pk_fma_f32 v[2:3], v[2:3], s[86:87], v[8:9] op_sel:[0,0,0] op_sel_hi:[1,1,0] neg_lo:[0,0,1] neg_hi:[0,0,1]\n\t"
      "v_pk_fma_f32 v[4:5], v[4:5], s[86:87], v[8:9] op_sel:[0,0,1] op_sel_hi:[1,1,1] neg_lo:[0,0,1] neg_hi:[0,0,1]\n\t"
      "v_pk_fma_f32 v[6:7], v[6:7], s[86:87], v[8:9] op_sel:[0,0,1] op_sel_hi:[1,1,1] neg_lo:[0,0,1] neg_hi:[0,0,1]\n\t"
      "v_exp_f32 v10, v10\n\t"
      "v_exp_f32 v11, v11\n\t"
      "v_exp_f32 v0, v0\n\t"
      "buffer_load_dwordx4 v250, s[36:39], s69 offen offset:2048 sc1 lds\n\t"
      "v_exp_f32 v1, v1\n\t"
      "v_exp_f32 v2, v2\n\t"
      "v_exp_f32 v3, v3\n\t"
      "v_exp_f32 v4, v4\n\t"
      "v_exp_f32 v5, v5\n\t"
      "buffer_load_dwordx4 v250, s[36:39], s69 offen offset:3072 sc1 lds\n\t"
      "v_exp_f32 v6, v6\n\t"
      "v_exp_f32 v7, v7\n\t"
      "v_pk_mul_f32 v[12:13], v[236:237], v[10:11]\n\t"
      "v_pk_add_f32 v[14:15], v[0:1], v[2:3]\n\t"
      "v_pk_add_f32 v[16:17], v[4:5], v[6:7]\n\t"
      "v_add_f32 v14, v14, v15\n\t"
      "v_add_f32 v15, v16, v17\n\t"
      "v_fma_f32 v12, v14, 0.5, v12\n\t"
      "s_add_u32 s69, s50, 12288\n\t"
      "s_add_u32 m0, s61, 4096\n\t"
      "s_nop 0\n\t"
      "buffer_load_dwordx4 v250, s[36:39], s69 offen sc1 lds\n\t"
      "v_fma_f32 v13, v15, 0.5, v13\n\t"
      "s_nop 1\n\t"
      "v_permlane16_swap_b32 v12, v13\n\t"
      "v_add_f32 v12, v12, v13\n\t"
      "v_mov_b32 v13, v12\n\t"
      "s_nop 1\n\t"
      "v_permlane32_swap_b32 v12, v13\n\t"
      "v_add_f32 v12, v12, v13\n\t"
      "v_mov_b32 v13, v12\n\t"
      "s_nop 1\n\t"
      "v_permlane16_swap_b32 v12, v13\n\t"
      "v_rcp_f32 v12, v12\n\t"
      "buffer_load_dwordx4 v250, s[36:39], s69 offen offset:1024 sc1 lds\n\t"
      "v_rcp_f32 v13, v13\n\t"
      "s_nop 0\n\t"
      "v_pk_mul_f32 v[10:11], v[10:11], v[12:13]\n\t"
      "v_pk_mul_f32 v[12:13], v[12:13], 0.5 op_sel_hi:[1,0]\n\t"
      "v_pk_mul_f32 v[0:1], v[0:1], v[12:13] op_sel:[0,0] op_sel_hi:[1,0]\n\t"
      "v_pk_mul_f32 v[2:3], v[2:3], v[12:13] op_sel:[0,0] op_sel_hi:[1,0]\n\t"
      "v_pk_mul_f32 v[4:5], v[4:5], v[12:13] op_sel:[0,1] op_sel_hi:[1,1]\n\t"
      "v_pk_mul_f32 v[6:7], v[6:7], v[12:13] op_sel:[0,1] op_sel_hi:[1,1]\n\t"
      "v_cvt_pk_bf16_f32 v16, v0, v1\n\t"
      "buffer_load_dwordx4 v250, s[36:39], s69 offen offset:2048 sc1 lds\n\t"
      "v_cvt_pk_bf16_f32 v17, v2, v3\n\t"
      "v_cvt_pk_bf16_f32 v18, v4, v5\n\t"
      "v_cvt_pk_bf16_f32 v19, v6, v7\n\t"
      "v_pk_mul_f32 v[64:65], v[64:65], v[10:11] op_sel:[0,0] op_sel_hi:[1,0]\n\t"
      "v_pk_mul_f32 v[66:67], v[66:67], v[10:11] op_sel:[0,0] op_sel_hi:[1,0]\n\t"
      "v_pk_mul_f32 v[68:69], v[68:69], v[10:11] op_sel:[0,0] op_sel_hi:[1,0]\n\t"
      "v_pk_mul_f32 v[70:71], v[70:71], v[10:11] op_sel:[0,0] op_sel_hi:[1,0]\n\t"
      "v_mfma_f32_16x16x16_bf16 v[64:67], v[144:145], v[16:17], v[64:67]\n\t"
      "v_pk_mul_f32 v[72:73], v[72:73], v[10:11] op_sel:[0,0] op_sel_hi:[1,0]\n\t"
      "buffer_load_dwordx4 v250, s[36:39], s69 offen offset:3072 sc1 lds\n\t"
      "v_pk_mul_f32 v[74:75], v[74:75], v[10:11] op_sel:[0,0] op_sel_hi:[1,0]\n\t"
      "v_mfma_f32_16x16x16_bf16 v[68:71], v[146:147], v[16:17], v[68:71]\n\t"
      "v_pk_mul_f32 v[76:77], v[76:77], v[10:11] op_sel:[0,0] op_sel_hi:[1,0]\n\t"
      "v_pk_mul_f32 v[78:79], v[78:79], v[10:11] op_sel:[0,0] op_sel_hi:[1,0]\n\t"
      "v_mfma_f32_16x16x16_bf16 v[72:75], v[148:149], v[16:17], v[72:75]\n\t"
      "v_pk_mul_f32 v[80:81], v[80:81], v[10:11] op_sel:[0,0] op_sel_hi:[1,0]\n\t"
      "v_pk_mul_f32 v[82:83], v[82:83], v[10:11] op_sel:[0,0] op_sel_hi:[1,0]\n\t"
      "v_mfma_f32_16x16x16_bf16 v[76:79], v[150:151], v[16:17], v[76:79]\n\t"
      "s_add_u32 s69, s50, 16384\n\t"
      "buffer_load_dwordx4 a[160:163], v248, s[36:39], s69 offen  sc1\n\t"
      "v_pk_mul_f32 v[84:85], v[84:85], v[10:11] op_sel:[0,0] op_sel_hi:[1,0]\n\t"
      "v_pk_mul_f32 v[86:87], v[86:87], v[10:11] op_sel:[0,0] op_sel_hi:[1,0]\n\t"
      "v_mfma_f32_16x16x16_bf16 v[80:83], v[152:153], v[16:17], v[80:83]\n\t"
      "v_pk_mul_f32 v[88:89], v[88:89], v[10:11] op_sel:[0,0] op_sel_hi:[1,0]\n\t"
      "v_pk_mul_f32 v[90:91], v[90:91], v[10:11] op_sel:[0,0] op_sel_hi:[1,0]\n\t"
      "v_mfma_f32_16x16x16_bf16 v[84:87], v[154:155], v[16:17], v[84:87]\n\t"
      "v_pk_mul_f32 v[92:93], v[92:93], v[10:11] op_sel:[0,0] op_sel_hi:[1,0]\n\t"
      "buffer_load_dwordx4 a[164:167], v248, s[36:39], s69 offen offset:1024  sc1\n\t"
      "v_pk_mul_f32 v[94:95], v[94:95], v[10:11] op_sel:[0,0] op_sel_hi:[1,0]\n\t"
      "v_cvt_pk_bf16_f32 v24, v64, v65\n\t"
      "v_cvt_pk_bf16_f32 v25, v66, v67\n\t"
      "v_cvt_pk_bf16_f32 v26, v68, v69\n\t"
      "v_cvt_pk_bf16_f32 v27, v70, v71\n\t"
      "ds_write_b128 v254, v[24:27]\n\t"
      "v_mfma_f32_16x16x16_bf16 v[88:91], v[156:157], v[16:17], v[88:91]\n\t"
      "v_pk_mul_f32 v[96:97], v[96:97], v[10:11] op_sel:[0,1] op_sel_hi:[1,1]\n\t"
      "buffer_load_dwordx4 a[168:171], v248, s[36:39], s69 offen offset:2048  sc1\n\t"
      "v_pk_mul_f32 v[98:99], v[98:99], v[10:11] op_sel:[0,1] op_sel_hi:[1,1]\n\t"
      "v_mfma_f32_16x16x16_bf16 v[92:95], v[158:159], v[16:17], v[92:95]\n\t"
      "v_pk_mul_f32 v[100:101], v[100:101], v[10:11] op_sel:[0,1] op_sel_hi:[1,1]\n\t"
      "v_pk_mul_f32 v[102:103], v[102:103], v[10:11] op_sel:[0,1] op_sel_hi:[1,1]\n\t"
      "v_cvt_pk_bf16_f32 v28, v72, v73\n\t"
      "v_cvt_pk_bf16_f32 v29, v74, v75\n\t"
      "v_cvt_pk_bf16_f32 v30, v76, v77\n\t"
      "v_cvt_pk_bf16_f32 v31, v78, v79\n\t"
      "ds_write_b128 v254, v[28:31] offset:2048\n\t"
      "buffer_load_dwordx4 a[172:175], v248, s[36:39], s69 offen offset:3072  sc1\n\t"
      "v_mfma_f32_16x16x16_bf16 v[96:99], v[144:145], v[18:19], v[96:99]\n\t"
      "v_pk_mul_f32 v[104:105], v[104:105], v[10:11] op_sel:[0,1] op_sel_hi:[1,1]\n\t"
      "v_pk_mul_f32 v[106:107], v[106:107], v[10:11] op_sel:[0,1] op_sel_hi:[1,1]\n\t"
      "v_mfma_f32_16x16x16_bf16 v[100:103], v[146:147], v[18:19], v[100:103]\n\t"
      "v_pk_mul_f32 v[108:109], v[108:109], v[10:11] op_sel:[0,1] op_sel_hi:[1,1]\n\t"
      "v_pk_mul_f32 v[110:111], v[110:111], v[10:11] op_sel:[0,1] op_sel_hi:[1,1]\n\t"
      "v_cvt_pk_bf16_f32 v24, v80, v81\n\t"
      "v_cvt_pk_bf16_f32 v25, v82, v83\n\t"
      "s_add_u32 s69, s50, 20480\n\t"
      "buffer_load_dwordx4 a[176:179], v248, s[36:39], s69 offen  sc1\n\t"
      "v_cvt_pk_bf16_f32 v26, v84, v85\n\t"
      "v_cvt_pk_bf16_f32 v27, v86, v87\n\t"
      "ds_write_b128 v254, v[24:27] offset:4096\n\t"
      "v_mfma_f32_16x16x16_bf16 v[104:107], v[148:149], v[18:19], v[104:107]\n\t"
      "v_pk_mul_f32 v[112:113], v[112:113], v[10:11] op_sel:[0,1] op_sel_hi:[1,1]\n\t"
      "v_pk_mul_f32 v[114:115], v[114:115], v[10:11] op_sel:[0,1] op_sel_hi:[1,1]\n\t"
      "v_mfma_f32_16x16x16_bf16 v[108:111], v[150:151], v[18:19], v[108:111]\n\t"
      "buffer_load_dwordx4 a[180:183], v248, s[36:39], s69 offen offset:1024  sc1\n\t"
      "v_pk_mul_f32 v[116:117], v[116:117], v[10:11] op_sel:[0,1] op_sel_hi:[1,1]\n\t"
      "v_pk_mul_f32 v[118:119], v[118:119], v[10:11] op_sel:[0,1] op_sel_hi:[1,1]\n\t"
      "v_cvt_pk_bf16_f32 v28, v88, v89\n\t"
      "v_cvt_pk_bf16_f32 v29, v90, v91\n\t"
      "v_cvt_pk_bf16_f32 v30, v92, v93\n\t"
      "v_cvt_pk_bf16_f32 v31, v94, v95\n\t"
      "ds_write_b128 v254, v[28:31] offset:6144\n\t"
      "v_mfma_f32_16x16x16_bf16 v[112:115], v[152:153], v[18:19], v[112:115]\n\t"
      "v_pk_mul_f32 v[120:121], v[120:121], v[10:11] op_sel:[0,1] op_sel_hi:[1,1]\n\t"
      "buffer_load_dwordx4 a[184:187], v248, s[36:39], s69 offen offset:2048  sc1\n\t"
      "v_pk_mul_f32 v[122:123], v[122:123], v[10:11] op_sel:[0,1] op_sel_hi:[1,1]\n\t"
      "v_mfma_f32_16x16x16_bf16 v[116:119], v[154:155], v[18:19], v[116:119]\n\t"
      "v_pk_mul_f32 v[124:125], v[124:125], v[10:11] op_sel:[0,1] op_sel_hi:[1,1]\n\t"
      "v_pk_mul_f32 v[126:127], v[126:127], v[10:11] op_sel:[0,1] op_sel_hi:[1,1]\n\t"
      "v_cvt_pk_bf16_f32 v24, v96, v97\n\t"
      "v_cvt_pk_bf16_f32 v25, v98, v99\n\t"
      "v_cvt_pk_bf16_f32 v26, v100, v101\n\t"
      "v_cvt_pk_bf16_f32 v27, v102, v103\n\t"
      "ds_write_b128 v254, v[24:27] offset:1024\n\t"
      "buffer_load_dwordx4 a[188:191], v248, s[36:39], s69 offen offset:3072  sc1\n\t"
      "v_mfma_f32_16x16x16_bf16 v[120:123], v[156:157], v[18:19], v[120:123]\n\t"
      "v_mfma_f32_16x16x16_bf16 v[124:127], v[158:159], v[18:19], v[124:127]\n\t"
      "v_cvt_pk_bf16_f32 v28, v104, v105\n\t"
      "v_cvt_pk_bf16_f32 v29, v106, v107\n\t"
      "v_cvt_pk_bf16_f32 v30, v108, v109\n\t"
      "v_cvt_pk_bf16_f32 v31, v110, v111\n\t"
      "ds_write_b128 v254, v[28:31] offset:3072\n\t"
      "v_cvt_pk_bf16_f32 v24, v112, v113\n\t"
      "v_cvt_pk_bf16_f32 v25, v114, v115\n\t"
      "v_cvt_pk_bf16_f32 v26, v116, v117\n\t"
      "v_cvt_pk_bf16_f32 v27, v118, v119\n\t"
      "ds_write_b128 v254, v[24:27] offset:5120\n\t"
      "v_cvt_pk_bf16_f32 v28, v120, v121\n\t"
      "v_cvt_pk_bf16_f32 v29, v122, v123\n\t"
      "v_cvt_pk_bf16_f32 v30, v124, v125\n\t"
      "v_cvt_pk_bf16_f32 v31, v126, v127\n\t"
      "ds_write_b128 v254, v[28:31] offset:7168\n\t"
      "s_waitcnt vmcnt(40) lgkmcnt(0)\n\t"
      "s_barrier\n\t"
      "s_branch WO%=\n\t"
      ".p2align 5\n\t"
      "WOL1%=:\n\t"
      "buffer_load_dwordx4 v[128:131], v252, s[36:39], s51 offen sc1\n\t"
      "s_waitcnt lgkmcnt(11)\n\t"
      "v_mfma_f32_16x16x32_bf16 v[0:3], a[0:3], v[64:67], 0\n\t"
      "v_mfma_f32_16x16x32_bf16 v[4:7], a[64:67], v[64:67], 0\n\t"
      "ds_read_b128 v[112:115], v255 offset:12288\n\t"
      "s_waitcnt lgkmcnt(11)\n\t"
      "v_mfma_f32_16x16x32_bf16 v[8:11], a[0:3], v[68:71], 0\n\t"
      "v_mfma_f32_16x16x32_bf16 v[12:15], a[64:67], v[68:71], 0\n\t"
      "ds_read_b128 v[116:119], v255 offset:13312\n\t"
      "s_waitcnt lgkmcnt(11)\n\t"
      "v_mfma_f32_16x16x32_bf16 v[0:3], a[4:7], v[72:75], v[0:3]\n\t"
      "v_mfma_f32_16x16x32_bf16 v[4:7], a[68:71], v[72:75], v[4:7]\n\t"
      "ds_read_b128 v[120:123], v255 offset:14336\n\t"
      "s_waitcnt lgkmcnt(11)\n\t"
      "v_mfma_f32_16x16x32_bf16 v[8:11], a[4:7], v[76:79], v[8:11]\n\t"
      "v_mfma_f32_16x16x32_bf16 v[12:15], a[68:71], v[76:79], v[12:15]\n\t"
      "buffer_load_dwordx4 v[132:135], v252, s[36:39], s51 offen offset:512 sc1\n\t"
      "ds_read_b128 v[124:127], v255 offset:15360\n\t"
      "s_waitcnt lgkmcnt(11)\n\t"
      "v_mfma_f32_16x16x32_bf16 v[0:3], a[8:11], v[80:83], v[0:3]\n\t"
      "v_mfma_f32_16x16x32_bf16 v[4:7], a[72:75], v[80:83], v[4:7]\n\t"
      "ds_read_b128 v[64:67], v255 offset:16384\n\t"
      "s_waitcnt lgkmcnt(11)\n\t"
      "v_mfma_f32_16x16x32_bf16 v[8:11], a[8:11], v[84:87], v[8:11]\n\t"
      "v_mfma_f32_16x16x32_bf16 v[12:15], a[72:75], v[84:87], v[12:15]\n\t"
      "ds_read_b128 v[68:71], v255 offset:17408\n\t"
      "s_waitcnt lgkmcnt(11)\n\t"
      "v_mfma_f32_16x16x32_bf16 v[0:3], a[12:15], v[88:91], v[0:3]\n\t"
      "v_mfma_f32_16x16x32_bf16 v[4:7], a[76:79], v[88:91], v[4:7]\n\t"
      "ds_read_b128 v[72:75], v255 offset:18432\n\t"
      "s_waitcnt lgkmcnt(11)\n\t"
      "v_mfma_f32_16x16x32_bf16 v[8:11], a[12:15], v[92:95], v[8:11]\n\t"
      "v_mfma_f32_16x16x32_bf16 v[12:15], a[76:79], v[92:95], v[12:15]\n\t"
      "buffer_load_dwordx4 v[136:139], v252, s[36:39], s51 offen offset:1024 sc1\n\t"
      "ds_read_b128 v[76:79], v255 offset:19456\n\t"
      "s_waitcnt lgkmcnt(11)\n\t"
      "v_mfma_f32_16x16x32_bf16 v[0:3], a[16:19], v[96:99], v[0:3]\n\t"
      "v_mfma_f32_16x16x32_bf16 v[4:7], a[80:83], v[96:99], v[4:7]\n\t"
      "ds_read_b128 v[80:83], v255 offset:20480\n\t"
      "s_waitcnt lgkmcnt(11)\n\t"
      "v_mfma_f32_16x16x32_bf16 v[8:11], a[16:19], v[100:103], v[8:11]\n\t"
      "v_mfma_f32_16x16x32_bf16 v[12:15], a[80:83], v[100:103], v[12:15]\n\t"
      "ds_read_b128 v[84:87], v255 offset:21504\n\t"
      "s_waitcnt lgkmcnt(11)\n\t"
      "v_mfma_f32_16x16x32_bf16 v[0:3], a[20:23], v[104:107], v[0:3]\n\t"
      "v_mfma_f32_16x16x32_bf16 v[4:7], a[84:87], v[104:107], v[4:7]\n\t"
      "ds_read_b128 v[88:91], v255 offset:22528\n\t"
      "s_waitcnt lgkmcnt(11)\n\t"
      "v_mfma_f32_16x16x32_bf16 v[8:11], a[20:23], v[108:111], v[8:11]\n\t"
      "v_mfma_f32_16x16x32_bf16 v[12:15], a[84:87], v[108:111], v[12:15]\n\t"
      "buffer_load_dwordx4 v[140:143], v252, s[36:39], s51 offen offset:1536 sc1\n\t"
      "ds_read_b128 v[92:95], v255 offset:23552\n\t"
      "s_waitcnt lgkmcnt(11)\n\t"
      "v_mfma_f32_16x16x32_bf16 v[0:3], a[24:27], v[112:115], v[0:3]\n\t"
      "v_mfma_f32_16x16x32_bf16 v[4:7], a[88:91], v[112:115], v[4:7]\n\t"
      "ds_read_b128 v[96:99], v255 offset:24576\n\t"
      "s_waitcnt lgkmcnt(11)\n\t"
      "v_mfma_f32_16x16x32_bf16 v[8:11], a[24:27], v[116:119], v[8:11]\n\t"
      "v_mfma_f32_16x16x32_bf16 v[12:15], a[88:91], v[116:119], v[12:15]\n\t"
      "ds_read_b128 v[100:103], v255 offset:25600\n\t"
      "s_waitcnt lgkmcnt(11)\n\t"
      "v_mfma_f32_16x16x32_bf16 v[0:3], a[28:31], v[120:123], v[0:3]\n\t"
      "v_mfma_f32_16x16x32_bf16 v[4:7], a[92:95], v[120:123], v[4:7]\n\t"
      "ds_read_b128 v[104:107], v255 offset:26624\n\t"
      "s_waitcnt lgkmcnt(11)\n\t"
      "v_mfma_f32_16x16x32_bf16 v[8:11], a[28:31], v[124:127], v[8:11]\n\t"
      "buffer_load_dwordx4 v[144:147], v253, s[36:39], s52 offen sc1\n\t"
      "v_mfma_f32_16x16x32_bf16 v[12:15], a[92:95], v[124:127], v[12:15]\n\t"
      "ds_read_b128 v[108:111], v255 offset:27648\n\t"
      "s_waitcnt lgkmcnt(11)\n\t"
      "v_mfma_f32_16x16x32_bf16 v[0:3], a[32:35], v[64:67], v[0:3]\n\t"
      "v_mfma_f32_16x16x32_bf16 v[4:7], a[96:99], v[64:67], v[4:7]\n\t"
      "ds_read_b128 v[112:115], v255 offset:28672\n\t"
      "s_waitcnt lgkmcnt(11)\n\t"
      "v_mfma_f32_16x16x32_bf16 v[8:11], a[32:35], v[68:71], v[8:11]\n\t"
      "v_mfma_f32_16x16x32_bf16 v[12:15], a[96:99], v[68:71], v[12:15]\n\t"
      "ds_read_b128 v[116:119], v255 offset:29696\n\t"
      "s_waitcnt lgkmcnt(11)\n\t"
      "v_mfma_f32_16x16x32_bf16 v[0:3], a[36:39], v[72:75], v[0:3]\n\t"
      "v_mfma_f32_16x16x32_bf16 v[4:7], a[100:103], v[72:75], v[4:7]\n\t"
      "ds_read_b128 v[120:123], v255 offset:30720\n\t"
      "s_waitcnt lgkmcnt(11)\n\t"
      "v_mfma_f32_16x16x32_bf16 v[8:11], a[36:39], v[76:79], v[8:11]\n\t"
      "buffer_load_dwordx4 v[148:151], v253, s[36:39], s52 offen offset:512 sc1\n\t"
      "v_mfma_f32_16x16x32_bf16 v[12:15], a[100:103], v[76:79], v[12:15]\n\t"
      "ds_read_b128 v[124:127], v255 offset:31744\n\t"
      "s_waitcnt lgkmcnt(11)\n\t"
      "v_mfma_f32_16x16x32_bf16 v[0:3], a[40:43], v[80:83], v[0:3]\n\t"
      "v_mfma_f32_16x16x32_bf16 v[4:7], a[104:107], v[80:83], v[4:7]\n\t"
      "s_waitcnt lgkmcnt(10)\n\t"
      "v_mfma_f32_16x16x32_bf16 v[8:11], a[40:43], v[84:87], v[8:11]\n\t"
      "v_mfma_f32_16x16x32_bf16 v[12:15], a[104:107], v[84:87], v[12:15]\n\t"
      "s_waitcnt lgkmcnt(9)\n\t"
      "v_mfma_f32_16x16x32_bf16 v[0:3], a[44:47], v[88:91], v[0:3]\n\t"
      "v_mfma_f32_16x16x32_bf16 v[4:7], a[108:111], v[88:91], v[4:7]\n\t"
      "s_waitcnt lgkmcnt(8)\n\t"
      "v_mfma_f32_16x16x32_bf16 v[8:11], a[44:47], v[92:95], v[8:11]\n\t"
      "buffer_load_dwordx4 v[152:155], v253, s[36:39], s52 offen offset:1024 sc1\n\t"
      "v_mfma_f32_16x16x32_bf16 v[12:15], a[108:111], v[92:95], v[12:15]\n\t"
      "s_waitcnt lgkmcnt(7)\n\t"
      "v_mfma_f32_16x16x32_bf16 v[0:3], a[48:51], v[96:99], v[0:3]\n\t"
      "v_mfma_f32_16x16x32_bf16 v[4:7], a[112:115], v[96:99], v[4:7]\n\t"
      "s_waitcnt lgkmcnt(6)\n\t"
      "v_mfma_f32_16x16x32_bf16 v[8:11], a[48:51], v[100:103], v[8:11]\n\t"
      "v_mfma_f32_16x16x32_bf16 v[12:15], a[112:115], v[100:103], v[12:15]\n\t"
      "s_waitcnt lgkmcnt(5)\n\t"
      "v_mfma_f32_16x16x32_bf16 v[0:3], a[52:55], v[104:107], v[0:3]\n\t"
      "v_mfma_f32_16x16x32_bf16 v[4:7], a[116:119], v[104:107], v[4:7]\n\t"
      "s_waitcnt lgkmcnt(4)\n\t"
      "v_mfma_f32_16x16x32_bf16 v[8:11], a[52:55], v[108:111], v[8:11]\n\t"
      "v_mfma_f32_16x16x32_bf16 v[12:15], a[116:119], v[108:111], v[12:15]\n\t"
      "buffer_load_dwordx4 v[156:159], v253, s[36:39], s52 offen offset:1536 sc1\n\t"
      "s_waitcnt lgkmcnt(3)\n\t"
      "v_mfma_f32_16x16x32_bf16 v[0:3], a[56:59], v[112:115], v[0:3]\n\t"
      "v_mfma_f32_16x16x32_bf16 v[4:7], a[120:123], v[112:115], v[4:7]\n\t"
      "s_waitcnt lgkmcnt(2)\n\t"
      "v_mfma_f32_16x16x32_bf16 v[8:11], a[56:59], v[116:119], v[8:11]\n\t"
      "v_mfma_f32_16x16x32_bf16 v[12:15], a[120:123], v[116:119], v[12:15]\n\t"
      "s_waitcnt lgkmcnt(1)\n\t"
      "v_mfma_f32_16x16x32_bf16 v[0:3], a[60:63], v[120:123], v[0:3]\n\t"
      "v_mfma_f32_16x16x32_bf16 v[4:7], a[124:127], v[120:123], v[4:7]\n\t"
      "s_waitcnt lgkmcnt(0)\n\t"
      "v_mfma_f32_16x16x32_bf16 v[8:11], a[60:63], v[124:127], v[8:11]\n\t"
      "v_mfma_f32_16x16x32_bf16 v[12:15], a[124:127], v[124:127], v[12:15]\n\t"
      "v_xor_b32 v254, 0x8000, v254\n\t"
      "v_xor_b32 v255, 0x8000, v255\n\t"
      "s_add_u32 s69, s53, 0x4000\n\t"
      "s_add_u32 s48, s48, 4\n\t"
      "s_waitcnt vmcnt(40)\n\t"
      "v_cvt_pk_bf16_f32 v0, v0, v1\n\t"
      "v_cvt_pk_bf16_f32 v1, v2, v3\n\t"
      "v_cvt_pk_bf16_f32 v2, v4, v5\n\t"
      "v_cvt_pk_bf16_f32 v3, v6, v7\n\t"
      "v_min_u32 v0, -2, v0\n\t"
      "v_min_u32 v1, -2, v1\n\t"
      "v_min_u32 v2, -2, v2\n\t"
      "v_min_u32 v3, -2, v3\n\t"
      "buffer_store_dwordx4 v[0:3], v238, s[36:39], s53 offen sc1\n\t"
      "v_cvt_pk_bf16_f32 v8, v8, v9\n\t"
      "v_cvt_pk_bf16_f32 v9, v10, v11\n\t"
      "v_cvt_pk_bf16_f32 v10, v12, v13\n\t"
      "v_cvt_pk_bf16_f32 v11, v14, v15\n\t"
      "v_min_u32 v8, -2, v8\n\t"
      "v_min_u32 v9, -2, v9\n\t"
      "v_min_u32 v10, -2, v10\n\t"
      "v_min_u32 v11, -2, v11\n\t"
      "buffer_store_dwordx4 v[8:11], v238, s[36:39], s69 offen sc1\n\t"
      "s_add_u32 s53, s53, 1048576\n\t"
      "s_nop 1\n\t"
      "s_branch WTOP%=\n\t"
      ".p2align 5\n\t"
      "WR2%=:\n\t"
      "v_cvt_pk_bf16_f32 v204, v204, v205\n\t"
      "v_and_b32 v205, s74, v204\n\t"
      "v_lshlrev_b32 v204, 16, v204\n\t"
      "v_add_f32 v205, 0x3727c5ac, v205\n\t"
      "v_pk_add_f32 v[40:41], v[192:193], v[204:205] op_sel_hi:[1,0] neg_lo:[0,1] neg_hi:[0,1]\n\t"
      "v_pk_add_f32 v[42:43], v[194:195], v[204:205] op_sel_hi:[1,0] neg_lo:[0,1] neg_hi:[0,1]\n\t"
      "v_pk_add_f32 v[44:45], v[196:197], v[204:205] op_sel_hi:[1,0] neg_lo:[0,1] neg_hi:[0,1]\n\t"
      "v_pk_add_f32 v[46:47], v[198:199], v[204:205] op_sel_hi:[1,0] neg_lo:[0,1] neg_hi:[0,1]\n\t"
      "v_cvt_pk_bf16_f32 v205, v205, v205\n\t"
      "v_and_b32 v205, s74, v205\n\t"
      "v_sqrt_f32 v205, v205\n\t"
      "v_cvt_pk_bf16_f32 v40, v40, v41\n\t"
      "v_cvt_pk_bf16_f32 v42, v42, v43\n\t"
      "v_cvt_pk_bf16_f32 v44, v44, v45\n\t"
      "v_cvt_pk_bf16_f32 v46, v46, v47\n\t"
      "v_cvt_pk_bf16_f32 v205, v205, v205\n\t"
      "v_and_b32 v205, s74, v205\n\t"
      "v_rcp_f32 v204, v205\n\t"
      "v_and_b32 v41, s74, v40\n\t"
      "v_lshlrev_b32 v40, 16, v40\n\t"
      "v_and_b32 v43, s74, v42\n\t"
      "v_lshlrev_b32 v42, 16, v42\n\t"
      "v_and_b32 v45, s74, v44\n\t"
      "v_lshlrev_b32 v44, 16, v44\n\t"
      "v_and_b32 v47, s74, v46\n\t"
      "v_lshlrev_b32 v46, 16, v46\n\t"
      "v_pk_mul_f32 v[40:41], v[40:41], v[204:205] op_sel_hi:[1,0]\n\t"
      "v_pk_mul_f32 v[42:43], v[42:43], v[204:205] op_sel_hi:[1,0]\n\t"
      "v_pk_mul_f32 v[44:45], v[44:45], v[204:205] op_sel_hi:[1,0]\n\t"
      "v_pk_mul_f32 v[46:47], v[46:47], v[204:205] op_sel_hi:[1,0]\n\t"
      "v_cvt_pk_bf16_f32 v52, v40, v41\n\t"
      "v_cvt_pk_bf16_f32 v53, v42, v43\n\t"
      "v_cvt_pk_bf16_f32 v54, v44, v45\n\t"
      "v_cvt_pk_bf16_f32 v55, v46, v47\n\t"
      "v_and_b32 v33, s74, v52\n\t"
      "v_lshlrev_b32 v32, 16, v52\n\t"
      "v_and_b32 v35, s74, v53\n\t"
      "v_lshlrev_b32 v34, 16, v53\n\t"
      "v_and_b32 v37, s74, v54\n\t"
      "v_lshlrev_b32 v36, 16, v54\n\t"
      "v_and_b32 v39, s74, v55\n\t"
      "v_lshlrev_b32 v38, 16, v55\n\t"
      "v_pk_mul_f32 v[40:41], v[32:33], v[240:241]\n\t"
      "v_pk_fma_f32 v[40:41], v[34:35], v[242:243], v[40:41]\n\t"
      "v_pk_fma_f32 v[40:41], v[36:37], v[244:245], v[40:41]\n\t"
      "v_pk_fma_f32 v[40:41], v[38:39], v[246:247], v[40:41]\n\t"
      "v_add_f32 v40, v40, v41\n\t"
      "s_lshl_b32 s73, s49, 5\n\t"
      "s_add_u32 s73, s73, s60\n\t"
      "s_lshl_b32 s69, s73, 2\n\t"
      "s_add_u32 s69, s69, s57\n\t"
      "s_lshl_b32 s70, s73, 7\n\t"
      "s_add_u32 s70, s70, s57\n\t"
      "s_sub_u32 s70, s70, 212992\n\t"
      "s_cmp_ge_u32 s49, 56\n\t"
      "s_cselect_b32 s70, s70, s69\n\t"
      "v_add_f32_dpp v40, v40, v40 quad_perm:[1,0,3,2] row_mask:0xf bank_mask:0xf\n\t"
      "s_nop 1\n\t"
      "v_add_f32_dpp v40, v40, v40 quad_perm:[2,3,0,1] row_mask:0xf bank_mask:0xf\n\t"
      "s_nop 1\n\t"
      "v_add_f32_dpp v40, v40, v40 row_ror:4 row_mask:0xf bank_mask:0xf\n\t"
      "s_nop 1\n\t"
      "v_add_f32_dpp v40, v40, v40 row_ror:8 row_mask:0xf bank_mask:0xf\n\t"
      "v_mov_b32 v41, v40\n\t"
      "s_nop 1\n\t"
      "v_permlane16_swap_b32 v40, v41\n\t"
      "v_add_f32 v40, v40, v41\n\t"
      "v_mov_b32 v41, v40\n\t"
      "s_nop 1\n\t"
      "v_permlane32_swap_b32 v40, v41\n\t"
      "v_add_f32 v40, v40, v41\n\t"
      "v_cvt_pk_bf16_f32 v40, v40, v40\n\t"
      "v_and_b32 v40, s74, v40\n\t"
      "s_mov_b64 exec, 1\n\t"
      "buffer_store_dword v40, off, s[36:39], s70 sc1\n\t"
      "s_mov_b64 exec, -1\n\t"
      "v_pk_add_f32 v[192:193], v[192:193], v[32:33]\n\t"
      "v_pk_add_f32 v[194:195], v[194:195], v[34:35]\n\t"
      "v_pk_add_f32 v[196:197], v[196:197], v[36:37]\n\t"
      "v_pk_add_f32 v[198:199], v[198:199], v[38:39]\n\t"
      "v_readfirstlane_b32 s68, v40\n\t"
      "v_cvt_pk_bf16_f32 v56, v192, v193\n\t"
      "v_cvt_pk_bf16_f32 v57, v194, v195\n\t"
      "v_cvt_pk_bf16_f32 v58, v196, v197\n\t"
      "v_cvt_pk_bf16_f32 v59, v198, v199\n\t"
      "s_add_u32 s67, s69, 8192\n\t"
      "s_lshl_b32 s73, s73, 10\n\t"
      "s_add_u32 s69, s73, s58\n\t"
      "s_add_u32 s70, s73, s59\n\t"
      "buffer_store_dwordx2 v[56:57], v249, s[44:47], s73 offen sc1\n\t"
      "buffer_store_dwordx2 v[52:53], v249, s[36:39], s69 offen sc1\n\t"
      "buffer_store_dwordx2 v[200:201], v249, s[36:39], s70 offen sc1\n\t"
      "buffer_store_dwordx2 v[58:59], v249, s[44:47], s73 offen offset:512 sc1\n\t"
      "buffer_store_dwordx2 v[54:55], v249, s[36:39], s69 offen offset:512 sc1\n\t"
      "buffer_store_dwordx2 v[202:203], v249, s[36:39], s70 offen offset:512 sc1\n\t"
      "s_mov_b32 s49, s89\n\t"
      "s_add_u32 s89, s89, 16\n\t"
      "s_lshr_b32 s69, s89, 4\n\t"
      "s_cmp_eq_u32 s69, 3\n\t"
      "s_cselect_b32 s89, s93, s89\n\t"
      "s_cmp_ge_u32 s49, 48\n\t"
      "s_cselect_b32 s60, s94, s60\n\t"
      "s_cselect_b32 s54, s95, s54\n\t"
      "s_mov_b32 s65, 0\n\t"
      "s_branch WGO2%=\n\t"
      ".p2align 5\n\t"
      "WOM1%=:\n\t"
      "s_mov_b32 s65, 2\n\t"
      "s_cmp_lg_u32 s96, 0\n\t"
      "s_cbranch_scc1 WOL3%=\n\t"
      "WOL2%=:\n\t"
      "s_waitcnt lgkmcnt(11)\n\t"
      "v_mfma_f32_16x16x32_bf16 v[0:3], a[0:3], v[64:67], 0\n\t"
      "v_mfma_f32_16x16x32_bf16 v[16:19], a[224:227], v[160:163], 0\n\t"
      "v_mfma_f32_16x16x32_bf16 v[4:7], a[64:67], v[64:67], 0\n\t"
      "v_mfma_f32_16x16x32_bf16 v[20:23], a[228:231], v[160:163], 0\n\t"
      "v_and_b32 v193, s74, v224\n\t"
      "v_lshlrev_b32 v192, 16, v224\n\t"
      "ds_read_b128 v[112:115], v255 offset:12288\n\t"
      "s_waitcnt lgkmcnt(11)\n\t"
      "v_mfma_f32_16x16x32_bf16 v[8:11], a[0:3], v[68:71], 0\n\t"
      "v_mfma_f32_16x16x32_bf16 v[16:19], a[224:227], v[164:167], v[16:19]\n\t"
      "v_mfma_f32_16x16x32_bf16 v[12:15], a[64:67], v[68:71], 0\n\t"
      "v_mfma_f32_16x16x32_bf16 v[20:23], a[228:231], v[164:167], v[20:23]\n\t"
      "v_and_b32 v195, s74, v225\n\t"
      "v_lshlrev_b32 v194, 16, v225\n\t"
      "ds_read_b128 v[116:119], v255 offset:13312\n\t"
      "s_waitcnt lgkmcnt(11)\n\t"
      "v_mfma_f32_16x16x32_bf16 v[0:3], a[4:7], v[72:75], v[0:3]\n\t"
      "v_mfma_f32_16x16x32_bf16 v[16:19], a[224:227], v[168:171], v[16:19]\n\t"
      "v_mfma_f32_16x16x32_bf16 v[4:7], a[68:71], v[72:75], v[4:7]\n\t"
      "v_mfma_f32_16x16x32_bf16 v[20:23], a[228:231], v[168:171], v[20:23]\n\t"
      "v_and_b32 v197, s74, v226\n\t"
      "v_lshlrev_b32 v196, 16, v226\n\t"
      "ds_read_b128 v[120:123], v255 offset:14336\n\t"
      "s_waitcnt lgkmcnt(11)\n\t"
      "v_mfma_f32_16x16x32_bf16 v[8:11], a[4:7], v[76:79], v[8:11]\n\t"
      "v_mfma_f32_16x16x32_bf16 v[16:19], a[224:227], v[172:175], v[16:19]\n\t"
      "v_mfma_f32_16x16x32_bf16 v[12:15], a[68:71], v[76:79], v[12:15]\n\t"
      "v_mfma_f32_16x16x32_bf16 v[20:23], a[228:231], v[172:175], v[20:23]\n\t"
      "v_and_b32 v199, s74, v227\n\t"
      "v_lshlrev_b32 v198, 16, v227\n\t"
      "ds_read_b128 v[124:127], v255 offset:15360\n\t"
      "s_waitcnt lgkmcnt(11)\n\t"
      "v_mfma_f32_16x16x32_bf16 v[0:3], a[8:11], v[80:83], v[0:3]\n\t"
      "v_mfma_f32_16x16x32_bf16 v[16:19], a[224:227], v[176:179], v[16:19]\n\t"
      "v_mfma_f32_16x16x32_bf16 v[4:7], a[72:75], v[80:83], v[4:7]\n\t"
      "v_mfma_f32_16x16x32_bf16 v[20:23], a[228:231], v[176:179], v[20:23]\n\t"
      "ds_read_b128 v[64:67], v255 offset:16384\n\t"
      "s_waitcnt lgkmcnt(11)\n\t"
      "v_mfma_f32_16x16x32_bf16 v[8:11], a[8:11], v[84:87], v[8:11]\n\t"
      "v_mfma_f32_16x16x32_bf16 v[16:19], a[224:227], v[180:183], v[16:19]\n\t"
      "v_mfma_f32_16x16x32_bf16 v[12:15], a[72:75], v[84:87], v[12:15]\n\t"
      "v_mfma_f32_16x16x32_bf16 v[20:23], a[228:231], v[180:183], v[20:23]\n\t"
      "ds_read_b128 v[68:71], v255 offset:17408\n\t"
      "s_waitcnt lgkmcnt(11)\n\t"
      "v_mfma_f32_16x16x32_bf16 v[0:3], a[12:15], v[88:91], v[0:3]\n\t"
      "v_mfma_f32_16x16x32_bf16 v[16:19], a[224:227], v[184:187], v[16:19]\n\t"
      "v_mfma_f32_16x16x32_bf16 v[4:7], a[76:79], v[88:91], v[4:7]\n\t"
      "v_mfma_f32_16x16x32_bf16 v[20:23], a[228:231], v[184:187], v[20:23]\n\t"
      "ds_read_b128 v[72:75], v255 offset:18432\n\t"
      "s_waitcnt lgkmcnt(11)\n\t"
      "v_mfma_f32_16x16x32_bf16 v[8:11], a[12:15], v[92:95], v[8:11]\n\t"
      "v_mfma_f32_16x16x32_bf16 v[16:19], a[224:227], v[188:191], v[16:19]\n\t"
      "v_mfma_f32_16x16x32_bf16 v[12:15], a[76:79], v[92:95], v[12:15]\n\t"
      "v_mfma_f32_16x16x32_bf16 v[20:23], a[228:231], v[188:191], v[20:23]\n\t"
      "ds_read_b128 v[76:79], v255 offset:19456\n\t"
      "s_waitcnt lgkmcnt(11)\n\t"
      "v_mfma_f32_16x16x32_bf16 v[0:3], a[16:19], v[96:99], v[0:3]\n\t"
      "v_cvt_pk_bf16_f32 v32, v16, v17\n\t"
      "v_cvt_pk_bf16_f32 v34, v18, v19\n\t"
      "v_mfma_f32_16x16x32_bf16 v[4:7], a[80:83], v[96:99], v[4:7]\n\t"
      "ds_read_b128 v[80:83], v255 offset:20480\n\t"
      "s_waitcnt lgkmcnt(11)\n\t"
      "v_mfma_f32_16x16x32_bf16 v[8:11], a[16:19], v[100:103], v[8:11]\n\t"
      "v_cvt_pk_bf16_f32 v36, v20, v21\n\t"
      "v_cvt_pk_bf16_f32 v38, v22, v23\n\t"
      "v_mfma_f32_16x16x32_bf16 v[12:15], a[80:83], v[100:103], v[12:15]\n\t"
      "v_and_b32 v33, s74, v32\n\t"
      "v_lshlrev_b32 v32, 16, v32\n\t"
      "ds_read_b128 v[84:87], v255 offset:21504\n\t"
      "s_waitcnt lgkmcnt(11)\n\t"
      "v_mfma_f32_16x16x32_bf16 v[0:3], a[20:23], v[104:107], v[0:3]\n\t"
      "v_and_b32 v35, s74, v34\n\t"
      "v_lshlrev_b32 v34, 16, v34\n\t"
      "v_mfma_f32_16x16x32_bf16 v[4:7], a[84:87], v[104:107], v[4:7]\n\t"
      "v_and_b32 v37, s74, v36\n\t"
      "v_lshlrev_b32 v36, 16, v36\n\t"
      "ds_read_b128 v[88:91], v255 offset:22528\n\t"
      "s_waitcnt lgkmcnt(11)\n\t"
      "v_mfma_f32_16x16x32_bf16 v[8:11], a[20:23], v[108:111], v[8:11]\n\t"
      "v_and_b32 v39, s74, v38\n\t"
      "v_lshlrev_b32 v38, 16, v38\n\t"
      "v_mfma_f32_16x16x32_bf16 v[12:15], a[84:87], v[108:111], v[12:15]\n\t"
      "v_add_f32 v192, v192, v32\n\t"
      "v_add_f32 v193, v193, v33\n\t"
      "ds_read_b128 v[92:95], v255 offset:23552\n\t"
      "s_waitcnt lgkmcnt(11)\n\t"
      "v_mfma_f32_16x16x32_bf16 v[0:3], a[24:27], v[112:115], v[0:3]\n\t"
      "v_add_f32 v194, v194, v34\n\t"
      "v_add_f32 v195, v195, v35\n\t"
      "v_mfma_f32_16x16x32_bf16 v[4:7], a[88:91], v[112:115], v[4:7]\n\t"
      "v_add_f32 v196, v196, v36\n\t"
      "v_add_f32 v197, v197, v37\n\t"
      "ds_read_b128 v[96:99], v255 offset:24576\n\t"
      "s_waitcnt lgkmcnt(11)\n\t"
      "v_mfma_f32_16x16x32_bf16 v[8:11], a[24:27], v[116:119], v[8:11]\n\t"
      "v_add_f32 v198, v198, v38\n\t"
      "v_add_f32 v199, v199, v39\n\t"
      "v_mfma_f32_16x16x32_bf16 v[12:15], a[88:91], v[116:119], v[12:15]\n\t"
      "v_cvt_pk_bf16_f32 v200, v192, v193\n\t"
      "v_cvt_pk_bf16_f32 v201, v194, v195\n\t"
      "ds_read_b128 v[100:103], v255 offset:25600\n\t"
      "s_waitcnt lgkmcnt(11)\n\t"
      "v_mfma_f32_16x16x32_bf16 v[0:3], a[28:31], v[120:123], v[0:3]\n\t"
      "v_cvt_pk_bf16_f32 v202, v196, v197\n\t"
      "v_cvt_pk_bf16_f32 v203, v198, v199\n\t"
      "v_mfma_f32_16x16x32_bf16 v[4:7], a[92:95], v[120:123], v[4:7]\n\t"
      "v_and_b32 v193, s74, v200\n\t"
      "v_lshlrev_b32 v192, 16, v200\n\t"
      "ds_read_b128 v[104:107], v255 offset:26624\n\t"
      "s_waitcnt lgkmcnt(11)\n\t"
      "v_mfma_f32_16x16x32_bf16 v[8:11], a[28:31], v[124:127], v[8:11]\n\t"
      "v_and_b32 v195, s74, v201\n\t"
      "v_lshlrev_b32 v194, 16, v201\n\t"
      "v_mfma_f32_16x16x32_bf16 v[12:15], a[92:95], v[124:127], v[12:15]\n\t"
      "v_and_b32 v197, s74, v202\n\t"
      "v_lshlrev_b32 v196, 16, v202\n\t"
      "ds_read_b128 v[108:111], v255 offset:27648\n\t"
      "s_waitcnt lgkmcnt(11)\n\t"
      "v_mfma_f32_16x16x32_bf16 v[0:3], a[32:35], v[64:67], v[0:3]\n\t"
      "v_and_b32 v199, s74, v203\n\t"
      "v_lshlrev_b32 v198, 16, v203\n\t"
      "v_mfma_f32_16x16x32_bf16 v[4:7], a[96:99], v[64:67], v[4:7]\n\t"
      "v_add_f32 v32, v192, v196\n\t"
      "v_add_f32 v33, v193, v197\n\t"
      "ds_read_b128 v[112:115], v255 offset:28672\n\t"
      "s_waitcnt lgkmcnt(11)\n\t"
      "v_mfma_f32_16x16x32_bf16 v[8:11], a[32:35], v[68:71], v[8:11]\n\t"
      "v_add_f32 v34, v194, v198\n\t"
      "v_add_f32 v35, v195, v199\n\t"
      "v_mfma_f32_16x16x32_bf16 v[12:15], a[96:99], v[68:71], v[12:15]\n\t"
      "v_add_f32 v32, v32, v34\n\t"
      "v_add_f32 v33, v33, v35\n\t"
      "ds_read_b128 v[116:119], v255 offset:29696\n\t"
      "s_waitcnt lgkmcnt(11)\n\t"
      "v_mfma_f32_16x16x32_bf16 v[0:3], a[36:39], v[72:75], v[0:3]\n\t"
      "v_add_f32 v32, v32, v33\n\t"
      "v_mfma_f32_16x16x32_bf16 v[4:7], a[100:103], v[72:75], v[4:7]\n\t"
      "ds_read_b128 v[120:123], v255 offset:30720\n\t"
      "s_waitcnt lgkmcnt(11)\n\t"
      "v_mfma_f32_16x16x32_bf16 v[8:11], a[36:39], v[76:79], v[8:11]\n\t"
      "v_add_f32_dpp v32, v32, v32 quad_perm:[1,0,3,2] row_mask:0xf bank_mask:0xf\n\t"
      "v_mfma_f32_16x16x32_bf16 v[12:15], a[100:103], v[76:79], v[12:15]\n\t"
      "ds_read_b128 v[124:127], v255 offset:31744\n\t"
      "s_waitcnt lgkmcnt(11)\n\t"
      "v_mfma_f32_16x16x32_bf16 v[0:3], a[40:43], v[80:83], v[0:3]\n\t"
      "v_add_f32_dpp v32, v32, v32 quad_perm:[2,3,0,1] row_mask:0xf bank_mask:0xf\n\t"
      "v_mfma_f32_16x16x32_bf16 v[4:7], a[104:107], v[80:83], v[4:7]\n\t"
      "s_waitcnt lgkmcnt(10)\n\t"
      "v_mfma_f32_16x16x32_bf16 v[8:11], a[40:43], v[84:87], v[8:11]\n\t"
      "v_add_f32_dpp v32, v32, v32 row_ror:4 row_mask:0xf bank_mask:0xf\n\t"
      "v_mfma_f32_16x16x32_bf16 v[12:15], a[104:107], v[84:87], v[12:15]\n\t"
      "s_waitcnt lgkmcnt(9)\n\t"
      "v_mfma_f32_16x16x32_bf16 v[0:3], a[44:47], v[88:91], v[0:3]\n\t"
      "v_add_f32_dpp v32, v32, v32 row_ror:8 row_mask:0xf bank_mask:0xf\n\t"
      "v_mov_b32 v33, v32\n\t"
      "v_mfma_f32_16x16x32_bf16 v[4:7], a[108:111], v[88:91], v[4:7]\n\t"
      "s_waitcnt lgkmcnt(8)\n\t"
      "v_mfma_f32_16x16x32_bf16 v[8:11], a[44:47], v[92:95], v[8:11]\n\t"
      "v_permlane16_swap_b32 v32, v33\n\t"
      "v_add_f32 v32, v32, v33\n\t"
      "v_mfma_f32_16x16x32_bf16 v[12:15], a[108:111], v[92:95], v[12:15]\n\t"
      "v_mov_b32 v33, v32\n\t"
      "s_waitcnt lgkmcnt(7)\n\t"
      "v_mfma_f32_16x16x32_bf16 v[0:3], a[48:51], v[96:99], v[0:3]\n\t"
      "v_permlane32_swap_b32 v32, v33\n\t"
      "v_add_f32 v32, v32, v33\n\t"
      "v_mfma_f32_16x16x32_bf16 v[4:7], a[112:115], v[96:99], v[4:7]\n\t"
      "v_mul_f32 v204, 0x3b000000, v32\n\t"
      "v_sub_f32 v40, v192, v204\n\t"
      "s_waitcnt lgkmcnt(6)\n\t"
      "v_mfma_f32_16x16x32_bf16 v[8:11], a[48:51], v[100:103], v[8:11]\n\t"
      "v_sub_f32 v41, v193, v204\n\t"
      "v_sub_f32 v42, v194, v204\n\t"
      "v_mfma_f32_16x16x32_bf16 v[12:15], a[112:115], v[100:103], v[12:15]\n\t"
      "v_sub_f32 v43, v195, v204\n\t"
      "v_sub_f32 v44, v196, v204\n\t"
      "s_waitcnt lgkmcnt(5)\n\t"
      "v_mfma_f32_16x16x32_bf16 v[0:3], a[52:55], v[104:107], v[0:3]\n\t"
      "v_sub_f32 v45, v197, v204\n\t"
      "v_sub_f32 v46, v198, v204\n\t"
      "v_mfma_f32_16x16x32_bf16 v[4:7], a[116:119], v[104:107], v[4:7]\n\t"
      "v_sub_f32 v47, v199, v204\n\t"
      "v_mul_f32 v32, v40, v40\n\t"
      "s_waitcnt lgkmcnt(4)\n\t"
      "v_mfma_f32_16x16x32_bf16 v[8:11], a[52:55], v[108:111], v[8:11]\n\t"
      "v_mul_f32 v33, v41, v41\n\t"
      "v_fma_f32 v32, v42, v42, v32\n\t"
      "v_mfma_f32_16x16x32_bf16 v[12:15], a[116:119], v[108:111], v[12:15]\n\t"
      "v_fma_f32 v33, v43, v43, v33\n\t"
      "v_fma_f32 v32, v44, v44, v32\n\t"
      "s_waitcnt lgkmcnt(3)\n\t"
      "v_mfma_f32_16x16x32_bf16 v[0:3], a[56:59], v[112:115], v[0:3]\n\t"
      "v_fma_f32 v33, v45, v45, v33\n\t"
      "v_fma_f32 v32, v46, v46, v32\n\t"
      "v_mfma_f32_16x16x32_bf16 v[4:7], a[120:123], v[112:115], v[4:7]\n\t"
      "v_fma_f32 v33, v47, v47, v33\n\t"
      "v_add_f32 v32, v32, v33\n\t"
      "s_waitcnt lgkmcnt(2)\n\t"
      "v_mfma_f32_16x16x32_bf16 v[8:11], a[56:59], v[116:119], v[8:11]\n\t"
      "v_add_f32_dpp v32, v32, v32 quad_perm:[1,0,3,2] row_mask:0xf bank_mask:0xf\n\t"
      "v_mfma_f32_16x16x32_bf16 v[12:15], a[120:123], v[116:119], v[12:15]\n\t"
      "s_waitcnt lgkmcnt(1)\n\t"
      "v_mfma_f32_16x16x32_bf16 v[0:3], a[60:63], v[120:123], v[0:3]\n\t"
      "v_add_f32_dpp v32, v32, v32 quad_perm:[2,3,0,1] row_mask:0xf bank_mask:0xf\n\t"
      "v_mfma_f32_16x16x32_bf16 v[4:7], a[124:127], v[120:123], v[4:7]\n\t"
      "s_waitcnt lgkmcnt(0)\n\t"
      "v_mfma_f32_16x16x32_bf16 v[8:11], a[60:63], v[124:127], v[8:11]\n\t"
      "v_add_f32_dpp v32, v32, v32 row_ror:4 row_mask:0xf bank_mask:0xf\n\t"
      "v_mfma_f32_16x16x32_bf16 v[12:15], a[124:127], v[124:127], v[12:15]\n\t"
      "s_nop 0\n\t"
      "v_add_f32_dpp v32, v32, v32 row_ror:8 row_mask:0xf bank_mask:0xf\n\t"
      "v_mov_b32 v33, v32\n\t"
      "s_nop 1\n\t"
      "v_permlane16_swap_b32 v32, v33\n\t"
      "v_add_f32 v32, v32, v33\n\t"
      "v_mov_b32 v33, v32\n\t"
      "s_nop 1\n\t"
      "v_permlane32_swap_b32 v32, v33\n\t"
      "v_add_f32 v32, v32, v33\n\t"
      "v_mul_f32 v205, 0x3b000000, v32\n\t"
      "v_xor_b32 v254, 0x8000, v254\n\t"
      "v_xor_b32 v255, 0x8000, v255\n\t"
      "s_add_u32 s69, s53, 0x4000\n\t"
      "s_add_u32 s48, s48, 4\n\t"
      "s_waitcnt vmcnt(40)\n\t"
      "v_cvt_pk_bf16_f32 v0, v0, v1\n\t"
      "v_cvt_pk_bf16_f32 v1, v2, v3\n\t"
      "v_cvt_pk_bf16_f32 v2, v4, v5\n\t"
      "v_cvt_pk_bf16_f32 v3, v6, v7\n\t"
      "v_min_u32 v0, -2, v0\n\t"
      "v_min_u32 v1, -2, v1\n\t"
      "v_min_u32 v2, -2, v2\n\t"
      "v_min_u32 v3, -2, v3\n\t"
      "buffer_store_dwordx4 v[0:3], v238, s[36:39], s53 offen sc1\n\t"
      "v_cvt_pk_bf16_f32 v8, v8, v9\n\t"
      "v_cvt_pk_bf16_f32 v9, v10, v11\n\t"
      "v_cvt_pk_bf16_f32 v10, v12, v13\n\t"
      "v_cvt_pk_bf16_f32 v11, v14, v15\n\t"
      "v_min_u32 v8, -2, v8\n\t"
      "v_min_u32 v9, -2, v9\n\t"
      "v_min_u32 v10, -2, v10\n\t"
      "v_min_u32 v11, -2, v11\n\t"
      "buffer_store_dwordx4 v[8:11], v238, s[36:39], s69 offen sc1\n\t"
      "s_add_u32 s53, s53, 1048576\n\t"
      "s_nop 1\n\t"
      "s_branch WTOP%=\n\t"
      ".p2align 5\n\t"
      "WOL3%=:\n\t"
      "buffer_load_dwordx4 v[128:131], v252, s[36:39], s51 offen sc1\n\t"
      "s_waitcnt lgkmcnt(11)\n\t"
      "v_mfma_f32_16x16x32_bf16 v[0:3], a[0:3], v[64:67], 0\n\t"
      "v_mfma_f32_16x16x32_bf16 v[16:19], a[224:227], v[160:163], 0\n\t"
      "v_mfma_f32_16x16x32_bf16 v[4:7], a[64:67], v[64:67], 0\n\t"
      "v_mfma_f32_16x16x32_bf16 v[20:23], a[228:231], v[160:163], 0\n\t"
      "v_and_b32 v193, s74, v224\n\t"
      "v_lshlrev_b32 v192, 16, v224\n\t"
      "ds_read_b128 v[112:115], v255 offset:12288\n\t"
      "s_waitcnt lgkmcnt(11)\n\t"
      "v_mfma_f32_16x16x32_bf16 v[8:11], a[0:3], v[68:71], 0\n\t"
      "v_mfma_f32_16x16x32_bf16 v[16:19], a[224:227], v[164:167], v[16:19]\n\t"
      "v_mfma_f32_16x16x32_bf16 v[12:15], a[64:67], v[68:71], 0\n\t"
      "v_mfma_f32_16x16x32_bf16 v[20:23], a[228:231], v[164:167], v[20:23]\n\t"
      "v_and_b32 v195, s74, v225\n\t"
      "v_lshlrev_b32 v194, 16, v225\n\t"
      "ds_read_b128 v[116:119], v255 offset:13312\n\t"
      "s_waitcnt lgkmcnt(11)\n\t"
      "v_mfma_f32_16x16x32_bf16 v[0:3], a[4:7], v[72:75], v[0:3]\n\t"
      "v_mfma_f32_16x16x32_bf16 v[16:19], a[224:227], v[168:171], v[16:19]\n\t"
      "v_mfma_f32_16x16x32_bf16 v[4:7], a[68:71], v[72:75], v[4:7]\n\t"
      "v_mfma_f32_16x16x32_bf16 v[20:23], a[228:231], v[168:171], v[20:23]\n\t"
      "v_and_b32 v197, s74, v226\n\t"
      "v_lshlrev_b32 v196, 16, v226\n\t"
      "ds_read_b128 v[120:123], v255 offset:14336\n\t"
      "s_waitcnt lgkmcnt(11)\n\t"
      "v_mfma_f32_16x16x32_bf16 v[8:11], a[4:7], v[76:79], v[8:11]\n\t"
      "v_mfma_f32_16x16x32_bf16 v[16:19], a[224:227], v[172:175], v[16:19]\n\t"
      "v_mfma_f32_16x16x32_bf16 v[12:15], a[68:71], v[76:79], v[12:15]\n\t"
      "buffer_load_dwordx4 v[132:135], v252, s[36:39], s51 offen offset:512 sc1\n\t"
      "ds_read_b128 v[124:127], v255 offset:15360\n\t"
      "s_waitcnt lgkmcnt(11)\n\t"
      "v_mfma_f32_16x16x32_bf16 v[0:3], a[8:11], v[80:83], v[0:3]\n\t"
      "v_mfma_f32_16x16x32_bf16 v[20:23], a[228:231], v[172:175], v[20:23]\n\t"
      "v_and_b32 v199, s74, v227\n\t"
      "v_lshlrev_b32 v198, 16, v227\n\t"
      "v_mfma_f32_16x16x32_bf16 v[4:7], a[72:75], v[80:83], v[4:7]\n\t"
      "v_mfma_f32_16x16x32_bf16 v[16:19], a[224:227], v[176:179], v[16:19]\n\t"
      "ds_read_b128 v[64:67], v255 offset:16384\n\t"
      "s_waitcnt lgkmcnt(11)\n\t"
      "v_mfma_f32_16x16x32_bf16 v[8:11], a[8:11], v[84:87], v[8:11]\n\t"
      "v_mfma_f32_16x16x32_bf16 v[20:23], a[228:231], v[176:179], v[20:23]\n\t"
      "v_mfma_f32_16x16x32_bf16 v[12:15], a[72:75], v[84:87], v[12:15]\n\t"
      "v_mfma_f32_16x16x32_bf16 v[16:19], a[224:227], v[180:183], v[16:19]\n\t"
      "ds_read_b128 v[68:71], v255 offset:17408\n\t"
      "s_waitcnt lgkmcnt(11)\n\t"
      "v_mfma_f32_16x16x32_bf16 v[0:3], a[12:15], v[88:91], v[0:3]\n\t"
      "v_mfma_f32_16x16x32_bf16 v[20:23], a[228:231], v[180:183], v[20:23]\n\t"
      "v_mfma_f32_16x16x32_bf16 v[4:7], a[76:79], v[88:91], v[4:7]\n\t"
      "v_mfma_f32_16x16x32_bf16 v[16:19], a[224:227], v[184:187], v[16:19]\n\t"
      "ds_read_b128 v[72:75], v255 offset:18432\n\t"
      "s_waitcnt lgkmcnt(11)\n\t"
      "v_mfma_f32_16x16x32_bf16 v[8:11], a[12:15], v[92:95], v[8:11]\n\t"
      "v_mfma_f32_16x16x32_bf16 v[20:23], a[228:231], v[184:187], v[20:23]\n\t"
      "v_mfma_f32_16x16x32_bf16 v[12:15], a[76:79], v[92:95], v[12:15]\n\t"
      "buffer_load_dwordx4 v[136:139], v252, s[36:39], s51 offen offset:1024 sc1\n\t"
      "ds_read_b128 v[76:79], v255 offset:19456\n\t"
      "s_waitcnt lgkmcnt(11)\n\t"
      "v_mfma_f32_16x16x32_bf16 v[0:3], a[16:19], v[96:99], v[0:3]\n\t"
      "v_mfma_f32_16x16x32_bf16 v[16:19], a[224:227], v[188:191], v[16:19]\n\t"
      "v_mfma_f32_16x16x32_bf16 v[4:7], a[80:83], v[96:99], v[4:7]\n\t"
      "v_mfma_f32_16x16x32_bf16 v[20:23], a[228:231], v[188:191], v[20:23]\n\t"
      "ds_read_b128 v[80:83], v255 offset:20480\n\t"
      "s_waitcnt lgkmcnt(11)\n\t"
      "v_mfma_f32_16x16x32_bf16 v[8:11], a[16:19], v[100:103], v[8:11]\n\t"
      "v_cvt_pk_bf16_f32 v32, v16, v17\n\t"
      "v_cvt_pk_bf16_f32 v34, v18, v19\n\t"
      "v_mfma_f32_16x16x32_bf16 v[12:15], a[80:83], v[100:103], v[12:15]\n\t"
      "ds_read_b128 v[84:87], v255 offset:21504\n\t"
      "s_waitcnt lgkmcnt(11)\n\t"
      "v_mfma_f32_16x16x32_bf16 v[0:3], a[20:23], v[104:107], v[0:3]\n\t"
      "v_cvt_pk_bf16_f32 v36, v20, v21\n\t"
      "v_cvt_pk_bf16_f32 v38, v22, v23\n\t"
      "v_mfma_f32_16x16x32_bf16 v[4:7], a[84:87], v[104:107], v[4:7]\n\t"
      "v_and_b32 v33, s74, v32\n\t"
      "v_lshlrev_b32 v32, 16, v32\n\t"
      "ds_read_b128 v[88:91], v255 offset:22528\n\t"
      "s_waitcnt lgkmcnt(11)\n\t"
      "v_mfma_f32_16x16x32_bf16 v[8:11], a[20:23], v[108:111], v[8:11]\n\t"
      "v_and_b32 v35, s74, v34\n\t"
      "v_lshlrev_b32 v34, 16, v34\n\t"
      "v_mfma_f32_16x16x32_bf16 v[12:15], a[84:87], v[108:111], v[12:15]\n\t"
      "buffer_load_dwordx4 v[140:143], v252, s[36:39], s51 offen offset:1536 sc1\n\t"
      "ds_read_b128 v[92:95], v255 offset:23552\n\t"
      "s_waitcnt lgkmcnt(11)\n\t"
      "v_mfma_f32_16x16x32_bf16 v[0:3], a[24:27], v[112:115], v[0:3]\n\t"
      "v_and_b32 v37, s74, v36\n\t"
      "v_lshlrev_b32 v36, 16, v36\n\t"
      "v_mfma_f32_16x16x32_bf16 v[4:7], a[88:91], v[112:115], v[4:7]\n\t"
      "v_and_b32 v39, s74, v38\n\t"
      "v_lshlrev_b32 v38, 16, v38\n\t"
      "ds_read_b128 v[96:99], v255 offset:24576\n\t"
      "s_waitcnt lgkmcnt(11)\n\t"
      "v_mfma_f32_16x16x32_bf16 v[8:11], a[24:27], v[116:119], v[8:11]\n\t"
      "v_add_f32 v192, v192, v32\n\t"
      "v_add_f32 v193, v193, v33\n\t"
      "v_mfma_f32_16x16x32_bf16 v[12:15], a[88:91], v[116:119], v[12:15]\n\t"
      "v_add_f32 v194, v194, v34\n\t"
      "v_add_f32 v195, v195, v35\n\t"
      "ds_read_b128 v[100:103], v255 offset:25600\n\t"
      "s_waitcnt lgkmcnt(11)\n\t"
      "v_mfma_f32_16x16x32_bf16 v[0:3], a[28:31], v[120:123], v[0:3]\n\t"
      "v_add_f32 v196, v196, v36\n\t"
      "v_add_f32 v197, v197, v37\n\t"
      "v_mfma_f32_16x16x32_bf16 v[4:7], a[92:95], v[120:123], v[4:7]\n\t"
      "v_add_f32 v198, v198, v38\n\t"
      "v_add_f32 v199, v199, v39\n\t"
      "ds_read_b128 v[104:107], v255 offset:26624\n\t"
      "s_waitcnt lgkmcnt(11)\n\t"
      "v_mfma_f32_16x16x32_bf16 v[8:11], a[28:31], v[124:127], v[8:11]\n\t"
      "buffer_load_dwordx4 v[144:147], v253, s[36:39], s52 offen sc1\n\t"
      "v_mfma_f32_16x16x32_bf16 v[12:15], a[92:95], v[124:127], v[12:15]\n\t"
      "v_cvt_pk_bf16_f32 v200, v192, v193\n\t"
      "v_cvt_pk_bf16_f32 v201, v194, v195\n\t"
      "ds_read_b128 v[108:111], v255 offset:27648\n\t"
      "s_waitcnt lgkmcnt(11)\n\t"
      "v_mfma_f32_16x16x32_bf16 v[0:3], a[32:35], v[64:67], v[0:3]\n\t"
      "v_cvt_pk_bf16_f32 v202, v196, v197\n\t"
      "v_cvt_pk_bf16_f32 v203, v198, v199\n\t"
      "v_mfma_f32_16x16x32_bf16 v[4:7], a[96:99], v[64:67], v[4:7]\n\t"
      "v_and_b32 v193, s74, v200\n\t"
      "v_lshlrev_b32 v192, 16, v200\n\t"
      "ds_read_b128 v[112:115], v255 offset:28672\n\t"
      "s_waitcnt lgkmcnt(11)\n\t"
      "v_mfma_f32_16x16x32_bf16 v[8:11], a[32:35], v[68:71], v[8:11]\n\t"
      "v_and_b32 v195, s74, v201\n\t"
      "v_lshlrev_b32 v194, 16, v201\n\t"
      "v_mfma_f32_16x16x32_bf16 v[12:15], a[96:99], v[68:71], v[12:15]\n\t"
      "v_and_b32 v197, s74, v202\n\t"
      "v_lshlrev_b32 v196, 16, v202\n\t"
      "ds_read_b128 v[116:119], v255 offset:29696\n\t"
      "s_waitcnt lgkmcnt(11)\n\t"
      "v_mfma_f32_16x16x32_bf16 v[0:3], a[36:39], v[72:75], v[0:3]\n\t"
      "v_and_b32 v199, s74, v203\n\t"
      "v_lshlrev_b32 v198, 16, v203\n\t"
      "v_mfma_f32_16x16x32_bf16 v[4:7], a[100:103], v[72:75], v[4:7]\n\t"
      "v_add_f32 v32, v192, v196\n\t"
      "v_add_f32 v33, v193, v197\n\t"
      "ds_read_b128 v[120:123], v255 offset:30720\n\t"
      "s_waitcnt lgkmcnt(11)\n\t"
      "v_mfma_f32_16x16x32_bf16 v[8:11], a[36:39], v[76:79], v[8:11]\n\t"
      "buffer_load_dwordx4 v[148:151], v253, s[36:39], s52 offen offset:512 sc1\n\t"
      "v_mfma_f32_16x16x32_bf16 v[12:15], a[100:103], v[76:79], v[12:15]\n\t"
      "v_add_f32 v34, v194, v198\n\t"
      "v_add_f32 v35, v195, v199\n\t"
      "ds_read_b128 v[124:127], v255 offset:31744\n\t"
      "s_waitcnt lgkmcnt(11)\n\t"
      "v_mfma_f32_16x16x32_bf16 v[0:3], a[40:43], v[80:83], v[0:3]\n\t"
      "v_add_f32 v32, v32, v34\n\t"
      "v_add_f32 v33, v33, v35\n\t"
      "v_mfma_f32_16x16x32_bf16 v[4:7], a[104:107], v[80:83], v[4:7]\n\t"
      "v_add_f32 v32, v32, v33\n\t"
      "s_waitcnt lgkmcnt(10)\n\t"
      "v_mfma_f32_16x16x32_bf16 v[8:11], a[40:43], v[84:87], v[8:11]\n\t"
      "v_add_f32_dpp v32, v32, v32 quad_perm:[1,0,3,2] row_mask:0xf bank_mask:0xf\n\t"
      "v_mfma_f32_16x16x32_bf16 v[12:15], a[104:107], v[84:87], v[12:15]\n\t"
      "s_waitcnt lgkmcnt(9)\n\t"
      "v_mfma_f32_16x16x32_bf16 v[0:3], a[44:47], v[88:91], v[0:3]\n\t"
      "v_add_f32_dpp v32, v32, v32 quad_perm:[2,3,0,1] row_mask:0xf bank_mask:0xf\n\t"
      "v_mfma_f32_16x16x32_bf16 v[4:7], a[108:111], v[88:91], v[4:7]\n\t"
      "s_waitcnt lgkmcnt(8)\n\t"
      "v_mfma_f32_16x16x32_bf16 v[8:11], a[44:47], v[92:95], v[8:11]\n\t"
      "buffer_load_dwordx4 v[152:155], v253, s[36:39], s52 offen offset:1024 sc1\n\t"
      "v_mfma_f32_16x16x32_bf16 v[12:15], a[108:111], v[92:95], v[12:15]\n\t"
      "v_add_f32_dpp v32, v32, v32 row_ror:4 row_mask:0xf bank_mask:0xf\n\t"
      "s_waitcnt lgkmcnt(7)\n\t"
      "v_mfma_f32_16x16x32_bf16 v[0:3], a[48:51], v[96:99], v[0:3]\n\t"
      "v_add_f32_dpp v32, v32, v32 row_ror:8 row_mask:0xf bank_mask:0xf\n\t"
      "v_mov_b32 v33, v32\n\t"
      "v_mfma_f32_16x16x32_bf16 v[4:7], a[112:115], v[96:99], v[4:7]\n\t"
      "s_waitcnt lgkmcnt(6)\n\t"
      "v_mfma_f32_16x16x32_bf16 v[8:11], a[48:51], v[100:103], v[8:11]\n\t"
      "v_permlane16_swap_b32 v32, v33\n\t"
      "v_add_f32 v32, v32, v33\n\t"
      "v_mfma_f32_16x16x32_bf16 v[12:15], a[112:115], v[100:103], v[12:15]\n\t"
      "v_mov_b32 v33, v32\n\t"
      "s_waitcnt lgkmcnt(5)\n\t"
      "v_mfma_f32_16x16x32_bf16 v[0:3], a[52:55], v[104:107], v[0:3]\n\t"
      "v_permlane32_swap_b32 v32, v33\n\t"
      "v_add_f32 v32, v32, v33\n\t"
      "v_mfma_f32_16x16x32_bf16 v[4:7], a[116:119], v[104:107], v[4:7]\n\t"
      "v_mul_f32 v204, 0x3b000000, v32\n\t"
      "v_sub_f32 v40, v192, v204\n\t"
      "s_waitcnt lgkmcnt(4)\n\t"
      "v_mfma_f32_16x16x32_bf16 v[8:11], a[52:55], v[108:111], v[8:11]\n\t"
      "v_sub_f32 v41, v193, v204\n\t"
      "v_sub_f32 v42, v194, v204\n\t"
      "v_mfma_f32_16x16x32_bf16 v[12:15], a[116:119], v[108:111], v[12:15]\n\t"
      "buffer_load_dwordx4 v[156:159], v253, s[36:39], s52 offen offset:1536 sc1\n\t"
      "s_waitcnt lgkmcnt(3)\n\t"
      "v_mfma_f32_16x16x32_bf16 v[0:3], a[56:59], v[112:115], v[0:3]\n\t"
      "v_sub_f32 v43, v195, v204\n\t"
      "v_sub_f32 v44, v196, v204\n\t"
      "v_mfma_f32_16x16x32_bf16 v[4:7], a[120:123], v[112:115], v[4:7]\n\t"
      "v_sub_f32 v45, v197, v204\n\t"
      "v_sub_f32 v46, v198, v204\n\t"
      "s_waitcnt lgkmcnt(2)\n\t"
      "v_mfma_f32_16x16x32_bf16 v[8:11], a[56:59], v[116:119], v[8:11]\n\t"
      "v_sub_f32 v47, v199, v204\n\t"
      "v_mul_f32 v32, v40, v40\n\t"
      "v_mfma_f32_16x16x32_bf16 v[12:15], a[120:123], v[116:119], v[12:15]\n\t"
      "v_mul_f32 v33, v41, v41\n\t"
      "v_fma_f32 v32, v42, v42, v32\n\t"
      "s_waitcnt lgkmcnt(1)\n\t"
      "v_mfma_f32_16x16x32_bf16 v[0:3], a[60:63], v[120:123], v[0:3]\n\t"
      "v_fma_f32 v33, v43, v43, v33\n\t"
      "v_fma_f32 v32, v44, v44, v32\n\t"
      "v_mfma_f32_16x16x32_bf16 v[4:7], a[124:127], v[120:123], v[4:7]\n\t"
      "v_fma_f32 v33, v45, v45, v33\n\t"
      "v_fma_f32 v32, v46, v46, v32\n\t"
      "s_waitcnt lgkmcnt(0)\n\t"
      "v_mfma_f32_16x16x32_bf16 v[8:11], a[60:63], v[124:127], v[8:11]\n\t"
      "v_fma_f32 v33, v47, v47, v33\n\t"
      "v_add_f32 v32, v32, v33\n\t"
      "v_mfma_f32_16x16x32_bf16 v[12:15], a[124:127], v[124:127], v[12:15]\n\t"
      "s_nop 0\n\t"
      "v_add_f32_dpp v32, v32, v32 quad_perm:[1,0,3,2] row_mask:0xf bank_mask:0xf\n\t"
      "s_nop 1\n\t"
      "v_add_f32_dpp v32, v32, v32 quad_perm:[2,3,0,1] row_mask:0xf bank_mask:0xf\n\t"
      "s_nop 1\n\t"
      "v_add_f32_dpp v32, v32, v32 row_ror:4 row_mask:0xf bank_mask:0xf\n\t"
      "s_nop 1\n\t"
      "v_add_f32_dpp v32, v32, v32 row_ror:8 row_mask:0xf bank_mask:0xf\n\t"
      "v_mov_b32 v33, v32\n\t"
      "s_nop 1\n\t"
      "v_permlane16_swap_b32 v32, v33\n\t"
      "v_add_f32 v32, v32, v33\n\t"
      "v_mov_b32 v33, v32\n\t"
      "s_nop 1\n\t"
      "v_permlane32_swap_b32 v32, v33\n\t"
      "v_add_f32 v32, v32, v33\n\t"
      "v_mul_f32 v205, 0x3b000000, v32\n\t"
      "v_xor_b32 v254, 0x8000, v254\n\t"
      "v_xor_b32 v255, 0x8000, v255\n\t"
      "s_add_u32 s69, s53, 0x4000\n\t"
      "s_add_u32 s48, s48, 4\n\t"
      "s_waitcnt vmcnt(40)\n\t"
      "v_cvt_pk_bf16_f32 v0, v0, v1\n\t"
      "v_cvt_pk_bf16_f32 v1, v2, v3\n\t"
      "v_cvt_pk_bf16_f32 v2, v4, v5\n\t"
      "v_cvt_pk_bf16_f32 v3, v6, v7\n\t"
      "v_min_u32 v0, -2, v0\n\t"
      "v_min_u32 v1, -2, v1\n\t"
      "v_min_u32 v2, -2, v2\n\t"
      "v_min_u32 v3, -2, v3\n\t"
      "buffer_store_dwordx4 v[0:3], v238, s[36:39], s53 offen sc1\n\t"
      "v_cvt_pk_bf16_f32 v8, v8, v9\n\t"
      "v_cvt_pk_bf16_f32 v9, v10, v11\n\t"
      "v_cvt_pk_bf16_f32 v10, v12, v13\n\t"
      "v_cvt_pk_bf16_f32 v11, v14, v15\n\t"
      "v_min_u32 v8, -2, v8\n\t"
      "v_min_u32 v9, -2, v9\n\t"
      "v_min_u32 v10, -2, v10\n\t"
      "v_min_u32 v11, -2, v11\n\t"
      "buffer_store_dwordx4 v[8:11], v238, s[36:39], s69 offen sc1\n\t"
      "s_add_u32 s53, s53, 1048576\n\t"
      "s_nop 1\n\t"
      "s_branch WTOP%=\n\t"
      ".p2align 5\n\t"
      "WOM2%=:\n\t"
      "s_mov_b32 s65, 0\n\t"
      "s_add_u32 s24, s96, 8\n\t"
      "s_cmp_lg_u32 s96, 0\n\t"
      "s_cbranch_scc1 WOL5%=\n\t"
      "WOL4%=:\n\t"
      "s_waitcnt lgkmcnt(11)\n\t"
      "v_mfma_f32_16x16x32_bf16 v[0:3], a[0:3], v[64:67], 0\n\t"
      "v_cvt_pk_bf16_f32 v204, v204, v205\n\t"
      "v_and_b32 v205, s74, v204\n\t"
      "v_mfma_f32_16x16x32_bf16 v[4:7], a[64:67], v[64:67], 0\n\t"
      "v_lshlrev_b32 v204, 16, v204\n\t"
      "v_add_f32 v205, 0x3727c5ac, v205\n\t"
      "ds_read_b128 v[112:115], v255 offset:12288\n\t"
      "s_waitcnt lgkmcnt(11)\n\t"
      "v_mfma_f32_16x16x32_bf16 v[8:11], a[0:3], v[68:71], 0\n\t"
      "v_sub_f32 v40, v192, v204\n\t"
      "v_sub_f32 v41, v193, v204\n\t"
      "v_mfma_f32_16x16x32_bf16 v[12:15], a[64:67], v[68:71], 0\n\t"
      "v_sub_f32 v42, v194, v204\n\t"
      "v_sub_f32 v43, v195, v204\n\t"
      "ds_read_b128 v[116:119], v255 offset:13312\n\t"
      "s_waitcnt lgkmcnt(11)\n\t"
      "v_mfma_f32_16x16x32_bf16 v[0:3], a[4:7], v[72:75], v[0:3]\n\t"
      "v_sub_f32 v44, v196, v204\n\t"
      "v_sub_f32 v45, v197, v204\n\t"
      "v_mfma_f32_16x16x32_bf16 v[4:7], a[68:71], v[72:75], v[4:7]\n\t"
      "v_sub_f32 v46, v198, v204\n\t"
      "v_sub_f32 v47, v199, v204\n\t"
      "ds_read_b128 v[120:123], v255 offset:14336\n\t"
      "s_waitcnt lgkmcnt(11)\n\t"
      "v_mfma_f32_16x16x32_bf16 v[8:11], a[4:7], v[76:79], v[8:11]\n\t"
      "v_cvt_pk_bf16_f32 v205, v205, v205\n\t"
      "v_and_b32 v205, s74, v205\n\t"
      "v_mfma_f32_16x16x32_bf16 v[12:15], a[68:71], v[76:79], v[12:15]\n\t"
      "v_sqrt_f32 v205, v205\n\t"
      "ds_read_b128 v[124:127], v255 offset:15360\n\t"
      "s_waitcnt lgkmcnt(11)\n\t"
      "v_mfma_f32_16x16x32_bf16 v[0:3], a[8:11], v[80:83], v[0:3]\n\t"
      "v_cvt_pk_bf16_f32 v40, v40, v41\n\t"
      "v_cvt_pk_bf16_f32 v42, v42, v43\n\t"
      "v_mfma_f32_16x16x32_bf16 v[4:7], a[72:75], v[80:83], v[4:7]\n\t"
      "v_cvt_pk_bf16_f32 v44, v44, v45\n\t"
      "v_cvt_pk_bf16_f32 v46, v46, v47\n\t"
      "ds_read_b128 v[64:67], v255 offset:16384\n\t"
      "s_waitcnt lgkmcnt(11)\n\t"
      "v_mfma_f32_16x16x32_bf16 v[8:11], a[8:11], v[84:87], v[8:11]\n\t"
      "v_cvt_pk_bf16_f32 v205, v205, v205\n\t"
      "v_and_b32 v205, s74, v205\n\t"
      "v_mfma_f32_16x16x32_bf16 v[12:15], a[72:75], v[84:87], v[12:15]\n\t"
      "v_rcp_f32 v204, v205\n\t"
      "ds_read_b128 v[68:71], v255 offset:17408\n\t"
      "s_waitcnt lgkmcnt(11)\n\t"
      "v_mfma_f32_16x16x32_bf16 v[0:3], a[12:15], v[88:91], v[0:3]\n\t"
      "v_and_b32 v41, s74, v40\n\t"
      "v_lshlrev_b32 v40, 16, v40\n\t"
      "v_mfma_f32_16x16x32_bf16 v[4:7], a[76:79], v[88:91], v[4:7]\n\t"
      "v_and_b32 v43, s74, v42\n\t"
      "v_lshlrev_b32 v42, 16, v42\n\t"
      "ds_read_b128 v[72:75], v255 offset:18432\n\t"
      "s_waitcnt lgkmcnt(11)\n\t"
      "v_mfma_f32_16x16x32_bf16 v[8:11], a[12:15], v[92:95], v[8:11]\n\t"
      "v_and_b32 v45, s74, v44\n\t"
      "v_lshlrev_b32 v44, 16, v44\n\t"
      "v_mfma_f32_16x16x32_bf16 v[12:15], a[76:79], v[92:95], v[12:15]\n\t"
      "v_and_b32 v47, s74, v46\n\t"
      "v_lshlrev_b32 v46, 16, v46\n\t"
      "ds_read_b128 v[76:79], v255 offset:19456\n\t"
      "s_waitcnt lgkmcnt(11)\n\t"
      "v_mfma_f32_16x16x32_bf16 v[0:3], a[16:19], v[96:99], v[0:3]\n\t"
      "v_mul_f32 v40, v40, v204\n\t"
      "v_mul_f32 v41, v41, v204\n\t"
      "v_mfma_f32_16x16x32_bf16 v[4:7], a[80:83], v[96:99], v[4:7]\n\t"
      "v_mul_f32 v42, v42, v204\n\t"
      "v_mul_f32 v43, v43, v204\n\t"
      "ds_read_b128 v[80:83], v255 offset:20480\n\t"
      "s_waitcnt lgkmcnt(11)\n\t"
      "v_mfma_f32_16x16x32_bf16 v[8:11], a[16:19], v[100:103], v[8:11]\n\t"
      "v_mul_f32 v44, v44, v204\n\t"
      "v_mul_f32 v45, v45, v204\n\t"
      "v_mfma_f32_16x16x32_bf16 v[12:15], a[80:83], v[100:103], v[12:15]\n\t"
      "v_mul_f32 v46, v46, v204\n\t"
      "v_mul_f32 v47, v47, v204\n\t"
      "ds_read_b128 v[84:87], v255 offset:21504\n\t"
      "s_waitcnt lgkmcnt(11)\n\t"
      "v_mfma_f32_16x16x32_bf16 v[0:3], a[20:23], v[104:107], v[0:3]\n\t"
      "v_cvt_pk_bf16_f32 v52, v40, v41\n\t"
      "v_cvt_pk_bf16_f32 v53, v42, v43\n\t"
      "v_mfma_f32_16x16x32_bf16 v[4:7], a[84:87], v[104:107], v[4:7]\n\t"
      "v_cvt_pk_bf16_f32 v54, v44, v45\n\t"
      "v_cvt_pk_bf16_f32 v55, v46, v47\n\t"
      "ds_read_b128 v[88:91], v255 offset:22528\n\t"
      "s_waitcnt lgkmcnt(11)\n\t"
      "v_mfma_f32_16x16x32_bf16 v[8:11], a[20:23], v[108:111], v[8:11]\n\t"
      "v_and_b32 v33, s74, v52\n\t"
      "v_lshlrev_b32 v32, 16, v52\n\t"
      "v_mfma_f32_16x16x32_bf16 v[12:15], a[84:87], v[108:111], v[12:15]\n\t"
      "v_and_b32 v35, s74, v53\n\t"
      "v_lshlrev_b32 v34, 16, v53\n\t"
      "ds_read_b128 v[92:95], v255 offset:23552\n\t"
      "s_waitcnt lgkmcnt(11)\n\t"
      "v_mfma_f32_16x16x32_bf16 v[0:3], a[24:27], v[112:115], v[0:3]\n\t"
      "v_and_b32 v37, s74, v54\n\t"
      "v_lshlrev_b32 v36, 16, v54\n\t"
      "v_mfma_f32_16x16x32_bf16 v[4:7], a[88:91], v[112:115], v[4:7]\n\t"
      "v_and_b32 v39, s74, v55\n\t"
      "v_lshlrev_b32 v38, 16, v55\n\t"
      "ds_read_b128 v[96:99], v255 offset:24576\n\t"
      "s_waitcnt lgkmcnt(11)\n\t"
      "v_mfma_f32_16x16x32_bf16 v[8:11], a[24:27], v[116:119], v[8:11]\n\t"
      "v_mul_f32 v40, v32, v240\n\t"
      "v_mul_f32 v41, v33, v241\n\t"
      "v_mfma_f32_16x16x32_bf16 v[12:15], a[88:91], v[116:119], v[12:15]\n\t"
      "v_fma_f32 v40, v34, v242, v40\n\t"
      "v_fma_f32 v41, v35, v243, v41\n\t"
      "ds_read_b128 v[100:103], v255 offset:25600\n\t"
      "s_waitcnt lgkmcnt(11)\n\t"
      "v_mfma_f32_16x16x32_bf16 v[0:3], a[28:31], v[120:123], v[0:3]\n\t"
      "v_fma_f32 v40, v36, v244, v40\n\t"
      "v_fma_f32 v41, v37, v245, v41\n\t"
      "v_mfma_f32_16x16x32_bf16 v[4:7], a[92:95], v[120:123], v[4:7]\n\t"
      "v_fma_f32 v40, v38, v246, v40\n\t"
      "v_fma_f32 v41, v39, v247, v41\n\t"
      "ds_read_b128 v[104:107], v255 offset:26624\n\t"
      "s_waitcnt lgkmcnt(11)\n\t"
      "v_mfma_f32_16x16x32_bf16 v[8:11], a[28:31], v[124:127], v[8:11]\n\t"
      "v_add_f32 v40, v40, v41\n\t"
      "s_lshl_b32 s73, s49, 5\n\t"
      "s_add_u32 s73, s73, s60\n\t"
      "s_lshl_b32 s69, s73, 2\n\t"
      "s_add_u32 s69, s69, s57\n\t"
      "s_lshl_b32 s70, s73, 7\n\t"
      "s_add_u32 s70, s70, s57\n\t"
      "s_sub_u32 s70, s70, 212992\n\t"
      "s_cmp_ge_u32 s49, 56\n\t"
      "s_cselect_b32 s70, s70, s69\n\t"
      "v_add_f32_dpp v40, v40, v40 quad_perm:[1,0,3,2] row_mask:0xf bank_mask:0xf\n\t"
      "v_mfma_f32_16x16x32_bf16 v[12:15], a[92:95], v[124:127], v[12:15]\n\t"
      "ds_read_b128 v[108:111], v255 offset:27648\n\t"
      "s_waitcnt lgkmcnt(11)\n\t"
      "v_mfma_f32_16x16x32_bf16 v[0:3], a[32:35], v[64:67], v[0:3]\n\t"
      "v_add_f32_dpp v40, v40, v40 quad_perm:[2,3,0,1] row_mask:0xf bank_mask:0xf\n\t"
      "v_mfma_f32_16x16x32_bf16 v[4:7], a[96:99], v[64:67], v[4:7]\n\t"
      "ds_read_b128 v[112:115], v255 offset:28672\n\t"
      "s_waitcnt lgkmcnt(11)\n\t"
      "v_mfma_f32_16x16x32_bf16 v[8:11], a[32:35], v[68:71], v[8:11]\n\t"
      "v_add_f32_dpp v40, v40, v40 row_ror:4 row_mask:0xf bank_mask:0xf\n\t"
      "v_mfma_f32_16x16x32_bf16 v[12:15], a[96:99], v[68:71], v[12:15]\n\t"
      "ds_read_b128 v[116:119], v255 offset:29696\n\t"
      "s_waitcnt lgkmcnt(11)\n\t"
      "v_mfma_f32_16x16x32_bf16 v[0:3], a[36:39], v[72:75], v[0:3]\n\t"
      "v_add_f32_dpp v40, v40, v40 row_ror:8 row_mask:0xf bank_mask:0xf\n\t"
      "v_mov_b32 v41, v40\n\t"
      "v_mfma_f32_16x16x32_bf16 v[4:7], a[100:103], v[72:75], v[4:7]\n\t"
      "ds_read_b128 v[120:123], v255 offset:30720\n\t"
      "s_waitcnt lgkmcnt(11)\n\t"
      "v_mfma_f32_16x16x32_bf16 v[8:11], a[36:39], v[76:79], v[8:11]\n\t"
      "v_permlane16_swap_b32 v40, v41\n\t"
      "v_add_f32 v40, v40, v41\n\t"
      "v_mfma_f32_16x16x32_bf16 v[12:15], a[100:103], v[76:79], v[12:15]\n\t"
      "v_mov_b32 v41, v40\n\t"
      "ds_read_b128 v[124:127], v255 offset:31744\n\t"
      "s_waitcnt lgkmcnt(11)\n\t"
      "v_mfma_f32_16x16x32_bf16 v[0:3], a[40:43], v[80:83], v[0:3]\n\t"
      "v_permlane32_swap_b32 v40, v41\n\t"
      "v_add_f32 v40, v40, v41\n\t"
      "v_mfma_f32_16x16x32_bf16 v[4:7], a[104:107], v[80:83], v[4:7]\n\t"
      "v_cvt_pk_bf16_f32 v40, v40, v40\n\t"
      "v_and_b32 v40, s74, v40\n\t"
      "s_waitcnt lgkmcnt(10)\n\t"
      "v_mfma_f32_16x16x32_bf16 v[8:11], a[40:43], v[84:87], v[8:11]\n\t"
      "s_mov_b64 exec, 1\n\t"
      "buffer_store_dword v40, off, s[36:39], s70 sc1\n\t"
      "s_mov_b64 exec, -1\n\t"
      "v_add_f32 v192, v192, v32\n\t"
      "v_add_f32 v193, v193, v33\n\t"
      "v_mfma_f32_16x16x32_bf16 v[12:15], a[104:107], v[84:87], v[12:15]\n\t"
      "v_add_f32 v194, v194, v34\n\t"
      "v_add_f32 v195, v195, v35\n\t"
      "s_waitcnt lgkmcnt(9)\n\t"
      "v_mfma_f32_16x16x32_bf16 v[0:3], a[44:47], v[88:91], v[0:3]\n\t"
      "v_add_f32 v196, v196, v36\n\t"
      "v_add_f32 v197, v197, v37\n\t"
      "v_mfma_f32_16x16x32_bf16 v[4:7], a[108:111], v[88:91], v[4:7]\n\t"
      "v_add_f32 v198, v198, v38\n\t"
      "v_add_f32 v199, v199, v39\n\t"
      "s_waitcnt lgkmcnt(8)\n\t"
      "v_mfma_f32_16x16x32_bf16 v[8:11], a[44:47], v[92:95], v[8:11]\n\t"
      "v_readfirstlane_b32 s68, v40\n\t"
      "v_cvt_pk_bf16_f32 v56, v192, v193\n\t"
      "v_mfma_f32_16x16x32_bf16 v[12:15], a[108:111], v[92:95], v[12:15]\n\t"
      "v_cvt_pk_bf16_f32 v57, v194, v195\n\t"
      "v_cvt_pk_bf16_f32 v58, v196, v197\n\t"
      "s_waitcnt lgkmcnt(7)\n\t"
      "v_mfma_f32_16x16x32_bf16 v[0:3], a[48:51], v[96:99], v[0:3]\n\t"
      "v_cvt_pk_bf16_f32 v59, v198, v199\n\t"
      "s_add_u32 s67, s69, 8192\n\t"
      "s_lshl_b32 s73, s73, 10\n\t"
      "s_add_u32 s69, s73, s58\n\t"
      "s_add_u32 s70, s73, s59\n\t"
      "buffer_store_dwordx2 v[56:57], v249, s[44:47], s73 offen sc1\n\t"
      "v_mfma_f32_16x16x32_bf16 v[4:7], a[112:115], v[96:99], v[4:7]\n\t"
      "buffer_store_dwordx2 v[52:53], v249, s[36:39], s69 offen sc1\n\t"
      "buffer_store_dwordx2 v[200:201], v249, s[36:39], s70 offen sc1\n\t"
      "s_waitcnt lgkmcnt(6)\n\t"
      "v_mfma_f32_16x16x32_bf16 v[8:11], a[48:51], v[100:103], v[8:11]\n\t"
      "buffer_store_dwordx2 v[58:59], v249, s[44:47], s73 offen offset:512 sc1\n\t"
      "buffer_store_dwordx2 v[54:55], v249, s[36:39], s69 offen offset:512 sc1\n\t"
      "v_mfma_f32_16x16x32_bf16 v[12:15], a[112:115], v[100:103], v[12:15]\n\t"
      "buffer_store_dwordx2 v[202:203], v249, s[36:39], s70 offen offset:512 sc1\n\t"
      "s_mov_b32 s49, s89\n\t"
      "s_add_u32 s89, s89, 16\n\t"
      "s_lshr_b32 s69, s89, 4\n\t"
      "s_cmp_eq_u32 s69, 3\n\t"
      "s_cselect_b32 s89, s93, s89\n\t"
      "s_cmp_ge_u32 s49, 48\n\t"
      "s_cselect_b32 s60, s94, s60\n\t"
      "s_cselect_b32 s54, s95, s54\n\t"
      "s_waitcnt lgkmcnt(5)\n\t"
      "v_mfma_f32_16x16x32_bf16 v[0:3], a[52:55], v[104:107], v[0:3]\n\t"
      "v_mfma_f32_16x16x32_bf16 v[4:7], a[116:119], v[104:107], v[4:7]\n\t"
      "s_waitcnt lgkmcnt(4)\n\t"
      "v_mfma_f32_16x16x32_bf16 v[8:11], a[52:55], v[108:111], v[8:11]\n\t"
      "v_mfma_f32_16x16x32_bf16 v[12:15], a[116:119], v[108:111], v[12:15]\n\t"
      "s_waitcnt lgkmcnt(3)\n\t"
      "v_mfma_f32_16x16x32_bf16 v[0:3], a[56:59], v[112:115], v[0:3]\n\t"
      "v_mfma_f32_16x16x32_bf16 v[4:7], a[120:123], v[112:115], v[4:7]\n\t"
      "s_waitcnt lgkmcnt(2)\n\t"
      "v_mfma_f32_16x16x32_bf16 v[8:11], a[56:59], v[116:119], v[8:11]\n\t"
      "v_mfma_f32_16x16x32_bf16 v[12:15], a[120:123], v[116:119], v[12:15]\n\t"
      "s_waitcnt lgkmcnt(1)\n\t"
      "v_mfma_f32_16x16x32_bf16 v[0:3], a[60:63], v[120:123], v[0:3]\n\t"
      "v_mfma_f32_16x16x32_bf16 v[4:7], a[124:127], v[120:123], v[4:7]\n\t"
      "s_waitcnt lgkmcnt(0)\n\t"
      "v_mfma_f32_16x16x32_bf16 v[8:11], a[60:63], v[124:127], v[8:11]\n\t"
      "v_mfma_f32_16x16x32_bf16 v[12:15], a[124:127], v[124:127], v[12:15]\n\t"
      "v_xor_b32 v254, 0x8000, v254\n\t"
      "v_xor_b32 v255, 0x8000, v255\n\t"
      "s_add_u32 s69, s53, 0x4000\n\t"
      "s_add_u32 s48, s48, 4\n\t"
      "s_waitcnt vmcnt(40)\n\t"
      "v_cvt_pk_bf16_f32 v0, v0, v1\n\t"
      "v_cvt_pk_bf16_f32 v1, v2, v3\n\t"
      "v_cvt_pk_bf16_f32 v2, v4, v5\n\t"
      "v_cvt_pk_bf16_f32 v3, v6, v7\n\t"
      "v_min_u32 v0, -2, v0\n\t"
      "v_min_u32 v1, -2, v1\n\t"
      "v_min_u32 v2, -2, v2\n\t"
      "v_min_u32 v3, -2, v3\n\t"
      "buffer_store_dwordx4 v[0:3], v238, s[36:39], s53 offen sc1\n\t"
      "v_cvt_pk_bf16_f32 v8, v8, v9\n\t"
      "v_cvt_pk_bf16_f32 v9, v10, v11\n\t"
      "v_cvt_pk_bf16_f32 v10, v12, v13\n\t"
      "v_cvt_pk_bf16_f32 v11, v14, v15\n\t"
      "v_min_u32 v8, -2, v8\n\t"
      "v_min_u32 v9, -2, v9\n\t"
      "v_min_u32 v10, -2, v10\n\t"
      "v_min_u32 v11, -2, v11\n\t"
      "buffer_store_dwordx4 v[8:11], v238, s[36:39], s69 offen sc1\n\t"
      "s_add_u32 s53, s53, 1048576\n\t"
      "s_nop 1\n\t"
      "s_branch WTOP%=\n\t"
      ".p2align 5\n\t"
      "WOL5%=:\n\t"
      "buffer_load_dwordx4 v[128:131], v252, s[36:39], s51 offen sc1\n\t"
      "s_waitcnt lgkmcnt(11)\n\t"
      "v_mfma_f32_16x16x32_bf16 v[0:3], a[0:3], v[64:67], 0\n\t"
      "v_cvt_pk_bf16_f32 v204, v204, v205\n\t"
      "v_and_b32 v205, s74, v204\n\t"
      "v_mfma_f32_16x16x32_bf16 v[4:7], a[64:67], v[64:67], 0\n\t"
      "v_lshlrev_b32 v204, 16, v204\n\t"
      "v_add_f32 v205, 0x3727c5ac, v205\n\t"
      "ds_read_b128 v[112:115], v255 offset:12288\n\t"
      "s_waitcnt lgkmcnt(11)\n\t"
      "v_mfma_f32_16x16x32_bf16 v[8:11], a[0:3], v[68:71], 0\n\t"
      "v_sub_f32 v40, v192, v204\n\t"
      "v_sub_f32 v41, v193, v204\n\t"
      "v_mfma_f32_16x16x32_bf16 v[12:15], a[64:67], v[68:71], 0\n\t"
      "v_sub_f32 v42, v194, v204\n\t"
      "v_sub_f32 v43, v195, v204\n\t"
      "ds_read_b128 v[116:119], v255 offset:13312\n\t"
      "s_waitcnt lgkmcnt(11)\n\t"
      "v_mfma_f32_16x16x32_bf16 v[0:3], a[4:7], v[72:75], v[0:3]\n\t"
      "v_sub_f32 v44, v196, v204\n\t"
      "v_sub_f32 v45, v197, v204\n\t"
      "v_mfma_f32_16x16x32_bf16 v[4:7], a[68:71], v[72:75], v[4:7]\n\t"
      "v_sub_f32 v46, v198, v204\n\t"
      "v_sub_f32 v47, v199, v204\n\t"
      "ds_read_b128 v[120:123], v255 offset:14336\n\t"
      "s_waitcnt lgkmcnt(11)\n\t"
      "v_mfma_f32_16x16x32_bf16 v[8:11], a[4:7], v[76:79], v[8:11]\n\t"
      "v_cvt_pk_bf16_f32 v205, v205, v205\n\t"
      "v_and_b32 v205, s74, v205\n\t"
      "v_mfma_f32_16x16x32_bf16 v[12:15], a[68:71], v[76:79], v[12:15]\n\t"
      "buffer_load_dwordx4 v[132:135], v252, s[36:39], s51 offen offset:512 sc1\n\t"
      "ds_read_b128 v[124:127], v255 offset:15360\n\t"
      "s_waitcnt lgkmcnt(11)\n\t"
      "v_mfma_f32_16x16x32_bf16 v[0:3], a[8:11], v[80:83], v[0:3]\n\t"
      "v_sqrt_f32 v205, v205\n\t"
      "v_mfma_f32_16x16x32_bf16 v[4:7], a[72:75], v[80:83], v[4:7]\n\t"
      "v_cvt_pk_bf16_f32 v40, v40, v41\n\t"
      "v_cvt_pk_bf16_f32 v42, v42, v43\n\t"
      "ds_read_b128 v[64:67], v255 offset:16384\n\t"
      "s_waitcnt lgkmcnt(11)\n\t"
      "v_mfma_f32_16x16x32_bf16 v[8:11], a[8:11], v[84:87], v[8:11]\n\t"
      "v_cvt_pk_bf16_f32 v44, v44, v45\n\t"
      "v_cvt_pk_bf16_f32 v46, v46, v47\n\t"
      "v_mfma_f32_16x16x32_bf16 v[12:15], a[72:75], v[84:87], v[12:15]\n\t"
      "v_cvt_pk_bf16_f32 v205, v205, v205\n\t"
      "v_and_b32 v205, s74, v205\n\t"
      "ds_read_b128 v[68:71], v255 offset:17408\n\t"
      "s_waitcnt lgkmcnt(11)\n\t"
      "v_mfma_f32_16x16x32_bf16 v[0:3], a[12:15], v[88:91], v[0:3]\n\t"
      "v_rcp_f32 v204, v205\n\t"
      "v_mfma_f32_16x16x32_bf16 v[4:7], a[76:79], v[88:91], v[4:7]\n\t"
      "v_and_b32 v41, s74, v40\n\t"
      "v_lshlrev_b32 v40, 16, v40\n\t"
      "ds_read_b128 v[72:75], v255 offset:18432\n\t"
      "s_waitcnt lgkmcnt(11)\n\t"
      "v_mfma_f32_16x16x32_bf16 v[8:11], a[12:15], v[92:95], v[8:11]\n\t"
      "v_and_b32 v43, s74, v42\n\t"
      "v_lshlrev_b32 v42, 16, v42\n\t"
      "v_mfma_f32_16x16x32_bf16 v[12:15], a[76:79], v[92:95], v[12:15]\n\t"
      "buffer_load_dwordx4 v[136:139], v252, s[36:39], s51 offen offset:1024 sc1\n\t"
      "ds_read_b128 v[76:79], v255 offset:19456\n\t"
      "s_waitcnt lgkmcnt(11)\n\t"
      "v_mfma_f32_16x16x32_bf16 v[0:3], a[16:19], v[96:99], v[0:3]\n\t"
      "v_and_b32 v45, s74, v44\n\t"
      "v_lshlrev_b32 v44, 16, v44\n\t"
      "v_mfma_f32_16x16x32_bf16 v[4:7], a[80:83], v[96:99], v[4:7]\n\t"
      "v_and_b32 v47, s74, v46\n\t"
      "v_lshlrev_b32 v46, 16, v46\n\t"
      "ds_read_b128 v[80:83], v255 offset:20480\n\t"
      "s_waitcnt lgkmcnt(11)\n\t"
      "v_mfma_f32_16x16x32_bf16 v[8:11], a[16:19], v[100:103], v[8:11]\n\t"
      "v_mul_f32 v40, v40, v204\n\t"
      "v_mul_f32 v41, v41, v204\n\t"
      "v_mfma_f32_16x16x32_bf16 v[12:15], a[80:83], v[100:103], v[12:15]\n\t"
      "v_mul_f32 v42, v42, v204\n\t"
      "v_mul_f32 v43, v43, v204\n\t"
      "ds_read_b128 v[84:87], v255 offset:21504\n\t"
      "s_waitcnt lgkmcnt(11)\n\t"
      "v_mfma_f32_16x16x32_bf16 v[0:3], a[20:23], v[104:107], v[0:3]\n\t"
      "v_mul_f32 v44, v44, v204\n\t"
      "v_mul_f32 v45, v45, v204\n\t"
      "v_mfma_f32_16x16x32_bf16 v[4:7], a[84:87], v[104:107], v[4:7]\n\t"
      "v_mul_f32 v46, v46, v204\n\t"
      "v_mul_f32 v47, v47, v204\n\t"
      "ds_read_b128 v[88:91], v255 offset:22528\n\t"
      "s_waitcnt lgkmcnt(11)\n\t"
      "v_mfma_f32_16x16x32_bf16 v[8:11], a[20:23], v[108:111], v[8:11]\n\t"
      "v_cvt_pk_bf16_f32 v52, v40, v41\n\t"
      "v_cvt_pk_bf16_f32 v53, v42, v43\n\t"
      "v_mfma_f32_16x16x32_bf16 v[12:15], a[84:87], v[108:111], v[12:15]\n\t"
      "buffer_load_dwordx4 v[140:143], v252, s[36:39], s51 offen offset:1536 sc1\n\t"
      "ds_read_b128 v[92:95], v255 offset:23552\n\t"
      "s_waitcnt lgkmcnt(11)\n\t"
      "v_mfma_f32_16x16x32_bf16 v[0:3], a[24:27], v[112:115], v[0:3]\n\t"
      "v_cvt_pk_bf16_f32 v54, v44, v45\n\t"
      "v_cvt_pk_bf16_f32 v55, v46, v47\n\t"
      "v_mfma_f32_16x16x32_bf16 v[4:7], a[88:91], v[112:115], v[4:7]\n\t"
      "v_and_b32 v33, s74, v52\n\t"
      "v_lshlrev_b32 v32, 16, v52\n\t"
      "ds_read_b128 v[96:99], v255 offset:24576\n\t"
      "s_waitcnt lgkmcnt(11)\n\t"
      "v_mfma_f32_16x16x32_bf16 v[8:11], a[24:27], v[116:119], v[8:11]\n\t"
      "v_and_b32 v35, s74, v53\n\t"
      "v_lshlrev_b32 v34, 16, v53\n\t"
      "v_mfma_f32_16x16x32_bf16 v[12:15], a[88:91], v[116:119], v[12:15]\n\t"
      "v_and_b32 v37, s74, v54\n\t"
      "v_lshlrev_b32 v36, 16, v54\n\t"
      "ds_read_b128 v[100:103], v255 offset:25600\n\t"
      "s_waitcnt lgkmcnt(11)\n\t"
      "v_mfma_f32_16x16x32_bf16 v[0:3], a[28:31], v[120:123], v[0:3]\n\t"
      "v_and_b32 v39, s74, v55\n\t"
      "v_lshlrev_b32 v38, 16, v55\n\t"
      "v_mfma_f32_16x16x32_bf16 v[4:7], a[92:95], v[120:123], v[4:7]\n\t"
      "v_mul_f32 v40, v32, v240\n\t"
      "v_mul_f32 v41, v33, v241\n\t"
      "ds_read_b128 v[104:107], v255 offset:26624\n\t"
      "s_waitcnt lgkmcnt(11)\n\t"
      "v_mfma_f32_16x16x32_bf16 v[8:11], a[28:31], v[124:127], v[8:11]\n\t"
      "buffer_load_dwordx4 v[144:147], v253, s[36:39], s52 offen sc1\n\t"
      "v_mfma_f32_16x16x32_bf16 v[12:15], a[92:95], v[124:127], v[12:15]\n\t"
      "v_fma_f32 v40, v34, v242, v40\n\t"
      "v_fma_f32 v41, v35, v243, v41\n\t"
      "ds_read_b128 v[108:111], v255 offset:27648\n\t"
      "s_waitcnt lgkmcnt(11)\n\t"
      "v_mfma_f32_16x16x32_bf16 v[0:3], a[32:35], v[64:67], v[0:3]\n\t"
      "v_fma_f32 v40, v36, v244, v40\n\t"
      "v_fma_f32 v41, v37, v245, v41\n\t"
      "v_mfma_f32_16x16x32_bf16 v[4:7], a[96:99], v[64:67], v[4:7]\n\t"
      "v_fma_f32 v40, v38, v246, v40\n\t"
      "v_fma_f32 v41, v39, v247, v41\n\t"
      "ds_read_b128 v[112:115], v255 offset:28672\n\t"
      "s_waitcnt lgkmcnt(11)\n\t"
      "v_mfma_f32_16x16x32_bf16 v[8:11], a[32:35], v[68:71], v[8:11]\n\t"
      "v_add_f32 v40, v40, v41\n\t"
      "s_lshl_b32 s73, s49, 5\n\t"
      "s_add_u32 s73, s73, s60\n\t"
      "s_lshl_b32 s69, s73, 2\n\t"
      "s_add_u32 s69, s69, s57\n\t"
      "s_lshl_b32 s70, s73, 7\n\t"
      "s_add_u32 s70, s70, s57\n\t"
      "s_sub_u32 s70, s70, 212992\n\t"
      "s_cmp_ge_u32 s49, 56\n\t"
      "s_cselect_b32 s70, s70, s69\n\t"
      "v_add_f32_dpp v40, v40, v40 quad_perm:[1,0,3,2] row_mask:0xf bank_mask:0xf\n\t"
      "v_mfma_f32_16x16x32_bf16 v[12:15], a[96:99], v[68:71], v[12:15]\n\t"
      "ds_read_b128 v[116:119], v255 offset:29696\n\t"
      "s_waitcnt lgkmcnt(11)\n\t"
      "v_mfma_f32_16x16x32_bf16 v[0:3], a[36:39], v[72:75], v[0:3]\n\t"
      "v_add_f32_dpp v40, v40, v40 quad_perm:[2,3,0,1] row_mask:0xf bank_mask:0xf\n\t"
      "v_mfma_f32_16x16x32_bf16 v[4:7], a[100:103], v[72:75], v[4:7]\n\t"
      "ds_read_b128 v[120:123], v255 offset:30720\n\t"
      "s_waitcnt lgkmcnt(11)\n\t"
      "v_mfma_f32_16x16x32_bf16 v[8:11], a[36:39], v[76:79], v[8:11]\n\t"
      "buffer_load_dwordx4 v[148:151], v253, s[36:39], s52 offen offset:512 sc1\n\t"
      "v_mfma_f32_16x16x32_bf16 v[12:15], a[100:103], v[76:79], v[12:15]\n\t"
      "v_add_f32_dpp v40, v40, v40 row_ror:4 row_mask:0xf bank_mask:0xf\n\t"
      "ds_read_b128 v[124:127], v255 offset:31744\n\t"
      "s_waitcnt lgkmcnt(11)\n\t"
      "v_mfma_f32_16x16x32_bf16 v[0:3], a[40:43], v[80:83], v[0:3]\n\t"
      "v_add_f32_dpp v40, v40, v40 row_ror:8 row_mask:0xf bank_mask:0xf\n\t"
      "v_mov_b32 v41, v40\n\t"
      "v_mfma_f32_16x16x32_bf16 v[4:7], a[104:107], v[80:83], v[4:7]\n\t"
      "s_waitcnt lgkmcnt(10)\n\t"
      "v_mfma_f32_16x16x32_bf16 v[8:11], a[40:43], v[84:87], v[8:11]\n\t"
      "v_permlane16_swap_b32 v40, v41\n\t"
      "v_add_f32 v40, v40, v41\n\t"
      "v_mfma_f32_16x16x32_bf16 v[12:15], a[104:107], v[84:87], v[12:15]\n\t"
      "v_mov_b32 v41, v40\n\t"
      "s_waitcnt lgkmcnt(9)\n\t"
      "v_mfma_f32_16x16x32_bf16 v[0:3], a[44:47], v[88:91], v[0:3]\n\t"
      "v_permlane32_swap_b32 v40, v41\n\t"
      "v_add_f32 v40, v40, v41\n\t"
      "v_mfma_f32_16x16x32_bf16 v[4:7], a[108:111], v[88:91], v[4:7]\n\t"
      "v_cvt_pk_bf16_f32 v40, v40, v40\n\t"
      "v_and_b32 v40, s74, v40\n\t"
      "s_waitcnt lgkmcnt(8)\n\t"
      "v_mfma_f32_16x16x32_bf16 v[8:11], a[44:47], v[92:95], v[8:11]\n\t"
      "buffer_load_dwordx4 v[152:155], v253, s[36:39], s52 offen offset:1024 sc1\n\t"
      "v_mfma_f32_16x16x32_bf16 v[12:15], a[108:111], v[92:95], v[12:15]\n\t"
      "s_mov_b64 exec, 1\n\t"
      "buffer_store_dword v40, off, s[36:39], s70 sc1\n\t"
      "s_mov_b64 exec, -1\n\t"
      "v_add_f32 v192, v192, v32\n\t"
      "v_add_f32 v193, v193, v33\n\t"
      "s_waitcnt lgkmcnt(7)\n\t"
      "v_mfma_f32_16x16x32_bf16 v[0:3], a[48:51], v[96:99], v[0:3]\n\t"
      "v_add_f32 v194, v194, v34\n\t"
      "v_add_f32 v195, v195, v35\n\t"
      "v_mfma_f32_16x16x32_bf16 v[4:7], a[112:115], v[96:99], v[4:7]\n\t"
      "v_add_f32 v196, v196, v36\n\t"
      "v_add_f32 v197, v197, v37\n\t"
      "s_waitcnt lgkmcnt(6)\n\t"
      "v_mfma_f32_16x16x32_bf16 v[8:11], a[48:51], v[100:103], v[8:11]\n\t"
      "v_add_f32 v198, v198, v38\n\t"
      "v_add_f32 v199, v199, v39\n\t"
      "v_mfma_f32_16x16x32_bf16 v[12:15], a[112:115], v[100:103], v[12:15]\n\t"
      "v_readfirstlane_b32 s68, v40\n\t"
      "v_cvt_pk_bf16_f32 v56, v192, v193\n\t"
      "s_waitcnt lgkmcnt(5)\n\t"
      "v_mfma_f32_16x16x32_bf16 v[0:3], a[52:55], v[104:107], v[0:3]\n\t"
      "v_cvt_pk_bf16_f32 v57, v194, v195\n\t"
      "v_cvt_pk_bf16_f32 v58, v196, v197\n\t"
      "v_mfma_f32_16x16x32_bf16 v[4:7], a[116:119], v[104:107], v[4:7]\n\t"
      "v_cvt_pk_bf16_f32 v59, v198, v199\n\t"
      "s_add_u32 s67, s69, 8192\n\t"
      "s_lshl_b32 s73, s73, 10\n\t"
      "s_add_u32 s69, s73, s58\n\t"
      "s_add_u32 s70, s73, s59\n\t"
      "buffer_store_dwordx2 v[56:57], v249, s[44:47], s73 offen sc1\n\t"
      "s_waitcnt lgkmcnt(4)\n\t"
      "v_mfma_f32_16x16x32_bf16 v[8:11], a[52:55], v[108:111], v[8:11]\n\t"
      "buffer_store_dwordx2 v[52:53], v249, s[36:39], s69 offen sc1\n\t"
      "buffer_store_dwordx2 v[200:201], v249, s[36:39], s70 offen sc1\n\t"
      "v_mfma_f32_16x16x32_bf16 v[12:15], a[116:119], v[108:111], v[12:15]\n\t"
      "buffer_load_dwordx4 v[156:159], v253, s[36:39], s52 offen offset:1536 sc1\n\t"
      "s_waitcnt lgkmcnt(3)\n\t"
      "v_mfma_f32_16x16x32_bf16 v[0:3], a[56:59], v[112:115], v[0:3]\n\t"
      "buffer_store_dwordx2 v[58:59], v249, s[44:47], s73 offen offset:512 sc1\n\t"
      "buffer_store_dwordx2 v[54:55], v249, s[36:39], s69 offen offset:512 sc1\n\t"
      "v_mfma_f32_16x16x32_bf16 v[4:7], a[120:123], v[112:115], v[4:7]\n\t"
      "buffer_store_dwordx2 v[202:203], v249, s[36:39], s70 offen offset:512 sc1\n\t"
      "s_mov_b32 s49, s89\n\t"
      "s_add_u32 s89, s89, 16\n\t"
      "s_lshr_b32 s69, s89, 4\n\t"
      "s_cmp_eq_u32 s69, 3\n\t"
      "s_cselect_b32 s89, s93, s89\n\t"
      "s_cmp_ge_u32 s49, 48\n\t"
      "s_cselect_b32 s60, s94, s60\n\t"
      "s_cselect_b32 s54, s95, s54\n\t"
      "s_waitcnt lgkmcnt(2)\n\t"
      "v_mfma_f32_16x16x32_bf16 v[8:11], a[56:59], v[116:119], v[8:11]\n\t"
      "v_mfma_f32_16x16x32_bf16 v[12:15], a[120:123], v[116:119], v[12:15]\n\t"
      "s_waitcnt lgkmcnt(1)\n\t"
      "v_mfma_f32_16x16x32_bf16 v[0:3], a[60:63], v[120:123], v[0:3]\n\t"
      "v_mfma_f32_16x16x32_bf16 v[4:7], a[124:127], v[120:123], v[4:7]\n\t"
      "s_waitcnt lgkmcnt(0)\n\t"
      "v_mfma_f32_16x16x32_bf16 v[8:11], a[60:63], v[124:127], v[8:11]\n\t"
      "v_mfma_f32_16x16x32_bf16 v[12:15], a[124:127], v[124:127], v[12:15]\n\t"
      "v_xor_b32 v254, 0x8000, v254\n\t"
      "v_xor_b32 v255, 0x8000, v255\n\t"
      "s_add_u32 s69, s53, 0x4000\n\t"
      "s_add_u32 s48, s48, 4\n\t"
      "s_waitcnt vmcnt(40)\n\t"
      "v_cvt_pk_bf16_f32 v0, v0, v1\n\t"
      "v_cvt_pk_bf16_f32 v1, v2, v3\n\t"
      "v_cvt_pk_bf16_f32 v2, v4, v5\n\t"
      "v_cvt_pk_bf16_f32 v3, v6, v7\n\t"
      "v_min_u32 v0, -2, v0\n\t"
      "v_min_u32 v1, -2, v1\n\t"
      "v_min_u32 v2, -2, v2\n\t"
      "v_min_u32 v3, -2, v3\n\t"
      "buffer_store_dwordx4 v[0:3], v238, s[36:39], s53 offen sc1\n\t"
      "v_cvt_pk_bf16_f32 v8, v8, v9\n\t"
      "v_cvt_pk_bf16_f32 v9, v10, v11\n\t"
      "v_cvt_pk_bf16_f32 v10, v12, v13\n\t"
      "v_cvt_pk_bf16_f32 v11, v14, v15\n\t"
      "v_min_u32 v8, -2, v8\n\t"
      "v_min_u32 v9, -2, v9\n\t"
      "v_min_u32 v10, -2, v10\n\t"
      "v_min_u32 v11, -2, v11\n\t"
      "buffer_store_dwordx4 v[8:11], v238, s[36:39], s69 offen sc1\n\t"
      "s_add_u32 s53, s53, 1048576\n\t"
      "s_nop 1\n\t"
      "s_branch WTOP%=\n\t"
      ".p2align 5\n\t"
      "WKF%=:\n\t"
      "s_cmp_eq_u32 s48, 63\n\t"
      "s_cselect_b32 s69, 0, -9\n\t"
      "s_add_u32 s88, s88, s69\n\t"
      "s_add_u32 s88, s88, 39\n\t"
      "WKT%=:\n\t"
      "s_memrealtime s[90:91]\n\t"
      "s_waitcnt lgkmcnt(0)\n\t"
      "s_sub_u32 s69, s88, s90\n\t"
      "s_and_b32 s69, s69, 0xffff\n\t"
      "s_cmp_lt_u32 s69, 400\n\t"
      "s_cbranch_scc1 WKT%=\n\t"
      "WKL%=:\n\t"
      "buffer_load_dwordx4 v[128:131], v252, s[36:39], s51 offen sc1\n\t"
      "buffer_load_dwordx4 v[132:135], v252, s[36:39], s51 offen offset:512 sc1\n\t"
      "buffer_load_dwordx4 v[136:139], v252, s[36:39], s51 offen offset:1024 sc1\n\t"
      "buffer_load_dwordx4 v[140:143], v252, s[36:39], s51 offen offset:1536 sc1\n\t"
      "buffer_load_dwordx4 v[144:147], v253, s[36:39], s52 offen sc1\n\t"
      "buffer_load_dwordx4 v[148:151], v253, s[36:39], s52 offen offset:512 sc1\n\t"
      "buffer_load_dwordx4 v[152:155], v253, s[36:39], s52 offen offset:1024 sc1\n\t"
      "buffer_load_dwordx4 v[156:159], v253, s[36:39], s52 offen offset:1536 sc1\n\t"
      "v_lshlrev_b32 v6, 4, v248\n\t"
      "v_mov_b64 v[4:5], 0\n\t"
      "s_cmp_ge_u32 s49, s48\n\t"
      "s_cbranch_scc1 WKL1%=\n\t"
      "buffer_load_dword v4, v6, s[36:39], s55 offen sc1\n\t"
      "buffer_load_dword v5, v6, s[36:39], s55 offen offset:128 sc1\n\t"
      "WKL1%=:\n\t"
      "s_waitcnt vmcnt(0)\n\t"
      "v_min_u32 v4, v4, v5\n\t"
      "v_cmp_ge_u32 vcc, s49, v4\n\t"
      "s_nop 0\n\t"
      "s_cmp_eq_u64 vcc, 0\n\t"
      "s_cselect_b32 s66, 1, 0\n\t"
      "s_cmp_lt_u32 s48, 40\n\t"
      "s_cbranch_scc1 WKV%=\n\t"
      "WKC%=:\n\t"
      "v_max3_u32 v0, v128, v129, v130\n\t"
      "v_max3_u32 v0, v0, v131, v132\n\t"
      "v_max3_u32 v0, v0, v133, v134\n\t"
      "v_max3_u32 v0, v0, v135, v136\n\t"
      "v_max3_u32 v0, v0, v137, v138\n\t"
      "v_max3_u32 v0, v0, v139, v140\n\t"
      "v_max3_u32 v0, v0, v141, v142\n\t"
      "v_max3_u32 v0, v0, v143, v144\n\t"
      "v_max3_u32 v0, v0, v145, v146\n\t"
      "v_max3_u32 v0, v0, v147, v148\n\t"
      "v_max3_u32 v0, v0, v149, v150\n\t"
      "v_max3_u32 v0, v0, v151, v152\n\t"
      "v_max3_u32 v0, v0, v153, v154\n\t"
      "v_max3_u32 v0, v0, v155, v156\n\t"
      "v_max3_u32 v0, v0, v157, v158\n\t"
      "v_max_u32 v0, v0, v159\n\t"
      "v_cmp_eq_u32 vcc, -1, v0\n\t"
      "s_nop 0\n\t"
      "s_cbranch_vccz WKV%=\n\t"
      "s_sleep 2\n\t"
      "s_sub_u32 s72, s72, 1\n\t"
      "s_cmp_gt_i32 s72, 0\n\t"
      "s_cbranch_scc1 WKL%=\n\t"
      "WKV%=:\n\t"
      "s_cmp_lt_u32 s48, 60\n\t"
      "s_cbranch_scc1 WKV1%=\n\t"
      "s_cmp_eq_u32 s66, 0\n\t"
      "s_cbranch_scc1 WB0%=\n\t"
      "s_lshl_b32 s70, s49, 5\n\t"
      "s_add_u32 s70, s70, s60\n\t"
      "s_lshl_b32 s70, s70, 10\n\t"
      "buffer_load_dwordx2 v[224:225], v249, s[40:43], s70 offen \n\t"
      "buffer_load_dwordx2 v[226:227], v249, s[40:43], s70 offen offset:512 \n\t"
      "s_lshl_b32 s69, s49, 18\n\t"
      "s_add_u32 s69, s69, s54\n\t"
      "buffer_load_dwordx4 v[160:163], v248, s[36:39], s69 offen sc1\n\t"
      "s_add_u32 s69, s69, 0x8000\n\t"
      "buffer_load_dwordx4 v[164:167], v248, s[36:39], s69 offen sc1\n\t"
      "s_add_u32 s69, s69, 0x8000\n\t"
      "buffer_load_dwordx4 v[168:171], v248, s[36:39], s69 offen sc1\n\t"
      "s_add_u32 s69, s69, 0x8000\n\t"
      "buffer_load_dwordx4 v[172:175], v248, s[36:39], s69 offen sc1\n\t"
      "s_add_u32 s69, s69, 0x8000\n\t"
      "buffer_load_dwordx4 v[176:179], v248, s[36:39], s69 offen sc1\n\t"
      "s_add_u32 s69, s69, 0x8000\n\t"
      "buffer_load_dwordx4 v[180:183], v248, s[36:39], s69 offen sc1\n\t"
      "s_add_u32 s69, s69, 0x8000\n\t"
      "buffer_load_dwordx4 v[184:187], v248, s[36:39], s69 offen sc1\n\t"
      "s_add_u32 s69, s69, 0x8000\n\t"
      "buffer_load_dwordx4 v[188:191], v248, s[36:39], s69 offen sc1\n\t"
      "s_mov_b32 s65, 1\n\t"
      "WB0%=:\n\t"
      "v_mfma_f32_16x16x32_bf16 v[0:3], v[128:131], a[160:163], 0\n\t"
      "v_mfma_f32_16x16x32_bf16 v[4:7], v[128:131], a[164:167], 0\n\t"
      "v_mfma_f32_16x16x32_bf16 v[0:3], v[132:135], a[168:171], v[0:3]\n\t"
      "v_mfma_f32_16x16x32_bf16 v[4:7], v[132:135], a[172:175], v[4:7]\n\t"
      "v_mfma_f32_16x16x32_bf16 v[0:3], v[136:139], a[176:179], v[0:3]\n\t"
      "v_mfma_f32_16x16x32_bf16 v[4:7], v[136:139], a[180:183], v[4:7]\n\t"
      "v_mfma_f32_16x16x32_bf16 v[0:3], v[140:143], a[184:187], v[0:3]\n\t"
      "v_mfma_f32_16x16x32_bf16 v[4:7], v[140:143], a[188:191], v[4:7]\n\t"
      "s_add_u32 s51, s51, 262144\n\t"
      "s_add_u32 s52, s52, 262144\n\t"
      "s_nop 4\n\t"
      "v_max3_f32 v8, v0, v1, v2\n\t"
      "v_max3_f32 v8, v8, v3, v234\n\t"
      "v_max3_f32 v9, v4, v5, v6\n\t"
      "v_max3_f32 v9, v9, v7, v235\n\t"
      "s_nop 1\n\t"
      "v_permlane16_swap_b32 v8, v9\n\t"
      "v_max_f32 v8, v8, v9\n\t"
      "v_mov_b32 v9, v8\n\t"
      "s_nop 1\n\t"
      "v_permlane16_swap_b32 v8, v9\n\t"
      "v_pk_mul_f32 v[8:9], v[8:9], s[86:87]\n\t"
      "v_pk_fma_f32 v[10:11], v[234:235], s[86:87], v[8:9] neg_lo:[0,0,1] neg_hi:[0,0,1]\n\t"
      "v_pk_fma_f32 v[0:1], v[0:1], s[86:87], v[8:9] op_sel:[0,0,0] op_sel_hi:[1,1,0] neg_lo:[0,0,1] neg_hi:[0,0,1]\n\t"
      "v_pk_fma_f32 v[2:3], v[2:3], s[86:87], v[8:9] op_sel:[0,0,0] op_sel_hi:[1,1,0] neg_lo:[0,0,1] neg_hi:[0,0,1]\n\t"
      "v_pk_fma_f32 v[4:5], v[4:5], s[86:87], v[8:9] op_sel:[0,0,1] op_sel_hi:[1,1,1] neg_lo:[0,0,1] neg_hi:[0,0,1]\n\t"
      "v_pk_fma_f32 v[6:7], v[6:7], s[86:87], v[8:9] op_sel:[0,0,1] op_sel_hi:[1,1,1] neg_lo:[0,0,1] neg_hi:[0,0,1]\n\t"
      "v_exp_f32 v10, v10\n\t"
      "v_exp_f32 v11, v11\n\t"
      "v_exp_f32 v0, v0\n\t"
      "v_exp_f32 v1, v1\n\t"
      "v_exp_f32 v2, v2\n\t"
      "v_exp_f32 v3, v3\n\t"
      "v_exp_f32 v4, v4\n\t"
      "v_exp_f32 v5, v5\n\t"
      "v_exp_f32 v6, v6\n\t"
      "v_exp_f32 v7, v7\n\t"
      "v_pk_mul_f32 v[12:13], v[236:237], v[10:11]\n\t"
      "v_pk_add_f32 v[14:15], v[0:1], v[2:3]\n\t"
      "v_pk_add_f32 v[16:17], v[4:5], v[6:7]\n\t"
      "v_add_f32 v14, v14, v15\n\t"
      "v_add_f32 v15, v16, v17\n\t"
      "v_fma_f32 v12, v14, 0.5, v12\n\t"
      "v_fma_f32 v13, v15, 0.5, v13\n\t"
      "s_nop 1\n\t"
      "v_permlane16_swap_b32 v12, v13\n\t"
      "v_add_f32 v12, v12, v13\n\t"
      "v_mov_b32 v13, v12\n\t"
      "s_nop 1\n\t"
      "v_permlane32_swap_b32 v12, v13\n\t"
      "v_add_f32 v12, v12, v13\n\t"
      "v_mov_b32 v13, v12\n\t"
      "s_nop 1\n\t"
      "v_permlane16_swap_b32 v12, v13\n\t"
      "v_rcp_f32 v12, v12\n\t"
      "v_rcp_f32 v13, v13\n\t"
      "s_nop 0\n\t"
      "v_pk_mul_f32 v[10:11], v[10:11], v[12:13]\n\t"
      "v_pk_mul_f32 v[12:13], v[12:13], 0.5 op_sel_hi:[1,0]\n\t"
      "v_pk_mul_f32 v[0:1], v[0:1], v[12:13] op_sel:[0,0] op_sel_hi:[1,0]\n\t"
      "v_pk_mul_f32 v[2:3], v[2:3], v[12:13] op_sel:[0,0] op_sel_hi:[1,0]\n\t"
      "v_pk_mul_f32 v[4:5], v[4:5], v[12:13] op_sel:[0,1] op_sel_hi:[1,1]\n\t"
      "v_pk_mul_f32 v[6:7], v[6:7], v[12:13] op_sel:[0,1] op_sel_hi:[1,1]\n\t"
      "v_cvt_pk_bf16_f32 v16, v0, v1\n\t"
      "v_cvt_pk_bf16_f32 v17, v2, v3\n\t"
      "v_cvt_pk_bf16_f32 v18, v4, v5\n\t"
      "v_cvt_pk_bf16_f32 v19, v6, v7\n\t"
      "v_pk_mul_f32 v[64:65], v[64:65], v[10:11] op_sel:[0,0] op_sel_hi:[1,0]\n\t"
      "v_pk_mul_f32 v[66:67], v[66:67], v[10:11] op_sel:[0,0] op_sel_hi:[1,0]\n\t"
      "v_pk_mul_f32 v[68:69], v[68:69], v[10:11] op_sel:[0,0] op_sel_hi:[1,0]\n\t"
      "v_pk_mul_f32 v[70:71], v[70:71], v[10:11] op_sel:[0,0] op_sel_hi:[1,0]\n\t"
      "v_mfma_f32_16x16x16_bf16 v[64:67], v[144:145], v[16:17], v[64:67]\n\t"
      "v_pk_mul_f32 v[72:73], v[72:73], v[10:11] op_sel:[0,0] op_sel_hi:[1,0]\n\t"
      "v_pk_mul_f32 v[74:75], v[74:75], v[10:11] op_sel:[0,0] op_sel_hi:[1,0]\n\t"
      "v_mfma_f32_16x16x16_bf16 v[68:71], v[146:147], v[16:17], v[68:71]\n\t"
      "v_pk_mul_f32 v[76:77], v[76:77], v[10:11] op_sel:[0,0] op_sel_hi:[1,0]\n\t"
      "v_pk_mul_f32 v[78:79], v[78:79], v[10:11] op_sel:[0,0] op_sel_hi:[1,0]\n\t"
      "v_mfma_f32_16x16x16_bf16 v[72:75], v[148:149], v[16:17], v[72:75]\n\t"
      "v_pk_mul_f32 v[80:81], v[80:81], v[10:11] op_sel:[0,0] op_sel_hi:[1,0]\n\t"
      "v_pk_mul_f32 v[82:83], v[82:83], v[10:11] op_sel:[0,0] op_sel_hi:[1,0]\n\t"
      "v_mfma_f32_16x16x16_bf16 v[76:79], v[150:151], v[16:17], v[76:79]\n\t"
      "v_pk_mul_f32 v[84:85], v[84:85], v[10:11] op_sel:[0,0] op_sel_hi:[1,0]\n\t"
      "v_pk_mul_f32 v[86:87], v[86:87], v[10:11] op_sel:[0,0] op_sel_hi:[1,0]\n\t"
      "v_mfma_f32_16x16x16_bf16 v[80:83], v[152:153], v[16:17], v[80:83]\n\t"
      "v_pk_mul_f32 v[88:89], v[88:89], v[10:11] op_sel:[0,0] op_sel_hi:[1,0]\n\t"
      "v_pk_mul_f32 v[90:91], v[90:91], v[10:11] op_sel:[0,0] op_sel_hi:[1,0]\n\t"
      "v_mfma_f32_16x16x16_bf16 v[84:87], v[154:155], v[16:17], v[84:87]\n\t"
      "v_pk_mul_f32 v[92:93], v[92:93], v[10:11] op_sel:[0,0] op_sel_hi:[1,0]\n\t"
      "v_pk_mul_f32 v[94:95], v[94:95], v[10:11] op_sel:[0,0] op_sel_hi:[1,0]\n\t"
      "v_cvt_pk_bf16_f32 v24, v64, v65\n\t"
      "v_cvt_pk_bf16_f32 v25, v66, v67\n\t"
      "v_cvt_pk_bf16_f32 v26, v68, v69\n\t"
      "v_cvt_pk_bf16_f32 v27, v70, v71\n\t"
      "ds_write_b128 v254, v[24:27]\n\t"
      "v_mfma_f32_16x16x16_bf16 v[88:91], v[156:157], v[16:17], v[88:91]\n\t"
      "v_pk_mul_f32 v[96:97], v[96:97], v[10:11] op_sel:[0,1] op_sel_hi:[1,1]\n\t"
      "v_pk_mul_f32 v[98:99], v[98:99], v[10:11] op_sel:[0,1] op_sel_hi:[1,1]\n\t"
      "v_mfma_f32_16x16x16_bf16 v[92:95], v[158:159], v[16:17], v[92:95]\n\t"
      "v_pk_mul_f32 v[100:101], v[100:101], v[10:11] op_sel:[0,1] op_sel_hi:[1,1]\n\t"
      "v_pk_mul_f32 v[102:103], v[102:103], v[10:11] op_sel:[0,1] op_sel_hi:[1,1]\n\t"
      "v_cvt_pk_bf16_f32 v28, v72, v73\n\t"
      "v_cvt_pk_bf16_f32 v29, v74, v75\n\t"
      "v_cvt_pk_bf16_f32 v30, v76, v77\n\t"
      "v_cvt_pk_bf16_f32 v31, v78, v79\n\t"
      "ds_write_b128 v254, v[28:31] offset:2048\n\t"
      "v_mfma_f32_16x16x16_bf16 v[96:99], v[144:145], v[18:19], v[96:99]\n\t"
      "v_pk_mul_f32 v[104:105], v[104:105], v[10:11] op_sel:[0,1] op_sel_hi:[1,1]\n\t"
      "v_pk_mul_f32 v[106:107], v[106:107], v[10:11] op_sel:[0,1] op_sel_hi:[1,1]\n\t"
      "v_mfma_f32_16x16x16_bf16 v[100:103], v[146:147], v[18:19], v[100:103]\n\t"
      "v_pk_mul_f32 v[108:109], v[108:109], v[10:11] op_sel:[0,1] op_sel_hi:[1,1]\n\t"
      "v_pk_mul_f32 v[110:111], v[110:111], v[10:11] op_sel:[0,1] op_sel_hi:[1,1]\n\t"
      "v_cvt_pk_bf16_f32 v24, v80, v81\n\t"
      "v_cvt_pk_bf16_f32 v25, v82, v83\n\t"
      "v_cvt_pk_bf16_f32 v26, v84, v85\n\t"
      "v_cvt_pk_bf16_f32 v27, v86, v87\n\t"
      "ds_write_b128 v254, v[24:27] offset:4096\n\t"
      "v_mfma_f32_16x16x16_bf16 v[104:107], v[148:149], v[18:19], v[104:107]\n\t"
      "v_pk_mul_f32 v[112:113], v[112:113], v[10:11] op_sel:[0,1] op_sel_hi:[1,1]\n\t"
      "v_pk_mul_f32 v[114:115], v[114:115], v[10:11] op_sel:[0,1] op_sel_hi:[1,1]\n\t"
      "v_mfma_f32_16x16x16_bf16 v[108:111], v[150:151], v[18:19], v[108:111]\n\t"
      "v_pk_mul_f32 v[116:117], v[116:117], v[10:11] op_sel:[0,1] op_sel_hi:[1,1]\n\t"
      "v_pk_mul_f32 v[118:119], v[118:119], v[10:11] op_sel:[0,1] op_sel_hi:[1,1]\n\t"
      "v_cvt_pk_bf16_f32 v28, v88, v89\n\t"
      "v_cvt_pk_bf16_f32 v29, v90, v91\n\t"
      "v_cvt_pk_bf16_f32 v30, v92, v93\n\t"
      "v_cvt_pk_bf16_f32 v31, v94, v95\n\t"
      "ds_write_b128 v254, v[28:31] offset:6144\n\t"
      "v_mfma_f32_16x16x16_bf16 v[112:115], v[152:153], v[18:19], v[112:115]\n\t"
      "v_pk_mul_f32 v[120:121], v[120:121], v[10:11] op_sel:[0,1] op_sel_hi:[1,1]\n\t"
      "v_pk_mul_f32 v[122:123], v[122:123], v[10:11] op_sel:[0,1] op_sel_hi:[1,1]\n\t"
      "v_mfma_f32_16x16x16_bf16 v[116:119], v[154:155], v[18:19], v[116:119]\n\t"
      "v_pk_mul_f32 v[124:125], v[124:125], v[10:11] op_sel:[0,1] op_sel_hi:[1,1]\n\t"
      "v_pk_mul_f32 v[126:127], v[126:127], v[10:11] op_sel:[0,1] op_sel_hi:[1,1]\n\t"
      "v_cvt_pk_bf16_f32 v24, v96, v97\n\t"
      "v_cvt_pk_bf16_f32 v25, v98, v99\n\t"
      "v_cvt_pk_bf16_f32 v26, v100, v101\n\t"
      "v_cvt_pk_bf16_f32 v27, v102, v103\n\t"
      "ds_write_b128 v254, v[24:27] offset:1024\n\t"
      "v_mfma_f32_16x16x16_bf16 v[120:123], v[156:157], v[18:19], v[120:123]\n\t"
      "v_mfma_f32_16x16x16_bf16 v[124:127], v[158:159], v[18:19], v[124:127]\n\t"
      "v_cvt_pk_bf16_f32 v28, v104, v105\n\t"
      "v_cvt_pk_bf16_f32 v29, v106, v107\n\t"
      "v_cvt_pk_bf16_f32 v30, v108, v109\n\t"
      "v_cvt_pk_bf16_f32 v31, v110, v111\n\t"
      "ds_write_b128 v254, v[28:31] offset:3072\n\t"
      "v_cvt_pk_bf16_f32 v24, v112, v113\n\t"
      "v_cvt_pk_bf16_f32 v25, v114, v115\n\t"
      "v_cvt_pk_bf16_f32 v26, v116, v117\n\t"
      "v_cvt_pk_bf16_f32 v27, v118, v119\n\t"
      "ds_write_b128 v254, v[24:27] offset:5120\n\t"
      "v_cvt_pk_bf16_f32 v28, v120, v121\n\t"
      "v_cvt_pk_bf16_f32 v29, v122, v123\n\t"
      "v_cvt_pk_bf16_f32 v30, v124, v125\n\t"
      "v_cvt_pk_bf16_f32 v31, v126, v127\n\t"
      "ds_write_b128 v254, v[28:31] offset:7168\n\t"
      "s_waitcnt vmcnt(40) lgkmcnt(0)\n\t"
      "s_barrier\n\t"
      "WO%=:\n\t"
      "s_waitcnt vmcnt(16)\n\t"
      "ds_read_b128 v[64:67], v255\n\t"
      "ds_read_b128 v[68:71], v255 offset:1024\n\t"
      "ds_read_b128 v[72:75], v255 offset:2048\n\t"
      "ds_read_b128 v[76:79], v255 offset:3072\n\t"
      "ds_read_b128 v[80:83], v255 offset:4096\n\t"
      "ds_read_b128 v[84:87], v255 offset:5120\n\t"
      "ds_read_b128 v[88:91], v255 offset:6144\n\t"
      "ds_read_b128 v[92:95], v255 offset:7168\n\t"
      "ds_read_b128 v[96:99], v255 offset:8192\n\t"
      "ds_read_b128 v[100:103], v255 offset:9216\n\t"
      "ds_read_b128 v[104:107], v255 offset:10240\n\t"
      "ds_read_b128 v[108:111], v255 offset:11264\n\t"
      "s_cmp_ge_u32 s48, 32\n\t"
      "s_cbranch_scc1 WO3%=\n\t"
      "v_mov_b64 v[20:21], -1\n\t"
      "v_mov_b64 v[22:23], -1\n\t"
      "buffer_store_dwordx4 v[20:23], v238, s[36:39], s98 offen sc1\n\t"
      "s_bitcmp0_b32 s48, 2\n\t"
      "s_cselect_b32 s69, 1, 63\n\t"
      "s_lshl_b32 s69, s69, 14\n\t"
      "s_add_u32 s98, s98, s69\n\t"
      "WO3%=:\n\t"
      "v_lshlrev_b32 v16, 4, v248\n\t"
      "s_cmp_ge_u32 s48, 60\n\t"
      "s_cbranch_scc1 WO1%=\n\t"
      "buffer_load_dwordx4 v[228:231], off, s[36:39], s56 sc1\n\t"
      "s_mov_b32 s97, s96\n\t"
      "v_mov_b64 v[232:233], 0\n\t"
      "s_cmp_lg_u32 s65, 0\n\t"
      "s_cselect_b32 s70, s89, s49\n\t"
      "s_cmp_ge_u32 s70, s48\n\t"
      "s_cbranch_scc1 WO1%=\n\t"
      "buffer_load_dword v232, v16, s[36:39], s55 offen sc1\n\t"
      "buffer_load_dword v233, v16, s[36:39], s55 offen offset:128 sc1\n\t"
      "WO1%=:\n\t"
      "s_cmp_lt_u32 s48, 56\n\t"
      "s_cselect_b32 s69, 3145728, 0\n\t"
      "s_add_u32 s50, s50, s69\n\t"
      "s_mov_b32 s24, 0\n\t"
      "s_cmp_eq_u32 s65, 2\n\t"
      "s_cbranch_scc1 WOM2%=\n\t"
      "s_cmp_lt_u32 s48, 60\n\t"
      "s_cselect_b32 s69, s65, 0\n\t"
      "s_cmp_lg_u32 s69, 0\n\t"
      "s_cbranch_scc1 WOM1%=\n\t"
      "s_cmp_lg_u32 s96, 0\n\t"
      "s_cbranch_scc1 WOL1%=\n\t"
      "WOL0%=:\n\t"
      "s_waitcnt lgkmcnt(11)\n\t"
      "v_mfma_f32_16x16x32_bf16 v[0:3], a[0:3], v[64:67], 0\n\t"
      "v_mfma_f32_16x16x32_bf16 v[4:7], a[64:67], v[64:67], 0\n\t"
      "ds_read_b128 v[112:115], v255 offset:12288\n\t"
      "s_waitcnt lgkmcnt(11)\n\t"
      "v_mfma_f32_16x16x32_bf16 v[8:11], a[0:3], v[68:71], 0\n\t"
      "v_mfma_f32_16x16x32_bf16 v[12:15], a[64:67], v[68:71], 0\n\t"
      "ds_read_b128 v[116:119], v255 offset:13312\n\t"
      "s_waitcnt lgkmcnt(11)\n\t"
      "v_mfma_f32_16x16x32_bf16 v[0:3], a[4:7], v[72:75], v[0:3]\n\t"
      "v_mfma_f32_16x16x32_bf16 v[4:7], a[68:71], v[72:75], v[4:7]\n\t"
      "ds_read_b128 v[120:123], v255 offset:14336\n\t"
      "s_waitcnt lgkmcnt(11)\n\t"
      "v_mfma_f32_16x16x32_bf16 v[8:11], a[4:7], v[76:79], v[8:11]\n\t"
      "v_mfma_f32_16x16x32_bf16 v[12:15], a[68:71], v[76:79], v[12:15]\n\t"
      "ds_read_b128 v[124:127], v255 offset:15360\n\t"
      "s_waitcnt lgkmcnt(11)\n\t"
      "v_mfma_f32_16x16x32_bf16 v[0:3], a[8:11], v[80:83], v[0:3]\n\t"
      "v_mfma_f32_16x16x32_bf16 v[4:7], a[72:75], v[80:83], v[4:7]\n\t"
      "ds_read_b128 v[64:67], v255 offset:16384\n\t"
      "s_waitcnt lgkmcnt(11)\n\t"
      "v_mfma_f32_16x16x32_bf16 v[8:11], a[8:11], v[84:87], v[8:11]\n\t"
      "v_mfma_f32_16x16x32_bf16 v[12:15], a[72:75], v[84:87], v[12:15]\n\t"
      "ds_read_b128 v[68:71], v255 offset:17408\n\t"
      "s_waitcnt lgkmcnt(11)\n\t"
      "v_mfma_f32_16x16x32_bf16 v[0:3], a[12:15], v[88:91], v[0:3]\n\t"
      "v_mfma_f32_16x16x32_bf16 v[4:7], a[76:79], v[88:91], v[4:7]\n\t"
      "ds_read_b128 v[72:75], v255 offset:18432\n\t"
      "s_waitcnt lgkmcnt(11)\n\t"
      "v_mfma_f32_16x16x32_bf16 v[8:11], a[12:15], v[92:95], v[8:11]\n\t"
      "v_mfma_f32_16x16x32_bf16 v[12:15], a[76:79], v[92:95], v[12:15]\n\t"
      "ds_read_b128 v[76:79], v255 offset:19456\n\t"
      "s_waitcnt lgkmcnt(11)\n\t"
      "v_mfma_f32_16x16x32_bf16 v[0:3], a[16:19], v[96:99], v[0:3]\n\t"
      "v_mfma_f32_16x16x32_bf16 v[4:7], a[80:83], v[96:99], v[4:7]\n\t"
      "ds_read_b128 v[80:83], v255 offset:20480\n\t"
      "s_waitcnt lgkmcnt(11)\n\t"
      "v_mfma_f32_16x16x32_bf16 v[8:11], a[16:19], v[100:103], v[8:11]\n\t"
      "v_mfma_f32_16x16x32_bf16 v[12:15], a[80:83], v[100:103], v[12:15]\n\t"
      "ds_read_b128 v[84:87], v255 offset:21504\n\t"
      "s_waitcnt lgkmcnt(11)\n\t"
      "v_mfma_f32_16x16x32_bf16 v[0:3], a[20:23], v[104:107], v[0:3]\n\t"
      "v_mfma_f32_16x16x32_bf16 v[4:7], a[84:87], v[104:107], v[4:7]\n\t"
      "ds_read_b128 v[88:91], v255 offset:22528\n\t"
      "s_waitcnt lgkmcnt(11)\n\t"
      "v_mfma_f32_16x16x32_bf16 v[8:11], a[20:23], v[108:111], v[8:11]\n\t"
      "v_mfma_f32_16x16x32_bf16 v[12:15], a[84:87], v[108:111], v[12:15]\n\t"
      "ds_read_b128 v[92:95], v255 offset:23552\n\t"
      "s_waitcnt lgkmcnt(11)\n\t"
      "v_mfma_f32_16x16x32_bf16 v[0:3], a[24:27], v[112:115], v[0:3]\n\t"
      "v_mfma_f32_16x16x32_bf16 v[4:7], a[88:91], v[112:115], v[4:7]\n\t"
      "ds_read_b128 v[96:99], v255 offset:24576\n\t"
      "s_waitcnt lgkmcnt(11)\n\t"
      "v_mfma_f32_16x16x32_bf16 v[8:11], a[24:27], v[116:119], v[8:11]\n\t"
      "v_mfma_f32_16x16x32_bf16 v[12:15], a[88:91], v[116:119], v[12:15]\n\t"
      "ds_read_b128 v[100:103], v255 offset:25600\n\t"
      "s_waitcnt lgkmcnt(11)\n\t"
      "v_mfma_f32_16x16x32_bf16 v[0:3], a[28:31], v[120:123], v[0:3]\n\t"
      "v_mfma_f32_16x16x32_bf16 v[4:7], a[92:95], v[120:123], v[4:7]\n\t"
      "ds_read_b128 v[104:107], v255 offset:26624\n\t"
      "s_waitcnt lgkmcnt(11)\n\t"
      "v_mfma_f32_16x16x32_bf16 v[8:11], a[28:31], v[124:127], v[8:11]\n\t"
      "v_mfma_f32_16x16x32_bf16 v[12:15], a[92:95], v[124:127], v[12:15]\n\t"
      "ds_read_b128 v[108:111], v255 offset:27648\n\t"
      "s_waitcnt lgkmcnt(11)\n\t"
      "v_mfma_f32_16x16x32_bf16 v[0:3], a[32:35], v[64:67], v[0:3]\n\t"
      "v_mfma_f32_16x16x32_bf16 v[4:7], a[96:99], v[64:67], v[4:7]\n\t"
      "ds_read_b128 v[112:115], v255 offset:28672\n\t"
      "s_waitcnt lgkmcnt(11)\n\t"
      "v_mfma_f32_16x16x32_bf16 v[8:11], a[32:35], v[68:71], v[8:11]\n\t"
      "v_mfma_f32_16x16x32_bf16 v[12:15], a[96:99], v[68:71], v[12:15]\n\t"
      "ds_read_b128 v[116:119], v255 offset:29696\n\t"
      "s_waitcnt lgkmcnt(11)\n\t"
      "v_mfma_f32_16x16x32_bf16 v[0:3], a[36:39], v[72:75], v[0:3]\n\t"
      "v_mfma_f32_16x16x32_bf16 v[4:7], a[100:103], v[72:75], v[4:7]\n\t"
      "ds_read_b128 v[120:123], v255 offset:30720\n\t"
      "s_waitcnt lgkmcnt(11)\n\t"
      "v_mfma_f32_16x16x32_bf16 v[8:11], a[36:39], v[76:79], v[8:11]\n\t"
      "v_mfma_f32_16x16x32_bf16 v[12:15], a[100:103], v[76:79], v[12:15]\n\t"
      "ds_read_b128 v[124:127], v255 offset:31744\n\t"
      "s_waitcnt lgkmcnt(11)\n\t"
      "v_mfma_f32_16x16x32_bf16 v[0:3], a[40:43], v[80:83], v[0:3]\n\t"
      "v_mfma_f32_16x16x32_bf16 v[4:7], a[104:107], v[80:83], v[4:7]\n\t"
      "s_waitcnt lgkmcnt(10)\n\t"
      "v_mfma_f32_16x16x32_bf16 v[8:11], a[40:43], v[84:87], v[8:11]\n\t"
      "v_mfma_f32_16x16x32_bf16 v[12:15], a[104:107], v[84:87], v[12:15]\n\t"
      "s_waitcnt lgkmcnt(9)\n\t"
      "v_mfma_f32_16x16x32_bf16 v[0:3], a[44:47], v[88:91], v[0:3]\n\t"
      "v_mfma_f32_16x16x32_bf16 v[4:7], a[108:111], v[88:91], v[4:7]\n\t"
      "s_waitcnt lgkmcnt(8)\n\t"
      "v_mfma_f32_16x16x32_bf16 v[8:11], a[44:47], v[92:95], v[8:11]\n\t"
      "v_mfma_f32_16x16x32_bf16 v[12:15], a[108:111], v[92:95], v[12:15]\n\t"
      "s_waitcnt lgkmcnt(7)\n\t"
      "v_mfma_f32_16x16x32_bf16 v[0:3], a[48:51], v[96:99], v[0:3]\n\t"
      "v_mfma_f32_16x16x32_bf16 v[4:7], a[112:115], v[96:99], v[4:7]\n\t"
      "s_waitcnt lgkmcnt(6)\n\t"
      "v_mfma_f32_16x16x32_bf16 v[8:11], a[48:51], v[100:103], v[8:11]\n\t"
      "v_mfma_f32_16x16x32_bf16 v[12:15], a[112:115], v[100:103], v[12:15]\n\t"
      "s_waitcnt lgkmcnt(5)\n\t"
      "v_mfma_f32_16x16x32_bf16 v[0:3], a[52:55], v[104:107], v[0:3]\n\t"
      "v_mfma_f32_16x16x32_bf16 v[4:7], a[116:119], v[104:107], v[4:7]\n\t"
      "s_waitcnt lgkmcnt(4)\n\t"
      "v_mfma_f32_16x16x32_bf16 v[8:11], a[52:55], v[108:111], v[8:11]\n\t"
      "v_mfma_f32_16x16x32_bf16 v[12:15], a[116:119], v[108:111], v[12:15]\n\t"
      "s_waitcnt lgkmcnt(3)\n\t"
      "v_mfma_f32_16x16x32_bf16 v[0:3], a[56:59], v[112:115], v[0:3]\n\t"
      "v_mfma_f32_16x16x32_bf16 v[4:7], a[120:123], v[112:115], v[4:7]\n\t"
      "s_waitcnt lgkmcnt(2)\n\t"
      "v_mfma_f32_16x16x32_bf16 v[8:11], a[56:59], v[116:119], v[8:11]\n\t"
      "v_mfma_f32_16x16x32_bf16 v[12:15], a[120:123], v[116:119], v[12:15]\n\t"
      "s_waitcnt lgkmcnt(1)\n\t"
      "v_mfma_f32_16x16x32_bf16 v[0:3], a[60:63], v[120:123], v[0:3]\n\t"
      "v_mfma_f32_16x16x32_bf16 v[4:7], a[124:127], v[120:123], v[4:7]\n\t"
      "s_waitcnt lgkmcnt(0)\n\t"
      "v_mfma_f32_16x16x32_bf16 v[8:11], a[60:63], v[124:127], v[8:11]\n\t"
      "v_mfma_f32_16x16x32_bf16 v[12:15], a[124:127], v[124:127], v[12:15]\n\t"
      "v_xor_b32 v254, 0x8000, v254\n\t"
      "v_xor_b32 v255, 0x8000, v255\n\t"
      "s_add_u32 s69, s53, 0x4000\n\t"
      "s_add_u32 s48, s48, 4\n\t"
      "s_waitcnt vmcnt(40)\n\t"
      "v_cvt_pk_bf16_f32 v0, v0, v1\n\t"
      "v_cvt_pk_bf16_f32 v1, v2, v3\n\t"
      "v_cvt_pk_bf16_f32 v2, v4, v5\n\t"
      "v_cvt_pk_bf16_f32 v3, v6, v7\n\t"
      "v_min_u32 v0, -2, v0\n\t"
      "v_min_u32 v1, -2, v1\n\t"
      "v_min_u32 v2, -2, v2\n\t"
      "v_min_u32 v3, -2, v3\n\t"
      "buffer_store_dwordx4 v[0:3], v238, s[36:39], s53 offen sc1\n\t"
      "v_cvt_pk_bf16_f32 v8, v8, v9\n\t"
      "v_cvt_pk_bf16_f32 v9, v10, v11\n\t"
      "v_cvt_pk_bf16_f32 v10, v12, v13\n\t"
      "v_cvt_pk_bf16_f32 v11, v14, v15\n\t"
      "v_min_u32 v8, -2, v8\n\t"
      "v_min_u32 v9, -2, v9\n\t"
      "v_min_u32 v10, -2, v10\n\t"
      "v_min_u32 v11, -2, v11\n\t"
      "buffer_store_dwordx4 v[8:11], v238, s[36:39], s69 offen sc1\n\t"
      "s_add_u32 s53, s53, 1048576\n\t"
      "s_nop 1\n\t"
      "s_branch WTOP%=\n\t"
      ".p2align 5\n\t"
      "WEND%=:\n\t"
      : "+{v[0:15]}"(c0), "+{v[16:31]}"(c1), "+{v[240:255]}"(c2), "+{v238}"(c3), "+{v[32:39]}"(c4), "+s"(w), "+s"(wv), "+s"(A),
        "+{a[0:31]}"(wo.a), "+{a[32:63]}"(wo.b), "+{a[64:95]}"(wo.c), "+{a[96:127]}"(wo.d)
      :
      : "memory", "vcc", "scc", "v40", "v41", "v42", "v43", "v44", "v45", "v46", "v47", "v48", "v49", "v50", "v51", "v52", "v53", "v54", "v55", "v56", "v57", "v58", "v59", "v60", "v61", "v62", "v63", "v64", "v65", "v66", "v67", "v68", "v69", "v70", "v71", "v72", "v73", "v74", "v75", "v76", "v77", "v78", "v79", "v80", "v81", "v82", "v83", "v84", "v85", "v86", "v87", "v88", "v89", "v90", "v91", "v92", "v93", "v94", "v95", "v96", "v97", "v98", "v99", "v100", "v101", "v102", "v103", "v104", "v105", "v106", "v107", "v108", "v109", "v110", "v111", "v112", "v113", "v114", "v115", "v116", "v117", "v118", "v119", "v120", "v121", "v122", "v123", "v124", "v125", "v126", "v127", "v128", "v129", "v130", "v131", "v132", "v133", "v134", "v135", "v136", "v137", "v138", "v139", "v140", "v141", "v142", "v143", "v144", "v145", "v146", "v147", "v148", "v149", "v150", "v151", "v152", "v153", "v154", "v155", "v156", "v157", "v158", "v159", "v160", "v161", "v162", "v163", "v164", "v165", "v166", "v167", "v168", "v169", "v170", "v171", "v172", "v173", "v174", "v175", "v176", "v177", "v178", "v179", "v180", "v181", "v182", "v183", "v184", "v185", "v186", "v187", "v188", "v189", "v190", "v191", "v192", "v193", "v194", "v195", "v196", "v197", "v198", "v199", "v200", "v201", "v202", "v203", "v204", "v205", "v206", "v207", "v208", "v209", "v210", "v211", "v212", "v213", "v214", "v215", "v216", "v217", "v218", "v219", "v220", "v221", "v222", "v223", "v224", "v225", "v226", "v227", "v228", "v229", "v230", "v231", "v232", "v233", "v234", "v235", "v236", "v237", "v239", "a128", "a129", "a130", "a131", "a132", "a133", "a134", "a135", "a136", "a137", "a138", "a139", "a140", "a141", "a142", "a143", "a144", "a145", "a146", "a147", "a148", "a149", "a150", "a151", "a152", "a153", "a154", "a155", "a156", "a157", "a158", "a159", "a160", "a161", "a162", "a163", "a164", "a165", "a166", "a167", "a168", "a169", "a170", "a171", "a172", "a173", "a174", "a175", "a176", "a177", "a178", "a179", "a180", "a181", "a182", "a183", "a184", "a185", "a186", "a187", "a188", "a189", "a190", "a191", "a192", "a193", "a194", "a195", "a196", "a197", "a198", "a199", "a200", "a201", "a202", "a203", "a204", "a205", "a206", "a207", "a208", "a209", "a210", "a211", "a212", "a213", "a214", "a215", "a216", "a217", "a218", "a219", "a220", "a221", "a222", "a223", "a224", "a225", "a226", "a227", "a228", "a229", "a230", "a231", "a232", "a233", "a234", "a235", "a236", "a237", "a238", "a239", "a240", "a241", "a242", "a243", "a244", "a245", "a246", "a247", "a248", "a249", "a250", "a251", "a252", "a253", "a254", "a255", "s24", "s36", "s37", "s38", "s39", "s40", "s41", "s42", "s43", "s44", "s45", "s46", "s47", "s48", "s49", "s50", "s51", "s52", "s53", "s54", "s55", "s56", "s57", "s58", "s59", "s60", "s61", "s62", "s63", "s64", "s65", "s66", "s67", "s68", "s69", "s70", "s71", "s72", "s73", "s74", "s75", "s76", "s77", "s78", "s79", "s80", "s81", "s82", "s83", "s84", "s85", "s86", "s87", "s88", "s89", "s90", "s91", "s92", "s93", "s94", "s95", "s96", "s97", "s98", "s99");
}

__global__ void __launch_bounds__(256, 1) mega_kernel(MegaParams P) {
  __shared__ __attribute__((aligned(16))) char lds[LDS_BYTES];
  int b, wv;
  KArgs A = (KArgs)__builtin_amdgcn_kernarg_segment_ptr();
  u32x16 ka; u32x8 kb;
  asm volatile("s_load_dwordx16 s[36:51], %4, 0\n\t"
               "s_load_dwordx8 s[52:59], %4, 64\n\t"
               "s_getpc_b64 s[60:61]\n"
               "KPC:\n\t"
               "s_lshr_b32 %0, %5, 3\n\t"
               "s_and_b32 %0, %0, 7\n\t"
               "s_lshl_b32 %0, %0, 14\n\t"
               "v_lshl_add_u32 v254, %6, 6, %0\n\t"
               "v_min_u32 v254, KENDW-KPC, v254\n\t"
               "global_load_dword v255, v254, s[60:61]\n\t"
               "v_readfirstlane_b32 %1, %6\n\t"
               "s_lshr_b32 %0, %5, 1\n\t"
               "s_andn2_b32 %0, %0, 3\n\t"
               "s_and_b32 s60, %5, 3\n\t"
               "s_or_b32 %0, %0, s60\n\t"
               "s_lshr_b32 %1, %1, 6\n\t"
               "s_bitcmp1_b32 %5, 2\n\t"
               "s_cbranch_scc1 GSTART\n\t"
               "s_waitcnt lgkmcnt(0)"
               : "=&{s34}"(b), "=&{s33}"(wv), "=&{s[36:51]}"(ka), "=&{s[52:59]}"(kb), "+{s[0:1]}"(A)
               : "s"(blockIdx.x), "v"(threadIdx.x) : "v252", "v253", "v254", "v255", "s60", "s61", "s62", "s63", "scc");
  static_assert(XS == 0, "");
  if (b >= NREC) {
    int w = b, wv2 = wv;
    WoRegs wo;
    const unsigned tag = worker_role(w, wv2, A, wo);
    worker_tasks(lds, w, wv2, A, tag, wo);
    asm volatile(".p2align 6");
    if (w == 0 && wv2 == 0) {
      const int lane = fresh_lane();
#pragma unroll 1
      for (unsigned spins = 0; spins < (1u << 16); ++spins) {
        const u32x2 sel = top2_wave((const float*)(A->ws + O_RBUF) + S_, lane, spins == 0, spins < 8);
        if (spins && sel[1]) {
          if (lane < 8) __hip_atomic_store((gu32*)(A->ws + O_CNT + lane * 64 + 8), (unsigned)__builtin_amdgcn_s_memrealtime(), RLX_AGENT);
          if (lane < 8) __hip_atomic_store((gu64*)(A->ws + O_CNT + lane * 64), (u64)sel[0] | ((u64)sel[1] << 32), RLX_AGENT);
          break;
        }
        __builtin_amdgcn_s_sleep(SSLEEP);
        if (spins == 0 && SWAIT) {
          unsigned f = 0;
#pragma unroll 1
          for (unsigned n = 0; n < 4096; ++n) {
            f = __builtin_amdgcn_readfirstlane(__hip_atomic_load((gu32*)(A->ws + O_KVSTEP), RLX_AGENT));
            if ((f >> 16) >= 61) break;
            __builtin_amdgcn_s_sleep(4);
          }
          f += SWAIT - ((f >> 16) - 61) * 156;
#pragma unroll 1
          while (((f - (unsigned)__builtin_amdgcn_s_memrealtime()) & 0xffffu) < 400u) __builtin_amdgcn_s_sleep(1);
        }
      }
    }
    __syncthreads();
    if ((w & 2) == 0) mlp2_role(A, lds, (w & 1) * 32 + (w >> 2), wv2 * 64 + fresh_lane());
    asm volatile("KENDW:");
    __builtin_amdgcn_endpgm();
    __builtin_unreachable();
  } else {
    KArgs kp = A;
    rec_role(lds, kp, b, wv, ka, kb);
    asm volatile(".p2align 6");
    const int tid = wv * 64 + fresh_lane();
    __syncthreads();
    if ((b & 31) < M1_WGS / 4) mlp1_role(kp, lds, (b & 31) * 4 + (b >> 5), tid);
    else if (SW2 && b == M1_WGS / 4 + 1) {
      if (wv == 0) {
        const unsigned t0 = (unsigned)__builtin_amdgcn_s_memrealtime();
#pragma unroll 1
        for (unsigned spins = 0; spins < (1u << 16); ++spins) {
          const u32x2 sel = top2_wave((const float*)(kp->ws + O_RBUF) + S_, tid, spins == 0, false);
          if (spins && sel[1]) {
            if (tid < 32) __hip_atomic_store((gu64*)(kp->ws + O_SEL2 + tid * 128), (u64)sel[0] | ((u64)sel[1] << 32), RLX_AGENT);
            break;
          }
#pragma unroll 1
          while ((unsigned)__builtin_amdgcn_s_memrealtime() - t0 < SW2) __builtin_amdgcn_s_sleep(1);
        }
      }
    }
    else if (b == M1_WGS / 4) {
      if (tid == 64) {
        gu32* e = (gu32*)(kp->ws + O_EPOCH);
        __hip_atomic_store(e, mktag(__hip_atomic_load(e, RLX_AGENT)), RLX_AGENT);
      }
    }
    __builtin_amdgcn_endpgm();
    __builtin_unreachable();
  }
}

extern "C" void kernel_launch(void* const* d_in, const int* in_sizes, int n_in, void* d_out, int out_size, void* d_ws, size_t ws_size,
                              hipStream_t stream) {
  MegaParams P;
  P.x = (const u16*)d_in[0]; P.mem0 = (const u16*)d_in[1];
  P.Wq = (const u16*)d_in[2]; P.Wk = (const u16*)d_in[3]; P.Wv = (const u16*)d_in[4]; P.Wo = (const u16*)d_in[5];
  P.w_r = (const u16*)d_in[8]; P.wg = (const u16*)d_in[9]; P.wu = (const u16*)d_in[10]; P.wd = (const u16*)d_in[11];
  P.out = (u16*)d_out; P.ws = (char*)d_ws;
  mega_kernel<<<256, 256, 0, stream>>>(P);
}
```

```cpp
#include <hip/hip_runtime.h>
#include <hip/hip_bf16.h>
#define SWAIT 0
#define SW2 0
#define ASLEEP 32
#define ASALL 0
#define PSLEEP 8
#define FSLEEP 6
#define RDLY 210
#define RDL2 100
#define ADLY 130
#define CPF 1
#define TCH 0
#define SNEED 0
#define MPF 1
#define RDO 100

typedef unsigned short u16;
typedef unsigned long long u64;
typedef __attribute__((ext_vector_type(8))) short bf16x8;
typedef __attribute__((ext_vector_type(4))) float f32x4;
typedef __attribute__((ext_vector_type(4))) unsigned u32x4;
#define GAS __attribute__((address_space(1)))
typedef GAS u64 gu64;
typedef GAS unsigned gu32;
#define RLX_AGENT __ATOMIC_RELAXED, __HIP_MEMORY_SCOPE_AGENT

constexpr int S_ = 2048, DM = 512, NH = 32, DH = 128, MM = 8, SEG = 32, NSEG = 64;
constexpr int HD = NH * DH;
constexpr int QKVN = 3 * HD;
constexpr int FF = 1792;

__device__ __forceinline__ float bf2f(u16 v) { return __builtin_bit_cast(float, (unsigned)v << 16); }
__device__ __forceinline__ u16 f2bf(float f) { unsigned u = __builtin_bit_cast(unsigned, f); return (u16)((u + 0x7fffu + ((u >> 16) & 1u)) >> 16); }
__device__ __forceinline__ float rbf(float f) { return bf2f(f2bf(f)); }

typedef __attribute__((ext_vector_type(2))) float f32x2;
typedef __attribute__((ext_vector_type(2))) __bf16 bf16x2_t;
typedef __attribute__((ext_vector_type(2))) unsigned u32x2;
__device__ __forceinline__ unsigned pk2(float a, float b) { return __builtin_bit_cast(unsigned, __builtin_convertvector((f32x2){a, b}, bf16x2_t)); }
__device__ __forceinline__ unsigned f2u(float f) { return __builtin_bit_cast(unsigned, f); }
__device__ __forceinline__ float u2f(unsigned u) { return __builtin_bit_cast(float, u); }
__device__ __forceinline__ float lo2f(unsigned u) { return __builtin_bit_cast(float, u << 16); }
__device__ __forceinline__ float hi2f(unsigned u) { return __builtin_bit_cast(float, u & 0xffff0000u); }
__device__ __forceinline__ void unpack8(const u32x4 v, float (&f)[8]) {
#pragma unroll
  for (int i = 0; i < 4; ++i) { f[2 * i] = lo2f(v[i]); f[2 * i + 1] = hi2f(v[i]); }
}

constexpr unsigned O_EPOCH = 0;
constexpr unsigned O_TFLAG = 256;
constexpr unsigned O_MFLAG = O_TFLAG + 16 * 32 * 64;
constexpr unsigned O_LFLAG = O_MFLAG + 32 * 128;
constexpr unsigned O_WM = O_LFLAG + 128 * 128;
constexpr unsigned O_KVSTEP = O_WM + 128 * 128;
constexpr unsigned O_CNT = O_KVSTEP + 2048;
constexpr unsigned O_PFLAG = O_CNT + 2048;
constexpr unsigned O_SEL2 = O_PFLAG + 4 * 128 * 128;
constexpr unsigned O_ACT = O_SEL2 + 32 * 128;
constexpr unsigned O_RBUF = O_ACT + 3 * 112 * 128;
constexpr unsigned O_INIT_END = O_RBUF + 2 * S_ * 4 + 256 * 128;
constexpr unsigned O_STAMPS = 196608 + 65 * 32768;
constexpr unsigned O_XN = O_STAMPS + (4 << 20);
constexpr unsigned O_QKV = O_XN + S_ * DM * 2;
constexpr unsigned O_KVM = O_QKV + S_ * QKVN * 2;
constexpr unsigned O_VTM = O_KVM + NSEG * 8 * HD * 2;
constexpr unsigned O_PART = O_VTM + NSEG * 8 * HD * 2;
constexpr unsigned O_HBUF = O_PART + S_ * 8 * DM * 4;
constexpr unsigned O_LNH = O_HBUF + S_ * DM * 2;
constexpr unsigned O_MFRAG = O_LNH + S_ * DM * 2;
constexpr unsigned O_SLOTS = (O_MFRAG + 2 * DM * FF * 2 + 0x1fffff) & ~0x1fffffu;
constexpr unsigned WS_BYTES = O_SLOTS + 65 * 32768;
#define QO(i) (((i) & 1) * 16384 + ((i) >> 1) * 8192)
#define CAL(b) (O_SLOTS + 128 + ((b) >> 3) * 16384 + ((b) & 7) * 1024)
static_assert(O_INIT_END <= O_STAMPS && (O_INIT_END - O_KVSTEP) / 16 <= 128 * 128, "layout");
static_assert(O_TFLAG == 256 && O_MFLAG == 33024 && O_LFLAG == 37120 && O_STAMPS == 2326528 && O_XN == 6520832 && O_QKV == 8617984 && O_KVM == 58949632 && O_VTM == 63143936 && O_PART == 67338240 && O_MFRAG == 105086976 && O_SLOTS == 109051904 && O_CNT + 1024 == 72960 && O_INIT_END <= 262144 && O_WM == 53504, "layout");
constexpr int L_BS = 520, L_QN = 49152, L_TB = 98304, L_VT = 32768;
struct MegaParams {
  const u16* x; const u16* mem0; const u16* Wq; const u16* Wk; const u16* Wv; const u16* Wo;
  const u16* w_r; const u16* wg; const u16* wu; const u16* wd;
  u16* out;
  char* ws;
};
__device__ __forceinline__ unsigned mktag(unsigned e) { return e * 0x9e3779b1u + 0x7f4a7c15u; }

typedef __attribute__((ext_vector_type(4))) short s16x4;
#define LAS __attribute__((address_space(3)))
constexpr int LDS_BYTES = 163840;
#define SB() __builtin_amdgcn_sched_barrier(0)
#define RARE(x) __builtin_expect_with_probability(!!(x), 0, 0.9999)
#define LDS_BARRIER() asm volatile("s_waitcnt lgkmcnt(0)\n\ts_barrier" ::: "memory")
#define VM_DRAIN() asm volatile("s_waitcnt vmcnt(0)" ::: "memory")
__device__ __forceinline__ float xmax16_32(float m) {
  u32x2 p = __builtin_amdgcn_permlane16_swap(f2u(m), f2u(m), false, false);
  m = fmaxf(u2f(p[0]), u2f(p[1]));
  p = __builtin_amdgcn_permlane32_swap(f2u(m), f2u(m), false, false);
  return fmaxf(u2f(p[0]), u2f(p[1]));
}
__device__ __forceinline__ float xsum16_32(float m) {
  u32x2 p = __builtin_amdgcn_permlane16_swap(f2u(m), f2u(m), false, false);
  m = u2f(p[0]) + u2f(p[1]);
  p = __builtin_amdgcn_permlane32_swap(f2u(m), f2u(m), false, false);
  return u2f(p[0]) + u2f(p[1]);
}
__device__ __forceinline__ unsigned wave_max_u32(unsigned v) {
  v = max(v, (unsigned)__builtin_amdgcn_mov_dpp(v, 0xb1, 0xf, 0xf, true));
  v = max(v, (unsigned)__builtin_amdgcn_mov_dpp(v, 0x4e, 0xf, 0xf, true));
  v = max(v, (unsigned)__builtin_amdgcn_mov_dpp(v, 0x124, 0xf, 0xf, true));
  v = max(v, (unsigned)__builtin_amdgcn_mov_dpp(v, 0x128, 0xf, 0xf, true));
  u32x2 p = __builtin_amdgcn_permlane16_swap(v, v, false, false);
  v = max(p[0], p[1]);
  p = __builtin_amdgcn_permlane32_swap(v, v, false, false);
  return max(p[0], p[1]);
}
__device__ __forceinline__ u32x4 ld_sc1(__amdgpu_buffer_rsrc_t rs, unsigned off) { return __builtin_amdgcn_raw_buffer_load_b128(rs, off, 0, 16); }
__device__ __forceinline__ u32x2 ld8_sc1(__amdgpu_buffer_rsrc_t rs, unsigned off) { return __builtin_amdgcn_raw_buffer_load_b64(rs, off, 0, 16); }

#define XS 0
#define VBX __builtin_amdgcn_readfirstlane((int)(((blockIdx.x & 4) << 5) + (blockIdx.x >> 3) * 4 + (blockIdx.x & 3)))
constexpr int NREC = 128, NWORK = 128;
typedef __attribute__((ext_vector_type(8))) unsigned u32x8;
typedef __attribute__((ext_vector_type(16))) unsigned u32x16;
typedef __attribute__((ext_vector_type(32))) unsigned u32x32;
#define RSRC(p, n) __builtin_amdgcn_make_buffer_rsrc((void*)(p), 0, (n), 0x00020000)

__device__ __forceinline__ int fresh_lane() {
  int l;
  asm volatile("v_mbcnt_lo_u32_b32 %0, -1, 0\n\tv_mbcnt_hi_u32_b32 %0, -1, %0" : "=v"(l));
  return l;
}
typedef const __attribute__((address_space(4))) MegaParams* KArgs;
#define ARGS() KArgs A = (KArgs)__builtin_amdgcn_kernarg_segment_ptr(); asm volatile("" : "+s"(A))
#define MS(k)
__device__ __forceinline__ void rec_role(char* lds, KArgs& A, int& b, int& wv, const u32x16 ka, const u32x8 kb) {
#define HMAP 0
  const int h = HMAP == 1 ? (b & 3) * 8 + ((b >> 2) & 7) : (HMAP == 2 ? (b & 1) * 16 + (b & 2) / 2 + ((b >> 2) & 7) * 2 : b & 31), part = b >> 5;
  const int lane = fresh_lane(), l15 = lane & 15, l4 = lane >> 4;
  const int b1 = (l15 >> 1) & 1, mrow = 2 * (l15 >> 2) + (l15 & 1);
  const int r4 = l15 >> 2, c4 = l15 & 3;
#define KA(v, i) ((u64)v[i] | (u64)v[(i) + 1] << 32)
  const u64 wsa = KA(kb, 6), woa = KA(ka, 10), wva = KA(ka, 8), wqa = KA(ka, 4), wka = KA(ka, 6), m0a = KA(ka, 2);
  const unsigned awv = (unsigned)((8 * wv + l4) * 8192 + h * 256 + ((l15 ^ (l4 | (wv << 2))) << 4)), rot = 1 ? part : 0;
  const int sig = 1 ? (h >> 2) & 1 : 0;
  const int sig2 = 1 ? (h >> 3) & 1 : 0;
  unsigned tch;
  asm volatile(
      "s_lshl_b32 s91, %5, 4\n\t"
      "s_add_u32 m0, %5, 0\n\t"
      "s_xor_b32 s90, s91, 0\n\t"
      "buffer_load_dwordx4 %1, %3, s90 offen lds\n\t"
      "s_add_u32 m0, %5, 1024\n\t"
      "s_xor_b32 s90, s91, 16384\n\t"
      "buffer_load_dwordx4 %1, %3, s90 offen lds\n\t"
      "s_add_u32 m0, %5, 8192\n\t"
      "s_xor_b32 s90, s91, 32768\n\t"
      "buffer_load_dwordx4 %1, %3, s90 offen lds\n\t"
      "s_add_u32 m0, %5, 9216\n\t"
      "s_xor_b32 s90, s91, 49152\n\t"
      "buffer_load_dwordx4 %1, %3, s90 offen lds\n\t"
      "s_add_u32 m0, %5, 16384\n\t"
      "s_xor_b32 s90, s91, 65536\n\t"
      "buffer_load_dwordx4 %1, %3, s90 offen lds\n\t"
      "s_add_u32 m0, %5, 17408\n\t"
      "s_xor_b32 s90, s91, 81920\n\t"
      "buffer_load_dwordx4 %1, %3, s90 offen lds\n\t"
      "s_add_u32 m0, %5, 24576\n\t"
      "s_xor_b32 s90, s91, 98304\n\t"
      "buffer_load_dwordx4 %1, %3, s90 offen lds\n\t"
      "s_add_u32 m0, %5, 25600\n\t"
      "s_xor_b32 s90, s91, 114688\n\t"
      "buffer_load_dwordx4 %1, %3, s90 offen lds\n\t"
      : "=&{s87}"(tch)
      : "v"((unsigned)((h * DH + (lane >> 2)) * DM + 128 * part + 32 * wv + 8 * (2 * (lane & 1) + (((lane >> 1) & 1) ^ sig))) * 2), "v"(awv), "s"(RSRC(woa, DM * HD * 2)), "s"(RSRC(wva, DM * HD * 2)),
        "s"(wv * 2048), "s"(rot << 19), "s"(wva), "s"(wqa), "s"(wka), "s"(wsa), "s"(m0a)
      : "memory", "scc", "s90", "s91", "s92");
  SB();
  const unsigned i0 = 1024 + b * 512 + wv * 64 + lane, o1 = O_KVSTEP + ((wv >> 1) * 8192 + b * 64 + lane) * 16;
  const unsigned oz = wv == 2 ? CAL(b ^ 1) + (lane >> 5) * 8192 + ((lane >> 3) & 3) * 256 + (lane & 7) * 16 : ((wv & 1) && o1 < O_INIT_END ? o1 : 0xffffff00u);
  const int sl = lane ^ ((l4 & 1) << 1), su = ((l15 >> 3) * 16 + c4 * 4 + l4) ^ ((l15 >> 3) << 1);
  const int jt = wv ^ rot;
  u32x8 cc = {oz, (unsigned)lane, (unsigned)lane,
              wv == 2 || o1 < O_ACT ? 0u : ~0u,
              (unsigned)(QO(jt >> 1) + (((jt & 1) * 2 + (l4 >> 1)) * 8 + (r4 & 1) * 2 + (l15 >> 3)) * 256 + ((l15 & 1) ^ sig2) * 64 + (2 * b1 + (l4 & 1)) * 16),
              (unsigned)(h * 2048 + (2 * (part & 1) + (l4 >> 1)) * 512 + (2 * b1 + (l4 & 1)) * 128 + mrow * 16),
              (unsigned)(h * 2048 + part * 512 + (l4 >> 1) * 256 + (8 * wv + 4 * (l15 >> 3) + c4) * 16 + (r4 & 1) * 8 + (l4 & 1) * 4),
              (unsigned)((2 * (wv >> 1) + (l4 >> 1)) * 2048 + ((2 * (wv & 1) + (l4 & 1)) * 16 + l15) * 16)};
  u32x8 c3 = {(unsigned)lane, O_SLOTS + (i0 >> 3) * 256 + (i0 & 7) * 16,
              (unsigned)(wv * 2048 + (8 * l4 + r4) * 64 + c4 * 16),
              (unsigned)(wv * 2048 + (r4 >> 1) * 1024 + (l4 * 16 + 8 * (c4 >> 1) + 4 * (r4 & 1) + (c4 & 1)) * 16),
              (unsigned)(L_VT + (8 * r4 + c4) * 256 + (((4 * wv + l4) ^ l15) << 4)),
              awv,
              (unsigned)(L_VT + ((wv & 1) * 32 + 8 * l4 + r4) * 256 + (((4 * part + 2 * (wv >> 1) + (c4 >> 1)) ^ (r4 | (l4 << 2))) << 4) + (c4 & 1) * 8),
              (unsigned)(QO(part) + h * 256 + (lane & 7) * 16)};
  const int L = (h & 7) * 8 + (lane & 7);
  const u64 mp = 1 ? (u64)((const u16*)m0a + (4 * (l15 >> 3) + 2 * ((l15 & 1) ^ sig2) + (r4 & 1)) * DM + 64 * jt + 16 * l4 + 2 * b1) :
                (u64)((const u16*)m0a + (2 * ((L >> 2) & 3) + ((L >> 4) & 1)) * DM + 128 * part + 32 * (h >> 3) + 8 * (L >> 5) + 16 * (L & 1) + (L & 2));
  u32x8 c0 = {(unsigned)((8 * wv + l4) * 8192 + h * 256 + ((l15 ^ (2 * l4 + 8 * (wv & 1))) << 4)),
              (unsigned)(L_TB + (8 * l4 + r4) * 256 + (((4 * wv + (c4 >> 1)) ^ (2 * r4 + 8 * (l4 & 1))) << 4) + (c4 & 1) * 8),
              (unsigned)mp, (unsigned)(mp >> 32), (unsigned)(CAL(b) + wv * 256 + ((wv ^ (b >> 2)) & 1) * 8192) & (unsigned)((b - 32) >> 31) & (unsigned)((1 + 1 - wv) >> 31), (unsigned)(QO(part) + sig * 1024 + sig2 * 512),
              b1 ? 0xEEEEu : 0x4444u, (unsigned)((wv * 8 + (lane >> 5)) * 256 + (lane & 31) * 4)};
  u32x4 c1 = {(unsigned)(sl * 8),
                    (unsigned)(L_QN + ((wv >> 1) * 2 + (r4 & 1)) * L_BS + ((wv & 1) * 32 + su) * 8),
                    (unsigned)lane, (unsigned)lane * 16};
  const unsigned k0 = O_QKV + h * 24576, v0 = O_QKV + 8192 + SEG * QKVN * 2 + h * 24576 + wv * 2048;
  const unsigned role = wv < 2 ? wv : (wv == 3 ? 4 : (part < 2 ? 2 + part : 5));
  u32x16 sc = {(unsigned)lane, (unsigned)wv, role, O_SLOTS, k0, O_MFRAG + (min(h, 27) * 4 + part) * 32768 + wv * 4096, v0, rot << 19, O_KVM, O_VTM,
                     O_TFLAG + h * 64, O_KVSTEP + h * 64 + part * 4, O_STAMPS + (b * 4 + wv) * 65 * 64,
                     O_EPOCH, O_MFLAG + h * 128, (unsigned)wv * 2048};
  u32x16 s2 = {(unsigned)woa, (unsigned)(woa >> 32) & 0xffffu, DM * HD * 2, 0x00020000u, (unsigned)wva, (unsigned)(wva >> 32) & 0xffffu, O_WM, O_WM + b * 128,
               (unsigned)wsa, (unsigned)(wsa >> 32) & 0xffffu, WS_BYTES, 0x00020000u,
               (unsigned)wqa, (unsigned)(wqa >> 32) & 0xffffu, (unsigned)wka, (unsigned)(wka >> 32) & 0xffffu};
  asm volatile(
      "s_nop 1\n\t"
      "v_readfirstlane_b32 s40, v0\n\t"
      "v_readfirstlane_b32 s41, v1\n\t"
      "v_readfirstlane_b32 s42, v2\n\t"
      "v_readfirstlane_b32 s43, v3\n\t"
      "v_readfirstlane_b32 s44, v4\n\t"
      "v_readfirstlane_b32 s45, v5\n\t"
      "v_readfirstlane_b32 s46, v6\n\t"
      "v_readfirstlane_b32 s47, v7\n\t"
      "v_readfirstlane_b32 s48, v8\n\t"
      "v_readfirstlane_b32 s49, v9\n\t"
      "v_readfirstlane_b32 s50, v10\n\t"
      "v_readfirstlane_b32 s51, v11\n\t"
      "v_readfirstlane_b32 s52, v12\n\t"
      "v_readfirstlane_b32 s53, v13\n\t"
      "v_readfirstlane_b32 s54, v14\n\t"
      "v_readfirstlane_b32 s55, v15\n\t"
      "v_readfirstlane_b32 s88, v16\n\t"
      "v_readfirstlane_b32 s89, v17\n\t"
      "v_readfirstlane_b32 s90, v18\n\t"
      "v_readfirstlane_b32 s91, v19\n\t"
      "v_readfirstlane_b32 s92, v20\n\t"
      "v_readfirstlane_b32 s93, v21\n\t"
      "v_readfirstlane_b32 s94, v22\n\t"
      "v_readfirstlane_b32 s95, v23\n\t"
      "v_readfirstlane_b32 s36, v24\n\t"
      "v_readfirstlane_b32 s37, v25\n\t"
      "v_readfirstlane_b32 s38, v26\n\t"
      "v_readfirstlane_b32 s39, v27\n\t"
      "v_readfirstlane_b32 s96, v28\n\t"
      "v_readfirstlane_b32 s97, v29\n\t"
      "v_readfirstlane_b32 s98, v30\n\t"
      "v_readfirstlane_b32 s99, v31\n\t"
      "s_nop 4\n\t"
      "s_memtime s[80:81]\n\t"
      "s_memrealtime s[82:83]\n\t"
      "v_readfirstlane_b32 s86, v120\n\t"
      "v_add_u32 v100, s55, v127\n\t"
      "s_mov_b64 s[88:89], s[92:93]\n\t"
      "s_add_u32 m0, s55, 32768\n\t"
      "s_xor_b32 s61, s47, 0\n\t"
      "buffer_load_dwordx4 v105, s[88:91], s61 offen lds\n\t"
      "s_add_u32 m0, s55, 33792\n\t"
      "s_xor_b32 s61, s47, 32768\n\t"
      "buffer_load_dwordx4 v105, s[88:91], s61 offen lds\n\t"
      "s_add_u32 m0, s55, 40960\n\t"
      "s_xor_b32 s61, s47, 262144\n\t"
      "buffer_load_dwordx4 v105, s[88:91], s61 offen lds\n\t"
      "s_add_u32 m0, s55, 41984\n\t"
      "s_xor_b32 s61, s47, 294912\n\t"
      "buffer_load_dwordx4 v105, s[88:91], s61 offen lds\n\t"
      "s_add_u32 m0, s55, 49152\n\t"
      "s_xor_b32 s61, s47, 524288\n\t"
      "buffer_load_dwordx4 v105, s[88:91], s61 offen lds\n\t"
      "s_add_u32 m0, s55, 50176\n\t"
      "s_xor_b32 s61, s47, 557056\n\t"
      "buffer_load_dwordx4 v105, s[88:91], s61 offen lds\n\t"
      "s_add_u32 m0, s55, 57344\n\t"
      "s_xor_b32 s61, s47, 786432\n\t"
      "buffer_load_dwordx4 v105, s[88:91], s61 offen lds\n\t"
      "s_add_u32 m0, s55, 58368\n\t"
      "s_xor_b32 s61, s47, 819200\n\t"
      "buffer_load_dwordx4 v105, s[88:91], s61 offen lds\n\t"
      "buffer_load_dword v126, off, s[36:39], s53 sc1\n\t"
      "global_load_dword a172, v[118:119], off offset:0\n\t"
      "global_load_dword a173, v[118:119], off offset:8\n\t"
      "global_load_dword a174, v[118:119], off offset:16\n\t"
      "global_load_dword a175, v[118:119], off offset:24\n\t"
      "global_load_dword a236, v[118:119], off offset:512\n\t"
      "global_load_dword a237, v[118:119], off offset:520\n\t"
      "global_load_dword a238, v[118:119], off offset:528\n\t"
      "global_load_dword a239, v[118:119], off offset:536\n\t"
      "s_xor_b32 s61, s86, 0x2000\n\t"
      "s_buffer_load_dword s87, s[36:39], s61\n\t"
      "s_waitcnt vmcnt(17)\n\t"
      "s_waitcnt lgkmcnt(0)\n\t"
      "ds_read_b64_tr_b16 v[64:65], v102\n\t"
      "ds_read_b64_tr_b16 v[66:67], v102 offset:256\n\t"
      "ds_read_b64_tr_b16 v[68:69], v102 offset:8\n\t"
      "ds_read_b64_tr_b16 v[70:71], v102 offset:264\n\t"
      "ds_read_b64_tr_b16 v[72:73], v102 offset:8192\n\t"
      "ds_read_b64_tr_b16 v[74:75], v102 offset:8448\n\t"
      "ds_read_b64_tr_b16 v[76:77], v102 offset:8200\n\t"
      "ds_read_b64_tr_b16 v[78:79], v102 offset:8456\n\t"
      "ds_read_b64_tr_b16 v[80:81], v102 offset:16384\n\t"
      "ds_read_b64_tr_b16 v[82:83], v102 offset:16640\n\t"
      "ds_read_b64_tr_b16 v[84:85], v102 offset:16392\n\t"
      "ds_read_b64_tr_b16 v[86:87], v102 offset:16648\n\t"
      "s_waitcnt lgkmcnt(4)\n\t"
      "ds_read_b64_tr_b16 v[88:89], v102 offset:24576\n\t"
      "ds_read_b64_tr_b16 v[90:91], v102 offset:24832\n\t"
      "ds_read_b64_tr_b16 v[92:93], v102 offset:24584\n\t"
      "ds_read_b64_tr_b16 v[94:95], v102 offset:24840\n\t"
      "s_mov_b64 s[88:89], s[96:97]\n\t"
      "s_add_u32 m0, s55, 98304\n\t"
      "s_xor_b32 s61, s47, 0\n\t"
      "buffer_load_dwordx4 v116, s[88:91], s61 offen lds\n\t"
      "s_add_u32 m0, s55, 99328\n\t"
      "s_xor_b32 s61, s47, 32768\n\t"
      "buffer_load_dwordx4 v116, s[88:91], s61 offen lds\n\t"
      "s_add_u32 m0, s55, 106496\n\t"
      "s_xor_b32 s61, s47, 262144\n\t"
      "buffer_load_dwordx4 v116, s[88:91], s61 offen lds\n\t"
      "s_add_u32 m0, s55, 107520\n\t"
      "s_xor_b32 s61, s47, 294912\n\t"
      "buffer_load_dwordx4 v116, s[88:91], s61 offen lds\n\t"
      "s_waitcnt lgkmcnt(0)\n\t"
      "ds_write_b128 v103, v[64:67]\n\t"
      "ds_write_b128 v103, v[68:71] offset:32\n\t"
      "ds_write_b128 v103, v[72:75] offset:8192\n\t"
      "ds_write_b128 v103, v[76:79] offset:8224\n\t"
      "ds_write_b128 v103, v[80:83] offset:16384\n\t"
      "ds_write_b128 v103, v[84:87] offset:16416\n\t"
      "ds_write_b128 v103, v[88:91] offset:24576\n\t"
      "ds_write_b128 v103, v[92:95] offset:24608\n\t"
      "s_waitcnt lgkmcnt(4)\n\t"
      "ds_read_b128 v[32:35], v100\n\t"
      "ds_read_b128 v[36:39], v100 offset:1024\n\t"
      "ds_read_b128 v[40:43], v100 offset:8192\n\t"
      "ds_read_b128 v[44:47], v100 offset:9216\n\t"
      "ds_read_b128 v[48:51], v100 offset:16384\n\t"
      "ds_read_b128 v[52:55], v100 offset:17408\n\t"
      "ds_read_b128 v[56:59], v100 offset:24576\n\t"
      "ds_read_b128 v[60:63], v100 offset:25600\n\t"
      "v_xor_b32 v118, 32, v117\n\t"
      "v_xor_b32 v119, 64, v104\n\t"
      "v_xor_b32 v102, 128, v104\n\t"
      "v_xor_b32 v103, 192, v104\n\t"
      "s_mov_b64 s[88:89], s[92:93]\n\t"
      "s_add_u32 m0, s55, 65536\n\t"
      "s_xor_b32 s61, s47, 1048576\n\t"
      "buffer_load_dwordx4 v105, s[88:91], s61 offen lds\n\t"
      "s_add_u32 m0, s55, 66560\n\t"
      "s_xor_b32 s61, s47, 1081344\n\t"
      "buffer_load_dwordx4 v105, s[88:91], s61 offen lds\n\t"
      "s_mov_b64 s[88:89], s[96:97]\n\t"
      "s_add_u32 m0, s55, 114688\n\t"
      "s_xor_b32 s61, s47, 524288\n\t"
      "buffer_load_dwordx4 v116, s[88:91], s61 offen lds\n\t"
      "s_add_u32 m0, s55, 115712\n\t"
      "s_xor_b32 s61, s47, 557056\n\t"
      "buffer_load_dwordx4 v116, s[88:91], s61 offen lds\n\t"
      "s_add_u32 m0, s55, 122880\n\t"
      "s_xor_b32 s61, s47, 786432\n\t"
      "buffer_load_dwordx4 v116, s[88:91], s61 offen lds\n\t"
      "s_add_u32 m0, s55, 123904\n\t"
      "s_xor_b32 s61, s47, 819200\n\t"
      "buffer_load_dwordx4 v116, s[88:91], s61 offen lds\n\t"
      "s_waitcnt lgkmcnt(0)\n\t"
      "s_waitcnt vmcnt(25)\n\t"
      "s_barrier\n\t"
      "ds_read_b128 v[0:3], v104\n\t"
      "ds_read_b128 v[4:7], v119\n\t"
      "ds_read_b128 v[8:11], v102\n\t"
      "ds_read_b128 v[12:15], v103\n\t"
      "ds_read_b128 v[64:67], v104 offset:1024\n\t"
      "ds_read_b128 v[68:71], v119 offset:1024\n\t"
      "ds_read_b128 v[72:75], v102 offset:1024\n\t"
      "ds_read_b128 v[76:79], v103 offset:1024\n\t"
      "s_mov_b64 s[88:89], s[92:93]\n\t"
      "s_add_u32 m0, s55, 73728\n\t"
      "s_xor_b32 s61, s47, 1310720\n\t"
      "buffer_load_dwordx4 v105, s[88:91], s61 offen lds\n\t"
      "s_add_u32 m0, s55, 74752\n\t"
      "s_xor_b32 s61, s47, 1343488\n\t"
      "buffer_load_dwordx4 v105, s[88:91], s61 offen lds\n\t"
      "s_mov_b64 s[88:89], s[96:97]\n\t"
      "s_add_u32 m0, s55, 131072\n\t"
      "s_xor_b32 s61, s47, 1048576\n\t"
      "buffer_load_dwordx4 v116, s[88:91], s61 offen lds\n\t"
      "s_add_u32 m0, s55, 132096\n\t"
      "s_xor_b32 s61, s47, 1081344\n\t"
      "buffer_load_dwordx4 v116, s[88:91], s61 offen lds\n\t"
      "s_add_u32 m0, s55, 139264\n\t"
      "s_xor_b32 s61, s47, 1310720\n\t"
      "buffer_load_dwordx4 v116, s[88:91], s61 offen lds\n\t"
      "s_add_u32 m0, s55, 140288\n\t"
      "s_xor_b32 s61, s47, 1343488\n\t"
      "buffer_load_dwordx4 v116, s[88:91], s61 offen lds\n\t"
      "s_mov_b64 s[88:89], s[92:93]\n\t"
      "s_add_u32 m0, s55, 81920\n\t"
      "s_xor_b32 s61, s47, 1572864\n\t"
      "buffer_load_dwordx4 v105, s[88:91], s61 offen lds\n\t"
      "s_add_u32 m0, s55, 82944\n\t"
      "s_xor_b32 s61, s47, 1605632\n\t"
      "buffer_load_dwordx4 v105, s[88:91], s61 offen lds\n\t"
      "s_waitcnt vmcnt(31)\n\t"
      "s_barrier\n\t"
      "s_waitcnt lgkmcnt(7)\n\t"
      "v_mfma_f32_16x16x32_bf16 v[16:19], v[0:3], v[32:35], 0\n\t"
      "ds_read_b128 v[80:83], v104 offset:8192\n\t"
      "v_mfma_f32_16x16x32_bf16 v[20:23], v[0:3], v[36:39], 0\n\t"
      "s_mov_b64 s[88:89], s[96:97]\n\t"
      "s_add_u32 m0, s55, 147456\n\t"
      "s_xor_b32 s61, s47, 1572864\n\t"
      "buffer_load_dwordx4 v116, s[88:91], s61 offen lds\n\t"
      "ds_read_b64_tr_b16 a[176:177], v106\n\t"
      "s_waitcnt lgkmcnt(8)\n\t"
      "v_mfma_f32_16x16x32_bf16 v[16:19], v[4:7], v[40:43], v[16:19]\n\t"
      "ds_read_b128 v[84:87], v119 offset:8192\n\t"
      "v_mfma_f32_16x16x32_bf16 v[20:23], v[4:7], v[44:47], v[20:23]\n\t"
      "s_add_u32 m0, s55, 148480\n\t"
      "s_xor_b32 s61, s47, 1605632\n\t"
      "buffer_load_dwordx4 v116, s[88:91], s61 offen lds\n\t"
      "ds_read_b64_tr_b16 a[178:179], v106 offset:1024\n\t"
      "s_waitcnt lgkmcnt(9)\n\t"
      "v_mfma_f32_16x16x32_bf16 v[16:19], v[8:11], v[48:51], v[16:19]\n\t"
      "ds_read_b128 v[88:91], v102 offset:8192\n\t"
      "v_mfma_f32_16x16x32_bf16 v[20:23], v[8:11], v[52:55], v[20:23]\n\t"
      "s_add_u32 m0, s55, 155648\n\t"
      "s_xor_b32 s61, s47, 1835008\n\t"
      "buffer_load_dwordx4 v116, s[88:91], s61 offen lds\n\t"
      "s_add_u32 m0, s55, 156672\n\t"
      "s_xor_b32 s61, s47, 1867776\n\t"
      "buffer_load_dwordx4 v116, s[88:91], s61 offen lds\n\t"
      "s_waitcnt lgkmcnt(9)\n\t"
      "v_mfma_f32_16x16x32_bf16 v[16:19], v[12:15], v[56:59], v[16:19]\n\t"
      "ds_read_b128 v[92:95], v103 offset:8192\n\t"
      "v_mfma_f32_16x16x32_bf16 v[20:23], v[12:15], v[60:63], v[20:23]\n\t"
      "s_mov_b64 s[88:89], s[92:93]\n\t"
      "s_add_u32 m0, s55, 90112\n\t"
      "s_xor_b32 s61, s47, 1835008\n\t"
      "buffer_load_dwordx4 v105, s[88:91], s61 offen lds\n\t"
      "s_add_u32 m0, s55, 91136\n\t"
      "s_xor_b32 s61, s47, 1867776\n\t"
      "buffer_load_dwordx4 v105, s[88:91], s61 offen lds\n\t"
      "s_waitcnt lgkmcnt(9)\n\t"
      "v_mfma_f32_16x16x32_bf16 v[24:27], v[64:67], v[32:35], 0\n\t"
      "ds_read_b128 v[0:3], v104 offset:9216\n\t"
      "v_mfma_f32_16x16x32_bf16 v[28:31], v[64:67], v[36:39], 0\n\t"
      "s_waitcnt lgkmcnt(9)\n\t"
      "v_mfma_f32_16x16x32_bf16 v[24:27], v[68:71], v[40:43], v[24:27]\n\t"
      "ds_read_b128 v[4:7], v119 offset:9216\n\t"
      "v_cvt_pk_bf16_f32 v128, v16, v17\n\t"
      "v_mfma_f32_16x16x32_bf16 v[28:31], v[68:71], v[44:47], v[28:31]\n\t"
      "v_cvt_pk_bf16_f32 v129, v18, v19\n\t"
      "s_waitcnt lgkmcnt(9)\n\t"
      "v_mfma_f32_16x16x32_bf16 v[24:27], v[72:75], v[48:51], v[24:27]\n\t"
      "ds_read_b128 v[8:11], v102 offset:9216\n\t"
      "v_cvt_pk_bf16_f32 v192, v20, v21\n\t"
      "v_mfma_f32_16x16x32_bf16 v[28:31], v[72:75], v[52:55], v[28:31]\n\t"
      "v_cvt_pk_bf16_f32 v193, v22, v23\n\t"
      "s_waitcnt lgkmcnt(9)\n\t"
      "v_mfma_f32_16x16x32_bf16 v[24:27], v[76:79], v[56:59], v[24:27]\n\t"
      "ds_read_b128 v[12:15], v103 offset:9216\n\t"
      "v_mfma_f32_16x16x32_bf16 v[28:31], v[76:79], v[60:63], v[28:31]\n\t"
      "s_waitcnt vmcnt(35)\n\t"
      "s_waitcnt lgkmcnt(6)\n\t"
      "s_barrier\n\t"
      "v_mfma_f32_16x16x32_bf16 v[16:19], v[80:83], v[32:35], 0\n\t"
      "ds_read_b128 v[64:67], v104 offset:16384\n\t"
      "v_mfma_f32_16x16x32_bf16 v[20:23], v[80:83], v[36:39], 0\n\t"
      "s_add_u32 m0, s55, 32768\n\t"
      "s_xor_b32 s61, s47, 2097152\n\t"
      "buffer_load_dwordx4 v105, s[88:91], s61 offen lds\n\t"
      "s_add_u32 m0, s55, 33792\n\t"
      "s_xor_b32 s61, s47, 2129920\n\t"
      "buffer_load_dwordx4 v105, s[88:91], s61 offen lds\n\t"
      "v_mfma_f32_16x16x32_bf16 v[16:19], v[84:87], v[40:43], v[16:19]\n\t"
      "ds_read_b128 v[68:71], v119 offset:16384\n\t"
      "v_cvt_pk_bf16_f32 v130, v24, v25\n\t"
      "v_mfma_f32_16x16x32_bf16 v[20:23], v[84:87], v[44:47], v[20:23]\n\t"
      "v_cvt_pk_bf16_f32 v131, v26, v27\n\t"
      "s_waitcnt lgkmcnt(7)\n\t"
      "v_mfma_f32_16x16x32_bf16 v[16:19], v[88:91], v[48:51], v[16:19]\n\t"
      "ds_read_b128 v[72:75], v102 offset:16384\n\t"
      "v_cvt_pk_bf16_f32 v194, v28, v29\n\t"
      "v_mfma_f32_16x16x32_bf16 v[20:23], v[88:91], v[52:55], v[20:23]\n\t"
      "v_cvt_pk_bf16_f32 v195, v30, v31\n\t"
      "s_waitcnt lgkmcnt(7)\n\t"
      "v_mfma_f32_16x16x32_bf16 v[16:19], v[92:95], v[56:59], v[16:19]\n\t"
      "ds_read_b128 v[76:79], v103 offset:16384\n\t"
      "v_mfma_f32_16x16x32_bf16 v[20:23], v[92:95], v[60:63], v[20:23]\n\t"
      "v_mfma_f32_16x16x32_bf16 a[168:171], v[128:131], v[128:131], 0\n\t"
      "s_waitcnt lgkmcnt(7)\n\t"
      "v_mfma_f32_16x16x32_bf16 v[24:27], v[0:3], v[32:35], 0\n\t"
      "ds_read_b128 v[80:83], v104 offset:17408\n\t"
      "v_mfma_f32_16x16x32_bf16 v[28:31], v[0:3], v[36:39], 0\n\t"
      "s_waitcnt lgkmcnt(7)\n\t"
      "v_mfma_f32_16x16x32_bf16 v[24:27], v[4:7], v[40:43], v[24:27]\n\t"
      "ds_read_b128 v[84:87], v119 offset:17408\n\t"
      "v_cvt_pk_bf16_f32 v132, v16, v17\n\t"
      "v_mfma_f32_16x16x32_bf16 v[28:31], v[4:7], v[44:47], v[28:31]\n\t"
      "v_cvt_pk_bf16_f32 v133, v18, v19\n\t"
      "s_waitcnt lgkmcnt(7)\n\t"
      "v_mfma_f32_16x16x32_bf16 v[24:27], v[8:11], v[48:51], v[24:27]\n\t"
      "ds_read_b128 v[88:91], v102 offset:17408\n\t"
      "v_cvt_pk_bf16_f32 v196, v20, v21\n\t"
      "v_mfma_f32_16x16x32_bf16 v[28:31], v[8:11], v[52:55], v[28:31]\n\t"
      "v_cvt_pk_bf16_f32 v197, v22, v23\n\t"
      "s_waitcnt lgkmcnt(7)\n\t"
      "v_mfma_f32_16x16x32_bf16 v[24:27], v[12:15], v[56:59], v[24:27]\n\t"
      "ds_read_b128 v[92:95], v103 offset:17408\n\t"
      "v_mfma_f32_16x16x32_bf16 v[28:31], v[12:15], v[60:63], v[28:31]\n\t"
      "v_mfma_f32_16x16x32_bf16 a[232:235], v[192:195], v[192:195], 0\n\t"
      "s_waitcnt vmcnt(35)\n\t"
      "s_waitcnt vmcnt(22)\n\t"
      "s_barrier\n\t"
      "s_waitcnt lgkmcnt(7)\n\t"
      "v_mfma_f32_16x16x32_bf16 v[16:19], v[64:67], v[32:35], 0\n\t"
      "ds_read_b128 v[0:3], v104 offset:24576\n\t"
      "v_mfma_f32_16x16x32_bf16 v[20:23], v[64:67], v[36:39], 0\n\t"
      "s_add_u32 m0, s55, 40960\n\t"
      "s_xor_b32 s61, s47, 2359296\n\t"
      "buffer_load_dwordx4 v105, s[88:91], s61 offen lds\n\t"
      "ds_read_b64_tr_b16 a[180:181], v106 offset:16384\n\t"
      "s_waitcnt lgkmcnt(8)\n\t"
      "v_mfma_f32_16x16x32_bf16 v[16:19], v[68:71], v[40:43], v[16:19]\n\t"
      "ds_read_b128 v[4:7], v119 offset:24576\n\t"
      "v_cvt_pk_bf16_f32 v134, v24, v25\n\t"
      "v_mfma_f32_16x16x32_bf16 v[20:23], v[68:71], v[44:47], v[20:23]\n\t"
      "s_add_u32 m0, s55, 41984\n\t"
      "s_xor_b32 s61, s47, 2392064\n\t"
      "buffer_load_dwordx4 v105, s[88:91], s61 offen lds\n\t"
      "ds_read_b64_tr_b16 a[182:183], v106 offset:17408\n\t"
      "v_cvt_pk_bf16_f32 v135, v26, v27\n\t"
      "s_waitcnt lgkmcnt(9)\n\t"
      "v_mfma_f32_16x16x32_bf16 v[16:19], v[72:75], v[48:51], v[16:19]\n\t"
      "ds_read_b128 v[8:11], v102 offset:24576\n\t"
      "v_cvt_pk_bf16_f32 v198, v28, v29\n\t"
      "v_mfma_f32_16x16x32_bf16 v[20:23], v[72:75], v[52:55], v[20:23]\n\t"
      "ds_read_b64_tr_b16 a[0:1], v117\n\t"
      "ds_read_b64_tr_b16 a[2:3], v117 offset:1024\n\t"
      "v_cvt_pk_bf16_f32 v199, v30, v31\n\t"
      "s_waitcnt lgkmcnt(11)\n\t"
      "v_mfma_f32_16x16x32_bf16 v[16:19], v[76:79], v[56:59], v[16:19]\n\t"
      "ds_read_b128 v[12:15], v103 offset:24576\n\t"
      "v_mfma_f32_16x16x32_bf16 v[20:23], v[76:79], v[60:63], v[20:23]\n\t"
      "ds_read_b64_tr_b16 a[64:65], v118\n\t"
      "ds_read_b64_tr_b16 a[66:67], v118 offset:1024\n\t"
      "v_mfma_f32_16x16x32_bf16 a[168:171], v[132:135], v[132:135], a[168:171]\n\t"
      "s_waitcnt lgkmcnt(13)\n\t"
      "v_mfma_f32_16x16x32_bf16 v[24:27], v[80:83], v[32:35], 0\n\t"
      "ds_read_b128 v[64:67], v104 offset:25600\n\t"
      "v_mfma_f32_16x16x32_bf16 v[28:31], v[80:83], v[36:39], 0\n\t"
      "ds_read_b64_tr_b16 a[4:5], v117 offset:8192\n\t"
      "s_waitcnt lgkmcnt(12)\n\t"
      "ds_read_b64_tr_b16 a[6:7], v117 offset:9216\n\t"
      "v_mfma_f32_16x16x32_bf16 v[24:27], v[84:87], v[40:43], v[24:27]\n\t"
      "ds_read_b128 v[68:71], v119 offset:25600\n\t"
      "v_cvt_pk_bf16_f32 v136, v16, v17\n\t"
      "v_mfma_f32_16x16x32_bf16 v[28:31], v[84:87], v[44:47], v[28:31]\n\t"
      "ds_read_b64_tr_b16 a[68:69], v118 offset:8192\n\t"
      "s_waitcnt lgkmcnt(12)\n\t"
      "ds_read_b64_tr_b16 a[70:71], v118 offset:9216\n\t"
      "v_cvt_pk_bf16_f32 v137, v18, v19\n\t"
      "v_mfma_f32_16x16x32_bf16 v[24:27], v[88:91], v[48:51], v[24:27]\n\t"
      "ds_read_b128 v[72:75], v102 offset:25600\n\t"
      "v_cvt_pk_bf16_f32 v200, v20, v21\n\t"
      "v_mfma_f32_16x16x32_bf16 v[28:31], v[88:91], v[52:55], v[28:31]\n\t"
      "v_cvt_pk_bf16_f32 v201, v22, v23\n\t"
      "v_mfma_f32_16x16x32_bf16 v[24:27], v[92:95], v[56:59], v[24:27]\n\t"
      "ds_read_b128 v[76:79], v103 offset:25600\n\t"
      "v_mfma_f32_16x16x32_bf16 v[28:31], v[92:95], v[60:63], v[28:31]\n\t"
      "v_mfma_f32_16x16x32_bf16 a[232:235], v[196:199], v[196:199], a[232:235]\n\t"
      "s_waitcnt vmcnt(22)\n\t"
      "s_waitcnt vmcnt(18)\n\t"
      "s_waitcnt lgkmcnt(2)\n\t"
      "s_barrier\n\t"
      "v_mfma_f32_16x16x32_bf16 v[16:19], v[0:3], v[32:35], 0\n\t"
      "ds_read_b128 v[80:83], v104 offset:32768\n\t"
      "v_mfma_f32_16x16x32_bf16 v[20:23], v[0:3], v[36:39], 0\n\t"
      "s_add_u32 m0, s55, 49152\n\t"
      "s_xor_b32 s61, s47, 2621440\n\t"
      "buffer_load_dwordx4 v105, s[88:91], s61 offen lds\n\t"
      "ds_read_b64_tr_b16 a[8:9], v117 offset:16384\n\t"
      "v_mfma_f32_16x16x32_bf16 v[16:19], v[4:7], v[40:43], v[16:19]\n\t"
      "ds_read_b128 v[84:87], v119 offset:32768\n\t"
      "v_cvt_pk_bf16_f32 v138, v24, v25\n\t"
      "v_mfma_f32_16x16x32_bf16 v[20:23], v[4:7], v[44:47], v[20:23]\n\t"
      "s_add_u32 m0, s55, 50176\n\t"
      "s_xor_b32 s61, s47, 2654208\n\t"
      "buffer_load_dwordx4 v105, s[88:91], s61 offen lds\n\t"
      "ds_read_b64_tr_b16 a[10:11], v117 offset:17408\n\t"
      "v_cvt_pk_bf16_f32 v139, v26, v27\n\t"
      "v_mfma_f32_16x16x32_bf16 v[16:19], v[8:11], v[48:51], v[16:19]\n\t"
      "ds_read_b128 v[88:91], v102 offset:32768\n\t"
      "v_cvt_pk_bf16_f32 v202, v28, v29\n\t"
      "v_mfma_f32_16x16x32_bf16 v[20:23], v[8:11], v[52:55], v[20:23]\n\t"
      "s_mov_b64 s[88:89], s[96:97]\n\t"
      "s_add_u32 m0, s55, 98304\n\t"
      "s_xor_b32 s61, s47, 2097152\n\t"
      "buffer_load_dwordx4 v116, s[88:91], s61 offen lds\n\t"
      "ds_read_b64_tr_b16 a[72:73], v118 offset:16384\n\t"
      "v_cvt_pk_bf16_f32 v203, v30, v31\n\t"
      "v_mfma_f32_16x16x32_bf16 v[16:19], v[12:15], v[56:59], v[16:19]\n\t"
      "ds_read_b128 v[92:95], v103 offset:32768\n\t"
      "v_mfma_f32_16x16x32_bf16 v[20:23], v[12:15], v[60:63], v[20:23]\n\t"
      "s_add_u32 m0, s55, 99328\n\t"
      "s_xor_b32 s61, s47, 2129920\n\t"
      "buffer_load_dwordx4 v116, s[88:91], s61 offen lds\n\t"
      "ds_read_b64_tr_b16 a[74:75], v118 offset:17408\n\t"
      "v_mfma_f32_16x16x32_bf16 a[168:171], v[136:139], v[136:139], a[168:171]\n\t"
      "v_mfma_f32_16x16x32_bf16 v[24:27], v[64:67], v[32:35], 0\n\t"
      "ds_read_b128 v[0:3], v104 offset:33792\n\t"
      "v_mfma_f32_16x16x32_bf16 v[28:31], v[64:67], v[36:39], 0\n\t"
      "s_add_u32 m0, s55, 106496\n\t"
      "s_xor_b32 s61, s47, 2359296\n\t"
      "buffer_load_dwordx4 v116, s[88:91], s61 offen lds\n\t"
      "ds_read_b64_tr_b16 a[12:13], v117 offset:24576\n\t"
      "v_mfma_f32_16x16x32_bf16 v[24:27], v[68:71], v[40:43], v[24:27]\n\t"
      "ds_read_b128 v[4:7], v119 offset:33792\n\t"
      "v_cvt_pk_bf16_f32 v140, v16, v17\n\t"
      "v_mfma_f32_16x16x32_bf16 v[28:31], v[68:71], v[44:47], v[28:31]\n\t"
      "s_add_u32 m0, s55, 107520\n\t"
      "s_xor_b32 s61, s47, 2392064\n\t"
      "buffer_load_dwordx4 v116, s[88:91], s61 offen lds\n\t"
      "ds_read_b64_tr_b16 a[14:15], v117 offset:25600\n\t"
      "v_cvt_pk_bf16_f32 v141, v18, v19\n\t"
      "s_waitcnt lgkmcnt(13)\n\t"
      "v_mfma_f32_16x16x32_bf16 v[24:27], v[72:75], v[48:51], v[24:27]\n\t"
      "ds_read_b128 v[8:11], v102 offset:33792\n\t"
      "v_cvt_pk_bf16_f32 v204, v20, v21\n\t"
      "v_mfma_f32_16x16x32_bf16 v[28:31], v[72:75], v[52:55], v[28:31]\n\t"
      "ds_read_b64_tr_b16 a[76:77], v118 offset:24576\n\t"
      "s_waitcnt lgkmcnt(12)\n\t"
      "ds_read_b64_tr_b16 a[78:79], v118 offset:25600\n\t"
      "v_cvt_pk_bf16_f32 v205, v22, v23\n\t"
      "v_mfma_f32_16x16x32_bf16 v[24:27], v[76:79], v[56:59], v[24:27]\n\t"
      "ds_read_b128 v[12:15], v103 offset:33792\n\t"
      "v_mfma_f32_16x16x32_bf16 v[28:31], v[76:79], v[60:63], v[28:31]\n\t"
      "v_mfma_f32_16x16x32_bf16 a[232:235], v[200:203], v[200:203], a[232:235]\n\t"
      "s_waitcnt vmcnt(22)\n\t"
      "s_waitcnt vmcnt(18)\n\t"
      "s_waitcnt lgkmcnt(1)\n\t"
      "s_barrier\n\t"
      "v_mfma_f32_16x16x32_bf16 v[16:19], v[80:83], v[32:35], 0\n\t"
      "ds_read_b128 v[64:67], v104 offset:40960\n\t"
      "v_mfma_f32_16x16x32_bf16 v[20:23], v[80:83], v[36:39], 0\n\t"
      "s_mov_b64 s[88:89], s[92:93]\n\t"
      "s_add_u32 m0, s55, 57344\n\t"
      "s_xor_b32 s61, s47, 2883584\n\t"
      "buffer_load_dwordx4 v105, s[88:91], s61 offen lds\n\t"
      "ds_read_b64_tr_b16 a[184:185], v106 offset:32768\n\t"
      "v_mfma_f32_16x16x32_bf16 v[16:19], v[84:87], v[40:43], v[16:19]\n\t"
      "ds_read_b128 v[68:71], v119 offset:40960\n\t"
      "v_cvt_pk_bf16_f32 v142, v24, v25\n\t"
      "v_mfma_f32_16x16x32_bf16 v[20:23], v[84:87], v[44:47], v[20:23]\n\t"
      "s_add_u32 m0, s55, 58368\n\t"
      "s_xor_b32 s61, s47, 2916352\n\t"
      "buffer_load_dwordx4 v105, s[88:91], s61 offen lds\n\t"
      "ds_read_b64_tr_b16 a[186:187], v106 offset:33792\n\t"
      "v_cvt_pk_bf16_f32 v143, v26, v27\n\t"
      "v_mfma_f32_16x16x32_bf16 v[16:19], v[88:91], v[48:51], v[16:19]\n\t"
      "ds_read_b128 v[72:75], v102 offset:40960\n\t"
      "v_cvt_pk_bf16_f32 v206, v28, v29\n\t"
      "v_mfma_f32_16x16x32_bf16 v[20:23], v[88:91], v[52:55], v[20:23]\n\t"
      "s_mov_b64 s[88:89], s[96:97]\n\t"
      "s_add_u32 m0, s55, 114688\n\t"
      "s_xor_b32 s61, s47, 2621440\n\t"
      "buffer_load_dwordx4 v116, s[88:91], s61 offen lds\n\t"
      "ds_read_b64_tr_b16 a[16:17], v117 offset:32768\n\t"
      "v_cvt_pk_bf16_f32 v207, v30, v31\n\t"
      "v_mfma_f32_16x16x32_bf16 v[16:19], v[92:95], v[56:59], v[16:19]\n\t"
      "ds_read_b128 v[76:79], v103 offset:40960\n\t"
      "v_mfma_f32_16x16x32_bf16 v[20:23], v[92:95], v[60:63], v[20:23]\n\t"
      "s_add_u32 m0, s55, 115712\n\t"
      "s_xor_b32 s61, s47, 2654208\n\t"
      "buffer_load_dwordx4 v116, s[88:91], s61 offen lds\n\t"
      "ds_read_b64_tr_b16 a[18:19], v117 offset:33792\n\t"
      "v_mfma_f32_16x16x32_bf16 a[168:171], v[140:143], v[140:143], a[168:171]\n\t"
      "v_mfma_f32_16x16x32_bf16 v[24:27], v[0:3], v[32:35], 0\n\t"
      "ds_read_b128 v[80:83], v104 offset:41984\n\t"
      "v_mfma_f32_16x16x32_bf16 v[28:31], v[0:3], v[36:39], 0\n\t"
      "s_add_u32 m0, s55, 122880\n\t"
      "s_xor_b32 s61, s47, 2883584\n\t"
      "buffer_load_dwordx4 v116, s[88:91], s61 offen lds\n\t"
      "ds_read_b64_tr_b16 a[80:81], v118 offset:32768\n\t"
      "v_mfma_f32_16x16x32_bf16 v[24:27], v[4:7], v[40:43], v[24:27]\n\t"
      "ds_read_b128 v[84:87], v119 offset:41984\n\t"
      "v_cvt_pk_bf16_f32 v144, v16, v17\n\t"
      "v_mfma_f32_16x16x32_bf16 v[28:31], v[4:7], v[44:47], v[28:31]\n\t"
      "s_add_u32 m0, s55, 123904\n\t"
      "s_xor_b32 s61, s47, 2916352\n\t"
      "buffer_load_dwordx4 v116, s[88:91], s61 offen lds\n\t"
      "ds_read_b64_tr_b16 a[82:83], v118 offset:33792\n\t"
      "v_cvt_pk_bf16_f32 v145, v18, v19\n\t"
      "v_mfma_f32_16x16x32_bf16 v[24:27], v[8:11], v[48:51], v[24:27]\n\t"
      "ds_read_b128 v[88:91], v102 offset:41984\n\t"
      "v_cvt_pk_bf16_f32 v208, v20, v21\n\t"
      "v_mfma_f32_16x16x32_bf16 v[28:31], v[8:11], v[52:55], v[28:31]\n\t"
      "ds_read_b64_tr_b16 a[20:21], v117 offset:40960\n\t"
      "s_waitcnt lgkmcnt(12)\n\t"
      "ds_read_b64_tr_b16 a[22:23], v117 offset:41984\n\t"
      "v_cvt_pk_bf16_f32 v209, v22, v23\n\t"
      "v_mfma_f32_16x16x32_bf16 v[24:27], v[12:15], v[56:59], v[24:27]\n\t"
      "ds_read_b128 v[92:95], v103 offset:41984\n\t"
      "v_mfma_f32_16x16x32_bf16 v[28:31], v[12:15], v[60:63], v[28:31]\n\t"
      "ds_read_b64_tr_b16 a[84:85], v118 offset:40960\n\t"
      "s_waitcnt lgkmcnt(12)\n\t"
      "ds_read_b64_tr_b16 a[86:87], v118 offset:41984\n\t"
      "v_mfma_f32_16x16x32_bf16 a[232:235], v[204:207], v[204:207], a[232:235]\n\t"
      "s_waitcnt vmcnt(22)\n\t"
      "s_waitcnt vmcnt(18)\n\t"
      "s_waitcnt lgkmcnt(0)\n\t"
      "s_barrier\n\t"
      "v_mfma_f32_16x16x32_bf16 v[16:19], v[64:67], v[32:35], 0\n\t"
      "ds_read_b128 v[0:3], v104 offset:49152\n\t"
      "v_mfma_f32_16x16x32_bf16 v[20:23], v[64:67], v[36:39], 0\n\t"
      "s_mov_b64 s[88:89], s[92:93]\n\t"
      "s_add_u32 m0, s55, 65536\n\t"
      "s_xor_b32 s61, s47, 3145728\n\t"
      "buffer_load_dwordx4 v105, s[88:91], s61 offen lds\n\t"
      "ds_read_b64_tr_b16 a[24:25], v117 offset:49152\n\t"
      "v_mfma_f32_16x16x32_bf16 v[16:19], v[68:71], v[40:43], v[16:19]\n\t"
      "ds_read_b128 v[4:7], v119 offset:49152\n\t"
      "v_cvt_pk_bf16_f32 v146, v24, v25\n\t"
      "v_mfma_f32_16x16x32_bf16 v[20:23], v[68:71], v[44:47], v[20:23]\n\t"
      "s_add_u32 m0, s55, 66560\n\t"
      "s_xor_b32 s61, s47, 3178496\n\t"
      "buffer_load_dwordx4 v105, s[88:91], s61 offen lds\n\t"
      "ds_read_b64_tr_b16 a[26:27], v117 offset:50176\n\t"
      "v_cvt_pk_bf16_f32 v147, v26, v27\n\t"
      "v_mfma_f32_16x16x32_bf16 v[16:19], v[72:75], v[48:51], v[16:19]\n\t"
      "ds_read_b128 v[8:11], v102 offset:49152\n\t"
      "v_cvt_pk_bf16_f32 v210, v28, v29\n\t"
      "v_mfma_f32_16x16x32_bf16 v[20:23], v[72:75], v[52:55], v[20:23]\n\t"
      "s_mov_b64 s[88:89], s[96:97]\n\t"
      "s_add_u32 m0, s55, 131072\n\t"
      "s_xor_b32 s61, s47, 3145728\n\t"
      "buffer_load_dwordx4 v116, s[88:91], s61 offen lds\n\t"
      "ds_read_b64_tr_b16 a[88:89], v118 offset:49152\n\t"
      "v_cvt_pk_bf16_f32 v211, v30, v31\n\t"
      "v_mfma_f32_16x16x32_bf16 v[16:19], v[76:79], v[56:59], v[16:19]\n\t"
      "ds_read_b128 v[12:15], v103 offset:49152\n\t"
      "v_mfma_f32_16x16x32_bf16 v[20:23], v[76:79], v[60:63], v[20:23]\n\t"
      "s_add_u32 m0, s55, 132096\n\t"
      "s_xor_b32 s61, s47, 3178496\n\t"
      "buffer_load_dwordx4 v116, s[88:91], s61 offen lds\n\t"
      "ds_read_b64_tr_b16 a[90:91], v118 offset:50176\n\t"
      "v_mfma_f32_16x16x32_bf16 a[168:171], v[144:147], v[144:147], a[168:171]\n\t"
      "v_mfma_f32_16x16x32_bf16 v[24:27], v[80:83], v[32:35], 0\n\t"
      "ds_read_b128 v[64:67], v104 offset:50176\n\t"
      "v_mfma_f32_16x16x32_bf16 v[28:31], v[80:83], v[36:39], 0\n\t"
      "s_add_u32 m0, s55, 139264\n\t"
      "s_xor_b32 s61, s47, 3407872\n\t"
      "buffer_load_dwordx4 v116, s[88:91], s61 offen lds\n\t"
      "ds_read_b64_tr_b16 a[28:29], v117 offset:57344\n\t"
      "v_mfma_f32_16x16x32_bf16 v[24:27], v[84:87], v[40:43], v[24:27]\n\t"
      "ds_read_b128 v[68:71], v119 offset:50176\n\t"
      "v_cvt_pk_bf16_f32 v148, v16, v17\n\t"
      "v_mfma_f32_16x16x32_bf16 v[28:31], v[84:87], v[44:47], v[28:31]\n\t"
      "s_add_u32 m0, s55, 140288\n\t"
      "s_xor_b32 s61, s47, 3440640\n\t"
      "buffer_load_dwordx4 v116, s[88:91], s61 offen lds\n\t"
      "ds_read_b64_tr_b16 a[30:31], v117 offset:58368\n\t"
      "v_cvt_pk_bf16_f32 v149, v18, v19\n\t"
      "v_mfma_f32_16x16x32_bf16 v[24:27], v[88:91], v[48:51], v[24:27]\n\t"
      "ds_read_b128 v[72:75], v102 offset:50176\n\t"
      "v_cvt_pk_bf16_f32 v212, v20, v21\n\t"
      "v_mfma_f32_16x16x32_bf16 v[28:31], v[88:91], v[52:55], v[28:31]\n\t"
      "ds_read_b64_tr_b16 a[92:93], v118 offset:57344\n\t"
      "ds_read_b64_tr_b16 a[94:95], v118 offset:58368\n\t"
      "v_cvt_pk_bf16_f32 v213, v22, v23\n\t"
      "v_mfma_f32_16x16x32_bf16 v[24:27], v[92:95], v[56:59], v[24:27]\n\t"
      "s_waitcnt lgkmcnt(12)\n\t"
      "ds_read_b128 v[76:79], v103 offset:50176\n\t"
      "v_mfma_f32_16x16x32_bf16 v[28:31], v[92:95], v[60:63], v[28:31]\n\t"
      "v_mfma_f32_16x16x32_bf16 a[232:235], v[208:211], v[208:211], a[232:235]\n\t"
      "s_waitcnt vmcnt(22)\n\t"
      "s_waitcnt vmcnt(12)\n\t"
      "s_waitcnt lgkmcnt(1)\n\t"
      "s_barrier\n\t"
      "v_mfma_f32_16x16x32_bf16 v[16:19], v[0:3], v[32:35], 0\n\t"
      "ds_read_b128 v[80:83], v104 offset:57344\n\t"
      "v_mfma_f32_16x16x32_bf16 v[20:23], v[0:3], v[36:39], 0\n\t"
      "s_mov_b64 s[88:89], s[92:93]\n\t"
      "s_add_u32 m0, s55, 73728\n\t"
      "s_xor_b32 s61, s47, 3407872\n\t"
      "buffer_load_dwordx4 v105, s[88:91], s61 offen lds\n\t"
      "ds_read_b64_tr_b16 a[188:189], v106 offset:49152\n\t"
      "v_mfma_f32_16x16x32_bf16 v[16:19], v[4:7], v[40:43], v[16:19]\n\t"
      "ds_read_b128 v[84:87], v119 offset:57344\n\t"
      "v_cvt_pk_bf16_f32 v150, v24, v25\n\t"
      "v_mfma_f32_16x16x32_bf16 v[20:23], v[4:7], v[44:47], v[20:23]\n\t"
      "s_add_u32 m0, s55, 74752\n\t"
      "s_xor_b32 s61, s47, 3440640\n\t"
      "buffer_load_dwordx4 v105, s[88:91], s61 offen lds\n\t"
      "ds_read_b64_tr_b16 a[190:191], v106 offset:50176\n\t"
      "v_cvt_pk_bf16_f32 v151, v26, v27\n\t"
      "v_mfma_f32_16x16x32_bf16 v[16:19], v[8:11], v[48:51], v[16:19]\n\t"
      "ds_read_b128 v[88:91], v102 offset:57344\n\t"
      "v_cvt_pk_bf16_f32 v214, v28, v29\n\t"
      "v_mfma_f32_16x16x32_bf16 v[20:23], v[8:11], v[52:55], v[20:23]\n\t"
      "s_mov_b64 s[88:89], s[96:97]\n\t"
      "s_add_u32 m0, s55, 147456\n\t"
      "s_xor_b32 s61, s47, 3670016\n\t"
      "buffer_load_dwordx4 v116, s[88:91], s61 offen lds\n\t"
      "ds_read_b64_tr_b16 a[32:33], v117\n\t"
      "v_cvt_pk_bf16_f32 v215, v30, v31\n\t"
      "v_mfma_f32_16x16x32_bf16 v[16:19], v[12:15], v[56:59], v[16:19]\n\t"
      "ds_read_b128 v[92:95], v103 offset:57344\n\t"
      "v_mfma_f32_16x16x32_bf16 v[20:23], v[12:15], v[60:63], v[20:23]\n\t"
      "s_add_u32 m0, s55, 148480\n\t"
      "s_xor_b32 s61, s47, 3702784\n\t"
      "buffer_load_dwordx4 v116, s[88:91], s61 offen lds\n\t"
      "ds_read_b64_tr_b16 a[34:35], v117 offset:1024\n\t"
      "v_mfma_f32_16x16x32_bf16 a[168:171], v[148:151], v[148:151], a[168:171]\n\t"
      "v_readfirstlane_b32 s53, v126\n\t"
      "s_nop 0\n\t"
      "s_mul_i32 s53, s53, 0x9e3779b1\n\t"
      "s_add_u32 s53, s53, 0x7f4a7c15\n\t"
      "v_mov_b32 v96, 0x20000000\n\t"
      "v_mov_b32 v97, 0x20000000\n\t"
      "v_mov_b32 v98, 0x20000000\n\t"
      "v_mov_b32 v99, 0x20000000\n\t"
      "s_mov_b32 s61, 8192\n\t"
      "buffer_store_dwordx4 v[96:99], v101, s[36:39], 0 offen sc1\n\t"
      "buffer_store_dwordx4 v[96:99], v101, s[36:39], s61 offen sc1\n\t"
      "s_nop 1\n\t"
      "v_mov_b32 v96, v111\n\t"
      "v_mov_b32 v97, v111\n\t"
      "v_mov_b32 v98, v111\n\t"
      "v_mov_b32 v99, v111\n\t"
      "buffer_store_dwordx4 v[96:99], v108, s[36:39], 0 offen sc1\n\t"
      "v_mfma_f32_16x16x32_bf16 v[24:27], v[64:67], v[32:35], 0\n\t"
      "ds_read_b128 v[0:3], v104 offset:58368\n\t"
      "v_mfma_f32_16x16x32_bf16 v[28:31], v[64:67], v[36:39], 0\n\t"
      "s_add_u32 m0, s55, 155648\n\t"
      "s_xor_b32 s61, s47, 3932160\n\t"
      "buffer_load_dwordx4 v116, s[88:91], s61 offen lds\n\t"
      "ds_read_b64_tr_b16 a[96:97], v118\n\t"
      "v_mfma_f32_16x16x32_bf16 v[24:27], v[68:71], v[40:43], v[24:27]\n\t"
      "ds_read_b128 v[4:7], v119 offset:58368\n\t"
      "v_cvt_pk_bf16_f32 v152, v16, v17\n\t"
      "v_mfma_f32_16x16x32_bf16 v[28:31], v[68:71], v[44:47], v[28:31]\n\t"
      "s_add_u32 m0, s55, 156672\n\t"
      "s_xor_b32 s61, s47, 3964928\n\t"
      "buffer_load_dwordx4 v116, s[88:91], s61 offen lds\n\t"
      "ds_read_b64_tr_b16 a[98:99], v118 offset:1024\n\t"
      "v_cvt_pk_bf16_f32 v153, v18, v19\n\t"
      "v_mfma_f32_16x16x32_bf16 v[24:27], v[72:75], v[48:51], v[24:27]\n\t"
      "ds_read_b128 v[8:11], v102 offset:58368\n\t"
      "v_cvt_pk_bf16_f32 v216, v20, v21\n\t"
      "v_mfma_f32_16x16x32_bf16 v[28:31], v[72:75], v[52:55], v[28:31]\n\t"
      "ds_read_b64_tr_b16 a[36:37], v117 offset:8192\n\t"
      "s_waitcnt lgkmcnt(12)\n\t"
      "ds_read_b64_tr_b16 a[38:39], v117 offset:9216\n\t"
      "v_cvt_pk_bf16_f32 v217, v22, v23\n\t"
      "v_mfma_f32_16x16x32_bf16 v[24:27], v[76:79], v[56:59], v[24:27]\n\t"
      "ds_read_b128 v[12:15], v103 offset:58368\n\t"
      "v_mfma_f32_16x16x32_bf16 v[28:31], v[76:79], v[60:63], v[28:31]\n\t"
      "ds_read_b64_tr_b16 a[100:101], v118 offset:8192\n\t"
      "s_waitcnt lgkmcnt(12)\n\t"
      "ds_read_b64_tr_b16 a[102:103], v118 offset:9216\n\t"
      "v_mfma_f32_16x16x32_bf16 a[232:235], v[212:215], v[212:215], a[232:235]\n\t"
      "s_waitcnt vmcnt(15)\n\t"
      "s_waitcnt lgkmcnt(0)\n\t"
      "s_barrier\n\t"
      "v_mfma_f32_16x16x32_bf16 v[16:19], v[80:83], v[32:35], 0\n\t"
      "ds_read_b128 v[64:67], v104\n\t"
      "v_mfma_f32_16x16x32_bf16 v[20:23], v[80:83], v[36:39], 0\n\t"
      "s_mov_b64 s[88:89], s[92:93]\n\t"
      "s_add_u32 m0, s55, 81920\n\t"
      "s_xor_b32 s61, s47, 3670016\n\t"
      "buffer_load_dwordx4 v105, s[88:91], s61 offen lds\n\t"
      "ds_read_b64_tr_b16 a[40:41], v117 offset:16384\n\t"
      "v_mfma_f32_16x16x32_bf16 v[16:19], v[84:87], v[40:43], v[16:19]\n\t"
      "ds_read_b128 v[68:71], v119\n\t"
      "v_cvt_pk_bf16_f32 v154, v24, v25\n\t"
      "v_mfma_f32_16x16x32_bf16 v[20:23], v[84:87], v[44:47], v[20:23]\n\t"
      "s_add_u32 m0, s55, 82944\n\t"
      "s_xor_b32 s61, s47, 3702784\n\t"
      "buffer_load_dwordx4 v105, s[88:91], s61 offen lds\n\t"
      "ds_read_b64_tr_b16 a[42:43], v117 offset:17408\n\t"
      "v_cvt_pk_bf16_f32 v155, v26, v27\n\t"
      "v_mfma_f32_16x16x32_bf16 v[16:19], v[88:91], v[48:51], v[16:19]\n\t"
      "ds_read_b128 v[72:75], v102\n\t"
      "v_cvt_pk_bf16_f32 v218, v28, v29\n\t"
      "v_mfma_f32_16x16x32_bf16 v[20:23], v[88:91], v[52:55], v[20:23]\n\t"
      "s_mov_b64 s[88:89], s[98:99]\n\t"
      "s_add_u32 m0, s55, 98304\n\t"
      "s_xor_b32 s61, s47, 0\n\t"
      "buffer_load_dwordx4 v116, s[88:91], s61 offen lds\n\t"
      "ds_read_b64_tr_b16 a[104:105], v118 offset:16384\n\t"
      "v_cvt_pk_bf16_f32 v219, v30, v31\n\t"
      "v_mfma_f32_16x16x32_bf16 v[16:19], v[92:95], v[56:59], v[16:19]\n\t"
      "ds_read_b128 v[76:79], v103\n\t"
      "v_mfma_f32_16x16x32_bf16 v[20:23], v[92:95], v[60:63], v[20:23]\n\t"
      "s_add_u32 m0, s55, 99328\n\t"
      "s_xor_b32 s61, s47, 32768\n\t"
      "buffer_load_dwordx4 v116, s[88:91], s61 offen lds\n\t"
      "ds_read_b64_tr_b16 a[106:107], v118 offset:17408\n\t"
      "v_mfma_f32_16x16x32_bf16 a[168:171], v[152:155], v[152:155], a[168:171]\n\t"
      "v_mfma_f32_16x16x32_bf16 v[24:27], v[0:3], v[32:35], 0\n\t"
      "ds_read_b128 v[80:83], v104 offset:1024\n\t"
      "v_mfma_f32_16x16x32_bf16 v[28:31], v[0:3], v[36:39], 0\n\t"
      "s_add_u32 m0, s55, 106496\n\t"
      "s_xor_b32 s61, s47, 262144\n\t"
      "buffer_load_dwordx4 v116, s[88:91], s61 offen lds\n\t"
      "ds_read_b64_tr_b16 a[44:45], v117 offset:24576\n\t"
      "v_mfma_f32_16x16x32_bf16 v[24:27], v[4:7], v[40:43], v[24:27]\n\t"
      "ds_read_b128 v[84:87], v119 offset:1024\n\t"
      "v_cvt_pk_bf16_f32 v156, v16, v17\n\t"
      "v_mfma_f32_16x16x32_bf16 v[28:31], v[4:7], v[44:47], v[28:31]\n\t"
      "s_add_u32 m0, s55, 107520\n\t"
      "s_xor_b32 s61, s47, 294912\n\t"
      "buffer_load_dwordx4 v116, s[88:91], s61 offen lds\n\t"
      "ds_read_b64_tr_b16 a[46:47], v117 offset:25600\n\t"
      "v_cvt_pk_bf16_f32 v157, v18, v19\n\t"
      "v_mfma_f32_16x16x32_bf16 v[24:27], v[8:11], v[48:51], v[24:27]\n\t"
      "ds_read_b128 v[88:91], v102 offset:1024\n\t"
      "v_cvt_pk_bf16_f32 v220, v20, v21\n\t"
      "v_mfma_f32_16x16x32_bf16 v[28:31], v[8:11], v[52:55], v[28:31]\n\t"
      "ds_read_b64_tr_b16 a[108:109], v118 offset:24576\n\t"
      "ds_read_b64_tr_b16 a[110:111], v118 offset:25600\n\t"
      "v_cvt_pk_bf16_f32 v221, v22, v23\n\t"
      "v_mfma_f32_16x16x32_bf16 v[24:27], v[12:15], v[56:59], v[24:27]\n\t"
      "s_waitcnt lgkmcnt(12)\n\t"
      "ds_read_b128 v[92:95], v103 offset:1024\n\t"
      "v_mfma_f32_16x16x32_bf16 v[28:31], v[12:15], v[60:63], v[28:31]\n\t"
      "v_mfma_f32_16x16x32_bf16 a[232:235], v[216:219], v[216:219], a[232:235]\n\t"
      "s_waitcnt vmcnt(15)\n\t"
      "s_waitcnt lgkmcnt(1)\n\t"
      "s_barrier\n\t"
      "v_mfma_f32_16x16x32_bf16 v[16:19], v[64:67], v[32:35], 0\n\t"
      "ds_read_b128 v[0:3], v104 offset:8192\n\t"
      "v_mfma_f32_16x16x32_bf16 v[20:23], v[64:67], v[36:39], 0\n\t"
      "s_mov_b64 s[88:89], s[92:93]\n\t"
      "s_add_u32 m0, s55, 90112\n\t"
      "s_xor_b32 s61, s47, 3932160\n\t"
      "buffer_load_dwordx4 v105, s[88:91], s61 offen lds\n\t"
      "ds_read_b64_tr_b16 a[240:241], v106\n\t"
      "v_mfma_f32_16x16x32_bf16 v[16:19], v[68:71], v[40:43], v[16:19]\n\t"
      "ds_read_b128 v[4:7], v119 offset:8192\n\t"
      "v_cvt_pk_bf16_f32 v158, v24, v25\n\t"
      "v_mfma_f32_16x16x32_bf16 v[20:23], v[68:71], v[44:47], v[20:23]\n\t"
      "s_add_u32 m0, s55, 91136\n\t"
      "s_xor_b32 s61, s47, 3964928\n\t"
      "buffer_load_dwordx4 v105, s[88:91], s61 offen lds\n\t"
      "ds_read_b64_tr_b16 a[242:243], v106 offset:1024\n\t"
      "v_cvt_pk_bf16_f32 v159, v26, v27\n\t"
      "v_mfma_f32_16x16x32_bf16 v[16:19], v[72:75], v[48:51], v[16:19]\n\t"
      "ds_read_b128 v[8:11], v102 offset:8192\n\t"
      "v_cvt_pk_bf16_f32 v222, v28, v29\n\t"
      "v_mfma_f32_16x16x32_bf16 v[20:23], v[72:75], v[52:55], v[20:23]\n\t"
      "s_mov_b64 s[88:89], s[98:99]\n\t"
      "s_add_u32 m0, s55, 114688\n\t"
      "s_xor_b32 s61, s47, 524288\n\t"
      "buffer_load_dwordx4 v116, s[88:91], s61 offen lds\n\t"
      "ds_read_b64_tr_b16 a[48:49], v117 offset:32768\n\t"
      "v_cvt_pk_bf16_f32 v223, v30, v31\n\t"
      "v_mfma_f32_16x16x32_bf16 v[16:19], v[76:79], v[56:59], v[16:19]\n\t"
      "ds_read_b128 v[12:15], v103 offset:8192\n\t"
      "v_mfma_f32_16x16x32_bf16 v[20:23], v[76:79], v[60:63], v[20:23]\n\t"
      "s_add_u32 m0, s55, 115712\n\t"
      "s_xor_b32 s61, s47, 557056\n\t"
      "buffer_load_dwordx4 v116, s[88:91], s61 offen lds\n\t"
      "ds_read_b64_tr_b16 a[50:51], v117 offset:33792\n\t"
      "v_mfma_f32_16x16x32_bf16 a[168:171], v[156:159], v[156:159], a[168:171]\n\t"
      "v_mfma_f32_16x16x32_bf16 v[24:27], v[80:83], v[32:35], 0\n\t"
      "ds_read_b128 v[64:67], v104 offset:9216\n\t"
      "v_mfma_f32_16x16x32_bf16 v[28:31], v[80:83], v[36:39], 0\n\t"
      "s_add_u32 m0, s55, 122880\n\t"
      "s_xor_b32 s61, s47, 786432\n\t"
      "buffer_load_dwordx4 v116, s[88:91], s61 offen lds\n\t"
      "ds_read_b64_tr_b16 a[112:113], v118 offset:32768\n\t"
      "v_mfma_f32_16x16x32_bf16 v[24:27], v[84:87], v[40:43], v[24:27]\n\t"
      "ds_read_b128 v[68:71], v119 offset:9216\n\t"
      "v_cvt_pk_bf16_f32 v160, v16, v17\n\t"
      "v_mfma_f32_16x16x32_bf16 v[28:31], v[84:87], v[44:47], v[28:31]\n\t"
      "s_add_u32 m0, s55, 123904\n\t"
      "s_xor_b32 s61, s47, 819200\n\t"
      "buffer_load_dwordx4 v116, s[88:91], s61 offen lds\n\t"
      "ds_read_b64_tr_b16 a[114:115], v118 offset:33792\n\t"
      "v_cvt_pk_bf16_f32 v161, v18, v19\n\t"
      "v_mfma_f32_16x16x32_bf16 v[24:27], v[88:91], v[48:51], v[24:27]\n\t"
      "ds_read_b128 v[72:75], v102 offset:9216\n\t"
      "v_cvt_pk_bf16_f32 v224, v20, v21\n\t"
      "v_mfma_f32_16x16x32_bf16 v[28:31], v[88:91], v[52:55], v[28:31]\n\t"
      "ds_read_b64_tr_b16 a[52:53], v117 offset:40960\n\t"
      "s_waitcnt lgkmcnt(12)\n\t"
      "ds_read_b64_tr_b16 a[54:55], v117 offset:41984\n\t"
      "v_cvt_pk_bf16_f32 v225, v22, v23\n\t"
      "v_mfma_f32_16x16x32_bf16 v[24:27], v[92:95], v[56:59], v[24:27]\n\t"
      "ds_read_b128 v[76:79], v103 offset:9216\n\t"
      "v_mfma_f32_16x16x32_bf16 v[28:31], v[92:95], v[60:63], v[28:31]\n\t"
      "ds_read_b64_tr_b16 a[116:117], v118 offset:40960\n\t"
      "s_waitcnt lgkmcnt(12)\n\t"
      "ds_read_b64_tr_b16 a[118:119], v118 offset:41984\n\t"
      "v_mfma_f32_16x16x32_bf16 a[232:235], v[220:223], v[220:223], a[232:235]\n\t"
      "s_waitcnt vmcnt(12)\n\t"
      "s_waitcnt lgkmcnt(0)\n\t"
      "s_barrier\n\t"
      "v_mfma_f32_16x16x32_bf16 v[16:19], v[0:3], v[32:35], 0\n\t"
      "ds_read_b128 v[80:83], v104 offset:16384\n\t"
      "v_mfma_f32_16x16x32_bf16 v[20:23], v[0:3], v[36:39], 0\n\t"
      "s_add_u32 m0, s55, 131072\n\t"
      "s_xor_b32 s61, s47, 1048576\n\t"
      "buffer_load_dwordx4 v116, s[88:91], s61 offen lds\n\t"
      "ds_read_b64_tr_b16 a[56:57], v117 offset:49152\n\t"
      "v_mfma_f32_16x16x32_bf16 v[16:19], v[4:7], v[40:43], v[16:19]\n\t"
      "ds_read_b128 v[84:87], v119 offset:16384\n\t"
      "v_cvt_pk_bf16_f32 v162, v24, v25\n\t"
      "v_mfma_f32_16x16x32_bf16 v[20:23], v[4:7], v[44:47], v[20:23]\n\t"
      "s_add_u32 m0, s55, 132096\n\t"
      "s_xor_b32 s61, s47, 1081344\n\t"
      "buffer_load_dwordx4 v116, s[88:91], s61 offen lds\n\t"
      "ds_read_b64_tr_b16 a[58:59], v117 offset:50176\n\t"
      "v_cvt_pk_bf16_f32 v163, v26, v27\n\t"
      "v_mfma_f32_16x16x32_bf16 v[16:19], v[8:11], v[48:51], v[16:19]\n\t"
      "ds_read_b128 v[88:91], v102 offset:16384\n\t"
      "v_cvt_pk_bf16_f32 v226, v28, v29\n\t"
      "v_mfma_f32_16x16x32_bf16 v[20:23], v[8:11], v[52:55], v[20:23]\n\t"
      "s_add_u32 m0, s55, 139264\n\t"
      "s_xor_b32 s61, s47, 1310720\n\t"
      "buffer_load_dwordx4 v116, s[88:91], s61 offen lds\n\t"
      "ds_read_b64_tr_b16 a[120:121], v118 offset:49152\n\t"
      "v_cvt_pk_bf16_f32 v227, v30, v31\n\t"
      "v_mfma_f32_16x16x32_bf16 v[16:19], v[12:15], v[56:59], v[16:19]\n\t"
      "ds_read_b128 v[92:95], v103 offset:16384\n\t"
      "v_mfma_f32_16x16x32_bf16 v[20:23], v[12:15], v[60:63], v[20:23]\n\t"
      "s_add_u32 m0, s55, 140288\n\t"
      "s_xor_b32 s61, s47, 1343488\n\t"
      "buffer_load_dwordx4 v116, s[88:91], s61 offen lds\n\t"
      "ds_read_b64_tr_b16 a[122:123], v118 offset:50176\n\t"
      "v_mfma_f32_16x16x32_bf16 a[168:171], v[160:163], v[160:163], a[168:171]\n\t"
      "v_mfma_f32_16x16x32_bf16 v[24:27], v[64:67], v[32:35], 0\n\t"
      "ds_read_b128 v[0:3], v104 offset:17408\n\t"
      "v_mfma_f32_16x16x32_bf16 v[28:31], v[64:67], v[36:39], 0\n\t"
      "ds_read_b64_tr_b16 a[60:61], v117 offset:57344\n\t"
      "ds_read_b64_tr_b16 a[62:63], v117 offset:58368\n\t"
      "v_mfma_f32_16x16x32_bf16 v[24:27], v[68:71], v[40:43], v[24:27]\n\t"
      "ds_read_b128 v[4:7], v119 offset:17408\n\t"
      "v_cvt_pk_bf16_f32 v164, v16, v17\n\t"
      "v_mfma_f32_16x16x32_bf16 v[28:31], v[68:71], v[44:47], v[28:31]\n\t"
      "ds_read_b64_tr_b16 a[124:125], v118 offset:57344\n\t"
      "ds_read_b64_tr_b16 a[126:127], v118 offset:58368\n\t"
      "v_cvt_pk_bf16_f32 v165, v18, v19\n\t"
      "v_mfma_f32_16x16x32_bf16 v[24:27], v[72:75], v[48:51], v[24:27]\n\t"
      "ds_read_b128 v[8:11], v102 offset:17408\n\t"
      "v_cvt_pk_bf16_f32 v228, v20, v21\n\t"
      "v_mfma_f32_16x16x32_bf16 v[28:31], v[72:75], v[52:55], v[28:31]\n\t"
      "v_cvt_pk_bf16_f32 v229, v22, v23\n\t"
      "v_mfma_f32_16x16x32_bf16 v[24:27], v[76:79], v[56:59], v[24:27]\n\t"
      "s_waitcnt lgkmcnt(12)\n\t"
      "ds_read_b128 v[12:15], v103 offset:17408\n\t"
      "v_mfma_f32_16x16x32_bf16 v[28:31], v[76:79], v[60:63], v[28:31]\n\t"
      "v_mfma_f32_16x16x32_bf16 a[232:235], v[224:227], v[224:227], a[232:235]\n\t"
      "s_waitcnt vmcnt(10)\n\t"
      "s_waitcnt lgkmcnt(2)\n\t"
      "s_barrier\n\t"
      "v_mfma_f32_16x16x32_bf16 v[16:19], v[80:83], v[32:35], 0\n\t"
      "ds_read_b128 v[64:67], v104 offset:24576\n\t"
      "v_mfma_f32_16x16x32_bf16 v[20:23], v[80:83], v[36:39], 0\n\t"
      "s_add_u32 m0, s55, 147456\n\t"
      "s_xor_b32 s61, s47, 1572864\n\t"
      "buffer_load_dwordx4 v116, s[88:91], s61 offen lds\n\t"
      "ds_read_b64_tr_b16 a[244:245], v106 offset:16384\n\t"
      "v_mfma_f32_16x16x32_bf16 v[16:19], v[84:87], v[40:43], v[16:19]\n\t"
      "ds_read_b128 v[68:71], v119 offset:24576\n\t"
      "v_cvt_pk_bf16_f32 v166, v24, v25\n\t"
      "v_mfma_f32_16x16x32_bf16 v[20:23], v[84:87], v[44:47], v[20:23]\n\t"
      "s_add_u32 m0, s55, 148480\n\t"
      "s_xor_b32 s61, s47, 1605632\n\t"
      "buffer_load_dwordx4 v116, s[88:91], s61 offen lds\n\t"
      "ds_read_b64_tr_b16 a[246:247], v106 offset:17408\n\t"
      "v_cvt_pk_bf16_f32 v167, v26, v27\n\t"
      "v_mfma_f32_16x16x32_bf16 v[16:19], v[88:91], v[48:51], v[16:19]\n\t"
      "ds_read_b128 v[72:75], v102 offset:24576\n\t"
      "v_cvt_pk_bf16_f32 v230, v28, v29\n\t"
      "v_mfma_f32_16x16x32_bf16 v[20:23], v[88:91], v[52:55], v[20:23]\n\t"
      "s_add_u32 m0, s55, 155648\n\t"
      "s_xor_b32 s61, s47, 1835008\n\t"
      "buffer_load_dwordx4 v116, s[88:91], s61 offen lds\n\t"
      "ds_read_b64_tr_b16 a[128:129], v117\n\t"
      "v_cvt_pk_bf16_f32 v231, v30, v31\n\t"
      "v_mfma_f32_16x16x32_bf16 v[16:19], v[92:95], v[56:59], v[16:19]\n\t"
      "ds_read_b128 v[76:79], v103 offset:24576\n\t"
      "v_mfma_f32_16x16x32_bf16 v[20:23], v[92:95], v[60:63], v[20:23]\n\t"
      "s_add_u32 m0, s55, 156672\n\t"
      "s_xor_b32 s61, s47, 1867776\n\t"
      "buffer_load_dwordx4 v116, s[88:91], s61 offen lds\n\t"
      "ds_read_b64_tr_b16 a[130:131], v117 offset:1024\n\t"
      "v_mfma_f32_16x16x32_bf16 a[168:171], v[164:167], v[164:167], a[168:171]\n\t"
      "v_mfma_f32_16x16x32_bf16 v[24:27], v[0:3], v[32:35], 0\n\t"
      "ds_read_b128 v[80:83], v104 offset:25600\n\t"
      "v_mfma_f32_16x16x32_bf16 v[28:31], v[0:3], v[36:39], 0\n\t"
      "ds_read_b64_tr_b16 a[192:193], v118\n\t"
      "ds_read_b64_tr_b16 a[194:195], v118 offset:1024\n\t"
      "v_mfma_f32_16x16x32_bf16 v[24:27], v[4:7], v[40:43], v[24:27]\n\t"
      "ds_read_b128 v[84:87], v119 offset:25600\n\t"
      "v_cvt_pk_bf16_f32 v168, v16, v17\n\t"
      "v_mfma_f32_16x16x32_bf16 v[28:31], v[4:7], v[44:47], v[28:31]\n\t"
      "ds_read_b64_tr_b16 a[132:133], v117 offset:8192\n\t"
      "s_waitcnt lgkmcnt(12)\n\t"
      "ds_read_b64_tr_b16 a[134:135], v117 offset:9216\n\t"
      "v_cvt_pk_bf16_f32 v169, v18, v19\n\t"
      "v_mfma_f32_16x16x32_bf16 v[24:27], v[8:11], v[48:51], v[24:27]\n\t"
      "ds_read_b128 v[88:91], v102 offset:25600\n\t"
      "v_cvt_pk_bf16_f32 v232, v20, v21\n\t"
      "v_mfma_f32_16x16x32_bf16 v[28:31], v[8:11], v[52:55], v[28:31]\n\t"
      "ds_read_b64_tr_b16 a[196:197], v118 offset:8192\n\t"
      "s_waitcnt lgkmcnt(12)\n\t"
      "ds_read_b64_tr_b16 a[198:199], v118 offset:9216\n\t"
      "v_cvt_pk_bf16_f32 v233, v22, v23\n\t"
      "v_mfma_f32_16x16x32_bf16 v[24:27], v[12:15], v[56:59], v[24:27]\n\t"
      "ds_read_b128 v[92:95], v103 offset:25600\n\t"
      "v_mfma_f32_16x16x32_bf16 v[28:31], v[12:15], v[60:63], v[28:31]\n\t"
      "v_mfma_f32_16x16x32_bf16 a[232:235], v[228:231], v[228:231], a[232:235]\n\t"
      "s_waitcnt vmcnt(8)\n\t"
      "s_waitcnt lgkmcnt(1)\n\t"
      "s_barrier\n\t"
      "v_mfma_f32_16x16x32_bf16 v[16:19], v[64:67], v[32:35], 0\n\t"
      "ds_read_b128 v[0:3], v104 offset:32768\n\t"
      "v_mfma_f32_16x16x32_bf16 v[20:23], v[64:67], v[36:39], 0\n\t"
      "s_add_u32 m0, s55, 98304\n\t"
      "s_xor_b32 s61, s47, 2097152\n\t"
      "buffer_load_dwordx4 v116, s[88:91], s61 offen lds\n\t"
      "ds_read_b64_tr_b16 a[136:137], v117 offset:16384\n\t"
      "v_mfma_f32_16x16x32_bf16 v[16:19], v[68:71], v[40:43], v[16:19]\n\t"
      "ds_read_b128 v[4:7], v119 offset:32768\n\t"
      "v_cvt_pk_bf16_f32 v170, v24, v25\n\t"
      "v_mfma_f32_16x16x32_bf16 v[20:23], v[68:71], v[44:47], v[20:23]\n\t"
      "s_add_u32 m0, s55, 99328\n\t"
      "s_xor_b32 s61, s47, 2129920\n\t"
      "buffer_load_dwordx4 v116, s[88:91], s61 offen lds\n\t"
      "ds_read_b64_tr_b16 a[138:139], v117 offset:17408\n\t"
      "v_cvt_pk_bf16_f32 v171, v26, v27\n\t"
      "v_mfma_f32_16x16x32_bf16 v[16:19], v[72:75], v[48:51], v[16:19]\n\t"
      "ds_read_b128 v[8:11], v102 offset:32768\n\t"
      "v_cvt_pk_bf16_f32 v234, v28, v29\n\t"
      "v_mfma_f32_16x16x32_bf16 v[20:23], v[72:75], v[52:55], v[20:23]\n\t"
      "s_add_u32 m0, s55, 106496\n\t"
      "s_xor_b32 s61, s47, 2359296\n\t"
      "buffer_load_dwordx4 v116, s[88:91], s61 offen lds\n\t"
      "ds_read_b64_tr_b16 a[200:201], v118 offset:16384\n\t"
      "v_cvt_pk_bf16_f32 v235, v30, v31\n\t"
      "v_mfma_f32_16x16x32_bf16 v[16:19], v[76:79], v[56:59], v[16:19]\n\t"
      "ds_read_b128 v[12:15], v103 offset:32768\n\t"
      "v_mfma_f32_16x16x32_bf16 v[20:23], v[76:79], v[60:63], v[20:23]\n\t"
      "s_add_u32 m0, s55, 107520\n\t"
      "s_xor_b32 s61, s47, 2392064\n\t"
      "buffer_load_dwordx4 v116, s[88:91], s61 offen lds\n\t"
      "ds_read_b64_tr_b16 a[202:203], v118 offset:17408\n\t"
      "v_mfma_f32_16x16x32_bf16 a[168:171], v[168:171], v[168:171], a[168:171]\n\t"
      "v_mfma_f32_16x16x32_bf16 v[24:27], v[80:83], v[32:35], 0\n\t"
      "ds_read_b128 v[64:67], v104 offset:33792\n\t"
      "v_mfma_f32_16x16x32_bf16 v[28:31], v[80:83], v[36:39], 0\n\t"
      "ds_read_b64_tr_b16 a[140:141], v117 offset:24576\n\t"
      "ds_read_b64_tr_b16 a[142:143], v117 offset:25600\n\t"
      "v_mfma_f32_16x16x32_bf16 v[24:27], v[84:87], v[40:43], v[24:27]\n\t"
      "ds_read_b128 v[68:71], v119 offset:33792\n\t"
      "v_cvt_pk_bf16_f32 v172, v16, v17\n\t"
      "v_mfma_f32_16x16x32_bf16 v[28:31], v[84:87], v[44:47], v[28:31]\n\t"
      "ds_read_b64_tr_b16 a[204:205], v118 offset:24576\n\t"
      "ds_read_b64_tr_b16 a[206:207], v118 offset:25600\n\t"
      "v_cvt_pk_bf16_f32 v173, v18, v19\n\t"
      "v_mfma_f32_16x16x32_bf16 v[24:27], v[88:91], v[48:51], v[24:27]\n\t"
      "s_waitcnt lgkmcnt(12)\n\t"
      "ds_read_b128 v[72:75], v102 offset:33792\n\t"
      "v_cvt_pk_bf16_f32 v236, v20, v21\n\t"
      "v_mfma_f32_16x16x32_bf16 v[28:31], v[88:91], v[52:55], v[28:31]\n\t"
      "v_cvt_pk_bf16_f32 v237, v22, v23\n\t"
      "v_mfma_f32_16x16x32_bf16 v[24:27], v[92:95], v[56:59], v[24:27]\n\t"
      "ds_read_b128 v[76:79], v103 offset:33792\n\t"
      "v_mfma_f32_16x16x32_bf16 v[28:31], v[92:95], v[60:63], v[28:31]\n\t"
      "v_mfma_f32_16x16x32_bf16 a[232:235], v[232:235], v[232:235], a[232:235]\n\t"
      "s_waitcnt vmcnt(8)\n\t"
      "s_waitcnt lgkmcnt(2)\n\t"
      "s_barrier\n\t"
      "v_mfma_f32_16x16x32_bf16 v[16:19], v[0:3], v[32:35], 0\n\t"
      "ds_read_b128 v[80:83], v104 offset:40960\n\t"
      "v_mfma_f32_16x16x32_bf16 v[20:23], v[0:3], v[36:39], 0\n\t"
      "s_add_u32 m0, s55, 114688\n\t"
      "s_xor_b32 s61, s47, 2621440\n\t"
      "buffer_load_dwordx4 v116, s[88:91], s61 offen lds\n\t"
      "ds_read_b64_tr_b16 a[248:249], v106 offset:32768\n\t"
      "v_mfma_f32_16x16x32_bf16 v[16:19], v[4:7], v[40:43], v[16:19]\n\t"
      "ds_read_b128 v[84:87], v119 offset:40960\n\t"
      "v_cvt_pk_bf16_f32 v174, v24, v25\n\t"
      "v_mfma_f32_16x16x32_bf16 v[20:23], v[4:7], v[44:47], v[20:23]\n\t"
      "s_add_u32 m0, s55, 115712\n\t"
      "s_xor_b32 s61, s47, 2654208\n\t"
      "buffer_load_dwordx4 v116, s[88:91], s61 offen lds\n\t"
      "ds_read_b64_tr_b16 a[250:251], v106 offset:33792\n\t"
      "v_cvt_pk_bf16_f32 v175, v26, v27\n\t"
      "v_mfma_f32_16x16x32_bf16 v[16:19], v[8:11], v[48:51], v[16:19]\n\t"
      "ds_read_b128 v[88:91], v102 offset:40960\n\t"
      "v_cvt_pk_bf16_f32 v238, v28, v29\n\t"
      "v_mfma_f32_16x16x32_bf16 v[20:23], v[8:11], v[52:55], v[20:23]\n\t"
      "s_add_u32 m0, s55, 122880\n\t"
      "s_xor_b32 s61, s47, 2883584\n\t"
      "buffer_load_dwordx4 v116, s[88:91], s61 offen lds\n\t"
      "ds_read_b64_tr_b16 a[144:145], v117 offset:32768\n\t"
      "v_cvt_pk_bf16_f32 v239, v30, v31\n\t"
      "v_mfma_f32_16x16x32_bf16 v[16:19], v[12:15], v[56:59], v[16:19]\n\t"
      "ds_read_b128 v[92:95], v103 offset:40960\n\t"
      "v_mfma_f32_16x16x32_bf16 v[20:23], v[12:15], v[60:63], v[20:23]\n\t"
      "s_add_u32 m0, s55, 123904\n\t"
      "s_xor_b32 s61, s47, 2916352\n\t"
      "buffer_load_dwordx4 v116, s[88:91], s61 offen lds\n\t"
      "ds_read_b64_tr_b16 a[146:147], v117 offset:33792\n\t"
      "v_mfma_f32_16x16x32_bf16 a[168:171], v[172:175], v[172:175], a[168:171]\n\t"
      "v_mfma_f32_16x16x32_bf16 v[24:27], v[64:67], v[32:35], 0\n\t"
      "ds_read_b128 v[0:3], v104 offset:41984\n\t"
      "v_mfma_f32_16x16x32_bf16 v[28:31], v[64:67], v[36:39], 0\n\t"
      "ds_read_b64_tr_b16 a[208:209], v118 offset:32768\n\t"
      "ds_read_b64_tr_b16 a[210:211], v118 offset:33792\n\t"
      "v_mfma_f32_16x16x32_bf16 v[24:27], v[68:71], v[40:43], v[24:27]\n\t"
      "ds_read_b128 v[4:7], v119 offset:41984\n\t"
      "v_cvt_pk_bf16_f32 v176, v16, v17\n\t"
      "v_mfma_f32_16x16x32_bf16 v[28:31], v[68:71], v[44:47], v[28:31]\n\t"
      "ds_read_b64_tr_b16 a[148:149], v117 offset:40960\n\t"
      "s_waitcnt lgkmcnt(12)\n\t"
      "ds_read_b64_tr_b16 a[150:151], v117 offset:41984\n\t"
      "v_cvt_pk_bf16_f32 v177, v18, v19\n\t"
      "v_mfma_f32_16x16x32_bf16 v[24:27], v[72:75], v[48:51], v[24:27]\n\t"
      "ds_read_b128 v[8:11], v102 offset:41984\n\t"
      "v_cvt_pk_bf16_f32 v240, v20, v21\n\t"
      "v_mfma_f32_16x16x32_bf16 v[28:31], v[72:75], v[52:55], v[28:31]\n\t"
      "ds_read_b64_tr_b16 a[212:213], v118 offset:40960\n\t"
      "s_waitcnt lgkmcnt(12)\n\t"
      "ds_read_b64_tr_b16 a[214:215], v118 offset:41984\n\t"
      "v_cvt_pk_bf16_f32 v241, v22, v23\n\t"
      "v_mfma_f32_16x16x32_bf16 v[24:27], v[76:79], v[56:59], v[24:27]\n\t"
      "ds_read_b128 v[12:15], v103 offset:41984\n\t"
      "v_mfma_f32_16x16x32_bf16 v[28:31], v[76:79], v[60:63], v[28:31]\n\t"
      "v_mfma_f32_16x16x32_bf16 a[232:235], v[236:239], v[236:239], a[232:235]\n\t"
      "s_waitcnt vmcnt(8)\n\t"
      "s_waitcnt lgkmcnt(1)\n\t"
      "s_barrier\n\t"
      "v_mfma_f32_16x16x32_bf16 v[16:19], v[80:83], v[32:35], 0\n\t"
      "ds_read_b128 v[64:67], v104 offset:49152\n\t"
      "v_mfma_f32_16x16x32_bf16 v[20:23], v[80:83], v[36:39], 0\n\t"
      "s_add_u32 m0, s55, 131072\n\t"
      "s_xor_b32 s61, s47, 3145728\n\t"
      "buffer_load_dwordx4 v116, s[88:91], s61 offen lds\n\t"
      "ds_read_b64_tr_b16 a[152:153], v117 offset:49152\n\t"
      "v_mfma_f32_16x16x32_bf16 v[16:19], v[84:87], v[40:43], v[16:19]\n\t"
      "ds_read_b128 v[68:71], v119 offset:49152\n\t"
      "v_cvt_pk_bf16_f32 v178, v24, v25\n\t"
      "v_mfma_f32_16x16x32_bf16 v[20:23], v[84:87], v[44:47], v[20:23]\n\t"
      "s_add_u32 m0, s55, 132096\n\t"
      "s_xor_b32 s61, s47, 3178496\n\t"
      "buffer_load_dwordx4 v116, s[88:91], s61 offen lds\n\t"
      "ds_read_b64_tr_b16 a[154:155], v117 offset:50176\n\t"
      "v_cvt_pk_bf16_f32 v179, v26, v27\n\t"
      "v_mfma_f32_16x16x32_bf16 v[16:19], v[88:91], v[48:51], v[16:19]\n\t"
      "ds_read_b128 v[72:75], v102 offset:49152\n\t"
      "v_cvt_pk_bf16_f32 v242, v28, v29\n\t"
      "v_mfma_f32_16x16x32_bf16 v[20:23], v[88:91], v[52:55], v[20:23]\n\t"
      "s_add_u32 m0, s55, 139264\n\t"
      "s_xor_b32 s61, s47, 3407872\n\t"
      "buffer_load_dwordx4 v116, s[88:91], s61 offen lds\n\t"
      "ds_read_b64_tr_b16 a[216:217], v118 offset:49152\n\t"
      "v_cvt_pk_bf16_f32 v243, v30, v31\n\t"
      "v_mfma_f32_16x16x32_bf16 v[16:19], v[92:95], v[56:59], v[16:19]\n\t"
      "ds_read_b128 v[76:79], v103 offset:49152\n\t"
      "v_mfma_f32_16x16x32_bf16 v[20:23], v[92:95], v[60:63], v[20:23]\n\t"
      "s_add_u32 m0, s55, 140288\n\t"
      "s_xor_b32 s61, s47, 3440640\n\t"
      "buffer_load_dwordx4 v116, s[88:91], s61 offen lds\n\t"
      "ds_read_b64_tr_b16 a[218:219], v118 offset:50176\n\t"
      "v_mfma_f32_16x16x32_bf16 a[168:171], v[176:179], v[176:179], a[168:171]\n\t"
      "v_mfma_f32_16x16x32_bf16 v[24:27], v[0:3], v[32:35], 0\n\t"
      "ds_read_b128 v[80:83], v104 offset:50176\n\t"
      "v_mfma_f32_16x16x32_bf16 v[28:31], v[0:3], v[36:39], 0\n\t"
      "ds_read_b64_tr_b16 a[156:157], v117 offset:57344\n\t"
      "ds_read_b64_tr_b16 a[158:159], v117 offset:58368\n\t"
      "v_mfma_f32_16x16x32_bf16 v[24:27], v[4:7], v[40:43], v[24:27]\n\t"
      "ds_read_b128 v[84:87], v119 offset:50176\n\t"
      "v_cvt_pk_bf16_f32 v180, v16, v17\n\t"
      "v_mfma_f32_16x16x32_bf16 v[28:31], v[4:7], v[44:47], v[28:31]\n\t"
      "ds_read_b64_tr_b16 a[220:221], v118 offset:57344\n\t"
      "ds_read_b64_tr_b16 a[222:223], v118 offset:58368\n\t"
      "v_cvt_pk_bf16_f32 v181, v18, v19\n\t"
      "v_mfma_f32_16x16x32_bf16 v[24:27], v[8:11], v[48:51], v[24:27]\n\t"
      "s_waitcnt lgkmcnt(12)\n\t"
      "ds_read_b128 v[88:91], v102 offset:50176\n\t"
      "v_cvt_pk_bf16_f32 v244, v20, v21\n\t"
      "v_mfma_f32_16x16x32_bf16 v[28:31], v[8:11], v[52:55], v[28:31]\n\t"
      "v_cvt_pk_bf16_f32 v245, v22, v23\n\t"
      "v_mfma_f32_16x16x32_bf16 v[24:27], v[12:15], v[56:59], v[24:27]\n\t"
      "ds_read_b128 v[92:95], v103 offset:50176\n\t"
      "v_mfma_f32_16x16x32_bf16 v[28:31], v[12:15], v[60:63], v[28:31]\n\t"
      "v_mfma_f32_16x16x32_bf16 a[232:235], v[240:243], v[240:243], a[232:235]\n\t"
      "s_waitcnt vmcnt(8)\n\t"
      "s_waitcnt lgkmcnt(2)\n\t"
      "s_barrier\n\t"
      "v_mfma_f32_16x16x32_bf16 v[16:19], v[64:67], v[32:35], 0\n\t"
      "ds_read_b128 v[0:3], v104 offset:57344\n\t"
      "v_mfma_f32_16x16x32_bf16 v[20:23], v[64:67], v[36:39], 0\n\t"
      "s_add_u32 m0, s55, 147456\n\t"
      "s_xor_b32 s61, s47, 3670016\n\t"
      "buffer_load_dwordx4 v116, s[88:91], s61 offen lds\n\t"
      "ds_read_b64_tr_b16 a[252:253], v106 offset:49152\n\t"
      "v_mfma_f32_16x16x32_bf16 v[16:19], v[68:71], v[40:43], v[16:19]\n\t"
      "ds_read_b128 v[4:7], v119 offset:57344\n\t"
      "v_cvt_pk_bf16_f32 v182, v24, v25\n\t"
      "v_mfma_f32_16x16x32_bf16 v[20:23], v[68:71], v[44:47], v[20:23]\n\t"
      "s_add_u32 m0, s55, 148480\n\t"
      "s_xor_b32 s61, s47, 3702784\n\t"
      "buffer_load_dwordx4 v116, s[88:91], s61 offen lds\n\t"
      "ds_read_b64_tr_b16 a[254:255], v106 offset:50176\n\t"
      "v_cvt_pk_bf16_f32 v183, v26, v27\n\t"
      "v_mfma_f32_16x16x32_bf16 v[16:19], v[72:75], v[48:51], v[16:19]\n\t"
      "ds_read_b128 v[8:11], v102 offset:57344\n\t"
      "v_cvt_pk_bf16_f32 v246, v28, v29\n\t"
      "v_mfma_f32_16x16x32_bf16 v[20:23], v[72:75], v[52:55], v[20:23]\n\t"
      "s_add_u32 m0, s55, 155648\n\t"
      "s_xor_b32 s61, s47, 3932160\n\t"
      "buffer_load_dwordx4 v116, s[88:91], s61 offen lds\n\t"
      "ds_read_b64_tr_b16 a[160:161], v117\n\t"
      "v_cvt_pk_bf16_f32 v247, v30, v31\n\t"
      "v_mfma_f32_16x16x32_bf16 v[16:19], v[76:79], v[56:59], v[16:19]\n\t"
      "ds_read_b128 v[12:15], v103 offset:57344\n\t"
      "v_mfma_f32_16x16x32_bf16 v[20:23], v[76:79], v[60:63], v[20:23]\n\t"
      "s_add_u32 m0, s55, 156672\n\t"
      "s_xor_b32 s61, s47, 3964928\n\t"
      "buffer_load_dwordx4 v116, s[88:91], s61 offen lds\n\t"
      "ds_read_b64_tr_b16 a[162:163], v117 offset:1024\n\t"
      "v_mfma_f32_16x16x32_bf16 a[168:171], v[180:183], v[180:183], a[168:171]\n\t"
      "v_mfma_f32_16x16x32_bf16 v[24:27], v[80:83], v[32:35], 0\n\t"
      "ds_read_b128 v[64:67], v104 offset:58368\n\t"
      "v_mfma_f32_16x16x32_bf16 v[28:31], v[80:83], v[36:39], 0\n\t"
      "ds_read_b64_tr_b16 a[224:225], v118\n\t"
      "ds_read_b64_tr_b16 a[226:227], v118 offset:1024\n\t"
      "v_mfma_f32_16x16x32_bf16 v[24:27], v[84:87], v[40:43], v[24:27]\n\t"
      "ds_read_b128 v[68:71], v119 offset:58368\n\t"
      "v_cvt_pk_bf16_f32 v184, v16, v17\n\t"
      "v_mfma_f32_16x16x32_bf16 v[28:31], v[84:87], v[44:47], v[28:31]\n\t"
      "ds_read_b64_tr_b16 a[164:165], v117 offset:8192\n\t"
      "s_waitcnt lgkmcnt(12)\n\t"
      "ds_read_b64_tr_b16 a[166:167], v117 offset:9216\n\t"
      "v_cvt_pk_bf16_f32 v185, v18, v19\n\t"
      "v_mfma_f32_16x16x32_bf16 v[24:27], v[88:91], v[48:51], v[24:27]\n\t"
      "ds_read_b128 v[72:75], v102 offset:58368\n\t"
      "v_cvt_pk_bf16_f32 v248, v20, v21\n\t"
      "v_mfma_f32_16x16x32_bf16 v[28:31], v[88:91], v[52:55], v[28:31]\n\t"
      "ds_read_b64_tr_b16 a[228:229], v118 offset:8192\n\t"
      "s_waitcnt lgkmcnt(12)\n\t"
      "ds_read_b64_tr_b16 a[230:231], v118 offset:9216\n\t"
      "v_cvt_pk_bf16_f32 v249, v22, v23\n\t"
      "v_mfma_f32_16x16x32_bf16 v[24:27], v[92:95], v[56:59], v[24:27]\n\t"
      "ds_read_b128 v[76:79], v103 offset:58368\n\t"
      "v_mfma_f32_16x16x32_bf16 v[28:31], v[92:95], v[60:63], v[28:31]\n\t"
      "v_mfma_f32_16x16x32_bf16 a[232:235], v[244:247], v[244:247], a[232:235]\n\t"
      "v_mfma_f32_16x16x32_bf16 v[16:19], v[0:3], v[32:35], 0\n\t"
      "v_mfma_f32_16x16x32_bf16 v[20:23], v[0:3], v[36:39], 0\n\t"
      "v_mfma_f32_16x16x32_bf16 v[16:19], v[4:7], v[40:43], v[16:19]\n\t"
      "v_cvt_pk_bf16_f32 v186, v24, v25\n\t"
      "v_mfma_f32_16x16x32_bf16 v[20:23], v[4:7], v[44:47], v[20:23]\n\t"
      "v_cvt_pk_bf16_f32 v187, v26, v27\n\t"
      "s_waitcnt lgkmcnt(13)\n\t"
      "v_mfma_f32_16x16x32_bf16 v[16:19], v[8:11], v[48:51], v[16:19]\n\t"
      "v_cvt_pk_bf16_f32 v250, v28, v29\n\t"
      "v_mfma_f32_16x16x32_bf16 v[20:23], v[8:11], v[52:55], v[20:23]\n\t"
      "v_cvt_pk_bf16_f32 v251, v30, v31\n\t"
      "s_waitcnt lgkmcnt(11)\n\t"
      "v_mfma_f32_16x16x32_bf16 v[16:19], v[12:15], v[56:59], v[16:19]\n\t"
      "v_mfma_f32_16x16x32_bf16 v[20:23], v[12:15], v[60:63], v[20:23]\n\t"
      "v_mfma_f32_16x16x32_bf16 a[168:171], v[184:187], v[184:187], a[168:171]\n\t"
      "buffer_load_dwordx4 v[96:99], off, s[36:39], s54 sc1\n\t"
      "s_waitcnt lgkmcnt(9)\n\t"
      "v_mfma_f32_16x16x32_bf16 v[24:27], v[64:67], v[32:35], 0\n\t"
      "v_mfma_f32_16x16x32_bf16 v[28:31], v[64:67], v[36:39], 0\n\t"
      "s_waitcnt lgkmcnt(6)\n\t"
      "v_mfma_f32_16x16x32_bf16 v[24:27], v[68:71], v[40:43], v[24:27]\n\t"
      "v_cvt_pk_bf16_f32 v188, v16, v17\n\t"
      "v_mfma_f32_16x16x32_bf16 v[28:31], v[68:71], v[44:47], v[28:31]\n\t"
      "v_cvt_pk_bf16_f32 v189, v18, v19\n\t"
      "s_waitcnt lgkmcnt(3)\n\t"
      "v_mfma_f32_16x16x32_bf16 v[24:27], v[72:75], v[48:51], v[24:27]\n\t"
      "v_cvt_pk_bf16_f32 v252, v20, v21\n\t"
      "v_mfma_f32_16x16x32_bf16 v[28:31], v[72:75], v[52:55], v[28:31]\n\t"
      "v_cvt_pk_bf16_f32 v253, v22, v23\n\t"
      "s_waitcnt lgkmcnt(0)\n\t"
      "v_mfma_f32_16x16x32_bf16 v[24:27], v[76:79], v[56:59], v[24:27]\n\t"
      "v_mfma_f32_16x16x32_bf16 v[28:31], v[76:79], v[60:63], v[28:31]\n\t"
      "v_mfma_f32_16x16x32_bf16 a[232:235], v[248:251], v[248:251], a[232:235]\n\t"
      "v_lshl_add_u32 v8, s41, 10, v127\n\t"
      "v_add_u32 v8, 65536, v8\n\t"
      "v_mov_b32 v12, 0\n\t"
      "v_mov_b32 v9, s41\n\t"
      "v_lshlrev_b32 v9, 2, v9\n\t"
      "v_cvt_pk_bf16_f32 v190, v24, v25\n\t"
      "v_cvt_pk_bf16_f32 v191, v26, v27\n\t"
      "v_cvt_pk_bf16_f32 v254, v28, v29\n\t"
      "v_cvt_pk_bf16_f32 v255, v30, v31\n\t"
      "s_nop 1\n\t"
      "v_mfma_f32_16x16x32_bf16 a[168:171], v[188:191], v[188:191], a[168:171]\n\t"
      "v_mfma_f32_16x16x32_bf16 a[232:235], v[252:255], v[252:255], a[232:235]\n\t"
      "s_sub_u32 s64, s46, 786432\n\t"
      "s_mov_b32 s71, 98304\n\t"
      "s_mov_b32 s63, 0x10000\n\t"
      "s_nop 4\n\t"
      "v_accvgpr_read_b32 v0, a168\n\t"
      "v_accvgpr_read_b32 v1, a169\n\t"
      "v_accvgpr_read_b32 v2, a170\n\t"
      "v_accvgpr_read_b32 v3, a171\n\t"
      "v_accvgpr_read_b32 v4, a232\n\t"
      "v_accvgpr_read_b32 v5, a233\n\t"
      "v_accvgpr_read_b32 v6, a234\n\t"
      "v_accvgpr_read_b32 v7, a235\n\t"
      "v_and_b32 v0, 0x7fffffff, v0\n\t"
      "v_and_b32 v1, 0x7fffffff, v1\n\t"
      "v_and_b32 v2, 0x7fffffff, v2\n\t"
      "v_and_b32 v3, 0x7fffffff, v3\n\t"
      "v_and_b32 v4, 0x7fffffff, v4\n\t"
      "v_and_b32 v5, 0x7fffffff, v5\n\t"
      "v_and_b32 v6, 0x7fffffff, v6\n\t"
      "v_and_b32 v7, 0x7fffffff, v7\n\t"
      "v_max3_u32 v0, v0, v1, v2\n\t"
      "v_max3_u32 v3, v3, v4, v5\n\t"
      "v_max3_u32 v0, v0, v6, v7\n\t"
      "v_max_u32 v0, v0, v3\n\t"
      "s_nop 1\n\t"
      "v_max_u32_dpp v0, v0, v0 quad_perm:[1,0,3,2] row_mask:0xf bank_mask:0xf\n\t"
      "s_nop 1\n\t"
      "v_max_u32_dpp v0, v0, v0 quad_perm:[2,3,0,1] row_mask:0xf bank_mask:0xf\n\t"
      "s_nop 1\n\t"
      "v_max_u32_dpp v0, v0, v0 row_ror:4 row_mask:0xf bank_mask:0xf\n\t"
      "s_nop 1\n\t"
      "v_max_u32_dpp v0, v0, v0 row_ror:8 row_mask:0xf bank_mask:0xf\n\t"
      "v_mov_b32 v1, v0\n\t"
      "s_nop 1\n\t"
      "v_permlane16_swap_b32 v0, v1\n\t"
      "v_max_u32 v0, v0, v1\n\t"
      "v_mov_b32 v1, v0\n\t"
      "s_nop 1\n\t"
      "v_permlane32_swap_b32 v0, v1\n\t"
      "v_max_u32 v0, v0, v1\n\t"
      "ds_write_b32 v9, v0 offset:32768\n\t"
      "s_waitcnt lgkmcnt(0)\n\t"
      "s_barrier\n\t"
      "ds_read_b128 v[4:7], v12 offset:32768\n\t"
      "s_waitcnt lgkmcnt(0)\n\t"
      "v_max_u32 v4, v4, v5\n\t"
      "v_max3_u32 v4, v4, v6, v7\n\t"
      "v_mov_b32 v5, s53\n\t"
      "s_memrealtime s[92:93]\n\t"
      "v_add_u32 v4, 0x10000, v4\n\t"
      "s_mov_b64 exec, 1\n\t"
      "s_mov_b32 s61, 0xffff\n\t"
      "s_waitcnt lgkmcnt(0)\n\t"
      "v_mov_b32 v6, s92\n\t"
      "v_bfi_b32 v4, s61, v6, v4\n\t"
      "buffer_store_dwordx2 v[4:5], off, s[36:39], s95 sc1\n\t"
      "s_mov_b64 exec, -1\n\t"
      "s_waitcnt vmcnt(1)\n\t"
      "v_bitop3_b32 v96, v96, v97, s53 bitop3:0x7e\n\t"
      "v_bitop3_b32 v98, v98, v99, s53 bitop3:0x7e\n\t"
      "v_or_b32 v96, v96, v98\n\t"
      "s_add_u32 m0, s55, 98304\n\t"
      "v_readfirstlane_b32 s56, v96\n\t"
      "s_nop 1\n\t"
      "s_cmp_eq_u32 s56, 0\n\t"
      "s_cbranch_scc0 LF2%=\n\t"
      "buffer_load_dwordx4 v127, s[36:39], s64 offen sc1 lds\n\t"
      "buffer_load_dwordx4 v127, s[36:39], s64 offen offset:1024 sc1 lds\n\t"
      "LF2%=:\n\t"
      "buffer_load_dwordx4 v[96:99], off, s[36:39], s54 sc1\n\t"
      "ds_write_b128 v8, a[176:179]\n\t"
      "ds_write_b128 v8, a[180:183] offset:4096\n\t"
      "ds_write_b128 v8, a[184:187] offset:8192\n\t"
      "ds_write_b128 v8, a[188:191] offset:12288\n\t"
      "ds_write_b128 v8, a[240:243] offset:16384\n\t"
      "ds_write_b128 v8, a[244:247] offset:20480\n\t"
      "ds_write_b128 v8, a[248:251] offset:24576\n\t"
      "ds_write_b128 v8, a[252:255] offset:28672\n\t"
      "ds_write_b128 v100, v[112:115] offset:57344\n\t"
      "s_cbranch_scc1 LF3%=\n\t"
      "LF0%=:\n\t"
      "s_waitcnt vmcnt(0)\n\t"
      "v_bitop3_b32 v96, v96, v97, s53 bitop3:0x7e\n\t"
      "v_bitop3_b32 v98, v98, v99, s53 bitop3:0x7e\n\t"
      "v_or_b32 v96, v96, v98\n\t"
      "s_add_u32 m0, s55, 98304\n\t"
      "v_readfirstlane_b32 s56, v96\n\t"
      "s_nop 1\n\t"
      "s_cmp_eq_u32 s56, 0\n\t"
      "s_cbranch_scc1 LF1%=\n\t"
      "buffer_load_dwordx4 v[96:99], off, s[36:39], s54 sc1\n\t"
      "s_sub_u32 s63, s63, 1\n\t"
      "s_cmp_lg_u32 s63, 0\n\t"
      "s_cbranch_scc1 LF0%=\n\t"
      "LF1%=:\n\t"
      "buffer_load_dwordx4 v127, s[36:39], s64 offen sc1 lds\n\t"
      "buffer_load_dwordx4 v127, s[36:39], s64 offen offset:1024 sc1 lds\n\t"
      "LF3%=:\n\t"
      "v_lshlrev_b32 v52, 4, v127\n\t"
      "s_mov_b32 s62, 0xffff0000\n\t"
      "v_accvgpr_read_b32 v85, a172\n\t"
      "v_accvgpr_read_b32 v87, a173\n\t"
      "v_accvgpr_read_b32 v89, a174\n\t"
      "v_accvgpr_read_b32 v91, a175\n\t"
      "v_accvgpr_read_b32 v93, a236\n\t"
      "v_accvgpr_read_b32 v95, a237\n\t"
      "v_lshl_or_b32 v84, v85, 16, 1\n\t"
      "v_and_or_b32 v85, v85, s62, 1\n\t"
      "v_lshl_or_b32 v86, v87, 16, 1\n\t"
      "v_and_or_b32 v87, v87, s62, 1\n\t"
      "v_lshl_or_b32 v88, v89, 16, 1\n\t"
      "v_and_or_b32 v89, v89, s62, 1\n\t"
      "v_lshl_or_b32 v90, v91, 16, 1\n\t"
      "v_and_or_b32 v91, v91, s62, 1\n\t"
      "v_lshl_or_b32 v92, v93, 16, 1\n\t"
      "v_and_or_b32 v93, v93, s62, 1\n\t"
      "v_lshl_or_b32 v94, v95, 16, 1\n\t"
      "v_and_or_b32 v95, v95, s62, 1\n\t"
      "s_cmp_eq_u32 s86, 0\n\t"
      "s_cbranch_scc1 LC9%=\n\t"
      "s_waitcnt lgkmcnt(0)\n\t"
      "s_memtime s[96:97]\n\t"
      "s_waitcnt lgkmcnt(0)\n\t"
      "s_buffer_load_dword s87, s[36:39], s86\n\t"
      "s_waitcnt lgkmcnt(0)\n\t"
      "s_memtime s[98:99]\n\t"
      "s_waitcnt lgkmcnt(0)\n\t"
      "LC9%=:\n\t"
      "s_sub_u32 s61, s98, s96\n\t"
      "s_lshr_b32 s61, s61, 2\n\t"
      "s_min_u32 s61, s61, 511\n\t"
      "s_sub_u32 s62, 0, s61\n\t"
      "s_bitcmp1_b32 s86, 13\n\t"
      "s_cselect_b32 s61, s62, s61\n\t"
      "s_cmp_eq_u32 s86, 0\n\t"
      "s_cselect_b32 s65, 0, s61\n\t"
      "v_readfirstlane_b32 s97, v121\n\t"
      "v_mov_b32 v65, v115\n\t"
      "v_mov_b32 v64, 0\n\t"
      "v_mov_b32 v66, 0\n\t"
      "v_mov_b32 v67, 0\n\t"
      "v_mov_b32 v109, 0\n\t"
      "v_mov_b32 v110, 0\n\t"
      "v_mov_b32 v111, 0\n\t"
      "s_mov_b32 s40, -1\n\t"
      "s_add_u32 s45, s55, 32768\n\t"
      "v_mov_b32 v126, 0\n\t"
      "s_mov_b32 s56, 0\n\t"
      "s_mov_b32 s58, 1\n\t"
      "s_mov_b32 s59, 0x3e0293ee\n\t"
      "s_mov_b32 s96, 0x40820000\n\t"
      "s_mov_b32 s57, 1.0\n\t"
      "s_add_u32 s68, s43, 8192\n\t"
      "s_add_u32 s97, s97, s43\n\t"
      "s_add_u32 s97, s97, 0x8000\n\t"
      "s_mov_b32 s99, 0\n\t"
      "s_mov_b32 s84, 3\n\t"
      "s_mov_b32 s85, 0x80400000\n\t"
      "s_mov_b32 s26, 2460\n\t"
      "s_mov_b32 s25, 0\n\t"
      "s_memtime s[78:79]\n\t"
      "s_waitcnt lgkmcnt(0)\n\t"
      "s_sub_u32 s78, s78, 4000\n\t"
      "s_mov_b32 s88, s78\n\t"
      "s_mov_b32 s27, s78\n\t"
      "s_mov_b32 s24, s26\n\t"
      "s_mov_b32 s23, s80\n\t"
      "s_mov_b32 s30, 0\n\t"
      "s_add_u32 s50, s50, 32\n\t"
      "s_mov_b32 s72, 0xcccccccc\n\t"
      "s_mov_b32 s73, 0xcccccccc\n\t"
      "s_waitcnt vmcnt(0) lgkmcnt(0)\n\t"
      "s_barrier\n\t"
      "s_mov_b32 m0, s45\n\t"
      "s_sub_u32 s61, 204800, s71\n\t"
      "buffer_load_dwordx4 v65, s[36:39], s44 offen sc1 lds\n\t"
      "buffer_load_dwordx4 v65, s[36:39], s44 offen offset:1024 sc1 lds\n\t"
      "s_add_u32 m0, s55, s61\n\t"
      "s_nop 0\n\t"
      "buffer_load_dwordx4 v127, s[36:39], s46 offen sc1 lds\n\t"
      "buffer_load_dwordx4 v127, s[36:39], s46 offen offset:1024 sc1 lds\n\t"
      "buffer_load_dwordx2 v[48:49], v52, s[36:39], s94 offen sc1\n\t"
      "buffer_load_dwordx2 v[50:51], v52, s[36:39], s94 offen offset:128 sc1\n\t"
      "s_mov_b32 s62, 0xffff0000\n\t"
      "v_accvgpr_read_b32 v97, a238\n\t"
      "v_accvgpr_read_b32 v99, a239\n\t"
      "v_lshl_or_b32 v96, v97, 16, 1\n\t"
      "v_and_or_b32 v97, v97, s62, 1\n\t"
      "v_lshl_or_b32 v98, v99, 16, 1\n\t"
      "v_and_or_b32 v99, v99, s62, 1\n\t"
      "ds_read_b64_tr_b16 a[168:169], v117 offset:16384\n\t"
      "ds_read_b64_tr_b16 a[170:171], v117 offset:17408\n\t"
      "ds_read_b64_tr_b16 a[232:233], v118 offset:16384\n\t"
      "ds_read_b64_tr_b16 a[234:235], v118 offset:17408\n\t"
      "ds_read_b64_tr_b16 a[172:173], v117 offset:24576\n\t"
      "ds_read_b64_tr_b16 a[174:175], v117 offset:25600\n\t"
      "ds_read_b64_tr_b16 a[236:237], v118 offset:24576\n\t"
      "ds_read_b64_tr_b16 a[238:239], v118 offset:25600\n\t"
      "ds_read_b64_tr_b16 a[176:177], v117 offset:32768\n\t"
      "ds_read_b64_tr_b16 a[178:179], v117 offset:33792\n\t"
      "ds_read_b64_tr_b16 a[240:241], v118 offset:32768\n\t"
      "ds_read_b64_tr_b16 a[242:243], v118 offset:33792\n\t"
      "ds_read_b64_tr_b16 a[180:181], v117 offset:40960\n\t"
      "ds_read_b64_tr_b16 a[182:183], v117 offset:41984\n\t"
      "ds_read_b64_tr_b16 a[244:245], v118 offset:40960\n\t"
      "s_waitcnt lgkmcnt(12)\n\t"
      "ds_read_b64_tr_b16 a[246:247], v118 offset:41984\n\t"
      "ds_read_b64_tr_b16 a[184:185], v117 offset:49152\n\t"
      "ds_read_b64_tr_b16 a[186:187], v117 offset:50176\n\t"
      "s_waitcnt lgkmcnt(12)\n\t"
      "ds_read_b64_tr_b16 a[248:249], v118 offset:49152\n\t"
      "ds_read_b64_tr_b16 a[250:251], v118 offset:50176\n\t"
      "ds_read_b64_tr_b16 a[188:189], v117 offset:57344\n\t"
      "s_waitcnt lgkmcnt(12)\n\t"
      "ds_read_b64_tr_b16 a[190:191], v117 offset:58368\n\t"
      "ds_read_b64_tr_b16 a[252:253], v118 offset:57344\n\t"
      "ds_read_b64_tr_b16 a[254:255], v118 offset:58368\n\t"
      "s_waitcnt lgkmcnt(6)\n\t"
      "s_cmp_eq_u32 s41, 1\n\t"
      "s_cbranch_scc1 LWIN_1%=\n\t"
      "s_cmp_eq_u32 s41, 2\n\t"
      "s_cbranch_scc1 LWIN_2%=\n\t"
      "s_cmp_eq_u32 s41, 3\n\t"
      "s_cbranch_scc1 LWIN_3%=\n\t"
      "s_branch LWIN_0%=\n\t"
      ".p2align 6\n\t.fill 16, 4, 0xbf800000\n\t"
      "LPOLL_0%=:\n\t"
      "buffer_load_dwordx4 v[84:87], v60, s[36:39], s43 offen sc1\n\t"
      "buffer_load_dwordx4 v[88:91], v60, s[36:39], s43 offen offset:1024 sc1\n\t"
      "s_waitcnt lgkmcnt(4)\n\t"
      "v_mfma_f32_16x16x32_bf16 v[32:35], v[0:3], v[8:11], v[32:35]\n\t"
      "v_mfma_f32_16x16x32_bf16 v[36:39], v[4:7], v[8:11], v[36:39]\n\t"
      "v_mfma_f32_16x16x32_bf16 v[40:43], v[0:3], v[12:15], v[40:43]\n\t"
      "v_mfma_f32_16x16x32_bf16 v[44:47], v[4:7], v[12:15], v[44:47]\n\t"
      "buffer_load_dwordx4 v[92:95], v60, s[36:39], s68 offen sc1\n\t"
      "buffer_load_dwordx4 v[96:99], v60, s[36:39], s68 offen offset:1024 sc1\n\t"
      "ds_read_b64 v[54:55], v124 offset:53312\n\t"
      "s_xor_b32 s69, s97, 1024\n\t"
      "s_xor_b32 s98, s97, 512\n\t"
      "s_xor_b32 s23, s97, 1536\n\t"
      "s_waitcnt lgkmcnt(1)\n\t"
      "v_mfma_f32_16x16x32_bf16 v[32:35], v[76:79], v[112:115], v[32:35]\n\t"
      "v_mfma_f32_16x16x32_bf16 v[36:39], v[80:83], v[112:115], v[36:39]\n\t"
      "v_mfma_f32_16x16x32_bf16 v[40:43], v[76:79], v[116:119], v[40:43]\n\t"
      "v_mfma_f32_16x16x32_bf16 v[44:47], v[80:83], v[116:119], v[44:47]\n\t"
      "s_waitcnt lgkmcnt(0)\n\t"
      "v_add_f32 v48, v48, v54\n\t"
      "v_add_f32_e64 v49, v49, v55\n\t"
      "v_cvt_pk_bf16_f32 v48, v48, v49\n\t"
      "buffer_store_dword v48, v62, s[36:39], s28 offen sc1\n\t"
      "s_mov_b64 exec, 1\n\t"
      "ds_write_b64 v109, v[110:111] offset:55392\n\t"
      "s_mov_b64 exec, -1\n\t"
      "s_nop 4\n\t"
      "v_cvt_pk_bf16_f32 v68, v32, v33\n\t"
      "v_cvt_pk_bf16_f32 v69, v34, v35\n\t"
      "v_cvt_pk_bf16_f32 v70, v36, v37\n\t"
      "v_cvt_pk_bf16_f32 v71, v38, v39\n\t"
      "v_cvt_pk_bf16_f32 v74, v40, v41\n\t"
      "v_cvt_pk_bf16_f32 v75, v42, v43\n\t"
      "v_cvt_pk_bf16_f32 v72, v44, v45\n\t"
      "v_cvt_pk_bf16_f32 v73, v46, v47\n\t"
      "v_mov_b64 v[32:33], 0\n\t"
      "v_mov_b64 v[34:35], 0\n\t"
      "v_mov_b64 v[36:37], 0\n\t"
      "v_mov_b64 v[38:39], 0\n\t"
      "v_mov_b64 v[40:41], 0\n\t"
      "v_mov_b64 v[42:43], 0\n\t"
      "v_mov_b64 v[44:45], 0\n\t"
      "v_mov_b64 v[46:47], 0\n\t"
      "v_mov_b64 v[48:49], 0\n\t"
      "v_mov_b64 v[50:51], 0\n\t"
      "v_mov_b64 v[52:53], 0\n\t"
      "v_mov_b64 v[54:55], 0\n\t"
      "LGO_0%=:\n\t"
      "s_nop 0\n\t"
      "s_nop 0\n\t"
      "s_nop 0\n\t"
      "s_nop 0\n\t"
      "s_waitcnt vmcnt(4)\n\t"
      "v_pk_fma_f32 v[6:7], v[86:87], s[56:57], v[126:127] op_sel:[0,1,0] op_sel_hi:[1,1,0]\n\t"
      "v_pk_fma_f32 v[4:5], v[84:85], s[56:57], v[126:127] op_sel:[0,1,0] op_sel_hi:[1,1,0]\n\t"
      "v_cvt_pk_bf16_f32 v0, v4, v5\n\t"
      "v_cvt_pk_bf16_f32 v1, v6, v7\n\t"
      "s_nop 0\n\t"
      "ds_write_b64 v127, v[0:1] offset:40960\n\t"
      "s_waitcnt vmcnt(3)\n\t"
      "v_pk_fma_f32 v[8:9], v[88:89], s[56:57], v[126:127] op_sel:[0,1,0] op_sel_hi:[1,1,0]\n\t"
      "v_pk_fma_f32 v[10:11], v[90:91], s[56:57], v[126:127] op_sel:[0,1,0] op_sel_hi:[1,1,0]\n\t"
      "v_cvt_pk_bf16_f32 v2, v8, v9\n\t"
      "v_cvt_pk_bf16_f32 v3, v10, v11\n\t"
      "ds_write_b64 v127, v[2:3] offset:40968\n\t"
      "v_bitop3_b32 v64, v84, v85, v86 bitop3:0x80\n\t"
      "v_mfma_f32_16x16x32_bf16 v[56:59], v[0:3], v[0:3], 0\n\t"
      "v_bitop3_b32 v64, v64, v87, v88 bitop3:0x80\n\t"
      "v_bitop3_b32 v64, v64, v89, v90 bitop3:0x80\n\t"
      "v_bitop3_b32 v64, v64, v91, s58 bitop3:0x80\n\t"
      "v_cmp_eq_u32_e64 vcc, 0, v64\n\t"
      "v_smfmac_f32_16x16x64_bf16 v[36:39], v[0:3], a[64:71], v122\n\t"
      "s_nop 0\n\t"
      "v_bitop3_b32 v120, v120, v121, s53 bitop3:0x7e\n\t"
      "v_smfmac_f32_16x16x64_bf16 v[44:47], v[0:3], a[192:199], v122\n\t"
      "s_cbranch_vccnz LR0_0%=\n\t"
      "LC0_0%=:\n\t"
      "s_waitcnt lgkmcnt(0)\n\t"
      "s_barrier\n\t"
      "ds_read_b128 v[4:7], v127 offset:41984\n\t"
      "v_smfmac_f32_16x16x64_bf16 v[52:55], v[0:3], v[192:199], v122\n\t"
      "ds_read_b128 v[8:11], v127 offset:43008\n\t"
      "v_smfmac_f32_16x16x64_bf16 v[48:51], v[0:3], v[128:135], v122\n\t"
      "ds_read_b128 v[12:15], v127 offset:44032\n\t"
      "v_smfmac_f32_16x16x64_bf16 v[32:35], v[0:3], a[0:7], v122\n\t"
      "ds_read_b128 v[84:87], v127 offset:36864\n\t"
      "v_smfmac_f32_16x16x64_bf16 v[40:43], v[0:3], a[128:135], v122\n\t"
      "s_waitcnt lgkmcnt(3)\n\t"
      "v_smfmac_f32_16x16x64_bf16 v[32:35], v[4:7], a[8:15], v122\n\t"
      "ds_read_b128 v[80:83], v127 offset:35840\n\t"
      "v_smfmac_f32_16x16x64_bf16 v[40:43], v[4:7], a[136:143], v122\n\t"
      "s_waitcnt vmcnt(2)\n\t"
      "v_fma_f32 v23, v95, s57, v126\n\t"
      "v_fma_f32 v20, v92, s57, v126\n\t"
      "v_smfmac_f32_16x16x64_bf16 v[36:39], v[4:7], a[72:79], v122\n\t"
      "v_fma_f32 v22, v94, s57, v126\n\t"
      "v_cvt_pk_bf16_f32 v17, v22, v23\n\t"
      "v_bitop3_b32 v64, v92, v93, v94 bitop3:0x80\n\t"
      "v_smfmac_f32_16x16x64_bf16 v[44:47], v[4:7], a[200:207], v122\n\t"
      "s_waitcnt lgkmcnt(2)\n\t"
      "v_fma_f32 v21, v93, s57, v126\n\t"
      "v_cvt_pk_bf16_f32 v16, v20, v21\n\t"
      "v_smfmac_f32_16x16x64_bf16 v[36:39], v[8:11], a[80:87], v122\n\t"
      "s_waitcnt vmcnt(1)\n\t"
      "ds_write_b64 v127, v[16:17] offset:45056\n\t"
      "v_fma_f32 v27, v99, s57, v126\n\t"
      "v_fma_f32 v26, v98, s57, v126\n\t"
      "v_cvt_pk_bf16_f32 v19, v26, v27\n\t"
      "v_fma_f32 v25, v97, s57, v126\n\t"
      "v_fma_f32 v24, v96, s57, v126\n\t"
      "v_cvt_pk_bf16_f32 v18, v24, v25\n\t"
      "v_smfmac_f32_16x16x64_bf16 v[44:47], v[8:11], a[208:215], v122\n\t"
      "ds_write_b64 v127, v[18:19] offset:45064\n\t"
      "v_smfmac_f32_16x16x64_bf16 v[40:43], v[8:11], a[144:151], v122\n\t"
      "ds_read_b128 v[88:91], v127 offset:39936\n\t"
      "v_bitop3_b32 v64, v64, v95, v96 bitop3:0x80\n\t"
      "v_bitop3_b32 v64, v64, v97, v98 bitop3:0x80\n\t"
      "v_smfmac_f32_16x16x64_bf16 v[40:43], v[12:15], a[152:159], v122\n\t"
      "v_bitop3_b32 v64, v64, v99, s60 bitop3:0x80\n\t"
      "v_smfmac_f32_16x16x64_bf16 v[36:39], v[12:15], a[88:95], v122\n\t"
      "v_cmp_eq_u32_e64 vcc, 0, v64\n\t"
      "v_smfmac_f32_16x16x64_bf16 v[44:47], v[12:15], a[216:223], v122\n\t"
      "s_waitcnt lgkmcnt(1)\n\t"
      "v_smfmac_f32_16x16x64_bf16 v[32:35], v[8:11], a[16:23], v122\n\t"
      "s_cbranch_vccnz LR1_0%=\n\t"
      "LC1_0%=:\n\t"
      "v_smfmac_f32_16x16x64_bf16 v[40:43], v[16:19], a[160:167], v122\n\t"
      "s_barrier\n\t"
      "v_mfma_f32_16x16x32_bf16 v[56:59], v[16:19], v[16:19], v[56:59]\n\t"
      "ds_read_b128 v[20:23], v127 offset:46080\n\t"
      "s_nop 0\n\t"
      "v_smfmac_f32_16x16x64_bf16 v[32:35], v[12:15], a[24:31], v122\n\t"
      "ds_read_b128 v[24:27], v127 offset:47104\n\t"
      "v_smfmac_f32_16x16x64_bf16 v[44:47], v[16:19], a[224:231], v122\n\t"
      "ds_read_b128 v[28:31], v127 offset:48128\n\t"
      "v_max3_f32 v56, v56, v57, v58\n\t"
      "v_smfmac_f32_16x16x64_bf16 v[36:39], v[16:19], a[96:103], v122\n\t"
      "ds_read_b128 v[76:79], v127 offset:32768\n\t"
      "v_max_f32_e64 v56, v56, v59\n\t"
      "v_smfmac_f32_16x16x64_bf16 v[32:35], v[16:19], a[32:39], v122\n\t"
      "s_waitcnt lgkmcnt(3)\n\t"
      "v_readfirstlane_b32 s56, v120\n\t"
      "v_smfmac_f32_16x16x64_bf16 v[32:35], v[20:23], a[40:47], v122\n\t"
      "s_waitcnt lgkmcnt(2)\n\t"
      "v_max_f32_dpp v56, v56, v56 quad_perm:[1,0,3,2] row_mask:0xf bank_mask:0xf\n\t"
      "v_smfmac_f32_16x16x64_bf16 v[32:35], v[24:27], a[48:55], v122\n\t"
      "s_waitcnt lgkmcnt(1)\n\t"
      "v_max_f32_dpp v56, v56, v56 quad_perm:[2,3,0,1] row_mask:0xf bank_mask:0xf\n\t"
      "v_smfmac_f32_16x16x64_bf16 v[32:35], v[28:31], a[56:63], v122\n\t"
      "s_mov_b32 exec_lo, 0x100001\n\t"
      "s_mov_b32 exec_hi, 0x10000100\n\t"
      "v_smfmac_f32_16x16x64_bf16 v[36:39], v[20:23], a[104:111], v122\n\t"
      "ds_max_i32 v109, v56 offset:55392\n\t"
      "v_smfmac_f32_16x16x64_bf16 v[40:43], v[20:23], a[168:175], v122\n\t"
      "s_mov_b64 exec, -1\n\t"
      "v_add_f32 v33, v33, v35\n\t"
      "v_smfmac_f32_16x16x64_bf16 v[36:39], v[24:27], a[112:119], v122\n\t"
      "ds_read_b128 v[60:63], v127 offset:37888\n\t"
      "v_mul_f32_e64 v33, s59, v33\n\t"
      "v_smfmac_f32_16x16x64_bf16 v[36:39], v[28:31], a[120:127], v122\n\t"
      "v_add_f32_e64 v32, v32, v34\n\t"
      "v_smfmac_f32_16x16x64_bf16 v[44:47], v[20:23], a[232:239], v122\n\t"
      "ds_read_b128 v[56:59], v127 offset:38912\n\t"
      "v_mul_f32_e64 v32, s59, v32\n\t"
      "v_smfmac_f32_16x16x64_bf16 v[44:47], v[24:27], a[240:247], v122\n\t"
      "v_add_f32 v36, v36, v38\n\t"
      "v_add_f32 v37, v37, v39\n\t"
      "v_mul_f32_e64 v37, s59, v37\n\t"
      "v_smfmac_f32_16x16x64_bf16 v[40:43], v[24:27], a[176:183], v122\n\t"
      "ds_read_b128 v[92:95], v127 offset:34816\n\t"
      "v_cvt_pk_bf16_f32 v112, v32, v33\n\t"
      "v_smfmac_f32_16x16x64_bf16 v[44:47], v[28:31], a[248:255], v122\n\t"
      "ds_read_b128 v[96:99], v127 offset:33792\n\t"
      "v_mul_f32_e64 v36, s59, v36\n\t"
      "v_cvt_pk_bf16_f32 v113, v36, v37\n\t"
      "v_smfmac_f32_16x16x64_bf16 v[40:43], v[28:31], a[184:191], v122\n\t"
      "ds_write_b64 v125, v[112:113]\n\t"
      "v_smfmac_f32_16x16x64_bf16 v[48:51], v[4:7], v[136:143], v122\n\t"
      "s_nop 0\n\t"
      "v_add_f32 v45, v45, v47\n\t"
      "v_add_f32_e64 v44, v44, v46\n\t"
      "v_cvt_pk_bf16_f32 v121, v44, v45\n\t"
      "v_add_f32 v41, v41, v43\n\t"
      "v_add_f32 v40, v40, v42\n\t"
      "v_cvt_pk_bf16_f32 v120, v40, v41\n\t"
      "v_smfmac_f32_16x16x64_bf16 v[48:51], v[8:11], v[144:151], v122\n\t"
      "s_mov_b32 s58, 0x10000000\n\t"
      "ds_write_b64 v125, v[120:121] offset:2080\n\t"
      "v_smfmac_f32_16x16x64_bf16 v[48:51], v[12:15], v[152:159], v122\n\t"
      "s_waitcnt lgkmcnt(1)\n\t"
      "s_barrier\n\t"
      "ds_read_b64_tr_b16 v[36:37], v124 offset:50192\n\t"
      "v_smfmac_f32_16x16x64_bf16 v[48:51], v[16:19], v[160:167], v122\n\t"
      "ds_read_b32 v108, v109 offset:55392\n\t"
      "ds_read_b64_tr_b16 v[32:33], v124 offset:49152\n\t"
      "v_smfmac_f32_16x16x64_bf16 v[52:55], v[4:7], v[200:207], v122\n\t"
      "ds_read_b64_tr_b16 v[34:35], v124 offset:49672\n\t"
      "s_waitcnt vmcnt(0) lgkmcnt(4)\n\t"
      "ds_read_b64_tr_b16 v[38:39], v124 offset:50712\n\t"
      "v_smfmac_f32_16x16x64_bf16 v[48:51], v[20:23], v[168:175], v122\n\t"
      "s_barrier\n\t"
      "ds_read_b64_tr_b16 v[46:47], v124 offset:52792\n\t"
      "v_smfmac_f32_16x16x64_bf16 v[48:51], v[24:27], v[176:183], v122\n\t"
      "s_waitcnt lgkmcnt(4)\n\t"
      "ds_read_b64_tr_b16 v[44:45], v124 offset:52272\n\t"
      "v_readfirstlane_b32 s61, v108\n\t"
      "v_smfmac_f32_16x16x64_bf16 v[48:51], v[28:31], v[184:191], v122\n\t"
      "s_waitcnt lgkmcnt(3)\n\t"
      "ds_read_b64_tr_b16 v[42:43], v124 offset:51752\n\t"
      "ds_read_b64_tr_b16 v[40:41], v124 offset:51232\n\t"
      "s_waitcnt lgkmcnt(4)\n\t"
      "v_mfma_f32_16x16x32_bf16 v[112:115], v[76:79], v[32:35], 0\n\t"
      "s_lshr_b32 s61, s61, 23\n\t"
      "s_add_u32 s61, s61, s67\n\t"
      "v_mov_b32_dpp v102, v34 quad_perm:[2,3,0,1] row_mask:0xf bank_mask:0xf\n\t"
      "v_mov_b32_dpp v106, v38 quad_perm:[2,3,0,1] row_mask:0xf bank_mask:0xf\n\t"
      "s_max_u32 s61, s61, s66\n\t"
      "v_mfma_f32_16x16x32_bf16 v[116:119], v[80:83], v[32:35], 0\n\t"
      "s_lshr_b32 s61, s61, 1\n\t"
      "v_add_f32_e64 v48, v48, v50\n\t"
      "v_mov_b32_dpp v100, v32 quad_perm:[2,3,0,1] row_mask:0xf bank_mask:0xf\n\t"
      "v_mfma_f32_16x16x32_bf16 v[116:119], v[88:91], v[36:39], v[116:119]\n\t"
      "s_min_u32 s61, s61, 200\n\t"
      "v_mov_b32_dpp v107, v39 quad_perm:[2,3,0,1] row_mask:0xf bank_mask:0xf\n\t"
      "s_waitcnt lgkmcnt(0)\n\t"
      "v_mfma_f32_16x16x32_bf16 v[76:79], v[40:43], v[32:35], 0\n\t"
      "buffer_load_dwordx4 v127, s[36:39], s46 offen sc1 lds\n\t"
      "s_lshl_b32 s61, s61, 23\n\t"
      "v_mfma_f32_16x16x32_bf16 v[76:79], v[44:47], v[36:39], v[76:79]\n\t"
      "s_add_u32 s65, s61, 33554432\n\t"
      "v_mov_b32_dpp v104, v36 quad_perm:[2,3,0,1] row_mask:0xf bank_mask:0xf\n\t"
      "v_mov_b32_dpp v103, v35 quad_perm:[2,3,0,1] row_mask:0xf bank_mask:0xf\n\t"
      "v_mov_b32_dpp v101, v33 quad_perm:[2,3,0,1] row_mask:0xf bank_mask:0xf\n\t"
      "v_mfma_f32_16x16x32_bf16 v[112:115], v[84:87], v[36:39], v[112:115]\n\t"
      "s_pack_hh_b32_b16 s75, s65, s65\n\t"
      "v_add_f32 v49, v49, v51\n\t"
      "v_cvt_pk_bf16_f32 v64, v48, v49\n\t"
      "v_cndmask_b32_e64 v120, v76, v78, s[72:73]\n\t"
      "v_cndmask_b32_e64 v121, v77, v79, s[72:73]\n\t"
      "v_mov_b32_dpp v105, v37 quad_perm:[2,3,0,1] row_mask:0xf bank_mask:0xf\n\t"
      "v_mfma_f32_16x16x32_bf16 v[112:115], v[92:95], v[100:103], v[112:115]\n\t"
      "buffer_load_dwordx4 v127, s[36:39], s46 offen offset:1024 sc1 lds\n\t"
      "v_mfma_f32_16x16x32_bf16 v[116:119], v[96:99], v[100:103], v[116:119]\n\t"
      "s_pack_hh_b32_b16 s74, s65, s65\n\t"
      "v_add_f32_dpp v121, v121, v121 quad_perm:[2,3,0,1] row_mask:0xf bank_mask:0xf\n\t"
      "v_add_f32_dpp v120, v120, v120 quad_perm:[2,3,0,1] row_mask:0xf bank_mask:0xf\n\t"
      "v_mfma_f32_16x16x32_bf16 v[112:115], v[56:59], v[104:107], v[112:115]\n\t"
      "v_exp_f32 v81, v121\n\t"
      "v_mfma_f32_16x16x32_bf16 v[116:119], v[60:63], v[104:107], v[116:119]\n\t"
      "s_mov_b32 m0, s45\n\t"
      "v_mov_b64 v[90:91], s[74:75]\n\t"
      "v_exp_f32 v80, v120\n\t"
      "v_mov_b64 v[88:89], s[74:75]\n\t"
      "v_cvt_pk_bf16_f32 v108, v80, v81\n\t"
      "v_smfmac_f32_16x16x64_bf16 v[52:55], v[8:11], v[208:215], v122\n\t"
      "buffer_load_dwordx4 v65, s[36:39], s44 offen offset:1024 sc1 lds\n\t"
      "v_mfma_f32_16x16x32_bf16 v[84:87], v[108:111], v[64:67], 0\n\t"
      "s_add_u32 s57, s61, 310378496\n\t"
      "v_cndmask_b32_e64 v112, v112, v116, s[72:73]\n\t"
      "v_exp_f32_e64 v56, v112\n\t"
      "v_mfma_f32_16x16x32_bf16 v[48:51], v[108:111], v[88:91], 0\n\t"
      "s_add_u32 s43, s43, 0x8000\n\t"
      "v_cndmask_b32_e64 v113, v113, v117, s[72:73]\n\t"
      "v_cndmask_b32_e64 v114, v114, v118, s[72:73]\n\t"
      "v_exp_f32_e64 v58, v114\n\t"
      "v_cndmask_b32_e64 v115, v115, v119, s[72:73]\n\t"
      "v_exp_f32 v57, v113\n\t"
      "v_exp_f32 v59, v115\n\t"
      "v_cvt_pk_bf16_f32 v96, v56, v57\n\t"
      "v_cvt_pk_bf16_f32 v97, v58, v59\n\t"
      "v_max3_f32 v82, |v112|, |v113|, |v114|\n\t"
      "v_mov_b32_dpp v98, v96 quad_perm:[2,3,0,1] row_mask:0xf bank_mask:0xf\n\t"
      "v_mov_b32_dpp v99, v97 quad_perm:[2,3,0,1] row_mask:0xf bank_mask:0xf\n\t"
      "v_smfmac_f32_16x16x64_bf16 v[52:55], v[12:15], v[216:223], v122\n\t"
      "v_max3_f32 v83, |v115|, |v120|, |v121|\n\t"
      "v_mfma_f32_16x16x32_bf16 v[48:51], v[96:99], v[88:91], v[48:51]\n\t"
      "v_max_f32_e64 v82, v82, v83\n\t"
      "v_cmp_nge_f32 vcc, 0x42000000, v82\n\t"
      "v_mfma_f32_16x16x32_bf16 v[84:87], v[96:99], v[68:71], v[84:87]\n\t"
      "buffer_load_dwordx4 v65, s[36:39], s44 offen sc1 lds\n\t"
      "v_smfmac_f32_16x16x64_bf16 v[52:55], v[16:19], v[224:231], v122\n\t"
      "s_add_u32 s68, s68, 0x8000\n\t"
      "s_cbranch_vccnz LMX_0%=\n\t"
      "v_rcp_f32 v49, v49\n\t"
      "v_rcp_f32_e64 v48, v48\n\t"
      "v_smfmac_f32_16x16x64_bf16 v[52:55], v[20:23], v[232:239], v122\n\t"
      "v_fma_f32 v85, v85, v49, s96\n\t"
      "v_fma_f32 v84, v84, v48, s96\n\t"
      "v_smfmac_f32_16x16x64_bf16 v[52:55], v[24:27], v[240:247], v122\n\t"
      "buffer_atomic_add v84, v123, s[36:39], s97 offen\n\t"
      "buffer_atomic_add v85, v123, s[36:39], s98 offen\n\t"
      "v_smfmac_f32_16x16x64_bf16 v[52:55], v[28:31], v[248:255], v122\n\t"
      "v_mfma_f32_16x16x32_bf16 v[92:95], v[96:99], v[72:75], 0\n\t"
      "s_nop 3\n\t"
      "v_add_f32 v53, v53, v55\n\t"
      "v_add_f32_e64 v52, v52, v54\n\t"
      "v_cvt_pk_bf16_f32 v66, v52, v53\n\t"
      "s_add_u32 s62, s61, 0x83400000\n\t"
      "v_mov_b32_e64 v126, s62\n\t"
      "v_mfma_f32_16x16x32_bf16 v[92:95], v[108:111], v[66:69], v[92:95]\n\t"
      "s_add_u32 s97, s97, 0x8000\n\t"
      "s_nop 6\n\t"
      "v_fma_f32 v95, v95, v49, s96\n\t"
      "buffer_atomic_add v95, v123, s[36:39], s23 offen\n\t"
      "v_fma_f32 v94, v94, v48, s96\n\t"
      "buffer_atomic_add v94, v123, s[36:39], s69 offen\n\t"
      "LWIN_0%=:\n\t"
      "v_add_u32 v100, s55, v127\n\t"
      "ds_read_b128 v[60:63], v100 offset:57344\n\t"
      "s_cmp_eq_u32 s56, 0\n\t"
      "s_cbranch_scc0 LSLOW_0%=\n\t"
      "LWIN1_0%=:\n\t"
      "buffer_load_dwordx2 v[120:121], off, s[36:39], s50 sc1\n\t"
      "s_mov_b32 s64, s50\n\t"
      "s_bitcmp1_b64 s[84:85], s40\n\t"
      "s_cbranch_scc1 LEARLY_0%=\n\t"
      "LROLE_0%=:\n\t"
      "v_add_u32 v52, 65536, v127\n\t"
      "ds_read_b128 v[88:91], v52\n\t"
      "ds_read_b128 v[92:95], v52 offset:1024\n\t"
      "ds_read_b128 v[32:35], v52 offset:4096\n\t"
      "ds_read_b128 v[36:39], v52 offset:5120\n\t"
      "ds_read_b128 v[40:43], v52 offset:8192\n\t"
      "ds_read_b128 v[44:47], v52 offset:9216\n\t"
      "v_mov_b64 v[48:49], 0\n\t"
      "v_mov_b64 v[50:51], 0\n\t"
      "v_add_u32 v104, s71, v100\n\t"
      "ds_read_b128 v[76:79], v104\n\t"
      "ds_read_b128 v[80:83], v104 offset:1024\n\t"
      "ds_read_b128 v[112:115], v100\n\t"
      "ds_read_b128 v[116:119], v100 offset:1024\n\t"
      "v_xor_b32 v101, 2048, v104\n\t"
      "v_xor_b32 v102, 4096, v104\n\t"
      "v_xor_b32 v103, 6144, v104\n\t"
      "s_waitcnt lgkmcnt(8)\n\t"
      "v_smfmac_f32_16x16x64_bf16 v[48:51], v[0:3], v[88:95], v122\n\t"
      "ds_read_b128 v[88:91], v52 offset:12288\n\t"
      "ds_read_b128 v[92:95], v52 offset:13312\n\t"
      "s_add_u32 m0, s55, s71\n\t"
      "s_sub_u32 s71, 204800, s71\n\t"
      "s_waitcnt lgkmcnt(8)\n\t"
      "v_smfmac_f32_16x16x64_bf16 v[48:51], v[4:7], v[32:39], v122\n\t"
      "ds_read_b128 v[16:19], v101\n\t"
      "ds_read_b128 v[20:23], v101 offset:1024\n\t"
      "s_add_u32 s40, s40, 1\n\t"
      "s_cmp_lt_u32 s40, 63\n\t"
      "s_waitcnt lgkmcnt(8)\n\t"
      "v_smfmac_f32_16x16x64_bf16 v[48:51], v[8:11], v[40:47], v122\n\t"
      "ds_read_b128 v[24:27], v100 offset:8192\n\t"
      "ds_read_b128 v[28:31], v100 offset:9216\n\t"
      "s_cselect_b32 s61, 786432, 0\n\t"
      "s_cselect_b32 s60, s58, 0\n\t"
      "s_waitcnt lgkmcnt(4)\n\t"
      "v_smfmac_f32_16x16x64_bf16 v[48:51], v[12:15], v[88:95], v122\n\t"
      "s_add_u32 s44, s44, s61\n\t"
      "s_cmp_lt_u32 s40, 62\n\t"
      "ds_read_b128 v[0:3], v102\n\t"
      "ds_read_b128 v[4:7], v102 offset:1024\n\t"
      "ds_read_b128 v[8:11], v100 offset:16384\n\t"
      "ds_read_b128 v[12:15], v100 offset:17408\n\t"
      "s_cselect_b32 s61, 786432, 0\n\t"
      "s_add_u32 s46, s46, s61\n\t"
      "s_add_u32 s61, s40, 3\n\t"
      "s_and_b32 s62, s61, 3\n\t"
      "s_cselect_b32 s62, 16, 2000\n\t"
      "s_cmp_lt_u32 s61, 64\n\t"
      "s_cselect_b32 s62, s62, 0\n\t"
      "s_add_u32 s50, s50, s62\n\t"
      "s_mov_b32 s28, s49\n\t"
      "s_add_u32 s49, s49, 65536\n\t"
      "v_add_f32 v48, v48, v50\n\t"
      "v_add_f32 v49, v49, v51\n\t"
      "v_mfma_f32_16x16x32_bf16 v[32:35], v[76:79], v[112:115], 0\n\t"
      "s_mov_b32 s63, 0x2000\n\t"
      "v_mfma_f32_16x16x32_bf16 v[36:39], v[80:83], v[112:115], 0\n\t"
      "v_mfma_f32_16x16x32_bf16 v[40:43], v[76:79], v[116:119], 0\n\t"
      "v_mfma_f32_16x16x32_bf16 v[44:47], v[80:83], v[116:119], 0\n\t"
      "ds_read_b128 v[76:79], v103\n\t"
      "ds_read_b128 v[80:83], v103 offset:1024\n\t"
      "ds_read_b128 v[112:115], v100 offset:24576\n\t"
      "ds_read_b128 v[116:119], v100 offset:25600\n\t"
      "s_waitcnt lgkmcnt(8)\n\t"
      "v_mfma_f32_16x16x32_bf16 v[32:35], v[16:19], v[24:27], v[32:35]\n\t"
      "v_mfma_f32_16x16x32_bf16 v[36:39], v[20:23], v[24:27], v[36:39]\n\t"
      "v_mfma_f32_16x16x32_bf16 v[40:43], v[16:19], v[28:31], v[40:43]\n\t"
      "v_mfma_f32_16x16x32_bf16 v[44:47], v[20:23], v[28:31], v[44:47]\n\t"
      "LGATE_0%=:\n\t"
      "LBAR_0%=:\n\t"
      "s_cmp_gt_u32 s40, 1\n\t"
      "s_barrier\n\t"
      "s_cbranch_scc1 LPOLL_0%=\n\t"
      "s_branch LFIRST_0%=\n\t"
      ".p2align 6\n\t.fill 16, 4, 0xbf800000\n\t"
      "LPOLL_1%=:\n\t"
      "buffer_load_dwordx4 v[84:87], v60, s[36:39], s43 offen sc1\n\t"
      "buffer_load_dwordx4 v[88:91], v60, s[36:39], s43 offen offset:1024 sc1\n\t"
      "s_waitcnt lgkmcnt(4)\n\t"
      "v_mfma_f32_16x16x32_bf16 v[32:35], v[0:3], v[8:11], v[32:35]\n\t"
      "v_mfma_f32_16x16x32_bf16 v[36:39], v[4:7], v[8:11], v[36:39]\n\t"
      "v_mfma_f32_16x16x32_bf16 v[40:43], v[0:3], v[12:15], v[40:43]\n\t"
      "v_mfma_f32_16x16x32_bf16 v[44:47], v[4:7], v[12:15], v[44:47]\n\t"
      "buffer_load_dwordx4 v[92:95], v60, s[36:39], s68 offen sc1\n\t"
      "buffer_load_dwordx4 v[96:99], v60, s[36:39], s68 offen offset:1024 sc1\n\t"
      "ds_read_b64 v[54:55], v124 offset:53832\n\t"
      "s_xor_b32 s69, s97, 1024\n\t"
      "s_xor_b32 s98, s97, 512\n\t"
      "s_xor_b32 s23, s97, 1536\n\t"
      "s_waitcnt lgkmcnt(1)\n\t"
      "v_mfma_f32_16x16x32_bf16 v[32:35], v[76:79], v[112:115], v[32:35]\n\t"
      "v_mfma_f32_16x16x32_bf16 v[36:39], v[80:83], v[112:115], v[36:39]\n\t"
      "v_mfma_f32_16x16x32_bf16 v[40:43], v[76:79], v[116:119], v[40:43]\n\t"
      "v_mfma_f32_16x16x32_bf16 v[44:47], v[80:83], v[116:119], v[44:47]\n\t"
      "s_waitcnt lgkmcnt(0)\n\t"
      "v_add_f32 v48, v48, v54\n\t"
      "v_add_f32_e64 v49, v49, v55\n\t"
      "v_cvt_pk_bf16_f32 v48, v48, v49\n\t"
      "buffer_store_dword v48, v62, s[36:39], s28 offen sc1\n\t"
      "s_mov_b64 exec, 1\n\t"
      "ds_write_b64 v109, v[110:111] offset:55392\n\t"
      "s_mov_b64 exec, -1\n\t"
      "s_nop 4\n\t"
      "v_cvt_pk_bf16_f32 v68, v32, v33\n\t"
      "v_cvt_pk_bf16_f32 v69, v34, v35\n\t"
      "v_cvt_pk_bf16_f32 v70, v36, v37\n\t"
      "v_cvt_pk_bf16_f32 v71, v38, v39\n\t"
      "v_cvt_pk_bf16_f32 v74, v40, v41\n\t"
      "v_cvt_pk_bf16_f32 v75, v42, v43\n\t"
      "v_cvt_pk_bf16_f32 v72, v44, v45\n\t"
      "v_cvt_pk_bf16_f32 v73, v46, v47\n\t"
      "v_mov_b64 v[32:33], 0\n\t"
      "v_mov_b64 v[34:35], 0\n\t"
      "v_mov_b64 v[36:37], 0\n\t"
      "v_mov_b64 v[38:39], 0\n\t"
      "v_mov_b64 v[40:41], 0\n\t"
      "v_mov_b64 v[42:43], 0\n\t"
      "v_mov_b64 v[44:45], 0\n\t"
      "v_mov_b64 v[46:47], 0\n\t"
      "v_mov_b64 v[48:49], 0\n\t"
      "v_mov_b64 v[50:51], 0\n\t"
      "v_mov_b64 v[52:53], 0\n\t"
      "v_mov_b64 v[54:55], 0\n\t"
      "LGO_1%=:\n\t"
      "s_nop 0\n\t"
      "s_nop 0\n\t"
      "s_nop 0\n\t"
      "s_nop 0\n\t"
      "s_waitcnt vmcnt(4)\n\t"
      "v_pk_fma_f32 v[2:3], v[86:87], s[56:57], v[126:127] op_sel:[0,1,0] op_sel_hi:[1,1,0]\n\t"
      "v_pk_fma_f32 v[0:1], v[84:85], s[56:57], v[126:127] op_sel:[0,1,0] op_sel_hi:[1,1,0]\n\t"
      "v_cvt_pk_bf16_f32 v4, v0, v1\n\t"
      "v_cvt_pk_bf16_f32 v5, v2, v3\n\t"
      "s_nop 0\n\t"
      "ds_write_b64 v127, v[4:5] offset:41984\n\t"
      "s_waitcnt vmcnt(3)\n\t"
      "v_pk_fma_f32 v[12:13], v[88:89], s[56:57], v[126:127] op_sel:[0,1,0] op_sel_hi:[1,1,0]\n\t"
      "v_pk_fma_f32 v[14:15], v[90:91], s[56:57], v[126:127] op_sel:[0,1,0] op_sel_hi:[1,1,0]\n\t"
      "v_cvt_pk_bf16_f32 v6, v12, v13\n\t"
      "v_cvt_pk_bf16_f32 v7, v14, v15\n\t"
      "ds_write_b64 v127, v[6:7] offset:41992\n\t"
      "v_bitop3_b32 v64, v84, v85, v86 bitop3:0x80\n\t"
      "v_mfma_f32_16x16x32_bf16 v[56:59], v[4:7], v[4:7], 0\n\t"
      "v_bitop3_b32 v64, v64, v87, v88 bitop3:0x80\n\t"
      "v_bitop3_b32 v64, v64, v89, v90 bitop3:0x80\n\t"
      "v_bitop3_b32 v64, v64, v91, s58 bitop3:0x80\n\t"
      "v_cmp_eq_u32_e64 vcc, 0, v64\n\t"
      "v_smfmac_f32_16x16x64_bf16 v[36:39], v[4:7], a[72:79], v122\n\t"
      "s_nop 0\n\t"
      "v_bitop3_b32 v120, v120, v121, s53 bitop3:0x7e\n\t"
      "v_smfmac_f32_16x16x64_bf16 v[44:47], v[4:7], a[200:207], v122\n\t"
      "s_cbranch_vccnz LR0_1%=\n\t"
      "LC0_1%=:\n\t"
      "s_waitcnt lgkmcnt(0)\n\t"
      "s_barrier\n\t"
      "ds_read_b128 v[0:3], v127 offset:40960\n\t"
      "v_smfmac_f32_16x16x64_bf16 v[52:55], v[4:7], v[200:207], v122\n\t"
      "ds_read_b128 v[12:15], v127 offset:44032\n\t"
      "v_smfmac_f32_16x16x64_bf16 v[48:51], v[4:7], v[136:143], v122\n\t"
      "ds_read_b128 v[8:11], v127 offset:43008\n\t"
      "v_smfmac_f32_16x16x64_bf16 v[32:35], v[4:7], a[8:15], v122\n\t"
      "ds_read_b128 v[84:87], v127 offset:36864\n\t"
      "v_smfmac_f32_16x16x64_bf16 v[40:43], v[4:7], a[136:143], v122\n\t"
      "s_waitcnt lgkmcnt(3)\n\t"
      "v_smfmac_f32_16x16x64_bf16 v[32:35], v[0:3], a[0:7], v122\n\t"
      "ds_read_b128 v[80:83], v127 offset:35840\n\t"
      "v_smfmac_f32_16x16x64_bf16 v[40:43], v[0:3], a[128:135], v122\n\t"
      "s_waitcnt vmcnt(2)\n\t"
      "v_fma_f32 v19, v95, s57, v126\n\t"
      "v_fma_f32 v16, v92, s57, v126\n\t"
      "v_smfmac_f32_16x16x64_bf16 v[36:39], v[0:3], a[64:71], v122\n\t"
      "v_fma_f32 v18, v94, s57, v126\n\t"
      "v_cvt_pk_bf16_f32 v21, v18, v19\n\t"
      "v_bitop3_b32 v64, v92, v93, v94 bitop3:0x80\n\t"
      "v_smfmac_f32_16x16x64_bf16 v[44:47], v[0:3], a[192:199], v122\n\t"
      "s_waitcnt lgkmcnt(2)\n\t"
      "v_fma_f32 v17, v93, s57, v126\n\t"
      "v_cvt_pk_bf16_f32 v20, v16, v17\n\t"
      "v_smfmac_f32_16x16x64_bf16 v[36:39], v[12:15], a[88:95], v122\n\t"
      "s_waitcnt vmcnt(1)\n\t"
      "ds_write_b64 v127, v[20:21] offset:46080\n\t"
      "v_fma_f32 v31, v99, s57, v126\n\t"
      "v_fma_f32 v30, v98, s57, v126\n\t"
      "v_cvt_pk_bf16_f32 v23, v30, v31\n\t"
      "v_fma_f32 v29, v97, s57, v126\n\t"
      "v_fma_f32 v28, v96, s57, v126\n\t"
      "v_cvt_pk_bf16_f32 v22, v28, v29\n\t"
      "v_smfmac_f32_16x16x64_bf16 v[44:47], v[12:15], a[216:223], v122\n\t"
      "ds_write_b64 v127, v[22:23] offset:46088\n\t"
      "v_smfmac_f32_16x16x64_bf16 v[40:43], v[12:15], a[152:159], v122\n\t"
      "ds_read_b128 v[88:91], v127 offset:39936\n\t"
      "v_bitop3_b32 v64, v64, v95, v96 bitop3:0x80\n\t"
      "v_bitop3_b32 v64, v64, v97, v98 bitop3:0x80\n\t"
      "v_smfmac_f32_16x16x64_bf16 v[40:43], v[8:11], a[144:151], v122\n\t"
      "v_bitop3_b32 v64, v64, v99, s60 bitop3:0x80\n\t"
      "v_smfmac_f32_16x16x64_bf16 v[36:39], v[8:11], a[80:87], v122\n\t"
      "v_cmp_eq_u32_e64 vcc, 0, v64\n\t"
      "v_smfmac_f32_16x16x64_bf16 v[44:47], v[8:11], a[208:215], v122\n\t"
      "s_waitcnt lgkmcnt(1)\n\t"
      "v_smfmac_f32_16x16x64_bf16 v[32:35], v[12:15], a[24:31], v122\n\t"
      "s_cbranch_vccnz LR1_1%=\n\t"
      "LC1_1%=:\n\t"
      "v_smfmac_f32_16x16x64_bf16 v[40:43], v[20:23], a[168:175], v122\n\t"
      "s_barrier\n\t"
      "v_mfma_f32_16x16x32_bf16 v[56:59], v[20:23], v[20:23], v[56:59]\n\t"
      "ds_read_b128 v[16:19], v127 offset:45056\n\t"
      "s_nop 0\n\t"
      "v_smfmac_f32_16x16x64_bf16 v[32:35], v[8:11], a[16:23], v122\n\t"
      "ds_read_b128 v[28:31], v127 offset:48128\n\t"
      "v_smfmac_f32_16x16x64_bf16 v[44:47], v[20:23], a[232:239], v122\n\t"
      "ds_read_b128 v[24:27], v127 offset:47104\n\t"
      "v_max3_f32 v56, v56, v57, v58\n\t"
      "v_smfmac_f32_16x16x64_bf16 v[36:39], v[20:23], a[104:111], v122\n\t"
      "ds_read_b128 v[76:79], v127 offset:32768\n\t"
      "v_max_f32_e64 v56, v56, v59\n\t"
      "v_smfmac_f32_16x16x64_bf16 v[32:35], v[20:23], a[40:47], v122\n\t"
      "s_waitcnt lgkmcnt(3)\n\t"
      "v_readfirstlane_b32 s56, v120\n\t"
      "v_smfmac_f32_16x16x64_bf16 v[32:35], v[16:19], a[32:39], v122\n\t"
      "s_waitcnt lgkmcnt(2)\n\t"
      "v_max_f32_dpp v56, v56, v56 quad_perm:[1,0,3,2] row_mask:0xf bank_mask:0xf\n\t"
      "v_smfmac_f32_16x16x64_bf16 v[32:35], v[28:31], a[56:63], v122\n\t"
      "s_waitcnt lgkmcnt(1)\n\t"
      "v_max_f32_dpp v56, v56, v56 quad_perm:[2,3,0,1] row_mask:0xf bank_mask:0xf\n\t"
      "v_smfmac_f32_16x16x64_bf16 v[32:35], v[24:27], a[48:55], v122\n\t"
      "s_mov_b32 exec_lo, 0x100001\n\t"
      "s_mov_b32 exec_hi, 0x10000100\n\t"
      "v_smfmac_f32_16x16x64_bf16 v[36:39], v[16:19], a[96:103], v122\n\t"
      "ds_max_i32 v109, v56 offset:55392\n\t"
      "v_smfmac_f32_16x16x64_bf16 v[40:43], v[16:19], a[160:167], v122\n\t"
      "s_mov_b64 exec, -1\n\t"
      "v_add_f32 v33, v33, v35\n\t"
      "v_smfmac_f32_16x16x64_bf16 v[36:39], v[28:31], a[120:127], v122\n\t"
      "ds_read_b128 v[60:63], v127 offset:37888\n\t"
      "v_mul_f32_e64 v33, s59, v33\n\t"
      "v_smfmac_f32_16x16x64_bf16 v[36:39], v[24:27], a[112:119], v122\n\t"
      "v_add_f32_e64 v32, v32, v34\n\t"
      "v_smfmac_f32_16x16x64_bf16 v[44:47], v[16:19], a[224:231], v122\n\t"
      "ds_read_b128 v[56:59], v127 offset:38912\n\t"
      "v_mul_f32_e64 v32, s59, v32\n\t"
      "v_smfmac_f32_16x16x64_bf16 v[44:47], v[28:31], a[248:255], v122\n\t"
      "v_add_f32 v36, v36, v38\n\t"
      "v_add_f32 v37, v37, v39\n\t"
      "v_mul_f32_e64 v37, s59, v37\n\t"
      "v_smfmac_f32_16x16x64_bf16 v[40:43], v[28:31], a[184:191], v122\n\t"
      "ds_read_b128 v[92:95], v127 offset:34816\n\t"
      "v_cvt_pk_bf16_f32 v112, v32, v33\n\t"
      "v_smfmac_f32_16x16x64_bf16 v[44:47], v[24:27], a[240:247], v122\n\t"
      "ds_read_b128 v[96:99], v127 offset:33792\n\t"
      "v_mul_f32_e64 v36, s59, v36\n\t"
      "v_cvt_pk_bf16_f32 v113, v36, v37\n\t"
      "v_smfmac_f32_16x16x64_bf16 v[40:43], v[24:27], a[176:183], v122\n\t"
      "ds_write_b64 v125, v[112:113]\n\t"
      "v_smfmac_f32_16x16x64_bf16 v[48:51], v[0:3], v[128:135], v122\n\t"
      "s_nop 0\n\t"
      "v_add_f32 v45, v45, v47\n\t"
      "v_add_f32_e64 v44, v44, v46\n\t"
      "v_cvt_pk_bf16_f32 v121, v44, v45\n\t"
      "v_add_f32 v41, v41, v43\n\t"
      "v_add_f32 v40, v40, v42\n\t"
      "v_cvt_pk_bf16_f32 v120, v40, v41\n\t"
      "v_smfmac_f32_16x16x64_bf16 v[48:51], v[12:15], v[152:159], v122\n\t"
      "s_mov_b32 s58, 0x10000000\n\t"
      "ds_write_b64 v125, v[120:121] offset:2080\n\t"
      "v_smfmac_f32_16x16x64_bf16 v[48:51], v[8:11], v[144:151], v122\n\t"
      "s_waitcnt lgkmcnt(1)\n\t"
      "s_barrier\n\t"
      "ds_read_b64_tr_b16 v[36:37], v124 offset:50192\n\t"
      "v_smfmac_f32_16x16x64_bf16 v[48:51], v[20:23], v[168:175], v122\n\t"
      "ds_read_b32 v108, v109 offset:55392\n\t"
      "ds_read_b64_tr_b16 v[32:33], v124 offset:49152\n\t"
      "v_smfmac_f32_16x16x64_bf16 v[52:55], v[0:3], v[192:199], v122\n\t"
      "ds_read_b64_tr_b16 v[34:35], v124 offset:49672\n\t"
      "s_waitcnt vmcnt(0) lgkmcnt(4)\n\t"
      "ds_read_b64_tr_b16 v[38:39], v124 offset:50712\n\t"
      "v_smfmac_f32_16x16x64_bf16 v[48:51], v[16:19], v[160:167], v122\n\t"
      "s_barrier\n\t"
      "ds_read_b64_tr_b16 v[46:47], v124 offset:52792\n\t"
      "v_smfmac_f32_16x16x64_bf16 v[48:51], v[28:31], v[184:191], v122\n\t"
      "s_waitcnt lgkmcnt(4)\n\t"
      "ds_read_b64_tr_b16 v[44:45], v124 offset:52272\n\t"
      "v_readfirstlane_b32 s61, v108\n\t"
      "v_smfmac_f32_16x16x64_bf16 v[48:51], v[24:27], v[176:183], v122\n\t"
      "s_waitcnt lgkmcnt(3)\n\t"
      "ds_read_b64_tr_b16 v[42:43], v124 offset:51752\n\t"
      "ds_read_b64_tr_b16 v[40:41], v124 offset:51232\n\t"
      "s_waitcnt lgkmcnt(4)\n\t"
      "v_mfma_f32_16x16x32_bf16 v[112:115], v[76:79], v[32:35], 0\n\t"
      "s_lshr_b32 s61, s61, 23\n\t"
      "s_add_u32 s61, s61, s67\n\t"
      "v_mov_b32_dpp v102, v34 quad_perm:[2,3,0,1] row_mask:0xf bank_mask:0xf\n\t"
      "v_mov_b32_dpp v106, v38 quad_perm:[2,3,0,1] row_mask:0xf bank_mask:0xf\n\t"
      "s_max_u32 s61, s61, s66\n\t"
      "v_mfma_f32_16x16x32_bf16 v[116:119], v[80:83], v[32:35], 0\n\t"
      "s_lshr_b32 s61, s61, 1\n\t"
      "v_add_f32_e64 v48, v48, v50\n\t"
      "v_mov_b32_dpp v100, v32 quad_perm:[2,3,0,1] row_mask:0xf bank_mask:0xf\n\t"
      "v_mfma_f32_16x16x32_bf16 v[116:119], v[88:91], v[36:39], v[116:119]\n\t"
      "s_min_u32 s61, s61, 200\n\t"
      "v_mov_b32_dpp v107, v39 quad_perm:[2,3,0,1] row_mask:0xf bank_mask:0xf\n\t"
      "s_waitcnt lgkmcnt(0)\n\t"
      "v_mfma_f32_16x16x32_bf16 v[76:79], v[40:43], v[32:35], 0\n\t"
      "buffer_load_dwordx4 v127, s[36:39], s46 offen sc1 lds\n\t"
      "s_lshl_b32 s61, s61, 23\n\t"
      "v_mfma_f32_16x16x32_bf16 v[76:79], v[44:47], v[36:39], v[76:79]\n\t"
      "s_add_u32 s65, s61, 33554432\n\t"
      "v_mov_b32_dpp v104, v36 quad_perm:[2,3,0,1] row_mask:0xf bank_mask:0xf\n\t"
      "v_mov_b32_dpp v103, v35 quad_perm:[2,3,0,1] row_mask:0xf bank_mask:0xf\n\t"
      "v_mov_b32_dpp v101, v33 quad_perm:[2,3,0,1] row_mask:0xf bank_mask:0xf\n\t"
      "v_mfma_f32_16x16x32_bf16 v[112:115], v[84:87], v[36:39], v[112:115]\n\t"
      "s_pack_hh_b32_b16 s75, s65, s65\n\t"
      "v_add_f32 v49, v49, v51\n\t"
      "v_cvt_pk_bf16_f32 v64, v48, v49\n\t"
      "v_cndmask_b32_e64 v120, v76, v78, s[72:73]\n\t"
      "v_cndmask_b32_e64 v121, v77, v79, s[72:73]\n\t"
      "v_mov_b32_dpp v105, v37 quad_perm:[2,3,0,1] row_mask:0xf bank_mask:0xf\n\t"
      "v_mfma_f32_16x16x32_bf16 v[112:115], v[92:95], v[100:103], v[112:115]\n\t"
      "buffer_load_dwordx4 v127, s[36:39], s46 offen offset:1024 sc1 lds\n\t"
      "v_mfma_f32_16x16x32_bf16 v[116:119], v[96:99], v[100:103], v[116:119]\n\t"
      "s_pack_hh_b32_b16 s74, s65, s65\n\t"
      "v_add_f32_dpp v121, v121, v121 quad_perm:[2,3,0,1] row_mask:0xf bank_mask:0xf\n\t"
      "v_add_f32_dpp v120, v120, v120 quad_perm:[2,3,0,1] row_mask:0xf bank_mask:0xf\n\t"
      "v_mfma_f32_16x16x32_bf16 v[112:115], v[56:59], v[104:107], v[112:115]\n\t"
      "v_exp_f32 v81, v121\n\t"
      "v_mfma_f32_16x16x32_bf16 v[116:119], v[60:63], v[104:107], v[116:119]\n\t"
      "s_mov_b32 m0, s45\n\t"
      "v_mov_b64 v[90:91], s[74:75]\n\t"
      "v_exp_f32 v80, v120\n\t"
      "v_mov_b64 v[88:89], s[74:75]\n\t"
      "v_cvt_pk_bf16_f32 v108, v80, v81\n\t"
      "v_smfmac_f32_16x16x64_bf16 v[52:55], v[12:15], v[216:223], v122\n\t"
      "buffer_load_dwordx4 v65, s[36:39], s44 offen offset:1024 sc1 lds\n\t"
      "v_mfma_f32_16x16x32_bf16 v[84:87], v[108:111], v[64:67], 0\n\t"
      "s_add_u32 s57, s61, 310378496\n\t"
      "v_cndmask_b32_e64 v112, v112, v116, s[72:73]\n\t"
      "v_exp_f32_e64 v56, v112\n\t"
      "v_mfma_f32_16x16x32_bf16 v[48:51], v[108:111], v[88:91], 0\n\t"
      "s_add_u32 s43, s43, 0x8000\n\t"
      "v_cndmask_b32_e64 v113, v113, v117, s[72:73]\n\t"
      "v_cndmask_b32_e64 v114, v114, v118, s[72:73]\n\t"
      "v_exp_f32_e64 v58, v114\n\t"
      "v_cndmask_b32_e64 v115, v115, v119, s[72:73]\n\t"
      "v_exp_f32 v57, v113\n\t"
      "v_exp_f32 v59, v115\n\t"
      "v_cvt_pk_bf16_f32 v96, v56, v57\n\t"
      "v_cvt_pk_bf16_f32 v97, v58, v59\n\t"
      "v_max3_f32 v82, |v112|, |v113|, |v114|\n\t"
      "v_mov_b32_dpp v98, v96 quad_perm:[2,3,0,1] row_mask:0xf bank_mask:0xf\n\t"
      "v_mov_b32_dpp v99, v97 quad_perm:[2,3,0,1] row_mask:0xf bank_mask:0xf\n\t"
      "v_smfmac_f32_16x16x64_bf16 v[52:55], v[8:11], v[208:215], v122\n\t"
      "v_max3_f32 v83, |v115|, |v120|, |v121|\n\t"
      "v_mfma_f32_16x16x32_bf16 v[48:51], v[96:99], v[88:91], v[48:51]\n\t"
      "v_max_f32_e64 v82, v82, v83\n\t"
      "v_cmp_nge_f32 vcc, 0x42000000, v82\n\t"
      "v_mfma_f32_16x16x32_bf16 v[84:87], v[96:99], v[68:71], v[84:87]\n\t"
      "buffer_load_dwordx4 v65, s[36:39], s44 offen sc1 lds\n\t"
      "v_smfmac_f32_16x16x64_bf16 v[52:55], v[20:23], v[232:239], v122\n\t"
      "s_add_u32 s68, s68, 0x8000\n\t"
      "s_cbranch_vccnz LMX_1%=\n\t"
      "v_rcp_f32 v49, v49\n\t"
      "v_rcp_f32_e64 v48, v48\n\t"
      "v_smfmac_f32_16x16x64_bf16 v[52:55], v[16:19], v[224:231], v122\n\t"
      "v_fma_f32 v85, v85, v49, s96\n\t"
      "v_fma_f32 v84, v84, v48, s96\n\t"
      "v_smfmac_f32_16x16x64_bf16 v[52:55], v[28:31], v[248:255], v122\n\t"
      "buffer_atomic_add v84, v123, s[36:39], s97 offen\n\t"
      "buffer_atomic_add v85, v123, s[36:39], s98 offen\n\t"
      "v_smfmac_f32_16x16x64_bf16 v[52:55], v[24:27], v[240:247], v122\n\t"
      "v_mfma_f32_16x16x32_bf16 v[92:95], v[96:99], v[72:75], 0\n\t"
      "s_nop 3\n\t"
      "v_add_f32 v53, v53, v55\n\t"
      "v_add_f32_e64 v52, v52, v54\n\t"
      "v_cvt_pk_bf16_f32 v66, v52, v53\n\t"
      "s_add_u32 s62, s61, 0x83400000\n\t"
      "v_mov_b32_e64 v126, s62\n\t"
      "v_mfma_f32_16x16x32_bf16 v[92:95], v[108:111], v[66:69], v[92:95]\n\t"
      "s_add_u32 s97, s97, 0x8000\n\t"
      "s_nop 6\n\t"
      "v_fma_f32 v95, v95, v49, s96\n\t"
      "buffer_atomic_add v95, v123, s[36:39], s23 offen\n\t"
      "v_fma_f32 v94, v94, v48, s96\n\t"
      "buffer_atomic_add v94, v123, s[36:39], s69 offen\n\t"
      "LWIN_1%=:\n\t"
      "v_add_u32 v100, s55, v127\n\t"
      "ds_read_b128 v[60:63], v100 offset:57344\n\t"
      "s_cmp_eq_u32 s56, 0\n\t"
      "s_cbranch_scc0 LSLOW_1%=\n\t"
      "LWIN1_1%=:\n\t"
      "buffer_load_dwordx2 v[120:121], off, s[36:39], s50 sc1\n\t"
      "s_mov_b32 s64, s50\n\t"
      "s_bitcmp1_b64 s[84:85], s40\n\t"
      "s_cbranch_scc1 LEARLY_1%=\n\t"
      "LROLE_1%=:\n\t"
      "v_add_u32 v52, 67584, v127\n\t"
      "ds_read_b128 v[88:91], v52\n\t"
      "ds_read_b128 v[92:95], v52 offset:1024\n\t"
      "ds_read_b128 v[32:35], v52 offset:4096\n\t"
      "ds_read_b128 v[36:39], v52 offset:5120\n\t"
      "ds_read_b128 v[40:43], v52 offset:8192\n\t"
      "ds_read_b128 v[44:47], v52 offset:9216\n\t"
      "v_mov_b64 v[48:49], 0\n\t"
      "v_mov_b64 v[50:51], 0\n\t"
      "v_add_u32 v104, s71, v100\n\t"
      "ds_read_b128 v[76:79], v104\n\t"
      "ds_read_b128 v[80:83], v104 offset:1024\n\t"
      "ds_read_b128 v[112:115], v100\n\t"
      "ds_read_b128 v[116:119], v100 offset:1024\n\t"
      "v_xor_b32 v101, 2048, v104\n\t"
      "v_xor_b32 v102, 4096, v104\n\t"
      "v_xor_b32 v103, 6144, v104\n\t"
      "s_waitcnt lgkmcnt(8)\n\t"
      "v_smfmac_f32_16x16x64_bf16 v[48:51], v[0:3], v[88:95], v122\n\t"
      "ds_read_b128 v[88:91], v52 offset:12288\n\t"
      "ds_read_b128 v[92:95], v52 offset:13312\n\t"
      "s_add_u32 m0, s55, s71\n\t"
      "s_sub_u32 s71, 204800, s71\n\t"
      "s_waitcnt lgkmcnt(8)\n\t"
      "v_smfmac_f32_16x16x64_bf16 v[48:51], v[4:7], v[32:39], v122\n\t"
      "ds_read_b128 v[16:19], v101\n\t"
      "ds_read_b128 v[20:23], v101 offset:1024\n\t"
      "s_add_u32 s40, s40, 1\n\t"
      "s_cmp_lt_u32 s40, 63\n\t"
      "s_waitcnt lgkmcnt(8)\n\t"
      "v_smfmac_f32_16x16x64_bf16 v[48:51], v[8:11], v[40:47], v122\n\t"
      "ds_read_b128 v[24:27], v100 offset:8192\n\t"
      "ds_read_b128 v[28:31], v100 offset:9216\n\t"
      "s_cselect_b32 s61, 786432, 0\n\t"
      "s_cselect_b32 s60, s58, 0\n\t"
      "s_waitcnt lgkmcnt(4)\n\t"
      "v_smfmac_f32_16x16x64_bf16 v[48:51], v[12:15], v[88:95], v122\n\t"
      "s_add_u32 s44, s44, s61\n\t"
      "s_cmp_lt_u32 s40, 62\n\t"
      "ds_read_b128 v[0:3], v102\n\t"
      "ds_read_b128 v[4:7], v102 offset:1024\n\t"
      "ds_read_b128 v[8:11], v100 offset:16384\n\t"
      "ds_read_b128 v[12:15], v100 offset:17408\n\t"
      "s_cselect_b32 s61, 786432, 0\n\t"
      "s_add_u32 s46, s46, s61\n\t"
      "s_add_u32 s61, s40, 3\n\t"
      "s_and_b32 s62, s61, 3\n\t"
      "s_cselect_b32 s62, 16, 2000\n\t"
      "s_cmp_lt_u32 s61, 64\n\t"
      "s_cselect_b32 s62, s62, 0\n\t"
      "s_add_u32 s50, s50, s62\n\t"
      "s_mov_b32 s28, s49\n\t"
      "s_add_u32 s49, s49, 65536\n\t"
      "v_add_f32 v48, v48, v50\n\t"
      "v_add_f32 v49, v49, v51\n\t"
      "v_mfma_f32_16x16x32_bf16 v[32:35], v[76:79], v[112:115], 0\n\t"
      "s_mov_b32 s63, 0x2000\n\t"
      "v_mfma_f32_16x16x32_bf16 v[36:39], v[80:83], v[112:115], 0\n\t"
      "v_mfma_f32_16x16x32_bf16 v[40:43], v[76:79], v[116:119], 0\n\t"
      "v_mfma_f32_16x16x32_bf16 v[44:47], v[80:83], v[116:119], 0\n\t"
      "ds_read_b128 v[76:79], v103\n\t"
      "ds_read_b128 v[80:83], v103 offset:1024\n\t"
      "ds_read_b128 v[112:115], v100 offset:24576\n\t"
      "ds_read_b128 v[116:119], v100 offset:25600\n\t"
      "s_waitcnt lgkmcnt(8)\n\t"
      "v_mfma_f32_16x16x32_bf16 v[32:35], v[16:19], v[24:27], v[32:35]\n\t"
      "v_mfma_f32_16x16x32_bf16 v[36:39], v[20:23], v[24:27], v[36:39]\n\t"
      "v_mfma_f32_16x16x32_bf16 v[40:43], v[16:19], v[28:31], v[40:43]\n\t"
      "v_mfma_f32_16x16x32_bf16 v[44:47], v[20:23], v[28:31], v[44:47]\n\t"
      "LGATE_1%=:\n\t"
      "LBAR_1%=:\n\t"
      "s_cmp_gt_u32 s40, 1\n\t"
      "s_barrier\n\t"
      "s_cbranch_scc1 LPOLL_1%=\n\t"
      "s_branch LFIRST_1%=\n\t"
      ".p2align 6\n\t.fill 16, 4, 0xbf800000\n\t"
      "LPOLL_2%=:\n\t"
      "buffer_load_dwordx4 v[84:87], v60, s[36:39], s43 offen sc1\n\t"
      "buffer_load_dwordx4 v[88:91], v60, s[36:39], s43 offen offset:1024 sc1\n\t"
      "s_waitcnt lgkmcnt(8)\n\t"
      "v_mfma_f32_16x16x32_bf16 v[32:35], v[0:3], v[8:11], v[32:35]\n\t"
      "v_mfma_f32_16x16x32_bf16 v[36:39], v[4:7], v[8:11], v[36:39]\n\t"
      "v_mfma_f32_16x16x32_bf16 v[40:43], v[0:3], v[12:15], v[40:43]\n\t"
      "v_mfma_f32_16x16x32_bf16 v[44:47], v[4:7], v[12:15], v[44:47]\n\t"
      "buffer_load_dwordx4 v[92:95], v60, s[36:39], s68 offen sc1\n\t"
      "buffer_load_dwordx4 v[96:99], v60, s[36:39], s68 offen offset:1024 sc1\n\t"
      "v_mov_b64 v[48:49], 0\n\t"
      "v_mov_b64 v[50:51], 0\n\t"
      "v_mov_b64 v[52:53], 0\n\t"
      "v_mov_b64 v[54:55], 0\n\t"
      "s_xor_b32 s69, s97, 1024\n\t"
      "s_xor_b32 s98, s97, 512\n\t"
      "s_xor_b32 s23, s97, 1536\n\t"
      "s_waitcnt lgkmcnt(4)\n\t"
      "v_mfma_f32_16x16x32_bf16 v[32:35], v[16:19], v[24:27], v[32:35]\n\t"
      "v_mfma_f32_16x16x32_bf16 v[36:39], v[20:23], v[24:27], v[36:39]\n\t"
      "v_mfma_f32_16x16x32_bf16 v[40:43], v[16:19], v[28:31], v[40:43]\n\t"
      "v_mfma_f32_16x16x32_bf16 v[44:47], v[20:23], v[28:31], v[44:47]\n\t"
      "s_waitcnt lgkmcnt(0)\n\t"
      "v_mfma_f32_16x16x32_bf16 v[32:35], v[76:79], v[112:115], v[32:35]\n\t"
      "v_mfma_f32_16x16x32_bf16 v[36:39], v[80:83], v[112:115], v[36:39]\n\t"
      "v_mfma_f32_16x16x32_bf16 v[40:43], v[76:79], v[116:119], v[40:43]\n\t"
      "v_mfma_f32_16x16x32_bf16 v[44:47], v[80:83], v[116:119], v[44:47]\n\t"
      "s_mov_b64 exec, 1\n\t"
      "ds_write_b64 v109, v[110:111] offset:55392\n\t"
      "s_mov_b64 exec, -1\n\t"
      "s_nop 4\n\t"
      "v_cvt_pk_bf16_f32 v68, v32, v33\n\t"
      "v_cvt_pk_bf16_f32 v69, v34, v35\n\t"
      "v_cvt_pk_bf16_f32 v70, v36, v37\n\t"
      "v_cvt_pk_bf16_f32 v71, v38, v39\n\t"
      "v_cvt_pk_bf16_f32 v74, v40, v41\n\t"
      "v_cvt_pk_bf16_f32 v75, v42, v43\n\t"
      "v_cvt_pk_bf16_f32 v72, v44, v45\n\t"
      "v_cvt_pk_bf16_f32 v73, v46, v47\n\t"
      "v_mov_b64 v[32:33], 0\n\t"
      "v_mov_b64 v[34:35], 0\n\t"
      "v_mov_b64 v[36:37], 0\n\t"
      "v_mov_b64 v[38:39], 0\n\t"
      "v_mov_b64 v[40:41], 0\n\t"
      "v_mov_b64 v[42:43], 0\n\t"
      "v_mov_b64 v[44:45], 0\n\t"
      "v_mov_b64 v[46:47], 0\n\t"
      "LGO_2%=:\n\t"
      "s_nop 0\n\t"
      "s_nop 0\n\t"
      "s_nop 0\n\t"
      "s_nop 0\n\t"
      "s_nop 0\n\t"
      "s_waitcnt vmcnt(3)\n\t"
      "v_pk_fma_f32 v[14:15], v[86:87], s[56:57], v[126:127] op_sel:[0,1,0] op_sel_hi:[1,1,0]\n\t"
      "v_pk_fma_f32 v[12:13], v[84:85], s[56:57], v[126:127] op_sel:[0,1,0] op_sel_hi:[1,1,0]\n\t"
      "v_cvt_pk_bf16_f32 v8, v12, v13\n\t"
      "v_cvt_pk_bf16_f32 v9, v14, v15\n\t"
      "s_nop 0\n\t"
      "ds_write_b64 v127, v[8:9] offset:43008\n\t"
      "s_waitcnt vmcnt(2)\n\t"
      "v_pk_fma_f32 v[0:1], v[88:89], s[56:57], v[126:127] op_sel:[0,1,0] op_sel_hi:[1,1,0]\n\t"
      "v_pk_fma_f32 v[2:3], v[90:91], s[56:57], v[126:127] op_sel:[0,1,0] op_sel_hi:[1,1,0]\n\t"
      "v_cvt_pk_bf16_f32 v10, v0, v1\n\t"
      "v_cvt_pk_bf16_f32 v11, v2, v3\n\t"
      "ds_write_b64 v127, v[10:11] offset:43016\n\t"
      "v_bitop3_b32 v64, v84, v85, v86 bitop3:0x80\n\t"
      "v_mfma_f32_16x16x32_bf16 v[56:59], v[8:11], v[8:11], 0\n\t"
      "v_bitop3_b32 v64, v64, v87, v88 bitop3:0x80\n\t"
      "v_bitop3_b32 v64, v64, v89, v90 bitop3:0x80\n\t"
      "v_bitop3_b32 v64, v64, v91, s58 bitop3:0x80\n\t"
      "v_cmp_eq_u32_e64 vcc, 0, v64\n\t"
      "v_smfmac_f32_16x16x64_bf16 v[36:39], v[8:11], a[80:87], v122\n\t"
      "s_nop 0\n\t"
      "v_bitop3_b32 v120, v120, v121, s53 bitop3:0x7e\n\t"
      "v_smfmac_f32_16x16x64_bf16 v[44:47], v[8:11], a[208:215], v122\n\t"
      "s_cbranch_vccnz LR0_2%=\n\t"
      "LC0_2%=:\n\t"
      "s_waitcnt lgkmcnt(0)\n\t"
      "s_barrier\n\t"
      "ds_read_b128 v[12:15], v127 offset:44032\n\t"
      "v_smfmac_f32_16x16x64_bf16 v[52:55], v[8:11], v[208:215], v122\n\t"
      "ds_read_b128 v[0:3], v127 offset:40960\n\t"
      "v_smfmac_f32_16x16x64_bf16 v[48:51], v[8:11], v[144:151], v122\n\t"
      "ds_read_b128 v[4:7], v127 offset:41984\n\t"
      "v_smfmac_f32_16x16x64_bf16 v[32:35], v[8:11], a[16:23], v122\n\t"
      "ds_read_b128 v[84:87], v127 offset:36864\n\t"
      "v_smfmac_f32_16x16x64_bf16 v[40:43], v[8:11], a[144:151], v122\n\t"
      "s_waitcnt lgkmcnt(3)\n\t"
      "v_smfmac_f32_16x16x64_bf16 v[32:35], v[12:15], a[24:31], v122\n\t"
      "ds_read_b128 v[80:83], v127 offset:35840\n\t"
      "v_smfmac_f32_16x16x64_bf16 v[40:43], v[12:15], a[152:159], v122\n\t"
      "s_waitcnt vmcnt(1)\n\t"
      "v_fma_f32 v31, v95, s57, v126\n\t"
      "v_fma_f32 v28, v92, s57, v126\n\t"
      "v_smfmac_f32_16x16x64_bf16 v[36:39], v[12:15], a[88:95], v122\n\t"
      "v_fma_f32 v30, v94, s57, v126\n\t"
      "v_cvt_pk_bf16_f32 v25, v30, v31\n\t"
      "v_bitop3_b32 v64, v92, v93, v94 bitop3:0x80\n\t"
      "v_smfmac_f32_16x16x64_bf16 v[44:47], v[12:15], a[216:223], v122\n\t"
      "s_waitcnt lgkmcnt(2)\n\t"
      "v_fma_f32 v29, v93, s57, v126\n\t"
      "v_cvt_pk_bf16_f32 v24, v28, v29\n\t"
      "v_smfmac_f32_16x16x64_bf16 v[36:39], v[0:3], a[64:71], v122\n\t"
      "s_waitcnt vmcnt(0)\n\t"
      "ds_write_b64 v127, v[24:25] offset:47104\n\t"
      "v_fma_f32 v19, v99, s57, v126\n\t"
      "v_fma_f32 v18, v98, s57, v126\n\t"
      "v_cvt_pk_bf16_f32 v27, v18, v19\n\t"
      "v_fma_f32 v17, v97, s57, v126\n\t"
      "v_fma_f32 v16, v96, s57, v126\n\t"
      "v_cvt_pk_bf16_f32 v26, v16, v17\n\t"
      "v_smfmac_f32_16x16x64_bf16 v[44:47], v[0:3], a[192:199], v122\n\t"
      "ds_write_b64 v127, v[26:27] offset:47112\n\t"
      "v_smfmac_f32_16x16x64_bf16 v[40:43], v[0:3], a[128:135], v122\n\t"
      "ds_read_b128 v[88:91], v127 offset:39936\n\t"
      "v_bitop3_b32 v64, v64, v95, v96 bitop3:0x80\n\t"
      "v_bitop3_b32 v64, v64, v97, v98 bitop3:0x80\n\t"
      "v_smfmac_f32_16x16x64_bf16 v[40:43], v[4:7], a[136:143], v122\n\t"
      "v_bitop3_b32 v64, v64, v99, s60 bitop3:0x80\n\t"
      "v_smfmac_f32_16x16x64_bf16 v[36:39], v[4:7], a[72:79], v122\n\t"
      "v_cmp_eq_u32_e64 vcc, 0, v64\n\t"
      "v_smfmac_f32_16x16x64_bf16 v[44:47], v[4:7], a[200:207], v122\n\t"
      "s_waitcnt lgkmcnt(1)\n\t"
      "v_smfmac_f32_16x16x64_bf16 v[32:35], v[0:3], a[0:7], v122\n\t"
      "s_cbranch_vccnz LR1_2%=\n\t"
      "LC1_2%=:\n\t"
      "v_smfmac_f32_16x16x64_bf16 v[40:43], v[24:27], a[176:183], v122\n\t"
      "s_barrier\n\t"
      "v_mfma_f32_16x16x32_bf16 v[56:59], v[24:27], v[24:27], v[56:59]\n\t"
      "ds_read_b128 v[28:31], v127 offset:48128\n\t"
      "s_nop 0\n\t"
      "v_smfmac_f32_16x16x64_bf16 v[32:35], v[4:7], a[8:15], v122\n\t"
      "ds_read_b128 v[16:19], v127 offset:45056\n\t"
      "v_smfmac_f32_16x16x64_bf16 v[44:47], v[24:27], a[240:247], v122\n\t"
      "ds_read_b128 v[20:23], v127 offset:46080\n\t"
      "v_max3_f32 v56, v56, v57, v58\n\t"
      "v_smfmac_f32_16x16x64_bf16 v[36:39], v[24:27], a[112:119], v122\n\t"
      "ds_read_b128 v[76:79], v127 offset:32768\n\t"
      "v_max_f32_e64 v56, v56, v59\n\t"
      "v_smfmac_f32_16x16x64_bf16 v[32:35], v[24:27], a[48:55], v122\n\t"
      "s_waitcnt lgkmcnt(3)\n\t"
      "v_readfirstlane_b32 s56, v120\n\t"
      "v_smfmac_f32_16x16x64_bf16 v[32:35], v[28:31], a[56:63], v122\n\t"
      "s_waitcnt lgkmcnt(2)\n\t"
      "v_max_f32_dpp v56, v56, v56 quad_perm:[1,0,3,2] row_mask:0xf bank_mask:0xf\n\t"
      "v_smfmac_f32_16x16x64_bf16 v[32:35], v[16:19], a[32:39], v122\n\t"
      "s_waitcnt lgkmcnt(1)\n\t"
      "v_max_f32_dpp v56, v56, v56 quad_perm:[2,3,0,1] row_mask:0xf bank_mask:0xf\n\t"
      "v_smfmac_f32_16x16x64_bf16 v[32:35], v[20:23], a[40:47], v122\n\t"
      "s_mov_b32 exec_lo, 0x100001\n\t"
      "s_mov_b32 exec_hi, 0x10000100\n\t"
      "v_smfmac_f32_16x16x64_bf16 v[36:39], v[28:31], a[120:127], v122\n\t"
      "ds_max_i32 v109, v56 offset:55392\n\t"
      "v_smfmac_f32_16x16x64_bf16 v[40:43], v[28:31], a[184:191], v122\n\t"
      "s_mov_b64 exec, -1\n\t"
      "v_add_f32 v33, v33, v35\n\t"
      "v_smfmac_f32_16x16x64_bf16 v[36:39], v[16:19], a[96:103], v122\n\t"
      "ds_read_b128 v[60:63], v127 offset:37888\n\t"
      "v_mul_f32_e64 v33, s59, v33\n\t"
      "v_smfmac_f32_16x16x64_bf16 v[36:39], v[20:23], a[104:111], v122\n\t"
      "v_add_f32_e64 v32, v32, v34\n\t"
      "v_smfmac_f32_16x16x64_bf16 v[44:47], v[28:31], a[248:255], v122\n\t"
      "ds_read_b128 v[56:59], v127 offset:38912\n\t"
      "v_mul_f32_e64 v32, s59, v32\n\t"
      "v_smfmac_f32_16x16x64_bf16 v[44:47], v[16:19], a[224:231], v122\n\t"
      "v_add_f32 v36, v36, v38\n\t"
      "v_add_f32 v37, v37, v39\n\t"
      "v_mul_f32_e64 v37, s59, v37\n\t"
      "v_smfmac_f32_16x16x64_bf16 v[40:43], v[16:19], a[160:167], v122\n\t"
      "ds_read_b128 v[92:95], v127 offset:34816\n\t"
      "v_cvt_pk_bf16_f32 v112, v32, v33\n\t"
      "v_smfmac_f32_16x16x64_bf16 v[44:47], v[20:23], a[232:239], v122\n\t"
      "ds_read_b128 v[96:99], v127 offset:33792\n\t"
      "v_mul_f32_e64 v36, s59, v36\n\t"
      "v_cvt_pk_bf16_f32 v113, v36, v37\n\t"
      "v_smfmac_f32_16x16x64_bf16 v[40:43], v[20:23], a[168:175], v122\n\t"
      "ds_write_b64 v125, v[112:113]\n\t"
      "v_smfmac_f32_16x16x64_bf16 v[48:51], v[12:15], v[152:159], v122\n\t"
      "s_nop 0\n\t"
      "v_add_f32 v45, v45, v47\n\t"
      "v_add_f32_e64 v44, v44, v46\n\t"
      "v_cvt_pk_bf16_f32 v121, v44, v45\n\t"
      "v_add_f32 v41, v41, v43\n\t"
      "v_add_f32 v40, v40, v42\n\t"
      "v_cvt_pk_bf16_f32 v120, v40, v41\n\t"
      "v_smfmac_f32_16x16x64_bf16 v[48:51], v[0:3], v[128:135], v122\n\t"
      "s_mov_b32 s58, 0x10000000\n\t"
      "ds_write_b64 v125, v[120:121] offset:2080\n\t"
      "v_smfmac_f32_16x16x64_bf16 v[48:51], v[4:7], v[136:143], v122\n\t"
      "s_waitcnt lgkmcnt(1)\n\t"
      "s_barrier\n\t"
      "ds_read_b64_tr_b16 v[36:37], v124 offset:50192\n\t"
      "v_smfmac_f32_16x16x64_bf16 v[48:51], v[24:27], v[176:183], v122\n\t"
      "ds_read_b32 v108, v109 offset:55392\n\t"
      "ds_read_b64_tr_b16 v[32:33], v124 offset:49152\n\t"
      "v_smfmac_f32_16x16x64_bf16 v[52:55], v[12:15], v[216:223], v122\n\t"
      "ds_read_b64_tr_b16 v[34:35], v124 offset:49672\n\t"
      "s_waitcnt lgkmcnt(4)\n\t"
      "ds_read_b64_tr_b16 v[38:39], v124 offset:50712\n\t"
      "v_smfmac_f32_16x16x64_bf16 v[48:51], v[28:31], v[184:191], v122\n\t"
      "s_barrier\n\t"
      "ds_read_b64_tr_b16 v[46:47], v124 offset:52792\n\t"
      "v_smfmac_f32_16x16x64_bf16 v[48:51], v[16:19], v[160:167], v122\n\t"
      "s_waitcnt lgkmcnt(4)\n\t"
      "ds_read_b64_tr_b16 v[44:45], v124 offset:52272\n\t"
      "v_readfirstlane_b32 s61, v108\n\t"
      "v_smfmac_f32_16x16x64_bf16 v[48:51], v[20:23], v[168:175], v122\n\t"
      "s_waitcnt lgkmcnt(3)\n\t"
      "ds_read_b64_tr_b16 v[42:43], v124 offset:51752\n\t"
      "ds_read_b64_tr_b16 v[40:41], v124 offset:51232\n\t"
      "s_waitcnt lgkmcnt(4)\n\t"
      "v_mfma_f32_16x16x32_bf16 v[112:115], v[76:79], v[32:35], 0\n\t"
      "s_lshr_b32 s61, s61, 23\n\t"
      "s_add_u32 s61, s61, s67\n\t"
      "v_mov_b32_dpp v102, v34 quad_perm:[2,3,0,1] row_mask:0xf bank_mask:0xf\n\t"
      "v_mov_b32_dpp v106, v38 quad_perm:[2,3,0,1] row_mask:0xf bank_mask:0xf\n\t"
      "s_max_u32 s61, s61, s66\n\t"
      "v_mfma_f32_16x16x32_bf16 v[116:119], v[80:83], v[32:35], 0\n\t"
      "s_lshr_b32 s61, s61, 1\n\t"
      "v_add_f32_e64 v48, v48, v50\n\t"
      "v_mov_b32_dpp v100, v32 quad_perm:[2,3,0,1] row_mask:0xf bank_mask:0xf\n\t"
      "v_mfma_f32_16x16x32_bf16 v[116:119], v[88:91], v[36:39], v[116:119]\n\t"
      "s_min_u32 s61, s61, 200\n\t"
      "v_mov_b32_dpp v107, v39 quad_perm:[2,3,0,1] row_mask:0xf bank_mask:0xf\n\t"
      "s_waitcnt lgkmcnt(0)\n\t"
      "v_mfma_f32_16x16x32_bf16 v[76:79], v[40:43], v[32:35], 0\n\t"
      "buffer_load_dwordx4 v127, s[36:39], s46 offen sc1 lds\n\t"
      "s_lshl_b32 s61, s61, 23\n\t"
      "v_mfma_f32_16x16x32_bf16 v[76:79], v[44:47], v[36:39], v[76:79]\n\t"
      "s_add_u32 s65, s61, 33554432\n\t"
      "v_mov_b32_dpp v104, v36 quad_perm:[2,3,0,1] row_mask:0xf bank_mask:0xf\n\t"
      "v_mov_b32_dpp v103, v35 quad_perm:[2,3,0,1] row_mask:0xf bank_mask:0xf\n\t"
      "v_mov_b32_dpp v101, v33 quad_perm:[2,3,0,1] row_mask:0xf bank_mask:0xf\n\t"
      "v_mfma_f32_16x16x32_bf16 v[112:115], v[84:87], v[36:39], v[112:115]\n\t"
      "s_pack_hh_b32_b16 s75, s65, s65\n\t"
      "v_add_f32 v49, v49, v51\n\t"
      "v_cvt_pk_bf16_f32 v64, v48, v49\n\t"
      "v_cndmask_b32_e64 v120, v76, v78, s[72:73]\n\t"
      "v_cndmask_b32_e64 v121, v77, v79, s[72:73]\n\t"
      "v_mov_b32_dpp v105, v37 quad_perm:[2,3,0,1] row_mask:0xf bank_mask:0xf\n\t"
      "v_mfma_f32_16x16x32_bf16 v[112:115], v[92:95], v[100:103], v[112:115]\n\t"
      "buffer_load_dwordx4 v127, s[36:39], s46 offen offset:1024 sc1 lds\n\t"
      "v_mfma_f32_16x16x32_bf16 v[116:119], v[96:99], v[100:103], v[116:119]\n\t"
      "s_pack_hh_b32_b16 s74, s65, s65\n\t"
      "v_add_f32_dpp v121, v121, v121 quad_perm:[2,3,0,1] row_mask:0xf bank_mask:0xf\n\t"
      "v_add_f32_dpp v120, v120, v120 quad_perm:[2,3,0,1] row_mask:0xf bank_mask:0xf\n\t"
      "v_mfma_f32_16x16x32_bf16 v[112:115], v[56:59], v[104:107], v[112:115]\n\t"
      "v_exp_f32 v81, v121\n\t"
      "v_mfma_f32_16x16x32_bf16 v[116:119], v[60:63], v[104:107], v[116:119]\n\t"
      "s_mov_b32 m0, s45\n\t"
      "v_mov_b64 v[90:91], s[74:75]\n\t"
      "v_exp_f32 v80, v120\n\t"
      "v_mov_b64 v[88:89], s[74:75]\n\t"
      "v_cvt_pk_bf16_f32 v108, v80, v81\n\t"
      "v_smfmac_f32_16x16x64_bf16 v[52:55], v[0:3], v[192:199], v122\n\t"
      "buffer_load_dwordx4 v65, s[36:39], s44 offen offset:1024 sc1 lds\n\t"
      "v_mfma_f32_16x16x32_bf16 v[84:87], v[108:111], v[64:67], 0\n\t"
      "s_add_u32 s57, s61, 310378496\n\t"
      "v_cndmask_b32_e64 v112, v112, v116, s[72:73]\n\t"
      "v_exp_f32_e64 v56, v112\n\t"
      "v_mfma_f32_16x16x32_bf16 v[48:51], v[108:111], v[88:91], 0\n\t"
      "s_add_u32 s43, s43, 0x8000\n\t"
      "v_cndmask_b32_e64 v113, v113, v117, s[72:73]\n\t"
      "v_cndmask_b32_e64 v114, v114, v118, s[72:73]\n\t"
      "v_exp_f32_e64 v58, v114\n\t"
      "v_cndmask_b32_e64 v115, v115, v119, s[72:73]\n\t"
      "v_exp_f32 v57, v113\n\t"
      "v_exp_f32 v59, v115\n\t"
      "v_cvt_pk_bf16_f32 v96, v56, v57\n\t"
      "v_cvt_pk_bf16_f32 v97, v58, v59\n\t"
      "v_max3_f32 v82, |v112|, |v113|, |v114|\n\t"
      "v_mov_b32_dpp v98, v96 quad_perm:[2,3,0,1] row_mask:0xf bank_mask:0xf\n\t"
      "v_mov_b32_dpp v99, v97 quad_perm:[2,3,0,1] row_mask:0xf bank_mask:0xf\n\t"
      "v_smfmac_f32_16x16x64_bf16 v[52:55], v[4:7], v[200:207], v122\n\t"
      "v_max3_f32 v83, |v115|, |v120|, |v121|\n\t"
      "v_mfma_f32_16x16x32_bf16 v[48:51], v[96:99], v[88:91], v[48:51]\n\t"
      "v_max_f32_e64 v82, v82, v83\n\t"
      "v_cmp_nge_f32 vcc, 0x42000000, v82\n\t"
      "v_mfma_f32_16x16x32_bf16 v[84:87], v[96:99], v[68:71], v[84:87]\n\t"
      "buffer_load_dwordx4 v65, s[36:39], s44 offen sc1 lds\n\t"
      "v_smfmac_f32_16x16x64_bf16 v[52:55], v[24:27], v[240:247], v122\n\t"
      "s_add_u32 s68, s68, 0x8000\n\t"
      "s_cbranch_vccnz LMX_2%=\n\t"
      "v_rcp_f32 v49, v49\n\t"
      "v_rcp_f32_e64 v48, v48\n\t"
      "v_smfmac_f32_16x16x64_bf16 v[52:55], v[28:31], v[248:255], v122\n\t"
      "v_fma_f32 v85, v85, v49, s96\n\t"
      "v_fma_f32 v84, v84, v48, s96\n\t"
      "v_smfmac_f32_16x16x64_bf16 v[52:55], v[16:19], v[224:231], v122\n\t"
      "buffer_atomic_add v84, v123, s[36:39], s97 offen\n\t"
      "buffer_atomic_add v85, v123, s[36:39], s98 offen\n\t"
      "v_smfmac_f32_16x16x64_bf16 v[52:55], v[20:23], v[232:239], v122\n\t"
      "v_mfma_f32_16x16x32_bf16 v[92:95], v[96:99], v[72:75], 0\n\t"
      "s_nop 3\n\t"
      "v_add_f32 v53, v53, v55\n\t"
      "v_add_f32_e64 v52, v52, v54\n\t"
      "v_cvt_pk_bf16_f32 v66, v52, v53\n\t"
      "s_add_u32 s62, s61, 0x83400000\n\t"
      "v_mov_b32_e64 v126, s62\n\t"
      "v_mfma_f32_16x16x32_bf16 v[92:95], v[108:111], v[66:69], v[92:95]\n\t"
      "s_add_u32 s97, s97, 0x8000\n\t"
      "s_nop 6\n\t"
      "v_fma_f32 v95, v95, v49, s96\n\t"
      "buffer_atomic_add v95, v123, s[36:39], s23 offen\n\t"
      "v_fma_f32 v94, v94, v48, s96\n\t"
      "buffer_atomic_add v94, v123, s[36:39], s69 offen\n\t"
      "LWIN_2%=:\n\t"
      "v_add_u32 v100, s55, v127\n\t"
      "ds_read_b128 v[60:63], v100 offset:57344\n\t"
      "s_cmp_eq_u32 s56, 0\n\t"
      "s_cbranch_scc0 LSLOW_2%=\n\t"
      "LWIN1_2%=:\n\t"
      "buffer_load_dwordx2 v[120:121], off, s[36:39], s50 sc1\n\t"
      "s_mov_b32 s64, s50\n\t"
      "s_bitcmp1_b64 s[84:85], s40\n\t"
      "s_cbranch_scc1 LEARLY_2%=\n\t"
      "LROLE_2%=:\n\t"
      "v_add_u32 v52, 65536, v127\n\t"
      "ds_read_b128 v[88:91], v52 offset:16384\n\t"
      "ds_read_b128 v[92:95], v52 offset:17408\n\t"
      "ds_read_b128 v[32:35], v52 offset:20480\n\t"
      "ds_read_b128 v[36:39], v52 offset:21504\n\t"
      "s_cmp_eq_u32 s42, 3\n\t"
      "s_waitcnt lgkmcnt(4)\n\t"
      "s_cbranch_scc1 LWK1_2%=\n\t"
      "s_cmp_eq_u32 s42, 2\n\t"
      "s_cbranch_scc0 LW4_2%=\n\t"
      "buffer_store_dwordx4 v[40:43], v61, s[36:39], s48 offen sc1\n\t"
      "LW4_2%=:\n\t"
      "s_add_u32 s48, s48, 65536\n\t"
      "s_nop 0\n\t"
      "ds_read_b128 v[40:43], v52 offset:24576\n\t"
      "ds_read_b128 v[44:47], v52 offset:25600\n\t"
      "v_mov_b64 v[48:49], 0\n\t"
      "v_mov_b64 v[50:51], 0\n\t"
      "s_memrealtime s[92:93]\n\t"
      "s_waitcnt lgkmcnt(0)\n\t"
      "s_sub_u32 s61, s92, s95\n\t"
      "s_mov_b32 s95, s92\n\t"
      "s_min_u32 s61, s61, 190\n\t"
      "s_add_u32 s61, s61, s92\n\t"
      "s_pack_ll_b32_b16 s61, s61, s40\n\t"
      "v_mov_b32 v63, s61\n\t"
      "s_mov_b64 exec, 1\n\t"
      "buffer_store_dword v63, off, s[36:39], s51 sc1\n\t"
      "s_mov_b64 exec, -1\n\t"
      "v_add_u32 v104, s71, v100\n\t"
      "ds_read_b128 v[76:79], v104\n\t"
      "ds_read_b128 v[80:83], v104 offset:1024\n\t"
      "ds_read_b128 v[112:115], v100\n\t"
      "ds_read_b128 v[116:119], v100 offset:1024\n\t"
      "v_xor_b32 v101, 2048, v104\n\t"
      "v_xor_b32 v102, 4096, v104\n\t"
      "v_xor_b32 v103, 6144, v104\n\t"
      "v_smfmac_f32_16x16x64_bf16 v[48:51], v[16:19], v[88:95], v122\n\t"
      "ds_read_b128 v[88:91], v52 offset:28672\n\t"
      "ds_read_b128 v[92:95], v52 offset:29696\n\t"
      "s_add_u32 m0, s55, s71\n\t"
      "s_sub_u32 s71, 204800, s71\n\t"
      "v_smfmac_f32_16x16x64_bf16 v[48:51], v[20:23], v[32:39], v122\n\t"
      "ds_read_b128 v[0:3], v101\n\t"
      "ds_read_b128 v[4:7], v101 offset:1024\n\t"
      "s_add_u32 s40, s40, 1\n\t"
      "s_cmp_lt_u32 s40, 63\n\t"
      "v_smfmac_f32_16x16x64_bf16 v[48:51], v[24:27], v[40:47], v122\n\t"
      "ds_read_b128 v[8:11], v100 offset:8192\n\t"
      "ds_read_b128 v[12:15], v100 offset:9216\n\t"
      "s_cselect_b32 s61, 786432, 0\n\t"
      "s_cselect_b32 s60, s58, 0\n\t"
      "s_waitcnt lgkmcnt(4)\n\t"
      "v_smfmac_f32_16x16x64_bf16 v[48:51], v[28:31], v[88:95], v122\n\t"
      "s_add_u32 s44, s44, s61\n\t"
      "s_cmp_lt_u32 s40, 62\n\t"
      "ds_read_b128 v[16:19], v102\n\t"
      "ds_read_b128 v[20:23], v102 offset:1024\n\t"
      "ds_read_b128 v[24:27], v100 offset:16384\n\t"
      "ds_read_b128 v[28:31], v100 offset:17408\n\t"
      "s_cselect_b32 s61, 786432, 0\n\t"
      "s_add_u32 s46, s46, s61\n\t"
      "s_add_u32 s61, s40, 3\n\t"
      "s_and_b32 s62, s61, 3\n\t"
      "s_cselect_b32 s62, 16, 2000\n\t"
      "s_cmp_lt_u32 s61, 64\n\t"
      "s_cselect_b32 s62, s62, 0\n\t"
      "s_add_u32 s50, s50, s62\n\t"
      "v_add_f32 v48, v48, v50\n\t"
      "v_add_f32 v49, v49, v51\n\t"
      "ds_write_b64 v124, v[48:49] offset:53312\n\t"
      "v_mfma_f32_16x16x32_bf16 v[32:35], v[76:79], v[112:115], 0\n\t"
      "s_mov_b32 s63, 0x2000\n\t"
      "v_mfma_f32_16x16x32_bf16 v[36:39], v[80:83], v[112:115], 0\n\t"
      "v_mfma_f32_16x16x32_bf16 v[40:43], v[76:79], v[116:119], 0\n\t"
      "v_mfma_f32_16x16x32_bf16 v[44:47], v[80:83], v[116:119], 0\n\t"
      "ds_read_b128 v[76:79], v103\n\t"
      "ds_read_b128 v[80:83], v103 offset:1024\n\t"
      "ds_read_b128 v[112:115], v100 offset:24576\n\t"
      "ds_read_b128 v[116:119], v100 offset:25600\n\t"
      "LGATE_2%=:\n\t"
      "LBAR_2%=:\n\t"
      "s_cmp_gt_u32 s40, 1\n\t"
      "s_waitcnt lgkmcnt(0)\n\t"
      "s_barrier\n\t"
      "s_cbranch_scc1 LPOLL_2%=\n\t"
      "s_branch LFIRST_2%=\n\t"
      ".p2align 6\n\t.fill 16, 4, 0xbf800000\n\t"
      "LPOLL_3%=:\n\t"
      "s_memtime s[78:79]\n\t"
      "buffer_load_dwordx4 v[84:87], v60, s[36:39], s43 offen sc1\n\t"
      "buffer_load_dwordx4 v[88:91], v60, s[36:39], s43 offen offset:1024 sc1\n\t"
      "s_waitcnt lgkmcnt(8)\n\t"
      "v_mfma_f32_16x16x32_bf16 v[32:35], v[0:3], v[8:11], v[32:35]\n\t"
      "v_mfma_f32_16x16x32_bf16 v[36:39], v[4:7], v[8:11], v[36:39]\n\t"
      "v_mfma_f32_16x16x32_bf16 v[40:43], v[0:3], v[12:15], v[40:43]\n\t"
      "v_mfma_f32_16x16x32_bf16 v[44:47], v[4:7], v[12:15], v[44:47]\n\t"
      "buffer_load_dwordx4 v[92:95], v60, s[36:39], s68 offen sc1\n\t"
      "buffer_load_dwordx4 v[96:99], v60, s[36:39], s68 offen offset:1024 sc1\n\t"
      "v_mov_b64 v[48:49], 0\n\t"
      "v_mov_b64 v[50:51], 0\n\t"
      "v_mov_b64 v[52:53], 0\n\t"
      "v_mov_b64 v[54:55], 0\n\t"
      "s_xor_b32 s69, s97, 1024\n\t"
      "s_xor_b32 s98, s97, 512\n\t"
      "s_xor_b32 s23, s97, 1536\n\t"
      "s_waitcnt lgkmcnt(4)\n\t"
      "v_mfma_f32_16x16x32_bf16 v[32:35], v[16:19], v[24:27], v[32:35]\n\t"
      "v_mfma_f32_16x16x32_bf16 v[36:39], v[20:23], v[24:27], v[36:39]\n\t"
      "v_mfma_f32_16x16x32_bf16 v[40:43], v[16:19], v[28:31], v[40:43]\n\t"
      "v_mfma_f32_16x16x32_bf16 v[44:47], v[20:23], v[28:31], v[44:47]\n\t"
      "s_waitcnt lgkmcnt(0)\n\t"
      "v_mfma_f32_16x16x32_bf16 v[32:35], v[76:79], v[112:115], v[32:35]\n\t"
      "v_mfma_f32_16x16x32_bf16 v[36:39], v[80:83], v[112:115], v[36:39]\n\t"
      "v_mfma_f32_16x16x32_bf16 v[40:43], v[76:79], v[116:119], v[40:43]\n\t"
      "v_mfma_f32_16x16x32_bf16 v[44:47], v[80:83], v[116:119], v[44:47]\n\t"
      "s_mov_b64 exec, 1\n\t"
      "ds_write_b64 v109, v[110:111] offset:55392\n\t"
      "s_mov_b64 exec, -1\n\t"
      "s_nop 4\n\t"
      "v_cvt_pk_bf16_f32 v68, v32, v33\n\t"
      "v_cvt_pk_bf16_f32 v69, v34, v35\n\t"
      "v_cvt_pk_bf16_f32 v70, v36, v37\n\t"
      "v_cvt_pk_bf16_f32 v71, v38, v39\n\t"
      "v_cvt_pk_bf16_f32 v74, v40, v41\n\t"
      "v_cvt_pk_bf16_f32 v75, v42, v43\n\t"
      "v_cvt_pk_bf16_f32 v72, v44, v45\n\t"
      "v_cvt_pk_bf16_f32 v73, v46, v47\n\t"
      "v_mov_b64 v[32:33], 0\n\t"
      "v_mov_b64 v[34:35], 0\n\t"
      "v_mov_b64 v[36:37], 0\n\t"
      "v_mov_b64 v[38:39], 0\n\t"
      "v_mov_b64 v[40:41], 0\n\t"
      "v_mov_b64 v[42:43], 0\n\t"
      "v_mov_b64 v[44:45], 0\n\t"
      "v_mov_b64 v[46:47], 0\n\t"
      "LGO_3%=:\n\t"
      "s_nop 0\n\t"
      "s_waitcnt vmcnt(3)\n\t"
      "s_memtime s[88:89]\n\t"
      "v_pk_fma_f32 v[10:11], v[86:87], s[56:57], v[126:127] op_sel:[0,1,0] op_sel_hi:[1,1,0]\n\t"
      "v_pk_fma_f32 v[8:9], v[84:85], s[56:57], v[126:127] op_sel:[0,1,0] op_sel_hi:[1,1,0]\n\t"
      "v_cvt_pk_bf16_f32 v12, v8, v9\n\t"
      "v_cvt_pk_bf16_f32 v13, v10, v11\n\t"
      "s_nop 0\n\t"
      "ds_write_b64 v127, v[12:13] offset:44032\n\t"
      "s_waitcnt vmcnt(2)\n\t"
      "v_pk_fma_f32 v[4:5], v[88:89], s[56:57], v[126:127] op_sel:[0,1,0] op_sel_hi:[1,1,0]\n\t"
      "v_pk_fma_f32 v[6:7], v[90:91], s[56:57], v[126:127] op_sel:[0,1,0] op_sel_hi:[1,1,0]\n\t"
      "v_cvt_pk_bf16_f32 v14, v4, v5\n\t"
      "v_cvt_pk_bf16_f32 v15, v6, v7\n\t"
      "ds_write_b64 v127, v[14:15] offset:44040\n\t"
      "v_bitop3_b32 v64, v84, v85, v86 bitop3:0x80\n\t"
      "v_mfma_f32_16x16x32_bf16 v[56:59], v[12:15], v[12:15], 0\n\t"
      "v_bitop3_b32 v64, v64, v87, v88 bitop3:0x80\n\t"
      "v_bitop3_b32 v64, v64, v89, v90 bitop3:0x80\n\t"
      "v_bitop3_b32 v64, v64, v91, s58 bitop3:0x80\n\t"
      "v_cmp_eq_u32_e64 vcc, 0, v64\n\t"
      "v_smfmac_f32_16x16x64_bf16 v[36:39], v[12:15], a[88:95], v122\n\t"
      "s_nop 0\n\t"
      "v_bitop3_b32 v120, v120, v121, s53 bitop3:0x7e\n\t"
      "v_smfmac_f32_16x16x64_bf16 v[44:47], v[12:15], a[216:223], v122\n\t"
      "s_cbranch_vccnz LR0_3%=\n\t"
      "LC0_3%=:\n\t"
      "s_waitcnt lgkmcnt(0)\n\t"
      "s_barrier\n\t"
      "ds_read_b128 v[8:11], v127 offset:43008\n\t"
      "v_smfmac_f32_16x16x64_bf16 v[52:55], v[12:15], v[216:223], v122\n\t"
      "ds_read_b128 v[4:7], v127 offset:41984\n\t"
      "v_smfmac_f32_16x16x64_bf16 v[48:51], v[12:15], v[152:159], v122\n\t"
      "ds_read_b128 v[0:3], v127 offset:40960\n\t"
      "v_smfmac_f32_16x16x64_bf16 v[32:35], v[12:15], a[24:31], v122\n\t"
      "ds_read_b128 v[84:87], v127 offset:36864\n\t"
      "v_smfmac_f32_16x16x64_bf16 v[40:43], v[12:15], a[152:159], v122\n\t"
      "s_waitcnt lgkmcnt(3)\n\t"
      "v_smfmac_f32_16x16x64_bf16 v[32:35], v[8:11], a[16:23], v122\n\t"
      "ds_read_b128 v[80:83], v127 offset:35840\n\t"
      "v_smfmac_f32_16x16x64_bf16 v[40:43], v[8:11], a[144:151], v122\n\t"
      "s_waitcnt vmcnt(1)\n\t"
      "v_fma_f32 v27, v95, s57, v126\n\t"
      "v_fma_f32 v24, v92, s57, v126\n\t"
      "v_smfmac_f32_16x16x64_bf16 v[36:39], v[8:11], a[80:87], v122\n\t"
      "v_fma_f32 v26, v94, s57, v126\n\t"
      "v_cvt_pk_bf16_f32 v29, v26, v27\n\t"
      "v_bitop3_b32 v64, v92, v93, v94 bitop3:0x80\n\t"
      "v_smfmac_f32_16x16x64_bf16 v[44:47], v[8:11], a[208:215], v122\n\t"
      "s_waitcnt lgkmcnt(2)\n\t"
      "v_fma_f32 v25, v93, s57, v126\n\t"
      "v_cvt_pk_bf16_f32 v28, v24, v25\n\t"
      "v_smfmac_f32_16x16x64_bf16 v[36:39], v[4:7], a[72:79], v122\n\t"
      "s_waitcnt vmcnt(0)\n\t"
      "ds_write_b64 v127, v[28:29] offset:48128\n\t"
      "v_fma_f32 v23, v99, s57, v126\n\t"
      "v_fma_f32 v22, v98, s57, v126\n\t"
      "v_cvt_pk_bf16_f32 v31, v22, v23\n\t"
      "v_fma_f32 v21, v97, s57, v126\n\t"
      "v_fma_f32 v20, v96, s57, v126\n\t"
      "v_cvt_pk_bf16_f32 v30, v20, v21\n\t"
      "v_smfmac_f32_16x16x64_bf16 v[44:47], v[4:7], a[200:207], v122\n\t"
      "ds_write_b64 v127, v[30:31] offset:48136\n\t"
      "v_smfmac_f32_16x16x64_bf16 v[40:43], v[4:7], a[136:143], v122\n\t"
      "ds_read_b128 v[88:91], v127 offset:39936\n\t"
      "v_bitop3_b32 v64, v64, v95, v96 bitop3:0x80\n\t"
      "v_bitop3_b32 v64, v64, v97, v98 bitop3:0x80\n\t"
      "v_smfmac_f32_16x16x64_bf16 v[40:43], v[0:3], a[128:135], v122\n\t"
      "v_bitop3_b32 v64, v64, v99, s60 bitop3:0x80\n\t"
      "v_smfmac_f32_16x16x64_bf16 v[36:39], v[0:3], a[64:71], v122\n\t"
      "v_cmp_eq_u32_e64 vcc, 0, v64\n\t"
      "v_smfmac_f32_16x16x64_bf16 v[44:47], v[0:3], a[192:199], v122\n\t"
      "s_waitcnt lgkmcnt(1)\n\t"
      "v_smfmac_f32_16x16x64_bf16 v[32:35], v[4:7], a[8:15], v122\n\t"
      "s_cbranch_vccnz LR1_3%=\n\t"
      "LC1_3%=:\n\t"
      "v_smfmac_f32_16x16x64_bf16 v[40:43], v[28:31], a[184:191], v122\n\t"
      "s_barrier\n\t"
      "v_mfma_f32_16x16x32_bf16 v[56:59], v[28:31], v[28:31], v[56:59]\n\t"
      "ds_read_b128 v[24:27], v127 offset:47104\n\t"
      "s_nop 0\n\t"
      "v_smfmac_f32_16x16x64_bf16 v[32:35], v[0:3], a[0:7], v122\n\t"
      "ds_read_b128 v[20:23], v127 offset:46080\n\t"
      "v_smfmac_f32_16x16x64_bf16 v[44:47], v[28:31], a[248:255], v122\n\t"
      "ds_read_b128 v[16:19], v127 offset:45056\n\t"
      "v_max3_f32 v56, v56, v57, v58\n\t"
      "v_smfmac_f32_16x16x64_bf16 v[36:39], v[28:31], a[120:127], v122\n\t"
      "ds_read_b128 v[76:79], v127 offset:32768\n\t"
      "v_max_f32_e64 v56, v56, v59\n\t"
      "v_smfmac_f32_16x16x64_bf16 v[32:35], v[28:31], a[56:63], v122\n\t"
      "s_waitcnt lgkmcnt(3)\n\t"
      "v_readfirstlane_b32 s56, v120\n\t"
      "v_smfmac_f32_16x16x64_bf16 v[32:35], v[24:27], a[48:55], v122\n\t"
      "s_waitcnt lgkmcnt(2)\n\t"
      "v_max_f32_dpp v56, v56, v56 quad_perm:[1,0,3,2] row_mask:0xf bank_mask:0xf\n\t"
      "v_smfmac_f32_16x16x64_bf16 v[32:35], v[20:23], a[40:47], v122\n\t"
      "s_waitcnt lgkmcnt(1)\n\t"
      "v_max_f32_dpp v56, v56, v56 quad_perm:[2,3,0,1] row_mask:0xf bank_mask:0xf\n\t"
      "v_smfmac_f32_16x16x64_bf16 v[32:35], v[16:19], a[32:39], v122\n\t"
      "s_mov_b32 exec_lo, 0x100001\n\t"
      "s_mov_b32 exec_hi, 0x10000100\n\t"
      "v_smfmac_f32_16x16x64_bf16 v[36:39], v[24:27], a[112:119], v122\n\t"
      "ds_max_i32 v109, v56 offset:55392\n\t"
      "v_smfmac_f32_16x16x64_bf16 v[40:43], v[24:27], a[176:183], v122\n\t"
      "s_mov_b64 exec, -1\n\t"
      "v_add_f32 v33, v33, v35\n\t"
      "v_smfmac_f32_16x16x64_bf16 v[36:39], v[20:23], a[104:111], v122\n\t"
      "ds_read_b128 v[60:63], v127 offset:37888\n\t"
      "v_mul_f32_e64 v33, s59, v33\n\t"
      "v_smfmac_f32_16x16x64_bf16 v[36:39], v[16:19], a[96:103], v122\n\t"
      "v_add_f32_e64 v32, v32, v34\n\t"
      "v_smfmac_f32_16x16x64_bf16 v[44:47], v[24:27], a[240:247], v122\n\t"
      "ds_read_b128 v[56:59], v127 offset:38912\n\t"
      "v_mul_f32_e64 v32, s59, v32\n\t"
      "v_smfmac_f32_16x16x64_bf16 v[44:47], v[20:23], a[232:239], v122\n\t"
      "v_add_f32 v36, v36, v38\n\t"
      "v_add_f32 v37, v37, v39\n\t"
      "v_mul_f32_e64 v37, s59, v37\n\t"
      "v_smfmac_f32_16x16x64_bf16 v[40:43], v[20:23], a[168:175], v122\n\t"
      "ds_read_b128 v[92:95], v127 offset:34816\n\t"
      "v_cvt_pk_bf16_f32 v112, v32, v33\n\t"
      "v_smfmac_f32_16x16x64_bf16 v[44:47], v[16:19], a[224:231], v122\n\t"
      "ds_read_b128 v[96:99], v127 offset:33792\n\t"
      "v_mul_f32_e64 v36, s59, v36\n\t"
      "v_cvt_pk_bf16_f32 v113, v36, v37\n\t"
      "v_smfmac_f32_16x16x64_bf16 v[40:43], v[16:19], a[160:167], v122\n\t"
      "ds_write_b64 v125, v[112:113]\n\t"
      "v_smfmac_f32_16x16x64_bf16 v[48:51], v[8:11], v[144:151], v122\n\t"
      "s_nop 0\n\t"
      "v_add_f32 v45, v45, v47\n\t"
      "v_add_f32_e64 v44, v44, v46\n\t"
      "v_cvt_pk_bf16_f32 v121, v44, v45\n\t"
      "v_add_f32 v41, v41, v43\n\t"
      "v_add_f32 v40, v40, v42\n\t"
      "v_cvt_pk_bf16_f32 v120, v40, v41\n\t"
      "v_smfmac_f32_16x16x64_bf16 v[48:51], v[4:7], v[136:143], v122\n\t"
      "s_mov_b32 s58, 0x10000000\n\t"
      "ds_write_b64 v125, v[120:121] offset:2080\n\t"
      "v_smfmac_f32_16x16x64_bf16 v[48:51], v[0:3], v[128:135], v122\n\t"
      "s_waitcnt lgkmcnt(1)\n\t"
      "s_barrier\n\t"
      "ds_read_b64_tr_b16 v[36:37], v124 offset:50192\n\t"
      "v_smfmac_f32_16x16x64_bf16 v[48:51], v[28:31], v[184:191], v122\n\t"
      "ds_read_b32 v108, v109 offset:55392\n\t"
      "ds_read_b64_tr_b16 v[32:33], v124 offset:49152\n\t"
      "v_smfmac_f32_16x16x64_bf16 v[52:55], v[8:11], v[208:215], v122\n\t"
      "ds_read_b64_tr_b16 v[34:35], v124 offset:49672\n\t"
      "s_waitcnt lgkmcnt(4)\n\t"
      "ds_read_b64_tr_b16 v[38:39], v124 offset:50712\n\t"
      "v_smfmac_f32_16x16x64_bf16 v[48:51], v[24:27], v[176:183], v122\n\t"
      "s_barrier\n\t"
      "ds_read_b64_tr_b16 v[46:47], v124 offset:52792\n\t"
      "v_smfmac_f32_16x16x64_bf16 v[48:51], v[20:23], v[168:175], v122\n\t"
      "s_waitcnt lgkmcnt(4)\n\t"
      "ds_read_b64_tr_b16 v[44:45], v124 offset:52272\n\t"
      "v_readfirstlane_b32 s61, v108\n\t"
      "v_smfmac_f32_16x16x64_bf16 v[48:51], v[16:19], v[160:167], v122\n\t"
      "s_waitcnt lgkmcnt(3)\n\t"
      "ds_read_b64_tr_b16 v[42:43], v124 offset:51752\n\t"
      "ds_read_b64_tr_b16 v[40:41], v124 offset:51232\n\t"
      "s_waitcnt lgkmcnt(4)\n\t"
      "v_mfma_f32_16x16x32_bf16 v[112:115], v[76:79], v[32:35], 0\n\t"
      "s_lshr_b32 s61, s61, 23\n\t"
      "s_add_u32 s61, s61, s67\n\t"
      "v_mov_b32_dpp v102, v34 quad_perm:[2,3,0,1] row_mask:0xf bank_mask:0xf\n\t"
      "v_mov_b32_dpp v106, v38 quad_perm:[2,3,0,1] row_mask:0xf bank_mask:0xf\n\t"
      "s_max_u32 s61, s61, s66\n\t"
      "v_mfma_f32_16x16x32_bf16 v[116:119], v[80:83], v[32:35], 0\n\t"
      "s_lshr_b32 s61, s61, 1\n\t"
      "v_add_f32_e64 v48, v48, v50\n\t"
      "v_mov_b32_dpp v100, v32 quad_perm:[2,3,0,1] row_mask:0xf bank_mask:0xf\n\t"
      "v_mfma_f32_16x16x32_bf16 v[116:119], v[88:91], v[36:39], v[116:119]\n\t"
      "s_min_u32 s61, s61, 200\n\t"
      "v_mov_b32_dpp v107, v39 quad_perm:[2,3,0,1] row_mask:0xf bank_mask:0xf\n\t"
      "s_waitcnt lgkmcnt(0)\n\t"
      "v_mfma_f32_16x16x32_bf16 v[76:79], v[40:43], v[32:35], 0\n\t"
      "buffer_load_dwordx4 v127, s[36:39], s46 offen sc1 lds\n\t"
      "s_lshl_b32 s61, s61, 23\n\t"
      "v_mfma_f32_16x16x32_bf16 v[76:79], v[44:47], v[36:39], v[76:79]\n\t"
      "s_add_u32 s65, s61, 33554432\n\t"
      "v_mov_b32_dpp v104, v36 quad_perm:[2,3,0,1] row_mask:0xf bank_mask:0xf\n\t"
      "v_mov_b32_dpp v103, v35 quad_perm:[2,3,0,1] row_mask:0xf bank_mask:0xf\n\t"
      "v_mov_b32_dpp v101, v33 quad_perm:[2,3,0,1] row_mask:0xf bank_mask:0xf\n\t"
      "v_mfma_f32_16x16x32_bf16 v[112:115], v[84:87], v[36:39], v[112:115]\n\t"
      "s_pack_hh_b32_b16 s75, s65, s65\n\t"
      "v_add_f32 v49, v49, v51\n\t"
      "v_cvt_pk_bf16_f32 v64, v48, v49\n\t"
      "v_cndmask_b32_e64 v120, v76, v78, s[72:73]\n\t"
      "v_cndmask_b32_e64 v121, v77, v79, s[72:73]\n\t"
      "v_mov_b32_dpp v105, v37 quad_perm:[2,3,0,1] row_mask:0xf bank_mask:0xf\n\t"
      "v_mfma_f32_16x16x32_bf16 v[112:115], v[92:95], v[100:103], v[112:115]\n\t"
      "buffer_load_dwordx4 v127, s[36:39], s46 offen offset:1024 sc1 lds\n\t"
      "v_mfma_f32_16x16x32_bf16 v[116:119], v[96:99], v[100:103], v[116:119]\n\t"
      "s_pack_hh_b32_b16 s74, s65, s65\n\t"
      "v_add_f32_dpp v121, v121, v121 quad_perm:[2,3,0,1] row_mask:0xf bank_mask:0xf\n\t"
      "v_add_f32_dpp v120, v120, v120 quad_perm:[2,3,0,1] row_mask:0xf bank_mask:0xf\n\t"
      "v_mfma_f32_16x16x32_bf16 v[112:115], v[56:59], v[104:107], v[112:115]\n\t"
      "v_exp_f32 v81, v121\n\t"
      "v_mfma_f32_16x16x32_bf16 v[116:119], v[60:63], v[104:107], v[116:119]\n\t"
      "s_mov_b32 m0, s45\n\t"
      "v_mov_b64 v[90:91], s[74:75]\n\t"
      "v_exp_f32 v80, v120\n\t"
      "v_mov_b64 v[88:89], s[74:75]\n\t"
      "v_cvt_pk_bf16_f32 v108, v80, v81\n\t"
      "v_smfmac_f32_16x16x64_bf16 v[52:55], v[4:7], v[200:207], v122\n\t"
      "buffer_load_dwordx4 v65, s[36:39], s44 offen offset:1024 sc1 lds\n\t"
      "v_mfma_f32_16x16x32_bf16 v[84:87], v[108:111], v[64:67], 0\n\t"
      "s_add_u32 s57, s61, 310378496\n\t"
      "v_cndmask_b32_e64 v112, v112, v116, s[72:73]\n\t"
      "v_exp_f32_e64 v56, v112\n\t"
      "v_mfma_f32_16x16x32_bf16 v[48:51], v[108:111], v[88:91], 0\n\t"
      "s_add_u32 s43, s43, 0x8000\n\t"
      "v_cndmask_b32_e64 v113, v113, v117, s[72:73]\n\t"
      "v_cndmask_b32_e64 v114, v114, v118, s[72:73]\n\t"
      "v_exp_f32_e64 v58, v114\n\t"
      "v_cndmask_b32_e64 v115, v115, v119, s[72:73]\n\t"
      "v_exp_f32 v57, v113\n\t"
      "v_exp_f32 v59, v115\n\t"
      "v_cvt_pk_bf16_f32 v96, v56, v57\n\t"
      "v_cvt_pk_bf16_f32 v97, v58, v59\n\t"
      "v_max3_f32 v82, |v112|, |v113|, |v114|\n\t"
      "v_mov_b32_dpp v98, v96 quad_perm:[2,3,0,1] row_mask:0xf bank_mask:0xf\n\t"
      "v_mov_b32_dpp v99, v97 quad_perm:[2,3,0,1] row_mask:0xf bank_mask:0xf\n\t"
      "v_smfmac_f32_16x16x64_bf16 v[52:55], v[0:3], v[192:199], v122\n\t"
      "v_max3_f32 v83, |v115|, |v120|, |v121|\n\t"
      "v_mfma_f32_16x16x32_bf16 v[48:51], v[96:99], v[88:91], v[48:51]\n\t"
      "v_max_f32_e64 v82, v82, v83\n\t"
      "v_cmp_nge_f32 vcc, 0x42000000, v82\n\t"
      "v_mfma_f32_16x16x32_bf16 v[84:87], v[96:99], v[68:71], v[84:87]\n\t"
      "buffer_load_dwordx4 v65, s[36:39], s44 offen sc1 lds\n\t"
      "v_smfmac_f32_16x16x64_bf16 v[52:55], v[28:31], v[248:255], v122\n\t"
      "s_add_u32 s68, s68, 0x8000\n\t"
      "s_cbranch_vccnz LMX_3%=\n\t"
      "v_rcp_f32 v49, v49\n\t"
      "v_rcp_f32_e64 v48, v48\n\t"
      "v_smfmac_f32_16x16x64_bf16 v[52:55], v[24:27], v[240:247], v122\n\t"
      "v_fma_f32 v85, v85, v49, s96\n\t"
      "v_fma_f32 v84, v84, v48, s96\n\t"
      "v_smfmac_f32_16x16x64_bf16 v[52:55], v[20:23], v[232:239], v122\n\t"
      "buffer_atomic_add v84, v123, s[36:39], s97 offen\n\t"
      "buffer_atomic_add v85, v123, s[36:39], s98 offen\n\t"
      "v_smfmac_f32_16x16x64_bf16 v[52:55], v[16:19], v[224:231], v122\n\t"
      "v_mfma_f32_16x16x32_bf16 v[92:95], v[96:99], v[72:75], 0\n\t"
      "s_nop 3\n\t"
      "v_add_f32 v53, v53, v55\n\t"
      "v_add_f32_e64 v52, v52, v54\n\t"
      "v_cvt_pk_bf16_f32 v66, v52, v53\n\t"
      "s_add_u32 s62, s61, 0x83400000\n\t"
      "v_mov_b32_e64 v126, s62\n\t"
      "v_mfma_f32_16x16x32_bf16 v[92:95], v[108:111], v[66:69], v[92:95]\n\t"
      "s_add_u32 s97, s97, 0x8000\n\t"
      "s_nop 6\n\t"
      "v_fma_f32 v95, v95, v49, s96\n\t"
      "buffer_atomic_add v95, v123, s[36:39], s23 offen\n\t"
      "v_fma_f32 v94, v94, v48, s96\n\t"
      "buffer_atomic_add v94, v123, s[36:39], s69 offen\n\t"
      "LWIN_3%=:\n\t"
      "s_memtime s[76:77]\n\t"
      "s_memrealtime s[80:81]\n\t"
      "v_add_u32 v100, s55, v127\n\t"
      "ds_read_b128 v[60:63], v100 offset:57344\n\t"
      "s_cmp_eq_u32 s56, 0\n\t"
      "s_cbranch_scc0 LSLOW_3%=\n\t"
      "LWIN1_3%=:\n\t"
      "buffer_load_dwordx2 v[120:121], off, s[36:39], s50 sc1\n\t"
      "s_mov_b32 s64, s50\n\t"
      "s_bitcmp1_b64 s[84:85], s40\n\t"
      "s_cbranch_scc1 LEARLY_3%=\n\t"
      "LROLE_3%=:\n\t"
      "v_add_u32 v52, 67584, v127\n\t"
      "ds_read_b128 v[88:91], v52 offset:16384\n\t"
      "ds_read_b128 v[92:95], v52 offset:17408\n\t"
      "ds_read_b128 v[32:35], v52 offset:20480\n\t"
      "ds_read_b128 v[36:39], v52 offset:21504\n\t"
      "ds_read_b128 v[40:43], v52 offset:24576\n\t"
      "ds_read_b128 v[44:47], v52 offset:25600\n\t"
      "v_mov_b64 v[48:49], 0\n\t"
      "v_mov_b64 v[50:51], 0\n\t"
      "v_add_u32 v104, s71, v100\n\t"
      "ds_read_b128 v[76:79], v104\n\t"
      "ds_read_b128 v[80:83], v104 offset:1024\n\t"
      "ds_read_b128 v[112:115], v100\n\t"
      "ds_read_b128 v[116:119], v100 offset:1024\n\t"
      "v_xor_b32 v101, 2048, v104\n\t"
      "v_xor_b32 v102, 4096, v104\n\t"
      "v_xor_b32 v103, 6144, v104\n\t"
      "s_waitcnt lgkmcnt(8)\n\t"
      "v_smfmac_f32_16x16x64_bf16 v[48:51], v[16:19], v[88:95], v122\n\t"
      "ds_read_b128 v[88:91], v52 offset:28672\n\t"
      "ds_read_b128 v[92:95], v52 offset:29696\n\t"
      "s_add_u32 m0, s55, s71\n\t"
      "s_sub_u32 s71, 204800, s71\n\t"
      "s_waitcnt lgkmcnt(8)\n\t"
      "v_smfmac_f32_16x16x64_bf16 v[48:51], v[20:23], v[32:39], v122\n\t"
      "ds_read_b128 v[0:3], v101\n\t"
      "ds_read_b128 v[4:7], v101 offset:1024\n\t"
      "s_add_u32 s40, s40, 1\n\t"
      "s_cmp_lt_u32 s40, 63\n\t"
      "s_waitcnt lgkmcnt(8)\n\t"
      "v_smfmac_f32_16x16x64_bf16 v[48:51], v[24:27], v[40:47], v122\n\t"
      "ds_read_b128 v[8:11], v100 offset:8192\n\t"
      "ds_read_b128 v[12:15], v100 offset:9216\n\t"
      "s_cselect_b32 s61, 786432, 0\n\t"
      "s_cselect_b32 s60, s58, 0\n\t"
      "s_waitcnt lgkmcnt(4)\n\t"
      "v_smfmac_f32_16x16x64_bf16 v[48:51], v[28:31], v[88:95], v122\n\t"
      "s_add_u32 s44, s44, s61\n\t"
      "s_cmp_lt_u32 s40, 62\n\t"
      "ds_read_b128 v[16:19], v102\n\t"
      "ds_read_b128 v[20:23], v102 offset:1024\n\t"
      "ds_read_b128 v[24:27], v100 offset:16384\n\t"
      "ds_read_b128 v[28:31], v100 offset:17408\n\t"
      "s_cselect_b32 s61, 786432, 0\n\t"
      "s_add_u32 s46, s46, s61\n\t"
      "s_add_u32 s61, s40, 3\n\t"
      "s_and_b32 s62, s61, 3\n\t"
      "s_cselect_b32 s62, 16, 2000\n\t"
      "s_cmp_lt_u32 s61, 64\n\t"
      "s_cselect_b32 s62, s62, 0\n\t"
      "s_add_u32 s50, s50, s62\n\t"
      "v_add_f32 v48, v48, v50\n\t"
      "v_add_f32 v49, v49, v51\n\t"
      "ds_write_b64 v124, v[48:49] offset:53832\n\t"
      "v_mfma_f32_16x16x32_bf16 v[32:35], v[76:79], v[112:115], 0\n\t"
      "s_sub_u32 s61, s88, s78\n\t"
      "s_sub_i32 s61, s61, 560\n\t"
      "s_lshr_b32 s62, s25, 3\n\t"
      "v_mfma_f32_16x16x32_bf16 v[36:39], v[80:83], v[112:115], 0\n\t"
      "s_sub_u32 s25, s25, s62\n\t"
      "s_sub_i32 s25, s25, 24\n\t"
      "s_max_i32 s25, s25, 0\n\t"
      "v_mfma_f32_16x16x32_bf16 v[40:43], v[76:79], v[116:119], 0\n\t"
      "s_sub_i32 s62, s61, 96\n\t"
      "s_max_i32 s62, s62, 0\n\t"
      "s_min_i32 s62, s62, 320\n\t"
      "v_mfma_f32_16x16x32_bf16 v[44:47], v[80:83], v[116:119], 0\n\t"
      "s_lshr_b32 s62, s62, 2\n\t"
      "s_add_u32 s25, s25, s62\n\t"
      "s_min_i32 s61, s61, 96\n\t"
      "ds_read_b128 v[76:79], v103\n\t"
      "ds_read_b128 v[80:83], v103 offset:1024\n\t"
      "ds_read_b128 v[112:115], v100 offset:24576\n\t"
      "ds_read_b128 v[116:119], v100 offset:25600\n\t"
      "s_max_i32 s61, s61, -96\n\t"
      "s_ashr_i32 s61, s61, 3\n\t"
      "s_add_u32 s26, s26, s61\n\t"
      "s_max_u32 s26, s26, 1500\n\t"
      "s_min_u32 s26, s26, 2300\n\t"
      "s_sub_u32 s62, s88, s27\n\t"
      "s_mov_b32 s27, s88\n\t"
      "s_sub_i32 s62, s62, s24\n\t"
      "s_cmp_gt_i32 s62, 1150\n\t"
      "s_cselect_b32 s62, 200, 0\n\t"
      "s_max_u32 s25, s25, s62\n\t"
      "s_sub_u32 s61, s76, s28\n\t"
      "s_sub_u32 s62, s80, s82\n\t"
      "s_mov_b32 s28, s76\n\t"
      "s_mov_b32 s82, s80\n\t"
      "s_lshl_b32 s61, s61, 8\n\t"
      "s_mul_i32 s62, s62, s83\n\t"
      "s_sub_i32 s61, s61, s62\n\t"
      "s_ashr_i32 s61, s61, 10\n\t"
      "s_min_i32 s61, s61, 100\n\t"
      "s_max_i32 s61, s61, -100\n\t"
      "s_add_u32 s83, s83, s61\n\t"
      "s_mul_i32 s24, s26, s83\n\t"
      "s_lshr_b32 s24, s24, 12\n\t"
      "s_add_u32 s24, s24, s25\n\t"
      "s_min_u32 s61, s30, 300\n\t"
      "s_sub_u32 s30, s30, s61\n\t"
      "s_sub_u32 s24, s24, s61\n\t"
      "s_mov_b32 s63, 0x2000\n\t"
      "LGATE_3%=:\n\t"
      "s_memtime s[92:93]\n\t"
      "s_waitcnt lgkmcnt(0)\n\t"
      "s_sub_u32 s61, s92, s88\n\t"
      "s_sub_i32 s61, s24, s61\n\t"
      "s_sub_i32 s61, s61, 70\n\t"
      "s_min_i32 s61, s61, 3000\n\t"
      "s_getpc_b64 s[92:93]\n\t"
      "LDPC_3%=:\n\t"
      "s_max_i32 s61, s61, 0\n\t"
      "s_andn2_b32 s62, 28, s61\n\t"
      "s_add_u32 s62, s62, LDSL_3%=-LDPC_3%=\n\t"
      "s_add_u32 s92, s92, s62\n\t"
      "s_addc_u32 s93, s93, 0\n\t"
      "LDLY_3%=:\n\t"
      "s_sub_i32 s61, s61, 32\n\t"
      "s_cmp_ge_i32 s61, 0\n\t"
      "s_cbranch_scc1 LDLY_3%=\n\t"
      "s_setpc_b64 s[92:93]\n\t"
      "LDSL_3%=:\n\t"
      "s_nop 0\n\t"
      "s_nop 0\n\t"
      "s_nop 0\n\t"
      "s_nop 0\n\t"
      "s_nop 0\n\t"
      "s_nop 0\n\t"
      "s_nop 0\n\t"
      "LBAR_3%=:\n\t"
      "s_cmp_gt_u32 s40, 1\n\t"
      "s_barrier\n\t"
      "s_cbranch_scc1 LPOLL_3%=\n\t"
      "s_branch LFIRST_3%=\n\t"
      "LUTOK_0%=:\n\t"
      "v_add_u32 v104, s71, v100\n\t"
      "ds_read_b128 v[76:79], v104\n\t"
      "ds_read_b128 v[80:83], v104 offset:1024\n\t"
      "ds_read_b128 v[112:115], v100\n\t"
      "ds_read_b128 v[116:119], v100 offset:1024\n\t"
      "v_xor_b32 v101, 2048, v104\n\t"
      "v_xor_b32 v102, 4096, v104\n\t"
      "v_xor_b32 v103, 6144, v104\n\t"
      "ds_read_b128 v[16:19], v101\n\t"
      "ds_read_b128 v[20:23], v101 offset:1024\n\t"
      "ds_read_b128 v[24:27], v100 offset:8192\n\t"
      "ds_read_b128 v[28:31], v100 offset:9216\n\t"
      "ds_read_b128 v[0:3], v102\n\t"
      "ds_read_b128 v[4:7], v102 offset:1024\n\t"
      "ds_read_b128 v[8:11], v100 offset:16384\n\t"
      "ds_read_b128 v[12:15], v100 offset:17408\n\t"
      "s_mov_b32 s28, s52\n\t"
      "s_add_u32 m0, s55, s71\n\t"
      "s_sub_u32 s71, 204800, s71\n\t"
      "s_add_u32 s40, s40, 1\n\t"
      "s_cmp_lt_u32 s40, 63\n\t"
      "s_cselect_b32 s61, 786432, 0\n\t"
      "s_cselect_b32 s60, s58, 0\n\t"
      "s_add_u32 s44, s44, s61\n\t"
      "s_cmp_lt_u32 s40, 62\n\t"
      "s_cselect_b32 s61, 786432, 0\n\t"
      "s_add_u32 s46, s46, s61\n\t"
      "s_add_u32 s61, s40, 3\n\t"
      "s_and_b32 s62, s61, 3\n\t"
      "s_cselect_b32 s62, 16, 2000\n\t"
      "s_cmp_lt_u32 s61, 64\n\t"
      "s_cselect_b32 s62, s62, 0\n\t"
      "s_add_u32 s50, s50, s62\n\t"
      "s_mov_b32 s63, 0x2000\n\t"
      "s_waitcnt lgkmcnt(8)\n\t"
      "v_mfma_f32_16x16x32_bf16 v[32:35], v[76:79], v[112:115], 0\n\t"
      "v_mfma_f32_16x16x32_bf16 v[36:39], v[80:83], v[112:115], 0\n\t"
      "v_mfma_f32_16x16x32_bf16 v[40:43], v[76:79], v[116:119], 0\n\t"
      "v_mfma_f32_16x16x32_bf16 v[44:47], v[80:83], v[116:119], 0\n\t"
      "ds_read_b128 v[76:79], v103\n\t"
      "ds_read_b128 v[80:83], v103 offset:1024\n\t"
      "ds_read_b128 v[112:115], v100 offset:24576\n\t"
      "ds_read_b128 v[116:119], v100 offset:25600\n\t"
      "s_waitcnt lgkmcnt(8)\n\t"
      "v_mfma_f32_16x16x32_bf16 v[32:35], v[16:19], v[24:27], v[32:35]\n\t"
      "v_mfma_f32_16x16x32_bf16 v[36:39], v[20:23], v[24:27], v[36:39]\n\t"
      "v_mfma_f32_16x16x32_bf16 v[40:43], v[16:19], v[28:31], v[40:43]\n\t"
      "v_mfma_f32_16x16x32_bf16 v[44:47], v[20:23], v[28:31], v[44:47]\n\t"
      "s_branch LGATE_0%=\n\t"
      "LFIRST_0%=:\n\t"
      "s_cmp_eq_u32 s40, 0\n\t"
      "s_cbranch_scc1 LWM0_0%=\n\t"
      "s_barrier\n\t"
      "ds_read_b32 v99, v109 offset:55400\n\t"
      "s_waitcnt lgkmcnt(0)\n\t"
      "v_readfirstlane_b32 s61, v99\n\t"
      "s_mov_b32 s63, 0x2000\n\t"
      "s_cmp_gt_i32 s61, 0\n\t"
      "s_cselect_b32 s99, 0x2000, 0\n\t"
      "s_xor_b32 s97, s97, s99\n\t"
      "s_branch LPOLL_0%=\n\t"
      "LWM0_0%=:\n\t"
      "s_mov_b32 s63, 0x10000\n\t"
      "LWM_0%=:\n\t"
      "s_waitcnt vmcnt(0) lgkmcnt(0)\n\t"
      "v_bitop3_b32 v49, v49, v51, s53 bitop3:0x7e\n\t"
      "v_max_u32 v48, v48, v50\n\t"
      "v_cmp_ne_u32 vcc, 0, v49\n\t"
      "s_nop 1\n\t"
      "v_max_u32_dpp v48, v48, v48 quad_perm:[1,0,3,2] row_mask:0xf bank_mask:0xf\n\t"
      "s_nop 1\n\t"
      "v_max_u32_dpp v48, v48, v48 quad_perm:[2,3,0,1] row_mask:0xf bank_mask:0xf\n\t"
      "s_nop 1\n\t"
      "v_max_u32_dpp v48, v48, v48 row_ror:4 row_mask:0xf bank_mask:0xf\n\t"
      "s_nop 1\n\t"
      "v_max_u32_dpp v48, v48, v48 row_ror:8 row_mask:0xf bank_mask:0xf\n\t"
      "v_mov_b32 v49, v48\n\t"
      "s_nop 1\n\t"
      "v_permlane16_swap_b32 v48, v49\n\t"
      "v_max_u32 v48, v48, v49\n\t"
      "v_mov_b32 v49, v48\n\t"
      "s_nop 1\n\t"
      "v_permlane32_swap_b32 v48, v49\n\t"
      "v_max_u32 v48, v48, v49\n\t"
      "v_mul_f32 v49, 0x40a00000, v48\n\t"
      "s_nop 0\n\t"
      "v_readfirstlane_b32 s67, v49\n\t"
      "v_mul_f32 v49, 0x43030000, v49\n\t"
      "s_nop 0\n\t"
      "v_readfirstlane_b32 s66, v49\n\t"
      "s_cbranch_vccz LWM1_0%=\n\t"
      "s_sleep 4\n\t"
      "buffer_load_dwordx2 v[48:49], v52, s[36:39], s94 offen sc1\n\t"
      "buffer_load_dwordx2 v[50:51], v52, s[36:39], s94 offen offset:128 sc1\n\t"
      "s_sub_u32 s63, s63, 1\n\t"
      "s_cmp_lg_u32 s63, 0\n\t"
      "s_cbranch_scc1 LWM_0%=\n\t"
      "s_waitcnt vmcnt(0)\n\t"
      "LWM1_0%=:\n\t"
      "buffer_load_dwordx2 v[120:121], off, s[36:39], s64 sc1\n\t"
      "s_lshr_b32 s67, s67, 23\n\t"
      "s_add_u32 s67, s67, 5\n\t"
      "s_lshr_b32 s66, s66, 23\n\t"
      "s_add_u32 s66, s66, 129\n\t"
      "s_max_u32 s66, s66, 80\n\t"
      "s_mov_b32 s63, 0x2000\n\t"
      "LPOL0_0%=:\n\t"
      "s_waitcnt lgkmcnt(4)\n\t"
      "v_mfma_f32_16x16x32_bf16 v[32:35], v[0:3], v[8:11], v[32:35]\n\t"
      "v_mfma_f32_16x16x32_bf16 v[36:39], v[4:7], v[8:11], v[36:39]\n\t"
      "v_mfma_f32_16x16x32_bf16 v[40:43], v[0:3], v[12:15], v[40:43]\n\t"
      "v_mfma_f32_16x16x32_bf16 v[44:47], v[4:7], v[12:15], v[44:47]\n\t"
      "v_mov_b64 v[48:49], 0\n\t"
      "v_mov_b64 v[50:51], 0\n\t"
      "v_mov_b64 v[52:53], 0\n\t"
      "v_mov_b64 v[54:55], 0\n\t"
      "s_xor_b32 s69, s97, 1024\n\t"
      "s_xor_b32 s98, s97, 512\n\t"
      "s_xor_b32 s23, s97, 1536\n\t"
      "s_waitcnt lgkmcnt(0)\n\t"
      "v_mfma_f32_16x16x32_bf16 v[32:35], v[76:79], v[112:115], v[32:35]\n\t"
      "v_mfma_f32_16x16x32_bf16 v[36:39], v[80:83], v[112:115], v[36:39]\n\t"
      "v_mfma_f32_16x16x32_bf16 v[40:43], v[76:79], v[116:119], v[40:43]\n\t"
      "v_mfma_f32_16x16x32_bf16 v[44:47], v[80:83], v[116:119], v[44:47]\n\t"
      "s_mov_b64 exec, 1\n\t"
      "ds_write_b64 v109, v[110:111] offset:55392\n\t"
      "s_mov_b64 exec, -1\n\t"
      "s_nop 4\n\t"
      "v_cvt_pk_bf16_f32 v68, v32, v33\n\t"
      "v_cvt_pk_bf16_f32 v69, v34, v35\n\t"
      "v_cvt_pk_bf16_f32 v70, v36, v37\n\t"
      "v_cvt_pk_bf16_f32 v71, v38, v39\n\t"
      "v_cvt_pk_bf16_f32 v74, v40, v41\n\t"
      "v_cvt_pk_bf16_f32 v75, v42, v43\n\t"
      "v_cvt_pk_bf16_f32 v72, v44, v45\n\t"
      "v_cvt_pk_bf16_f32 v73, v46, v47\n\t"
      "v_mov_b64 v[32:33], 0\n\t"
      "v_mov_b64 v[34:35], 0\n\t"
      "v_mov_b64 v[36:37], 0\n\t"
      "v_mov_b64 v[38:39], 0\n\t"
      "v_mov_b64 v[40:41], 0\n\t"
      "v_mov_b64 v[42:43], 0\n\t"
      "v_mov_b64 v[44:45], 0\n\t"
      "v_mov_b64 v[46:47], 0\n\t"
      "s_waitcnt vmcnt(0)\n\t"
      "s_branch LGO_0%=\n\t"
      "LEARLY_0%=:\n\t"
      "s_cmp_eq_u32 s40, 54\n\t"
      "s_cbranch_scc1 LPW_0%=\n\t"
      "s_cmp_lt_i32 s40, 0\n\t"
      "s_waitcnt lgkmcnt(0)\n\t"
      "s_cbranch_scc1 LUTOK_0%=\n\t"
      "s_xor_b32 s43, s43, s99\n\t"
      "s_xor_b32 s68, s68, s99\n\t"
      "s_branch LROLE_0%=\n\t"
      "LPW_0%=:\n\t"
      "s_and_b32 s61, s51, 0x70c\n\t"
      "s_cmp_lg_u32 s61, 0x100\n\t"
      "s_cbranch_scc1 LROLE_0%=\n\t"
      "s_getpc_b64 s[92:93]\n\t"
      "LPX_0%=:\n\t"
      "s_add_u32 s92, s92, LCOLD%=-LPX_0%=+0\n\t"
      "s_addc_u32 s93, s93, (LCOLD%=-LPX_0%=+0)>>32\n\t"
      "v_lshlrev_b32 v99, 2, v127\n\t"
      "global_load_dword v98, v99, s[92:93]\n\t"
      "v_add_u32 v99, 4096, v99\n\t"
      "global_load_dword v98, v99, s[92:93]\n\t"
      "v_add_u32 v99, 4096, v99\n\t"
      "s_branch LROLE_0%=\n\t"
      "LSLOW_0%=:\n\t"
      "s_mov_b32 s63, 0x10000\n\t"
      "LSLOW1_0%=:\n\t"
      "buffer_load_dwordx2 v[92:93], off, s[36:39], s64 sc1\n\t"
      "s_waitcnt vmcnt(0)\n\t"
      "v_bitop3_b32 v92, v92, v93, s53 bitop3:0x7e\n\t"
      "s_nop 0\n\t"
      "v_readfirstlane_b32 s56, v92\n\t"
      "s_nop 1\n\t"
      "s_cmp_eq_u32 s56, 0\n\t"
      "s_cbranch_scc1 LSLOW2_0%=\n\t"
      "s_sleep 8\n\t"
      "s_sub_u32 s63, s63, 1\n\t"
      "s_cmp_lg_u32 s63, 0\n\t"
      "s_cbranch_scc1 LSLOW1_0%=\n\t"
      "LSLOW2_0%=:\n\t"
      "s_mov_b32 m0, s45\n\t"
      "s_sub_u32 s61, 204800, s71\n\t"
      "buffer_load_dwordx4 v65, s[36:39], s44 offen sc1 lds\n\t"
      "buffer_load_dwordx4 v65, s[36:39], s44 offen offset:1024 sc1 lds\n\t"
      "s_add_u32 m0, s55, s61\n\t"
      "s_nop 0\n\t"
      "buffer_load_dwordx4 v127, s[36:39], s46 offen sc1 lds\n\t"
      "buffer_load_dwordx4 v127, s[36:39], s46 offen offset:1024 sc1 lds\n\t"
      "s_branch LWIN1_0%=\n\t"
      "LMX_0%=:\n\t"
      "s_waitcnt lgkmcnt(0)\n\t"
      "s_nop 7\n\t"
      "v_smfmac_f32_16x16x64_bf16 v[52:55], v[20:23], v[232:239], v122\n\t"
      "v_smfmac_f32_16x16x64_bf16 v[52:55], v[24:27], v[240:247], v122\n\t"
      "v_smfmac_f32_16x16x64_bf16 v[52:55], v[28:31], v[248:255], v122\n\t"
      "s_nop 7\n\t"
      "v_add_f32 v53, v53, v55\n\t"
      "v_add_f32_e64 v52, v52, v54\n\t"
      "v_cvt_pk_bf16_f32 v66, v52, v53\n\t"
      "s_add_u32 s62, s61, 0x83400000\n\t"
      "v_mov_b32_e64 v126, s62\n\t"
      "v_max3_f32 v82, v112, v113, v114\n\t"
      "v_max_f32 v82, v82, v115\n\t"
      "s_nop 1\n\t"
      "v_max_f32_dpp v82, v82, v82 quad_perm:[2,3,0,1] row_mask:0xf bank_mask:0xf\n\t"
      "v_max3_f32 v83, v82, v120, v121\n\t"
      "v_max3_f32 v60, v82, v120, v121\n\t"
      "s_nop 1\n\t"
      "v_permlane16_swap_b32 v83, v60\n\t"
      "v_max_f32 v61, v83, v60\n\t"
      "v_max_f32 v60, v83, v60\n\t"
      "s_nop 1\n\t"
      "v_permlane32_swap_b32 v61, v60\n\t"
      "v_max_f32 v83, v61, v60\n\t"
      "v_sub_f32 v112, v112, v83\n\t"
      "v_sub_f32 v113, v113, v83\n\t"
      "v_sub_f32 v114, v114, v83\n\t"
      "v_sub_f32 v115, v115, v83\n\t"
      "v_sub_f32 v120, v120, v83\n\t"
      "v_sub_f32 v121, v121, v83\n\t"
      "v_exp_f32_e64 v56, v112\n\t"
      "v_exp_f32 v57, v113\n\t"
      "v_exp_f32_e64 v58, v114\n\t"
      "v_exp_f32 v59, v115\n\t"
      "v_exp_f32 v80, v120\n\t"
      "v_exp_f32 v81, v121\n\t"
      "v_cvt_pk_bf16_f32 v96, v56, v57\n\t"
      "v_cvt_pk_bf16_f32 v97, v58, v59\n\t"
      "v_cvt_pk_bf16_f32 v108, v80, v81\n\t"
      "v_mov_b32_dpp v98, v96 quad_perm:[2,3,0,1] row_mask:0xf bank_mask:0xf\n\t"
      "v_mov_b32_dpp v99, v97 quad_perm:[2,3,0,1] row_mask:0xf bank_mask:0xf\n\t"
      "v_mfma_f32_16x16x32_bf16 v[48:51], v[108:111], v[88:91], 0\n\t"
      "s_nop 0\n\t"
      "v_mfma_f32_16x16x32_bf16 v[48:51], v[96:99], v[88:91], v[48:51]\n\t"
      "v_mfma_f32_16x16x32_bf16 v[84:87], v[108:111], v[64:67], 0\n\t"
      "v_mfma_f32_16x16x32_bf16 v[84:87], v[96:99], v[68:71], v[84:87]\n\t"
      "v_mfma_f32_16x16x32_bf16 v[92:95], v[96:99], v[72:75], 0\n\t"
      "v_mfma_f32_16x16x32_bf16 v[92:95], v[108:111], v[66:69], v[92:95]\n\t"
      "s_nop 3\n\t"
      "v_rcp_f32_e64 v48, v48\n\t"
      "v_rcp_f32 v49, v49\n\t"
      "v_fma_f32 v84, v84, v48, s96\n\t"
      "buffer_atomic_add v84, v123, s[36:39], s97 offen\n\t"
      "v_fma_f32 v85, v85, v49, s96\n\t"
      "buffer_atomic_add v85, v123, s[36:39], s98 offen\n\t"
      "v_fma_f32 v94, v94, v48, s96\n\t"
      "buffer_atomic_add v94, v123, s[36:39], s69 offen\n\t"
      "v_fma_f32 v95, v95, v49, s96\n\t"
      "buffer_atomic_add v95, v123, s[36:39], s23 offen\n\t"
      "s_add_u32 s97, s97, 0x8000\n\t"
      "s_branch LWIN_0%=\n\t"
      "LR0_0%=:\n\t"
      "buffer_load_dwordx4 v[84:87], v60, s[36:39], s43 offen sc1\n\t"
      "buffer_load_dwordx4 v[88:91], v60, s[36:39], s43 offen offset:1024 sc1\n\t"
      "s_waitcnt vmcnt(2)\n\t"
      "buffer_load_dwordx4 v[92:95], v60, s[36:39], s68 offen sc1\n\t"
      "buffer_load_dwordx4 v[96:99], v60, s[36:39], s68 offen offset:1024 sc1\n\t"
      "s_sub_u32 s63, s63, 1\n\t"
      "s_mov_b64 exec, 1\n\t"
      "buffer_store_dword v109, off, s[36:39], s52 sc1\n\t"
      "s_mov_b64 exec, -1\n\t"
      "s_waitcnt vmcnt(3) lgkmcnt(0)\n\t"
      "v_bitop3_b32 v64, v84, v85, v86 bitop3:0x80\n\t"
      "v_bitop3_b32 v64, v64, v87, v88 bitop3:0x80\n\t"
      "v_bitop3_b32 v64, v64, v89, v90 bitop3:0x80\n\t"
      "v_bitop3_b32 v64, v64, v91, s58 bitop3:0x80\n\t"
      "v_cmp_eq_u32 vcc, 0, v64\n\t"
      "v_pk_fma_f32 v[4:5], v[84:85], s[56:57], v[126:127] op_sel:[0,1,0] op_sel_hi:[1,1,0]\n\t"
      "v_pk_fma_f32 v[6:7], v[86:87], s[56:57], v[126:127] op_sel:[0,1,0] op_sel_hi:[1,1,0]\n\t"
      "v_cvt_pk_bf16_f32 v0, v4, v5\n\t"
      "v_cvt_pk_bf16_f32 v1, v6, v7\n\t"
      "ds_write_b64 v127, v[0:1] offset:40960\n\t"
      "v_pk_fma_f32 v[8:9], v[88:89], s[56:57], v[126:127] op_sel:[0,1,0] op_sel_hi:[1,1,0]\n\t"
      "v_pk_fma_f32 v[10:11], v[90:91], s[56:57], v[126:127] op_sel:[0,1,0] op_sel_hi:[1,1,0]\n\t"
      "v_cvt_pk_bf16_f32 v2, v8, v9\n\t"
      "v_cvt_pk_bf16_f32 v3, v10, v11\n\t"
      "ds_write_b64 v127, v[2:3] offset:40968\n\t"
      "s_cmp_le_i32 s63, 0\n\t"
      "s_waitcnt lgkmcnt(0)\n\t"
      "s_cbranch_scc1 LRZ_0%=\n\t"
      "s_cbranch_vccnz LR0_0%=\n\t"
      "LRZ_0%=:\n\t"
      "v_mov_b64 v[32:33], 0\n\t"
      "v_mov_b64 v[34:35], 0\n\t"
      "v_mov_b64 v[36:37], 0\n\t"
      "v_mov_b64 v[38:39], 0\n\t"
      "v_mov_b64 v[40:41], 0\n\t"
      "v_mov_b64 v[42:43], 0\n\t"
      "v_mov_b64 v[44:45], 0\n\t"
      "v_mov_b64 v[46:47], 0\n\t"
      "v_mov_b64 v[48:49], 0\n\t"
      "v_mov_b64 v[50:51], 0\n\t"
      "v_mov_b64 v[52:53], 0\n\t"
      "v_mov_b64 v[54:55], 0\n\t"
      "v_mfma_f32_16x16x32_bf16 v[56:59], v[0:3], v[0:3], 0\n\t"
      "v_smfmac_f32_16x16x64_bf16 v[36:39], v[0:3], a[64:71], v122\n\t"
      "v_smfmac_f32_16x16x64_bf16 v[44:47], v[0:3], a[192:199], v122\n\t"
      "s_branch LC0_0%=\n\t"
      "LUTOK_1%=:\n\t"
      "v_add_u32 v104, s71, v100\n\t"
      "ds_read_b128 v[76:79], v104\n\t"
      "ds_read_b128 v[80:83], v104 offset:1024\n\t"
      "ds_read_b128 v[112:115], v100\n\t"
      "ds_read_b128 v[116:119], v100 offset:1024\n\t"
      "v_xor_b32 v101, 2048, v104\n\t"
      "v_xor_b32 v102, 4096, v104\n\t"
      "v_xor_b32 v103, 6144, v104\n\t"
      "ds_read_b128 v[16:19], v101\n\t"
      "ds_read_b128 v[20:23], v101 offset:1024\n\t"
      "ds_read_b128 v[24:27], v100 offset:8192\n\t"
      "ds_read_b128 v[28:31], v100 offset:9216\n\t"
      "ds_read_b128 v[0:3], v102\n\t"
      "ds_read_b128 v[4:7], v102 offset:1024\n\t"
      "ds_read_b128 v[8:11], v100 offset:16384\n\t"
      "ds_read_b128 v[12:15], v100 offset:17408\n\t"
      "s_mov_b32 s28, s52\n\t"
      "s_add_u32 m0, s55, s71\n\t"
      "s_sub_u32 s71, 204800, s71\n\t"
      "s_add_u32 s40, s40, 1\n\t"
      "s_cmp_lt_u32 s40, 63\n\t"
      "s_cselect_b32 s61, 786432, 0\n\t"
      "s_cselect_b32 s60, s58, 0\n\t"
      "s_add_u32 s44, s44, s61\n\t"
      "s_cmp_lt_u32 s40, 62\n\t"
      "s_cselect_b32 s61, 786432, 0\n\t"
      "s_add_u32 s46, s46, s61\n\t"
      "s_add_u32 s61, s40, 3\n\t"
      "s_and_b32 s62, s61, 3\n\t"
      "s_cselect_b32 s62, 16, 2000\n\t"
      "s_cmp_lt_u32 s61, 64\n\t"
      "s_cselect_b32 s62, s62, 0\n\t"
      "s_add_u32 s50, s50, s62\n\t"
      "s_mov_b32 s63, 0x2000\n\t"
      "s_waitcnt lgkmcnt(8)\n\t"
      "v_mfma_f32_16x16x32_bf16 v[32:35], v[76:79], v[112:115], 0\n\t"
      "v_mfma_f32_16x16x32_bf16 v[36:39], v[80:83], v[112:115], 0\n\t"
      "v_mfma_f32_16x16x32_bf16 v[40:43], v[76:79], v[116:119], 0\n\t"
      "v_mfma_f32_16x16x32_bf16 v[44:47], v[80:83], v[116:119], 0\n\t"
      "ds_read_b128 v[76:79], v103\n\t"
      "ds_read_b128 v[80:83], v103 offset:1024\n\t"
      "ds_read_b128 v[112:115], v100 offset:24576\n\t"
      "ds_read_b128 v[116:119], v100 offset:25600\n\t"
      "s_waitcnt lgkmcnt(8)\n\t"
      "v_mfma_f32_16x16x32_bf16 v[32:35], v[16:19], v[24:27], v[32:35]\n\t"
      "v_mfma_f32_16x16x32_bf16 v[36:39], v[20:23], v[24:27], v[36:39]\n\t"
      "v_mfma_f32_16x16x32_bf16 v[40:43], v[16:19], v[28:31], v[40:43]\n\t"
      "v_mfma_f32_16x16x32_bf16 v[44:47], v[20:23], v[28:31], v[44:47]\n\t"
      "s_branch LGATE_1%=\n\t"
      "LFIRST_1%=:\n\t"
      "s_cmp_eq_u32 s40, 0\n\t"
      "s_cbranch_scc1 LWM0_1%=\n\t"
      "s_barrier\n\t"
      "ds_read_b32 v99, v109 offset:55400\n\t"
      "s_waitcnt lgkmcnt(0)\n\t"
      "v_readfirstlane_b32 s61, v99\n\t"
      "s_mov_b32 s63, 0x2000\n\t"
      "s_cmp_gt_i32 s61, 0\n\t"
      "s_cselect_b32 s99, 0x2000, 0\n\t"
      "s_xor_b32 s97, s97, s99\n\t"
      "s_branch LPOLL_1%=\n\t"
      "LWM0_1%=:\n\t"
      "s_mov_b32 s63, 0x10000\n\t"
      "LWM_1%=:\n\t"
      "s_waitcnt vmcnt(0) lgkmcnt(0)\n\t"
      "v_bitop3_b32 v49, v49, v51, s53 bitop3:0x7e\n\t"
      "v_max_u32 v48, v48, v50\n\t"
      "v_cmp_ne_u32 vcc, 0, v49\n\t"
      "s_nop 1\n\t"
      "v_max_u32_dpp v48, v48, v48 quad_perm:[1,0,3,2] row_mask:0xf bank_mask:0xf\n\t"
      "s_nop 1\n\t"
      "v_max_u32_dpp v48, v48, v48 quad_perm:[2,3,0,1] row_mask:0xf bank_mask:0xf\n\t"
      "s_nop 1\n\t"
      "v_max_u32_dpp v48, v48, v48 row_ror:4 row_mask:0xf bank_mask:0xf\n\t"
      "s_nop 1\n\t"
      "v_max_u32_dpp v48, v48, v48 row_ror:8 row_mask:0xf bank_mask:0xf\n\t"
      "v_mov_b32 v49, v48\n\t"
      "s_nop 1\n\t"
      "v_permlane16_swap_b32 v48, v49\n\t"
      "v_max_u32 v48, v48, v49\n\t"
      "v_mov_b32 v49, v48\n\t"
      "s_nop 1\n\t"
      "v_permlane32_swap_b32 v48, v49\n\t"
      "v_max_u32 v48, v48, v49\n\t"
      "v_mul_f32 v49, 0x40a00000, v48\n\t"
      "s_nop 0\n\t"
      "v_readfirstlane_b32 s67, v49\n\t"
      "v_mul_f32 v49, 0x43030000, v49\n\t"
      "s_nop 0\n\t"
      "v_readfirstlane_b32 s66, v49\n\t"
      "s_cbranch_vccz LWM1_1%=\n\t"
      "s_sleep 4\n\t"
      "buffer_load_dwordx2 v[48:49], v52, s[36:39], s94 offen sc1\n\t"
      "buffer_load_dwordx2 v[50:51], v52, s[36:39], s94 offen offset:128 sc1\n\t"
      "s_sub_u32 s63, s63, 1\n\t"
      "s_cmp_lg_u32 s63, 0\n\t"
      "s_cbranch_scc1 LWM_1%=\n\t"
      "s_waitcnt vmcnt(0)\n\t"
      "LWM1_1%=:\n\t"
      "buffer_load_dwordx2 v[120:121], off, s[36:39], s64 sc1\n\t"
      "s_lshr_b32 s67, s67, 23\n\t"
      "s_add_u32 s67, s67, 5\n\t"
      "s_lshr_b32 s66, s66, 23\n\t"
      "s_add_u32 s66, s66, 129\n\t"
      "s_max_u32 s66, s66, 80\n\t"
      "s_mov_b32 s63, 0x2000\n\t"
      "LPOL0_1%=:\n\t"
      "s_waitcnt lgkmcnt(4)\n\t"
      "v_mfma_f32_16x16x32_bf16 v[32:35], v[0:3], v[8:11], v[32:35]\n\t"
      "v_mfma_f32_16x16x32_bf16 v[36:39], v[4:7], v[8:11], v[36:39]\n\t"
      "v_mfma_f32_16x16x32_bf16 v[40:43], v[0:3], v[12:15], v[40:43]\n\t"
      "v_mfma_f32_16x16x32_bf16 v[44:47], v[4:7], v[12:15], v[44:47]\n\t"
      "v_mov_b64 v[48:49], 0\n\t"
      "v_mov_b64 v[50:51], 0\n\t"
      "v_mov_b64 v[52:53], 0\n\t"
      "v_mov_b64 v[54:55], 0\n\t"
      "s_xor_b32 s69, s97, 1024\n\t"
      "s_xor_b32 s98, s97, 512\n\t"
      "s_xor_b32 s23, s97, 1536\n\t"
      "s_waitcnt lgkmcnt(0)\n\t"
      "v_mfma_f32_16x16x32_bf16 v[32:35], v[76:79], v[112:115], v[32:35]\n\t"
      "v_mfma_f32_16x16x32_bf16 v[36:39], v[80:83], v[112:115], v[36:39]\n\t"
      "v_mfma_f32_16x16x32_bf16 v[40:43], v[76:79], v[116:119], v[40:43]\n\t"
      "v_mfma_f32_16x16x32_bf16 v[44:47], v[80:83], v[116:119], v[44:47]\n\t"
      "s_mov_b64 exec, 1\n\t"
      "ds_write_b64 v109, v[110:111] offset:55392\n\t"
      "s_mov_b64 exec, -1\n\t"
      "s_nop 4\n\t"
      "v_cvt_pk_bf16_f32 v68, v32, v33\n\t"
      "v_cvt_pk_bf16_f32 v69, v34, v35\n\t"
      "v_cvt_pk_bf16_f32 v70, v36, v37\n\t"
      "v_cvt_pk_bf16_f32 v71, v38, v39\n\t"
      "v_cvt_pk_bf16_f32 v74, v40, v41\n\t"
      "v_cvt_pk_bf16_f32 v75, v42, v43\n\t"
      "v_cvt_pk_bf16_f32 v72, v44, v45\n\t"
      "v_cvt_pk_bf16_f32 v73, v46, v47\n\t"
      "v_mov_b64 v[32:33], 0\n\t"
      "v_mov_b64 v[34:35], 0\n\t"
      "v_mov_b64 v[36:37], 0\n\t"
      "v_mov_b64 v[38:39], 0\n\t"
      "v_mov_b64 v[40:41], 0\n\t"
      "v_mov_b64 v[42:43], 0\n\t"
      "v_mov_b64 v[44:45], 0\n\t"
      "v_mov_b64 v[46:47], 0\n\t"
      "s_waitcnt vmcnt(0)\n\t"
      "s_branch LGO_1%=\n\t"
      "LEARLY_1%=:\n\t"
      "s_cmp_eq_u32 s40, 54\n\t"
      "s_cbranch_scc1 LROLE_1%=\n\t"
      "s_cmp_lt_i32 s40, 0\n\t"
      "s_waitcnt lgkmcnt(0)\n\t"
      "s_cbranch_scc1 LUTOK_1%=\n\t"
      "s_xor_b32 s43, s43, s99\n\t"
      "s_xor_b32 s68, s68, s99\n\t"
      "s_branch LROLE_1%=\n\t"
      "LSLOW_1%=:\n\t"
      "s_mov_b32 s63, 0x10000\n\t"
      "LSLOW1_1%=:\n\t"
      "buffer_load_dwordx2 v[92:93], off, s[36:39], s64 sc1\n\t"
      "s_waitcnt vmcnt(0)\n\t"
      "v_bitop3_b32 v92, v92, v93, s53 bitop3:0x7e\n\t"
      "s_nop 0\n\t"
      "v_readfirstlane_b32 s56, v92\n\t"
      "s_nop 1\n\t"
      "s_cmp_eq_u32 s56, 0\n\t"
      "s_cbranch_scc1 LSLOW2_1%=\n\t"
      "s_sleep 8\n\t"
      "s_sub_u32 s63, s63, 1\n\t"
      "s_cmp_lg_u32 s63, 0\n\t"
      "s_cbranch_scc1 LSLOW1_1%=\n\t"
      "LSLOW2_1%=:\n\t"
      "s_mov_b32 m0, s45\n\t"
      "s_sub_u32 s61, 204800, s71\n\t"
      "buffer_load_dwordx4 v65, s[36:39], s44 offen sc1 lds\n\t"
      "buffer_load_dwordx4 v65, s[36:39], s44 offen offset:1024 sc1 lds\n\t"
      "s_add_u32 m0, s55, s61\n\t"
      "s_nop 0\n\t"
      "buffer_load_dwordx4 v127, s[36:39], s46 offen sc1 lds\n\t"
      "buffer_load_dwordx4 v127, s[36:39], s46 offen offset:1024 sc1 lds\n\t"
      "s_branch LWIN1_1%=\n\t"
      "LMX_1%=:\n\t"
      "s_waitcnt lgkmcnt(0)\n\t"
      "s_nop 7\n\t"
      "v_smfmac_f32_16x16x64_bf16 v[52:55], v[16:19], v[224:231], v122\n\t"
      "v_smfmac_f32_16x16x64_bf16 v[52:55], v[28:31], v[248:255], v122\n\t"
      "v_smfmac_f32_16x16x64_bf16 v[52:55], v[24:27], v[240:247], v122\n\t"
      "s_nop 7\n\t"
      "v_add_f32 v53, v53, v55\n\t"
      "v_add_f32_e64 v52, v52, v54\n\t"
      "v_cvt_pk_bf16_f32 v66, v52, v53\n\t"
      "s_add_u32 s62, s61, 0x83400000\n\t"
      "v_mov_b32_e64 v126, s62\n\t"
      "v_max3_f32 v82, v112, v113, v114\n\t"
      "v_max_f32 v82, v82, v115\n\t"
      "s_nop 1\n\t"
      "v_max_f32_dpp v82, v82, v82 quad_perm:[2,3,0,1] row_mask:0xf bank_mask:0xf\n\t"
      "v_max3_f32 v83, v82, v120, v121\n\t"
      "v_max3_f32 v60, v82, v120, v121\n\t"
      "s_nop 1\n\t"
      "v_permlane16_swap_b32 v83, v60\n\t"
      "v_max_f32 v61, v83, v60\n\t"
      "v_max_f32 v60, v83, v60\n\t"
      "s_nop 1\n\t"
      "v_permlane32_swap_b32 v61, v60\n\t"
      "v_max_f32 v83, v61, v60\n\t"
      "v_sub_f32 v112, v112, v83\n\t"
      "v_sub_f32 v113, v113, v83\n\t"
      "v_sub_f32 v114, v114, v83\n\t"
      "v_sub_f32 v115, v115, v83\n\t"
      "v_sub_f32 v120, v120, v83\n\t"
      "v_sub_f32 v121, v121, v83\n\t"
      "v_exp_f32_e64 v56, v112\n\t"
      "v_exp_f32 v57, v113\n\t"
      "v_exp_f32_e64 v58, v114\n\t"
      "v_exp_f32 v59, v115\n\t"
      "v_exp_f32 v80, v120\n\t"
      "v_exp_f32 v81, v121\n\t"
      "v_cvt_pk_bf16_f32 v96, v56, v57\n\t"
      "v_cvt_pk_bf16_f32 v97, v58, v59\n\t"
      "v_cvt_pk_bf16_f32 v108, v80, v81\n\t"
      "v_mov_b32_dpp v98, v96 quad_perm:[2,3,0,1] row_mask:0xf bank_mask:0xf\n\t"
      "v_mov_b32_dpp v99, v97 quad_perm:[2,3,0,1] row_mask:0xf bank_mask:0xf\n\t"
      "v_mfma_f32_16x16x32_bf16 v[48:51], v[108:111], v[88:91], 0\n\t"
      "s_nop 0\n\t"
      "v_mfma_f32_16x16x32_bf16 v[48:51], v[96:99], v[88:91], v[48:51]\n\t"
      "v_mfma_f32_16x16x32_bf16 v[84:87], v[108:111], v[64:67], 0\n\t"
      "v_mfma_f32_16x16x32_bf16 v[84:87], v[96:99], v[68:71], v[84:87]\n\t"
      "v_mfma_f32_16x16x32_bf16 v[92:95], v[96:99], v[72:75], 0\n\t"
      "v_mfma_f32_16x16x32_bf16 v[92:95], v[108:111], v[66:69], v[92:95]\n\t"
      "s_nop 3\n\t"
      "v_rcp_f32_e64 v48, v48\n\t"
      "v_rcp_f32 v49, v49\n\t"
      "v_fma_f32 v84, v84, v48, s96\n\t"
      "buffer_atomic_add v84, v123, s[36:39], s97 offen\n\t"
      "v_fma_f32 v85, v85, v49, s96\n\t"
      "buffer_atomic_add v85, v123, s[36:39], s98 offen\n\t"
      "v_fma_f32 v94, v94, v48, s96\n\t"
      "buffer_atomic_add v94, v123, s[36:39], s69 offen\n\t"
      "v_fma_f32 v95, v95, v49, s96\n\t"
      "buffer_atomic_add v95, v123, s[36:39], s23 offen\n\t"
      "s_add_u32 s97, s97, 0x8000\n\t"
      "s_branch LWIN_1%=\n\t"
      "LR0_1%=:\n\t"
      "buffer_load_dwordx4 v[84:87], v60, s[36:39], s43 offen sc1\n\t"
      "buffer_load_dwordx4 v[88:91], v60, s[36:39], s43 offen offset:1024 sc1\n\t"
      "s_waitcnt vmcnt(2)\n\t"
      "buffer_load_dwordx4 v[92:95], v60, s[36:39], s68 offen sc1\n\t"
      "buffer_load_dwordx4 v[96:99], v60, s[36:39], s68 offen offset:1024 sc1\n\t"
      "s_sub_u32 s63, s63, 1\n\t"
      "s_mov_b64 exec, 1\n\t"
      "buffer_store_dword v109, off, s[36:39], s52 sc1\n\t"
      "s_mov_b64 exec, -1\n\t"
      "s_waitcnt vmcnt(3) lgkmcnt(0)\n\t"
      "v_bitop3_b32 v64, v84, v85, v86 bitop3:0x80\n\t"
      "v_bitop3_b32 v64, v64, v87, v88 bitop3:0x80\n\t"
      "v_bitop3_b32 v64, v64, v89, v90 bitop3:0x80\n\t"
      "v_bitop3_b32 v64, v64, v91, s58 bitop3:0x80\n\t"
      "v_cmp_eq_u32 vcc, 0, v64\n\t"
      "v_pk_fma_f32 v[0:1], v[84:85], s[56:57], v[126:127] op_sel:[0,1,0] op_sel_hi:[1,1,0]\n\t"
      "v_pk_fma_f32 v[2:3], v[86:87], s[56:57], v[126:127] op_sel:[0,1,0] op_sel_hi:[1,1,0]\n\t"
      "v_cvt_pk_bf16_f32 v4, v0, v1\n\t"
      "v_cvt_pk_bf16_f32 v5, v2, v3\n\t"
      "ds_write_b64 v127, v[4:5] offset:41984\n\t"
      "v_pk_fma_f32 v[12:13], v[88:89], s[56:57], v[126:127] op_sel:[0,1,0] op_sel_hi:[1,1,0]\n\t"
      "v_pk_fma_f32 v[14:15], v[90:91], s[56:57], v[126:127] op_sel:[0,1,0] op_sel_hi:[1,1,0]\n\t"
      "v_cvt_pk_bf16_f32 v6, v12, v13\n\t"
      "v_cvt_pk_bf16_f32 v7, v14, v15\n\t"
      "ds_write_b64 v127, v[6:7] offset:41992\n\t"
      "s_cmp_le_i32 s63, 0\n\t"
      "s_waitcnt lgkmcnt(0)\n\t"
      "s_cbranch_scc1 LRZ_1%=\n\t"
      "s_cbranch_vccnz LR0_1%=\n\t"
      "LRZ_1%=:\n\t"
      "v_mov_b64 v[32:33], 0\n\t"
      "v_mov_b64 v[34:35], 0\n\t"
      "v_mov_b64 v[36:37], 0\n\t"
      "v_mov_b64 v[38:39], 0\n\t"
      "v_mov_b64 v[40:41], 0\n\t"
      "v_mov_b64 v[42:43], 0\n\t"
      "v_mov_b64 v[44:45], 0\n\t"
      "v_mov_b64 v[46:47], 0\n\t"
      "v_mov_b64 v[48:49], 0\n\t"
      "v_mov_b64 v[50:51], 0\n\t"
      "v_mov_b64 v[52:53], 0\n\t"
      "v_mov_b64 v[54:55], 0\n\t"
      "v_mfma_f32_16x16x32_bf16 v[56:59], v[4:7], v[4:7], 0\n\t"
      "v_smfmac_f32_16x16x64_bf16 v[36:39], v[4:7], a[72:79], v122\n\t"
      "v_smfmac_f32_16x16x64_bf16 v[44:47], v[4:7], a[200:207], v122\n\t"
      "s_branch LC0_1%=\n\t"
      "LUTOK_2%=:\n\t"
      "v_add_u32 v104, s71, v100\n\t"
      "ds_read_b128 v[76:79], v104\n\t"
      "ds_read_b128 v[80:83], v104 offset:1024\n\t"
      "ds_read_b128 v[112:115], v100\n\t"
      "ds_read_b128 v[116:119], v100 offset:1024\n\t"
      "v_xor_b32 v101, 2048, v104\n\t"
      "v_xor_b32 v102, 4096, v104\n\t"
      "v_xor_b32 v103, 6144, v104\n\t"
      "ds_read_b128 v[0:3], v101\n\t"
      "ds_read_b128 v[4:7], v101 offset:1024\n\t"
      "ds_read_b128 v[8:11], v100 offset:8192\n\t"
      "ds_read_b128 v[12:15], v100 offset:9216\n\t"
      "ds_read_b128 v[16:19], v102\n\t"
      "ds_read_b128 v[20:23], v102 offset:1024\n\t"
      "ds_read_b128 v[24:27], v100 offset:16384\n\t"
      "ds_read_b128 v[28:31], v100 offset:17408\n\t"
      "s_add_u32 m0, s55, s71\n\t"
      "s_sub_u32 s71, 204800, s71\n\t"
      "s_add_u32 s40, s40, 1\n\t"
      "s_cmp_lt_u32 s40, 63\n\t"
      "s_cselect_b32 s61, 786432, 0\n\t"
      "s_cselect_b32 s60, s58, 0\n\t"
      "s_add_u32 s44, s44, s61\n\t"
      "s_cmp_lt_u32 s40, 62\n\t"
      "s_cselect_b32 s61, 786432, 0\n\t"
      "s_add_u32 s46, s46, s61\n\t"
      "s_add_u32 s61, s40, 3\n\t"
      "s_and_b32 s62, s61, 3\n\t"
      "s_cselect_b32 s62, 16, 2000\n\t"
      "s_cmp_lt_u32 s61, 64\n\t"
      "s_cselect_b32 s62, s62, 0\n\t"
      "s_add_u32 s50, s50, s62\n\t"
      "s_mov_b32 s63, 0x2000\n\t"
      "s_waitcnt lgkmcnt(8)\n\t"
      "v_mfma_f32_16x16x32_bf16 v[32:35], v[76:79], v[112:115], 0\n\t"
      "v_mfma_f32_16x16x32_bf16 v[36:39], v[80:83], v[112:115], 0\n\t"
      "v_mfma_f32_16x16x32_bf16 v[40:43], v[76:79], v[116:119], 0\n\t"
      "v_mfma_f32_16x16x32_bf16 v[44:47], v[80:83], v[116:119], 0\n\t"
      "ds_read_b128 v[76:79], v103\n\t"
      "ds_read_b128 v[80:83], v103 offset:1024\n\t"
      "ds_read_b128 v[112:115], v100 offset:24576\n\t"
      "ds_read_b128 v[116:119], v100 offset:25600\n\t"
      "s_branch LGATE_2%=\n\t"
      "LFIRST_2%=:\n\t"
      "s_cmp_eq_u32 s40, 0\n\t"
      "s_cbranch_scc1 LWM0_2%=\n\t"
      "s_barrier\n\t"
      "ds_read_b32 v99, v109 offset:55400\n\t"
      "s_waitcnt lgkmcnt(0)\n\t"
      "v_readfirstlane_b32 s61, v99\n\t"
      "s_mov_b32 s63, 0x2000\n\t"
      "s_cmp_gt_i32 s61, 0\n\t"
      "s_cselect_b32 s99, 0x2000, 0\n\t"
      "s_xor_b32 s97, s97, s99\n\t"
      "s_branch LPOLL_2%=\n\t"
      "LWM0_2%=:\n\t"
      "s_mov_b32 s63, 0x10000\n\t"
      "LWM_2%=:\n\t"
      "s_waitcnt vmcnt(0) lgkmcnt(0)\n\t"
      "v_bitop3_b32 v49, v49, v51, s53 bitop3:0x7e\n\t"
      "v_max_u32 v48, v48, v50\n\t"
      "v_cmp_ne_u32 vcc, 0, v49\n\t"
      "s_nop 1\n\t"
      "v_max_u32_dpp v48, v48, v48 quad_perm:[1,0,3,2] row_mask:0xf bank_mask:0xf\n\t"
      "s_nop 1\n\t"
      "v_max_u32_dpp v48, v48, v48 quad_perm:[2,3,0,1] row_mask:0xf bank_mask:0xf\n\t"
      "s_nop 1\n\t"
      "v_max_u32_dpp v48, v48, v48 row_ror:4 row_mask:0xf bank_mask:0xf\n\t"
      "s_nop 1\n\t"
      "v_max_u32_dpp v48, v48, v48 row_ror:8 row_mask:0xf bank_mask:0xf\n\t"
      "v_mov_b32 v49, v48\n\t"
      "s_nop 1\n\t"
      "v_permlane16_swap_b32 v48, v49\n\t"
      "v_max_u32 v48, v48, v49\n\t"
      "v_mov_b32 v49, v48\n\t"
      "s_nop 1\n\t"
      "v_permlane32_swap_b32 v48, v49\n\t"
      "v_max_u32 v48, v48, v49\n\t"
      "v_mul_f32 v49, 0x40a00000, v48\n\t"
      "s_nop 0\n\t"
      "v_readfirstlane_b32 s67, v49\n\t"
      "v_mul_f32 v49, 0x43030000, v49\n\t"
      "s_nop 0\n\t"
      "v_readfirstlane_b32 s66, v49\n\t"
      "s_cbranch_vccz LWM1_2%=\n\t"
      "s_sleep 4\n\t"
      "buffer_load_dwordx2 v[48:49], v52, s[36:39], s94 offen sc1\n\t"
      "buffer_load_dwordx2 v[50:51], v52, s[36:39], s94 offen offset:128 sc1\n\t"
      "s_sub_u32 s63, s63, 1\n\t"
      "s_cmp_lg_u32 s63, 0\n\t"
      "s_cbranch_scc1 LWM_2%=\n\t"
      "s_waitcnt vmcnt(0)\n\t"
      "LWM1_2%=:\n\t"
      "buffer_load_dwordx2 v[120:121], off, s[36:39], s64 sc1\n\t"
      "s_lshr_b32 s67, s67, 23\n\t"
      "s_add_u32 s67, s67, 5\n\t"
      "s_lshr_b32 s66, s66, 23\n\t"
      "s_add_u32 s66, s66, 129\n\t"
      "s_max_u32 s66, s66, 80\n\t"
      "s_mov_b32 s63, 0x2000\n\t"
      "LPOL0_2%=:\n\t"
      "s_waitcnt lgkmcnt(8)\n\t"
      "v_mfma_f32_16x16x32_bf16 v[32:35], v[0:3], v[8:11], v[32:35]\n\t"
      "v_mfma_f32_16x16x32_bf16 v[36:39], v[4:7], v[8:11], v[36:39]\n\t"
      "v_mfma_f32_16x16x32_bf16 v[40:43], v[0:3], v[12:15], v[40:43]\n\t"
      "v_mfma_f32_16x16x32_bf16 v[44:47], v[4:7], v[12:15], v[44:47]\n\t"
      "v_mov_b64 v[48:49], 0\n\t"
      "v_mov_b64 v[50:51], 0\n\t"
      "v_mov_b64 v[52:53], 0\n\t"
      "v_mov_b64 v[54:55], 0\n\t"
      "s_xor_b32 s69, s97, 1024\n\t"
      "s_xor_b32 s98, s97, 512\n\t"
      "s_xor_b32 s23, s97, 1536\n\t"
      "s_waitcnt lgkmcnt(4)\n\t"
      "v_mfma_f32_16x16x32_bf16 v[32:35], v[16:19], v[24:27], v[32:35]\n\t"
      "v_mfma_f32_16x16x32_bf16 v[36:39], v[20:23], v[24:27], v[36:39]\n\t"
      "v_mfma_f32_16x16x32_bf16 v[40:43], v[16:19], v[28:31], v[40:43]\n\t"
      "v_mfma_f32_16x16x32_bf16 v[44:47], v[20:23], v[28:31], v[44:47]\n\t"
      "s_waitcnt lgkmcnt(0)\n\t"
      "v_mfma_f32_16x16x32_bf16 v[32:35], v[76:79], v[112:115], v[32:35]\n\t"
      "v_mfma_f32_16x16x32_bf16 v[36:39], v[80:83], v[112:115], v[36:39]\n\t"
      "v_mfma_f32_16x16x32_bf16 v[40:43], v[76:79], v[116:119], v[40:43]\n\t"
      "v_mfma_f32_16x16x32_bf16 v[44:47], v[80:83], v[116:119], v[44:47]\n\t"
      "s_mov_b64 exec, 1\n\t"
      "ds_write_b64 v109, v[110:111] offset:55392\n\t"
      "s_mov_b64 exec, -1\n\t"
      "s_nop 4\n\t"
      "v_cvt_pk_bf16_f32 v68, v32, v33\n\t"
      "v_cvt_pk_bf16_f32 v69, v34, v35\n\t"
      "v_cvt_pk_bf16_f32 v70, v36, v37\n\t"
      "v_cvt_pk_bf16_f32 v71, v38, v39\n\t"
      "v_cvt_pk_bf16_f32 v74, v40, v41\n\t"
      "v_cvt_pk_bf16_f32 v75, v42, v43\n\t"
      "v_cvt_pk_bf16_f32 v72, v44, v45\n\t"
      "v_cvt_pk_bf16_f32 v73, v46, v47\n\t"
      "v_mov_b64 v[32:33], 0\n\t"
      "v_mov_b64 v[34:35], 0\n\t"
      "v_mov_b64 v[36:37], 0\n\t"
      "v_mov_b64 v[38:39], 0\n\t"
      "v_mov_b64 v[40:41], 0\n\t"
      "v_mov_b64 v[42:43], 0\n\t"
      "v_mov_b64 v[44:45], 0\n\t"
      "v_mov_b64 v[46:47], 0\n\t"
      "s_waitcnt vmcnt(0)\n\t"
      "s_branch LGO_2%=\n\t"
      "LEARLY_2%=:\n\t"
      "s_cmp_eq_u32 s40, 54\n\t"
      "s_cbranch_scc1 LROLE_2%=\n\t"
      "s_cmp_lt_i32 s40, 0\n\t"
      "s_waitcnt lgkmcnt(0)\n\t"
      "s_cbranch_scc1 LUTOK_2%=\n\t"
      "s_xor_b32 s43, s43, s99\n\t"
      "s_xor_b32 s68, s68, s99\n\t"
      "s_branch LROLE_2%=\n\t"
      "LSLOW_2%=:\n\t"
      "s_mov_b32 s63, 0x10000\n\t"
      "LSLOW1_2%=:\n\t"
      "buffer_load_dwordx2 v[92:93], off, s[36:39], s64 sc1\n\t"
      "s_waitcnt vmcnt(0)\n\t"
      "v_bitop3_b32 v92, v92, v93, s53 bitop3:0x7e\n\t"
      "s_nop 0\n\t"
      "v_readfirstlane_b32 s56, v92\n\t"
      "s_nop 1\n\t"
      "s_cmp_eq_u32 s56, 0\n\t"
      "s_cbranch_scc1 LSLOW2_2%=\n\t"
      "s_sleep 8\n\t"
      "s_sub_u32 s63, s63, 1\n\t"
      "s_cmp_lg_u32 s63, 0\n\t"
      "s_cbranch_scc1 LSLOW1_2%=\n\t"
      "LSLOW2_2%=:\n\t"
      "s_mov_b32 m0, s45\n\t"
      "s_sub_u32 s61, 204800, s71\n\t"
      "buffer_load_dwordx4 v65, s[36:39], s44 offen sc1 lds\n\t"
      "buffer_load_dwordx4 v65, s[36:39], s44 offen offset:1024 sc1 lds\n\t"
      "s_add_u32 m0, s55, s61\n\t"
      "s_nop 0\n\t"
      "buffer_load_dwordx4 v127, s[36:39], s46 offen sc1 lds\n\t"
      "buffer_load_dwordx4 v127, s[36:39], s46 offen offset:1024 sc1 lds\n\t"
      "s_branch LWIN1_2%=\n\t"
      "LMX_2%=:\n\t"
      "s_waitcnt lgkmcnt(0)\n\t"
      "s_nop 7\n\t"
      "v_smfmac_f32_16x16x64_bf16 v[52:55], v[28:31], v[248:255], v122\n\t"
      "v_smfmac_f32_16x16x64_bf16 v[52:55], v[16:19], v[224:231], v122\n\t"
      "v_smfmac_f32_16x16x64_bf16 v[52:55], v[20:23], v[232:239], v122\n\t"
      "s_nop 7\n\t"
      "v_add_f32 v53, v53, v55\n\t"
      "v_add_f32_e64 v52, v52, v54\n\t"
      "v_cvt_pk_bf16_f32 v66, v52, v53\n\t"
      "s_add_u32 s62, s61, 0x83400000\n\t"
      "v_mov_b32_e64 v126, s62\n\t"
      "v_max3_f32 v82, v112, v113, v114\n\t"
      "v_max_f32 v82, v82, v115\n\t"
      "s_nop 1\n\t"
      "v_max_f32_dpp v82, v82, v82 quad_perm:[2,3,0,1] row_mask:0xf bank_mask:0xf\n\t"
      "v_max3_f32 v83, v82, v120, v121\n\t"
      "v_max3_f32 v60, v82, v120, v121\n\t"
      "s_nop 1\n\t"
      "v_permlane16_swap_b32 v83, v60\n\t"
      "v_max_f32 v61, v83, v60\n\t"
      "v_max_f32 v60, v83, v60\n\t"
      "s_nop 1\n\t"
      "v_permlane32_swap_b32 v61, v60\n\t"
      "v_max_f32 v83, v61, v60\n\t"
      "v_sub_f32 v112, v112, v83\n\t"
      "v_sub_f32 v113, v113, v83\n\t"
      "v_sub_f32 v114, v114, v83\n\t"
      "v_sub_f32 v115, v115, v83\n\t"
      "v_sub_f32 v120, v120, v83\n\t"
      "v_sub_f32 v121, v121, v83\n\t"
      "v_exp_f32_e64 v56, v112\n\t"
      "v_exp_f32 v57, v113\n\t"
      "v_exp_f32_e64 v58, v114\n\t"
      "v_exp_f32 v59, v115\n\t"
      "v_exp_f32 v80, v120\n\t"
      "v_exp_f32 v81, v121\n\t"
      "v_cvt_pk_bf16_f32 v96, v56, v57\n\t"
      "v_cvt_pk_bf16_f32 v97, v58, v59\n\t"
      "v_cvt_pk_bf16_f32 v108, v80, v81\n\t"
      "v_mov_b32_dpp v98, v96 quad_perm:[2,3,0,1] row_mask:0xf bank_mask:0xf\n\t"
      "v_mov_b32_dpp v99, v97 quad_perm:[2,3,0,1] row_mask:0xf bank_mask:0xf\n\t"
      "v_mfma_f32_16x16x32_bf16 v[48:51], v[108:111], v[88:91], 0\n\t"
      "s_nop 0\n\t"
      "v_mfma_f32_16x16x32_bf16 v[48:51], v[96:99], v[88:91], v[48:51]\n\t"
      "v_mfma_f32_16x16x32_bf16 v[84:87], v[108:111], v[64:67], 0\n\t"
      "v_mfma_f32_16x16x32_bf16 v[84:87], v[96:99], v[68:71], v[84:87]\n\t"
      "v_mfma_f32_16x16x32_bf16 v[92:95], v[96:99], v[72:75], 0\n\t"
      "v_mfma_f32_16x16x32_bf16 v[92:95], v[108:111], v[66:69], v[92:95]\n\t"
      "s_nop 3\n\t"
      "v_rcp_f32_e64 v48, v48\n\t"
      "v_rcp_f32 v49, v49\n\t"
      "v_fma_f32 v84, v84, v48, s96\n\t"
      "buffer_atomic_add v84, v123, s[36:39], s97 offen\n\t"
      "v_fma_f32 v85, v85, v49, s96\n\t"
      "buffer_atomic_add v85, v123, s[36:39], s98 offen\n\t"
      "v_fma_f32 v94, v94, v48, s96\n\t"
      "buffer_atomic_add v94, v123, s[36:39], s69 offen\n\t"
      "v_fma_f32 v95, v95, v49, s96\n\t"
      "buffer_atomic_add v95, v123, s[36:39], s23 offen\n\t"
      "s_add_u32 s97, s97, 0x8000\n\t"
      "s_branch LWIN_2%=\n\t"
      "LR0_2%=:\n\t"
      "buffer_load_dwordx4 v[84:87], v60, s[36:39], s43 offen sc1\n\t"
      "buffer_load_dwordx4 v[88:91], v60, s[36:39], s43 offen offset:1024 sc1\n\t"
      "s_waitcnt vmcnt(2)\n\t"
      "buffer_load_dwordx4 v[92:95], v60, s[36:39], s68 offen sc1\n\t"
      "buffer_load_dwordx4 v[96:99], v60, s[36:39], s68 offen offset:1024 sc1\n\t"
      "s_sub_u32 s63, s63, 1\n\t"
      "s_waitcnt vmcnt(2) lgkmcnt(0)\n\t"
      "v_bitop3_b32 v64, v84, v85, v86 bitop3:0x80\n\t"
      "v_bitop3_b32 v64, v64, v87, v88 bitop3:0x80\n\t"
      "v_bitop3_b32 v64, v64, v89, v90 bitop3:0x80\n\t"
      "v_bitop3_b32 v64, v64, v91, s58 bitop3:0x80\n\t"
      "v_cmp_eq_u32 vcc, 0, v64\n\t"
      "v_pk_fma_f32 v[12:13], v[84:85], s[56:57], v[126:127] op_sel:[0,1,0] op_sel_hi:[1,1,0]\n\t"
      "v_pk_fma_f32 v[14:15], v[86:87], s[56:57], v[126:127] op_sel:[0,1,0] op_sel_hi:[1,1,0]\n\t"
      "v_cvt_pk_bf16_f32 v8, v12, v13\n\t"
      "v_cvt_pk_bf16_f32 v9, v14, v15\n\t"
      "ds_write_b64 v127, v[8:9] offset:43008\n\t"
      "v_pk_fma_f32 v[0:1], v[88:89], s[56:57], v[126:127] op_sel:[0,1,0] op_sel_hi:[1,1,0]\n\t"
      "v_pk_fma_f32 v[2:3], v[90:91], s[56:57], v[126:127] op_sel:[0,1,0] op_sel_hi:[1,1,0]\n\t"
      "v_cvt_pk_bf16_f32 v10, v0, v1\n\t"
      "v_cvt_pk_bf16_f32 v11, v2, v3\n\t"
      "ds_write_b64 v127, v[10:11] offset:43016\n\t"
      "s_cmp_le_i32 s63, 0\n\t"
      "s_waitcnt lgkmcnt(0)\n\t"
      "s_cbranch_scc1 LRZ_2%=\n\t"
      "s_cbranch_vccnz LR0_2%=\n\t"
      "LRZ_2%=:\n\t"
      "v_mov_b64 v[32:33], 0\n\t"
      "v_mov_b64 v[34:35], 0\n\t"
      "v_mov_b64 v[36:37], 0\n\t"
      "v_mov_b64 v[38:39], 0\n\t"
      "v_mov_b64 v[40:41], 0\n\t"
      "v_mov_b64 v[42:43], 0\n\t"
      "v_mov_b64 v[44:45], 0\n\t"
      "v_mov_b64 v[46:47], 0\n\t"
      "v_mov_b64 v[48:49], 0\n\t"
      "v_mov_b64 v[50:51], 0\n\t"
      "v_mov_b64 v[52:53], 0\n\t"
      "v_mov_b64 v[54:55], 0\n\t"
      "v_mfma_f32_16x16x32_bf16 v[56:59], v[8:11], v[8:11], 0\n\t"
      "v_smfmac_f32_16x16x64_bf16 v[36:39], v[8:11], a[80:87], v122\n\t"
      "v_smfmac_f32_16x16x64_bf16 v[44:47], v[8:11], a[208:215], v122\n\t"
      "s_branch LC0_2%=\n\t"
      "LUTOK_3%=:\n\t"
      "v_add_u32 v104, s71, v100\n\t"
      "ds_read_b128 v[76:79], v104\n\t"
      "ds_read_b128 v[80:83], v104 offset:1024\n\t"
      "ds_read_b128 v[112:115], v100\n\t"
      "ds_read_b128 v[116:119], v100 offset:1024\n\t"
      "v_xor_b32 v101, 2048, v104\n\t"
      "v_xor_b32 v102, 4096, v104\n\t"
      "v_xor_b32 v103, 6144, v104\n\t"
      "ds_read_b128 v[0:3], v101\n\t"
      "ds_read_b128 v[4:7], v101 offset:1024\n\t"
      "ds_read_b128 v[8:11], v100 offset:8192\n\t"
      "ds_read_b128 v[12:15], v100 offset:9216\n\t"
      "ds_read_b128 v[16:19], v102\n\t"
      "ds_read_b128 v[20:23], v102 offset:1024\n\t"
      "ds_read_b128 v[24:27], v100 offset:16384\n\t"
      "ds_read_b128 v[28:31], v100 offset:17408\n\t"
      "s_sub_u32 s61, s88, s78\n\t"
      "s_sub_i32 s61, s61, 560\n\t"
      "s_lshr_b32 s62, s25, 3\n\t"
      "s_sub_u32 s25, s25, s62\n\t"
      "s_sub_i32 s25, s25, 24\n\t"
      "s_max_i32 s25, s25, 0\n\t"
      "s_sub_i32 s62, s61, 96\n\t"
      "s_max_i32 s62, s62, 0\n\t"
      "s_min_i32 s62, s62, 320\n\t"
      "s_lshr_b32 s62, s62, 2\n\t"
      "s_add_u32 s25, s25, s62\n\t"
      "s_min_i32 s61, s61, 96\n\t"
      "s_max_i32 s61, s61, -96\n\t"
      "s_ashr_i32 s61, s61, 3\n\t"
      "s_add_u32 s26, s26, s61\n\t"
      "s_max_u32 s26, s26, 1500\n\t"
      "s_min_u32 s26, s26, 2300\n\t"
      "s_sub_u32 s62, s88, s27\n\t"
      "s_mov_b32 s27, s88\n\t"
      "s_sub_i32 s62, s62, s24\n\t"
      "s_cmp_gt_i32 s62, 1150\n\t"
      "s_cselect_b32 s62, 200, 0\n\t"
      "s_max_u32 s25, s25, s62\n\t"
      "s_sub_u32 s61, s76, s28\n\t"
      "s_sub_u32 s62, s80, s82\n\t"
      "s_mov_b32 s28, s76\n\t"
      "s_mov_b32 s82, s80\n\t"
      "s_lshl_b32 s61, s61, 8\n\t"
      "s_mul_i32 s62, s62, s83\n\t"
      "s_sub_i32 s61, s61, s62\n\t"
      "s_ashr_i32 s61, s61, 10\n\t"
      "s_min_i32 s61, s61, 100\n\t"
      "s_max_i32 s61, s61, -100\n\t"
      "s_add_u32 s83, s83, s61\n\t"
      "s_mul_i32 s24, s26, s83\n\t"
      "s_lshr_b32 s24, s24, 12\n\t"
      "s_add_u32 s24, s24, s25\n\t"
      "s_min_u32 s61, s30, 300\n\t"
      "s_sub_u32 s30, s30, s61\n\t"
      "s_sub_u32 s24, s24, s61\n\t"
      "s_add_u32 m0, s55, s71\n\t"
      "s_sub_u32 s71, 204800, s71\n\t"
      "s_add_u32 s40, s40, 1\n\t"
      "s_cmp_lt_u32 s40, 63\n\t"
      "s_cselect_b32 s61, 786432, 0\n\t"
      "s_cselect_b32 s60, s58, 0\n\t"
      "s_add_u32 s44, s44, s61\n\t"
      "s_cmp_lt_u32 s40, 62\n\t"
      "s_cselect_b32 s61, 786432, 0\n\t"
      "s_add_u32 s46, s46, s61\n\t"
      "s_add_u32 s61, s40, 3\n\t"
      "s_and_b32 s62, s61, 3\n\t"
      "s_cselect_b32 s62, 16, 2000\n\t"
      "s_cmp_lt_u32 s61, 64\n\t"
      "s_cselect_b32 s62, s62, 0\n\t"
      "s_add_u32 s50, s50, s62\n\t"
      "s_mov_b32 s63, 0x2000\n\t"
      "s_waitcnt lgkmcnt(8)\n\t"
      "v_mfma_f32_16x16x32_bf16 v[32:35], v[76:79], v[112:115], 0\n\t"
      "v_mfma_f32_16x16x32_bf16 v[36:39], v[80:83], v[112:115], 0\n\t"
      "v_mfma_f32_16x16x32_bf16 v[40:43], v[76:79], v[116:119], 0\n\t"
      "v_mfma_f32_16x16x32_bf16 v[44:47], v[80:83], v[116:119], 0\n\t"
      "ds_read_b128 v[76:79], v103\n\t"
      "ds_read_b128 v[80:83], v103 offset:1024\n\t"
      "ds_read_b128 v[112:115], v100 offset:24576\n\t"
      "ds_read_b128 v[116:119], v100 offset:25600\n\t"
      "s_branch LGATE_3%=\n\t"
      "LFIRST_3%=:\n\t"
      "s_cmp_eq_u32 s40, 0\n\t"
      "s_cbranch_scc1 LWM0_3%=\n\t"
      "s_mov_b32 s63, 0x10000\n\t"
      "LST0_3%=:\n\t"
      "s_waitcnt vmcnt(0)\n\t"
      "v_bitop3_b32 v97, v57, v85, s53 bitop3:0x7e\n\t"
      "v_cmp_ne_u32 vcc, 0, v97\n\t"
      "v_add_u32 v99, v58, v86\n\t"
      "v_subrev_u32 v97, s87, v56\n\t"
      "v_subrev_u32 v98, s87, v84\n\t"
      "v_max_i32 v97, v97, v98\n\t"
      "s_cbranch_vccz LST3_3%=\n\t"
      "buffer_load_dwordx4 v[56:59], v96, s[36:39], s86 offen sc1\n\t"
      "buffer_load_dwordx4 v[84:87], v96, s[36:39], s86 offen offset:128 sc1\n\t"
      "s_sub_u32 s63, s63, 1\n\t"
      "s_cmp_eq_u32 s63, 0\n\t"
      "s_cbranch_scc0 LST0_3%=\n\t"
      "s_waitcnt vmcnt(0)\n\t"
      "LST3_3%=:\n\t"
      "v_max_u32 v98, v59, v87\n\t"
      "v_add_u32_dpp v99, v99, v99 quad_perm:[1,0,3,2] row_mask:0xf bank_mask:0xf\n\t"
      "v_max_i32_dpp v97, v97, v97 quad_perm:[1,0,3,2] row_mask:0xf bank_mask:0xf\n\t"
      "v_max_u32_dpp v98, v98, v98 quad_perm:[1,0,3,2] row_mask:0xf bank_mask:0xf\n\t"
      "v_add_u32_dpp v99, v99, v99 quad_perm:[2,3,0,1] row_mask:0xf bank_mask:0xf\n\t"
      "v_max_i32_dpp v97, v97, v97 quad_perm:[2,3,0,1] row_mask:0xf bank_mask:0xf\n\t"
      "v_max_u32_dpp v98, v98, v98 quad_perm:[2,3,0,1] row_mask:0xf bank_mask:0xf\n\t"
      "v_add_u32_dpp v99, v99, v99 row_ror:4 row_mask:0xf bank_mask:0xf\n\t"
      "v_max_i32_dpp v97, v97, v97 row_ror:4 row_mask:0xf bank_mask:0xf\n\t"
      "v_max_u32_dpp v98, v98, v98 row_ror:4 row_mask:0xf bank_mask:0xf\n\t"
      "v_add_u32_dpp v99, v99, v99 row_ror:8 row_mask:0xf bank_mask:0xf\n\t"
      "v_max_i32_dpp v97, v97, v97 row_ror:8 row_mask:0xf bank_mask:0xf\n\t"
      "v_max_u32_dpp v98, v98, v98 row_ror:8 row_mask:0xf bank_mask:0xf\n\t"
      "v_mov_b32 v57, v99\n\t"
      "v_mov_b32 v58, v97\n\t"
      "v_mov_b32 v85, v98\n\t"
      "v_permlane16_swap_b32 v99, v57\n\t"
      "v_permlane16_swap_b32 v97, v58\n\t"
      "v_permlane16_swap_b32 v98, v85\n\t"
      "v_add_u32 v99, v99, v57\n\t"
      "v_max_i32 v97, v97, v58\n\t"
      "v_max_u32 v98, v98, v85\n\t"
      "v_mov_b32 v57, v99\n\t"
      "v_mov_b32 v58, v97\n\t"
      "v_mov_b32 v85, v98\n\t"
      "v_permlane32_swap_b32 v99, v57\n\t"
      "v_permlane32_swap_b32 v97, v58\n\t"
      "v_permlane32_swap_b32 v98, v85\n\t"
      "v_add_u32 v99, v99, v57\n\t"
      "v_max_i32 v97, v97, v58\n\t"
      "v_max_u32 v98, v98, v85\n\t"
      "ds_write_b32 v109, v99 offset:55400\n\t"
      "v_rcp_f32 v98, v98\n\t"
      "v_readfirstlane_b32 s62, v97\n\t"
      "v_mul_f32 v98, 0x4719c000, v98\n\t"
      "v_cvt_u32_f32 v98, v98\n\t"
      "s_min_u32 s62, s62, 500\n\t"
      "s_add_u32 s62, s62, 212\n\t"
      "v_readfirstlane_b32 s26, v98\n\t"
      "s_mov_b32 s25, 0\n\t"
      "LST1_3%=:\n\t"
      "s_memrealtime s[92:93]\n\t"
      "s_waitcnt lgkmcnt(0)\n\t"
      "s_sub_u32 s61, s92, s87\n\t"
      "s_cmp_lt_u32 s61, s62\n\t"
      "s_cbranch_scc1 LST1_3%=\n\t"
      "s_sub_u32 s61, s61, s62\n\t"
      "s_mul_i32 s30, s61, 21\n\t"
      "s_barrier\n\t"
      "v_readfirstlane_b32 s61, v99\n\t"
      "s_mov_b32 s63, 0x2000\n\t"
      "s_cmp_gt_i32 s61, 0\n\t"
      "s_cselect_b32 s99, 0x2000, 0\n\t"
      "s_xor_b32 s97, s97, s99\n\t"
      "s_branch LPOLL_3%=\n\t"
      "LWM0_3%=:\n\t"
      "s_mov_b32 s63, 0x10000\n\t"
      "LWM_3%=:\n\t"
      "s_waitcnt vmcnt(0) lgkmcnt(0)\n\t"
      "v_bitop3_b32 v49, v49, v51, s53 bitop3:0x7e\n\t"
      "v_max_u32 v48, v48, v50\n\t"
      "v_cmp_ne_u32 vcc, 0, v49\n\t"
      "s_nop 1\n\t"
      "v_max_u32_dpp v48, v48, v48 quad_perm:[1,0,3,2] row_mask:0xf bank_mask:0xf\n\t"
      "s_nop 1\n\t"
      "v_max_u32_dpp v48, v48, v48 quad_perm:[2,3,0,1] row_mask:0xf bank_mask:0xf\n\t"
      "s_nop 1\n\t"
      "v_max_u32_dpp v48, v48, v48 row_ror:4 row_mask:0xf bank_mask:0xf\n\t"
      "s_nop 1\n\t"
      "v_max_u32_dpp v48, v48, v48 row_ror:8 row_mask:0xf bank_mask:0xf\n\t"
      "v_mov_b32 v49, v48\n\t"
      "s_nop 1\n\t"
      "v_permlane16_swap_b32 v48, v49\n\t"
      "v_max_u32 v48, v48, v49\n\t"
      "v_mov_b32 v49, v48\n\t"
      "s_nop 1\n\t"
      "v_permlane32_swap_b32 v48, v49\n\t"
      "v_max_u32 v48, v48, v49\n\t"
      "v_mul_f32 v49, 0x40a00000, v48\n\t"
      "s_nop 0\n\t"
      "v_readfirstlane_b32 s67, v49\n\t"
      "v_mul_f32 v49, 0x43030000, v49\n\t"
      "s_nop 0\n\t"
      "v_readfirstlane_b32 s66, v49\n\t"
      "s_cbranch_vccz LWM1_3%=\n\t"
      "s_sleep 4\n\t"
      "buffer_load_dwordx2 v[48:49], v52, s[36:39], s94 offen sc1\n\t"
      "buffer_load_dwordx2 v[50:51], v52, s[36:39], s94 offen offset:128 sc1\n\t"
      "s_sub_u32 s63, s63, 1\n\t"
      "s_cmp_lg_u32 s63, 0\n\t"
      "s_cbranch_scc1 LWM_3%=\n\t"
      "s_waitcnt vmcnt(0)\n\t"
      "LWM1_3%=:\n\t"
      "buffer_load_dwordx2 v[120:121], off, s[36:39], s64 sc1\n\t"
      "s_lshr_b32 s67, s67, 23\n\t"
      "s_add_u32 s67, s67, 5\n\t"
      "s_lshr_b32 s66, s66, 23\n\t"
      "s_add_u32 s66, s66, 129\n\t"
      "s_max_u32 s66, s66, 80\n\t"
      "s_mov_b32 s63, 0x2000\n\t"
      "LPOL0_3%=:\n\t"
      "s_memtime s[78:79]\n\t"
      "s_waitcnt lgkmcnt(8)\n\t"
      "v_mfma_f32_16x16x32_bf16 v[32:35], v[0:3], v[8:11], v[32:35]\n\t"
      "v_mfma_f32_16x16x32_bf16 v[36:39], v[4:7], v[8:11], v[36:39]\n\t"
      "v_mfma_f32_16x16x32_bf16 v[40:43], v[0:3], v[12:15], v[40:43]\n\t"
      "v_mfma_f32_16x16x32_bf16 v[44:47], v[4:7], v[12:15], v[44:47]\n\t"
      "v_mov_b64 v[48:49], 0\n\t"
      "v_mov_b64 v[50:51], 0\n\t"
      "v_mov_b64 v[52:53], 0\n\t"
      "v_mov_b64 v[54:55], 0\n\t"
      "s_xor_b32 s69, s97, 1024\n\t"
      "s_xor_b32 s98, s97, 512\n\t"
      "s_xor_b32 s23, s97, 1536\n\t"
      "s_waitcnt lgkmcnt(4)\n\t"
      "v_mfma_f32_16x16x32_bf16 v[32:35], v[16:19], v[24:27], v[32:35]\n\t"
      "v_mfma_f32_16x16x32_bf16 v[36:39], v[20:23], v[24:27], v[36:39]\n\t"
      "v_mfma_f32_16x16x32_bf16 v[40:43], v[16:19], v[28:31], v[40:43]\n\t"
      "v_mfma_f32_16x16x32_bf16 v[44:47], v[20:23], v[28:31], v[44:47]\n\t"
      "s_waitcnt lgkmcnt(0)\n\t"
      "v_mfma_f32_16x16x32_bf16 v[32:35], v[76:79], v[112:115], v[32:35]\n\t"
      "v_mfma_f32_16x16x32_bf16 v[36:39], v[80:83], v[112:115], v[36:39]\n\t"
      "v_mfma_f32_16x16x32_bf16 v[40:43], v[76:79], v[116:119], v[40:43]\n\t"
      "v_mfma_f32_16x16x32_bf16 v[44:47], v[80:83], v[116:119], v[44:47]\n\t"
      "s_mov_b64 exec, 1\n\t"
      "ds_write_b64 v109, v[110:111] offset:55392\n\t"
      "s_mov_b64 exec, -1\n\t"
      "s_nop 4\n\t"
      "v_cvt_pk_bf16_f32 v68, v32, v33\n\t"
      "v_cvt_pk_bf16_f32 v69, v34, v35\n\t"
      "v_cvt_pk_bf16_f32 v70, v36, v37\n\t"
      "v_cvt_pk_bf16_f32 v71, v38, v39\n\t"
      "v_cvt_pk_bf16_f32 v74, v40, v41\n\t"
      "v_cvt_pk_bf16_f32 v75, v42, v43\n\t"
      "v_cvt_pk_bf16_f32 v72, v44, v45\n\t"
      "v_cvt_pk_bf16_f32 v73, v46, v47\n\t"
      "v_mov_b64 v[32:33], 0\n\t"
      "v_mov_b64 v[34:35], 0\n\t"
      "v_mov_b64 v[36:37], 0\n\t"
      "v_mov_b64 v[38:39], 0\n\t"
      "v_mov_b64 v[40:41], 0\n\t"
      "v_mov_b64 v[42:43], 0\n\t"
      "v_mov_b64 v[44:45], 0\n\t"
      "v_mov_b64 v[46:47], 0\n\t"
      "s_waitcnt vmcnt(0)\n\t"
      "s_branch LGO_3%=\n\t"
      "LEARLY_3%=:\n\t"
      "s_cmp_eq_u32 s40, 54\n\t"
      "s_cbranch_scc1 LROLE_3%=\n\t"
      "s_cmp_lt_i32 s40, 0\n\t"
      "s_waitcnt lgkmcnt(0)\n\t"
      "s_cbranch_scc1 LSTART_3%=\n\t"
      "s_xor_b32 s43, s43, s99\n\t"
      "s_xor_b32 s68, s68, s99\n\t"
      "s_cmp_lg_u32 s40, 0\n\t"
      "s_cbranch_scc1 LROLE_3%=\n\t"
      "v_lshlrev_b32 v96, 4, v127\n\t"
      "buffer_load_dwordx4 v[56:59], v96, s[36:39], s86 offen sc1\n\t"
      "buffer_load_dwordx4 v[84:87], v96, s[36:39], s86 offen offset:128 sc1\n\t"
      "s_branch LROLE_3%=\n\t"
      "LSTART_3%=:\n\t"
      "s_memrealtime s[92:93]\n\t"
      "s_memtime s[90:91]\n\t"
      "s_mov_b64 exec, 1\n\t"
      "v_mov_b32 v57, s53\n\t"
      "v_mov_b32 v58, s65\n\t"
      "s_add_u32 s86, s94, 208640\n\t"
      "s_add_u32 s61, s95, 208640\n\t"
      "s_waitcnt lgkmcnt(0)\n\t"
      "v_mov_b32 v56, s92\n\t"
      "s_mov_b32 s87, s92\n\t"
      "s_sub_u32 s80, s90, s23\n\t"
      "s_sub_u32 s82, s92, s82\n\t"
      "v_cvt_f32_u32 v59, s82\n\t"
      "v_cvt_f32_u32 v63, s80\n\t"
      "v_rcp_f32 v59, v59\n\t"
      "s_nop 0\n\t"
      "v_mul_f32 v59, v59, v63\n\t"
      "v_mul_f32 v63, 0x43800000, v59\n\t"
      "v_cvt_u32_f32 v63, v63\n\t"
      "s_nop 0\n\t"
      "v_readfirstlane_b32 s83, v63\n\t"
      "s_mov_b32 s76, s90\n\t"
      "s_mov_b32 s80, s92\n\t"
      "s_sub_u32 s28, s90, 2100\n\t"
      "s_sub_u32 s82, s92, 100\n\t"
      "s_mov_b32 s26, 1800\n\t"
      "buffer_store_dwordx4 v[56:59], off, s[36:39], s61 sc1\n\t"
      "s_mov_b64 exec, -1\n\t"
      "s_branch LUTOK_3%=\n\t"
      "LSLOW_3%=:\n\t"
      "s_mov_b32 s63, 0x10000\n\t"
      "LSLOW1_3%=:\n\t"
      "buffer_load_dwordx2 v[92:93], off, s[36:39], s64 sc1\n\t"
      "s_waitcnt vmcnt(0)\n\t"
      "v_bitop3_b32 v92, v92, v93, s53 bitop3:0x7e\n\t"
      "s_nop 0\n\t"
      "v_readfirstlane_b32 s56, v92\n\t"
      "s_nop 1\n\t"
      "s_cmp_eq_u32 s56, 0\n\t"
      "s_cbranch_scc1 LSLOW2_3%=\n\t"
      "s_sleep 8\n\t"
      "s_sub_u32 s63, s63, 1\n\t"
      "s_cmp_lg_u32 s63, 0\n\t"
      "s_cbranch_scc1 LSLOW1_3%=\n\t"
      "LSLOW2_3%=:\n\t"
      "s_mov_b32 m0, s45\n\t"
      "s_sub_u32 s61, 204800, s71\n\t"
      "buffer_load_dwordx4 v65, s[36:39], s44 offen sc1 lds\n\t"
      "buffer_load_dwordx4 v65, s[36:39], s44 offen offset:1024 sc1 lds\n\t"
      "s_add_u32 m0, s55, s61\n\t"
      "s_nop 0\n\t"
      "buffer_load_dwordx4 v127, s[36:39], s46 offen sc1 lds\n\t"
      "buffer_load_dwordx4 v127, s[36:39], s46 offen offset:1024 sc1 lds\n\t"
      "s_branch LWIN1_3%=\n\t"
      "LMX_3%=:\n\t"
      "s_waitcnt lgkmcnt(0)\n\t"
      "s_nop 7\n\t"
      "v_smfmac_f32_16x16x64_bf16 v[52:55], v[24:27], v[240:247], v122\n\t"
      "v_smfmac_f32_16x16x64_bf16 v[52:55], v[20:23], v[232:239], v122\n\t"
      "v_smfmac_f32_16x16x64_bf16 v[52:55], v[16:19], v[224:231], v122\n\t"
      "s_nop 7\n\t"
      "v_add_f32 v53, v53, v55\n\t"
      "v_add_f32_e64 v52, v52, v54\n\t"
      "v_cvt_pk_bf16_f32 v66, v52, v53\n\t"
      "s_add_u32 s62, s61, 0x83400000\n\t"
      "v_mov_b32_e64 v126, s62\n\t"
      "v_max3_f32 v82, v112, v113, v114\n\t"
      "v_max_f32 v82, v82, v115\n\t"
      "s_nop 1\n\t"
      "v_max_f32_dpp v82, v82, v82 quad_perm:[2,3,0,1] row_mask:0xf bank_mask:0xf\n\t"
      "v_max3_f32 v83, v82, v120, v121\n\t"
      "v_max3_f32 v60, v82, v120, v121\n\t"
      "s_nop 1\n\t"
      "v_permlane16_swap_b32 v83, v60\n\t"
      "v_max_f32 v61, v83, v60\n\t"
      "v_max_f32 v60, v83, v60\n\t"
      "s_nop 1\n\t"
      "v_permlane32_swap_b32 v61, v60\n\t"
      "v_max_f32 v83, v61, v60\n\t"
      "v_sub_f32 v112, v112, v83\n\t"
      "v_sub_f32 v113, v113, v83\n\t"
      "v_sub_f32 v114, v114, v83\n\t"
      "v_sub_f32 v115, v115, v83\n\t"
      "v_sub_f32 v120, v120, v83\n\t"
      "v_sub_f32 v121, v121, v83\n\t"
      "v_exp_f32_e64 v56, v112\n\t"
      "v_exp_f32 v57, v113\n\t"
      "v_exp_f32_e64 v58, v114\n\t"
      "v_exp_f32 v59, v115\n\t"
      "v_exp_f32 v80, v120\n\t"
      "v_exp_f32 v81, v121\n\t"
      "v_cvt_pk_bf16_f32 v96, v56, v57\n\t"
      "v_cvt_pk_bf16_f32 v97, v58, v59\n\t"
      "v_cvt_pk_bf16_f32 v108, v80, v81\n\t"
      "v_mov_b32_dpp v98, v96 quad_perm:[2,3,0,1] row_mask:0xf bank_mask:0xf\n\t"
      "v_mov_b32_dpp v99, v97 quad_perm:[2,3,0,1] row_mask:0xf bank_mask:0xf\n\t"
      "v_mfma_f32_16x16x32_bf16 v[48:51], v[108:111], v[88:91], 0\n\t"
      "s_nop 0\n\t"
      "v_mfma_f32_16x16x32_bf16 v[48:51], v[96:99], v[88:91], v[48:51]\n\t"
      "v_mfma_f32_16x16x32_bf16 v[84:87], v[108:111], v[64:67], 0\n\t"
      "v_mfma_f32_16x16x32_bf16 v[84:87], v[96:99], v[68:71], v[84:87]\n\t"
      "v_mfma_f32_16x16x32_bf16 v[92:95], v[96:99], v[72:75], 0\n\t"
      "v_mfma_f32_16x16x32_bf16 v[92:95], v[108:111], v[66:69], v[92:95]\n\t"
      "s_nop 3\n\t"
      "v_rcp_f32_e64 v48, v48\n\t"
      "v_rcp_f32 v49, v49\n\t"
      "v_fma_f32 v84, v84, v48, s96\n\t"
      "buffer_atomic_add v84, v123, s[36:39], s97 offen\n\t"
      "v_fma_f32 v85, v85, v49, s96\n\t"
      "buffer_atomic_add v85, v123, s[36:39], s98 offen\n\t"
      "v_fma_f32 v94, v94, v48, s96\n\t"
      "buffer_atomic_add v94, v123, s[36:39], s69 offen\n\t"
      "v_fma_f32 v95, v95, v49, s96\n\t"
      "buffer_atomic_add v95, v123, s[36:39], s23 offen\n\t"
      "s_add_u32 s97, s97, 0x8000\n\t"
      "s_branch LWIN_3%=\n\t"
      "LR0_3%=:\n\t"
      "buffer_load_dwordx4 v[84:87], v60, s[36:39], s43 offen sc1\n\t"
      "buffer_load_dwordx4 v[88:91], v60, s[36:39], s43 offen offset:1024 sc1\n\t"
      "s_waitcnt vmcnt(2)\n\t"
      "buffer_load_dwordx4 v[92:95], v60, s[36:39], s68 offen sc1\n\t"
      "buffer_load_dwordx4 v[96:99], v60, s[36:39], s68 offen offset:1024 sc1\n\t"
      "s_sub_u32 s63, s63, 1\n\t"
      "s_waitcnt vmcnt(2) lgkmcnt(0)\n\t"
      "v_bitop3_b32 v64, v84, v85, v86 bitop3:0x80\n\t"
      "v_bitop3_b32 v64, v64, v87, v88 bitop3:0x80\n\t"
      "v_bitop3_b32 v64, v64, v89, v90 bitop3:0x80\n\t"
      "v_bitop3_b32 v64, v64, v91, s58 bitop3:0x80\n\t"
      "v_cmp_eq_u32 vcc, 0, v64\n\t"
      "v_pk_fma_f32 v[8:9], v[84:85], s[56:57], v[126:127] op_sel:[0,1,0] op_sel_hi:[1,1,0]\n\t"
      "v_pk_fma_f32 v[10:11], v[86:87], s[56:57], v[126:127] op_sel:[0,1,0] op_sel_hi:[1,1,0]\n\t"
      "v_cvt_pk_bf16_f32 v12, v8, v9\n\t"
      "v_cvt_pk_bf16_f32 v13, v10, v11\n\t"
      "ds_write_b64 v127, v[12:13] offset:44032\n\t"
      "v_pk_fma_f32 v[4:5], v[88:89], s[56:57], v[126:127] op_sel:[0,1,0] op_sel_hi:[1,1,0]\n\t"
      "v_pk_fma_f32 v[6:7], v[90:91], s[56:57], v[126:127] op_sel:[0,1,0] op_sel_hi:[1,1,0]\n\t"
      "v_cvt_pk_bf16_f32 v14, v4, v5\n\t"
      "v_cvt_pk_bf16_f32 v15, v6, v7\n\t"
      "ds_write_b64 v127, v[14:15] offset:44040\n\t"
      "s_memtime s[88:89]\n\t"
      "s_cmp_le_i32 s63, 0\n\t"
      "s_waitcnt lgkmcnt(0)\n\t"
      "s_cbranch_scc1 LRZ_3%=\n\t"
      "s_cbranch_vccnz LR0_3%=\n\t"
      "LRZ_3%=:\n\t"
      "s_sub_u32 s88, s88, 0\n\t"
      "v_mov_b64 v[32:33], 0\n\t"
      "v_mov_b64 v[34:35], 0\n\t"
      "v_mov_b64 v[36:37], 0\n\t"
      "v_mov_b64 v[38:39], 0\n\t"
      "v_mov_b64 v[40:41], 0\n\t"
      "v_mov_b64 v[42:43], 0\n\t"
      "v_mov_b64 v[44:45], 0\n\t"
      "v_mov_b64 v[46:47], 0\n\t"
      "v_mov_b64 v[48:49], 0\n\t"
      "v_mov_b64 v[50:51], 0\n\t"
      "v_mov_b64 v[52:53], 0\n\t"
      "v_mov_b64 v[54:55], 0\n\t"
      "v_mfma_f32_16x16x32_bf16 v[56:59], v[12:15], v[12:15], 0\n\t"
      "v_smfmac_f32_16x16x64_bf16 v[36:39], v[12:15], a[88:95], v122\n\t"
      "v_smfmac_f32_16x16x64_bf16 v[44:47], v[12:15], a[216:223], v122\n\t"
      "s_branch LC0_3%=\n\t"
      "LCOLD%=:\n\t"
      "LR1_0%=:\n\t"
      "v_bitop3_b32 v64, v92, v93, v94 bitop3:0x80\n\t"
      "v_bitop3_b32 v64, v64, v95, v96 bitop3:0x80\n\t"
      "v_bitop3_b32 v64, v64, v97, v98 bitop3:0x80\n\t"
      "v_bitop3_b32 v64, v64, v99, s58 bitop3:0x80\n\t"
      "v_cmp_eq_u32 vcc, 0, v64\n\t"
      "s_nop 0\n\t"
      "s_cbranch_vccz LRX_0%=\n\t"
      "LRY_0%=:\n\t"
      "buffer_load_dwordx4 v[92:95], v60, s[36:39], s68 offen sc1\n\t"
      "buffer_load_dwordx4 v[96:99], v60, s[36:39], s68 offen offset:1024 sc1\n\t"
      "s_sub_u32 s63, s63, 1\n\t"
      "s_waitcnt vmcnt(0) lgkmcnt(0)\n\t"
      "v_bitop3_b32 v64, v92, v93, v94 bitop3:0x80\n\t"
      "v_bitop3_b32 v64, v64, v95, v96 bitop3:0x80\n\t"
      "v_bitop3_b32 v64, v64, v97, v98 bitop3:0x80\n\t"
      "v_bitop3_b32 v64, v64, v99, s58 bitop3:0x80\n\t"
      "v_cmp_eq_u32 vcc, 0, v64\n\t"
      "v_pk_fma_f32 v[20:21], v[92:93], s[56:57], v[126:127] op_sel:[0,1,0] op_sel_hi:[1,1,0]\n\t"
      "v_pk_fma_f32 v[22:23], v[94:95], s[56:57], v[126:127] op_sel:[0,1,0] op_sel_hi:[1,1,0]\n\t"
      "v_cvt_pk_bf16_f32 v16, v20, v21\n\t"
      "v_cvt_pk_bf16_f32 v17, v22, v23\n\t"
      "ds_write_b64 v127, v[16:17] offset:45056\n\t"
      "v_pk_fma_f32 v[24:25], v[96:97], s[56:57], v[126:127] op_sel:[0,1,0] op_sel_hi:[1,1,0]\n\t"
      "v_pk_fma_f32 v[26:27], v[98:99], s[56:57], v[126:127] op_sel:[0,1,0] op_sel_hi:[1,1,0]\n\t"
      "v_cvt_pk_bf16_f32 v18, v24, v25\n\t"
      "v_cvt_pk_bf16_f32 v19, v26, v27\n\t"
      "ds_write_b64 v127, v[18:19] offset:45064\n\t"
      "s_cmp_le_i32 s63, 0\n\t"
      "s_waitcnt lgkmcnt(0)\n\t"
      "s_cbranch_scc1 LRX_0%=\n\t"
      "s_cbranch_vccnz LRY_0%=\n\t"
      "LRX_0%=:\n\t"
      "s_cmp_eq_u32 s40, 63\n\t"
      "s_cbranch_scc1 LLAST_0%=\n\t"
      "s_branch LC1_0%=\n\t"
      "LLAST_0%=:\n\t"
      "s_waitcnt vmcnt(0) lgkmcnt(0)\n\t"
      "LLAS2_0%=:\n\t"
      "v_add_u32 v100, s55, v127\n\t"
      "v_add_u32 v52, 65536, v127\n\t"
      "s_barrier\n\t"
      "ds_read_b128 v[20:23], v127 offset:46080\n\t"
      "ds_read_b128 v[24:27], v127 offset:47104\n\t"
      "ds_read_b128 v[28:31], v127 offset:48128\n\t"
      "ds_read_b128 v[60:63], v100 offset:57344\n\t"
      "ds_read_b128 v[88:91], v52\n\t"
      "ds_read_b128 v[92:95], v52 offset:1024\n\t"
      "ds_read_b128 v[32:35], v52 offset:4096\n\t"
      "ds_read_b128 v[36:39], v52 offset:5120\n\t"
      "ds_read_b128 v[76:79], v52 offset:8192\n\t"
      "ds_read_b128 v[80:83], v52 offset:9216\n\t"
      "ds_read_b128 v[112:115], v52 offset:12288\n\t"
      "ds_read_b128 v[116:119], v52 offset:13312\n\t"
      "v_mov_b64 v[48:49], 0\n\t"
      "v_mov_b64 v[50:51], 0\n\t"
      "v_smfmac_f32_16x16x64_bf16 v[40:43], v[16:19], a[160:167], v122\n\t"
      "v_smfmac_f32_16x16x64_bf16 v[44:47], v[16:19], a[224:231], v122\n\t"
      "s_waitcnt lgkmcnt(11)\n\t"
      "v_smfmac_f32_16x16x64_bf16 v[40:43], v[20:23], a[168:175], v122\n\t"
      "v_smfmac_f32_16x16x64_bf16 v[44:47], v[20:23], a[232:239], v122\n\t"
      "s_waitcnt lgkmcnt(10)\n\t"
      "v_smfmac_f32_16x16x64_bf16 v[44:47], v[24:27], a[240:247], v122\n\t"
      "v_smfmac_f32_16x16x64_bf16 v[40:43], v[24:27], a[176:183], v122\n\t"
      "s_waitcnt lgkmcnt(9)\n\t"
      "v_smfmac_f32_16x16x64_bf16 v[44:47], v[28:31], a[248:255], v122\n\t"
      "v_smfmac_f32_16x16x64_bf16 v[40:43], v[28:31], a[184:191], v122\n\t"
      "s_waitcnt lgkmcnt(0)\n\t"
      "v_smfmac_f32_16x16x64_bf16 v[48:51], v[0:3], v[88:95], v122\n\t"
      "v_smfmac_f32_16x16x64_bf16 v[48:51], v[4:7], v[32:39], v122\n\t"
      "v_smfmac_f32_16x16x64_bf16 v[48:51], v[8:11], v[76:83], v122\n\t"
      "v_smfmac_f32_16x16x64_bf16 v[48:51], v[12:15], v[112:119], v122\n\t"
      "v_add_f32 v40, v40, v42\n\t"
      "v_add_f32 v41, v41, v43\n\t"
      "v_cvt_pk_bf16_f32 v120, v40, v41\n\t"
      "v_add_f32 v44, v44, v46\n\t"
      "v_add_f32 v45, v45, v47\n\t"
      "v_cvt_pk_bf16_f32 v121, v44, v45\n\t"
      "ds_write_b64 v125, v[120:121] offset:2080\n\t"
      "s_nop 0\n\t"
      "v_add_f32 v48, v48, v50\n\t"
      "v_add_f32 v49, v49, v51\n\t"
      "s_mov_b32 s28, s49\n\t"
      "s_waitcnt lgkmcnt(0)\n\t"
      "s_barrier\n\t"
      "ds_read_b64 v[54:55], v124 offset:53312\n\t"
      "s_waitcnt lgkmcnt(0)\n\t"
      "v_add_f32 v48, v48, v54\n\t"
      "v_add_f32 v49, v49, v55\n\t"
      "v_cvt_pk_bf16_f32 v48, v48, v49\n\t"
      "buffer_store_dword v48, v62, s[36:39], s28 offen sc1\n\t"
      "s_branch LEND%=\n\t"
      "LR1_1%=:\n\t"
      "v_bitop3_b32 v64, v92, v93, v94 bitop3:0x80\n\t"
      "v_bitop3_b32 v64, v64, v95, v96 bitop3:0x80\n\t"
      "v_bitop3_b32 v64, v64, v97, v98 bitop3:0x80\n\t"
      "v_bitop3_b32 v64, v64, v99, s58 bitop3:0x80\n\t"
      "v_cmp_eq_u32 vcc, 0, v64\n\t"
      "s_nop 0\n\t"
      "s_cbranch_vccz LRX_1%=\n\t"
      "LRY_1%=:\n\t"
      "buffer_load_dwordx4 v[92:95], v60, s[36:39], s68 offen sc1\n\t"
      "buffer_load_dwordx4 v[96:99], v60, s[36:39], s68 offen offset:1024 sc1\n\t"
      "s_sub_u32 s63, s63, 1\n\t"
      "s_waitcnt vmcnt(0) lgkmcnt(0)\n\t"
      "v_bitop3_b32 v64, v92, v93, v94 bitop3:0x80\n\t"
      "v_bitop3_b32 v64, v64, v95, v96 bitop3:0x80\n\t"
      "v_bitop3_b32 v64, v64, v97, v98 bitop3:0x80\n\t"
      "v_bitop3_b32 v64, v64, v99, s58 bitop3:0x80\n\t"
      "v_cmp_eq_u32 vcc, 0, v64\n\t"
      "v_pk_fma_f32 v[16:17], v[92:93], s[56:57], v[126:127] op_sel:[0,1,0] op_sel_hi:[1,1,0]\n\t"
      "v_pk_fma_f32 v[18:19], v[94:95], s[56:57], v[126:127] op_sel:[0,1,0] op_sel_hi:[1,1,0]\n\t"
      "v_cvt_pk_bf16_f32 v20, v16, v17\n\t"
      "v_cvt_pk_bf16_f32 v21, v18, v19\n\t"
      "ds_write_b64 v127, v[20:21] offset:46080\n\t"
      "v_pk_fma_f32 v[28:29], v[96:97], s[56:57], v[126:127] op_sel:[0,1,0] op_sel_hi:[1,1,0]\n\t"
      "v_pk_fma_f32 v[30:31], v[98:99], s[56:57], v[126:127] op_sel:[0,1,0] op_sel_hi:[1,1,0]\n\t"
      "v_cvt_pk_bf16_f32 v22, v28, v29\n\t"
      "v_cvt_pk_bf16_f32 v23, v30, v31\n\t"
      "ds_write_b64 v127, v[22:23] offset:46088\n\t"
      "s_cmp_le_i32 s63, 0\n\t"
      "s_waitcnt lgkmcnt(0)\n\t"
      "s_cbranch_scc1 LRX_1%=\n\t"
      "s_cbranch_vccnz LRY_1%=\n\t"
      "LRX_1%=:\n\t"
      "s_cmp_eq_u32 s40, 63\n\t"
      "s_cbranch_scc1 LLAST_1%=\n\t"
      "s_branch LC1_1%=\n\t"
      "LLAST_1%=:\n\t"
      "s_waitcnt vmcnt(0) lgkmcnt(0)\n\t"
      "LLAS2_1%=:\n\t"
      "v_add_u32 v100, s55, v127\n\t"
      "v_add_u32 v52, 67584, v127\n\t"
      "s_barrier\n\t"
      "ds_read_b128 v[16:19], v127 offset:45056\n\t"
      "ds_read_b128 v[28:31], v127 offset:48128\n\t"
      "ds_read_b128 v[24:27], v127 offset:47104\n\t"
      "ds_read_b128 v[60:63], v100 offset:57344\n\t"
      "ds_read_b128 v[88:91], v52\n\t"
      "ds_read_b128 v[92:95], v52 offset:1024\n\t"
      "ds_read_b128 v[32:35], v52 offset:4096\n\t"
      "ds_read_b128 v[36:39], v52 offset:5120\n\t"
      "ds_read_b128 v[76:79], v52 offset:8192\n\t"
      "ds_read_b128 v[80:83], v52 offset:9216\n\t"
      "ds_read_b128 v[112:115], v52 offset:12288\n\t"
      "ds_read_b128 v[116:119], v52 offset:13312\n\t"
      "v_mov_b64 v[48:49], 0\n\t"
      "v_mov_b64 v[50:51], 0\n\t"
      "v_smfmac_f32_16x16x64_bf16 v[40:43], v[20:23], a[168:175], v122\n\t"
      "v_smfmac_f32_16x16x64_bf16 v[44:47], v[20:23], a[232:239], v122\n\t"
      "s_waitcnt lgkmcnt(11)\n\t"
      "v_smfmac_f32_16x16x64_bf16 v[40:43], v[16:19], a[160:167], v122\n\t"
      "v_smfmac_f32_16x16x64_bf16 v[44:47], v[16:19], a[224:231], v122\n\t"
      "s_waitcnt lgkmcnt(10)\n\t"
      "v_smfmac_f32_16x16x64_bf16 v[44:47], v[28:31], a[248:255], v122\n\t"
      "v_smfmac_f32_16x16x64_bf16 v[40:43], v[28:31], a[184:191], v122\n\t"
      "s_waitcnt lgkmcnt(9)\n\t"
      "v_smfmac_f32_16x16x64_bf16 v[44:47], v[24:27], a[240:247], v122\n\t"
      "v_smfmac_f32_16x16x64_bf16 v[40:43], v[24:27], a[176:183], v122\n\t"
      "s_waitcnt lgkmcnt(0)\n\t"
      "v_smfmac_f32_16x16x64_bf16 v[48:51], v[0:3], v[88:95], v122\n\t"
      "v_smfmac_f32_16x16x64_bf16 v[48:51], v[4:7], v[32:39], v122\n\t"
      "v_smfmac_f32_16x16x64_bf16 v[48:51], v[8:11], v[76:83], v122\n\t"
      "v_smfmac_f32_16x16x64_bf16 v[48:51], v[12:15], v[112:119], v122\n\t"
      "v_add_f32 v40, v40, v42\n\t"
      "v_add_f32 v41, v41, v43\n\t"
      "v_cvt_pk_bf16_f32 v120, v40, v41\n\t"
      "v_add_f32 v44, v44, v46\n\t"
      "v_add_f32 v45, v45, v47\n\t"
      "v_cvt_pk_bf16_f32 v121, v44, v45\n\t"
      "ds_write_b64 v125, v[120:121] offset:2080\n\t"
      "s_nop 0\n\t"
      "v_add_f32 v48, v48, v50\n\t"
      "v_add_f32 v49, v49, v51\n\t"
      "s_mov_b32 s28, s49\n\t"
      "s_waitcnt lgkmcnt(0)\n\t"
      "s_barrier\n\t"
      "ds_read_b64 v[54:55], v124 offset:53832\n\t"
      "s_waitcnt lgkmcnt(0)\n\t"
      "v_add_f32 v48, v48, v54\n\t"
      "v_add_f32 v49, v49, v55\n\t"
      "v_cvt_pk_bf16_f32 v48, v48, v49\n\t"
      "buffer_store_dword v48, v62, s[36:39], s28 offen sc1\n\t"
      "s_branch LEND%=\n\t"
      "LWK1_2%=:\n\t"
      "buffer_store_dwordx4 v[44:47], v61, s[36:39], s48 offen sc1\n\t"
      "s_branch LW4_2%=\n\t"
      "LR1_2%=:\n\t"
      "v_bitop3_b32 v64, v92, v93, v94 bitop3:0x80\n\t"
      "v_bitop3_b32 v64, v64, v95, v96 bitop3:0x80\n\t"
      "v_bitop3_b32 v64, v64, v97, v98 bitop3:0x80\n\t"
      "v_bitop3_b32 v64, v64, v99, s58 bitop3:0x80\n\t"
      "v_cmp_eq_u32 vcc, 0, v64\n\t"
      "s_nop 0\n\t"
      "s_cbranch_vccz LRX_2%=\n\t"
      "LRY_2%=:\n\t"
      "buffer_load_dwordx4 v[92:95], v60, s[36:39], s68 offen sc1\n\t"
      "buffer_load_dwordx4 v[96:99], v60, s[36:39], s68 offen offset:1024 sc1\n\t"
      "s_sub_u32 s63, s63, 1\n\t"
      "s_waitcnt vmcnt(0) lgkmcnt(0)\n\t"
      "v_bitop3_b32 v64, v92, v93, v94 bitop3:0x80\n\t"
      "v_bitop3_b32 v64, v64, v95, v96 bitop3:0x80\n\t"
      "v_bitop3_b32 v64, v64, v97, v98 bitop3:0x80\n\t"
      "v_bitop3_b32 v64, v64, v99, s58 bitop3:0x80\n\t"
      "v_cmp_eq_u32 vcc, 0, v64\n\t"
      "v_pk_fma_f32 v[28:29], v[92:93], s[56:57], v[126:127] op_sel:[0,1,0] op_sel_hi:[1,1,0]\n\t"
      "v_pk_fma_f32 v[30:31], v[94:95], s[56:57], v[126:127] op_sel:[0,1,0] op_sel_hi:[1,1,0]\n\t"
      "v_cvt_pk_bf16_f32 v24, v28, v29\n\t"
      "v_cvt_pk_bf16_f32 v25, v30, v31\n\t"
      "ds_write_b64 v127, v[24:25] offset:47104\n\t"
      "v_pk_fma_f32 v[16:17], v[96:97], s[56:57], v[126:127] op_sel:[0,1,0] op_sel_hi:[1,1,0]\n\t"
      "v_pk_fma_f32 v[18:19], v[98:99], s[56:57], v[126:127] op_sel:[0,1,0] op_sel_hi:[1,1,0]\n\t"
      "v_cvt_pk_bf16_f32 v26, v16, v17\n\t"
      "v_cvt_pk_bf16_f32 v27, v18, v19\n\t"
      "ds_write_b64 v127, v[26:27] offset:47112\n\t"
      "s_cmp_le_i32 s63, 0\n\t"
      "s_waitcnt lgkmcnt(0)\n\t"
      "s_cbranch_scc1 LRX_2%=\n\t"
      "s_cbranch_vccnz LRY_2%=\n\t"
      "LRX_2%=:\n\t"
      "s_cmp_eq_u32 s40, 63\n\t"
      "s_cbranch_scc1 LLAST_2%=\n\t"
      "s_branch LC1_2%=\n\t"
      "LLAST_2%=:\n\t"
      "s_waitcnt vmcnt(0) lgkmcnt(0)\n\t"
      "LLAS2_2%=:\n\t"
      "v_add_u32 v100, s55, v127\n\t"
      "v_add_u32 v52, 65536, v127\n\t"
      "s_barrier\n\t"
      "ds_read_b128 v[28:31], v127 offset:48128\n\t"
      "ds_read_b128 v[16:19], v127 offset:45056\n\t"
      "ds_read_b128 v[20:23], v127 offset:46080\n\t"
      "ds_read_b128 v[60:63], v100 offset:57344\n\t"
      "ds_read_b128 v[88:91], v52 offset:16384\n\t"
      "ds_read_b128 v[92:95], v52 offset:17408\n\t"
      "ds_read_b128 v[32:35], v52 offset:20480\n\t"
      "ds_read_b128 v[36:39], v52 offset:21504\n\t"
      "ds_read_b128 v[76:79], v52 offset:24576\n\t"
      "ds_read_b128 v[80:83], v52 offset:25600\n\t"
      "ds_read_b128 v[112:115], v52 offset:28672\n\t"
      "ds_read_b128 v[116:119], v52 offset:29696\n\t"
      "v_mov_b64 v[48:49], 0\n\t"
      "v_mov_b64 v[50:51], 0\n\t"
      "v_smfmac_f32_16x16x64_bf16 v[40:43], v[24:27], a[176:183], v122\n\t"
      "v_smfmac_f32_16x16x64_bf16 v[44:47], v[24:27], a[240:247], v122\n\t"
      "s_waitcnt lgkmcnt(11)\n\t"
      "v_smfmac_f32_16x16x64_bf16 v[40:43], v[28:31], a[184:191], v122\n\t"
      "v_smfmac_f32_16x16x64_bf16 v[44:47], v[28:31], a[248:255], v122\n\t"
      "s_waitcnt lgkmcnt(10)\n\t"
      "v_smfmac_f32_16x16x64_bf16 v[44:47], v[16:19], a[224:231], v122\n\t"
      "v_smfmac_f32_16x16x64_bf16 v[40:43], v[16:19], a[160:167], v122\n\t"
      "s_waitcnt lgkmcnt(9)\n\t"
      "v_smfmac_f32_16x16x64_bf16 v[44:47], v[20:23], a[232:239], v122\n\t"
      "v_smfmac_f32_16x16x64_bf16 v[40:43], v[20:23], a[168:175], v122\n\t"
      "s_waitcnt lgkmcnt(0)\n\t"
      "v_smfmac_f32_16x16x64_bf16 v[48:51], v[16:19], v[88:95], v122\n\t"
      "v_smfmac_f32_16x16x64_bf16 v[48:51], v[20:23], v[32:39], v122\n\t"
      "v_smfmac_f32_16x16x64_bf16 v[48:51], v[24:27], v[76:83], v122\n\t"
      "v_smfmac_f32_16x16x64_bf16 v[48:51], v[28:31], v[112:119], v122\n\t"
      "v_add_f32 v40, v40, v42\n\t"
      "v_add_f32 v41, v41, v43\n\t"
      "v_cvt_pk_bf16_f32 v120, v40, v41\n\t"
      "v_add_f32 v44, v44, v46\n\t"
      "v_add_f32 v45, v45, v47\n\t"
      "v_cvt_pk_bf16_f32 v121, v44, v45\n\t"
      "ds_write_b64 v125, v[120:121] offset:2080\n\t"
      "s_nop 0\n\t"
      "v_add_f32 v48, v48, v50\n\t"
      "v_add_f32 v49, v49, v51\n\t"
      "ds_write_b64 v124, v[48:49] offset:53312\n\t"
      "s_waitcnt lgkmcnt(0)\n\t"
      "s_barrier\n\t"
      "s_cmp_eq_u32 s42, 3\n\t"
      "s_cselect_b32 s61, 1040, 0\n\t"
      "v_add_u32 v64, s61, v124\n\t"
      "ds_read_b64_tr_b16 v[40:41], v64 offset:51232\n\t"
      "ds_read_b64_tr_b16 v[42:43], v64 offset:51752\n\t"
      "s_cmp_lt_u32 s42, 4\n\t"
      "s_cselect_b64 exec, -1, 0\n\t"
      "s_waitcnt lgkmcnt(0)\n\t"
      "buffer_store_dwordx4 v[40:43], v61, s[36:39], s48 offen sc1\n\t"
      "s_mov_b64 exec, -1\n\t"
      "s_branch LEND%=\n\t"
      "LR1_3%=:\n\t"
      "v_bitop3_b32 v64, v92, v93, v94 bitop3:0x80\n\t"
      "v_bitop3_b32 v64, v64, v95, v96 bitop3:0x80\n\t"
      "v_bitop3_b32 v64, v64, v97, v98 bitop3:0x80\n\t"
      "v_bitop3_b32 v64, v64, v99, s58 bitop3:0x80\n\t"
      "v_cmp_eq_u32 vcc, 0, v64\n\t"
      "s_nop 0\n\t"
      "s_cbranch_vccz LRX_3%=\n\t"
      "LRY_3%=:\n\t"
      "buffer_load_dwordx4 v[92:95], v60, s[36:39], s68 offen sc1\n\t"
      "buffer_load_dwordx4 v[96:99], v60, s[36:39], s68 offen offset:1024 sc1\n\t"
      "s_sub_u32 s63, s63, 1\n\t"
      "s_waitcnt vmcnt(0) lgkmcnt(0)\n\t"
      "v_bitop3_b32 v64, v92, v93, v94 bitop3:0x80\n\t"
      "v_bitop3_b32 v64, v64, v95, v96 bitop3:0x80\n\t"
      "v_bitop3_b32 v64, v64, v97, v98 bitop3:0x80\n\t"
      "v_bitop3_b32 v64, v64, v99, s58 bitop3:0x80\n\t"
      "v_cmp_eq_u32 vcc, 0, v64\n\t"
      "v_pk_fma_f32 v[24:25], v[92:93], s[56:57], v[126:127] op_sel:[0,1,0] op_sel_hi:[1,1,0]\n\t"
      "v_pk_fma_f32 v[26:27], v[94:95], s[56:57], v[126:127] op_sel:[0,1,0] op_sel_hi:[1,1,0]\n\t"
      "v_cvt_pk_bf16_f32 v28, v24, v25\n\t"
      "v_cvt_pk_bf16_f32 v29, v26, v27\n\t"
      "ds_write_b64 v127, v[28:29] offset:48128\n\t"
      "v_pk_fma_f32 v[20:21], v[96:97], s[56:57], v[126:127] op_sel:[0,1,0] op_sel_hi:[1,1,0]\n\t"
      "v_pk_fma_f32 v[22:23], v[98:99], s[56:57], v[126:127] op_sel:[0,1,0] op_sel_hi:[1,1,0]\n\t"
      "v_cvt_pk_bf16_f32 v30, v20, v21\n\t"
      "v_cvt_pk_bf16_f32 v31, v22, v23\n\t"
      "ds_write_b64 v127, v[30:31] offset:48136\n\t"
      "s_memtime s[88:89]\n\t"
      "s_cmp_le_i32 s63, 0\n\t"
      "s_waitcnt lgkmcnt(0)\n\t"
      "s_cbranch_scc1 LRX_3%=\n\t"
      "s_cbranch_vccnz LRY_3%=\n\t"
      "s_sub_u32 s88, s88, 336\n\t"
      "LRX_3%=:\n\t"
      "s_cmp_eq_u32 s40, 63\n\t"
      "s_cbranch_scc1 LLAST_3%=\n\t"
      "s_branch LC1_3%=\n\t"
      "LLAST_3%=:\n\t"
      "s_waitcnt vmcnt(0) lgkmcnt(0)\n\t"
      "LLAS2_3%=:\n\t"
      "v_add_u32 v100, s55, v127\n\t"
      "v_add_u32 v52, 67584, v127\n\t"
      "s_barrier\n\t"
      "ds_read_b128 v[24:27], v127 offset:47104\n\t"
      "ds_read_b128 v[20:23], v127 offset:46080\n\t"
      "ds_read_b128 v[16:19], v127 offset:45056\n\t"
      "ds_read_b128 v[60:63], v100 offset:57344\n\t"
      "ds_read_b128 v[88:91], v52 offset:16384\n\t"
      "ds_read_b128 v[92:95], v52 offset:17408\n\t"
      "ds_read_b128 v[32:35], v52 offset:20480\n\t"
      "ds_read_b128 v[36:39], v52 offset:21504\n\t"
      "ds_read_b128 v[76:79], v52 offset:24576\n\t"
      "ds_read_b128 v[80:83], v52 offset:25600\n\t"
      "ds_read_b128 v[112:115], v52 offset:28672\n\t"
      "ds_read_b128 v[116:119], v52 offset:29696\n\t"
      "v_mov_b64 v[48:49], 0\n\t"
      "v_mov_b64 v[50:51], 0\n\t"
      "v_smfmac_f32_16x16x64_bf16 v[40:43], v[28:31], a[184:191], v122\n\t"
      "v_smfmac_f32_16x16x64_bf16 v[44:47], v[28:31], a[248:255], v122\n\t"
      "s_waitcnt lgkmcnt(11)\n\t"
      "v_smfmac_f32_16x16x64_bf16 v[40:43], v[24:27], a[176:183], v122\n\t"
      "v_smfmac_f32_16x16x64_bf16 v[44:47], v[24:27], a[240:247], v122\n\t"
      "s_waitcnt lgkmcnt(10)\n\t"
      "v_smfmac_f32_16x16x64_bf16 v[44:47], v[20:23], a[232:239], v122\n\t"
      "v_smfmac_f32_16x16x64_bf16 v[40:43], v[20:23], a[168:175], v122\n\t"
      "s_waitcnt lgkmcnt(9)\n\t"
      "v_smfmac_f32_16x16x64_bf16 v[44:47], v[16:19], a[224:231], v122\n\t"
      "v_smfmac_f32_16x16x64_bf16 v[40:43], v[16:19], a[160:167], v122\n\t"
      "s_waitcnt lgkmcnt(0)\n\t"
      "v_smfmac_f32_16x16x64_bf16 v[48:51], v[16:19], v[88:95], v122\n\t"
      "v_smfmac_f32_16x16x64_bf16 v[48:51], v[20:23], v[32:39], v122\n\t"
      "v_smfmac_f32_16x16x64_bf16 v[48:51], v[24:27], v[76:83], v122\n\t"
      "v_smfmac_f32_16x16x64_bf16 v[48:51], v[28:31], v[112:119], v122\n\t"
      "v_add_f32 v40, v40, v42\n\t"
      "v_add_f32 v41, v41, v43\n\t"
      "v_cvt_pk_bf16_f32 v120, v40, v41\n\t"
      "v_add_f32 v44, v44, v46\n\t"
      "v_add_f32 v45, v45, v47\n\t"
      "v_cvt_pk_bf16_f32 v121, v44, v45\n\t"
      "ds_write_b64 v125, v[120:121] offset:2080\n\t"
      "s_nop 0\n\t"
      "v_add_f32 v48, v48, v50\n\t"
      "v_add_f32 v49, v49, v51\n\t"
      "ds_write_b64 v124, v[48:49] offset:53832\n\t"
      "s_waitcnt lgkmcnt(0)\n\t"
      "s_barrier\n\t"
      "s_memrealtime s[92:93]\n\t"
      "s_waitcnt lgkmcnt(0)\n\t"
      "s_sub_u32 s61, s92, s95\n\t"
      "s_mov_b32 s95, s92\n\t"
      "s_min_u32 s61, s61, 190\n\t"
      "s_add_u32 s61, s61, s92\n\t"
      "s_pack_ll_b32_b16 s61, s61, s40\n\t"
      "v_mov_b32 v63, s61\n\t"
      "s_mov_b64 exec, 1\n\t"
      "buffer_store_dword v63, off, s[36:39], s51 sc1\n\t"
      "s_mov_b64 exec, -1\n\t"
      "s_branch LEND%=\n\t"
      "LEND%=:\n\t"
      "s_waitcnt vmcnt(2) lgkmcnt(0)\n\t"
      "s_cmp_lg_u32 s42, 9\n\t"
      "s_cbranch_scc1 LOUT%=\n\t"
      "s_waitcnt vmcnt(0)\n\t"
      "LOUT%=:\n\t"
      : "+{v[116:123]}"(c0), "+{v[124:127]}"(c1), "+{v[0:15]}"(sc), "+{v[16:31]}"(s2), "+{v[100:107]}"(c3), "+{v[108:115]}"(cc), "+s"(A), "+s"(b), "+s"(wv), "+{s87}"(tch)
      :
      : "memory", "vcc", "scc", "v32", "v33", "v34", "v35", "v36", "v37", "v38", "v39", "v40", "v41", "v42", "v43", "v44", "v45", "v46", "v47", "v48", "v49", "v50", "v51", "v52", "v53", "v54", "v55", "v56", "v57", "v58", "v59", "v60", "v61", "v62", "v63", "v64", "v65", "v66", "v67", "v68", "v69", "v70", "v71", "v72", "v73", "v74", "v75", "v76", "v77", "v78", "v79", "v80", "v81", "v82", "v83", "v84", "v85", "v86", "v87", "v88", "v89", "v90", "v91", "v92", "v93", "v94", "v95", "v96", "v97", "v98", "v99", "v128", "v129", "v130", "v131", "v132", "v133", "v134", "v135", "v136", "v137", "v138", "v139", "v140", "v141", "v142", "v143", "v144", "v145", "v146", "v147", "v148", "v149", "v150", "v151", "v152", "v153", "v154", "v155", "v156", "v157", "v158", "v159", "v160", "v161", "v162", "v163", "v164", "v165", "v166", "v167", "v168", "v169", "v170", "v171", "v172", "v173", "v174", "v175", "v176", "v177", "v178", "v179", "v180", "v181", "v182", "v183", "v184", "v185", "v186", "v187", "v188", "v189", "v190", "v191", "v192", "v193", "v194", "v195", "v196", "v197", "v198", "v199", "v200", "v201", "v202", "v203", "v204", "v205", "v206", "v207", "v208", "v209", "v210", "v211", "v212", "v213", "v214", "v215", "v216", "v217", "v218", "v219", "v220", "v221", "v222", "v223", "v224", "v225", "v226", "v227", "v228", "v229", "v230", "v231", "v232", "v233", "v234", "v235", "v236", "v237", "v238", "v239", "v240", "v241", "v242", "v243", "v244", "v245", "v246", "v247", "v248", "v249", "v250", "v251", "v252", "v253", "v254", "v255", "a0", "a1", "a2", "a3", "a4", "a5", "a6", "a7", "a8", "a9", "a10", "a11", "a12", "a13", "a14", "a15", "a16", "a17", "a18", "a19", "a20", "a21", "a22", "a23", "a24", "a25", "a26", "a27", "a28", "a29", "a30", "a31", "a32", "a33", "a34", "a35", "a36", "a37", "a38", "a39", "a40", "a41", "a42", "a43", "a44", "a45", "a46", "a47", "a48", "a49", "a50", "a51", "a52", "a53", "a54", "a55", "a56", "a57", "a58", "a59", "a60", "a61", "a62", "a63", "a64", "a65", "a66", "a67", "a68", "a69", "a70", "a71", "a72", "a73", "a74", "a75", "a76", "a77", "a78", "a79", "a80", "a81", "a82", "a83", "a84", "a85", "a86", "a87", "a88", "a89", "a90", "a91", "a92", "a93", "a94", "a95", "a96", "a97", "a98", "a99", "a100", "a101", "a102", "a103", "a104", "a105", "a106", "a107", "a108", "a109", "a110", "a111", "a112", "a113", "a114", "a115", "a116", "a117", "a118", "a119", "a120", "a121", "a122", "a123", "a124", "a125", "a126", "a127", "a128", "a129", "a130", "a131", "a132", "a133", "a134", "a135", "a136", "a137", "a138", "a139", "a140", "a141", "a142", "a143", "a144", "a145", "a146", "a147", "a148", "a149", "a150", "a151", "a152", "a153", "a154", "a155", "a156", "a157", "a158", "a159", "a160", "a161", "a162", "a163", "a164", "a165", "a166", "a167", "a168", "a169", "a170", "a171", "a172", "a173", "a174", "a175", "a176", "a177", "a178", "a179", "a180", "a181", "a182", "a183", "a184", "a185", "a186", "a187", "a188", "a189", "a190", "a191", "a192", "a193", "a194", "a195", "a196", "a197", "a198", "a199", "a200", "a201", "a202", "a203", "a204", "a205", "a206", "a207", "a208", "a209", "a210", "a211", "a212", "a213", "a214", "a215", "a216", "a217", "a218", "a219", "a220", "a221", "a222", "a223", "a224", "a225", "a226", "a227", "a228", "a229", "a230", "a231", "a232", "a233", "a234", "a235", "a236", "a237", "a238", "a239", "a240", "a241", "a242", "a243", "a244", "a245", "a246", "a247", "a248", "a249", "a250", "a251", "a252", "a253", "a254", "a255", "s22", "s23", "s24", "s25", "s26", "s27", "s28", "s29", "s30", "s31", "s36", "s37", "s38", "s39", "s40", "s41", "s42", "s43", "s44", "s45", "s46", "s47", "s48", "s49", "s50", "s51", "s52", "s53", "s54", "s55", "s56", "s57", "s58", "s59", "s60", "s61", "s62", "s63", "s64", "s65", "s66", "s67", "s68", "s69", "s70", "s71", "s72", "s73", "s74", "s75", "s76", "s77", "s78", "s79", "s80", "s81", "s82", "s83", "s84", "s85", "s86", "s88", "s89", "s90", "s91", "s92", "s93", "s94", "s95", "s96", "s97", "s98", "s99");
}

#define SSLEEP 1
#define TSLEEP 4
__device__ __forceinline__ unsigned mkkey(unsigned w, int idx) {
  unsigned u = w >> 16;
  u = (u & 0x7fffu) == 0 ? 0u : u;
  unsigned o = (u & 0x8000u) ? (~u & 0xffffu) : (u | 0x8000u);
  o = (u & 0x7fffu) > 0x7f80u ? 0u : o;
  return (o << 16) | (0xffffu - (unsigned)idx);
}
template <bool spread>
__device__ __forceinline__ u32x2 top2(const float* rbuf, int tid, int need, unsigned* sk) {
  unsigned key[9];
  const __amdgpu_buffer_rsrc_t rrs = __builtin_amdgcn_make_buffer_rsrc((void*)rbuf, 0, spread ? 2 * S_ * 4 + 256 * 128 : S_ * 4, 0x00020000);
  const int t8 = spread && tid >= 224 ? 0 : tid * 8;
#pragma unroll 1
  for (unsigned spins = 0; spins < (1u << 20); ++spins) {
    asm volatile("" ::: "memory");
    const u32x4 a = ld_sc1(rrs, t8 * 4), b = ld_sc1(rrs, t8 * 4 + 16);
    key[8] = spread ? __builtin_amdgcn_raw_buffer_load_b32(rrs, 2 * S_ * 4 + tid * 128, 0, 16) : a[0];
    unsigned mx = key[8];
#pragma unroll
    for (int k = 0; k < 4; ++k) { key[k] = a[k]; key[4 + k] = b[k]; mx = max(mx, max(a[k], b[k])); }
    if (mx != ~0u || tid * 8 >= need) break;
    __builtin_amdgcn_s_sleep(TSLEEP);
  }
  unsigned m = 0;
#pragma unroll
  for (int k = 0; k < 9; ++k) {
    key[k] = mkkey(key[k], k < 8 ? t8 + k : (spread ? S_ - 256 + tid : t8));
    m = max(m, key[k]);
  }
  m = wave_max_u32(m);
  __syncthreads();
  if ((tid & 63) == 0) sk[tid >> 6] = m;
  __syncthreads();
  const unsigned k0 = max(max(sk[0], sk[1]), max(sk[2], sk[3]));
  m = 0;
#pragma unroll
  for (int k = 0; k < 9; ++k) m = max(m, key[k] == k0 ? 0u : key[k]);
  m = wave_max_u32(m);
  if ((tid & 63) == 0) sk[4 + (tid >> 6)] = m;
  __syncthreads();
  const unsigned k1 = max(max(sk[4], sk[5]), max(sk[6], sk[7]));
  return (u32x2){(unsigned)__builtin_amdgcn_readfirstlane(k0), (unsigned)__builtin_amdgcn_readfirstlane(k1)};
}
__device__ __forceinline__ u32x2 top2_wave(const float* rbuf, int lane, bool dry, bool strict) {
  const __amdgpu_buffer_rsrc_t rrs = __builtin_amdgcn_make_buffer_rsrc((void*)rbuf, 0, S_ * 4, 0x00020000);
  asm volatile("" ::: "memory");
  u32x4 a[8];
#pragma unroll
  for (int i = 0; i < 8; ++i) a[i] = (u32x4){0u, 0u, 0u, 0u};
  if (!dry) {
#pragma unroll
    for (int i = 0; i < 8; ++i) a[i] = ld_sc1(rrs, (i * 64 + lane) * 16);
  }
  if (SNEED) {
    unsigned mx = 0;
#pragma unroll
    for (int i = 0; i < 8; ++i) {
      const unsigned x = max(max(a[i][0], a[i][1]), max(a[i][2], a[i][3]));
      mx = max(mx, i * 8 + (lane >> 3) < SNEED ? x : 0u);
    }
    if (strict && __builtin_amdgcn_ballot_w64(mx == ~0u)) return (u32x2){0u, 0u};
  }
  unsigned m1 = 0, m2 = 0;
#pragma unroll
  for (int i = 0; i < 32; ++i) {
    const unsigned w = a[i >> 2][i & 3], o = w ^ ((unsigned)((int)w >> 31) | 0x80000000u);
    const unsigned k = (o & 0xffff0000u) | ((0xffffu - (unsigned)(((i >> 2) * 64 + lane) * 4 + (i & 3))) & ~w);
    m2 = max(min(m1, k), m2);
    m1 = max(m1, k);
  }
  unsigned k[2];
  k[0] = wave_max_u32(m1);
  k[1] = wave_max_u32(m1 == k[0] ? m2 : m1);
  if (!k[1]) return (u32x2){0u, 0u};
#pragma unroll
  for (int i = 0; i < 2; ++i) k[i] = mkkey((k[i] >> 31) ? k[i] & 0x7fffffffu : ~k[i], 0xffff - (k[i] & 0xffff));
  return (u32x2){max(k[0], k[1]), min(k[0], k[1])};
}
__device__ __forceinline__ bool other_eval(const u32x4 a, const u32x4 b, int t8, unsigned k0, unsigned k1) {
  const unsigned o = k1 >> 16;
  const float v1 = u2f(((o & 0x8000u) ? o & 0x7fffu : ~o & 0xffffu) << 16);
  int n = 0;
#pragma unroll
  for (int k = 0; k < 4; ++k) n += (u2f(a[k]) >= v1) + (u2f(b[k]) >= v1);
  n -= (((0xffff - (k0 & 0xffff)) >> 3) * 8 == t8) + (((0xffff - (k1 & 0xffff)) >> 3) * 8 == t8);
  if (!RARE(!o || __builtin_amdgcn_ballot_w64(n != 0) != 0)) return false;
  bool bad = false;
  u32x4 x = a, y = b;
#pragma unroll 1
  for (int k = 0; k < 8; ++k) {
    const unsigned ka = mkkey(x[0], t8 + k);
    bad |= ka > k1 && ka != k0;
    x = (u32x4){x[1], x[2], x[3], y[0]}; y = (u32x4){y[1], y[2], y[3], 0u};
  }
  return __builtin_amdgcn_ballot_w64(bad) != 0;
}
__device__ __forceinline__ bool other_top2a(const float* rbuf, int tid, unsigned k0, unsigned k1, u32x4 a, u32x4 b) {
  const __amdgpu_buffer_rsrc_t rrs = __builtin_amdgcn_make_buffer_rsrc((void*)rbuf, 0, S_ * 4, 0x00020000);
  const int t8 = tid >= 224 ? 0 : tid * 8;
  if (RARE(__builtin_amdgcn_ballot_w64(max(max(max(a[0], a[1]), max(a[2], a[3])), max(max(b[0], b[1]), max(b[2], b[3]))) == ~0u) != 0)) {
#pragma unroll 1
    for (unsigned spins = 0; spins < (1u << 20); ++spins) {
      asm volatile("" ::: "memory");
      a = ld_sc1(rrs, t8 * 4); b = ld_sc1(rrs, t8 * 4 + 16);
      if (__builtin_amdgcn_ballot_w64(max(max(max(a[0], a[1]), max(a[2], a[3])), max(max(b[0], b[1]), max(b[2], b[3]))) == ~0u) == 0) break;
      __builtin_amdgcn_s_sleep(TSLEEP);
    }
  }
  return other_eval(a, b, t8, k0, k1);
}
__device__ __forceinline__ int other_top2b(const float* rbuf, int tid, u32x2& sel, bool bad, unsigned* sk, unsigned& tmp, unsigned f, unsigned w = ~0u) {
  tmp = 0;
  if (__builtin_amdgcn_ballot_w64(w == ~0u)) {
  if (RDLY && (f >> 16) == 63) {
    const unsigned t = f + RDLY - (3 - (tid >> 6)) * RDL2;
#pragma unroll 1
    while (((t - (unsigned)__builtin_amdgcn_s_memrealtime()) & 0xffffu) < 400u) __builtin_amdgcn_s_sleep(1);
  }
  const __amdgpu_buffer_rsrc_t rrs = __builtin_amdgcn_make_buffer_rsrc((void*)rbuf, 0, 2 * S_ * 4 + 256 * 128, 0x00020000);
  unsigned n;
  w = RDLY ? __builtin_amdgcn_raw_buffer_load_b32(rrs, 2 * S_ * 4 + tid * 128, 0, 16) : ~0u;
  if (RARE(!RDLY || __builtin_amdgcn_ballot_w64(w == ~0u))) {
  asm volatile("buffer_load_dword %1, %3, %4, 0 offen sc1\n\ts_sleep %5\n\t"
               "buffer_load_dword %1, %3, %4, 0 offen sc1\n\ts_sleep %5\n\t"
               "s_mov_b32 %2, 0x100000\n"
               "TP%=:\n\t"
               "buffer_load_dword %1, %3, %4, 0 offen sc1\n\t"
               "s_sub_u32 %2, %2, 1\n\t"
               "s_waitcnt vmcnt(2)\n\t"
               "v_mov_b32 %0, %1\n\t"
               "v_cmp_eq_u32 vcc, -1, %0\n\t"
               "s_cbranch_scc1 TQ%=\n\t"
               "s_cbranch_vccnz TP%=\n"
               "TQ%=:"
               : "=&v"(w), "=&v"(tmp), "=&s"(n) : "v"(2 * S_ * 4 + tid * 128), "s"(rrs), "n"(PSLEEP) : "memory", "vcc", "scc");
  }
  }
  unsigned k = mkkey(w, S_ - 256 + tid);
  k = k > sel[1] && k != sel[0] ? k : 0u;
  u32x2 m = {bad ? ~0u : 0u, 0u};
  if (RARE(!bad && __builtin_amdgcn_ballot_w64(k != 0))) { m[0] = wave_max_u32(k); m[1] = wave_max_u32(k == m[0] ? 0u : k); }
  if ((tid & 63) == 0) *(u32x2*)(sk + (tid >> 6) * 2) = m;
  LDS_BARRIER();
  const u32x4 v = *(const u32x4*)sk, x = *(const u32x4*)(sk + 4);
  const unsigned top = __builtin_amdgcn_readfirstlane(max(max(v[0], v[2]), max(x[0], x[2])));
  asm volatile("s_cmp_lg_u32 %0, 0\n\ts_cbranch_scc1 1f\n\ts_endpgm\n1:" :: "s"(top) : "scc");
  if (!RARE(top != 0)) return 0;
  if (top == ~0u) return 2;
  unsigned a = sel[0], b = sel[1];
#pragma unroll
  for (int i = 0; i < 8; ++i) { const unsigned c = i < 4 ? v[i & 3] : x[i & 3]; b = max(b, min(a, c)); a = max(a, c); }
  sel = (u32x2){(unsigned)__builtin_amdgcn_readfirstlane(a), (unsigned)__builtin_amdgcn_readfirstlane(b)};
  return 1;
}

constexpr int M1_WGS = FF / 16;
constexpr int L_WD = 131072;
__device__ __forceinline__ void wait_set(gu32* f) {
#pragma unroll 1
  for (unsigned spins = 0; spins < (1u << 18); ++spins) {
    if (__hip_atomic_load(f, RLX_AGENT) != ~0u) return;
    __builtin_amdgcn_s_sleep(4);
  }
}
__device__ __forceinline__ u32x2 wait_selt(char* ws, int b, unsigned& tm) {
  const __amdgpu_buffer_rsrc_t rs = __builtin_amdgcn_make_buffer_rsrc((void*)(ws + O_CNT), 0, 1024, 0x00020000);
  u32x4 v;
#pragma unroll 1
  for (unsigned spins = 0; spins < (1u << 18); ++spins) {
    asm volatile("" ::: "memory");
    v = ld_sc1(rs, (b & 7) * 64);
    if (v[1]) break;
    __builtin_amdgcn_s_sleep(FSLEEP);
  }
  tm = __builtin_amdgcn_readfirstlane(v[2]);
  return (u32x2){(unsigned)__builtin_amdgcn_readfirstlane(v[0]), (unsigned)__builtin_amdgcn_readfirstlane(v[1])};
}
__device__ __forceinline__ u32x2 wait_sel(gu32* f, u64 v = 0) {
#pragma unroll 1
  for (unsigned spins = 0; spins < (1u << 18) && !v; ++spins) {
    v = __hip_atomic_load((gu64*)f, RLX_AGENT);
    if (v) break;
    __builtin_amdgcn_s_sleep(FSLEEP);
  }
  return (u32x2){(unsigned)__builtin_amdgcn_readfirstlane((unsigned)v), (unsigned)__builtin_amdgcn_readfirstlane((unsigned)(v >> 32))};
}
__device__ __forceinline__ unsigned code_ahead(int tid) {
  unsigned d = 0;
  if (MPF) { u64 pc; asm volatile("s_getpc_b64 %1\n\tglobal_load_dword %0, %2, %1" : "=v"(d), "=&s"(pc) : "v"((tid & 127) * 64) : "memory"); }
  return d;
}
__device__ __forceinline__ bf16x8 as_frag(u32x4 v) { return __builtin_bit_cast(bf16x8, v); }
__device__ __forceinline__ u32x4 tr_frag(char* p0, char* p1) {
  const s16x4 a = __builtin_amdgcn_ds_read_tr16_b64_v4i16((LAS s16x4*)p0), b = __builtin_amdgcn_ds_read_tr16_b64_v4i16((LAS s16x4*)p1);
  const u32x2 x = __builtin_bit_cast(u32x2, a), y = __builtin_bit_cast(u32x2, b);
  return (u32x4){x[0], x[1], y[0], y[1]};
}
__device__ __forceinline__ void mlp1_role(KArgs P, char* lds, int b, int tid) {
  float* part = (float*)lds;
  unsigned* si = (unsigned*)(lds + 4096);
  const int lane = tid & 63, wv = tid >> 6, l15 = lane & 15, l4 = lane >> 4;
  const __amdgpu_buffer_rsrc_t lrs = __builtin_amdgcn_make_buffer_rsrc((void*)(P->ws + O_LNH), 0, S_ * DM * 2, 0x00020000);
  const __amdgpu_buffer_rsrc_t mrs = __builtin_amdgcn_make_buffer_rsrc((void*)(P->ws + O_MFRAG), 0, 2 * DM * FF * 2, 0x00020000);
  u32x4 aw[2][4];
  MS(0);
  unsigned ca = code_ahead(tid);
  const u64 s0 = __hip_atomic_load((gu64*)(P->ws + O_CNT + (b & 7) * 64), RLX_AGENT);
#pragma unroll
  for (int i = 0; i < 8; ++i) aw[i >> 2][i & 3] = ld_sc1(mrs, (unsigned)(b * 32768 + (i >> 2) * 16384 + (wv * 4 + (i & 3)) * 1024 + lane * 16));
  u32x2 sel = wait_sel((gu32*)(P->ws + O_CNT) + (b & 7) * 16, s0);
  const unsigned fl = __hip_atomic_load((gu32*)(P->ws + O_KVSTEP + (b >> 2) * 64 + (b & 3) * 4), RLX_AGENT);
  asm volatile("" :: "v"(ca));
  MS(1);
  int round = 0;
#pragma unroll 1
  for (;;) {
    const int idx = 0xffff - (((l15 & 1) ? sel[1] : sel[0]) & 0xffff);
    if (RARE(round == 2)) wait_set((gu32*)(P->ws + O_RBUF) + S_ + idx);
    u32x4 bx[4];
#pragma unroll
    for (int i = 0; i < 4; ++i) bx[i] = ld_sc1(lrs, (unsigned)(idx * 1024 + (wv * 4 + i) * 64 + l4 * 16));
    f32x4 ag = {0.f, 0.f, 0.f, 0.f}, au = {0.f, 0.f, 0.f, 0.f};
#pragma unroll
    for (int i = 0; i < 4; ++i) {
      ag = __builtin_amdgcn_mfma_f32_16x16x32_bf16(as_frag(aw[0][i]), as_frag(bx[i]), ag, 0, 0, 0);
      au = __builtin_amdgcn_mfma_f32_16x16x32_bf16(as_frag(aw[1][i]), as_frag(bx[i]), au, 0, 0, 0);
    }
    if (l15 < 2) {
      *(f32x4*)(part + wv * 64 + l15 * 16 + l4 * 4) = ag;
      *(f32x4*)(part + wv * 64 + 32 + l15 * 16 + l4 * 4) = au;
    }
    LDS_BARRIER();
    MS(2);
    if (tid < 16) {
      float a[2];
#pragma unroll
      for (int e = 0; e < 2; ++e) {
        const float* q = part + tid * 2 + e;
        const float g = rbf((q[0] + q[64]) + (q[128] + q[192])), u = rbf((q[32] + q[96]) + (q[160] + q[224]));
        a[e] = rbf(g / (1.0f + __expf(-g))) * u;
      }
      unsigned av = pk2(a[0], a[1]);
      av = av == ~0u ? 0x7fc07fc0u : av;
      __hip_atomic_store((gu32*)(P->ws + O_ACT) + (round * M1_WGS + b) * 32 + tid, av, RLX_AGENT);
    }
    MS(4);
    if (RARE(round == 2)) break;
    if (SW2 && !round) {
      const u32x2 s2 = wait_sel((gu32*)(P->ws + O_SEL2) + (b & 31) * 32);
      if (s2[0] != sel[0] || s2[1] != sel[1]) { sel = s2; round = 1; __syncthreads(); continue; }
    }
    unsigned tmp;
    const __amdgpu_buffer_rsrc_t rrs = __builtin_amdgcn_make_buffer_rsrc((void*)(P->ws + O_RBUF), 0, S_ * 4, 0x00020000);
    const int t8 = tid >= 224 ? 0 : tid * 8;
    const int again = other_top2b((const float*)(P->ws + O_RBUF), tid, sel, other_top2a((const float*)(P->ws + O_RBUF), tid, sel[0], sel[1], ld_sc1(rrs, t8 * 4), ld_sc1(rrs, t8 * 4 + 16)), si, tmp, __builtin_amdgcn_readfirstlane(fl));
    MS(5);
    if (!RARE(again)) break;
    asm volatile("s_waitcnt vmcnt(0)" :: "v"(tmp) : "memory");
    if (again == 2) sel = top2<true>((const float*)(P->ws + O_RBUF), tid, S_, si + 8);
    round = 2;
  }
}
__device__ __forceinline__ void mlp2_role(KArgs P, char* lds, int b, int tid) {
  float* part = (float*)lds;
  unsigned* si = (unsigned*)(lds + 4096);
  const int lane = tid & 63, wv = tid >> 6, l15 = lane & 15, l4 = lane >> 4;
  const __amdgpu_buffer_rsrc_t hrs = __builtin_amdgcn_make_buffer_rsrc((void*)(P->ws + O_HBUF), 0, S_ * DM * 2, 0x00020000);
  const __amdgpu_buffer_rsrc_t lrs = __builtin_amdgcn_make_buffer_rsrc((void*)(P->ws + O_LNH), 0, S_ * DM * 2, 0x00020000);
  const __amdgpu_buffer_rsrc_t ors = __builtin_amdgcn_make_buffer_rsrc((void*)P->out, 0, S_ * DM * 2, 0x00020000);
  const __amdgpu_buffer_rsrc_t ars = __builtin_amdgcn_make_buffer_rsrc((void*)(P->ws + O_ACT), 0, 3 * M1_WGS * 128, 0x00020000);
  u32x4 aw[7];
  const int c0 = b * 8;
  MS(0);
  unsigned ca = code_ahead(tid);
  {
    char* p = lds + L_WD + (((l15 >> 1) & 1) * 896 + wv * 224 + l4 * 8 + (l15 >> 2)) * 16 + (l15 & 1) * 8;
#pragma unroll
    for (int i = 0; i < 7; ++i) aw[i] = tr_frag(p + i * 512, p + i * 512 + 64);
  }
  unsigned tsel;
  u32x2 sel = wait_selt(P->ws, b, tsel);
  asm volatile("" :: "v"(ca));
  const unsigned fl = __hip_atomic_load((gu32*)(P->ws + O_KVSTEP + (c0 >> 4) * 64), RLX_AGENT);
  MS(1);
  int round = 0;
#pragma unroll 1
  for (;;) {
    const int idx = 0xffff - (((tid & 8) ? sel[1] : sel[0]) & 0xffff), col = c0 + (tid & 7);
    if (RARE(round == 2)) wait_set((gu32*)(P->ws + O_RBUF) + S_ + idx);
    if (ADLY) {
#pragma unroll 1
      while (tsel + ADLY - (unsigned)__builtin_amdgcn_s_memrealtime() < 400u) __builtin_amdgcn_s_sleep(1);
    }
    const float rr = __hip_atomic_load((const GAS float*)(P->ws + O_RBUF) + S_ + idx, RLX_AGENT);
    const u16 lw = __builtin_amdgcn_raw_buffer_load_b16(lrs, (unsigned)(idx * DM + col) * 2, 0, 16);
    const u16 hw = __builtin_amdgcn_raw_buffer_load_b16(hrs, (unsigned)(idx * DM + col) * 2, 0, 16);
    const __amdgpu_buffer_rsrc_t rrs = __builtin_amdgcn_make_buffer_rsrc((void*)(P->ws + O_RBUF), 0, 2 * S_ * 4 + 256 * 128, 0x00020000);
    const int t8 = tid >= 224 ? 0 : tid * 8;
    const u32x4 pa = ld_sc1(rrs, t8 * 4), pb = ld_sc1(rrs, t8 * 4 + 16);
    unsigned pw = ~0u;
    if (wv == 0) pw = __builtin_amdgcn_raw_buffer_load_b32(rrs, 2 * S_ * 4 + tid * 128, 0, 16);
    u32x4 av[7];
    u64 v2 = 0;
#pragma unroll 1
    for (unsigned spins = 0; spins < (1u << 20); ++spins) {
      asm volatile("" ::: "memory");
      unsigned mx = 0;
      if (SW2) v2 = __hip_atomic_load((gu64*)(P->ws + O_SEL2 + (b & 31) * 128), RLX_AGENT);
#pragma unroll
      for (int i = 0; i < 7; ++i) {
        av[i] = ld_sc1(ars, (unsigned)((round * M1_WGS + ((l15 >> 1) & 1) * 56 + (wv * 7 + i) * 2 + (l4 >> 1)) * 128 + (l15 & 1) * 32 + (l4 & 1) * 16));
        mx = max(max(mx, max(av[i][0], av[i][1])), max(av[i][2], av[i][3]));
      }
      if (!RARE(__builtin_amdgcn_ballot_w64(mx == ~0u) != 0)) break;
      __builtin_amdgcn_s_sleep(4);
    }
    MS(2);
    if (SW2 && !round) {
      const u32x2 s2 = wait_sel((gu32*)(P->ws + O_SEL2) + (b & 31) * 32, v2);
      if (s2[0] != sel[0] || s2[1] != sel[1]) { sel = s2; round = 1; continue; }
    }
    const float lv = bf2f(lw), hv = bf2f(hw), sg = rbf(1.0f / (1.0f + __expf(-rr)));
    f32x4 a0 = {0.f, 0.f, 0.f, 0.f}, a1 = {0.f, 0.f, 0.f, 0.f};
#pragma unroll
    for (int i = 0; i < 7; ++i) {
      if (i & 1) a1 = __builtin_amdgcn_mfma_f32_16x16x32_bf16(as_frag(aw[i]), as_frag(av[i]), a1, 0, 0, 0);
      else a0 = __builtin_amdgcn_mfma_f32_16x16x32_bf16(as_frag(aw[i]), as_frag(av[i]), a0, 0, 0, 0);
    }
    if ((l15 >> 1) == (l4 >> 1)) *(f32x4*)(part + wv * 32 + l15 * 8 + (l4 & 1) * 4) = a0 + a1;
    LDS_BARRIER();
    const float* q = part + (tid & 15);
    const float y = rbf(((q[0] + q[16]) + (q[32] + q[48])) + ((q[64] + q[80]) + (q[96] + q[112])));
    const float upd = rbf(y * sg);
    const float bb = rbf(lv + upd);
    const u16 res = f2bf(hv + bb);
    MS(4);
    if (RARE(round == 2)) VM_DRAIN();
    if (tid < 16) __builtin_amdgcn_raw_buffer_store_b16(res, ors, (unsigned)(idx * DM + col) * 2, 0, 16);
    if (RARE(round == 2)) break;
    unsigned tmp;
    const bool bad = other_top2a((const float*)(P->ws + O_RBUF), tid, sel[0], sel[1], pa, pb);
    const int again = other_top2b((const float*)(P->ws + O_RBUF), tid, sel, bad, si, tmp, __builtin_amdgcn_readfirstlane(fl), pw);
    MS(5);
    if (!RARE(again)) break;
    asm volatile("s_waitcnt vmcnt(0)" :: "v"(tmp) : "memory");
    if (tid < 16) __builtin_amdgcn_raw_buffer_store_b16(f2bf(hv + lv), ors, (unsigned)(idx * DM + col) * 2, 0, 16);
    if (again == 2) sel = top2<true>((const float*)(P->ws + O_RBUF), tid, S_, si + 8);
    tsel = (unsigned)__builtin_amdgcn_s_memrealtime() + RDO - ADLY;
    round = 2;
  }
}

struct WoRegs { u32x32 a, b, c, d; };
__device__ __forceinline__ unsigned worker_role(int& w, int& wv, KArgs& A, WoRegs& wo) {
  unsigned tag;
  asm volatile(
      "GSTART:\n\t"
      "v_mbcnt_lo_u32_b32 v96, -1, 0\n\t"
      "v_mbcnt_hi_u32_b32 v96, -1, v96\n\t"
      "s_and_b32 s82, %0, 31\n\t"
      "s_mov_b32 s66, 4194304\n\t"
      "s_mov_b32 s67, 0x20000\n\t"
      "s_mov_b32 s62, 111181824\n\t"
      "s_mov_b32 s63, 0x20000\n\t"
      "v_and_b32 v104, 3, v96\n\t"
      "v_lshrrev_b32 v105, 2, v96\n\t"
      "s_lshl_b32 s78, %0, 14\n\t"
      "s_lshl_b32 s79, %1, 12\n\t"
      "s_add_u32 s79, s79, s78\n\t"
      "v_lshlrev_b32 v106, 10, v104\n\t"
      "v_lshl_add_u32 v106, v105, 4, v106\n\t"
      "v_lshlrev_b32 v99, 13, v105\n\t"
      "v_lshl_add_u32 v99, v104, 4, v99\n\t"
      "s_lshl_b32 s80, s82, 8\n\t"
      "s_lshl_b32 s72, %1, 6\n\t"
      "s_add_u32 s80, s80, s72\n\t"
      "v_add_u32 v99, s80, v99\n\t"
      "s_bitcmp1_b32 %0, 5\n\t"
      "s_waitcnt lgkmcnt(0)\n\t"
      "s_mov_b64 s[64:65], s[36:37]\n\t"
      "s_mov_b64 s[60:61], s[58:59]\n\t"
      "buffer_load_dwordx4 v[64:67], v106, s[64:67], s79 offen \n\t"
      "buffer_load_dwordx4 v[68:71], v106, s[64:67], s79 offen offset:256 \n\t"
      "buffer_load_dwordx4 v[72:75], v106, s[64:67], s79 offen offset:512 \n\t"
      "buffer_load_dwordx4 v[76:79], v106, s[64:67], s79 offen offset:768 \n\t"
      "s_cselect_b64 s[38:39], s[44:45], s[42:43]\n\t"
      "s_mov_b32 s78, 0\n\t"
      "s_load_dword s87, s[38:39], s78\n\t"
      "s_mov_b32 s78, 4194300\n\t"
      "s_load_dword s87, s[38:39], s78\n\t"
      "s_mov_b32 s78, 128\n\t"
      "s_load_dword s87, s[60:61], s78\n\t"
      "s_mov_b32 s78, 6520704\n\t"
      "s_load_dword s87, s[60:61], s78\n\t"
      "v_mov_b32 v118, v106\n\t"
      "s_mov_b64 s[64:65], s[36:37]\n\t"
      "s_lshr_b32 s78, %0, 6\n\t"
      "s_lshl_b32 s78, s78, 15\n\t"
      "s_lshl_b32 s79, %1, 12\n\t"
      "s_add_u32 s79, s79, s78\n\t"
      "buffer_load_dwordx4 v[128:131], v118, s[64:67], s79 offen \n\t"
      "buffer_load_dwordx4 v[132:135], v118, s[64:67], s79 offen offset:256 \n\t"
      "buffer_load_dwordx4 v[136:139], v118, s[64:67], s79 offen offset:512 \n\t"
      "buffer_load_dwordx4 v[140:143], v118, s[64:67], s79 offen offset:768 \n\t"
      "s_add_u32 s79, s79, 16384\n\t"
      "buffer_load_dwordx4 v[144:147], v118, s[64:67], s79 offen \n\t"
      "buffer_load_dwordx4 v[148:151], v118, s[64:67], s79 offen offset:256 \n\t"
      "buffer_load_dwordx4 v[152:155], v118, s[64:67], s79 offen offset:512 \n\t"
      "buffer_load_dwordx4 v[156:159], v118, s[64:67], s79 offen offset:768 \n\t"
      "s_mov_b64 s[64:65], s[38:39]\n\t"
      "s_mov_b64 s[64:65], s[38:39]\n\t"
      "buffer_load_dword v107, off, s[60:63], 0 sc1\n\t"
      "v_lshrrev_b32 v101, 4, v96\n\t"
      "v_and_b32 v102, 15, v96\n\t"
      "v_lshlrev_b32 v103, 2, v101\n\t"
      "v_xor_b32 v102, v102, v103\n\t"
      "v_lshlrev_b32 v108, 13, v101\n\t"
      "v_lshl_add_u32 v108, v102, 4, v108\n\t"
      "s_lshl_b32 s78, s82, 8\n\t"
      "s_lshl_b32 s79, %1, 16\n\t"
      "s_add_u32 s78, s78, s79\n\t"
      "v_add_u32 v108, s78, v108\n\t"
      "s_lshl_b32 s76, %1, 11\n\t"
      "s_mov_b32 s77, 0\n\t"
      "s_add_u32 m0, s76, 0\n\t"
      "s_add_u32 s68, s77, 0\n\t"
      "buffer_load_dwordx4 v108, s[64:67], s68 offen lds\n\t"
      "s_add_u32 m0, s76, 1024\n\t"
      "s_add_u32 s68, s77, 32768\n\t"
      "s_add_u32 s76, s76, 8192\n\t"
      "s_add_u32 s77, s77, 0x40000\n\t"
      "buffer_load_dwordx4 v108, s[64:67], s68 offen lds\n\t"
      "s_mov_b32 s84, 0xffff0000\n\t"
      "s_mov_b32 s86, 0\n\t"
      "s_lshl_b32 s85, %1, 13\n\t"
      "v_lshlrev_b32 v97, 4, v96\n\t"
      "v_lshrrev_b32 v101, 4, v96\n\t"
      "v_and_b32 v102, 3, v105\n\t"
      "v_lshlrev_b32 v100, 9, v101\n\t"
      "v_lshl_add_u32 v100, v102, 6, v100\n\t"
      "v_lshl_add_u32 v100, v104, 4, v100\n\t"
      "v_lshlrev_b32 v106, 10, v101\n\t"
      "v_lshl_add_u32 v106, v102, 8, v106\n\t"
      "v_lshl_add_u32 v106, v104, 4, v106\n\t"
      "s_lshl_b32 s74, %0, 14\n\t"
      "s_add_u32 s74, s74, s72\n\t"
      "s_add_u32 s74, s74, 6520832\n\t"
      "s_lshr_b32 s78, %0, 6\n\t"
      "v_min_u32 v104, 31, v96\n\t"
      "v_lshl_add_u32 v104, v104, 1, s78\n\t"
      "s_lshr_b32 s79, %0, 5\n\t"
      "s_and_b32 s83, s79, 1\n\t"
      "s_add_u32 s80, s85, 6520832\n\t"
      "v_lshl_add_u32 v101, v104, 15, s80\n\t"
      "s_mul_i32 s80, s82, 24576\n\t"
      "s_lshl_b32 s72, %1, 11\n\t"
      "s_add_u32 s80, s80, s72\n\t"
      "s_add_u32 s80, s80, 8617984\n\t"
      "s_lshl_b32 s72, s83, 13\n\t"
      "s_add_u32 s80, s80, s72\n\t"
      "v_mov_b32 v102, s80\n\t"
      "s_mov_b32 s72, 786432\n\t"
      "v_mad_u32_u24 v102, v104, s72, v102\n\t"
      "s_lshl_b32 s80, s82, 6\n\t"
      "s_lshl_b32 s72, s83, 2\n\t"
      "s_add_u32 s80, s80, s72\n\t"
      "s_add_u32 s80, s80, 256\n\t"
      "v_lshrrev_b32 v105, 2, v104\n\t"
      "v_lshl_add_u32 v103, v105, 11, s80\n\t"
      "v_and_b32 v104, 3, v104\n\t"
      "v_lshl_add_u32 v103, v104, 4, v103\n\t"
      "s_lshl_b32 s80, s82, 7\n\t"
      "s_lshl_b32 s79, s79, 2\n\t"
      "s_add_u32 s80, s80, s79\n\t"
      "s_add_u32 s80, s80, 33024\n\t"
      "v_writelane_b32 v103, s80, 0\n\t"
      "v_writelane_b32 v103, 0, 63\n\t"
      "s_cmp_eq_u32 %1, 0\n\t"
      "s_cselect_b64 s[90:91], 1, 0\n\t"
      "v_lshrrev_b32 v104, 4, v96\n\t"
      "v_bfe_u32 v105, v96, 2, 2\n\t"
      "v_and_b32 v110, 3, v96\n\t"
      "s_lshl_b32 s78, %1, 2\n\t"
      "v_lshl_add_u32 v109, v104, 3, v105\n\t"
      "v_lshlrev_b32 v109, 8, v109\n\t"
      "v_or_b32 v110, s78, v110\n\t"
      "v_lshlrev_b32 v105, 2, v105\n\t"
      "v_xor_b32 v110, v110, v105\n\t"
      "v_lshl_add_u32 v109, v110, 4, v109\n\t"
      "s_lshl_b32 s78, %1, 6\n\t"
      "s_add_u32 s78, s78, 131072\n\t"
      "v_add_u32 v110, s78, v106\n\t"
      "s_bfe_u32 s80, %0, 0x10005\n\t"
      "s_and_b32 s79, %1, 1\n\t"
      "v_bfe_u32 v105, v96, 4, 1\n\t"
      "v_lshlrev_b32 v105, 1, v105\n\t"
      "v_and_b32 v116, 7, v96\n\t"
      "v_xor_b32 v116, v116, v105\n\t"
      "s_lshl_b32 s78, s79, 2\n\t"
      "v_xor_b32 v116, s78, v116\n\t"
      "v_lshlrev_b32 v116, 4, v116\n\t"
      "v_lshrrev_b32 v105, 3, v96\n\t"
      "v_lshl_add_u32 v116, v105, 13, v116\n\t"
      "s_lshl_b32 s78, s82, 8\n\t"
      "s_lshl_b32 s72, s80, 7\n\t"
      "s_add_u32 s78, s78, s72\n\t"
      "s_lshl_b32 s72, %1, 16\n\t"
      "s_add_u32 s78, s78, s72\n\t"
      "v_add_u32 v116, s78, v116\n\t"
      "v_bfe_u32 v105, v96, 2, 2\n\t"
      "v_lshl_add_u32 v117, v104, 3, v105\n\t"
      "v_lshlrev_b32 v117, 7, v117\n\t"
      "v_bfe_u32 v105, v96, 3, 2\n\t"
      "v_xor_b32 v105, %1, v105\n\t"
      "v_lshl_add_u32 v117, v105, 5, v117\n\t"
      "v_and_b32 v105, 3, v96\n\t"
      "v_lshl_add_u32 v117, v105, 3, v117\n\t"
      "v_and_b32 v111, 15, v96\n\t"
      "v_lshlrev_b32 v111, 4, v111\n\t"
      "v_and_b32 v105, 1, v104\n\t"
      "v_lshl_add_u32 v111, v105, 10, v111\n\t"
      "v_lshrrev_b32 v105, 1, v104\n\t"
      "v_lshl_add_u32 v111, v105, 8, v111\n\t"
      "s_lshl_b32 s78, s79, 9\n\t"
      "s_add_u32 s78, s78, 16384\n\t"
      "s_lshl_b32 s80, s80, 12\n\t"
      "s_sub_u32 s78, s78, s80\n\t"
      "s_add_u32 s80, %1, 1\n\t"
      "s_lshr_b32 s80, s80, 1\n\t"
      "s_lshl_b32 s80, s80, 11\n\t"
      "s_sub_u32 s78, s78, s80\n\t"
      "v_add_u32 v111, s78, v111\n\t"
      "s_mov_b32 s69, 0\n\t"
      "s_add_u32 m0, s76, 0\n\t"
      "s_add_u32 s68, s77, 0\n\t"
      "buffer_load_dwordx4 v108, s[64:67], s68 offen lds\n\t"
      "s_add_u32 m0, s76, 1024\n\t"
      "s_add_u32 s68, s77, 32768\n\t"
      "s_add_u32 s76, s76, 8192\n\t"
      "s_add_u32 s77, s77, 0x40000\n\t"
      "buffer_load_dwordx4 v108, s[64:67], s68 offen lds\n\t"
      "s_add_u32 m0, s76, 0\n\t"
      "s_add_u32 s68, s77, 0\n\t"
      "buffer_load_dwordx4 v108, s[64:67], s68 offen lds\n\t"
      "s_add_u32 m0, s76, 1024\n\t"
      "s_add_u32 s68, s77, 32768\n\t"
      "s_add_u32 s76, s76, 8192\n\t"
      "s_add_u32 s77, s77, 0x40000\n\t"
      "buffer_load_dwordx4 v108, s[64:67], s68 offen lds\n\t"
      "s_add_u32 m0, s76, 0\n\t"
      "s_add_u32 s68, s77, 0\n\t"
      "buffer_load_dwordx4 v108, s[64:67], s68 offen lds\n\t"
      "s_add_u32 m0, s76, 1024\n\t"
      "s_add_u32 s68, s77, 32768\n\t"
      "s_add_u32 s76, s76, 8192\n\t"
      "s_add_u32 s77, s77, 0x40000\n\t"
      "buffer_load_dwordx4 v108, s[64:67], s68 offen lds\n\t"
      "s_mov_b64 s[88:89], 0\n\t"
      "s_waitcnt vmcnt(8)\n\t"
      "GLN%=:\n\t"
      "v_and_b32 v1, s84, v64\n\t"
      "v_lshlrev_b32 v0, 16, v64\n\t"
      "v_and_b32 v3, s84, v65\n\t"
      "v_lshlrev_b32 v2, 16, v65\n\t"
      "v_and_b32 v5, s84, v66\n\t"
      "v_lshlrev_b32 v4, 16, v66\n\t"
      "v_and_b32 v7, s84, v67\n\t"
      "v_lshlrev_b32 v6, 16, v67\n\t"
      "v_and_b32 v9, s84, v68\n\t"
      "v_lshlrev_b32 v8, 16, v68\n\t"
      "v_and_b32 v11, s84, v69\n\t"
      "v_lshlrev_b32 v10, 16, v69\n\t"
      "v_and_b32 v13, s84, v70\n\t"
      "v_lshlrev_b32 v12, 16, v70\n\t"
      "v_and_b32 v15, s84, v71\n\t"
      "v_lshlrev_b32 v14, 16, v71\n\t"
      "s_mov_b64 exec, s[88:89]\n\t"
      "s_add_u32 m0, s76, 0\n\t"
      "s_add_u32 s68, s77, 0\n\t"
      "buffer_load_dwordx4 v108, s[64:67], s68 offen lds\n\t"
      "s_mov_b64 exec, -1\n\t"
      "v_and_b32 v17, s84, v72\n\t"
      "v_lshlrev_b32 v16, 16, v72\n\t"
      "v_and_b32 v19, s84, v73\n\t"
      "v_lshlrev_b32 v18, 16, v73\n\t"
      "v_and_b32 v21, s84, v74\n\t"
      "v_lshlrev_b32 v20, 16, v74\n\t"
      "v_and_b32 v23, s84, v75\n\t"
      "v_lshlrev_b32 v22, 16, v75\n\t"
      "v_and_b32 v25, s84, v76\n\t"
      "v_lshlrev_b32 v24, 16, v76\n\t"
      "v_and_b32 v27, s84, v77\n\t"
      "v_lshlrev_b32 v26, 16, v77\n\t"
      "v_and_b32 v29, s84, v78\n\t"
      "v_lshlrev_b32 v28, 16, v78\n\t"
      "v_and_b32 v31, s84, v79\n\t"
      "v_lshlrev_b32 v30, 16, v79\n\t"
      "s_mov_b64 exec, s[88:89]\n\t"
      "s_add_u32 m0, s76, 1024\n\t"
      "s_add_u32 s68, s77, 32768\n\t"
      "s_add_u32 s76, s76, 8192\n\t"
      "s_add_u32 s77, s77, 0x40000\n\t"
      "buffer_load_dwordx4 v108, s[64:67], s68 offen lds\n\t"
      "s_mov_b64 exec, -1\n\t"
      "v_pk_add_f32 v[32:33], v[0:1], v[2:3]\n\t"
      "v_pk_add_f32 v[34:35], v[4:5], v[6:7]\n\t"
      "v_pk_add_f32 v[36:37], v[8:9], v[10:11]\n\t"
      "v_pk_add_f32 v[38:39], v[12:13], v[14:15]\n\t"
      "v_pk_add_f32 v[40:41], v[16:17], v[18:19]\n\t"
      "v_pk_add_f32 v[42:43], v[20:21], v[22:23]\n\t"
      "v_pk_add_f32 v[44:45], v[24:25], v[26:27]\n\t"
      "v_pk_add_f32 v[46:47], v[28:29], v[30:31]\n\t"
      "v_pk_add_f32 v[32:33], v[32:33], v[40:41]\n\t"
      "v_pk_add_f32 v[34:35], v[34:35], v[42:43]\n\t"
      "v_pk_add_f32 v[36:37], v[36:37], v[44:45]\n\t"
      "v_pk_add_f32 v[38:39], v[38:39], v[46:47]\n\t"
      "v_pk_add_f32 v[32:33], v[32:33], v[36:37]\n\t"
      "v_pk_add_f32 v[34:35], v[34:35], v[38:39]\n\t"
      "v_pk_add_f32 v[32:33], v[32:33], v[34:35]\n\t"
      "v_add_f32 v32, v32, v33\n\t"
      "s_mov_b64 exec, s[88:89]\n\t"
      "s_add_u32 m0, s76, 0\n\t"
      "s_add_u32 s68, s77, 0\n\t"
      "buffer_load_dwordx4 v108, s[64:67], s68 offen lds\n\t"
      "s_mov_b64 exec, -1\n\t"
      "v_add_f32_dpp v32, v32, v32 row_ror:4 row_mask:0xf bank_mask:0xf\n\t"
      "s_nop 1\n\t"
      "v_add_f32_dpp v32, v32, v32 row_ror:8 row_mask:0xf bank_mask:0xf\n\t"
      "v_mov_b32 v33, v32\n\t"
      "s_nop 1\n\t"
      "v_permlane16_swap_b32 v32, v33\n\t"
      "v_add_f32 v32, v32, v33\n\t"
      "v_mov_b32 v33, v32\n\t"
      "s_nop 1\n\t"
      "v_permlane32_swap_b32 v32, v33\n\t"
      "v_add_f32 v32, v32, v33\n\t"
      "v_mul_f32 v80, 0x3b000000, v32\n\t"
      "v_pk_add_f32 v[32:33], v[0:1], v[80:81] op_sel_hi:[1,0] neg_lo:[0,1] neg_hi:[0,1]\n\t"
      "v_pk_add_f32 v[34:35], v[2:3], v[80:81] op_sel_hi:[1,0] neg_lo:[0,1] neg_hi:[0,1]\n\t"
      "v_pk_add_f32 v[36:37], v[4:5], v[80:81] op_sel_hi:[1,0] neg_lo:[0,1] neg_hi:[0,1]\n\t"
      "v_pk_add_f32 v[38:39], v[6:7], v[80:81] op_sel_hi:[1,0] neg_lo:[0,1] neg_hi:[0,1]\n\t"
      "v_pk_add_f32 v[40:41], v[8:9], v[80:81] op_sel_hi:[1,0] neg_lo:[0,1] neg_hi:[0,1]\n\t"
      "v_pk_add_f32 v[42:43], v[10:11], v[80:81] op_sel_hi:[1,0] neg_lo:[0,1] neg_hi:[0,1]\n\t"
      "v_pk_add_f32 v[44:45], v[12:13], v[80:81] op_sel_hi:[1,0] neg_lo:[0,1] neg_hi:[0,1]\n\t"
      "s_mov_b64 exec, s[88:89]\n\t"
      "s_add_u32 m0, s76, 1024\n\t"
      "s_add_u32 s68, s77, 32768\n\t"
      "s_add_u32 s76, s76, 8192\n\t"
      "s_add_u32 s77, s77, 0x40000\n\t"
      "buffer_load_dwordx4 v108, s[64:67], s68 offen lds\n\t"
      "s_mov_b64 exec, -1\n\t"
      "v_pk_add_f32 v[46:47], v[14:15], v[80:81] op_sel_hi:[1,0] neg_lo:[0,1] neg_hi:[0,1]\n\t"
      "v_pk_add_f32 v[48:49], v[16:17], v[80:81] op_sel_hi:[1,0] neg_lo:[0,1] neg_hi:[0,1]\n\t"
      "v_pk_add_f32 v[50:51], v[18:19], v[80:81] op_sel_hi:[1,0] neg_lo:[0,1] neg_hi:[0,1]\n\t"
      "v_pk_add_f32 v[52:53], v[20:21], v[80:81] op_sel_hi:[1,0] neg_lo:[0,1] neg_hi:[0,1]\n\t"
      "v_pk_add_f32 v[54:55], v[22:23], v[80:81] op_sel_hi:[1,0] neg_lo:[0,1] neg_hi:[0,1]\n\t"
      "v_pk_add_f32 v[56:57], v[24:25], v[80:81] op_sel_hi:[1,0] neg_lo:[0,1] neg_hi:[0,1]\n\t"
      "v_pk_add_f32 v[58:59], v[26:27], v[80:81] op_sel_hi:[1,0] neg_lo:[0,1] neg_hi:[0,1]\n\t"
      "v_pk_add_f32 v[60:61], v[28:29], v[80:81] op_sel_hi:[1,0] neg_lo:[0,1] neg_hi:[0,1]\n\t"
      "v_pk_add_f32 v[62:63], v[30:31], v[80:81] op_sel_hi:[1,0] neg_lo:[0,1] neg_hi:[0,1]\n\t"
      "v_pk_mul_f32 v[64:65], v[32:33], v[32:33]\n\t"
      "v_pk_mul_f32 v[66:67], v[34:35], v[34:35]\n\t"
      "v_pk_mul_f32 v[68:69], v[36:37], v[36:37]\n\t"
      "v_pk_mul_f32 v[70:71], v[38:39], v[38:39]\n\t"
      "v_pk_fma_f32 v[64:65], v[40:41], v[40:41], v[64:65]\n\t"
      "v_pk_fma_f32 v[66:67], v[42:43], v[42:43], v[66:67]\n\t"
      "v_pk_fma_f32 v[68:69], v[44:45], v[44:45], v[68:69]\n\t"
      "s_mov_b64 exec, s[88:89]\n\t"
      "s_add_u32 m0, s76, 0\n\t"
      "s_add_u32 s68, s77, 0\n\t"
      "buffer_load_dwordx4 v108, s[64:67], s68 offen lds\n\t"
      "s_mov_b64 exec, -1\n\t"
      "v_pk_fma_f32 v[70:71], v[46:47], v[46:47], v[70:71]\n\t"
      "v_pk_fma_f32 v[64:65], v[48:49], v[48:49], v[64:65]\n\t"
      "v_pk_fma_f32 v[66:67], v[50:51], v[50:51], v[66:67]\n\t"
      "v_pk_fma_f32 v[68:69], v[52:53], v[52:53], v[68:69]\n\t"
      "v_pk_fma_f32 v[70:71], v[54:55], v[54:55], v[70:71]\n\t"
      "v_pk_fma_f32 v[64:65], v[56:57], v[56:57], v[64:65]\n\t"
      "v_pk_fma_f32 v[66:67], v[58:59], v[58:59], v[66:67]\n\t"
      "v_pk_fma_f32 v[68:69], v[60:61], v[60:61], v[68:69]\n\t"
      "v_pk_fma_f32 v[70:71], v[62:63], v[62:63], v[70:71]\n\t"
      "v_pk_add_f32 v[64:65], v[64:65], v[66:67]\n\t"
      "v_pk_add_f32 v[68:69], v[68:69], v[70:71]\n\t"
      "v_pk_add_f32 v[64:65], v[64:65], v[68:69]\n\t"
      "v_add_f32 v64, v64, v65\n\t"
      "s_nop 1\n\t"
      "v_add_f32_dpp v64, v64, v64 row_ror:4 row_mask:0xf bank_mask:0xf\n\t"
      "s_nop 1\n\t"
      "v_add_f32_dpp v64, v64, v64 row_ror:8 row_mask:0xf bank_mask:0xf\n\t"
      "v_mov_b32 v65, v64\n\t"
      "s_mov_b64 exec, s[88:89]\n\t"
      "s_add_u32 m0, s76, 1024\n\t"
      "s_add_u32 s68, s77, 32768\n\t"
      "s_add_u32 s76, s76, 8192\n\t"
      "s_add_u32 s77, s77, 0x40000\n\t"
      "buffer_load_dwordx4 v108, s[64:67], s68 offen lds\n\t"
      "s_mov_b64 exec, -1\n\t"
      "v_permlane16_swap_b32 v64, v65\n\t"
      "v_add_f32 v64, v64, v65\n\t"
      "v_mov_b32 v65, v64\n\t"
      "s_nop 1\n\t"
      "v_permlane32_swap_b32 v64, v65\n\t"
      "v_add_f32 v64, v64, v65\n\t"
      "v_mul_f32 v81, 0x3b000000, v64\n\t"
      "v_cvt_pk_bf16_f32 v80, v80, v81\n\t"
      "v_and_b32 v81, s84, v80\n\t"
      "v_lshlrev_b32 v80, 16, v80\n\t"
      "v_add_f32 v81, 0x3727c5ac, v81\n\t"
      "v_pk_add_f32 v[32:33], v[0:1], v[80:81] op_sel_hi:[1,0] neg_lo:[0,1] neg_hi:[0,1]\n\t"
      "v_pk_add_f32 v[34:35], v[2:3], v[80:81] op_sel_hi:[1,0] neg_lo:[0,1] neg_hi:[0,1]\n\t"
      "v_pk_add_f32 v[36:37], v[4:5], v[80:81] op_sel_hi:[1,0] neg_lo:[0,1] neg_hi:[0,1]\n\t"
      "v_pk_add_f32 v[38:39], v[6:7], v[80:81] op_sel_hi:[1,0] neg_lo:[0,1] neg_hi:[0,1]\n\t"
      "v_pk_add_f32 v[40:41], v[8:9], v[80:81] op_sel_hi:[1,0] neg_lo:[0,1] neg_hi:[0,1]\n\t"
      "v_pk_add_f32 v[42:43], v[10:11], v[80:81] op_sel_hi:[1,0] neg_lo:[0,1] neg_hi:[0,1]\n\t"
      "s_mov_b64 exec, s[88:89]\n\t"
      "s_add_u32 m0, s76, 0\n\t"
      "s_add_u32 s68, s77, 0\n\t"
      "buffer_load_dwordx4 v108, s[64:67], s68 offen lds\n\t"
      "s_mov_b64 exec, -1\n\t"
      "v_pk_add_f32 v[44:45], v[12:13], v[80:81] op_sel_hi:[1,0] neg_lo:[0,1] neg_hi:[0,1]\n\t"
      "v_pk_add_f32 v[46:47], v[14:15], v[80:81] op_sel_hi:[1,0] neg_lo:[0,1] neg_hi:[0,1]\n\t"
      "v_pk_add_f32 v[48:49], v[16:17], v[80:81] op_sel_hi:[1,0] neg_lo:[0,1] neg_hi:[0,1]\n\t"
      "v_pk_add_f32 v[50:51], v[18:19], v[80:81] op_sel_hi:[1,0] neg_lo:[0,1] neg_hi:[0,1]\n\t"
      "v_pk_add_f32 v[52:53], v[20:21], v[80:81] op_sel_hi:[1,0] neg_lo:[0,1] neg_hi:[0,1]\n\t"
      "v_pk_add_f32 v[54:55], v[22:23], v[80:81] op_sel_hi:[1,0] neg_lo:[0,1] neg_hi:[0,1]\n\t"
      "v_pk_add_f32 v[56:57], v[24:25], v[80:81] op_sel_hi:[1,0] neg_lo:[0,1] neg_hi:[0,1]\n\t"
      "v_pk_add_f32 v[58:59], v[26:27], v[80:81] op_sel_hi:[1,0] neg_lo:[0,1] neg_hi:[0,1]\n\t"
      "v_pk_add_f32 v[60:61], v[28:29], v[80:81] op_sel_hi:[1,0] neg_lo:[0,1] neg_hi:[0,1]\n\t"
      "v_pk_add_f32 v[62:63], v[30:31], v[80:81] op_sel_hi:[1,0] neg_lo:[0,1] neg_hi:[0,1]\n\t"
      "v_cvt_pk_bf16_f32 v81, v81, v81\n\t"
      "v_and_b32 v81, s84, v81\n\t"
      "v_sqrt_f32 v81, v81\n\t"
      "v_cvt_pk_bf16_f32 v32, v32, v33\n\t"
      "v_cvt_pk_bf16_f32 v34, v34, v35\n\t"
      "v_cvt_pk_bf16_f32 v36, v36, v37\n\t"
      "s_mov_b64 exec, s[88:89]\n\t"
      "s_add_u32 m0, s76, 1024\n\t"
      "s_add_u32 s68, s77, 32768\n\t"
      "s_add_u32 s76, s76, 8192\n\t"
      "s_add_u32 s77, s77, 0x40000\n\t"
      "buffer_load_dwordx4 v108, s[64:67], s68 offen lds\n\t"
      "s_mov_b64 exec, -1\n\t"
      "v_cvt_pk_bf16_f32 v38, v38, v39\n\t"
      "v_cvt_pk_bf16_f32 v40, v40, v41\n\t"
      "v_cvt_pk_bf16_f32 v42, v42, v43\n\t"
      "v_cvt_pk_bf16_f32 v44, v44, v45\n\t"
      "v_cvt_pk_bf16_f32 v46, v46, v47\n\t"
      "v_cvt_pk_bf16_f32 v48, v48, v49\n\t"
      "v_cvt_pk_bf16_f32 v50, v50, v51\n\t"
      "v_cvt_pk_bf16_f32 v52, v52, v53\n\t"
      "v_cvt_pk_bf16_f32 v54, v54, v55\n\t"
      "v_cvt_pk_bf16_f32 v56, v56, v57\n\t"
      "v_cvt_pk_bf16_f32 v58, v58, v59\n\t"
      "v_cvt_pk_bf16_f32 v60, v60, v61\n\t"
      "v_cvt_pk_bf16_f32 v62, v62, v63\n\t"
      "v_cvt_pk_bf16_f32 v81, v81, v81\n\t"
      "v_and_b32 v81, s84, v81\n\t"
      "v_rcp_f32 v80, v81\n\t"
      "s_mov_b64 exec, s[88:89]\n\t"
      "s_add_u32 m0, s76, 0\n\t"
      "s_add_u32 s68, s77, 0\n\t"
      "buffer_load_dwordx4 v108, s[64:67], s68 offen lds\n\t"
      "s_mov_b64 exec, -1\n\t"
      "v_and_b32 v33, s84, v32\n\t"
      "v_lshlrev_b32 v32, 16, v32\n\t"
      "v_and_b32 v35, s84, v34\n\t"
      "v_lshlrev_b32 v34, 16, v34\n\t"
      "v_and_b32 v37, s84, v36\n\t"
      "v_lshlrev_b32 v36, 16, v36\n\t"
      "v_and_b32 v39, s84, v38\n\t"
      "v_lshlrev_b32 v38, 16, v38\n\t"
      "v_pk_mul_f32 v[32:33], v[32:33], v[80:81] op_sel_hi:[1,0]\n\t"
      "v_pk_mul_f32 v[34:35], v[34:35], v[80:81] op_sel_hi:[1,0]\n\t"
      "v_pk_mul_f32 v[36:37], v[36:37], v[80:81] op_sel_hi:[1,0]\n\t"
      "v_pk_mul_f32 v[38:39], v[38:39], v[80:81] op_sel_hi:[1,0]\n\t"
      "v_cvt_pk_bf16_f32 v64, v32, v33\n\t"
      "v_cvt_pk_bf16_f32 v65, v34, v35\n\t"
      "v_cvt_pk_bf16_f32 v66, v36, v37\n\t"
      "v_cvt_pk_bf16_f32 v67, v38, v39\n\t"
      "s_mov_b64 exec, s[88:89]\n\t"
      "s_add_u32 m0, s76, 1024\n\t"
      "s_add_u32 s68, s77, 32768\n\t"
      "s_add_u32 s76, s76, 8192\n\t"
      "s_add_u32 s77, s77, 0x40000\n\t"
      "buffer_load_dwordx4 v108, s[64:67], s68 offen lds\n\t"
      "s_mov_b64 exec, -1\n\t"
      "v_and_b32 v41, s84, v40\n\t"
      "v_lshlrev_b32 v40, 16, v40\n\t"
      "v_and_b32 v43, s84, v42\n\t"
      "v_lshlrev_b32 v42, 16, v42\n\t"
      "v_and_b32 v45, s84, v44\n\t"
      "v_lshlrev_b32 v44, 16, v44\n\t"
      "v_and_b32 v47, s84, v46\n\t"
      "v_lshlrev_b32 v46, 16, v46\n\t"
      "v_pk_mul_f32 v[40:41], v[40:41], v[80:81] op_sel_hi:[1,0]\n\t"
      "v_pk_mul_f32 v[42:43], v[42:43], v[80:81] op_sel_hi:[1,0]\n\t"
      "v_pk_mul_f32 v[44:45], v[44:45], v[80:81] op_sel_hi:[1,0]\n\t"
      "v_pk_mul_f32 v[46:47], v[46:47], v[80:81] op_sel_hi:[1,0]\n\t"
      "v_cvt_pk_bf16_f32 v68, v40, v41\n\t"
      "v_cvt_pk_bf16_f32 v69, v42, v43\n\t"
      "v_cvt_pk_bf16_f32 v70, v44, v45\n\t"
      "v_cvt_pk_bf16_f32 v71, v46, v47\n\t"
      "s_mov_b64 exec, s[88:89]\n\t"
      "s_add_u32 m0, s76, 0\n\t"
      "s_add_u32 s68, s77, 0\n\t"
      "buffer_load_dwordx4 v108, s[64:67], s68 offen lds\n\t"
      "s_mov_b64 exec, -1\n\t"
      "v_and_b32 v49, s84, v48\n\t"
      "v_lshlrev_b32 v48, 16, v48\n\t"
      "v_and_b32 v51, s84, v50\n\t"
      "v_lshlrev_b32 v50, 16, v50\n\t"
      "v_and_b32 v53, s84, v52\n\t"
      "v_lshlrev_b32 v52, 16, v52\n\t"
      "v_and_b32 v55, s84, v54\n\t"
      "v_lshlrev_b32 v54, 16, v54\n\t"
      "v_pk_mul_f32 v[48:49], v[48:49], v[80:81] op_sel_hi:[1,0]\n\t"
      "v_pk_mul_f32 v[50:51], v[50:51], v[80:81] op_sel_hi:[1,0]\n\t"
      "v_pk_mul_f32 v[52:53], v[52:53], v[80:81] op_sel_hi:[1,0]\n\t"
      "v_pk_mul_f32 v[54:55], v[54:55], v[80:81] op_sel_hi:[1,0]\n\t"
      "v_cvt_pk_bf16_f32 v72, v48, v49\n\t"
      "v_cvt_pk_bf16_f32 v73, v50, v51\n\t"
      "v_cvt_pk_bf16_f32 v74, v52, v53\n\t"
      "v_cvt_pk_bf16_f32 v75, v54, v55\n\t"
      "s_mov_b64 exec, s[88:89]\n\t"
      "s_add_u32 m0, s76, 1024\n\t"
      "s_add_u32 s68, s77, 32768\n\t"
      "s_add_u32 s76, s76, 8192\n\t"
      "s_add_u32 s77, s77, 0x40000\n\t"
      "buffer_load_dwordx4 v108, s[64:67], s68 offen lds\n\t"
      "s_mov_b64 exec, -1\n\t"
      "v_and_b32 v57, s84, v56\n\t"
      "v_lshlrev_b32 v56, 16, v56\n\t"
      "v_and_b32 v59, s84, v58\n\t"
      "v_lshlrev_b32 v58, 16, v58\n\t"
      "v_and_b32 v61, s84, v60\n\t"
      "v_lshlrev_b32 v60, 16, v60\n\t"
      "v_and_b32 v63, s84, v62\n\t"
      "v_lshlrev_b32 v62, 16, v62\n\t"
      "v_pk_mul_f32 v[56:57], v[56:57], v[80:81] op_sel_hi:[1,0]\n\t"
      "v_pk_mul_f32 v[58:59], v[58:59], v[80:81] op_sel_hi:[1,0]\n\t"
      "v_pk_mul_f32 v[60:61], v[60:61], v[80:81] op_sel_hi:[1,0]\n\t"
      "v_pk_mul_f32 v[62:63], v[62:63], v[80:81] op_sel_hi:[1,0]\n\t"
      "v_cvt_pk_bf16_f32 v76, v56, v57\n\t"
      "v_cvt_pk_bf16_f32 v77, v58, v59\n\t"
      "v_cvt_pk_bf16_f32 v78, v60, v61\n\t"
      "v_cvt_pk_bf16_f32 v79, v62, v63\n\t"
      "s_cmp_lg_u32 s69, 0\n\t"
      "s_cbranch_scc1 GLN1%=\n\t"
      "buffer_store_dwordx4 v[64:67], v106, s[60:63], s74 offen sc1\n\t"
      "s_add_u32 s74, s74, 4096\n\t"
      "buffer_store_dwordx4 v[68:71], v106, s[60:63], s74 offen sc1\n\t"
      "s_add_u32 s74, s74, 4096\n\t"
      "buffer_store_dwordx4 v[72:75], v106, s[60:63], s74 offen sc1\n\t"
      "s_add_u32 s74, s74, 4096\n\t"
      "buffer_store_dwordx4 v[76:79], v106, s[60:63], s74 offen sc1\n\t"
      "s_add_u32 s74, s74, 4096\n\t"
      "s_sub_u32 s76, s76, 49152\n\t"
      "s_sub_u32 s77, s77, 1572864\n\t"
      "s_mov_b64 s[88:89], -1\n\t"
      "v_readfirstlane_b32 %3, v107\n\t"
      "v_mov_b64 v[64:65], v[128:129]\n\t"
      "v_mov_b64 v[66:67], v[130:131]\n\t"
      "v_mov_b64 v[68:69], v[132:133]\n\t"
      "v_mov_b64 v[70:71], v[134:135]\n\t"
      "v_mov_b64 v[72:73], v[136:137]\n\t"
      "v_mov_b64 v[74:75], v[138:139]\n\t"
      "v_mov_b64 v[76:77], v[140:141]\n\t"
      "v_mov_b64 v[78:79], v[142:143]\n\t"
      "s_mul_i32 %3, %3, 0x9e3779b1\n\t"
      "s_add_u32 %3, %3, 0x7f4a7c15\n\t"
      "s_mov_b32 s69, 1\n\t"
      "s_branch GLN%=\n\t"
      "GLN1%=:\n\t"
      "ds_write_b128 v110, v[64:67]\n\t"
      "ds_write_b128 v110, v[68:71] offset:4096\n\t"
      "ds_write_b128 v110, v[72:75] offset:8192\n\t"
      "ds_write_b128 v110, v[76:79] offset:12288\n\t"
      "s_cmp_eq_u32 s69, 2\n\t"
      "s_cbranch_scc1 GLN2%=\n\t"
      "v_mov_b32 v107, %3\n\t"
      "v_add_u32 v110, 0x4000, v110\n\t"
      "s_lshl_b32 s79, %0, 7\n\t"
      "s_add_u32 s79, s79, 37120\n\t"
      "s_waitcnt vmcnt(12) lgkmcnt(0)\n\t"
      "s_barrier\n\t"
      "s_mov_b64 exec, s[90:91]\n\t"
      "buffer_store_dword v107, off, s[60:63], s79 sc1\n\t"
      "s_mov_b64 exec, -1\n\t"
      "v_mov_b64 v[64:65], v[144:145]\n\t"
      "v_mov_b64 v[66:67], v[146:147]\n\t"
      "v_mov_b64 v[68:69], v[148:149]\n\t"
      "v_mov_b64 v[70:71], v[150:151]\n\t"
      "v_mov_b64 v[72:73], v[152:153]\n\t"
      "v_mov_b64 v[74:75], v[154:155]\n\t"
      "v_mov_b64 v[76:77], v[156:157]\n\t"
      "v_mov_b64 v[78:79], v[158:159]\n\t"
      "s_mov_b32 s69, 2\n\t"
      "s_branch GLN%=\n\t"
      "GLN2%=:\n\t"
      "s_waitcnt lgkmcnt(10)\n\t"
      "ds_read_b64_tr_b16 a[0:1], v109\n\t"
      "ds_read_b64_tr_b16 a[2:3], v109 offset:1024\n\t"
      "ds_read_b64_tr_b16 a[4:5], v109 offset:8\n\t"
      "ds_read_b64_tr_b16 a[6:7], v109 offset:1032\n\t"
      "s_waitcnt lgkmcnt(10)\n\t"
      "ds_read_b64_tr_b16 a[8:9], v109 offset:8192\n\t"
      "ds_read_b64_tr_b16 a[10:11], v109 offset:9216\n\t"
      "ds_read_b64_tr_b16 a[12:13], v109 offset:8200\n\t"
      "ds_read_b64_tr_b16 a[14:15], v109 offset:9224\n\t"
      "s_waitcnt lgkmcnt(10)\n\t"
      "ds_read_b64_tr_b16 a[16:17], v109 offset:16384\n\t"
      "ds_read_b64_tr_b16 a[18:19], v109 offset:17408\n\t"
      "ds_read_b64_tr_b16 a[20:21], v109 offset:16392\n\t"
      "ds_read_b64_tr_b16 a[22:23], v109 offset:17416\n\t"
      "s_waitcnt lgkmcnt(10)\n\t"
      "ds_read_b64_tr_b16 a[24:25], v109 offset:24576\n\t"
      "ds_read_b64_tr_b16 a[26:27], v109 offset:25600\n\t"
      "ds_read_b64_tr_b16 a[28:29], v109 offset:24584\n\t"
      "ds_read_b64_tr_b16 a[30:31], v109 offset:25608\n\t"
      "v_lshlrev_b32 v106, 8, v96\n\t"
      "s_mov_b32 s79, 37120\n\t"
      "s_mov_b32 s81, 0x40000\n\t"
      "v_add_u32 v98, 131072, v97\n\t"
      "v_readlane_b32 s73, v102, 0\n\t"
      "v_readlane_b32 s75, v103, 0\n\t"
      "s_mov_b64 s[64:65], s[40:41]\n\t"
      "s_lshl_b32 s74, %1, 10\n\t"
      "v_readlane_b32 s72, v101, 1\n\t"
      "s_add_u32 s76, s85, 0\n\t"
      "s_waitcnt vmcnt(12)\n\t"
      "s_barrier\n\t"
      "s_waitcnt lgkmcnt(10)\n\t"
      "ds_read_b64_tr_b16 a[32:33], v109 offset:32768\n\t"
      "ds_read_b64_tr_b16 a[34:35], v109 offset:33792\n\t"
      "ds_read_b64_tr_b16 a[36:37], v109 offset:32776\n\t"
      "ds_read_b64_tr_b16 a[38:39], v109 offset:33800\n\t"
      "s_waitcnt lgkmcnt(10)\n\t"
      "ds_read_b64_tr_b16 a[40:41], v109 offset:40960\n\t"
      "ds_read_b64_tr_b16 a[42:43], v109 offset:41984\n\t"
      "ds_read_b64_tr_b16 a[44:45], v109 offset:40968\n\t"
      "ds_read_b64_tr_b16 a[46:47], v109 offset:41992\n\t"
      "s_waitcnt lgkmcnt(10)\n\t"
      "ds_read_b64_tr_b16 a[48:49], v109 offset:49152\n\t"
      "ds_read_b64_tr_b16 a[50:51], v109 offset:50176\n\t"
      "ds_read_b64_tr_b16 a[52:53], v109 offset:49160\n\t"
      "ds_read_b64_tr_b16 a[54:55], v109 offset:50184\n\t"
      "s_waitcnt lgkmcnt(10)\n\t"
      "ds_read_b64_tr_b16 a[56:57], v109 offset:57344\n\t"
      "ds_read_b64_tr_b16 a[58:59], v109 offset:58368\n\t"
      "ds_read_b64_tr_b16 a[60:61], v109 offset:57352\n\t"
      "ds_read_b64_tr_b16 a[62:63], v109 offset:58376\n\t"
      "v_add_u32 v109, 0x10000, v109\n\t"
      "s_waitcnt lgkmcnt(10)\n\t"
      "ds_read_b64_tr_b16 a[64:65], v109\n\t"
      "ds_read_b64_tr_b16 a[66:67], v109 offset:1024\n\t"
      "ds_read_b64_tr_b16 a[68:69], v109 offset:8\n\t"
      "ds_read_b64_tr_b16 a[70:71], v109 offset:1032\n\t"
      "s_waitcnt lgkmcnt(10)\n\t"
      "ds_read_b64_tr_b16 a[72:73], v109 offset:8192\n\t"
      "ds_read_b64_tr_b16 a[74:75], v109 offset:9216\n\t"
      "ds_read_b64_tr_b16 a[76:77], v109 offset:8200\n\t"
      "ds_read_b64_tr_b16 a[78:79], v109 offset:9224\n\t"
      "s_waitcnt vmcnt(0)\n\t"
      "s_barrier\n\t"
      "buffer_load_dword v128, v106, s[60:63], s79 offen sc1\n\t"
      "buffer_load_dword v129, v106, s[60:63], s79 offen offset:128 sc1\n\t"
      "s_waitcnt lgkmcnt(10)\n\t"
      "ds_read_b64_tr_b16 a[80:81], v109 offset:16384\n\t"
      "ds_read_b64_tr_b16 a[82:83], v109 offset:17408\n\t"
      "ds_read_b64_tr_b16 a[84:85], v109 offset:16392\n\t"
      "ds_read_b64_tr_b16 a[86:87], v109 offset:17416\n\t"
      "s_waitcnt lgkmcnt(10)\n\t"
      "ds_read_b64_tr_b16 a[88:89], v109 offset:24576\n\t"
      "ds_read_b64_tr_b16 a[90:91], v109 offset:25600\n\t"
      "ds_read_b64_tr_b16 a[92:93], v109 offset:24584\n\t"
      "ds_read_b64_tr_b16 a[94:95], v109 offset:25608\n\t"
      "s_waitcnt lgkmcnt(10)\n\t"
      "ds_read_b64_tr_b16 a[96:97], v109 offset:32768\n\t"
      "ds_read_b64_tr_b16 a[98:99], v109 offset:33792\n\t"
      "ds_read_b64_tr_b16 a[100:101], v109 offset:32776\n\t"
      "ds_read_b64_tr_b16 a[102:103], v109 offset:33800\n\t"
      "s_waitcnt lgkmcnt(10)\n\t"
      "ds_read_b64_tr_b16 a[104:105], v109 offset:40960\n\t"
      "ds_read_b64_tr_b16 a[106:107], v109 offset:41984\n\t"
      "ds_read_b64_tr_b16 a[108:109], v109 offset:40968\n\t"
      "ds_read_b64_tr_b16 a[110:111], v109 offset:41992\n\t"
      "s_waitcnt lgkmcnt(10)\n\t"
      "ds_read_b64_tr_b16 a[112:113], v109 offset:49152\n\t"
      "ds_read_b64_tr_b16 a[114:115], v109 offset:50176\n\t"
      "ds_read_b64_tr_b16 a[116:117], v109 offset:49160\n\t"
      "ds_read_b64_tr_b16 a[118:119], v109 offset:50184\n\t"
      "s_waitcnt lgkmcnt(10)\n\t"
      "ds_read_b64_tr_b16 a[120:121], v109 offset:57344\n\t"
      "ds_read_b64_tr_b16 a[122:123], v109 offset:58368\n\t"
      "ds_read_b64_tr_b16 a[124:125], v109 offset:57352\n\t"
      "ds_read_b64_tr_b16 a[126:127], v109 offset:58376\n\t"
      "s_mov_b64 s[88:89], 0\n\t"
      "s_mov_b64 s[38:39], 0\n\t"
      "s_waitcnt lgkmcnt(4)\n\t"
      "s_mov_b32 s71, 0\n\t"
      "GKV%=:\n\t"
      "ds_read_b128 v[0:3], v98\n\t"
      "ds_read_b128 v[4:7], v98 offset:1024\n\t"
      "ds_read_b128 v[8:11], v98 offset:2048\n\t"
      "ds_read_b128 v[12:15], v98 offset:3072\n\t"
      "ds_read_b128 v[16:19], v98 offset:4096\n\t"
      "ds_read_b128 v[20:23], v98 offset:5120\n\t"
      "ds_read_b128 v[24:27], v98 offset:6144\n\t"
      "ds_read_b128 v[28:31], v98 offset:7168\n\t"
      "ds_read_b128 v[32:35], v98 offset:8192\n\t"
      "ds_read_b128 v[36:39], v98 offset:9216\n\t"
      "s_waitcnt lgkmcnt(9)\n\t"
      "v_mfma_f32_16x16x32_bf16 v[64:67], a[0:3], v[0:3], 0\n\t"
      "ds_read_b128 v[40:43], v98 offset:10240\n\t"
      "v_mfma_f32_16x16x32_bf16 v[68:71], a[4:7], v[0:3], 0\n\t"
      "s_waitcnt lgkmcnt(9)\n\t"
      "v_mfma_f32_16x16x32_bf16 v[64:67], a[8:11], v[4:7], v[64:67]\n\t"
      "ds_read_b128 v[44:47], v98 offset:11264\n\t"
      "v_mfma_f32_16x16x32_bf16 v[68:71], a[12:15], v[4:7], v[68:71]\n\t"
      "s_waitcnt lgkmcnt(9)\n\t"
      "v_mfma_f32_16x16x32_bf16 v[64:67], a[16:19], v[8:11], v[64:67]\n\t"
      "ds_read_b128 v[48:51], v98 offset:12288\n\t"
      "v_mfma_f32_16x16x32_bf16 v[68:71], a[20:23], v[8:11], v[68:71]\n\t"
      "s_waitcnt lgkmcnt(9)\n\t"
      "v_mfma_f32_16x16x32_bf16 v[64:67], a[24:27], v[12:15], v[64:67]\n\t"
      "ds_read_b128 v[52:55], v98 offset:13312\n\t"
      "v_mfma_f32_16x16x32_bf16 v[68:71], a[28:31], v[12:15], v[68:71]\n\t"
      "s_waitcnt lgkmcnt(9)\n\t"
      "v_mfma_f32_16x16x32_bf16 v[64:67], a[32:35], v[16:19], v[64:67]\n\t"
      "ds_read_b128 v[56:59], v98 offset:14336\n\t"
      "v_mfma_f32_16x16x32_bf16 v[68:71], a[36:39], v[16:19], v[68:71]\n\t"
      "s_waitcnt lgkmcnt(9)\n\t"
      "v_mfma_f32_16x16x32_bf16 v[64:67], a[40:43], v[20:23], v[64:67]\n\t"
      "ds_read_b128 v[60:63], v98 offset:15360\n\t"
      "v_mfma_f32_16x16x32_bf16 v[68:71], a[44:47], v[20:23], v[68:71]\n\t"
      "s_waitcnt lgkmcnt(9)\n\t"
      "v_mfma_f32_16x16x32_bf16 v[64:67], a[48:51], v[24:27], v[64:67]\n\t"
      "ds_read_b128 v[0:3], v98 offset:16384\n\t"
      "v_mfma_f32_16x16x32_bf16 v[68:71], a[52:55], v[24:27], v[68:71]\n\t"
      "s_waitcnt lgkmcnt(9)\n\t"
      "v_mfma_f32_16x16x32_bf16 v[64:67], a[56:59], v[28:31], v[64:67]\n\t"
      "ds_read_b128 v[4:7], v98 offset:17408\n\t"
      "v_mfma_f32_16x16x32_bf16 v[68:71], a[60:63], v[28:31], v[68:71]\n\t"
      "s_waitcnt lgkmcnt(9)\n\t"
      "v_mfma_f32_16x16x32_bf16 v[64:67], a[64:67], v[32:35], v[64:67]\n\t"
      "ds_read_b128 v[8:11], v98 offset:18432\n\t"
      "v_mfma_f32_16x16x32_bf16 v[68:71], a[68:71], v[32:35], v[68:71]\n\t"
      "s_waitcnt lgkmcnt(9)\n\t"
      "v_mfma_f32_16x16x32_bf16 v[64:67], a[72:75], v[36:39], v[64:67]\n\t"
      "ds_read_b128 v[12:15], v98 offset:19456\n\t"
      "v_mfma_f32_16x16x32_bf16 v[68:71], a[76:79], v[36:39], v[68:71]\n\t"
      "s_waitcnt lgkmcnt(9)\n\t"
      "v_mfma_f32_16x16x32_bf16 v[64:67], a[80:83], v[40:43], v[64:67]\n\t"
      "ds_read_b128 v[16:19], v98 offset:20480\n\t"
      "v_mfma_f32_16x16x32_bf16 v[68:71], a[84:87], v[40:43], v[68:71]\n\t"
      "s_waitcnt lgkmcnt(9)\n\t"
      "v_mfma_f32_16x16x32_bf16 v[64:67], a[88:91], v[44:47], v[64:67]\n\t"
      "ds_read_b128 v[20:23], v98 offset:21504\n\t"
      "v_mfma_f32_16x16x32_bf16 v[68:71], a[92:95], v[44:47], v[68:71]\n\t"
      "s_waitcnt lgkmcnt(9)\n\t"
      "v_mfma_f32_16x16x32_bf16 v[64:67], a[96:99], v[48:51], v[64:67]\n\t"
      "ds_read_b128 v[24:27], v98 offset:22528\n\t"
      "v_mfma_f32_16x16x32_bf16 v[68:71], a[100:103], v[48:51], v[68:71]\n\t"
      "s_cmp_lg_u32 s71, 0\n\t"
      "s_barrier\n\t"
      "s_cbranch_scc1 GKV0%=\n\t"
      "s_waitcnt vmcnt(0)\n\t"
      "v_bitop3_b32 v128, v128, v129, %3 bitop3:0x7e\n\t"
      "v_cmp_ne_u32 vcc, 0, v128\n\t"
      "s_nop 0\n\t"
      "s_cmp_eq_u64 vcc, 0\n\t"
      "s_cselect_b64 s[36:37], -1, 0\n\t"
      "GKV0%=:\n\t"
      "s_waitcnt lgkmcnt(9)\n\t"
      "v_mfma_f32_16x16x32_bf16 v[64:67], a[104:107], v[52:55], v[64:67]\n\t"
      "ds_read_b128 v[28:31], v98 offset:23552\n\t"
      "v_mfma_f32_16x16x32_bf16 v[68:71], a[108:111], v[52:55], v[68:71]\n\t"
      "s_add_u32 m0, s76, 0\n\t"
      "s_mov_b64 exec, s[36:37]\n\t"
      "buffer_load_dwordx4 v97, s[60:63], s72 offen sc1 lds\n\t"
      "s_mov_b64 exec, -1\n\t"
      "s_waitcnt lgkmcnt(9)\n\t"
      "v_mfma_f32_16x16x32_bf16 v[64:67], a[112:115], v[56:59], v[64:67]\n\t"
      "ds_read_b128 v[32:35], v98 offset:24576\n\t"
      "v_mfma_f32_16x16x32_bf16 v[68:71], a[116:119], v[56:59], v[68:71]\n\t"
      "s_waitcnt lgkmcnt(9)\n\t"
      "v_mfma_f32_16x16x32_bf16 v[64:67], a[120:123], v[60:63], v[64:67]\n\t"
      "ds_read_b128 v[36:39], v98 offset:25600\n\t"
      "v_mfma_f32_16x16x32_bf16 v[68:71], a[124:127], v[60:63], v[68:71]\n\t"
      "s_add_u32 m0, s76, 0\n\t"
      "s_mov_b64 exec, s[36:37]\n\t"
      "buffer_load_dwordx4 v97, s[60:63], s72 offen offset:1024 sc1 lds\n\t"
      "s_mov_b64 exec, -1\n\t"
      "s_waitcnt lgkmcnt(9)\n\t"
      "v_mfma_f32_16x16x32_bf16 v[72:75], a[0:3], v[0:3], 0\n\t"
      "ds_read_b128 v[40:43], v98 offset:26624\n\t"
      "v_mfma_f32_16x16x32_bf16 v[76:79], a[4:7], v[0:3], 0\n\t"
      "s_waitcnt lgkmcnt(9)\n\t"
      "v_mfma_f32_16x16x32_bf16 v[72:75], a[8:11], v[4:7], v[72:75]\n\t"
      "ds_read_b128 v[44:47], v98 offset:27648\n\t"
      "v_mfma_f32_16x16x32_bf16 v[76:79], a[12:15], v[4:7], v[76:79]\n\t"
      "s_add_u32 m0, s76, 0\n\t"
      "s_mov_b64 exec, s[36:37]\n\t"
      "buffer_load_dwordx4 v97, s[60:63], s72 offen offset:2048 sc1 lds\n\t"
      "s_mov_b64 exec, -1\n\t"
      "s_cmp_eq_u32 s71, 1\n\t"
      "s_cbranch_scc0 GKV2%=\n\t"
      "s_waitcnt vmcnt(3)\n\t"
      "s_barrier\n\t"
      "s_mov_b64 exec, s[38:39]\n\t"
      "buffer_store_dword v107, off, s[60:63], s75 sc1\n\t"
      "s_mov_b64 exec, -1\n\t"
      "GKV2%=:\n\t"
      "s_waitcnt lgkmcnt(9)\n\t"
      "v_mfma_f32_16x16x32_bf16 v[72:75], a[16:19], v[8:11], v[72:75]\n\t"
      "ds_read_b128 v[48:51], v98 offset:28672\n\t"
      "v_mfma_f32_16x16x32_bf16 v[76:79], a[20:23], v[8:11], v[76:79]\n\t"
      "s_add_u32 m0, s74, 65536\n\t"
      "s_mov_b32 s78, 0\n\t"
      "s_mov_b64 exec, s[88:89]\n\t"
      "buffer_load_dwordx4 v116, s[64:67], s78 offen lds\n\t"
      "s_mov_b64 exec, -1\n\t"
      "s_waitcnt lgkmcnt(9)\n\t"
      "v_mfma_f32_16x16x32_bf16 v[72:75], a[24:27], v[12:15], v[72:75]\n\t"
      "ds_read_b128 v[52:55], v98 offset:29696\n\t"
      "v_mfma_f32_16x16x32_bf16 v[76:79], a[28:31], v[12:15], v[76:79]\n\t"
      "s_add_u32 m0, s76, 0\n\t"
      "s_mov_b64 exec, s[36:37]\n\t"
      "buffer_load_dwordx4 v97, s[60:63], s72 offen offset:3072 sc1 lds\n\t"
      "s_mov_b64 exec, -1\n\t"
      "s_waitcnt lgkmcnt(9)\n\t"
      "v_mfma_f32_16x16x32_bf16 v[72:75], a[32:35], v[16:19], v[72:75]\n\t"
      "ds_read_b128 v[56:59], v98 offset:30720\n\t"
      "v_mfma_f32_16x16x32_bf16 v[76:79], a[36:39], v[16:19], v[76:79]\n\t"
      "s_add_u32 m0, s74, 69632\n\t"
      "s_mov_b32 s78, 262144\n\t"
      "s_mov_b64 exec, s[88:89]\n\t"
      "buffer_load_dwordx4 v116, s[64:67], s78 offen lds\n\t"
      "s_mov_b64 exec, -1\n\t"
      "s_waitcnt lgkmcnt(9)\n\t"
      "v_mfma_f32_16x16x32_bf16 v[72:75], a[40:43], v[20:23], v[72:75]\n\t"
      "ds_read_b128 v[60:63], v98 offset:31744\n\t"
      "v_mfma_f32_16x16x32_bf16 v[76:79], a[44:47], v[20:23], v[76:79]\n\t"
      "s_add_u32 m0, s76, 4096\n\t"
      "s_add_u32 s72, s72, 4096\n\t"
      "s_mov_b64 exec, s[36:37]\n\t"
      "buffer_load_dwordx4 v97, s[60:63], s72 offen sc1 lds\n\t"
      "s_mov_b64 exec, -1\n\t"
      "v_cvt_pk_bf16_f32 v80, v64, v65\n\t"
      "v_cvt_pk_bf16_f32 v81, v66, v67\n\t"
      "v_cvt_pk_bf16_f32 v82, v68, v69\n\t"
      "v_cvt_pk_bf16_f32 v83, v70, v71\n\t"
      "buffer_store_dwordx4 v[80:83], v97, s[60:63], s73 offen sc1\n\t"
      "s_waitcnt lgkmcnt(9)\n\t"
      "v_mfma_f32_16x16x32_bf16 v[72:75], a[48:51], v[24:27], v[72:75]\n\t"
      "v_mfma_f32_16x16x32_bf16 v[76:79], a[52:55], v[24:27], v[76:79]\n\t"
      "s_add_u32 m0, s74, 73728\n\t"
      "s_mov_b32 s78, 524288\n\t"
      "s_mov_b64 exec, s[88:89]\n\t"
      "buffer_load_dwordx4 v116, s[64:67], s78 offen lds\n\t"
      "s_mov_b64 exec, -1\n\t"
      "s_waitcnt lgkmcnt(8)\n\t"
      "v_mfma_f32_16x16x32_bf16 v[72:75], a[56:59], v[28:31], v[72:75]\n\t"
      "v_mfma_f32_16x16x32_bf16 v[76:79], a[60:63], v[28:31], v[76:79]\n\t"
      "s_add_u32 m0, s76, 4096\n\t"
      "s_mov_b64 exec, s[36:37]\n\t"
      "buffer_load_dwordx4 v97, s[60:63], s72 offen offset:1024 sc1 lds\n\t"
      "s_mov_b64 exec, -1\n\t"
      "s_waitcnt lgkmcnt(7)\n\t"
      "v_mfma_f32_16x16x32_bf16 v[72:75], a[64:67], v[32:35], v[72:75]\n\t"
      "v_mfma_f32_16x16x32_bf16 v[76:79], a[68:71], v[32:35], v[76:79]\n\t"
      "s_add_u32 m0, s74, 77824\n\t"
      "s_mov_b32 s78, 786432\n\t"
      "s_mov_b64 exec, s[88:89]\n\t"
      "buffer_load_dwordx4 v116, s[64:67], s78 offen lds\n\t"
      "s_mov_b64 exec, -1\n\t"
      "s_waitcnt lgkmcnt(6)\n\t"
      "v_mfma_f32_16x16x32_bf16 v[72:75], a[72:75], v[36:39], v[72:75]\n\t"
      "v_mfma_f32_16x16x32_bf16 v[76:79], a[76:79], v[36:39], v[76:79]\n\t"
      "s_add_u32 m0, s76, 4096\n\t"
      "s_mov_b64 exec, s[36:37]\n\t"
      "buffer_load_dwordx4 v97, s[60:63], s72 offen offset:2048 sc1 lds\n\t"
      "s_mov_b64 exec, -1\n\t"
      "s_waitcnt lgkmcnt(5)\n\t"
      "v_mfma_f32_16x16x32_bf16 v[72:75], a[80:83], v[40:43], v[72:75]\n\t"
      "v_mfma_f32_16x16x32_bf16 v[76:79], a[84:87], v[40:43], v[76:79]\n\t"
      "s_add_u32 m0, s74, 81920\n\t"
      "s_mov_b32 s78, 1048576\n\t"
      "s_mov_b64 exec, s[88:89]\n\t"
      "buffer_load_dwordx4 v116, s[64:67], s78 offen lds\n\t"
      "s_mov_b64 exec, -1\n\t"
      "s_waitcnt lgkmcnt(4)\n\t"
      "v_mfma_f32_16x16x32_bf16 v[72:75], a[88:91], v[44:47], v[72:75]\n\t"
      "v_mfma_f32_16x16x32_bf16 v[76:79], a[92:95], v[44:47], v[76:79]\n\t"
      "s_add_u32 m0, s76, 4096\n\t"
      "s_mov_b64 exec, s[36:37]\n\t"
      "buffer_load_dwordx4 v97, s[60:63], s72 offen offset:3072 sc1 lds\n\t"
      "s_mov_b64 exec, -1\n\t"
      "s_waitcnt lgkmcnt(3)\n\t"
      "v_mfma_f32_16x16x32_bf16 v[72:75], a[96:99], v[48:51], v[72:75]\n\t"
      "v_mfma_f32_16x16x32_bf16 v[76:79], a[100:103], v[48:51], v[76:79]\n\t"
      "s_add_u32 m0, s74, 86016\n\t"
      "s_mov_b32 s78, 1310720\n\t"
      "s_mov_b64 exec, s[88:89]\n\t"
      "buffer_load_dwordx4 v116, s[64:67], s78 offen lds\n\t"
      "s_mov_b64 exec, -1\n\t"
      "s_waitcnt lgkmcnt(2)\n\t"
      "v_mfma_f32_16x16x32_bf16 v[72:75], a[104:107], v[52:55], v[72:75]\n\t"
      "v_mfma_f32_16x16x32_bf16 v[76:79], a[108:111], v[52:55], v[76:79]\n\t"
      "s_waitcnt lgkmcnt(1)\n\t"
      "v_mfma_f32_16x16x32_bf16 v[72:75], a[112:115], v[56:59], v[72:75]\n\t"
      "v_mfma_f32_16x16x32_bf16 v[76:79], a[116:119], v[56:59], v[76:79]\n\t"
      "s_add_u32 m0, s74, 90112\n\t"
      "s_mov_b32 s78, 1572864\n\t"
      "s_mov_b64 exec, s[88:89]\n\t"
      "buffer_load_dwordx4 v116, s[64:67], s78 offen lds\n\t"
      "s_mov_b64 exec, -1\n\t"
      "s_waitcnt lgkmcnt(0)\n\t"
      "v_mfma_f32_16x16x32_bf16 v[72:75], a[120:123], v[60:63], v[72:75]\n\t"
      "v_mfma_f32_16x16x32_bf16 v[76:79], a[124:127], v[60:63], v[76:79]\n\t"
      "s_nop 6\n\t"
      "v_cvt_pk_bf16_f32 v84, v72, v73\n\t"
      "v_cvt_pk_bf16_f32 v85, v74, v75\n\t"
      "v_cvt_pk_bf16_f32 v86, v76, v77\n\t"
      "v_cvt_pk_bf16_f32 v87, v78, v79\n\t"
      "buffer_store_dwordx4 v[84:87], v97, s[60:63], s73 offen offset:1024 sc1\n\t"
      "s_cmp_eq_u32 s71, 2\n\t"
      "s_cbranch_scc1 GKV3%=\n\t"
      "s_cmp_lg_u32 s71, 0\n\t"
      "s_cbranch_scc1 GKV1%=\n\t"
      "s_cmp_lg_u64 s[36:37], 0\n\t"
      "s_cbranch_scc1 GL2%=\n\t"
      "GL0%=:\n\t"
      "buffer_load_dword v128, v106, s[60:63], s79 offen sc1\n\t"
      "buffer_load_dword v129, v106, s[60:63], s79 offen offset:128 sc1\n\t"
      "s_sub_u32 s81, s81, 1\n\t"
      "s_waitcnt vmcnt(0)\n\t"
      "v_bitop3_b32 v128, v128, v129, %3 bitop3:0x7e\n\t"
      "v_cmp_ne_u32 vcc, 0, v128\n\t"
      "s_cmp_eq_u32 s81, 0\n\t"
      "s_cbranch_scc1 GL1%=\n\t"
      "s_cbranch_vccnz GL0%=\n\t"
      "GL1%=:\n\t"
      "v_readlane_b32 s72, v101, 1\n\t"
      "s_add_u32 s76, s85, 0\n\t"
      "s_add_u32 m0, s76, 0\n\t"
      "s_nop 2\n\t"
      "buffer_load_dwordx4 v97, s[60:63], s72 offen sc1 lds\n\t"
      "buffer_load_dwordx4 v97, s[60:63], s72 offen offset:1024 sc1 lds\n\t"
      "buffer_load_dwordx4 v97, s[60:63], s72 offen offset:2048 sc1 lds\n\t"
      "buffer_load_dwordx4 v97, s[60:63], s72 offen offset:3072 sc1 lds\n\t"
      "s_add_u32 m0, s76, 4096\n\t"
      "s_add_u32 s72, s72, 4096\n\t"
      "buffer_load_dwordx4 v97, s[60:63], s72 offen sc1 lds\n\t"
      "buffer_load_dwordx4 v97, s[60:63], s72 offen offset:1024 sc1 lds\n\t"
      "buffer_load_dwordx4 v97, s[60:63], s72 offen offset:2048 sc1 lds\n\t"
      "buffer_load_dwordx4 v97, s[60:63], s72 offen offset:3072 sc1 lds\n\t"
      "s_waitcnt vmcnt(0)\n\t"
      "GL2%=:\n\t"
      "s_mov_b32 s77, s73\n\t"
      "v_readlane_b32 s73, v102, 1\n\t"
      "v_mov_b32 v98, v97\n\t"
      "s_mov_b64 s[88:89], -1\n\t"
      "s_mov_b64 s[38:39], s[90:91]\n\t"
      "s_mov_b64 s[36:37], -1\n\t"
      "s_mov_b32 s71, 1\n\t"
      "v_readlane_b32 s72, v101, 2\n\t"
      "s_add_u32 s76, s85, 32768\n\t"
      "s_waitcnt vmcnt(1)\n\t"
      "s_barrier\n\t"
      "s_branch GKV%=\n\t"
      "GKV1%=:\n\t"
      "v_readlane_b32 s75, v103, 1\n\t"
      "v_add_u32 v117, 0x10000, v117\n\t"
      "v_add_u32 v98, 131072, v97\n\t"
      "s_mov_b32 s68, 0\n\t"
      "s_add_u32 m0, s74, 94208\n\t"
      "s_mov_b32 s78, 1835008\n\t"
      "buffer_load_dwordx4 v116, s[64:67], s78 offen lds\n\t"
      "s_add_u32 m0, s74, 98304\n\t"
      "s_mov_b32 s78, 2097152\n\t"
      "buffer_load_dwordx4 v116, s[64:67], s78 offen lds\n\t"
      "s_add_u32 m0, s74, 102400\n\t"
      "s_mov_b32 s78, 2359296\n\t"
      "buffer_load_dwordx4 v116, s[64:67], s78 offen lds\n\t"
      "s_add_u32 m0, s74, 106496\n\t"
      "s_mov_b32 s78, 2621440\n\t"
      "buffer_load_dwordx4 v116, s[64:67], s78 offen lds\n\t"
      "s_add_u32 m0, s74, 110592\n\t"
      "s_mov_b32 s78, 2883584\n\t"
      "buffer_load_dwordx4 v116, s[64:67], s78 offen lds\n\t"
      "s_add_u32 m0, s74, 114688\n\t"
      "s_mov_b32 s78, 3145728\n\t"
      "buffer_load_dwordx4 v116, s[64:67], s78 offen lds\n\t"
      "s_add_u32 m0, s74, 118784\n\t"
      "s_mov_b32 s78, 3407872\n\t"
      "buffer_load_dwordx4 v116, s[64:67], s78 offen lds\n\t"
      "s_add_u32 m0, s74, 122880\n\t"
      "s_mov_b32 s78, 3670016\n\t"
      "buffer_load_dwordx4 v116, s[64:67], s78 offen lds\n\t"
      "s_add_u32 m0, s74, 126976\n\t"
      "s_mov_b32 s78, 3932160\n\t"
      "buffer_load_dwordx4 v116, s[64:67], s78 offen lds\n\t"
      "v_add_u32 v98, 0x8000, v97\n\t"
      "v_readlane_b32 s73, v102, 2\n\t"
      "s_mov_b64 s[88:89], 0\n\t"
      "s_mov_b64 s[36:37], 0\n\t"
      "s_mov_b32 s71, 2\n\t"
      "s_waitcnt vmcnt(9)\n\t"
      "s_barrier\n\t"
      "s_mov_b64 exec, s[90:91]\n\t"
      "buffer_store_dword v107, off, s[60:63], s75 sc1\n\t"
      "s_mov_b64 exec, -1\n\t"
      "s_branch GKV%=\n\t"
      "GKV3%=:\n\t"
      "v_readlane_b32 s75, v103, 2\n\t"
      "v_add_u32 v98, 131072, v97\n\t"
      "s_waitcnt vmcnt(0)\n\t"
      "s_barrier\n\t"
      "s_mov_b64 exec, s[90:91]\n\t"
      "s_nop 0\n\t"
      "buffer_store_dword v107, off, s[60:63], s75 sc1\n\t"
      "s_mov_b64 exec, -1\n\t"
      "s_waitcnt lgkmcnt(12)\n\t"
      "ds_read_b64_tr_b16 a[128:129], v117\n\t"
      "ds_read_b64_tr_b16 a[130:131], v117 offset:512\n\t"
      "s_waitcnt lgkmcnt(12)\n\t"
      "ds_read_b64_tr_b16 a[132:133], v117 offset:4096\n\t"
      "ds_read_b64_tr_b16 a[134:135], v117 offset:4608\n\t"
      "s_waitcnt lgkmcnt(12)\n\t"
      "ds_read_b64_tr_b16 a[136:137], v117 offset:8192\n\t"
      "ds_read_b64_tr_b16 a[138:139], v117 offset:8704\n\t"
      "s_waitcnt lgkmcnt(12)\n\t"
      "ds_read_b64_tr_b16 a[140:141], v117 offset:12288\n\t"
      "ds_read_b64_tr_b16 a[142:143], v117 offset:12800\n\t"
      "s_waitcnt lgkmcnt(12)\n\t"
      "ds_read_b64_tr_b16 a[144:145], v117 offset:16384\n\t"
      "ds_read_b64_tr_b16 a[146:147], v117 offset:16896\n\t"
      "s_waitcnt lgkmcnt(12)\n\t"
      "ds_read_b64_tr_b16 a[148:149], v117 offset:20480\n\t"
      "ds_read_b64_tr_b16 a[150:151], v117 offset:20992\n\t"
      "s_waitcnt lgkmcnt(12)\n\t"
      "ds_read_b64_tr_b16 a[152:153], v117 offset:24576\n\t"
      "ds_read_b64_tr_b16 a[154:155], v117 offset:25088\n\t"
      "s_waitcnt lgkmcnt(12)\n\t"
      "ds_read_b64_tr_b16 a[156:157], v117 offset:28672\n\t"
      "ds_read_b64_tr_b16 a[158:159], v117 offset:29184\n\t"
      "s_waitcnt lgkmcnt(12)\n\t"
      "ds_read_b64_tr_b16 a[160:161], v117 offset:32768\n\t"
      "ds_read_b64_tr_b16 a[162:163], v117 offset:33280\n\t"
      "s_waitcnt lgkmcnt(12)\n\t"
      "ds_read_b64_tr_b16 a[164:165], v117 offset:36864\n\t"
      "ds_read_b64_tr_b16 a[166:167], v117 offset:37376\n\t"
      "s_waitcnt lgkmcnt(12)\n\t"
      "ds_read_b64_tr_b16 a[168:169], v117 offset:40960\n\t"
      "ds_read_b64_tr_b16 a[170:171], v117 offset:41472\n\t"
      "s_waitcnt lgkmcnt(12)\n\t"
      "ds_read_b64_tr_b16 a[172:173], v117 offset:45056\n\t"
      "ds_read_b64_tr_b16 a[174:175], v117 offset:45568\n\t"
      "s_waitcnt lgkmcnt(12)\n\t"
      "ds_read_b64_tr_b16 a[176:177], v117 offset:49152\n\t"
      "ds_read_b64_tr_b16 a[178:179], v117 offset:49664\n\t"
      "s_waitcnt lgkmcnt(12)\n\t"
      "ds_read_b64_tr_b16 a[180:181], v117 offset:53248\n\t"
      "ds_read_b64_tr_b16 a[182:183], v117 offset:53760\n\t"
      "s_waitcnt lgkmcnt(12)\n\t"
      "ds_read_b64_tr_b16 a[184:185], v117 offset:57344\n\t"
      "ds_read_b64_tr_b16 a[186:187], v117 offset:57856\n\t"
      "s_waitcnt lgkmcnt(12)\n\t"
      "ds_read_b64_tr_b16 a[188:189], v117 offset:61440\n\t"
      "ds_read_b64_tr_b16 a[190:191], v117 offset:61952\n\t"
      "s_waitcnt lgkmcnt(4)\n\t"
      "GQ%=:\n\t"
      "ds_read_b128 v[0:3], v98\n\t"
      "ds_read_b128 v[4:7], v98 offset:1024\n\t"
      "ds_read_b128 v[8:11], v98 offset:2048\n\t"
      "ds_read_b128 v[12:15], v98 offset:3072\n\t"
      "ds_read_b128 v[16:19], v98 offset:4096\n\t"
      "ds_read_b128 v[20:23], v98 offset:5120\n\t"
      "ds_read_b128 v[24:27], v98 offset:6144\n\t"
      "ds_read_b128 v[28:31], v98 offset:7168\n\t"
      "ds_read_b128 v[32:35], v98 offset:8192\n\t"
      "ds_read_b128 v[36:39], v98 offset:9216\n\t"
      "s_waitcnt lgkmcnt(9)\n\t"
      "v_mfma_f32_16x16x32_bf16 v[88:91], a[128:131], v[0:3], 0\n\t"
      "ds_read_b128 v[40:43], v98 offset:10240\n\t"
      "s_waitcnt lgkmcnt(9)\n\t"
      "v_mfma_f32_16x16x32_bf16 v[88:91], a[132:135], v[4:7], v[88:91]\n\t"
      "ds_read_b128 v[44:47], v98 offset:11264\n\t"
      "s_waitcnt lgkmcnt(9)\n\t"
      "v_mfma_f32_16x16x32_bf16 v[88:91], a[136:139], v[8:11], v[88:91]\n\t"
      "ds_read_b128 v[48:51], v98 offset:12288\n\t"
      "s_waitcnt lgkmcnt(9)\n\t"
      "v_mfma_f32_16x16x32_bf16 v[88:91], a[140:143], v[12:15], v[88:91]\n\t"
      "ds_read_b128 v[52:55], v98 offset:13312\n\t"
      "s_waitcnt lgkmcnt(9)\n\t"
      "v_mfma_f32_16x16x32_bf16 v[88:91], a[144:147], v[16:19], v[88:91]\n\t"
      "ds_read_b128 v[56:59], v98 offset:14336\n\t"
      "s_waitcnt lgkmcnt(9)\n\t"
      "v_mfma_f32_16x16x32_bf16 v[88:91], a[148:151], v[20:23], v[88:91]\n\t"
      "ds_read_b128 v[60:63], v98 offset:15360\n\t"
      "s_waitcnt lgkmcnt(9)\n\t"
      "v_mfma_f32_16x16x32_bf16 v[88:91], a[152:155], v[24:27], v[88:91]\n\t"
      "ds_read_b128 v[0:3], v98 offset:16384\n\t"
      "s_waitcnt lgkmcnt(9)\n\t"
      "v_mfma_f32_16x16x32_bf16 v[88:91], a[156:159], v[28:31], v[88:91]\n\t"
      "ds_read_b128 v[4:7], v98 offset:17408\n\t"
      "s_waitcnt lgkmcnt(9)\n\t"
      "v_mfma_f32_16x16x32_bf16 v[88:91], a[160:163], v[32:35], v[88:91]\n\t"
      "ds_read_b128 v[8:11], v98 offset:18432\n\t"
      "s_waitcnt lgkmcnt(9)\n\t"
      "v_mfma_f32_16x16x32_bf16 v[88:91], a[164:167], v[36:39], v[88:91]\n\t"
      "ds_read_b128 v[12:15], v98 offset:19456\n\t"
      "s_waitcnt lgkmcnt(9)\n\t"
      "v_mfma_f32_16x16x32_bf16 v[88:91], a[168:171], v[40:43], v[88:91]\n\t"
      "ds_read_b128 v[16:19], v98 offset:20480\n\t"
      "s_waitcnt lgkmcnt(9)\n\t"
      "v_mfma_f32_16x16x32_bf16 v[88:91], a[172:175], v[44:47], v[88:91]\n\t"
      "ds_read_b128 v[20:23], v98 offset:21504\n\t"
      "s_waitcnt lgkmcnt(9)\n\t"
      "v_mfma_f32_16x16x32_bf16 v[88:91], a[176:179], v[48:51], v[88:91]\n\t"
      "ds_read_b128 v[24:27], v98 offset:22528\n\t"
      "s_waitcnt lgkmcnt(9)\n\t"
      "v_mfma_f32_16x16x32_bf16 v[88:91], a[180:183], v[52:55], v[88:91]\n\t"
      "ds_read_b128 v[28:31], v98 offset:23552\n\t"
      "s_waitcnt lgkmcnt(9)\n\t"
      "v_mfma_f32_16x16x32_bf16 v[88:91], a[184:187], v[56:59], v[88:91]\n\t"
      "ds_read_b128 v[32:35], v98 offset:24576\n\t"
      "s_waitcnt lgkmcnt(9)\n\t"
      "v_mfma_f32_16x16x32_bf16 v[88:91], a[188:191], v[60:63], v[88:91]\n\t"
      "ds_read_b128 v[36:39], v98 offset:25600\n\t"
      "s_waitcnt lgkmcnt(9)\n\t"
      "v_mfma_f32_16x16x32_bf16 v[92:95], a[128:131], v[0:3], 0\n\t"
      "ds_read_b128 v[40:43], v98 offset:26624\n\t"
      "s_waitcnt lgkmcnt(9)\n\t"
      "v_mfma_f32_16x16x32_bf16 v[92:95], a[132:135], v[4:7], v[92:95]\n\t"
      "ds_read_b128 v[44:47], v98 offset:27648\n\t"
      "s_waitcnt lgkmcnt(9)\n\t"
      "v_mfma_f32_16x16x32_bf16 v[92:95], a[136:139], v[8:11], v[92:95]\n\t"
      "ds_read_b128 v[48:51], v98 offset:28672\n\t"
      "s_waitcnt lgkmcnt(9)\n\t"
      "v_mfma_f32_16x16x32_bf16 v[92:95], a[140:143], v[12:15], v[92:95]\n\t"
      "ds_read_b128 v[52:55], v98 offset:29696\n\t"
      "s_waitcnt lgkmcnt(9)\n\t"
      "v_mfma_f32_16x16x32_bf16 v[92:95], a[144:147], v[16:19], v[92:95]\n\t"
      "ds_read_b128 v[56:59], v98 offset:30720\n\t"
      "s_waitcnt lgkmcnt(9)\n\t"
      "v_mfma_f32_16x16x32_bf16 v[92:95], a[148:151], v[20:23], v[92:95]\n\t"
      "ds_read_b128 v[60:63], v98 offset:31744\n\t"
      "s_waitcnt lgkmcnt(9)\n\t"
      "v_mfma_f32_16x16x32_bf16 v[92:95], a[152:155], v[24:27], v[92:95]\n\t"
      "s_waitcnt lgkmcnt(8)\n\t"
      "v_mfma_f32_16x16x32_bf16 v[92:95], a[156:159], v[28:31], v[92:95]\n\t"
      "s_waitcnt lgkmcnt(7)\n\t"
      "v_mfma_f32_16x16x32_bf16 v[92:95], a[160:163], v[32:35], v[92:95]\n\t"
      "s_waitcnt lgkmcnt(6)\n\t"
      "v_mfma_f32_16x16x32_bf16 v[92:95], a[164:167], v[36:39], v[92:95]\n\t"
      "s_waitcnt lgkmcnt(5)\n\t"
      "v_mfma_f32_16x16x32_bf16 v[92:95], a[168:171], v[40:43], v[92:95]\n\t"
      "s_waitcnt lgkmcnt(4)\n\t"
      "v_mfma_f32_16x16x32_bf16 v[92:95], a[172:175], v[44:47], v[92:95]\n\t"
      "s_waitcnt lgkmcnt(3)\n\t"
      "v_mfma_f32_16x16x32_bf16 v[92:95], a[176:179], v[48:51], v[92:95]\n\t"
      "s_waitcnt lgkmcnt(2)\n\t"
      "v_mfma_f32_16x16x32_bf16 v[92:95], a[180:183], v[52:55], v[92:95]\n\t"
      "s_waitcnt lgkmcnt(1)\n\t"
      "v_mfma_f32_16x16x32_bf16 v[92:95], a[184:187], v[56:59], v[92:95]\n\t"
      "s_waitcnt lgkmcnt(0)\n\t"
      "v_mfma_f32_16x16x32_bf16 v[92:95], a[188:191], v[60:63], v[92:95]\n\t"
      "v_cvt_pk_bf16_f32 v112, v88, v89\n\t"
      "v_cvt_pk_bf16_f32 v113, v90, v91\n\t"
      "s_cmp_eq_u32 s68, 3\n\t"
      "s_waitcnt vmcnt(17)\n\t"
      "s_barrier\n\t"
      "s_cbranch_scc1 GQ1%=\n\t"
      "s_nop 3\n\t"
      "v_cvt_pk_bf16_f32 v114, v92, v93\n\t"
      "v_cvt_pk_bf16_f32 v115, v94, v95\n\t"
      "s_nop 0\n\t"
      "v_permlane16_swap_b32 v112, v114\n\t"
      "v_permlane16_swap_b32 v113, v115\n\t"
      "buffer_store_dwordx4 v[112:115], v111, s[60:63], s77 offen sc1\n\t"
      "s_cmp_lg_u32 s68, 0\n\t"
      "s_cbranch_scc1 GQ2%=\n\t"
      "v_readlane_b32 s72, v101, 3\n\t"
      "s_add_u32 s76, s85, 65536\n\t"
      "s_add_u32 m0, s76, 0\n\t"
      "s_nop 2\n\t"
      "buffer_load_dwordx4 v97, s[60:63], s72 offen sc1 lds\n\t"
      "buffer_load_dwordx4 v97, s[60:63], s72 offen offset:1024 sc1 lds\n\t"
      "buffer_load_dwordx4 v97, s[60:63], s72 offen offset:2048 sc1 lds\n\t"
      "buffer_load_dwordx4 v97, s[60:63], s72 offen offset:3072 sc1 lds\n\t"
      "s_add_u32 m0, s76, 4096\n\t"
      "s_add_u32 s72, s72, 4096\n\t"
      "buffer_load_dwordx4 v97, s[60:63], s72 offen sc1 lds\n\t"
      "buffer_load_dwordx4 v97, s[60:63], s72 offen offset:1024 sc1 lds\n\t"
      "buffer_load_dwordx4 v97, s[60:63], s72 offen offset:2048 sc1 lds\n\t"
      "buffer_load_dwordx4 v97, s[60:63], s72 offen offset:3072 sc1 lds\n\t"
      "v_readlane_b32 s72, v101, 4\n\t"
      "s_add_u32 s76, s85, 98304\n\t"
      "s_add_u32 m0, s76, 0\n\t"
      "s_nop 2\n\t"
      "buffer_load_dwordx4 v97, s[60:63], s72 offen sc1 lds\n\t"
      "buffer_load_dwordx4 v97, s[60:63], s72 offen offset:1024 sc1 lds\n\t"
      "buffer_load_dwordx4 v97, s[60:63], s72 offen offset:2048 sc1 lds\n\t"
      "buffer_load_dwordx4 v97, s[60:63], s72 offen offset:3072 sc1 lds\n\t"
      "s_add_u32 m0, s76, 4096\n\t"
      "s_add_u32 s72, s72, 4096\n\t"
      "buffer_load_dwordx4 v97, s[60:63], s72 offen sc1 lds\n\t"
      "buffer_load_dwordx4 v97, s[60:63], s72 offen offset:1024 sc1 lds\n\t"
      "buffer_load_dwordx4 v97, s[60:63], s72 offen offset:2048 sc1 lds\n\t"
      "buffer_load_dwordx4 v97, s[60:63], s72 offen offset:3072 sc1 lds\n\t"
      "v_mov_b32 v98, v97\n\t"
      "v_readlane_b32 s77, v102, 1\n\t"
      "s_mov_b32 s68, 1\n\t"
      "s_branch GQ%=\n\t"
      "GQ2%=:\n\t"
      "v_readlane_b32 s72, v101, 5\n\t"
      "s_add_u32 s76, s85, 0\n\t"
      "s_add_u32 m0, s76, 0\n\t"
      "s_nop 2\n\t"
      "buffer_load_dwordx4 v97, s[60:63], s72 offen sc1 lds\n\t"
      "buffer_load_dwordx4 v97, s[60:63], s72 offen offset:1024 sc1 lds\n\t"
      "buffer_load_dwordx4 v97, s[60:63], s72 offen offset:2048 sc1 lds\n\t"
      "buffer_load_dwordx4 v97, s[60:63], s72 offen offset:3072 sc1 lds\n\t"
      "s_add_u32 m0, s76, 4096\n\t"
      "s_add_u32 s72, s72, 4096\n\t"
      "buffer_load_dwordx4 v97, s[60:63], s72 offen sc1 lds\n\t"
      "buffer_load_dwordx4 v97, s[60:63], s72 offen offset:1024 sc1 lds\n\t"
      "buffer_load_dwordx4 v97, s[60:63], s72 offen offset:2048 sc1 lds\n\t"
      "buffer_load_dwordx4 v97, s[60:63], s72 offen offset:3072 sc1 lds\n\t"
      "v_add_u32 v98, 0x8000, v97\n\t"
      "s_mov_b32 s68, 3\n\t"
      "s_branch GQ%=\n\t"
      "GQ1%=:\n\t"
      "s_mov_b32 s69, 6\n\t"
      "s_mov_b32 s70, 63\n\t"
      "s_waitcnt vmcnt(9)\n\t"
      "s_mov_b32 s77, 0x10000\n\t"
      "s_mov_b32 s71, 5\n\t"
      "s_getpc_b64 s[88:89]\n\t"
      "GPC%=:\n\t"
      "s_add_u32 s88, s88, GJOBS%=-GPC%=\n\t"
      "s_addc_u32 s89, s89, 0\n\t"
      "v_add_u32 v98, s77, v97\n\t"
      "s_add_u32 s76, s85, 0x8000\n\t"
      "ds_read_b128 v[0:3], v98\n\t"
      "ds_read_b128 v[4:7], v98 offset:1024\n\t"
      "ds_read_b128 v[8:11], v98 offset:2048\n\t"
      "ds_read_b128 v[12:15], v98 offset:3072\n\t"
      "ds_read_b128 v[16:19], v98 offset:4096\n\t"
      "ds_read_b128 v[20:23], v98 offset:5120\n\t"
      "ds_read_b128 v[24:27], v98 offset:6144\n\t"
      "ds_read_b128 v[28:31], v98 offset:7168\n\t"
      "ds_read_b128 v[32:35], v98 offset:8192\n\t"
      "ds_read_b128 v[36:39], v98 offset:9216\n\t"
      ".p2align 6\n\t"
      "GU%=:\n\t"
      "s_cmp_eq_u32 s68, s71\n\t"
      "s_cbranch_scc1 GCALL%=\n\t"
      "GBACK%=:\n\t"
      "s_waitcnt lgkmcnt(9)\n\t"
      "v_mfma_f32_16x16x32_bf16 v[64:67], a[0:3], v[0:3], 0\n\t"
      "ds_read_b128 v[40:43], v98 offset:10240\n\t"
      "v_mfma_f32_16x16x32_bf16 v[68:71], a[4:7], v[0:3], 0\n\t"
      "v_readlane_b32 s72, v101, s69\n\t"
      "v_mfma_f32_16x16x32_bf16 v[88:91], a[128:131], v[0:3], 0\n\t"
      "s_waitcnt lgkmcnt(9)\n\t"
      "v_mfma_f32_16x16x32_bf16 v[64:67], a[8:11], v[4:7], v[64:67]\n\t"
      "ds_read_b128 v[44:47], v98 offset:11264\n\t"
      "v_mfma_f32_16x16x32_bf16 v[68:71], a[12:15], v[4:7], v[68:71]\n\t"
      "v_readlane_b32 s75, v103, s70\n\t"
      "v_mfma_f32_16x16x32_bf16 v[88:91], a[132:135], v[4:7], v[88:91]\n\t"
      "s_waitcnt lgkmcnt(9)\n\t"
      "v_mfma_f32_16x16x32_bf16 v[64:67], a[16:19], v[8:11], v[64:67]\n\t"
      "ds_read_b128 v[48:51], v98 offset:12288\n\t"
      "v_mfma_f32_16x16x32_bf16 v[68:71], a[20:23], v[8:11], v[68:71]\n\t"
      "v_cvt_pk_bf16_f32 v84, v72, v73\n\t"
      "v_cvt_pk_bf16_f32 v85, v74, v75\n\t"
      "v_mfma_f32_16x16x32_bf16 v[88:91], a[136:139], v[8:11], v[88:91]\n\t"
      "v_cvt_pk_bf16_f32 v114, v92, v93\n\t"
      "v_cvt_pk_bf16_f32 v115, v94, v95\n\t"
      "s_waitcnt lgkmcnt(9)\n\t"
      "v_mfma_f32_16x16x32_bf16 v[64:67], a[24:27], v[12:15], v[64:67]\n\t"
      "ds_read_b128 v[52:55], v98 offset:13312\n\t"
      "v_mfma_f32_16x16x32_bf16 v[68:71], a[28:31], v[12:15], v[68:71]\n\t"
      "v_cvt_pk_bf16_f32 v86, v76, v77\n\t"
      "v_cvt_pk_bf16_f32 v87, v78, v79\n\t"
      "v_mfma_f32_16x16x32_bf16 v[88:91], a[140:143], v[12:15], v[88:91]\n\t"
      "v_permlane16_swap_b32 v112, v114\n\t"
      "v_permlane16_swap_b32 v113, v115\n\t"
      "s_waitcnt lgkmcnt(9)\n\t"
      "v_mfma_f32_16x16x32_bf16 v[64:67], a[32:35], v[16:19], v[64:67]\n\t"
      "ds_read_b128 v[56:59], v98 offset:14336\n\t"
      "v_mfma_f32_16x16x32_bf16 v[68:71], a[36:39], v[16:19], v[68:71]\n\t"
      "buffer_store_dwordx4 v[84:87], v97, s[60:63], s73 offen offset:1024 sc1\n\t"
      "buffer_store_dwordx4 v[112:115], v111, s[60:63], s73 offen sc1\n\t"
      "v_mfma_f32_16x16x32_bf16 v[88:91], a[144:147], v[16:19], v[88:91]\n\t"
      "s_add_u32 m0, s76, 0\n\t"
      "buffer_load_dwordx4 v97, s[60:63], s72 offen sc1 lds\n\t"
      "s_waitcnt lgkmcnt(9)\n\t"
      "v_mfma_f32_16x16x32_bf16 v[64:67], a[40:43], v[20:23], v[64:67]\n\t"
      "ds_read_b128 v[60:63], v98 offset:15360\n\t"
      "v_mfma_f32_16x16x32_bf16 v[68:71], a[44:47], v[20:23], v[68:71]\n\t"
      "v_mfma_f32_16x16x32_bf16 v[88:91], a[148:151], v[20:23], v[88:91]\n\t"
      "s_waitcnt lgkmcnt(9)\n\t"
      "v_mfma_f32_16x16x32_bf16 v[64:67], a[48:51], v[24:27], v[64:67]\n\t"
      "ds_read_b128 v[0:3], v98 offset:16384\n\t"
      "v_mfma_f32_16x16x32_bf16 v[68:71], a[52:55], v[24:27], v[68:71]\n\t"
      "v_mfma_f32_16x16x32_bf16 v[88:91], a[152:155], v[24:27], v[88:91]\n\t"
      "s_waitcnt lgkmcnt(9)\n\t"
      "v_mfma_f32_16x16x32_bf16 v[64:67], a[56:59], v[28:31], v[64:67]\n\t"
      "ds_read_b128 v[4:7], v98 offset:17408\n\t"
      "v_mfma_f32_16x16x32_bf16 v[68:71], a[60:63], v[28:31], v[68:71]\n\t"
      "buffer_load_dwordx4 v97, s[60:63], s72 offen offset:1024 sc1 lds\n\t"
      "v_mfma_f32_16x16x32_bf16 v[88:91], a[156:159], v[28:31], v[88:91]\n\t"
      "s_waitcnt lgkmcnt(9)\n\t"
      "v_mfma_f32_16x16x32_bf16 v[64:67], a[64:67], v[32:35], v[64:67]\n\t"
      "ds_read_b128 v[8:11], v98 offset:18432\n\t"
      "v_mfma_f32_16x16x32_bf16 v[68:71], a[68:71], v[32:35], v[68:71]\n\t"
      "v_mfma_f32_16x16x32_bf16 v[88:91], a[160:163], v[32:35], v[88:91]\n\t"
      "s_waitcnt lgkmcnt(9)\n\t"
      "v_mfma_f32_16x16x32_bf16 v[64:67], a[72:75], v[36:39], v[64:67]\n\t"
      "ds_read_b128 v[12:15], v98 offset:19456\n\t"
      "v_mfma_f32_16x16x32_bf16 v[68:71], a[76:79], v[36:39], v[68:71]\n\t"
      "v_readlane_b32 s73, v102, s68\n\t"
      "v_mfma_f32_16x16x32_bf16 v[88:91], a[164:167], v[36:39], v[88:91]\n\t"
      "s_waitcnt lgkmcnt(9)\n\t"
      "v_mfma_f32_16x16x32_bf16 v[64:67], a[80:83], v[40:43], v[64:67]\n\t"
      "ds_read_b128 v[16:19], v98 offset:20480\n\t"
      "v_mfma_f32_16x16x32_bf16 v[68:71], a[84:87], v[40:43], v[68:71]\n\t"
      "buffer_load_dwordx4 v97, s[60:63], s72 offen offset:2048 sc1 lds\n\t"
      "v_mfma_f32_16x16x32_bf16 v[88:91], a[168:171], v[40:43], v[88:91]\n\t"
      "s_waitcnt lgkmcnt(9)\n\t"
      "v_mfma_f32_16x16x32_bf16 v[64:67], a[88:91], v[44:47], v[64:67]\n\t"
      "ds_read_b128 v[20:23], v98 offset:21504\n\t"
      "v_mfma_f32_16x16x32_bf16 v[68:71], a[92:95], v[44:47], v[68:71]\n\t"
      "v_mfma_f32_16x16x32_bf16 v[88:91], a[172:175], v[44:47], v[88:91]\n\t"
      "s_waitcnt lgkmcnt(9)\n\t"
      "v_mfma_f32_16x16x32_bf16 v[64:67], a[96:99], v[48:51], v[64:67]\n\t"
      "ds_read_b128 v[24:27], v98 offset:22528\n\t"
      "v_mfma_f32_16x16x32_bf16 v[68:71], a[100:103], v[48:51], v[68:71]\n\t"
      "v_mfma_f32_16x16x32_bf16 v[88:91], a[176:179], v[48:51], v[88:91]\n\t"
      "s_waitcnt lgkmcnt(9)\n\t"
      "v_mfma_f32_16x16x32_bf16 v[64:67], a[104:107], v[52:55], v[64:67]\n\t"
      "ds_read_b128 v[28:31], v98 offset:23552\n\t"
      "v_mfma_f32_16x16x32_bf16 v[68:71], a[108:111], v[52:55], v[68:71]\n\t"
      "buffer_load_dwordx4 v97, s[60:63], s72 offen offset:3072 sc1 lds\n\t"
      "v_mfma_f32_16x16x32_bf16 v[88:91], a[180:183], v[52:55], v[88:91]\n\t"
      "s_waitcnt lgkmcnt(9)\n\t"
      "v_mfma_f32_16x16x32_bf16 v[64:67], a[112:115], v[56:59], v[64:67]\n\t"
      "ds_read_b128 v[32:35], v98 offset:24576\n\t"
      "v_mfma_f32_16x16x32_bf16 v[68:71], a[116:119], v[56:59], v[68:71]\n\t"
      "v_mfma_f32_16x16x32_bf16 v[88:91], a[184:187], v[56:59], v[88:91]\n\t"
      "s_waitcnt lgkmcnt(9)\n\t"
      "v_mfma_f32_16x16x32_bf16 v[64:67], a[120:123], v[60:63], v[64:67]\n\t"
      "ds_read_b128 v[36:39], v98 offset:25600\n\t"
      "v_mfma_f32_16x16x32_bf16 v[68:71], a[124:127], v[60:63], v[68:71]\n\t"
      "s_add_u32 s68, s68, 1\n\t"
      "v_mfma_f32_16x16x32_bf16 v[88:91], a[188:191], v[60:63], v[88:91]\n\t"
      "s_waitcnt lgkmcnt(9)\n\t"
      "v_mfma_f32_16x16x32_bf16 v[72:75], a[0:3], v[0:3], 0\n\t"
      "ds_read_b128 v[40:43], v98 offset:26624\n\t"
      "v_mfma_f32_16x16x32_bf16 v[76:79], a[4:7], v[0:3], 0\n\t"
      "s_add_u32 m0, s76, 4096\n\t"
      "s_add_u32 s72, s72, 4096\n\t"
      "buffer_load_dwordx4 v97, s[60:63], s72 offen sc1 lds\n\t"
      "v_mfma_f32_16x16x32_bf16 v[92:95], a[128:131], v[0:3], 0\n\t"
      "s_add_u32 s69, s69, 1\n\t"
      "s_add_u32 s70, s70, 1\n\t"
      "s_waitcnt lgkmcnt(9)\n\t"
      "v_mfma_f32_16x16x32_bf16 v[72:75], a[8:11], v[4:7], v[72:75]\n\t"
      "ds_read_b128 v[44:47], v98 offset:27648\n\t"
      "v_mfma_f32_16x16x32_bf16 v[76:79], a[12:15], v[4:7], v[76:79]\n\t"
      "s_cmp_lt_u32 s68, 7\n\t"
      "s_cselect_b32 s70, 1, 3\n\t"
      "v_mfma_f32_16x16x32_bf16 v[92:95], a[132:135], v[4:7], v[92:95]\n\t"
      "s_sub_u32 s70, s68, s70\n\t"
      "s_add_u32 s76, s77, s85\n\t"
      "s_add_u32 s77, s77, 0x8000\n\t"
      "s_and_b32 s77, s77, 0x1ffff\n\t"
      "s_waitcnt lgkmcnt(9)\n\t"
      "v_mfma_f32_16x16x32_bf16 v[72:75], a[16:19], v[8:11], v[72:75]\n\t"
      "ds_read_b128 v[48:51], v98 offset:28672\n\t"
      "v_mfma_f32_16x16x32_bf16 v[76:79], a[20:23], v[8:11], v[76:79]\n\t"
      "v_cvt_pk_bf16_f32 v80, v64, v65\n\t"
      "v_cvt_pk_bf16_f32 v81, v66, v67\n\t"
      "v_mfma_f32_16x16x32_bf16 v[92:95], a[136:139], v[8:11], v[92:95]\n\t"
      "v_cvt_pk_bf16_f32 v112, v88, v89\n\t"
      "s_waitcnt lgkmcnt(9)\n\t"
      "v_mfma_f32_16x16x32_bf16 v[72:75], a[24:27], v[12:15], v[72:75]\n\t"
      "ds_read_b128 v[52:55], v98 offset:29696\n\t"
      "v_mfma_f32_16x16x32_bf16 v[76:79], a[28:31], v[12:15], v[76:79]\n\t"
      "buffer_load_dwordx4 v97, s[60:63], s72 offen offset:1024 sc1 lds\n\t"
      "v_cvt_pk_bf16_f32 v82, v68, v69\n\t"
      "v_mfma_f32_16x16x32_bf16 v[92:95], a[140:143], v[12:15], v[92:95]\n\t"
      "v_cvt_pk_bf16_f32 v83, v70, v71\n\t"
      "v_cvt_pk_bf16_f32 v113, v90, v91\n\t"
      "s_waitcnt lgkmcnt(9)\n\t"
      "v_mfma_f32_16x16x32_bf16 v[72:75], a[32:35], v[16:19], v[72:75]\n\t"
      "ds_read_b128 v[56:59], v98 offset:30720\n\t"
      "v_mfma_f32_16x16x32_bf16 v[76:79], a[36:39], v[16:19], v[76:79]\n\t"
      "buffer_store_dwordx4 v[80:83], v97, s[60:63], s73 offen sc1\n\t"
      "v_mfma_f32_16x16x32_bf16 v[92:95], a[144:147], v[16:19], v[92:95]\n\t"
      "s_waitcnt lgkmcnt(9)\n\t"
      "v_mfma_f32_16x16x32_bf16 v[72:75], a[40:43], v[20:23], v[72:75]\n\t"
      "ds_read_b128 v[60:63], v98 offset:31744\n\t"
      "v_mfma_f32_16x16x32_bf16 v[76:79], a[44:47], v[20:23], v[76:79]\n\t"
      "v_mfma_f32_16x16x32_bf16 v[92:95], a[148:151], v[20:23], v[92:95]\n\t"
      "s_waitcnt lgkmcnt(9)\n\t"
      "v_mfma_f32_16x16x32_bf16 v[72:75], a[48:51], v[24:27], v[72:75]\n\t"
      "v_mfma_f32_16x16x32_bf16 v[76:79], a[52:55], v[24:27], v[76:79]\n\t"
      "buffer_load_dwordx4 v97, s[60:63], s72 offen offset:2048 sc1 lds\n\t"
      "v_add_u32 v98, s77, v97\n\t"
      "v_mfma_f32_16x16x32_bf16 v[92:95], a[152:155], v[24:27], v[92:95]\n\t"
      "s_cmp_lg_u32 s75, 0\n\t"
      "s_waitcnt lgkmcnt(8)\n\t"
      "v_mfma_f32_16x16x32_bf16 v[72:75], a[56:59], v[28:31], v[72:75]\n\t"
      "v_mfma_f32_16x16x32_bf16 v[76:79], a[60:63], v[28:31], v[76:79]\n\t"
      "s_cselect_b64 vcc, s[90:91], 0\n\t"
      "v_mfma_f32_16x16x32_bf16 v[92:95], a[156:159], v[28:31], v[92:95]\n\t"
      "s_waitcnt lgkmcnt(7)\n\t"
      "v_mfma_f32_16x16x32_bf16 v[72:75], a[64:67], v[32:35], v[72:75]\n\t"
      "v_mfma_f32_16x16x32_bf16 v[76:79], a[68:71], v[32:35], v[76:79]\n\t"
      "buffer_load_dwordx4 v97, s[60:63], s72 offen offset:3072 sc1 lds\n\t"
      "v_mfma_f32_16x16x32_bf16 v[92:95], a[160:163], v[32:35], v[92:95]\n\t"
      "s_cmp_gt_u32 s68, 7\n\t"
      "s_waitcnt lgkmcnt(6)\n\t"
      "v_mfma_f32_16x16x32_bf16 v[72:75], a[72:75], v[36:39], v[72:75]\n\t"
      "v_mfma_f32_16x16x32_bf16 v[76:79], a[76:79], v[36:39], v[76:79]\n\t"
      "v_mfma_f32_16x16x32_bf16 v[92:95], a[164:167], v[36:39], v[92:95]\n\t"
      "s_waitcnt vmcnt(22) lgkmcnt(0)\n\t"
      "s_cbranch_scc1 GEF%=\n\t"
      "s_waitcnt vmcnt(9)\n\t"
      "GEF%=:\n\t"
      "s_barrier\n\t"
      "v_mfma_f32_16x16x32_bf16 v[72:75], a[80:83], v[40:43], v[72:75]\n\t"
      "ds_read_b128 v[0:3], v98\n\t"
      "v_mfma_f32_16x16x32_bf16 v[76:79], a[84:87], v[40:43], v[76:79]\n\t"
      "ds_read_b128 v[4:7], v98 offset:1024\n\t"
      "v_mfma_f32_16x16x32_bf16 v[92:95], a[168:171], v[40:43], v[92:95]\n\t"
      "v_mfma_f32_16x16x32_bf16 v[72:75], a[88:91], v[44:47], v[72:75]\n\t"
      "ds_read_b128 v[8:11], v98 offset:2048\n\t"
      "v_mfma_f32_16x16x32_bf16 v[76:79], a[92:95], v[44:47], v[76:79]\n\t"
      "ds_read_b128 v[12:15], v98 offset:3072\n\t"
      "v_mfma_f32_16x16x32_bf16 v[92:95], a[172:175], v[44:47], v[92:95]\n\t"
      "v_mfma_f32_16x16x32_bf16 v[72:75], a[96:99], v[48:51], v[72:75]\n\t"
      "ds_read_b128 v[16:19], v98 offset:4096\n\t"
      "v_mfma_f32_16x16x32_bf16 v[76:79], a[100:103], v[48:51], v[76:79]\n\t"
      "ds_read_b128 v[20:23], v98 offset:5120\n\t"
      "v_mfma_f32_16x16x32_bf16 v[92:95], a[176:179], v[48:51], v[92:95]\n\t"
      "v_mfma_f32_16x16x32_bf16 v[72:75], a[104:107], v[52:55], v[72:75]\n\t"
      "ds_read_b128 v[24:27], v98 offset:6144\n\t"
      "v_mfma_f32_16x16x32_bf16 v[76:79], a[108:111], v[52:55], v[76:79]\n\t"
      "ds_read_b128 v[28:31], v98 offset:7168\n\t"
      "v_mfma_f32_16x16x32_bf16 v[92:95], a[180:183], v[52:55], v[92:95]\n\t"
      "v_mfma_f32_16x16x32_bf16 v[72:75], a[112:115], v[56:59], v[72:75]\n\t"
      "ds_read_b128 v[32:35], v98 offset:8192\n\t"
      "v_mfma_f32_16x16x32_bf16 v[76:79], a[116:119], v[56:59], v[76:79]\n\t"
      "ds_read_b128 v[36:39], v98 offset:9216\n\t"
      "v_mfma_f32_16x16x32_bf16 v[92:95], a[184:187], v[56:59], v[92:95]\n\t"
      "s_cbranch_vccnz GFLAG%=\n\t"
      "GFB%=:\n\t"
      "v_mfma_f32_16x16x32_bf16 v[72:75], a[120:123], v[60:63], v[72:75]\n\t"
      "v_mfma_f32_16x16x32_bf16 v[76:79], a[124:127], v[60:63], v[76:79]\n\t"
      "v_mfma_f32_16x16x32_bf16 v[92:95], a[188:191], v[60:63], v[92:95]\n\t"
      "s_branch GU%=\n\t"
      "GFLAG%=:\n\t"
      "s_mov_b64 exec, 1\n\t"
      "buffer_store_dword v107, off, s[60:63], s75 sc1\n\t"
      "s_mov_b64 exec, -1\n\t"
      "s_branch GFB%=\n\t"
      "GCALL%=:\n\t"
      "s_setpc_b64 s[88:89]\n\t"
      "GJOBS%=:\n\t"
      "s_nop 7\n\t"
      "v_readlane_b32 s75, v103, 3\n\t"
      "s_waitcnt vmcnt(9)\n\t"
      "s_barrier\n\t"
      "s_mov_b64 exec, s[90:91]\n\t"
      "s_nop 1\n\t"
      "buffer_store_dword v107, off, s[60:63], s75 sc1\n\t"
      "s_mov_b64 exec, -1\n\t"
      "s_mov_b32 s71, 6\n\t"
      "s_getpc_b64 s[88:89]\n\t"
      "s_add_u32 s88, s88, 12\n\t"
      "s_addc_u32 s89, s89, 0\n\t"
      "s_branch GBACK%=\n\t"
      "s_bitcmp0_b32 %0, 1\n\t"
      "s_cselect_b64 s[36:37], -1, 0\n\t"
      "s_cmp_lt_u32 s82, 28\n\t"
      "s_cselect_b64 s[38:39], -1, 0\n\t"
      "s_min_u32 s74, s82, 27\n\t"
      "v_lshrrev_b32 v104, 3, v96\n\t"
      "v_and_b32 v105, 7, v96\n\t"
      "s_mov_b32 s78, 3584\n\t"
      "v_mul_u32_u24 v104, s78, v104\n\t"
      "v_lshl_add_u32 v104, v105, 4, v104\n\t"
      "s_lshr_b32 s78, %0, 5\n\t"
      "s_lshl_b32 s78, s78, 2\n\t"
      "s_add_u32 s78, s78, %1\n\t"
      "s_mul_i32 s78, s78, 114688\n\t"
      "s_lshl_b32 s80, s74, 7\n\t"
      "s_add_u32 s80, s80, s78\n\t"
      "s_mov_b64 s[64:65], s[50:51]\n\t"
      "s_add_u32 s78, s80, 0\n\t"
      "buffer_load_dwordx4 v[128:131], v104, s[64:67], s78 offen \n\t"
      "s_add_u32 s78, s80, 28672\n\t"
      "buffer_load_dwordx4 v[132:135], v104, s[64:67], s78 offen \n\t"
      "s_add_u32 s78, s80, 57344\n\t"
      "buffer_load_dwordx4 v[136:139], v104, s[64:67], s78 offen \n\t"
      "s_add_u32 s71, s71, 1\n\t"
      "s_getpc_b64 s[88:89]\n\t"
      "s_add_u32 s88, s88, 12\n\t"
      "s_addc_u32 s89, s89, 0\n\t"
      "s_branch GBACK%=\n\t"
      "s_add_u32 s78, s80, 86016\n\t"
      "buffer_load_dwordx4 v[140:143], v104, s[64:67], s78 offen \n\t"
      "s_mov_b64 s[64:65], s[52:53]\n\t"
      "s_add_u32 s78, s80, 0\n\t"
      "buffer_load_dwordx4 v[144:147], v104, s[64:67], s78 offen \n\t"
      "s_add_u32 s78, s80, 28672\n\t"
      "buffer_load_dwordx4 v[148:151], v104, s[64:67], s78 offen \n\t"
      "s_add_u32 s71, s71, 1\n\t"
      "s_getpc_b64 s[88:89]\n\t"
      "s_add_u32 s88, s88, 12\n\t"
      "s_addc_u32 s89, s89, 0\n\t"
      "s_branch GBACK%=\n\t"
      "s_add_u32 s78, s80, 57344\n\t"
      "buffer_load_dwordx4 v[152:155], v104, s[64:67], s78 offen \n\t"
      "s_add_u32 s78, s80, 86016\n\t"
      "buffer_load_dwordx4 v[156:159], v104, s[64:67], s78 offen \n\t"
      "v_mov_b32 v120, -1\n\t"
      "v_mov_b32 v121, -1\n\t"
      "v_mov_b32 v122, -1\n\t"
      "v_mov_b32 v123, -1\n\t"
      "s_lshr_b32 s78, %0, 5\n\t"
      "s_lshl_b32 s78, s78, 2\n\t"
      "s_add_u32 s78, s78, %1\n\t"
      "s_lshr_b32 s79, s78, 1\n\t"
      "s_lshl_b32 s79, s79, 16\n\t"
      "s_lshl_b32 s80, s82, 11\n\t"
      "s_add_u32 s79, s79, s80\n\t"
      "s_and_b32 s78, s78, 1\n\t"
      "s_lshl_b32 s78, s78, 10\n\t"
      "s_add_u32 s79, s79, s78\n\t"
      "s_add_u32 s79, s79, 66813952\n\t"
      "buffer_store_dwordx4 v[120:123], v97, s[60:63], s79 offen sc1\n\t"
      "s_add_u32 s71, s71, 1\n\t"
      "s_getpc_b64 s[88:89]\n\t"
      "s_add_u32 s88, s88, 12\n\t"
      "s_addc_u32 s89, s89, 0\n\t"
      "s_branch GBACK%=\n\t"
      "s_sub_u32 s79, s79, 4194304\n\t"
      "buffer_store_dwordx4 v[120:123], v97, s[60:63], s79 offen sc1\n\t"
      "s_add_u32 s79, s79, 3670016\n\t"
      "buffer_store_dwordx4 v[120:123], v97, s[60:63], s79 offen sc1\n\t"
      "s_sub_u32 s79, s79, 4194304\n\t"
      "buffer_store_dwordx4 v[120:123], v97, s[60:63], s79 offen sc1\n\t"
      "s_add_u32 s71, s71, 1\n\t"
      "s_getpc_b64 s[88:89]\n\t"
      "s_add_u32 s88, s88, 12\n\t"
      "s_addc_u32 s89, s89, 0\n\t"
      "s_branch GBACK%=\n\t"
      "s_add_u32 s79, s79, 3670016\n\t"
      "buffer_store_dwordx4 v[120:123], v97, s[60:63], s79 offen sc1\n\t"
      "s_sub_u32 s79, s79, 4194304\n\t"
      "buffer_store_dwordx4 v[120:123], v97, s[60:63], s79 offen sc1\n\t"
      "s_mov_b64 s[64:65], s[54:55]\n\t"
      "v_lshrrev_b32 v104, 5, v96\n\t"
      "v_and_b32 v105, 31, v96\n\t"
      "v_lshlrev_b32 v104, 10, v104\n\t"
      "v_lshl_add_u32 v104, v105, 4, v104\n\t"
      "s_andn2_b32 s78, %0, 3\n\t"
      "s_add_u32 s78, s78, %1\n\t"
      "s_mul_i32 s78, s78, 14336\n\t"
      "s_and_b32 s79, %0, 1\n\t"
      "s_lshl_b32 s79, s79, 9\n\t"
      "s_add_u32 s78, s78, s79\n\t"
      "s_mov_b64 exec, s[36:37]\n\t"
      "buffer_load_dwordx4 v[160:163], v104, s[64:67], s78 offen \n\t"
      "s_mov_b64 exec, -1\n\t"
      "s_add_u32 s71, s71, 1\n\t"
      "s_getpc_b64 s[88:89]\n\t"
      "s_add_u32 s88, s88, 12\n\t"
      "s_addc_u32 s89, s89, 0\n\t"
      "s_branch GBACK%=\n\t"
      "s_mov_b64 exec, s[36:37]\n\t"
      "buffer_load_dwordx4 v[164:167], v104, s[64:67], s78 offen offset:2048 \n\t"
      "s_add_u32 s78, s78, 4096\n\t"
      "buffer_load_dwordx4 v[168:171], v104, s[64:67], s78 offen \n\t"
      "buffer_load_dwordx4 v[172:175], v104, s[64:67], s78 offen offset:2048 \n\t"
      "s_mov_b64 exec, -1\n\t"
      "s_add_u32 s71, s71, 1\n\t"
      "s_getpc_b64 s[88:89]\n\t"
      "s_add_u32 s88, s88, 12\n\t"
      "s_addc_u32 s89, s89, 0\n\t"
      "s_branch GBACK%=\n\t"
      "s_mov_b64 exec, s[36:37]\n\t"
      "s_add_u32 s78, s78, 4096\n\t"
      "buffer_load_dwordx4 v[176:179], v104, s[64:67], s78 offen \n\t"
      "buffer_load_dwordx4 v[180:183], v104, s[64:67], s78 offen offset:2048 \n\t"
      "s_add_u32 s78, s78, 4096\n\t"
      "buffer_load_dwordx4 v[184:187], v104, s[64:67], s78 offen \n\t"
      "s_mov_b64 exec, -1\n\t"
      "s_add_u32 s71, s71, 1\n\t"
      "s_getpc_b64 s[88:89]\n\t"
      "s_add_u32 s88, s88, 12\n\t"
      "s_addc_u32 s89, s89, 0\n\t"
      "s_branch GBACK%=\n\t"
      "s_mov_b64 exec, s[36:37]\n\t"
      "s_mov_b64 exec, -1\n\t"
      "v_and_b32 v106, 3, v96\n\t"
      "v_mul_u32_u24 v106, 24, v106\n\t"
      "v_lshlrev_b32 v104, 1, v100\n\t"
      "v_sub_u32 v106, v104, v106\n\t"
      "v_add_u32 v104, s76, v97\n\t"
      "v_add_u32 v105, s76, v106\n\t"
      "s_waitcnt vmcnt(30) lgkmcnt(0)\n\t"
      "ds_write_b128 v104, v[128:131]\n\t"
      "ds_write_b128 v104, v[132:135] offset:1024\n\t"
      "ds_write_b128 v104, v[136:139] offset:2048\n\t"
      "ds_write_b128 v104, v[140:143] offset:3072\n\t"
      "ds_write_b128 v104, v[144:147] offset:4096\n\t"
      "ds_write_b128 v104, v[148:151] offset:5120\n\t"
      "ds_write_b128 v104, v[152:155] offset:6144\n\t"
      "ds_write_b128 v104, v[156:159] offset:7168\n\t"
      "s_lshr_b32 s78, %0, 5\n\t"
      "s_lshl_b32 s78, s78, 2\n\t"
      "s_add_u32 s78, s78, %1\n\t"
      "s_lshl_b32 s78, s78, 10\n\t"
      "s_lshl_b32 s79, s74, 17\n\t"
      "s_add_u32 s78, s78, s79\n\t"
      "s_add_u32 s78, s78, 105086976\n\t"
      "s_waitcnt lgkmcnt(6)\n\t"
      "ds_read_b64_tr_b16 v[128:129], v105\n\t"
      "ds_read_b64_tr_b16 v[130:131], v105 offset:512\n\t"
      "ds_read_b64_tr_b16 v[132:133], v105 offset:4096\n\t"
      "ds_read_b64_tr_b16 v[134:135], v105 offset:4608\n\t"
      "ds_read_b64_tr_b16 v[136:137], v105 offset:32\n\t"
      "ds_read_b64_tr_b16 v[138:139], v105 offset:544\n\t"
      "ds_read_b64_tr_b16 v[140:141], v105 offset:4128\n\t"
      "ds_read_b64_tr_b16 v[142:143], v105 offset:4640\n\t"
      "s_waitcnt lgkmcnt(6)\n\t"
      "ds_read_b64_tr_b16 v[144:145], v105 offset:64\n\t"
      "ds_read_b64_tr_b16 v[146:147], v105 offset:576\n\t"
      "ds_read_b64_tr_b16 v[148:149], v105 offset:4160\n\t"
      "ds_read_b64_tr_b16 v[150:151], v105 offset:4672\n\t"
      "ds_read_b64_tr_b16 v[152:153], v105 offset:96\n\t"
      "ds_read_b64_tr_b16 v[154:155], v105 offset:608\n\t"
      "ds_read_b64_tr_b16 v[156:157], v105 offset:4192\n\t"
      "ds_read_b64_tr_b16 v[158:159], v105 offset:4704\n\t"
      "s_waitcnt lgkmcnt(0)\n\t"
      "s_mov_b64 exec, s[38:39]\n\t"
      "buffer_store_dwordx4 v[128:131], v97, s[60:63], s78 offen sc1\n\t"
      "s_add_u32 s78, s78, 16384\n\t"
      "buffer_store_dwordx4 v[132:135], v97, s[60:63], s78 offen sc1\n\t"
      "s_add_u32 s78, s78, 16384\n\t"
      "buffer_store_dwordx4 v[136:139], v97, s[60:63], s78 offen sc1\n\t"
      "s_mov_b64 exec, -1\n\t"
      "s_add_u32 s71, s71, 1\n\t"
      "s_getpc_b64 s[88:89]\n\t"
      "s_add_u32 s88, s88, 12\n\t"
      "s_addc_u32 s89, s89, 0\n\t"
      "s_branch GBACK%=\n\t"
      "s_mov_b64 exec, s[38:39]\n\t"
      "s_add_u32 s78, s78, 16384\n\t"
      "buffer_store_dwordx4 v[140:143], v97, s[60:63], s78 offen sc1\n\t"
      "s_add_u32 s78, s78, 16384\n\t"
      "buffer_store_dwordx4 v[144:147], v97, s[60:63], s78 offen sc1\n\t"
      "s_add_u32 s78, s78, 16384\n\t"
      "buffer_store_dwordx4 v[148:151], v97, s[60:63], s78 offen sc1\n\t"
      "s_mov_b64 exec, -1\n\t"
      "s_add_u32 s71, s71, 1\n\t"
      "s_getpc_b64 s[88:89]\n\t"
      "s_add_u32 s88, s88, 12\n\t"
      "s_addc_u32 s89, s89, 0\n\t"
      "s_branch GBACK%=\n\t"
      "s_mov_b64 exec, s[38:39]\n\t"
      "s_add_u32 s78, s78, 16384\n\t"
      "buffer_store_dwordx4 v[152:155], v97, s[60:63], s78 offen sc1\n\t"
      "s_add_u32 s78, s78, 16384\n\t"
      "buffer_store_dwordx4 v[156:159], v97, s[60:63], s78 offen sc1\n\t"
      "s_add_u32 s78, s78, 16384\n\t"
      "s_mov_b64 exec, -1\n\t"
      "v_lshlrev_b32 v104, 10, v96\n\t"
      "s_and_b32 s78, %0, 1\n\t"
      "s_lshl_b32 s78, s78, 5\n\t"
      "s_lshr_b32 s79, %0, 2\n\t"
      "s_add_u32 s78, s78, s79\n\t"
      "s_lshl_b32 s78, s78, 4\n\t"
      "s_lshl_b32 s79, %1, 16\n\t"
      "s_add_u32 s78, s78, s79\n\t"
      "s_lshl_b32 s79, %1, 10\n\t"
      "s_mov_b64 exec, s[36:37]\n\t"
      "s_add_u32 m0, s79, 131072\n\t"
      "s_nop 0\n\t"
      "buffer_load_dwordx4 v104, s[64:67], s78 offen lds\n\t"
      "s_mov_b64 exec, -1\n\t"
      "s_add_u32 s71, s71, 1\n\t"
      "s_getpc_b64 s[88:89]\n\t"
      "s_add_u32 s88, s88, 12\n\t"
      "s_addc_u32 s89, s89, 0\n\t"
      "s_branch GBACK%=\n\t"
      "s_mov_b64 exec, s[36:37]\n\t"
      "s_add_u32 s78, s78, 0x40000\n\t"
      "s_add_u32 m0, s79, 135168\n\t"
      "s_nop 0\n\t"
      "buffer_load_dwordx4 v104, s[64:67], s78 offen lds\n\t"
      "s_add_u32 s78, s78, 0x40000\n\t"
      "s_add_u32 m0, s79, 139264\n\t"
      "s_nop 0\n\t"
      "buffer_load_dwordx4 v104, s[64:67], s78 offen lds\n\t"
      "s_add_u32 s78, s78, 0x40000\n\t"
      "s_add_u32 m0, s79, 143360\n\t"
      "s_nop 0\n\t"
      "buffer_load_dwordx4 v104, s[64:67], s78 offen lds\n\t"
      "s_mov_b64 exec, -1\n\t"
      "s_add_u32 s71, s71, 1\n\t"
      "s_getpc_b64 s[88:89]\n\t"
      "s_add_u32 s88, s88, 12\n\t"
      "s_addc_u32 s89, s89, 0\n\t"
      "s_branch GBACK%=\n\t"
      "s_mov_b64 exec, s[36:37]\n\t"
      "s_add_u32 s78, s78, 0x40000\n\t"
      "s_add_u32 m0, s79, 147456\n\t"
      "s_nop 0\n\t"
      "buffer_load_dwordx4 v104, s[64:67], s78 offen lds\n\t"
      "s_add_u32 s78, s78, 0x40000\n\t"
      "s_add_u32 m0, s79, 151552\n\t"
      "s_nop 0\n\t"
      "buffer_load_dwordx4 v104, s[64:67], s78 offen lds\n\t"
      "s_add_u32 s78, s78, 0x40000\n\t"
      "s_add_u32 m0, s79, 155648\n\t"
      "s_nop 0\n\t"
      "buffer_load_dwordx4 v104, s[64:67], s78 offen lds\n\t"
      "s_mov_b64 exec, -1\n\t"
      "s_add_u32 s71, s71, 1\n\t"
      "s_getpc_b64 s[88:89]\n\t"
      "s_add_u32 s88, s88, 12\n\t"
      "s_addc_u32 s89, s89, 0\n\t"
      "s_branch GBACK%=\n\t"
      "s_mov_b64 exec, s[36:37]\n\t"
      "s_add_u32 s78, s78, 0x40000\n\t"
      "s_mov_b64 exec, -1\n\t"
      "s_mov_b64 s[64:65], s[46:47]\n\t"
      "v_lshrrev_b32 v99, 2, v96\n\t"
      "v_and_b32 v106, 3, v96\n\t"
      "v_lshlrev_b32 v99, 10, v99\n\t"
      "v_lshl_add_u32 v99, v106, 4, v99\n\t"
      "s_lshr_b32 s78, %0, 4\n\t"
      "s_lshl_b32 s78, s78, 19\n\t"
      "s_bfe_u32 s79, %0, 0x20002\n\t"
      "s_lshl_b32 s79, s79, 8\n\t"
      "s_add_u32 s78, s78, s79\n\t"
      "s_lshl_b32 s79, %1, 6\n\t"
      "s_add_u32 s78, s78, s79\n\t"
      "v_add_u32 v99, s78, v99\n\t"
      "s_mov_b32 s78, 262144\n\t"
      "buffer_load_dwordx4 v[192:195], v99, s[64:67], s78 offen \n\t"
      "s_mov_b32 s78, 278528\n\t"
      "buffer_load_dwordx4 v[196:199], v99, s[64:67], s78 offen \n\t"
      "s_mov_b32 s78, 294912\n\t"
      "buffer_load_dwordx4 v[200:203], v99, s[64:67], s78 offen \n\t"
      "s_add_u32 s71, s71, 1\n\t"
      "s_getpc_b64 s[88:89]\n\t"
      "s_add_u32 s88, s88, 12\n\t"
      "s_addc_u32 s89, s89, 0\n\t"
      "s_branch GBACK%=\n\t"
      "s_mov_b32 s78, 311296\n\t"
      "buffer_load_dwordx4 v[204:207], v99, s[64:67], s78 offen \n\t"
      "s_mov_b32 s78, 327680\n\t"
      "buffer_load_dwordx4 v[208:211], v99, s[64:67], s78 offen \n\t"
      "s_mov_b32 s78, 344064\n\t"
      "buffer_load_dwordx4 v[212:215], v99, s[64:67], s78 offen \n\t"
      "s_add_u32 s71, s71, 1\n\t"
      "s_getpc_b64 s[88:89]\n\t"
      "s_add_u32 s88, s88, 12\n\t"
      "s_addc_u32 s89, s89, 0\n\t"
      "s_branch GBACK%=\n\t"
      "s_mov_b32 s78, 360448\n\t"
      "buffer_load_dwordx4 v[216:219], v99, s[64:67], s78 offen \n\t"
      "s_mov_b32 s78, 376832\n\t"
      "buffer_load_dwordx4 v[220:223], v99, s[64:67], s78 offen \n\t"
      "s_mov_b32 s78, 393216\n\t"
      "buffer_load_dwordx4 v[224:227], v99, s[64:67], s78 offen \n\t"
      "s_add_u32 s71, s71, 1\n\t"
      "s_getpc_b64 s[88:89]\n\t"
      "s_add_u32 s88, s88, 12\n\t"
      "s_addc_u32 s89, s89, 0\n\t"
      "s_branch GBACK%=\n\t"
      "s_mov_b32 s78, 409600\n\t"
      "buffer_load_dwordx4 v[228:231], v99, s[64:67], s78 offen \n\t"
      "s_mov_b32 s78, 425984\n\t"
      "buffer_load_dwordx4 v[232:235], v99, s[64:67], s78 offen \n\t"
      "s_mov_b32 s78, 442368\n\t"
      "buffer_load_dwordx4 v[236:239], v99, s[64:67], s78 offen \n\t"
      "s_add_u32 s71, s71, 1\n\t"
      "s_getpc_b64 s[88:89]\n\t"
      "s_add_u32 s88, s88, 12\n\t"
      "s_addc_u32 s89, s89, 0\n\t"
      "s_branch GBACK%=\n\t"
      "s_mov_b32 s78, 458752\n\t"
      "buffer_load_dwordx4 v[240:243], v99, s[64:67], s78 offen \n\t"
      "s_mov_b32 s78, 475136\n\t"
      "buffer_load_dwordx4 v[244:247], v99, s[64:67], s78 offen \n\t"
      "s_mov_b32 s78, 491520\n\t"
      "buffer_load_dwordx4 v[248:251], v99, s[64:67], s78 offen \n\t"
      "s_add_u32 s71, s71, 1\n\t"
      "s_getpc_b64 s[88:89]\n\t"
      "s_add_u32 s88, s88, 12\n\t"
      "s_addc_u32 s89, s89, 0\n\t"
      "s_branch GBACK%=\n\t"
      "s_mov_b32 s78, 507904\n\t"
      "buffer_load_dwordx4 v[252:255], v99, s[64:67], s78 offen \n\t"
      "s_mov_b32 s78, 0\n\t"
      "buffer_load_dwordx4 v[128:131], v99, s[64:67], s78 offen \n\t"
      "s_mov_b32 s78, 16384\n\t"
      "buffer_load_dwordx4 v[132:135], v99, s[64:67], s78 offen \n\t"
      "s_add_u32 s71, s71, 1\n\t"
      "s_getpc_b64 s[88:89]\n\t"
      "s_add_u32 s88, s88, 12\n\t"
      "s_addc_u32 s89, s89, 0\n\t"
      "s_branch GBACK%=\n\t"
      "s_mov_b32 s78, 32768\n\t"
      "buffer_load_dwordx4 v[136:139], v99, s[64:67], s78 offen \n\t"
      "s_mov_b32 s78, 49152\n\t"
      "buffer_load_dwordx4 v[140:143], v99, s[64:67], s78 offen \n\t"
      "s_mov_b32 s78, 65536\n\t"
      "buffer_load_dwordx4 v[144:147], v99, s[64:67], s78 offen \n\t"
      "s_add_u32 s71, s71, 1\n\t"
      "s_getpc_b64 s[88:89]\n\t"
      "s_add_u32 s88, s88, 12\n\t"
      "s_addc_u32 s89, s89, 0\n\t"
      "s_branch GBACK%=\n\t"
      "s_mov_b32 s78, 81920\n\t"
      "buffer_load_dwordx4 v[148:151], v99, s[64:67], s78 offen \n\t"
      "s_mov_b32 s78, 98304\n\t"
      "buffer_load_dwordx4 v[152:155], v99, s[64:67], s78 offen \n\t"
      "s_mov_b32 s78, 114688\n\t"
      "buffer_load_dwordx4 v[156:159], v99, s[64:67], s78 offen \n\t"
      "s_add_u32 s71, s71, 1\n\t"
      "s_getpc_b64 s[88:89]\n\t"
      "s_add_u32 s88, s88, 12\n\t"
      "s_addc_u32 s89, s89, 0\n\t"
      "s_branch GBACK%=\n\t"
      "s_mov_b32 s78, 131072\n\t"
      "buffer_load_dwordx4 v[160:163], v99, s[64:67], s78 offen \n\t"
      "s_mov_b32 s78, 147456\n\t"
      "buffer_load_dwordx4 v[164:167], v99, s[64:67], s78 offen \n\t"
      "s_mov_b32 s78, 163840\n\t"
      "buffer_load_dwordx4 v[168:171], v99, s[64:67], s78 offen \n\t"
      "s_add_u32 s71, s71, 1\n\t"
      "s_getpc_b64 s[88:89]\n\t"
      "s_add_u32 s88, s88, 12\n\t"
      "s_addc_u32 s89, s89, 0\n\t"
      "s_branch GBACK%=\n\t"
      "s_mov_b32 s78, 180224\n\t"
      "buffer_load_dwordx4 v[172:175], v99, s[64:67], s78 offen \n\t"
      "s_mov_b32 s78, 196608\n\t"
      "buffer_load_dwordx4 v[176:179], v99, s[64:67], s78 offen \n\t"
      "s_mov_b32 s78, 212992\n\t"
      "buffer_load_dwordx4 v[180:183], v99, s[64:67], s78 offen \n\t"
      "s_add_u32 s71, s71, 1\n\t"
      "s_getpc_b64 s[88:89]\n\t"
      "s_add_u32 s88, s88, 12\n\t"
      "s_addc_u32 s89, s89, 0\n\t"
      "s_branch GBACK%=\n\t"
      "s_mov_b32 s78, 229376\n\t"
      "buffer_load_dwordx4 v[184:187], v99, s[64:67], s78 offen \n\t"
      "s_mov_b32 s78, 245760\n\t"
      "buffer_load_dwordx4 v[188:191], v99, s[64:67], s78 offen \n\t"
      "s_mov_b32 s71, 32\n\t"
      "s_getpc_b64 s[88:89]\n\t"
      "s_add_u32 s88, s88, 12\n\t"
      "s_addc_u32 s89, s89, 0\n\t"
      "s_branch GBACK%=\n\t"
      "s_nop 7\n\t"
      "v_add_u32 v104, s76, v97\n\t"
      "v_add_u32 v105, s76, v100\n\t"
      "s_sub_u32 s68, s68, 1\n\t"
      "s_waitcnt vmcnt(11) lgkmcnt(0)\n\t"
      "v_cvt_pk_bf16_f32 v84, v72, v73\n\t"
      "v_cvt_pk_bf16_f32 v85, v74, v75\n\t"
      "v_cvt_pk_bf16_f32 v86, v76, v77\n\t"
      "v_cvt_pk_bf16_f32 v87, v78, v79\n\t"
      "buffer_store_dwordx4 v[84:87], v97, s[60:63], s73 offen offset:1024 sc1\n\t"
      "v_cvt_pk_bf16_f32 v114, v92, v93\n\t"
      "v_cvt_pk_bf16_f32 v115, v94, v95\n\t"
      "s_nop 0\n\t"
      "v_permlane16_swap_b32 v112, v114\n\t"
      "v_permlane16_swap_b32 v113, v115\n\t"
      "buffer_store_dwordx4 v[112:115], v111, s[60:63], s73 offen sc1\n\t"
      "v_readlane_b32 s75, v103, s68\n\t"
      "s_waitcnt lgkmcnt(6)\n\t"
      "ds_write_b128 v104, v[128:131]\n\t"
      "ds_write_b128 v104, v[132:135] offset:1024\n\t"
      "ds_read_b64_tr_b16 a[0:1], v105\n\t"
      "ds_read_b64_tr_b16 a[2:3], v105 offset:256\n\t"
      "ds_read_b64_tr_b16 a[64:65], v105 offset:8\n\t"
      "ds_read_b64_tr_b16 a[66:67], v105 offset:264\n\t"
      "s_waitcnt lgkmcnt(6)\n\t"
      "ds_write_b128 v104, v[136:139] offset:2048\n\t"
      "ds_write_b128 v104, v[140:143] offset:3072\n\t"
      "ds_read_b64_tr_b16 a[4:5], v105 offset:2048\n\t"
      "ds_read_b64_tr_b16 a[6:7], v105 offset:2304\n\t"
      "ds_read_b64_tr_b16 a[68:69], v105 offset:2056\n\t"
      "ds_read_b64_tr_b16 a[70:71], v105 offset:2312\n\t"
      "s_waitcnt lgkmcnt(6)\n\t"
      "ds_write_b128 v104, v[144:147] offset:4096\n\t"
      "ds_write_b128 v104, v[148:151] offset:5120\n\t"
      "ds_read_b64_tr_b16 a[8:9], v105 offset:4096\n\t"
      "ds_read_b64_tr_b16 a[10:11], v105 offset:4352\n\t"
      "ds_read_b64_tr_b16 a[72:73], v105 offset:4104\n\t"
      "ds_read_b64_tr_b16 a[74:75], v105 offset:4360\n\t"
      "s_waitcnt lgkmcnt(6)\n\t"
      "ds_write_b128 v104, v[152:155] offset:6144\n\t"
      "ds_write_b128 v104, v[156:159] offset:7168\n\t"
      "ds_read_b64_tr_b16 a[12:13], v105 offset:6144\n\t"
      "ds_read_b64_tr_b16 a[14:15], v105 offset:6400\n\t"
      "ds_read_b64_tr_b16 a[76:77], v105 offset:6152\n\t"
      "ds_read_b64_tr_b16 a[78:79], v105 offset:6408\n\t"
      "s_waitcnt lgkmcnt(6)\n\t"
      "ds_write_b128 v104, v[160:163]\n\t"
      "ds_write_b128 v104, v[164:167] offset:1024\n\t"
      "ds_read_b64_tr_b16 a[16:17], v105\n\t"
      "ds_read_b64_tr_b16 a[18:19], v105 offset:256\n\t"
      "ds_read_b64_tr_b16 a[80:81], v105 offset:8\n\t"
      "ds_read_b64_tr_b16 a[82:83], v105 offset:264\n\t"
      "s_waitcnt lgkmcnt(6)\n\t"
      "ds_write_b128 v104, v[168:171] offset:2048\n\t"
      "ds_write_b128 v104, v[172:175] offset:3072\n\t"
      "ds_read_b64_tr_b16 a[20:21], v105 offset:2048\n\t"
      "ds_read_b64_tr_b16 a[22:23], v105 offset:2304\n\t"
      "ds_read_b64_tr_b16 a[84:85], v105 offset:2056\n\t"
      "ds_read_b64_tr_b16 a[86:87], v105 offset:2312\n\t"
      "s_waitcnt lgkmcnt(6)\n\t"
      "ds_write_b128 v104, v[176:179] offset:4096\n\t"
      "ds_write_b128 v104, v[180:183] offset:5120\n\t"
      "ds_read_b64_tr_b16 a[24:25], v105 offset:4096\n\t"
      "ds_read_b64_tr_b16 a[26:27], v105 offset:4352\n\t"
      "ds_read_b64_tr_b16 a[88:89], v105 offset:4104\n\t"
      "ds_read_b64_tr_b16 a[90:91], v105 offset:4360\n\t"
      "s_waitcnt lgkmcnt(6)\n\t"
      "ds_write_b128 v104, v[184:187] offset:6144\n\t"
      "ds_write_b128 v104, v[188:191] offset:7168\n\t"
      "ds_read_b64_tr_b16 a[28:29], v105 offset:6144\n\t"
      "ds_read_b64_tr_b16 a[30:31], v105 offset:6400\n\t"
      "ds_read_b64_tr_b16 a[92:93], v105 offset:6152\n\t"
      "ds_read_b64_tr_b16 a[94:95], v105 offset:6408\n\t"
      "s_waitcnt lgkmcnt(6)\n\t"
      "ds_write_b128 v104, v[192:195]\n\t"
      "ds_write_b128 v104, v[196:199] offset:1024\n\t"
      "ds_read_b64_tr_b16 a[32:33], v105\n\t"
      "ds_read_b64_tr_b16 a[34:35], v105 offset:256\n\t"
      "ds_read_b64_tr_b16 a[96:97], v105 offset:8\n\t"
      "ds_read_b64_tr_b16 a[98:99], v105 offset:264\n\t"
      "s_waitcnt lgkmcnt(6)\n\t"
      "ds_write_b128 v104, v[200:203] offset:2048\n\t"
      "ds_write_b128 v104, v[204:207] offset:3072\n\t"
      "ds_read_b64_tr_b16 a[36:37], v105 offset:2048\n\t"
      "ds_read_b64_tr_b16 a[38:39], v105 offset:2304\n\t"
      "ds_read_b64_tr_b16 a[100:101], v105 offset:2056\n\t"
      "ds_read_b64_tr_b16 a[102:103], v105 offset:2312\n\t"
      "s_waitcnt lgkmcnt(6)\n\t"
      "ds_write_b128 v104, v[208:211] offset:4096\n\t"
      "ds_write_b128 v104, v[212:215] offset:5120\n\t"
      "ds_read_b64_tr_b16 a[40:41], v105 offset:4096\n\t"
      "ds_read_b64_tr_b16 a[42:43], v105 offset:4352\n\t"
      "ds_read_b64_tr_b16 a[104:105], v105 offset:4104\n\t"
      "ds_read_b64_tr_b16 a[106:107], v105 offset:4360\n\t"
      "s_waitcnt lgkmcnt(6)\n\t"
      "ds_write_b128 v104, v[216:219] offset:6144\n\t"
      "ds_write_b128 v104, v[220:223] offset:7168\n\t"
      "ds_read_b64_tr_b16 a[44:45], v105 offset:6144\n\t"
      "ds_read_b64_tr_b16 a[46:47], v105 offset:6400\n\t"
      "ds_read_b64_tr_b16 a[108:109], v105 offset:6152\n\t"
      "ds_read_b64_tr_b16 a[110:111], v105 offset:6408\n\t"
      "s_waitcnt lgkmcnt(6)\n\t"
      "ds_write_b128 v104, v[224:227]\n\t"
      "ds_write_b128 v104, v[228:231] offset:1024\n\t"
      "ds_read_b64_tr_b16 a[48:49], v105\n\t"
      "ds_read_b64_tr_b16 a[50:51], v105 offset:256\n\t"
      "ds_read_b64_tr_b16 a[112:113], v105 offset:8\n\t"
      "ds_read_b64_tr_b16 a[114:115], v105 offset:264\n\t"
      "s_waitcnt lgkmcnt(6)\n\t"
      "ds_write_b128 v104, v[232:235] offset:2048\n\t"
      "ds_write_b128 v104, v[236:239] offset:3072\n\t"
      "ds_read_b64_tr_b16 a[52:53], v105 offset:2048\n\t"
      "ds_read_b64_tr_b16 a[54:55], v105 offset:2304\n\t"
      "ds_read_b64_tr_b16 a[116:117], v105 offset:2056\n\t"
      "ds_read_b64_tr_b16 a[118:119], v105 offset:2312\n\t"
      "s_waitcnt lgkmcnt(6)\n\t"
      "ds_write_b128 v104, v[240:243] offset:4096\n\t"
      "ds_write_b128 v104, v[244:247] offset:5120\n\t"
      "ds_read_b64_tr_b16 a[56:57], v105 offset:4096\n\t"
      "ds_read_b64_tr_b16 a[58:59], v105 offset:4352\n\t"
      "ds_read_b64_tr_b16 a[120:121], v105 offset:4104\n\t"
      "ds_read_b64_tr_b16 a[122:123], v105 offset:4360\n\t"
      "s_waitcnt lgkmcnt(6)\n\t"
      "ds_write_b128 v104, v[248:251] offset:6144\n\t"
      "ds_write_b128 v104, v[252:255] offset:7168\n\t"
      "ds_read_b64_tr_b16 a[60:61], v105 offset:6144\n\t"
      "ds_read_b64_tr_b16 a[62:63], v105 offset:6400\n\t"
      "ds_read_b64_tr_b16 a[124:125], v105 offset:6152\n\t"
      "ds_read_b64_tr_b16 a[126:127], v105 offset:6408\n\t"
      "s_waitcnt vmcnt(0) lgkmcnt(0)\n\t"
      "s_barrier\n\t"
      "s_mov_b64 exec, s[90:91]\n\t"
      "buffer_store_dword v107, off, s[60:63], s75 sc1\n\t"
      "s_sub_u32 s75, s75, 32\n\t"
      "buffer_store_dword v107, off, s[60:63], s75 sc1\n\t"
      "s_sub_u32 s75, s75, 2016\n\t"
      "buffer_store_dword v107, off, s[60:63], s75 sc1\n\t"
      "s_mov_b64 exec, -1\n\t"
      : "+{s34}"(w), "+{s33}"(wv), "+{s[0:1]}"(A), "=&s"(tag), "={a[0:31]}"(wo.a), "={a[32:63]}"(wo.b), "={a[64:95]}"(wo.c), "={a[96:127]}"(wo.d)
      :
      : "memory", "vcc", "scc", "v0", "v1", "v2", "v3", "v4", "v5", "v6", "v7", "v8", "v9", "v10", "v11", "v12", "v13", "v14", "v15", "v16", "v17", "v18", "v19", "v20", "v21", "v22", "v23", "v24", "v25", "v26", "v27", "v28", "v29", "v30", "v31", "v32", "v33", "v34", "v35", "v36", "v37", "v38", "v39", "v40", "v41", "v42", "v43", "v44", "v45", "v46", "v47", "v48", "v49", "v50", "v51", "v52", "v53", "v54", "v55", "v56", "v57", "v58", "v59", "v60", "v61", "v62", "v63", "v64", "v65", "v66", "v67", "v68", "v69", "v70", "v71", "v72", "v73", "v74", "v75", "v76", "v77", "v78", "v79", "v80", "v81", "v82", "v83", "v84", "v85", "v86", "v87", "v88", "v89", "v90", "v91", "v92", "v93", "v94", "v95", "v96", "v97", "v98", "v99", "v100", "v101", "v102", "v103", "v104", "v105", "v106", "v107", "v108", "v109", "v110", "v111", "v112", "v113", "v114", "v115", "v116", "v117", "v118", "v119", "v120", "v121", "v122", "v123", "v124", "v125", "v126", "v127", "v128", "v129", "v130", "v131", "v132", "v133", "v134", "v135", "v136", "v137", "v138", "v139", "v140", "v141", "v142", "v143", "v144", "v145", "v146", "v147", "v148", "v149", "v150", "v151", "v152", "v153", "v154", "v155", "v156", "v157", "v158", "v159", "v160", "v161", "v162", "v163", "v164", "v165", "v166", "v167", "v168", "v169", "v170", "v171", "v172", "v173", "v174", "v175", "v176", "v177", "v178", "v179", "v180", "v181", "v182", "v183", "v184", "v185", "v186", "v187", "v188", "v189", "v190", "v191", "v192", "v193", "v194", "v195", "v196", "v197", "v198", "v199", "v200", "v201", "v202", "v203", "v204", "v205", "v206", "v207", "v208", "v209", "v210", "v211", "v212", "v213", "v214", "v215", "v216", "v217", "v218", "v219", "v220", "v221", "v222", "v223", "v224", "v225", "v226", "v227", "v228", "v229", "v230", "v231", "v232", "v233", "v234", "v235", "v236", "v237", "v238", "v239", "v240", "v241", "v242", "v243", "v244", "v245", "v246", "v247", "v248", "v249", "v250", "v251", "v252", "v253", "v254", "v255", "a128", "a129", "a130", "a131", "a132", "a133", "a134", "a135", "a136", "a137", "a138", "a139", "a140", "a141", "a142", "a143", "a144", "a145", "a146", "a147", "a148", "a149", "a150", "a151", "a152", "a153", "a154", "a155", "a156", "a157", "a158", "a159", "a160", "a161", "a162", "a163", "a164", "a165", "a166", "a167", "a168", "a169", "a170", "a171", "a172", "a173", "a174", "a175", "a176", "a177", "a178", "a179", "a180", "a181", "a182", "a183", "a184", "a185", "a186", "a187", "a188", "a189", "a190", "a191", "a192", "a193", "a194", "a195", "a196", "a197", "a198", "a199", "a200", "a201", "a202", "a203", "a204", "a205", "a206", "a207", "a208", "a209", "a210", "a211", "a212", "a213", "a214", "a215", "a216", "a217", "a218", "a219", "a220", "a221", "a222", "a223", "a224", "a225", "a226", "a227", "a228", "a229", "a230", "a231", "a232", "a233", "a234", "a235", "a236", "a237", "a238", "a239", "a240", "a241", "a242", "a243", "a244", "a245", "a246", "a247", "a248", "a249", "a250", "a251", "a252", "a253", "a254", "a255", "s36", "s37", "s38", "s39", "s40", "s41", "s42", "s43", "s44", "s45", "s46", "s47", "s48", "s49", "s50", "s51", "s52", "s53", "s54", "s55", "s56", "s57", "s58", "s59", "s60", "s61", "s62", "s63", "s64", "s65", "s66", "s67", "s68", "s69", "s70", "s71", "s72", "s73", "s74", "s75", "s76", "s77", "s78", "s79", "s80", "s81", "s82", "s83", "s84", "s85", "s86", "s87", "s88", "s89", "s90", "s91", "s92", "s93", "s94", "s95");
  return tag;
}

__device__ __forceinline__ void worker_tasks(char* lds, int& w, int& wv, KArgs& A, unsigned tag, WoRegs& wo) {
  asm volatile("" : "+s"(w), "+s"(wv), "+s"(A));
  const int lane = fresh_lane(), l15 = lane & 15, l4 = lane >> 4;
  const int team = w & 3, j = w >> 2, g = j >> 2, c = j & 3, h = g * 4 + wv;
  const u64 wsa = (u64)A->ws, xa = (u64)A->x, oa = (u64)A->out;
  u32x16 c0 = {(unsigned)wsa, (unsigned)(wsa >> 32) & 0xffffu, WS_BYTES, 0x00020000u, (unsigned)xa, (unsigned)(xa >> 32) & 0xffffu, S_ * DM * 2, 0x00020000u,
               (unsigned)oa, (unsigned)(oa >> 32) & 0xffffu, S_ * DM * 2, 0x00020000u, O_STAMPS + (310000 + j * 4 + wv) * 8, (unsigned)(l15 - l4 * 4),
               (unsigned)(team + 4 * wv + 48), (unsigned)j};
  const int r0 = 0 ? h : j;
  u32x16 c1 = {(unsigned)team, (unsigned)(team + 4 * (0 ? c : wv)), O_QKV + (team * 32 + h) * 24576, O_KVM + (team * 32 + h) * 2048, O_VTM + (team * 32 + h) * 2048,
               O_PART + (team * 8 + g) * 32768, O_PART + r0 * 1024, O_PFLAG + team * 16384, O_KVSTEP + h * 64, O_RBUF, O_LNH, O_HBUF, (unsigned)r0,
               (unsigned)wv * 8192, tag, O_PFLAG + team * 16384 + (j * 4 + wv) * 128};
  u32x8 c4 = {(unsigned)lane, (unsigned)lane, (unsigned)lane, (unsigned)lane, O_TFLAG + (6 * 32 + h) * 64 + 32 + (l15 & 1) * 16, O_TFLAG + (15 * 32 + h) * 64 + 32, O_WM + lane * 256 + 4, (unsigned)lane};
  const int rc = 8 * (l15 + 16 * (l4 >> 1)) + 4 * (l4 & 1);
  const u32x2 w0 = *(const u32x2*)(A->w_r + rc), w1 = *(const u32x2*)(A->w_r + 256 + rc);
  u32x16 c2 = {w0[0] << 16, w0[0] & 0xffff0000u, w0[1] << 16, w0[1] & 0xffff0000u, w1[0] << 16, w1[0] & 0xffff0000u, w1[1] << 16, w1[1] & 0xffff0000u,
               (unsigned)lane * 16, (unsigned)rc * 2, (unsigned)(lane ^ ((l4 & 1) * 8)) * 16,
               (unsigned)(wv * 8192 + (l15 & 3) * 256 + (((l4 * 4 + (l15 >> 2)) ^ ((l15 & 1) * 8)) << 4)),
               (unsigned)(l4 * 128 + (l15 & 7) * 16), (unsigned)(lane & 31) * 16,
               (unsigned)(65536 + wv * 8192 + lane * 16), (unsigned)(65536 + lane * 16)};
  unsigned c3 = (unsigned)(l15 * DM + c * 128 + wv * 32 + l4 * 8) * 2;
  asm volatile(
      ".p2align 5\n\t"
      "s_nop 1\n\t"
      "v_readfirstlane_b32 s36, v0\n\t"
      "v_readfirstlane_b32 s37, v1\n\t"
      "v_readfirstlane_b32 s38, v2\n\t"
      "v_readfirstlane_b32 s39, v3\n\t"
      "v_readfirstlane_b32 s40, v4\n\t"
      "v_readfirstlane_b32 s41, v5\n\t"
      "v_readfirstlane_b32 s42, v6\n\t"
      "v_readfirstlane_b32 s43, v7\n\t"
      "v_readfirstlane_b32 s44, v8\n\t"
      "v_readfirstlane_b32 s45, v9\n\t"
      "v_readfirstlane_b32 s46, v10\n\t"
      "v_readfirstlane_b32 s47, v11\n\t"
      "v_readfirstlane_b32 s48, v16\n\t"
      "v_readfirstlane_b32 s49, v17\n\t"
      "v_readfirstlane_b32 s50, v18\n\t"
      "v_readfirstlane_b32 s51, v19\n\t"
      "v_readfirstlane_b32 s52, v20\n\t"
      "v_readfirstlane_b32 s53, v21\n\t"
      "v_readfirstlane_b32 s54, v22\n\t"
      "v_readfirstlane_b32 s55, v23\n\t"
      "v_readfirstlane_b32 s56, v24\n\t"
      "v_readfirstlane_b32 s57, v25\n\t"
      "v_readfirstlane_b32 s58, v26\n\t"
      "v_readfirstlane_b32 s59, v27\n\t"
      "v_readfirstlane_b32 s60, v28\n\t"
      "v_readfirstlane_b32 s61, v29\n\t"
      "v_readfirstlane_b32 s62, v30\n\t"
      "v_readfirstlane_b32 s63, v31\n\t"
      "v_readfirstlane_b32 s75, v12\n\t"
      "v_readfirstlane_b32 s93, v14\n\t"
      "v_readfirstlane_b32 s94, v15\n\t"
      "v_cmp_ge_i32 s[76:77], v13, 0\n\t"
      "v_cmp_ge_i32 s[78:79], v13, 1\n\t"
      "v_cmp_ge_i32 s[80:81], v13, 2\n\t"
      "v_cmp_ge_i32 s[82:83], v13, 3\n\t"
      "s_nop 4\n\t"
      "v_readfirstlane_b32 s99, v37\n\t"
      "s_add_u32 s98, s53, 12582912\n\t"
      "v_lshrrev_b32 v40, 4, v248\n\t"
      "v_and_b32 v41, 15, v40\n\t"
      "v_lshrrev_b32 v40, 1, v40\n\t"
      "v_and_b32 v40, 24, v40\n\t"
      "v_sub_u32 v41, v41, v40\n\t"
      "s_mov_b32 s69, 0x3f80\n\t"
      "s_mov_b32 s70, 0x3f800000\n\t"
      "v_mov_b32 v42, 0\n\t"
      "v_cmp_eq_i32 vcc, 0, v41\n\t"
      "v_mov_b32 v43, s69\n\t"
      "s_nop 0\n\t"
      "v_cndmask_b32 v42, v42, v43\n\t"
      "v_cmp_eq_i32 vcc, 1, v41\n\t"
      "v_mov_b32 v43, s70\n\t"
      "s_nop 0\n\t"
      "v_cndmask_b32 v42, v42, v43\n\t"
      "v_accvgpr_write_b32 a224, v42\n\t"
      "v_mov_b32 v42, 0\n\t"
      "v_cmp_eq_i32 vcc, 2, v41\n\t"
      "v_mov_b32 v43, s69\n\t"
      "s_nop 0\n\t"
      "v_cndmask_b32 v42, v42, v43\n\t"
      "v_cmp_eq_i32 vcc, 3, v41\n\t"
      "v_mov_b32 v43, s70\n\t"
      "s_nop 0\n\t"
      "v_cndmask_b32 v42, v42, v43\n\t"
      "v_accvgpr_write_b32 a225, v42\n\t"
      "v_mov_b32 v42, 0\n\t"
      "v_cmp_eq_i32 vcc, 4, v41\n\t"
      "v_mov_b32 v43, s69\n\t"
      "s_nop 0\n\t"
      "v_cndmask_b32 v42, v42, v43\n\t"
      "v_cmp_eq_i32 vcc, 5, v41\n\t"
      "v_mov_b32 v43, s70\n\t"
      "s_nop 0\n\t"
      "v_cndmask_b32 v42, v42, v43\n\t"
      "v_accvgpr_write_b32 a226, v42\n\t"
      "v_mov_b32 v42, 0\n\t"
      "v_cmp_eq_i32 vcc, 6, v41\n\t"
      "v_mov_b32 v43, s69\n\t"
      "s_nop 0\n\t"
      "v_cndmask_b32 v42, v42, v43\n\t"
      "v_cmp_eq_i32 vcc, 7, v41\n\t"
      "v_mov_b32 v43, s70\n\t"
      "s_nop 0\n\t"
      "v_cndmask_b32 v42, v42, v43\n\t"
      "v_accvgpr_write_b32 a227, v42\n\t"
      "v_mov_b32 v42, 0\n\t"
      "v_cmp_eq_i32 vcc, -16, v41\n\t"
      "v_mov_b32 v43, s69\n\t"
      "s_nop 0\n\t"
      "v_cndmask_b32 v42, v42, v43\n\t"
      "v_cmp_eq_i32 vcc, -15, v41\n\t"
      "v_mov_b32 v43, s70\n\t"
      "s_nop 0\n\t"
      "v_cndmask_b32 v42, v42, v43\n\t"
      "v_accvgpr_write_b32 a228, v42\n\t"
      "v_mov_b32 v42, 0\n\t"
      "v_cmp_eq_i32 vcc, -14, v41\n\t"
      "v_mov_b32 v43, s69\n\t"
      "s_nop 0\n\t"
      "v_cndmask_b32 v42, v42, v43\n\t"
      "v_cmp_eq_i32 vcc, -13, v41\n\t"
      "v_mov_b32 v43, s70\n\t"
      "s_nop 0\n\t"
      "v_cndmask_b32 v42, v42, v43\n\t"
      "v_accvgpr_write_b32 a229, v42\n\t"
      "v_mov_b32 v42, 0\n\t"
      "v_cmp_eq_i32 vcc, -12, v41\n\t"
      "v_mov_b32 v43, s69\n\t"
      "s_nop 0\n\t"
      "v_cndmask_b32 v42, v42, v43\n\t"
      "v_cmp_eq_i32 vcc, -11, v41\n\t"
      "v_mov_b32 v43, s70\n\t"
      "s_nop 0\n\t"
      "v_cndmask_b32 v42, v42, v43\n\t"
      "v_accvgpr_write_b32 a230, v42\n\t"
      "v_mov_b32 v42, 0\n\t"
      "v_cmp_eq_i32 vcc, -10, v41\n\t"
      "v_mov_b32 v43, s69\n\t"
      "s_nop 0\n\t"
      "v_cndmask_b32 v42, v42, v43\n\t"
      "v_cmp_eq_i32 vcc, -9, v41\n\t"
      "v_mov_b32 v43, s70\n\t"
      "s_nop 0\n\t"
      "v_cndmask_b32 v42, v42, v43\n\t"
      "v_accvgpr_write_b32 a231, v42\n\t"
      "s_mov_b32 s72, 0x40000\n\t"
      "WE0%=:\n\t"
      "buffer_load_dwordx4 v[40:43], v36, s[36:39], 0 offen sc1\n\t"
      "buffer_load_dword v48, v38, s[36:39], 0 offen sc1\n\t"
      "buffer_load_dword v49, v38, s[36:39], 0 offen offset:128 sc1\n\t"
      "s_waitcnt vmcnt(0)\n\t"
      "v_bitop3_b32 v40, v40, v41, s62 bitop3:0x7e\n\t"
      "v_bitop3_b32 v48, v48, v49, s62 bitop3:0x7e\n\t"
      "v_or_b32 v40, v40, v48\n\t"
      "v_cmp_ne_u32 vcc, 0, v40\n\t"
      "s_nop 0\n\t"
      "s_cbranch_vccz WE1%=\n\t"
      "s_sleep 8\n\t"
      "s_sub_u32 s72, s72, 1\n\t"
      "s_cmp_lg_u32 s72, 0\n\t"
      "s_cbranch_scc1 WE0%=\n\t"
      "WE1%=:\n\t"
      "buffer_load_dwordx4 a[128:131], v248, s[36:39], s50 offen  sc1\n\t"
      "buffer_load_dwordx4 a[132:135], v248, s[36:39], s50 offen offset:1024  sc1\n\t"
      "buffer_load_dwordx4 a[136:139], v248, s[36:39], s50 offen offset:2048  sc1\n\t"
      "buffer_load_dwordx4 a[140:143], v248, s[36:39], s50 offen offset:3072  sc1\n\t"
      "s_add_u32 s69, s50, 4096\n\t"
      "buffer_load_dwordx4 a[144:147], v248, s[36:39], s69 offen  sc1\n\t"
      "buffer_load_dwordx4 a[148:151], v248, s[36:39], s69 offen offset:1024  sc1\n\t"
      "buffer_load_dwordx4 a[152:155], v248, s[36:39], s69 offen offset:2048  sc1\n\t"
      "buffer_load_dwordx4 a[156:159], v248, s[36:39], s69 offen offset:3072  sc1\n\t"
      "s_add_u32 s69, s50, 8192\n\t"
      "s_add_u32 m0, s61, 0\n\t"
      "s_nop 0\n\t"
      "buffer_load_dwordx4 v250, s[36:39], s69 offen sc1 lds\n\t"
      "buffer_load_dwordx4 v250, s[36:39], s69 offen offset:1024 sc1 lds\n\t"
      "buffer_load_dwordx4 v250, s[36:39], s69 offen offset:2048 sc1 lds\n\t"
      "buffer_load_dwordx4 v250, s[36:39], s69 offen offset:3072 sc1 lds\n\t"
      "s_add_u32 s69, s50, 12288\n\t"
      "s_add_u32 m0, s61, 4096\n\t"
      "s_nop 0\n\t"
      "buffer_load_dwordx4 v250, s[36:39], s69 offen sc1 lds\n\t"
      "buffer_load_dwordx4 v250, s[36:39], s69 offen offset:1024 sc1 lds\n\t"
      "buffer_load_dwordx4 v250, s[36:39], s69 offen offset:2048 sc1 lds\n\t"
      "buffer_load_dwordx4 v250, s[36:39], s69 offen offset:3072 sc1 lds\n\t"
      "s_add_u32 s69, s50, 16384\n\t"
      "buffer_load_dwordx4 a[160:163], v248, s[36:39], s69 offen  sc1\n\t"
      "buffer_load_dwordx4 a[164:167], v248, s[36:39], s69 offen offset:1024  sc1\n\t"
      "buffer_load_dwordx4 a[168:171], v248, s[36:39], s69 offen offset:2048  sc1\n\t"
      "buffer_load_dwordx4 a[172:175], v248, s[36:39], s69 offen offset:3072  sc1\n\t"
      "s_add_u32 s69, s50, 20480\n\t"
      "buffer_load_dwordx4 a[176:179], v248, s[36:39], s69 offen  sc1\n\t"
      "buffer_load_dwordx4 a[180:183], v248, s[36:39], s69 offen offset:1024  sc1\n\t"
      "buffer_load_dwordx4 a[184:187], v248, s[36:39], s69 offen offset:2048  sc1\n\t"
      "buffer_load_dwordx4 a[188:191], v248, s[36:39], s69 offen offset:3072  sc1\n\t"
      "s_add_u32 s50, s50, 3145728\n\t"
      "s_mov_b32 s64, 0\n\t"
      "s_mov_b32 s65, 0\n\t"
      "s_mov_b32 s66, 0\n\t"
      "s_mov_b32 s96, 0\n\t"
      "s_mov_b32 s97, 0\n\t"
      "s_add_u32 s89, s49, 16\n\t"
      "s_sub_u32 s95, s94, s60\n\t"
      "s_lshl_b32 s95, s95, 10\n\t"
      "s_add_u32 s95, s95, s54\n\t"
      "s_mov_b32 s67, -1\n\t"
      "s_mov_b32 s74, 0xffff0000\n\t"
      "s_mov_b32 s84, 0x3db504f3\n\t"
      "s_mov_b32 s86, 0x3e0293ee\n\t"
      "s_mov_b32 s85, 0x3db504f3\n\t"
      "s_mov_b32 s87, 0x3e0293ee\n\t"
      "v_mov_b32 v239, 0xff61b1e6\n\t"
      "s_waitcnt vmcnt(0) lgkmcnt(0)\n\t"
      "s_branch WA%=\n\t"
      ".p2align 5\n\t"
      "WTOP%=:\n\t"
      "s_cmp_ge_u32 s48, 64\n\t"
      "s_cbranch_scc1 WFIN%=\n\t"
      "s_lshr_b32 s69, s48, 2\n\t"
      "s_cmp_eq_u32 s69, 5\n\t"
      "s_cbranch_scc1 WH2%=\n\t"
      "WT0%=:\n\t"
      "s_waitcnt vmcnt(17)\n\t"
      "s_cmp_eq_u32 s24, 9\n\t"
      "s_cbranch_scc1 WT1%=\n\t"
      "s_waitcnt vmcnt(10)\n\t"
      "s_cmp_lg_u32 s97, 0\n\t"
      "s_cbranch_scc1 WT1%=\n\t"
      "s_waitcnt vmcnt(9)\n\t"
      "s_cmp_eq_u32 s24, 8\n\t"
      "s_cbranch_scc1 WT1%=\n\t"
      "s_waitcnt vmcnt(2)\n\t"
      "WT1%=:\n\t"
      "v_min_u32 v228, v228, v229\n\t"
      "v_min3_u32 v228, v228, v230, v231\n\t"
      "v_min_u32 v232, v232, v233\n\t"
      "s_cmp_lg_u32 s65, 0\n\t"
      "s_cselect_b32 s70, s89, s49\n\t"
      "v_readfirstlane_b32 s88, v228\n\t"
      "s_memrealtime s[90:91]\n\t"
      "v_cmp_ge_u32 vcc, s70, v232\n\t"
      "s_lshr_b32 s69, s88, 16\n\t"
      "s_cmp_gt_u32 s69, s48\n\t"
      "s_cselect_b32 s64, 1, 0\n\t"
      "s_cmp_eq_u64 vcc, 0\n\t"
      "s_cselect_b32 s66, 1, 0\n\t"
      "s_cmp_eq_u32 s65, 2\n\t"
      "s_cselect_b32 s66, 0, s66\n\t"
      "s_add_u32 s70, s48, 4\n\t"
      "s_cmp_gt_u32 s69, s70\n\t"
      "s_cselect_b32 s96, 1, 0\n\t"
      "s_or_b32 s64, s64, s97\n\t"
      "s_cmp_lg_u32 s97, 0\n\t"
      "s_cbranch_scc1 WA%=\n\t"
      "s_cmp_lg_u32 s64, 0\n\t"
      "s_cbranch_scc1 WT2%=\n\t"
      "s_sub_u32 s69, s48, s69\n\t"
      "s_mul_i32 s69, s69, 170\n\t"
      "s_waitcnt lgkmcnt(0)\n\t"
      "s_sub_u32 s70, s90, s88\n\t"
      "s_add_u32 s70, s70, 200\n\t"
      "s_and_b32 s70, s70, 0xffff\n\t"
      "s_add_u32 s69, s69, 60\n\t"
      "s_cmp_ge_u32 s70, s69\n\t"
      "s_cselect_b32 s69, s48, 0\n\t"
      "s_cmp_ge_u32 s69, 40\n\t"
      "s_cselect_b32 s64, 2, 0\n\t"
      "s_cmp_eq_u32 s64, 0\n\t"
      "s_cbranch_scc1 WA%=\n\t"
      "WT2%=:\n\t"
      "buffer_load_dwordx4 v[128:131], v252, s[36:39], s51 offen sc1\n\t"
      "buffer_load_dwordx4 v[132:135], v252, s[36:39], s51 offen offset:512 sc1\n\t"
      "buffer_load_dwordx4 v[136:139], v252, s[36:39], s51 offen offset:1024 sc1\n\t"
      "buffer_load_dwordx4 v[140:143], v252, s[36:39], s51 offen offset:1536 sc1\n\t"
      "buffer_load_dwordx4 v[144:147], v253, s[36:39], s52 offen sc1\n\t"
      "buffer_load_dwordx4 v[148:151], v253, s[36:39], s52 offen offset:512 sc1\n\t"
      "buffer_load_dwordx4 v[152:155], v253, s[36:39], s52 offen offset:1024 sc1\n\t"
      "buffer_load_dwordx4 v[156:159], v253, s[36:39], s52 offen offset:1536 sc1\n\t"
      "WA%=:\n\t"
      "v_mfma_f32_16x16x32_bf16 v[0:3], a[128:131], a[160:163], 0\n\t"
      "ds_read_b64_tr_b16 a[192:193], v251\n\t"
      "ds_read_b64_tr_b16 a[194:195], v251 offset:1024\n\t"
      "v_mfma_f32_16x16x32_bf16 v[4:7], a[128:131], a[164:167], 0\n\t"
      "ds_read_b64_tr_b16 a[196:197], v251 offset:8\n\t"
      "ds_read_b64_tr_b16 a[198:199], v251 offset:1032\n\t"
      "v_mfma_f32_16x16x32_bf16 v[8:11], a[132:135], a[164:167], 0\n\t"
      "ds_read_b64_tr_b16 a[200:201], v251 offset:2048\n\t"
      "ds_read_b64_tr_b16 a[202:203], v251 offset:3072\n\t"
      "v_mfma_f32_16x16x32_bf16 v[0:3], a[136:139], a[168:171], v[0:3]\n\t"
      "ds_read_b64_tr_b16 a[204:205], v251 offset:2056\n\t"
      "ds_read_b64_tr_b16 a[206:207], v251 offset:3080\n\t"
      "v_mfma_f32_16x16x32_bf16 v[4:7], a[136:139], a[172:175], v[4:7]\n\t"
      "v_mfma_f32_16x16x32_bf16 v[8:11], a[140:143], a[172:175], v[8:11]\n\t"
      "s_waitcnt lgkmcnt(0)\n\t"
      "ds_read_b64_tr_b16 a[208:209], v251 offset:4096\n\t"
      "ds_read_b64_tr_b16 a[210:211], v251 offset:5120\n\t"
      "ds_read_b64_tr_b16 a[212:213], v251 offset:4104\n\t"
      "ds_read_b64_tr_b16 a[214:215], v251 offset:5128\n\t"
      "ds_read_b64_tr_b16 a[216:217], v251 offset:6144\n\t"
      "ds_read_b64_tr_b16 a[218:219], v251 offset:7168\n\t"
      "ds_read_b64_tr_b16 a[220:221], v251 offset:6152\n\t"
      "ds_read_b64_tr_b16 a[222:223], v251 offset:7176\n\t"
      "v_mfma_f32_16x16x32_bf16 v[0:3], a[144:147], a[176:179], v[0:3]\n\t"
      "v_mfma_f32_16x16x32_bf16 v[4:7], a[144:147], a[180:183], v[4:7]\n\t"
      "v_mfma_f32_16x16x32_bf16 v[8:11], a[148:151], a[180:183], v[8:11]\n\t"
      "v_mfma_f32_16x16x32_bf16 v[0:3], a[152:155], a[184:187], v[0:3]\n\t"
      "v_mfma_f32_16x16x32_bf16 v[4:7], a[152:155], a[188:191], v[4:7]\n\t"
      "v_mfma_f32_16x16x32_bf16 v[8:11], a[156:159], a[188:191], v[8:11]\n\t"
      "s_waitcnt lgkmcnt(0)\n\t"
      "buffer_load_dwordx4 a[128:131], v248, s[36:39], s50 offen  sc1\n\t"
      "s_nop 3\n\t"
      "v_cndmask_b32_e64 v0, v239, v0, s[76:77]\n\t"
      "s_nop 0\n\t"
      "v_cndmask_b32_e64 v8, v239, v8, s[76:77]\n\t"
      "v_cndmask_b32_e64 v1, v239, v1, s[78:79]\n\t"
      "v_cndmask_b32_e64 v9, v239, v9, s[78:79]\n\t"
      "v_cndmask_b32_e64 v2, v239, v2, s[80:81]\n\t"
      "v_cndmask_b32_e64 v10, v239, v10, s[80:81]\n\t"
      "v_cndmask_b32_e64 v3, v239, v3, s[82:83]\n\t"
      "v_cndmask_b32_e64 v11, v239, v11, s[82:83]\n\t"
      "v_max3_f32 v234, v0, v1, v2\n\t"
      "v_max3_f32 v235, v4, v5, v6\n\t"
      "v_max_f32 v234, v234, v3\n\t"
      "v_max3_f32 v235, v235, v7, v8\n\t"
      "v_max3_f32 v235, v235, v9, v10\n\t"
      "v_max_f32 v235, v235, v11\n\t"
      "s_nop 1\n\t"
      "v_permlane16_swap_b32 v234, v235\n\t"
      "v_max_f32 v234, v234, v235\n\t"
      "buffer_load_dwordx4 a[132:135], v248, s[36:39], s50 offen offset:1024  sc1\n\t"
      "v_mov_b32 v235, v234\n\t"
      "s_nop 1\n\t"
      "v_permlane32_swap_b32 v234, v235\n\t"
      "v_max_f32 v234, v234, v235\n\t"
      "v_mov_b32 v235, v234\n\t"
      "s_nop 1\n\t"
      "v_permlane16_swap_b32 v234, v235\n\t"
      "v_pk_mul_f32 v[12:13], v[234:235], s[86:87]\n\t"
      "v_pk_fma_f32 v[0:1], v[0:1], s[86:87], v[12:13] op_sel:[0,0,0] op_sel_hi:[1,1,0] neg_lo:[0,0,1] neg_hi:[0,0,1]\n\t"
      "v_pk_fma_f32 v[2:3], v[2:3], s[86:87], v[12:13] op_sel:[0,0,0] op_sel_hi:[1,1,0] neg_lo:[0,0,1] neg_hi:[0,0,1]\n\t"
      "v_pk_fma_f32 v[4:5], v[4:5], s[86:87], v[12:13] op_sel:[0,0,1] op_sel_hi:[1,1,1] neg_lo:[0,0,1] neg_hi:[0,0,1]\n\t"
      "v_pk_fma_f32 v[6:7], v[6:7], s[86:87], v[12:13] op_sel:[0,0,1] op_sel_hi:[1,1,1] neg_lo:[0,0,1] neg_hi:[0,0,1]\n\t"
      "v_pk_fma_f32 v[8:9], v[8:9], s[86:87], v[12:13] op_sel:[0,0,1] op_sel_hi:[1,1,1] neg_lo:[0,0,1] neg_hi:[0,0,1]\n\t"
      "v_pk_fma_f32 v[10:11], v[10:11], s[86:87], v[12:13] op_sel:[0,0,1] op_sel_hi:[1,1,1] neg_lo:[0,0,1] neg_hi:[0,0,1]\n\t"
      "v_exp_f32 v0, v0\n\t"
      "v_exp_f32 v1, v1\n\t"
      "buffer_load_dwordx4 a[136:139], v248, s[36:39], s50 offen offset:2048  sc1\n\t"
      "v_exp_f32 v2, v2\n\t"
      "v_exp_f32 v3, v3\n\t"
      "v_exp_f32 v4, v4\n\t"
      "v_exp_f32 v5, v5\n\t"
      "v_exp_f32 v6, v6\n\t"
      "v_exp_f32 v7, v7\n\t"
      "v_exp_f32 v8, v8\n\t"
      "v_exp_f32 v9, v9\n\t"
      "buffer_load_dwordx4 a[140:143], v248, s[36:39], s50 offen offset:3072  sc1\n\t"
      "v_exp_f32 v10, v10\n\t"
      "v_exp_f32 v11, v11\n\t"
      "v_mov_b64 v[18:19], 0\n\t"
      "v_cvt_pk_bf16_f32 v16, v0, v1\n\t"
      "v_cvt_pk_bf16_f32 v17, v2, v3\n\t"
      "v_cvt_pk_bf16_f32 v20, v4, v5\n\t"
      "v_cvt_pk_bf16_f32 v21, v6, v7\n\t"
      "v_cvt_pk_bf16_f32 v22, v8, v9\n\t"
      "v_cvt_pk_bf16_f32 v23, v10, v11\n\t"
      "v_mfma_f32_16x16x32_bf16 v[64:67], a[192:195], v[16:19], 0\n\t"
      "v_pk_add_f32 v[0:1], v[0:1], v[2:3]\n\t"
      "s_add_u32 s69, s50, 4096\n\t"
      "buffer_load_dwordx4 a[144:147], v248, s[36:39], s69 offen  sc1\n\t"
      "v_mfma_f32_16x16x32_bf16 v[96:99], a[192:195], v[20:23], 0\n\t"
      "v_pk_add_f32 v[4:5], v[4:5], v[6:7]\n\t"
      "v_mfma_f32_16x16x32_bf16 v[68:71], a[196:199], v[16:19], 0\n\t"
      "v_pk_add_f32 v[8:9], v[8:9], v[10:11]\n\t"
      "v_mfma_f32_16x16x32_bf16 v[100:103], a[196:199], v[20:23], 0\n\t"
      "v_add_f32 v236, v0, v1\n\t"
      "v_mfma_f32_16x16x32_bf16 v[72:75], a[200:203], v[16:19], 0\n\t"
      "buffer_load_dwordx4 a[148:151], v248, s[36:39], s69 offen offset:1024  sc1\n\t"
      "v_pk_add_f32 v[4:5], v[4:5], v[8:9]\n\t"
      "v_mfma_f32_16x16x32_bf16 v[104:107], a[200:203], v[20:23], 0\n\t"
      "v_add_f32 v237, v4, v5\n\t"
      "v_mfma_f32_16x16x32_bf16 v[76:79], a[204:207], v[16:19], 0\n\t"
      "v_mfma_f32_16x16x32_bf16 v[108:111], a[204:207], v[20:23], 0\n\t"
      "buffer_load_dwordx4 a[152:155], v248, s[36:39], s69 offen offset:2048  sc1\n\t"
      "v_mfma_f32_16x16x32_bf16 v[80:83], a[208:211], v[16:19], 0\n\t"
      "v_mfma_f32_16x16x32_bf16 v[112:115], a[208:211], v[20:23], 0\n\t"
      "v_mfma_f32_16x16x32_bf16 v[84:87], a[212:215], v[16:19], 0\n\t"
      "v_mfma_f32_16x16x32_bf16 v[116:119], a[212:215], v[20:23], 0\n\t"
      "buffer_load_dwordx4 a[156:159], v248, s[36:39], s69 offen offset:3072  sc1\n\t"
      "v_mfma_f32_16x16x32_bf16 v[88:91], a[216:219], v[16:19], 0\n\t"
      "v_mfma_f32_16x16x32_bf16 v[120:123], a[216:219], v[20:23], 0\n\t"
      "v_mfma_f32_16x16x32_bf16 v[92:95], a[220:223], v[16:19], 0\n\t"
      "v_mfma_f32_16x16x32_bf16 v[124:127], a[220:223], v[20:23], 0\n\t"
      "s_waitcnt vmcnt(8)\n\t"
      "s_cmp_lt_u32 s48, 4\n\t"
      "s_cbranch_scc1 WGO%=\n\t"
      "v_mov_b32 v0, s48\n\t"
      "s_mov_b64 exec, 1\n\t"
      "buffer_store_dword v0, off, s[36:39], s63 sc1\n\t"
      "s_mov_b64 exec, -1\n\t"
      "s_cmp_lt_i32 s67, 0\n\t"
      "s_cbranch_scc1 WPB1%=\n\t"
      "v_mov_b32 v8, s68\n\t"
      "s_mov_b64 exec, 1\n\t"
      "buffer_store_dword v8, off, s[36:39], s67 sc1\n\t"
      "s_mov_b64 exec, -1\n\t"
      "s_mov_b32 s67, -1\n\t"
      "WPB1%=:\n\t"
      "WGO%=:\n\t"
      "s_cmp_eq_u32 s64, 1\n\t"
      "s_cbranch_scc1 WKV%=\n\t"
      "s_cmp_lg_u32 s65, 0\n\t"
      "s_cbranch_scc1 WR2%=\n\t"
      "WGO2%=:\n\t"
      "s_cmp_lt_u32 s48, 40\n\t"
      "s_cselect_b32 s71, 1, -1\n\t"
      "s_add_u32 s71, s71, s48\n\t"
      "s_mov_b32 s72, 0x40000\n\t"
      "WLOOP%=:\n\t"
      "s_cmp_ge_u32 s49, 48\n\t"
      "s_cselect_b32 s69, s49, 99\n\t"
      "s_cmp_lt_u32 s69, s48\n\t"
      "s_cbranch_scc1 WRW%=\n\t"
      "s_cmp_eq_u32 s64, 2\n\t"
      "s_cbranch_scc1 WKC%=\n\t"
      "v_lshlrev_b32 v6, 4, v248\n\t"
      "v_mov_b64 v[4:5], 0\n\t"
      "buffer_load_dwordx4 v[0:3], off, s[36:39], s56 sc1\n\t"
      "s_cmp_ge_u32 s49, s48\n\t"
      "s_cbranch_scc1 WL1%=\n\t"
      "buffer_load_dword v4, v6, s[36:39], s55 offen sc1\n\t"
      "buffer_load_dword v5, v6, s[36:39], s55 offen offset:128 sc1\n\t"
      "WL1%=:\n\t"
      "s_waitcnt vmcnt(0)\n\t"
      "s_cmp_lt_i32 s67, 0\n\t"
      "s_cbranch_scc1 WL2%=\n\t"
      "v_mov_b32 v8, s68\n\t"
      "s_mov_b64 exec, 1\n\t"
      "buffer_store_dword v8, off, s[36:39], s67 sc1\n\t"
      "s_mov_b64 exec, -1\n\t"
      "s_mov_b32 s67, -1\n\t"
      "WL2%=:\n\t"
      "v_min_u32 v0, v0, v1\n\t"
      "v_min3_u32 v0, v0, v2, v3\n\t"
      "v_min_u32 v4, v4, v5\n\t"
      "v_readfirstlane_b32 s88, v0\n\t"
      "v_cmp_ge_u32 vcc, s49, v4\n\t"
      "s_lshr_b32 s69, s88, 16\n\t"
      "s_max_u32 s70, s71, s48\n\t"
      "s_cmp_ge_u32 s69, s70\n\t"
      "s_cbranch_scc1 WKL%=\n\t"
      "s_cmp_ge_u32 s69, s71\n\t"
      "s_cbranch_scc1 WKF%=\n\t"
      "s_cbranch_vccz WRW%=\n\t"
      "s_sleep 6\n\t"
      "s_cmp_lt_u32 s49, s48\n\t"
      "s_cbranch_scc1 WKM%=\n\t"
      "s_sub_u32 s69, s71, s69\n\t"
      "s_min_u32 s69, s69, 9\n\t"
      "WKN%=:\n\t"
      "s_sub_u32 s69, s69, 1\n\t"
      "s_cmp_eq_u32 s69, 0\n\t"
      "s_cbranch_scc1 WKM%=\n\t"
      "s_sleep 48\n\t"
      "s_branch WKN%=\n\t"
      "WKM%=:\n\t"
      "s_sub_u32 s72, s72, 1\n\t"
      "s_cmp_gt_i32 s72, 0\n\t"
      "s_cbranch_scc1 WLOOP%=\n\t"
      "s_branch WKL%=\n\t"
      ".p2align 5\n\t"
      "WFIN%=:\n\t"
      "s_mov_b32 s64, 0\n\t"
      "s_mov_b32 s72, 0x40000\n\t"
      "s_waitcnt vmcnt(2)\n\t"
      "WFIN1%=:\n\t"
      "s_cmp_lt_i32 s67, 0\n\t"
      "s_cbranch_scc1 WF1%=\n\t"
      "v_mov_b32 v8, s68\n\t"
      "s_mov_b64 exec, 1\n\t"
      "buffer_store_dword v8, off, s[36:39], s67 sc1\n\t"
      "s_mov_b64 exec, -1\n\t"
      "s_mov_b32 s67, -1\n\t"
      "WF1%=:\n\t"
      "s_cmp_lg_u32 s65, 0\n\t"
      "s_cbranch_scc1 WRC%=\n\t"
      "s_cmp_ge_u32 s49, 64\n\t"
      "s_cbranch_scc1 WEND%=\n\t"
      "s_cmp_ge_u32 s49, 48\n\t"
      "s_cbranch_scc1 WRS%=\n\t"
      "v_lshlrev_b32 v6, 4, v248\n\t"
      "WRF%=:\n\t"
      "buffer_load_dword v4, v6, s[36:39], s55 offen sc1\n\t"
      "buffer_load_dword v5, v6, s[36:39], s55 offen offset:128 sc1\n\t"
      "s_waitcnt vmcnt(0)\n\t"
      "v_min_u32 v4, v4, v5\n\t"
      "v_cmp_ge_u32 vcc, s49, v4\n\t"
      "s_nop 0\n\t"
      "s_cbranch_vccz WRW%=\n\t"
      "s_sleep 8\n\t"
      "s_sub_u32 s72, s72, 1\n\t"
      "s_cmp_gt_i32 s72, 0\n\t"
      "s_cbranch_scc1 WRF%=\n\t"
      "WRS%=:\n\t"
      "s_waitcnt vmcnt(0)\n\t"
      "WRW%=:\n\t"
      "s_lshl_b32 s70, s49, 5\n\t"
      "s_add_u32 s70, s70, s60\n\t"
      "s_lshl_b32 s70, s70, 10\n\t"
      "buffer_load_dwordx2 v[224:225], v249, s[40:43], s70 offen \n\t"
      "buffer_load_dwordx2 v[226:227], v249, s[40:43], s70 offen offset:512 \n\t"
      "WRL%=:\n\t"
      "s_lshl_b32 s69, s49, 18\n\t"
      "s_add_u32 s69, s69, s54\n\t"
      "buffer_load_dwordx4 v[160:163], v248, s[36:39], s69 offen sc1\n\t"
      "s_add_u32 s69, s69, 0x8000\n\t"
      "buffer_load_dwordx4 v[164:167], v248, s[36:39], s69 offen sc1\n\t"
      "s_add_u32 s69, s69, 0x8000\n\t"
      "buffer_load_dwordx4 v[168:171], v248, s[36:39], s69 offen sc1\n\t"
      "s_add_u32 s69, s69, 0x8000\n\t"
      "buffer_load_dwordx4 v[172:175], v248, s[36:39], s69 offen sc1\n\t"
      "s_add_u32 s69, s69, 0x8000\n\t"
      "buffer_load_dwordx4 v[176:179], v248, s[36:39], s69 offen sc1\n\t"
      "s_add_u32 s69, s69, 0x8000\n\t"
      "buffer_load_dwordx4 v[180:183], v248, s[36:39], s69 offen sc1\n\t"
      "s_add_u32 s69, s69, 0x8000\n\t"
      "buffer_load_dwordx4 v[184:187], v248, s[36:39], s69 offen sc1\n\t"
      "s_add_u32 s69, s69, 0x8000\n\t"
      "buffer_load_dwordx4 v[188:191], v248, s[36:39], s69 offen sc1\n\t"
      "s_waitcnt vmcnt(0)\n\t"
      "s_cmp_lt_i32 s67, 0\n\t"
      "s_cbranch_scc1 WF2%=\n\t"
      "v_mov_b32 v8, s68\n\t"
      "s_mov_b64 exec, 1\n\t"
      "buffer_store_dword v8, off, s[36:39], s67 sc1\n\t"
      "s_mov_b64 exec, -1\n\t"
      "s_mov_b32 s67, -1\n\t"
      "WF2%=:\n\t"
      "WRC%=:\n\t"
      "v_mfma_f32_16x16x32_bf16 v[16:19], a[224:227], v[160:163], 0\n\t"
      "v_mfma_f32_16x16x32_bf16 v[20:23], a[228:231], v[160:163], 0\n\t"
      "v_and_b32 v193, s74, v224\n\t"
      "v_lshlrev_b32 v192, 16, v224\n\t"
      "v_mfma_f32_16x16x32_bf16 v[16:19], a[224:227], v[164:167], v[16:19]\n\t"
      "v_mfma_f32_16x16x32_bf16 v[20:23], a[228:231], v[164:167], v[20:23]\n\t"
      "v_and_b32 v195, s74, v225\n\t"
      "v_lshlrev_b32 v194, 16, v225\n\t"
      "v_mfma_f32_16x16x32_bf16 v[16:19], a[224:227], v[168:171], v[16:19]\n\t"
      "v_mfma_f32_16x16x32_bf16 v[20:23], a[228:231], v[168:171], v[20:23]\n\t"
      "v_and_b32 v197, s74, v226\n\t"
      "v_lshlrev_b32 v196, 16, v226\n\t"
      "v_mfma_f32_16x16x32_bf16 v[16:19], a[224:227], v[172:175], v[16:19]\n\t"
      "v_mfma_f32_16x16x32_bf16 v[20:23], a[228:231], v[172:175], v[20:23]\n\t"
      "v_and_b32 v199, s74, v227\n\t"
      "v_lshlrev_b32 v198, 16, v227\n\t"
      "v_mfma_f32_16x16x32_bf16 v[16:19], a[224:227], v[176:179], v[16:19]\n\t"
      "v_mfma_f32_16x16x32_bf16 v[20:23], a[228:231], v[176:179], v[20:23]\n\t"
      "v_mfma_f32_16x16x32_bf16 v[16:19], a[224:227], v[180:183], v[16:19]\n\t"
      "v_mfma_f32_16x16x32_bf16 v[20:23], a[228:231], v[180:183], v[20:23]\n\t"
      "v_mfma_f32_16x16x32_bf16 v[16:19], a[224:227], v[184:187], v[16:19]\n\t"
      "v_mfma_f32_16x16x32_bf16 v[20:23], a[228:231], v[184:187], v[20:23]\n\t"
      "v_mfma_f32_16x16x32_bf16 v[16:19], a[224:227], v[188:191], v[16:19]\n\t"
      "v_mfma_f32_16x16x32_bf16 v[20:23], a[228:231], v[188:191], v[20:23]\n\t"
      "s_nop 6\n\t"
      "v_cvt_pk_bf16_f32 v32, v16, v17\n\t"
      "v_cvt_pk_bf16_f32 v34, v18, v19\n\t"
      "v_cvt_pk_bf16_f32 v36, v20, v21\n\t"
      "v_cvt_pk_bf16_f32 v38, v22, v23\n\t"
      "v_and_b32 v33, s74, v32\n\t"
      "v_lshlrev_b32 v32, 16, v32\n\t"
      "v_and_b32 v35, s74, v34\n\t"
      "v_lshlrev_b32 v34, 16, v34\n\t"
      "v_and_b32 v37, s74, v36\n\t"
      "v_lshlrev_b32 v36, 16, v36\n\t"
      "v_and_b32 v39, s74, v38\n\t"
      "v_lshlrev_b32 v38, 16, v38\n\t"
      "v_pk_add_f32 v[192:193], v[192:193], v[32:33]\n\t"
      "v_pk_add_f32 v[194:195], v[194:195], v[34:35]\n\t"
      "v_pk_add_f32 v[196:197], v[196:197], v[36:37]\n\t"
      "v_pk_add_f32 v[198:199], v[198:199], v[38:39]\n\t"
      "v_cvt_pk_bf16_f32 v200, v192, v193\n\t"
      "v_cvt_pk_bf16_f32 v201, v194, v195\n\t"
      "v_cvt_pk_bf16_f32 v202, v196, v197\n\t"
      "v_cvt_pk_bf16_f32 v203, v198, v199\n\t"
      "v_and_b32 v193, s74, v200\n\t"
      "v_lshlrev_b32 v192, 16, v200\n\t"
      "v_and_b32 v195, s74, v201\n\t"
      "v_lshlrev_b32 v194, 16, v201\n\t"
      "v_and_b32 v197, s74, v202\n\t"
      "v_lshlrev_b32 v196, 16, v202\n\t"
      "v_and_b32 v199, s74, v203\n\t"
      "v_lshlrev_b32 v198, 16, v203\n\t"
      "v_pk_add_f32 v[32:33], v[192:193], v[196:197]\n\t"
      "v_pk_add_f32 v[34:35], v[194:195], v[198:199]\n\t"
      "v_pk_add_f32 v[32:33], v[32:33], v[34:35]\n\t"
      "v_add_f32 v32, v32, v33\n\t"
      "s_nop 1\n\t"
      "v_add_f32_dpp v32, v32, v32 quad_perm:[1,0,3,2] row_mask:0xf bank_mask:0xf\n\t"
      "s_nop 1\n\t"
      "v_add_f32_dpp v32, v32, v32 quad_perm:[2,3,0,1] row_mask:0xf bank_mask:0xf\n\t"
      "s_nop 1\n\t"
      "v_add_f32_dpp v32, v32, v32 row_ror:4 row_mask:0xf bank_mask:0xf\n\t"
      "s_nop 1\n\t"
      "v_add_f32_dpp v32, v32, v32 row_ror:8 row_mask:0xf bank_mask:0xf\n\t"
      "v_mov_b32 v33, v32\n\t"
      "s_nop 1\n\t"
      "v_permlane16_swap_b32 v32, v33\n\t"
      "v_add_f32 v32, v32, v33\n\t"
      "v_mov_b32 v33, v32\n\t"
      "s_nop 1\n\t"
      "v_permlane32_swap_b32 v32, v33\n\t"
      "v_add_f32 v32, v32, v33\n\t"
      "v_cmp_u_f32 vcc, v32, v32\n\t"
      "v_mul_f32 v204, 0x3b000000, v32\n\t"
      "s_cbranch_vccnz WRN%=\n\t"
      "WRD%=:\n\t"
      "v_pk_add_f32 v[40:41], v[192:193], v[204:205] op_sel_hi:[1,0] neg_lo:[0,1] neg_hi:[0,1]\n\t"
      "v_pk_add_f32 v[42:43], v[194:195], v[204:205] op_sel_hi:[1,0] neg_lo:[0,1] neg_hi:[0,1]\n\t"
      "v_pk_add_f32 v[44:45], v[196:197], v[204:205] op_sel_hi:[1,0] neg_lo:[0,1] neg_hi:[0,1]\n\t"
      "v_pk_add_f32 v[46:47], v[198:199], v[204:205] op_sel_hi:[1,0] neg_lo:[0,1] neg_hi:[0,1]\n\t"
      "v_pk_mul_f32 v[32:33], v[40:41], v[40:41]\n\t"
      "v_pk_fma_f32 v[32:33], v[42:43], v[42:43], v[32:33]\n\t"
      "v_pk_fma_f32 v[32:33], v[44:45], v[44:45], v[32:33]\n\t"
      "v_pk_fma_f32 v[32:33], v[46:47], v[46:47], v[32:33]\n\t"
      "v_add_f32 v32, v32, v33\n\t"
      "s_nop 1\n\t"
      "v_add_f32_dpp v32, v32, v32 quad_perm:[1,0,3,2] row_mask:0xf bank_mask:0xf\n\t"
      "s_nop 1\n\t"
      "v_add_f32_dpp v32, v32, v32 quad_perm:[2,3,0,1] row_mask:0xf bank_mask:0xf\n\t"
      "s_nop 1\n\t"
      "v_add_f32_dpp v32, v32, v32 row_ror:4 row_mask:0xf bank_mask:0xf\n\t"
      "s_nop 1\n\t"
      "v_add_f32_dpp v32, v32, v32 row_ror:8 row_mask:0xf bank_mask:0xf\n\t"
      "v_mov_b32 v33, v32\n\t"
      "s_nop 1\n\t"
      "v_permlane16_swap_b32 v32, v33\n\t"
      "v_add_f32 v32, v32, v33\n\t"
      "v_mov_b32 v33, v32\n\t"
      "s_nop 1\n\t"
      "v_permlane32_swap_b32 v32, v33\n\t"
      "v_add_f32 v32, v32, v33\n\t"
      "v_mul_f32 v205, 0x3b000000, v32\n\t"
      "v_cvt_pk_bf16_f32 v204, v204, v205\n\t"
      "v_and_b32 v205, s74, v204\n\t"
      "v_lshlrev_b32 v204, 16, v204\n\t"
      "v_add_f32 v205, 0x3727c5ac, v205\n\t"
      "v_pk_add_f32 v[40:41], v[192:193], v[204:205] op_sel_hi:[1,0] neg_lo:[0,1] neg_hi:[0,1]\n\t"
      "v_pk_add_f32 v[42:43], v[194:195], v[204:205] op_sel_hi:[1,0] neg_lo:[0,1] neg_hi:[0,1]\n\t"
      "v_pk_add_f32 v[44:45], v[196:197], v[204:205] op_sel_hi:[1,0] neg_lo:[0,1] neg_hi:[0,1]\n\t"
      "v_pk_add_f32 v[46:47], v[198:199], v[204:205] op_sel_hi:[1,0] neg_lo:[0,1] neg_hi:[0,1]\n\t"
      "v_cvt_pk_bf16_f32 v205, v205, v205\n\t"
      "v_and_b32 v205, s74, v205\n\t"
      "v_sqrt_f32 v205, v205\n\t"
      "v_cvt_pk_bf16_f32 v40, v40, v41\n\t"
      "v_cvt_pk_bf16_f32 v42, v42, v43\n\t"
      "v_cvt_pk_bf16_f32 v44, v44, v45\n\t"
      "v_cvt_pk_bf16_f32 v46, v46, v47\n\t"
      "v_cvt_pk_bf16_f32 v205, v205, v205\n\t"
      "v_and_b32 v205, s74, v205\n\t"
      "v_rcp_f32 v204, v205\n\t"
      "v_and_b32 v41, s74, v40\n\t"
      "v_lshlrev_b32 v40, 16, v40\n\t"
      "v_and_b32 v43, s74, v42\n\t"
      "v_lshlrev_b32 v42, 16, v42\n\t"
      "v_and_b32 v45, s74, v44\n\t"
      "v_lshlrev_b32 v44, 16, v44\n\t"
      "v_and_b32 v47, s74, v46\n\t"
      "v_lshlrev_b32 v46, 16, v46\n\t"
      "v_pk_mul_f32 v[40:41], v[40:41], v[204:205] op_sel_hi:[1,0]\n\t"
      "v_pk_mul_f32 v[42:43], v[42:43], v[204:205] op_sel_hi:[1,0]\n\t"
      "v_pk_mul_f32 v[44:45], v[44:45], v[204:205] op_sel_hi:[1,0]\n\t"
      "v_pk_mul_f32 v[46:47], v[46:47], v[204:205] op_sel_hi:[1,0]\n\t"
      "v_cvt_pk_bf16_f32 v52, v40, v41\n\t"
      "v_cvt_pk_bf16_f32 v53, v42, v43\n\t"
      "v_cvt_pk_bf16_f32 v54, v44, v45\n\t"
      "v_cvt_pk_bf16_f32 v55, v46, v47\n\t"
      "v_and_b32 v33, s74, v52\n\t"
      "v_lshlrev_b32 v32, 16, v52\n\t"
      "v_and_b32 v35, s74, v53\n\t"
      "v_lshlrev_b32 v34, 16, v53\n\t"
      "v_and_b32 v37, s74, v54\n\t"
      "v_lshlrev_b32 v36, 16, v54\n\t"
      "v_and_b32 v39, s74, v55\n\t"
      "v_lshlrev_b32 v38, 16, v55\n\t"
      "v_pk_mul_f32 v[40:41], v[32:33], v[240:241]\n\t"
      "v_pk_fma_f32 v[40:41], v[34:35], v[242:243], v[40:41]\n\t"
      "v_pk_fma_f32 v[40:41], v[36:37], v[244:245], v[40:41]\n\t"
      "v_pk_fma_f32 v[40:41], v[38:39], v[246:247], v[40:41]\n\t"
      "v_add_f32 v40, v40, v41\n\t"
      "s_lshl_b32 s73, s49, 5\n\t"
      "s_add_u32 s73, s73, s60\n\t"
      "s_lshl_b32 s69, s73, 2\n\t"
      "s_add_u32 s69, s69, s57\n\t"
      "s_lshl_b32 s70, s73, 7\n\t"
      "s_add_u32 s70, s70, s57\n\t"
      "s_sub_u32 s70, s70, 212992\n\t"
      "s_cmp_ge_u32 s49, 56\n\t"
      "s_cselect_b32 s70, s70, s69\n\t"
      "v_add_f32_dpp v40, v40, v40 quad_perm:[1,0,3,2] row_mask:0xf bank_mask:0xf\n\t"
      "s_nop 1\n\t"
      "v_add_f32_dpp v40, v40, v40 quad_perm:[2,3,0,1] row_mask:0xf bank_mask:0xf\n\t"
      "s_nop 1\n\t"
      "v_add_f32_dpp v40, v40, v40 row_ror:4 row_mask:0xf bank_mask:0xf\n\t"
      "s_nop 1\n\t"
      "v_add_f32_dpp v40, v40, v40 row_ror:8 row_mask:0xf bank_mask:0xf\n\t"
      "v_mov_b32 v41, v40\n\t"
      "s_nop 1\n\t"
      "v_permlane16_swap_b32 v40, v41\n\t"
      "v_add_f32 v40, v40, v41\n\t"
      "v_mov_b32 v41, v40\n\t"
      "s_nop 1\n\t"
      "v_permlane32_swap_b32 v40, v41\n\t"
      "v_add_f32 v40, v40, v41\n\t"
      "v_cvt_pk_bf16_f32 v40, v40, v40\n\t"
      "v_and_b32 v40, s74, v40\n\t"
      "s_mov_b64 exec, 1\n\t"
      "buffer_store_dword v40, off, s[36:39], s70 sc1\n\t"
      "s_mov_b64 exec, -1\n\t"
      "v_pk_add_f32 v[192:193], v[192:193], v[32:33]\n\t"
      "v_pk_add_f32 v[194:195], v[194:195], v[34:35]\n\t"
      "v_pk_add_f32 v[196:197], v[196:197], v[36:37]\n\t"
      "v_pk_add_f32 v[198:199], v[198:199], v[38:39]\n\t"
      "v_readfirstlane_b32 s68, v40\n\t"
      "v_cvt_pk_bf16_f32 v56, v192, v193\n\t"
      "v_cvt_pk_bf16_f32 v57, v194, v195\n\t"
      "v_cvt_pk_bf16_f32 v58, v196, v197\n\t"
      "v_cvt_pk_bf16_f32 v59, v198, v199\n\t"
      "s_add_u32 s67, s69, 8192\n\t"
      "s_lshl_b32 s73, s73, 10\n\t"
      "s_add_u32 s69, s73, s58\n\t"
      "s_add_u32 s70, s73, s59\n\t"
      "buffer_store_dwordx2 v[56:57], v249, s[44:47], s73 offen sc1\n\t"
      "buffer_store_dwordx2 v[52:53], v249, s[36:39], s69 offen sc1\n\t"
      "buffer_store_dwordx2 v[200:201], v249, s[36:39], s70 offen sc1\n\t"
      "buffer_store_dwordx2 v[58:59], v249, s[44:47], s73 offen offset:512 sc1\n\t"
      "buffer_store_dwordx2 v[54:55], v249, s[36:39], s69 offen offset:512 sc1\n\t"
      "buffer_store_dwordx2 v[202:203], v249, s[36:39], s70 offen offset:512 sc1\n\t"
      "s_mov_b32 s49, s89\n\t"
      "s_add_u32 s89, s89, 16\n\t"
      "s_lshr_b32 s69, s89, 4\n\t"
      "s_cmp_eq_u32 s69, 3\n\t"
      "s_cselect_b32 s89, s93, s89\n\t"
      "s_cmp_ge_u32 s49, 48\n\t"
      "s_cselect_b32 s60, s94, s60\n\t"
      "s_cselect_b32 s54, s95, s54\n\t"
      "s_cmp_ge_u32 s48, 64\n\t"
      "s_cbranch_scc1 WRX%=\n\t"
      "s_cmp_lg_u32 s65, 0\n\t"
      "s_mov_b32 s65, 0\n\t"
      "s_cbranch_scc1 WGO%=\n\t"
      "s_mov_b32 s66, 0\n\t"
      "s_branch WLOOP%=\n\t"
      ".p2align 5\n\t"
      "WRX%=:\n\t"
      "s_mov_b32 s65, 0\n\t"
      "s_waitcnt vmcnt(0)\n\t"
      "s_branch WFIN1%=\n\t"
      ".p2align 5\n\t"
      "WRN%=:\n\t"
      "v_max3_u32 v40, v160, v161, v162\n\t"
      "v_max3_u32 v40, v40, v163, v164\n\t"
      "v_max3_u32 v40, v40, v165, v166\n\t"
      "v_max3_u32 v40, v40, v167, v168\n\t"
      "v_max3_u32 v40, v40, v169, v170\n\t"
      "v_max3_u32 v40, v40, v171, v172\n\t"
      "v_max3_u32 v40, v40, v173, v174\n\t"
      "v_max3_u32 v40, v40, v175, v176\n\t"
      "v_max3_u32 v40, v40, v177, v178\n\t"
      "v_max3_u32 v40, v40, v179, v180\n\t"
      "v_max3_u32 v40, v40, v181, v182\n\t"
      "v_max3_u32 v40, v40, v183, v184\n\t"
      "v_max3_u32 v40, v40, v185, v186\n\t"
      "v_max3_u32 v40, v40, v187, v188\n\t"
      "v_max3_u32 v40, v40, v189, v190\n\t"
      "v_max_u32 v40, v40, v191\n\t"
      "v_cmp_eq_u32 vcc, -1, v40\n\t"
      "s_nop 0\n\t"
      "s_cbranch_vccz WRD%=\n\t"
      "s_cmp_lg_u32 s65, 0\n\t"
      "s_cbranch_scc1 WRD%=\n\t"
      "s_sub_u32 s72, s72, 1\n\t"
      "s_cmp_gt_i32 s72, 0\n\t"
      "s_cbranch_scc1 WRL%=\n\t"
      "s_branch WRD%=\n\t"
      ".p2align 5\n\t"
      "WH2%=:\n\t"
      "s_mov_b32 s72, 0x40000\n\t"
      "WH3%=:\n\t"
      "buffer_load_dwordx2 v[0:1], off, s[36:39], s99 sc1\n\t"
      "buffer_load_dwordx2 v[2:3], off, s[36:39], s99 offset:16 sc1\n\t"
      "s_sub_u32 s72, s72, 1\n\t"
      "s_waitcnt vmcnt(0)\n\t"
      "v_bitop3_b32 v0, v0, v1, s62 bitop3:0x7e\n\t"
      "v_bitop3_b32 v2, v2, v3, s62 bitop3:0x7e\n\t"
      "v_or_b32 v0, v0, v2\n\t"
      "v_cmp_ne_u32 vcc, 0, v0\n\t"
      "s_cmp_eq_u32 s72, 0\n\t"
      "s_cbranch_scc1 WT0%=\n\t"
      "s_cbranch_vccnz WH3%=\n\t"
      "s_branch WT0%=\n\t"
      ".p2align 5\n\t"
      "WKV1%=:\n\t"
      "s_cmp_eq_u32 s66, 0\n\t"
      "s_cbranch_scc1 WB1%=\n\t"
      "WB2%=:\n\t"
      "s_mov_b32 s65, 1\n\t"
      "v_mfma_f32_16x16x32_bf16 v[0:3], v[128:131], a[160:163], 0\n\t"
      "v_mfma_f32_16x16x32_bf16 v[4:7], v[128:131], a[164:167], 0\n\t"
      "v_mfma_f32_16x16x32_bf16 v[0:3], v[132:135], a[168:171], v[0:3]\n\t"
      "v_mfma_f32_16x16x32_bf16 v[4:7], v[132:135], a[172:175], v[4:7]\n\t"
      "v_mfma_f32_16x16x32_bf16 v[0:3], v[136:139], a[176:179], v[0:3]\n\t"
      "v_mfma_f32_16x16x32_bf16 v[4:7], v[136:139], a[180:183], v[4:7]\n\t"
      "v_mfma_f32_16x16x32_bf16 v[0:3], v[140:143], a[184:187], v[0:3]\n\t"
      "v_mfma_f32_16x16x32_bf16 v[4:7], v[140:143], a[188:191], v[4:7]\n\t"
      "s_add_u32 s51, s51, 262144\n\t"
      "s_add_u32 s52, s52, 262144\n\t"
      "s_lshl_b32 s70, s49, 5\n\t"
      "s_add_u32 s70, s70, s60\n\t"
      "s_lshl_b32 s70, s70, 10\n\t"
      "buffer_load_dwordx2 v[224:225], v249, s[40:43], s70 offen \n\t"
      "s_nop 0\n\t"
      "v_max3_f32 v8, v0, v1, v2\n\t"
      "v_max3_f32 v8, v8, v3, v234\n\t"
      "v_max3_f32 v9, v4, v5, v6\n\t"
      "v_max3_f32 v9, v9, v7, v235\n\t"
      "s_nop 1\n\t"
      "v_permlane16_swap_b32 v8, v9\n\t"
      "v_max_f32 v8, v8, v9\n\t"
      "buffer_load_dwordx2 v[226:227], v249, s[40:43], s70 offen offset:512 \n\t"
      "v_mov_b32 v9, v8\n\t"
      "s_nop 1\n\t"
      "v_permlane16_swap_b32 v8, v9\n\t"
      "v_pk_mul_f32 v[8:9], v[8:9], s[86:87]\n\t"
      "v_pk_fma_f32 v[10:11], v[234:235], s[86:87], v[8:9] neg_lo:[0,0,1] neg_hi:[0,0,1]\n\t"
      "v_pk_fma_f32 v[0:1], v[0:1], s[86:87], v[8:9] op_sel:[0,0,0] op_sel_hi:[1,1,0] neg_lo:[0,0,1] neg_hi:[0,0,1]\n\t"
      "v_pk_fma_f32 v[2:3], v[2:3], s[86:87], v[8:9] op_sel:[0,0,0] op_sel_hi:[1,1,0] neg_lo:[0,0,1] neg_hi:[0,0,1]\n\t"
      "s_lshl_b32 s69, s49, 18\n\t"
      "s_add_u32 s69, s69, s54\n\t"
      "buffer_load_dwordx4 v[160:163], v248, s[36:39], s69 offen sc1\n\t"
      "v_pk_fma_f32 v[4:5], v[4:5], s[86:87], v[8:9] op_sel:[0,0,1] op_sel_hi:[1,1,1] neg_lo:[0,0,1] neg_hi:[0,0,1]\n\t"
      "v_pk_fma_f32 v[6:7], v[6:7], s[86:87], v[8:9] op_sel:[0,0,1] op_sel_hi:[1,1,1] neg_lo:[0,0,1] neg_hi:[0,0,1]\n\t"
      "v_exp_f32 v10, v10\n\t"
      "v_exp_f32 v11, v11\n\t"
      "s_add_u32 s69, s69, 0x8000\n\t"
      "buffer_load_dwordx4 v[164:167], v248, s[36:39], s69 offen sc1\n\t"
      "v_exp_f32 v0, v0\n\t"
      "v_exp_f32 v1, v1\n\t"
      "v_exp_f32 v2, v2\n\t"
      "v_exp_f32 v3, v3\n\t"
      "s_add_u32 s69, s69, 0x8000\n\t"
      "buffer_load_dwordx4 v[168:171], v248, s[36:39], s69 offen sc1\n\t"
      "v_exp_f32 v4, v4\n\t"
      "v_exp_f32 v5, v5\n\t"
      "v_exp_f32 v6, v6\n\t"
      "s_add_u32 s69, s69, 0x8000\n\t"
      "buffer_load_dwordx4 v[172:175], v248, s[36:39], s69 offen sc1\n\t"
      "v_exp_f32 v7, v7\n\t"
      "v_pk_mul_f32 v[12:13], v[236:237], v[10:11]\n\t"
      "v_pk_add_f32 v[14:15], v[0:1], v[2:3]\n\t"
      "v_pk_add_f32 v[16:17], v[4:5], v[6:7]\n\t"
      "s_add_u32 s69, s69, 0x8000\n\t"
      "buffer_load_dwordx4 v[176:179], v248, s[36:39], s69 offen sc1\n\t"
      "v_add_f32 v14, v14, v15\n\t"
      "v_add_f32 v15, v16, v17\n\t"
      "v_fma_f32 v12, v14, 0.5, v12\n\t"
      "v_fma_f32 v13, v15, 0.5, v13\n\t"
      "s_nop 1\n\t"
      "v_permlane16_swap_b32 v12, v13\n\t"
      "v_add_f32 v12, v12, v13\n\t"
      "s_add_u32 s69, s69, 0x8000\n\t"
      "buffer_load_dwordx4 v[180:183], v248, s[36:39], s69 offen sc1\n\t"
      "v_mov_b32 v13, v12\n\t"
      "s_nop 1\n\t"
      "v_permlane32_swap_b32 v12, v13\n\t"
      "v_add_f32 v12, v12, v13\n\t"
      "v_mov_b32 v13, v12\n\t"
      "s_nop 1\n\t"
      "v_permlane16_swap_b32 v12, v13\n\t"
      "v_rcp_f32 v12, v12\n\t"
      "s_add_u32 s69, s69, 0x8000\n\t"
      "buffer_load_dwordx4 v[184:187], v248, s[36:39], s69 offen sc1\n\t"
      "v_rcp_f32 v13, v13\n\t"
      "s_nop 0\n\t"
      "v_pk_mul_f32 v[10:11], v[10:11], v[12:13]\n\t"
      "v_pk_mul_f32 v[12:13], v[12:13], 0.5 op_sel_hi:[1,0]\n\t"
      "v_pk_mul_f32 v[0:1], v[0:1], v[12:13] op_sel:[0,0] op_sel_hi:[1,0]\n\t"
      "s_add_u32 s69, s69, 0x8000\n\t"
      "buffer_load_dwordx4 v[188:191], v248, s[36:39], s69 offen sc1\n\t"
      "v_pk_mul_f32 v[2:3], v[2:3], v[12:13] op_sel:[0,0] op_sel_hi:[1,0]\n\t"
      "v_pk_mul_f32 v[4:5], v[4:5], v[12:13] op_sel:[0,1] op_sel_hi:[1,1]\n\t"
      "v_pk_mul_f32 v[6:7], v[6:7], v[12:13] op_sel:[0,1] op_sel_hi:[1,1]\n\t"
      "v_cvt_pk_bf16_f32 v16, v0, v1\n\t"
      "v_cvt_pk_bf16_f32 v17, v2, v3\n\t"
      "v_cvt_pk_bf16_f32 v18, v4, v5\n\t"
      "s_add_u32 s69, s50, 8192\n\t"
      "s_add_u32 m0, s61, 0\n\t"
      "s_nop 0\n\t"
      "buffer_load_dwordx4 v250, s[36:39], s69 offen sc1 lds\n\t"
      "v_cvt_pk_bf16_f32 v19, v6, v7\n\t"
      "v_pk_mul_f32 v[64:65], v[64:65], v[10:11] op_sel:[0,0] op_sel_hi:[1,0]\n\t"
      "v_pk_mul_f32 v[66:67], v[66:67], v[10:11] op_sel:[0,0] op_sel_hi:[1,0]\n\t"
      "v_pk_mul_f32 v[68:69], v[68:69], v[10:11] op_sel:[0,0] op_sel_hi:[1,0]\n\t"
      "v_pk_mul_f32 v[70:71], v[70:71], v[10:11] op_sel:[0,0] op_sel_hi:[1,0]\n\t"
      "v_mfma_f32_16x16x16_bf16 v[64:67], v[144:145], v[16:17], v[64:67]\n\t"
      "buffer_load_dwordx4 v250, s[36:39], s69 offen offset:1024 sc1 lds\n\t"
      "v_pk_mul_f32 v[72:73], v[72:73], v[10:11] op_sel:[0,0] op_sel_hi:[1,0]\n\t"
      "v_pk_mul_f32 v[74:75], v[74:75], v[10:11] op_sel:[0,0] op_sel_hi:[1,0]\n\t"
      "v_mfma_f32_16x16x16_bf16 v[68:71], v[146:147], v[16:17], v[68:71]\n\t"
      "v_pk_mul_f32 v[76:77], v[76:77], v[10:11] op_sel:[0,0] op_sel_hi:[1,0]\n\t"
      "buffer_load_dwordx4 v250, s[36:39], s69 offen offset:2048 sc1 lds\n\t"
      "v_pk_mul_f32 v[78:79], v[78:79], v[10:11] op_sel:[0,0] op_sel_hi:[1,0]\n\t"
      "v_mfma_f32_16x16x16_bf16 v[72:75], v[148:149], v[16:17], v[72:75]\n\t"
      "v_pk_mul_f32 v[80:81], v[80:81], v[10:11] op_sel:[0,0] op_sel_hi:[1,0]\n\t"
      "v_pk_mul_f32 v[82:83], v[82:83], v[10:11] op_sel:[0,0] op_sel_hi:[1,0]\n\t"
      "v_mfma_f32_16x16x16_bf16 v[76:79], v[150:151], v[16:17], v[76:79]\n\t"
      "buffer_load_dwordx4 v250, s[36:39], s69 offen offset:3072 sc1 lds\n\t"
      "v_pk_mul_f32 v[84:85], v[84:85], v[10:11] op_sel:[0,0] op_sel_hi:[1,0]\n\t"
      "v_pk_mul_f32 v[86:87], v[86:87], v[10:11] op_sel:[0,0] op_sel_hi:[1,0]\n\t"
      "v_mfma_f32_16x16x16_bf16 v[80:83], v[152:153], v[16:17], v[80:83]\n\t"
      "v_pk_mul_f32 v[88:89], v[88:89], v[10:11] op_sel:[0,0] op_sel_hi:[1,0]\n\t"
      "s_add_u32 s69, s50, 12288\n\t"
      "s_add_u32 m0, s61, 4096\n\t"
      "s_nop 0\n\t"
      "buffer_load_dwordx4 v250, s[36:39], s69 offen sc1 lds\n\t"
      "v_pk_mul_f32 v[90:91], v[90:91], v[10:11] op_sel:[0,0] op_sel_hi:[1,0]\n\t"
      "v_mfma_f32_16x16x16_bf16 v[84:87], v[154:155], v[16:17], v[84:87]\n\t"
      "v_pk_mul_f32 v[92:93], v[92:93], v[10:11] op_sel:[0,0] op_sel_hi:[1,0]\n\t"
      "v_pk_mul_f32 v[94:95], v[94:95], v[10:11] op_sel:[0,0] op_sel_hi:[1,0]\n\t"
      "v_cvt_pk_bf16_f32 v24, v64, v65\n\t"
      "buffer_load_dwordx4 v250, s[36:39], s69 offen offset:1024 sc1 lds\n\t"
      "v_cvt_pk_bf16_f32 v25, v66, v67\n\t"
      "v_cvt_pk_bf16_f32 v26, v68, v69\n\t"
      "v_cvt_pk_bf16_f32 v27, v70, v71\n\t"
      "ds_write_b128 v254, v[24:27]\n\t"
      "v_mfma_f32_16x16x16_bf16 v[88:91], v[156:157], v[16:17], v[88:91]\n\t"
      "buffer_load_dwordx4 v250, s[36:39], s69 offen offset:2048 sc1 lds\n\t"
      "v_pk_mul_f32 v[96:97], v[96:97], v[10:11] op_sel:[0,1] op_sel_hi:[1,1]\n\t"
      "v_pk_mul_f32 v[98:99], v[98:99], v[10:11] op_sel:[0,1] op_sel_hi:[1,1]\n\t"
      "v_mfma_f32_16x16x16_bf16 v[92:95], v[158:159], v[16:17], v[92:95]\n\t"
      "v_pk_mul_f32 v[100:101], v[100:101], v[10:11] op_sel:[0,1] op_sel_hi:[1,1]\n\t"
      "v_pk_mul_f32 v[102:103], v[102:103], v[10:11] op_sel:[0,1] op_sel_hi:[1,1]\n\t"
      "buffer_load_dwordx4 v250, s[36:39], s69 offen offset:3072 sc1 lds\n\t"
      "v_cvt_pk_bf16_f32 v28, v72, v73\n\t"
      "v_cvt_pk_bf16_f32 v29, v74, v75\n\t"
      "v_cvt_pk_bf16_f32 v30, v76, v77\n\t"
      "v_cvt_pk_bf16_f32 v31, v78, v79\n\t"
      "ds_write_b128 v254, v[28:31] offset:2048\n\t"
      "v_mfma_f32_16x16x16_bf16 v[96:99], v[144:145], v[18:19], v[96:99]\n\t"
      "s_add_u32 s69, s50, 16384\n\t"
      "buffer_load_dwordx4 a[160:163], v248, s[36:39], s69 offen  sc1\n\t"
      "v_pk_mul_f32 v[104:105], v[104:105], v[10:11] op_sel:[0,1] op_sel_hi:[1,1]\n\t"
      "v_pk_mul_f32 v[106:107], v[106:107], v[10:11] op_sel:[0,1] op_sel_hi:[1,1]\n\t"
      "v_mfma_f32_16x16x16_bf16 v[100:103], v[146:147], v[18:19], v[100:103]\n\t"
      "v_pk_mul_f32 v[108:109], v[108:109], v[10:11] op_sel:[0,1] op_sel_hi:[1,1]\n\t"
      "buffer_load_dwordx4 a[164:167], v248, s[36:39], s69 offen offset:1024  sc1\n\t"
      "v_pk_mul_f32 v[110:111], v[110:111], v[10:11] op_sel:[0,1] op_sel_hi:[1,1]\n\t"
      "v_cvt_pk_bf16_f32 v24, v80, v81\n\t"
      "v_cvt_pk_bf16_f32 v25, v82, v83\n\t"
      "v_cvt_pk_bf16_f32 v26, v84, v85\n\t"
      "v_cvt_pk_bf16_f32 v27, v86, v87\n\t"
      "ds_write_b128 v254, v[24:27] offset:4096\n\t"
      "buffer_load_dwordx4 a[168:171], v248, s[36:39], s69 offen offset:2048  sc1\n\t"
      "v_mfma_f32_16x16x16_bf16 v[104:107], v[148:149], v[18:19], v[104:107]\n\t"
      "v_pk_mul_f32 v[112:113], v[112:113], v[10:11] op_sel:[0,1] op_sel_hi:[1,1]\n\t"
      "v_pk_mul_f32 v[114:115], v[114:115], v[10:11] op_sel:[0,1] op_sel_hi:[1,1]\n\t"
      "v_mfma_f32_16x16x16_bf16 v[108:111], v[150:151], v[18:19], v[108:111]\n\t"
      "buffer_load_dwordx4 a[172:175], v248, s[36:39], s69 offen offset:3072  sc1\n\t"
      "v_pk_mul_f32 v[116:117], v[116:117], v[10:11] op_sel:[0,1] op_sel_hi:[1,1]\n\t"
      "v_pk_mul_f32 v[118:119], v[118:119], v[10:11] op_sel:[0,1] op_sel_hi:[1,1]\n\t"
      "v_cvt_pk_bf16_f32 v28, v88, v89\n\t"
      "v_cvt_pk_bf16_f32 v29, v90, v91\n\t"
      "v_cvt_pk_bf16_f32 v30, v92, v93\n\t"
      "v_cvt_pk_bf16_f32 v31, v94, v95\n\t"
      "s_add_u32 s69, s50, 20480\n\t"
      "buffer_load_dwordx4 a[176:179], v248, s[36:39], s69 offen  sc1\n\t"
      "ds_write_b128 v254, v[28:31] offset:6144\n\t"
      "v_mfma_f32_16x16x16_bf16 v[112:115], v[152:153], v[18:19], v[112:115]\n\t"
      "v_pk_mul_f32 v[120:121], v[120:121], v[10:11] op_sel:[0,1] op_sel_hi:[1,1]\n\t"
      "v_pk_mul_f32 v[122:123], v[122:123], v[10:11] op_sel:[0,1] op_sel_hi:[1,1]\n\t"
      "v_mfma_f32_16x16x16_bf16 v[116:119], v[154:155], v[18:19], v[116:119]\n\t"
      "buffer_load_dwordx4 a[180:183], v248, s[36:39], s69 offen offset:1024  sc1\n\t"
      "v_pk_mul_f32 v[124:125], v[124:125], v[10:11] op_sel:[0,1] op_sel_hi:[1,1]\n\t"
      "v_pk_mul_f32 v[126:127], v[126:127], v[10:11] op_sel:[0,1] op_sel_hi:[1,1]\n\t"
      "v_cvt_pk_bf16_f32 v24, v96, v97\n\t"
      "v_cvt_pk_bf16_f32 v25, v98, v99\n\t"
      "v_cvt_pk_bf16_f32 v26, v100, v101\n\t"
      "buffer_load_dwordx4 a[184:187], v248, s[36:39], s69 offen offset:2048  sc1\n\t"
      "v_cvt_pk_bf16_f32 v27, v102, v103\n\t"
      "ds_write_b128 v254, v[24:27] offset:1024\n\t"
      "v_mfma_f32_16x16x16_bf16 v[120:123], v[156:157], v[18:19], v[120:123]\n\t"
      "v_mfma_f32_16x16x16_bf16 v[124:127], v[158:159], v[18:19], v[124:127]\n\t"
      "buffer_load_dwordx4 a[188:191], v248, s[36:39], s69 offen offset:3072  sc1\n\t"
      "v_cvt_pk_bf16_f32 v28, v104, v105\n\t"
      "v_cvt_pk_bf16_f32 v29, v106, v107\n\t"
      "v_cvt_pk_bf16_f32 v30, v108, v109\n\t"
      "v_cvt_pk_bf16_f32 v31, v110, v111\n\t"
      "ds_write_b128 v254, v[28:31] offset:3072\n\t"
      "v_cvt_pk_bf16_f32 v24, v112, v113\n\t"
      "v_cvt_pk_bf16_f32 v25, v114, v115\n\t"
      "v_cvt_pk_bf16_f32 v26, v116, v117\n\t"
      "v_cvt_pk_bf16_f32 v27, v118, v119\n\t"
      "ds_write_b128 v254, v[24:27] offset:5120\n\t"
      "v_cvt_pk_bf16_f32 v28, v120, v121\n\t"
      "v_cvt_pk_bf16_f32 v29, v122, v123\n\t"
      "v_cvt_pk_bf16_f32 v30, v124, v125\n\t"
      "v_cvt_pk_bf16_f32 v31, v126, v127\n\t"
      "ds_write_b128 v254, v[28:31] offset:7168\n\t"
      "s_waitcnt vmcnt(40) lgkmcnt(0)\n\t"
      "s_barrier\n\t"
      "s_branch WO%=\n\t"
      ".p2align 5\n\t"
      "WB1%=:\n\t"
      "v_mfma_f32_16x16x32_bf16 v[0:3], v[128:131], a[160:163], 0\n\t"
      "v_mfma_f32_16x16x32_bf16 v[4:7], v[128:131], a[164:167], 0\n\t"
      "v_mfma_f32_16x16x32_bf16 v[0:3], v[132:135], a[168:171], v[0:3]\n\t"
      "v_mfma_f32_16x16x32_bf16 v[4:7], v[132:135], a[172:175], v[4:7]\n\t"
      "v_mfma_f32_16x16x32_bf16 v[0:3], v[136:139], a[176:179], v[0:3]\n\t"
      "v_mfma_f32_16x16x32_bf16 v[4:7], v[136:139], a[180:183], v[4:7]\n\t"
      "v_mfma_f32_16x16x32_bf16 v[0:3], v[140:143], a[184:187], v[0:3]\n\t"
      "v_mfma_f32_16x16x32_bf16 v[4:7], v[140:143], a[188:191], v[4:7]\n\t"
      "s_add_u32 s51, s51, 262144\n\t"
      "s_add_u32 s52, s52, 262144\n\t"
      "s_add_u32 s69, s50, 8192\n\t"
      "s_add_u32 m0, s61, 0\n\t"
      "s_nop 0\n\t"
      "buffer_load_dwordx4 v250, s[36:39], s69 offen sc1 lds\n\t"
      "s_nop 0\n\t"
      "v_max3_f32 v8, v0, v1, v2\n\t"
      "v_max3_f32 v8, v8, v3, v234\n\t"
      "v_max3_f32 v9, v4, v5, v6\n\t"
      "v_max3_f32 v9, v9, v7, v235\n\t"
      "s_nop 1\n\t"
      "v_permlane16_swap_b32 v8, v9\n\t"
      "v_max_f32 v8, v8, v9\n\t"
      "v_mov_b32 v9, v8\n\t"
      "s_nop 1\n\t"
      "v_permlane16_swap_b32 v8, v9\n\t"
      "v_pk_mul_f32 v[8:9], v[8:9], s[86:87]\n\t"
      "v_pk_fma_f32 v[10:11], v[234:235], s[86:87], v[8:9] neg_lo:[0,0,1] neg_hi:[0,0,1]\n\t"
      "buffer_load_dwordx4 v250, s[36:39], s69 offen offset:1024 sc1 lds\n\t"
      "v_pk_fma_f32 v[0:1], v[0:1], s[86:87], v[8:9] op_sel:[0,0,0] op_sel_hi:[1,1,0] neg_lo:[0,0,1] neg_hi:[0,0,1]\n\t"
      "v_pk_fma_f32 v[2:3], v[2:3], s[86:87], v[8:9] op_sel:[0,0,0] op_sel_hi:[1,1,0] neg_lo:[0,0,1] neg_hi:[0,0,1]\n\t"
      "v_pk_fma_f32 v[4:5], v[4:5], s[86:87], v[8:9] op_sel:[0,0,1] op_sel_hi:[1,1,1] neg_lo:[0,0,1] neg_hi:[0,0,1]\n\t"
      "v_pk_fma_f32 v[6:7], v[6:7], s[86:87], v[8:9] op_sel:[0,0,1] op_sel_hi:[1,1,1] neg_lo:[0,0,1] neg_hi:[0,0,1]\n\t"
      "v_exp_f32 v10, v10\n\t"
      "v_exp_f32 v11, v11\n\t"
      "v_exp_f32 v0, v0\n\t"
      "buffer_load_dwordx4 v250, s[36:39], s69 offen offset:2048 sc1 lds\n\t"
      "v_exp_f32 v1, v1\n\t"
      "v_exp_f32 v2, v2\n\t"
      "v_exp_f32 v3, v3\n\t"
      "v_exp_f32 v4, v4\n\t"
      "v_exp_f32 v5, v5\n\t"
      "buffer_load_dwordx4 v250, s[36:39], s69 offen offset:3072 sc1 lds\n\t"
      "v_exp_f32 v6, v6\n\t"
      "v_exp_f32 v7, v7\n\t"
      "v_pk_mul_f32 v[12:13], v[236:237], v[10:11]\n\t"
      "v_pk_add_f32 v[14:15], v[0:1], v[2:3]\n\t"
      "v_pk_add_f32 v[16:17], v[4:5], v[6:7]\n\t"
      "v_add_f32 v14, v14, v15\n\t"
      "v_add_f32 v15, v16, v17\n\t"
      "v_fma_f32 v12, v14, 0.5, v12\n\t"
      "s_add_u32 s69, s50, 12288\n\t"
      "s_add_u32 m0, s61, 4096\n\t"
      "s_nop 0\n\t"
      "buffer_load_dwordx4 v250, s[36:39], s69 offen sc1 lds\n\t"
      "v_fma_f32 v13, v15, 0.5, v13\n\t"
      "s_nop 1\n\t"
      "v_permlane16_swap_b32 v12, v13\n\t"
      "v_add_f32 v12, v12, v13\n\t"
      "v_mov_b32 v13, v12\n\t"
      "s_nop 1\n\t"
      "v_permlane32_swap_b32 v12, v13\n\t"
      "v_add_f32 v12, v12, v13\n\t"
      "v_mov_b32 v13, v12\n\t"
      "s_nop 1\n\t"
      "v_permlane16_swap_b32 v12, v13\n\t"
      "v_rcp_f32 v12, v12\n\t"
      "buffer_load_dwordx4 v250, s[36:39], s69 offen offset:1024 sc1 lds\n\t"
      "v_rcp_f32 v13, v13\n\t"
      "s_nop 0\n\t"
      "v_pk_mul_f32 v[10:11], v[10:11], v[12:13]\n\t"
      "v_pk_mul_f32 v[12:13], v[12:13], 0.5 op_sel_hi:[1,0]\n\t"
      "v_pk_mul_f32 v[0:1], v[0:1], v[12:13] op_sel:[0,0] op_sel_hi:[1,0]\n\t"
      "v_pk_mul_f32 v[2:3], v[2:3], v[12:13] op_sel:[0,0] op_sel_hi:[1,0]\n\t"
      "v_pk_mul_f32 v[4:5], v[4:5], v[12:13] op_sel:[0,1] op_sel_hi:[1,1]\n\t"
      "v_pk_mul_f32 v[6:7], v[6:7], v[12:13] op_sel:[0,1] op_sel_hi:[1,1]\n\t"
      "v_cvt_pk_bf16_f32 v16, v0, v1\n\t"
      "buffer_load_dwordx4 v250, s[36:39], s69 offen offset:2048 sc1 lds\n\t"
      "v_cvt_pk_bf16_f32 v17, v2, v3\n\t"
      "v_cvt_pk_bf16_f32 v18, v4, v5\n\t"
      "v_cvt_pk_bf16_f32 v19, v6, v7\n\t"
      "v_pk_mul_f32 v[64:65], v[64:65], v[10:11] op_sel:[0,0] op_sel_hi:[1,0]\n\t"
      "v_pk_mul_f32 v[66:67], v[66:67], v[10:11] op_sel:[0,0] op_sel_hi:[1,0]\n\t"
      "v_pk_mul_f32 v[68:69], v[68:69], v[10:11] op_sel:[0,0] op_sel_hi:[1,0]\n\t"
      "v_pk_mul_f32 v[70:71], v[70:71], v[10:11] op_sel:[0,0] op_sel_hi:[1,0]\n\t"
      "v_mfma_f32_16x16x16_bf16 v[64:67], v[144:145], v[16:17], v[64:67]\n\t"
      "v_pk_mul_f32 v[72:73], v[72:73], v[10:11] op_sel:[0,0] op_sel_hi:[1,0]\n\t"
      "buffer_load_dwordx4 v250, s[36:39], s69 offen offset:3072 sc1 lds\n\t"
      "v_pk_mul_f32 v[74:75], v[74:75], v[10:11] op_sel:[0,0] op_sel_hi:[1,0]\n\t"
      "v_mfma_f32_16x16x16_bf16 v[68:71], v[146:147], v[16:17], v[68:71]\n\t"
      "v_pk_mul_f32 v[76:77], v[76:77], v[10:11] op_sel:[0,0] op_sel_hi:[1,0]\n\t"
      "v_pk_mul_f32 v[78:79], v[78:79], v[10:11] op_sel:[0,0] op_sel_hi:[1,0]\n\t"
      "v_mfma_f32_16x16x16_bf16 v[72:75], v[148:149], v[16:17], v[72:75]\n\t"
      "v_pk_mul_f32 v[80:81], v[80:81], v[10:11] op_sel:[0,0] op_sel_hi:[1,0]\n\t"
      "v_pk_mul_f32 v[82:83], v[82:83], v[10:11] op_sel:[0,0] op_sel_hi:[1,0]\n\t"
      "v_mfma_f32_16x16x16_bf16 v[76:79], v[150:151], v[16:17], v[76:79]\n\t"
      "s_add_u32 s69, s50, 16384\n\t"
      "buffer_load_dwordx4 a[160:163], v248, s[36:39], s69 offen  sc1\n\t"
      "v_pk_mul_f32 v[84:85], v[84:85], v[10:11] op_sel:[0,0] op_sel_hi:[1,0]\n\t"
      "v_pk_mul_f32 v[86:87], v[86:87], v[10:11] op_sel:[0,0] op_sel_hi:[1,0]\n\t"
      "v_mfma_f32_16x16x16_bf16 v[80:83], v[152:153], v[16:17], v[80:83]\n\t"
      "v_pk_mul_f32 v[88:89], v[88:89], v[10:11] op_sel:[0,0] op_sel_hi:[1,0]\n\t"
      "v_pk_mul_f32 v[90:91], v[90:91], v[10:11] op_sel:[0,0] op_sel_hi:[1,0]\n\t"
      "v_mfma_f32_16x16x16_bf16 v[84:87], v[154:155], v[16:17], v[84:87]\n\t"
      "v_pk_mul_f32 v[92:93], v[92:93], v[10:11] op_sel:[0,0] op_sel_hi:[1,0]\n\t"
      "buffer_load_dwordx4 a[164:167], v248, s[36:39], s69 offen offset:1024  sc1\n\t"
      "v_pk_mul_f32 v[94:95], v[94:95], v[10:11] op_sel:[0,0] op_sel_hi:[1,0]\n\t"
      "v_cvt_pk_bf16_f32 v24, v64, v65\n\t"
      "v_cvt_pk_bf16_f32 v25, v66, v67\n\t"
      "v_cvt_pk_bf16_f32 v26, v68, v69\n\t"
      "v_cvt_pk_bf16_f32 v27, v70, v71\n\t"
      "ds_write_b128 v254, v[24:27]\n\t"
      "v_mfma_f32_16x16x16_bf16 v[88:91], v[156:157], v[16:17], v[88:91]\n\t"
      "v_pk_mul_f32 v[96:97], v[96:97], v[10:11] op_sel:[0,1] op_sel_hi:[1,1]\n\t"
      "buffer_load_dwordx4 a[168:171], v248, s[36:39], s69 offen offset:2048  sc1\n\t"
      "v_pk_mul_f32 v[98:99], v[98:99], v[10:11] op_sel:[0,1] op_sel_hi:[1,1]\n\t"
      "v_mfma_f32_16x16x16_bf16 v[92:95], v[158:159], v[16:17], v[92:95]\n\t"
      "v_pk_mul_f32 v[100:101], v[100:101], v[10:11] op_sel:[0,1] op_sel_hi:[1,1]\n\t"
      "v_pk_mul_f32 v[102:103], v[102:103], v[10:11] op_sel:[0,1] op_sel_hi:[1,1]\n\t"
      "v_cvt_pk_bf16_f32 v28, v72, v73\n\t"
      "v_cvt_pk_bf16_f32 v29, v74, v75\n\t"
      "v_cvt_pk_bf16_f32 v30, v76, v77\n\t"
      "v_cvt_pk_bf16_f32 v31, v78, v79\n\t"
      "ds_write_b128 v254, v[28:31] offset:2048\n\t"
      "buffer_load_dwordx4 a[172:175], v248, s[36:39], s69 offen offset:3072  sc1\n\t"
      "v_mfma_f32_16x16x16_bf16 v[96:99], v[144:145], v[18:19], v[96:99]\n\t"
      "v_pk_mul_f32 v[104:105], v[104:105], v[10:11] op_sel:[0,1] op_sel_hi:[1,1]\n\t"
      "v_pk_mul_f32 v[106:107], v[106:107], v[10:11] op_sel:[0,1] op_sel_hi:[1,1]\n\t"
      "v_mfma_f32_16x16x16_bf16 v[100:103], v[146:147], v[18:19], v[100:103]\n\t"
      "v_pk_mul_f32 v[108:109], v[108:109], v[10:11] op_sel:[0,1] op_sel_hi:[1,1]\n\t"
      "v_pk_mul_f32 v[110:111], v[110:111], v[10:11] op_sel:[0,1] op_sel_hi:[1,1]\n\t"
      "v_cvt_pk_bf16_f32 v24, v80, v81\n\t"
      "v_cvt_pk_bf16_f32 v25, v82, v83\n\t"
      "s_add_u32 s69, s50, 20480\n\t"
      "buffer_load_dwordx4 a[176:179], v248, s[36:39], s69 offen  sc1\n\t"
      "v_cvt_pk_bf16_f32 v26, v84, v85\n\t"
      "v_cvt_pk_bf16_f32 v27, v86, v87\n\t"
      "ds_write_b128 v254, v[24:27] offset:4096\n\t"
      "v_mfma_f32_16x16x16_bf16 v[104:107], v[148:149], v[18:19], v[104:107]\n\t"
      "v_pk_mul_f32 v[112:113], v[112:113], v[10:11] op_sel:[0,1] op_sel_hi:[1,1]\n\t"
      "v_pk_mul_f32 v[114:115], v[114:115], v[10:11] op_sel:[0,1] op_sel_hi:[1,1]\n\t"
      "v_mfma_f32_16x16x16_bf16 v[108:111], v[150:151], v[18:19], v[108:111]\n\t"
      "buffer_load_dwordx4 a[180:183], v248, s[36:39], s69 offen offset:1024  sc1\n\t"
      "v_pk_mul_f32 v[116:117], v[116:117], v[10:11] op_sel:[0,1] op_sel_hi:[1,1]\n\t"
      "v_pk_mul_f32 v[118:119], v[118:119], v[10:11] op_sel:[0,1] op_sel_hi:[1,1]\n\t"
      "v_cvt_pk_bf16_f32 v28, v88, v89\n\t"
      "v_cvt_pk_bf16_f32 v29, v90, v91\n\t"
      "v_cvt_pk_bf16_f32 v30, v92, v93\n\t"
      "v_cvt_pk_bf16_f32 v31, v94, v95\n\t"
      "ds_write_b128 v254, v[28:31] offset:6144\n\t"
      "v_mfma_f32_16x16x16_bf16 v[112:115], v[152:153], v[18:19], v[112:115]\n\t"
      "v_pk_mul_f32 v[120:121], v[120:121], v[10:11] op_sel:[0,1] op_sel_hi:[1,1]\n\t"
      "buffer_load_dwordx4 a[184:187], v248, s[36:39], s69 offen offset:2048  sc1\n\t"
      "v_pk_mul_f32 v[122:123], v[122:123], v[10:11] op_sel:[0,1] op_sel_hi:[1,1]\n\t"
      "v_mfma_f32_16x16x16_bf16 v[116:119], v[154:155], v[18:19], v[116:119]\n\t"
      "v_pk_mul_f32 v[124:125], v[124:125], v[10:11] op_sel:[0,1] op_sel_hi:[1,1]\n\t"
      "v_pk_mul_f32 v[126:127], v[126:127], v[10:11] op_sel:[0,1] op_sel_hi:[1,1]\n\t"
      "v_cvt_pk_bf16_f32 v24, v96, v97\n\t"
      "v_cvt_pk_bf16_f32 v25, v98, v99\n\t"
      "v_cvt_pk_bf16_f32 v26, v100, v101\n\t"
      "v_cvt_pk_bf16_f32 v27, v102, v103\n\t"
      "ds_write_b128 v254, v[24:27] offset:1024\n\t"
      "buffer_load_dwordx4 a[188:191], v248, s[36:39], s69 offen offset:3072  sc1\n\t"
      "v_mfma_f32_16x16x16_bf16 v[120:123], v[156:157], v[18:19], v[120:123]\n\t"
      "v_mfma_f32_16x16x16_bf16 v[124:127], v[158:159], v[18:19], v[124:127]\n\t"
      "v_cvt_pk_bf16_f32 v28, v104, v105\n\t"
      "v_cvt_pk_bf16_f32 v29, v106, v107\n\t"
      "v_cvt_pk_bf16_f32 v30, v108, v109\n\t"
      "v_cvt_pk_bf16_f32 v31, v110, v111\n\t"
      "ds_write_b128 v254, v[28:31] offset:3072\n\t"
      "v_cvt_pk_bf16_f32 v24, v112, v113\n\t"
      "v_cvt_pk_bf16_f32 v25, v114, v115\n\t"
      "v_cvt_pk_bf16_f32 v26, v116, v117\n\t"
      "v_cvt_pk_bf16_f32 v27, v118, v119\n\t"
      "ds_write_b128 v254, v[24:27] offset:5120\n\t"
      "v_cvt_pk_bf16_f32 v28, v120, v121\n\t"
      "v_cvt_pk_bf16_f32 v29, v122, v123\n\t"
      "v_cvt_pk_bf16_f32 v30, v124, v125\n\t"
      "v_cvt_pk_bf16_f32 v31, v126, v127\n\t"
      "ds_write_b128 v254, v[28:31] offset:7168\n\t"
      "s_waitcnt vmcnt(40) lgkmcnt(0)\n\t"
      "s_barrier\n\t"
      "s_branch WO%=\n\t"
      ".p2align 5\n\t"
      "WOL1%=:\n\t"
      "buffer_load_dwordx4 v[128:131], v252, s[36:39], s51 offen sc1\n\t"
      "s_waitcnt lgkmcnt(11)\n\t"
      "v_mfma_f32_16x16x32_bf16 v[0:3], a[0:3], v[64:67], 0\n\t"
      "v_mfma_f32_16x16x32_bf16 v[4:7], a[64:67], v[64:67], 0\n\t"
      "ds_read_b128 v[112:115], v255 offset:12288\n\t"
      "s_waitcnt lgkmcnt(11)\n\t"
      "v_mfma_f32_16x16x32_bf16 v[8:11], a[0:3], v[68:71], 0\n\t"
      "v_mfma_f32_16x16x32_bf16 v[12:15], a[64:67], v[68:71], 0\n\t"
      "ds_read_b128 v[116:119], v255 offset:13312\n\t"
      "s_waitcnt lgkmcnt(11)\n\t"
      "v_mfma_f32_16x16x32_bf16 v[0:3], a[4:7], v[72:75], v[0:3]\n\t"
      "v_mfma_f32_16x16x32_bf16 v[4:7], a[68:71], v[72:75], v[4:7]\n\t"
      "ds_read_b128 v[120:123], v255 offset:14336\n\t"
      "s_waitcnt lgkmcnt(11)\n\t"
      "v_mfma_f32_16x16x32_bf16 v[8:11], a[4:7], v[76:79], v[8:11]\n\t"
      "v_mfma_f32_16x16x32_bf16 v[12:15], a[68:71], v[76:79], v[12:15]\n\t"
      "buffer_load_dwordx4 v[132:135], v252, s[36:39], s51 offen offset:512 sc1\n\t"
      "ds_read_b128 v[124:127], v255 offset:15360\n\t"
      "s_waitcnt lgkmcnt(11)\n\t"
      "v_mfma_f32_16x16x32_bf16 v[0:3], a[8:11], v[80:83], v[0:3]\n\t"
      "v_mfma_f32_16x16x32_bf16 v[4:7], a[72:75], v[80:83], v[4:7]\n\t"
      "ds_read_b128 v[64:67], v255 offset:16384\n\t"
      "s_waitcnt lgkmcnt(11)\n\t"
      "v_mfma_f32_16x16x32_bf16 v[8:11], a[8:11], v[84:87], v[8:11]\n\t"
      "v_mfma_f32_16x16x32_bf16 v[12:15], a[72:75], v[84:87], v[12:15]\n\t"
      "ds_read_b128 v[68:71], v255 offset:17408\n\t"
      "s_waitcnt lgkmcnt(11)\n\t"
      "v_mfma_f32_16x16x32_bf16 v[0:3], a[12:15], v[88:91], v[0:3]\n\t"
      "v_mfma_f32_16x16x32_bf16 v[4:7], a[76:79], v[88:91], v[4:7]\n\t"
      "ds_read_b128 v[72:75], v255 offset:18432\n\t"
      "s_waitcnt lgkmcnt(11)\n\t"
      "v_mfma_f32_16x16x32_bf16 v[8:11], a[12:15], v[92:95], v[8:11]\n\t"
      "v_mfma_f32_16x16x32_bf16 v[12:15], a[76:79], v[92:95], v[12:15]\n\t"
      "buffer_load_dwordx4 v[136:139], v252, s[36:39], s51 offen offset:1024 sc1\n\t"
      "ds_read_b128 v[76:79], v255 offset:19456\n\t"
      "s_waitcnt lgkmcnt(11)\n\t"
      "v_mfma_f32_16x16x32_bf16 v[0:3], a[16:19], v[96:99], v[0:3]\n\t"
      "v_mfma_f32_16x16x32_bf16 v[4:7], a[80:83], v[96:99], v[4:7]\n\t"
      "ds_read_b128 v[80:83], v255 offset:20480\n\t"
      "s_waitcnt lgkmcnt(11)\n\t"
      "v_mfma_f32_16x16x32_bf16 v[8:11], a[16:19], v[100:103], v[8:11]\n\t"
      "v_mfma_f32_16x16x32_bf16 v[12:15], a[80:83], v[100:103], v[12:15]\n\t"
      "ds_read_b128 v[84:87], v255 offset:21504\n\t"
      "s_waitcnt lgkmcnt(11)\n\t"
      "v_mfma_f32_16x16x32_bf16 v[0:3], a[20:23], v[104:107], v[0:3]\n\t"
      "v_mfma_f32_16x16x32_bf16 v[4:7], a[84:87], v[104:107], v[4:7]\n\t"
      "ds_read_b128 v[88:91], v255 offset:22528\n\t"
      "s_waitcnt lgkmcnt(11)\n\t"
      "v_mfma_f32_16x16x32_bf16 v[8:11], a[20:23], v[108:111], v[8:11]\n\t"
      "v_mfma_f32_16x16x32_bf16 v[12:15], a[84:87], v[108:111], v[12:15]\n\t"
      "buffer_load_dwordx4 v[140:143], v252, s[36:39], s51 offen offset:1536 sc1\n\t"
      "ds_read_b128 v[92:95], v255 offset:23552\n\t"
      "s_waitcnt lgkmcnt(11)\n\t"
      "v_mfma_f32_16x16x32_bf16 v[0:3], a[24:27], v[112:115], v[0:3]\n\t"
      "v_mfma_f32_16x16x32_bf16 v[4:7], a[88:91], v[112:115], v[4:7]\n\t"
      "ds_read_b128 v[96:99], v255 offset:24576\n\t"
      "s_waitcnt lgkmcnt(11)\n\t"
      "v_mfma_f32_16x16x32_bf16 v[8:11], a[24:27], v[116:119], v[8:11]\n\t"
      "v_mfma_f32_16x16x32_bf16 v[12:15], a[88:91], v[116:119], v[12:15]\n\t"
      "ds_read_b128 v[100:103], v255 offset:25600\n\t"
      "s_waitcnt lgkmcnt(11)\n\t"
      "v_mfma_f32_16x16x32_bf16 v[0:3], a[28:31], v[120:123], v[0:3]\n\t"
      "v_mfma_f32_16x16x32_bf16 v[4:7], a[92:95], v[120:123], v[4:7]\n\t"
      "ds_read_b128 v[104:107], v255 offset:26624\n\t"
      "s_waitcnt lgkmcnt(11)\n\t"
      "v_mfma_f32_16x16x32_bf16 v[8:11], a[28:31], v[124:127], v[8:11]\n\t"
      "buffer_load_dwordx4 v[144:147], v253, s[36:39], s52 offen sc1\n\t"
      "v_mfma_f32_16x16x32_bf16 v[12:15], a[92:95], v[124:127], v[12:15]\n\t"
      "ds_read_b128 v[108:111], v255 offset:27648\n\t"
      "s_waitcnt lgkmcnt(11)\n\t"
      "v_mfma_f32_16x16x32_bf16 v[0:3], a[32:35], v[64:67], v[0:3]\n\t"
      "v_mfma_f32_16x16x32_bf16 v[4:7], a[96:99], v[64:67], v[4:7]\n\t"
      "ds_read_b128 v[112:115], v255 offset:28672\n\t"
      "s_waitcnt lgkmcnt(11)\n\t"
      "v_mfma_f32_16x16x32_bf16 v[8:11], a[32:35], v[68:71], v[8:11]\n\t"
      "v_mfma_f32_16x16x32_bf16 v[12:15], a[96:99], v[68:71], v[12:15]\n\t"
      "ds_read_b128 v[116:119], v255 offset:29696\n\t"
      "s_waitcnt lgkmcnt(11)\n\t"
      "v_mfma_f32_16x16x32_bf16 v[0:3], a[36:39], v[72:75], v[0:3]\n\t"
      "v_mfma_f32_16x16x32_bf16 v[4:7], a[100:103], v[72:75], v[4:7]\n\t"
      "ds_read_b128 v[120:123], v255 offset:30720\n\t"
      "s_waitcnt lgkmcnt(11)\n\t"
      "v_mfma_f32_16x16x32_bf16 v[8:11], a[36:39], v[76:79], v[8:11]\n\t"
      "buffer_load_dwordx4 v[148:151], v253, s[36:39], s52 offen offset:512 sc1\n\t"
      "v_mfma_f32_16x16x32_bf16 v[12:15], a[100:103], v[76:79], v[12:15]\n\t"
      "ds_read_b128 v[124:127], v255 offset:31744\n\t"
      "s_waitcnt lgkmcnt(11)\n\t"
      "v_mfma_f32_16x16x32_bf16 v[0:3], a[40:43], v[80:83], v[0:3]\n\t"
      "v_mfma_f32_16x16x32_bf16 v[4:7], a[104:107], v[80:83], v[4:7]\n\t"
      "s_waitcnt lgkmcnt(10)\n\t"
      "v_mfma_f32_16x16x32_bf16 v[8:11], a[40:43], v[84:87], v[8:11]\n\t"
      "v_mfma_f32_16x16x32_bf16 v[12:15], a[104:107], v[84:87], v[12:15]\n\t"
      "s_waitcnt lgkmcnt(9)\n\t"
      "v_mfma_f32_16x16x32_bf16 v[0:3], a[44:47], v[88:91], v[0:3]\n\t"
      "v_mfma_f32_16x16x32_bf16 v[4:7], a[108:111], v[88:91], v[4:7]\n\t"
      "s_waitcnt lgkmcnt(8)\n\t"
      "v_mfma_f32_16x16x32_bf16 v[8:11], a[44:47], v[92:95], v[8:11]\n\t"
      "buffer_load_dwordx4 v[152:155], v253, s[36:39], s52 offen offset:1024 sc1\n\t"
      "v_mfma_f32_16x16x32_bf16 v[12:15], a[108:111], v[92:95], v[12:15]\n\t"
      "s_waitcnt lgkmcnt(7)\n\t"
      "v_mfma_f32_16x16x32_bf16 v[0:3], a[48:51], v[96:99], v[0:3]\n\t"
      "v_mfma_f32_16x16x32_bf16 v[4:7], a[112:115], v[96:99], v[4:7]\n\t"
      "s_waitcnt lgkmcnt(6)\n\t"
      "v_mfma_f32_16x16x32_bf16 v[8:11], a[48:51], v[100:103], v[8:11]\n\t"
      "v_mfma_f32_16x16x32_bf16 v[12:15], a[112:115], v[100:103], v[12:15]\n\t"
      "s_waitcnt lgkmcnt(5)\n\t"
      "v_mfma_f32_16x16x32_bf16 v[0:3], a[52:55], v[104:107], v[0:3]\n\t"
      "v_mfma_f32_16x16x32_bf16 v[4:7], a[116:119], v[104:107], v[4:7]\n\t"
      "s_waitcnt lgkmcnt(4)\n\t"
      "v_mfma_f32_16x16x32_bf16 v[8:11], a[52:55], v[108:111], v[8:11]\n\t"
      "v_mfma_f32_16x16x32_bf16 v[12:15], a[116:119], v[108:111], v[12:15]\n\t"
      "buffer_load_dwordx4 v[156:159], v253, s[36:39], s52 offen offset:1536 sc1\n\t"
      "s_waitcnt lgkmcnt(3)\n\t"
      "v_mfma_f32_16x16x32_bf16 v[0:3], a[56:59], v[112:115], v[0:3]\n\t"
      "v_mfma_f32_16x16x32_bf16 v[4:7], a[120:123], v[112:115], v[4:7]\n\t"
      "s_waitcnt lgkmcnt(2)\n\t"
      "v_mfma_f32_16x16x32_bf16 v[8:11], a[56:59], v[116:119], v[8:11]\n\t"
      "v_mfma_f32_16x16x32_bf16 v[12:15], a[120:123], v[116:119], v[12:15]\n\t"
      "s_waitcnt lgkmcnt(1)\n\t"
      "v_mfma_f32_16x16x32_bf16 v[0:3], a[60:63], v[120:123], v[0:3]\n\t"
      "v_mfma_f32_16x16x32_bf16 v[4:7], a[124:127], v[120:123], v[4:7]\n\t"
      "s_waitcnt lgkmcnt(0)\n\t"
      "v_mfma_f32_16x16x32_bf16 v[8:11], a[60:63], v[124:127], v[8:11]\n\t"
      "v_mfma_f32_16x16x32_bf16 v[12:15], a[124:127], v[124:127], v[12:15]\n\t"
      "v_xor_b32 v254, 0x8000, v254\n\t"
      "v_xor_b32 v255, 0x8000, v255\n\t"
      "s_add_u32 s69, s53, 0x4000\n\t"
      "s_add_u32 s48, s48, 4\n\t"
      "s_waitcnt vmcnt(40)\n\t"
      "v_cvt_pk_bf16_f32 v0, v0, v1\n\t"
      "v_cvt_pk_bf16_f32 v1, v2, v3\n\t"
      "v_cvt_pk_bf16_f32 v2, v4, v5\n\t"
      "v_cvt_pk_bf16_f32 v3, v6, v7\n\t"
      "v_min_u32 v0, -2, v0\n\t"
      "v_min_u32 v1, -2, v1\n\t"
      "v_min_u32 v2, -2, v2\n\t"
      "v_min_u32 v3, -2, v3\n\t"
      "buffer_store_dwordx4 v[0:3], v238, s[36:39], s53 offen sc1\n\t"
      "v_cvt_pk_bf16_f32 v8, v8, v9\n\t"
      "v_cvt_pk_bf16_f32 v9, v10, v11\n\t"
      "v_cvt_pk_bf16_f32 v10, v12, v13\n\t"
      "v_cvt_pk_bf16_f32 v11, v14, v15\n\t"
      "v_min_u32 v8, -2, v8\n\t"
      "v_min_u32 v9, -2, v9\n\t"
      "v_min_u32 v10, -2, v10\n\t"
      "v_min_u32 v11, -2, v11\n\t"
      "buffer_store_dwordx4 v[8:11], v238, s[36:39], s69 offen sc1\n\t"
      "s_add_u32 s53, s53, 1048576\n\t"
      "s_nop 1\n\t"
      "s_branch WTOP%=\n\t"
      ".p2align 5\n\t"
      "WR2%=:\n\t"
      "v_cvt_pk_bf16_f32 v204, v204, v205\n\t"
      "v_and_b32 v205, s74, v204\n\t"
      "v_lshlrev_b32 v204, 16, v204\n\t"
      "v_add_f32 v205, 0x3727c5ac, v205\n\t"
      "v_pk_add_f32 v[40:41], v[192:193], v[204:205] op_sel_hi:[1,0] neg_lo:[0,1] neg_hi:[0,1]\n\t"
      "v_pk_add_f32 v[42:43], v[194:195], v[204:205] op_sel_hi:[1,0] neg_lo:[0,1] neg_hi:[0,1]\n\t"
      "v_pk_add_f32 v[44:45], v[196:197], v[204:205] op_sel_hi:[1,0] neg_lo:[0,1] neg_hi:[0,1]\n\t"
      "v_pk_add_f32 v[46:47], v[198:199], v[204:205] op_sel_hi:[1,0] neg_lo:[0,1] neg_hi:[0,1]\n\t"
      "v_cvt_pk_bf16_f32 v205, v205, v205\n\t"
      "v_and_b32 v205, s74, v205\n\t"
      "v_sqrt_f32 v205, v205\n\t"
      "v_cvt_pk_bf16_f32 v40, v40, v41\n\t"
      "v_cvt_pk_bf16_f32 v42, v42, v43\n\t"
      "v_cvt_pk_bf16_f32 v44, v44, v45\n\t"
      "v_cvt_pk_bf16_f32 v46, v46, v47\n\t"
      "v_cvt_pk_bf16_f32 v205, v205, v205\n\t"
      "v_and_b32 v205, s74, v205\n\t"
      "v_rcp_f32 v204, v205\n\t"
      "v_and_b32 v41, s74, v40\n\t"
      "v_lshlrev_b32 v40, 16, v40\n\t"
      "v_and_b32 v43, s74, v42\n\t"
      "v_lshlrev_b32 v42, 16, v42\n\t"
      "v_and_b32 v45, s74, v44\n\t"
      "v_lshlrev_b32 v44, 16, v44\n\t"
      "v_and_b32 v47, s74, v46\n\t"
      "v_lshlrev_b32 v46, 16, v46\n\t"
      "v_pk_mul_f32 v[40:41], v[40:41], v[204:205] op_sel_hi:[1,0]\n\t"
      "v_pk_mul_f32 v[42:43], v[42:43], v[204:205] op_sel_hi:[1,0]\n\t"
      "v_pk_mul_f32 v[44:45], v[44:45], v[204:205] op_sel_hi:[1,0]\n\t"
      "v_pk_mul_f32 v[46:47], v[46:47], v[204:205] op_sel_hi:[1,0]\n\t"
      "v_cvt_pk_bf16_f32 v52, v40, v41\n\t"
      "v_cvt_pk_bf16_f32 v53, v42, v43\n\t"
      "v_cvt_pk_bf16_f32 v54, v44, v45\n\t"
      "v_cvt_pk_bf16_f32 v55, v46, v47\n\t"
      "v_and_b32 v33, s74, v52\n\t"
      "v_lshlrev_b32 v32, 16, v52\n\t"
      "v_and_b32 v35, s74, v53\n\t"
      "v_lshlrev_b32 v34, 16, v53\n\t"
      "v_and_b32 v37, s74, v54\n\t"
      "v_lshlrev_b32 v36, 16, v54\n\t"
      "v_and_b32 v39, s74, v55\n\t"
      "v_lshlrev_b32 v38, 16, v55\n\t"
      "v_pk_mul_f32 v[40:41], v[32:33], v[240:241]\n\t"
      "v_pk_fma_f32 v[40:41], v[34:35], v[242:243], v[40:41]\n\t"
      "v_pk_fma_f32 v[40:41], v[36:37], v[244:245], v[40:41]\n\t"
      "v_pk_fma_f32 v[40:41], v[38:39], v[246:247], v[40:41]\n\t"
      "v_add_f32 v40, v40, v41\n\t"
      "s_lshl_b32 s73, s49, 5\n\t"
      "s_add_u32 s73, s73, s60\n\t"
      "s_lshl_b32 s69, s73, 2\n\t"
      "s_add_u32 s69, s69, s57\n\t"
      "s_lshl_b32 s70, s73, 7\n\t"
      "s_add_u32 s70, s70, s57\n\t"
      "s_sub_u32 s70, s70, 212992\n\t"
      "s_cmp_ge_u32 s49, 56\n\t"
      "s_cselect_b32 s70, s70, s69\n\t"
      "v_add_f32_dpp v40, v40, v40 quad_perm:[1,0,3,2] row_mask:0xf bank_mask:0xf\n\t"
      "s_nop 1\n\t"
      "v_add_f32_dpp v40, v40, v40 quad_perm:[2,3,0,1] row_mask:0xf bank_mask:0xf\n\t"
      "s_nop 1\n\t"
      "v_add_f32_dpp v40, v40, v40 row_ror:4 row_mask:0xf bank_mask:0xf\n\t"
      "s_nop 1\n\t"
      "v_add_f32_dpp v40, v40, v40 row_ror:8 row_mask:0xf bank_mask:0xf\n\t"
      "v_mov_b32 v41, v40\n\t"
      "s_nop 1\n\t"
      "v_permlane16_swap_b32 v40, v41\n\t"
      "v_add_f32 v40, v40, v41\n\t"
      "v_mov_b32 v41, v40\n\t"
      "s_nop 1\n\t"
      "v_permlane32_swap_b32 v40, v41\n\t"
      "v_add_f32 v40, v40, v41\n\t"
      "v_cvt_pk_bf16_f32 v40, v40, v40\n\t"
      "v_and_b32 v40, s74, v40\n\t"
      "s_mov_b64 exec, 1\n\t"
      "buffer_store_dword v40, off, s[36:39], s70 sc1\n\t"
      "s_mov_b64 exec, -1\n\t"
      "v_pk_add_f32 v[192:193], v[192:193], v[32:33]\n\t"
      "v_pk_add_f32 v[194:195], v[194:195], v[34:35]\n\t"
      "v_pk_add_f32 v[196:197], v[196:197], v[36:37]\n\t"
      "v_pk_add_f32 v[198:199], v[198:199], v[38:39]\n\t"
      "v_readfirstlane_b32 s68, v40\n\t"
      "v_cvt_pk_bf16_f32 v56, v192, v193\n\t"
      "v_cvt_pk_bf16_f32 v57, v194, v195\n\t"
      "v_cvt_pk_bf16_f32 v58, v196, v197\n\t"
      "v_cvt_pk_bf16_f32 v59, v198, v199\n\t"
      "s_add_u32 s67, s69, 8192\n\t"
      "s_lshl_b32 s73, s73, 10\n\t"
      "s_add_u32 s69, s73, s58\n\t"
      "s_add_u32 s70, s73, s59\n\t"
      "buffer_store_dwordx2 v[56:57], v249, s[44:47], s73 offen sc1\n\t"
      "buffer_store_dwordx2 v[52:53], v249, s[36:39], s69 offen sc1\n\t"
      "buffer_store_dwordx2 v[200:201], v249, s[36:39], s70 offen sc1\n\t"
      "buffer_store_dwordx2 v[58:59], v249, s[44:47], s73 offen offset:512 sc1\n\t"
      "buffer_store_dwordx2 v[54:55], v249, s[36:39], s69 offen offset:512 sc1\n\t"
      "buffer_store_dwordx2 v[202:203], v249, s[36:39], s70 offen offset:512 sc1\n\t"
      "s_mov_b32 s49, s89\n\t"
      "s_add_u32 s89, s89, 16\n\t"
      "s_lshr_b32 s69, s89, 4\n\t"
      "s_cmp_eq_u32 s69, 3\n\t"
      "s_cselect_b32 s89, s93, s89\n\t"
      "s_cmp_ge_u32 s49, 48\n\t"
      "s_cselect_b32 s60, s94, s60\n\t"
      "s_cselect_b32 s54, s95, s54\n\t"
      "s_mov_b32 s65, 0\n\t"
      "s_branch WGO2%=\n\t"
      ".p2align 5\n\t"
      "WOM1%=:\n\t"
      "s_mov_b32 s65, 2\n\t"
      "s_cmp_lg_u32 s96, 0\n\t"
      "s_cbranch_scc1 WOL3%=\n\t"
      "WOL2%=:\n\t"
      "s_waitcnt lgkmcnt(11)\n\t"
      "v_mfma_f32_16x16x32_bf16 v[0:3], a[0:3], v[64:67], 0\n\t"
      "v_mfma_f32_16x16x32_bf16 v[16:19], a[224:227], v[160:163], 0\n\t"
      "v_mfma_f32_16x16x32_bf16 v[4:7], a[64:67], v[64:67], 0\n\t"
      "v_mfma_f32_16x16x32_bf16 v[20:23], a[228:231], v[160:163], 0\n\t"
      "v_and_b32 v193, s74, v224\n\t"
      "v_lshlrev_b32 v192, 16, v224\n\t"
      "ds_read_b128 v[112:115], v255 offset:12288\n\t"
      "s_waitcnt lgkmcnt(11)\n\t"
      "v_mfma_f32_16x16x32_bf16 v[8:11], a[0:3], v[68:71], 0\n\t"
      "v_mfma_f32_16x16x32_bf16 v[16:19], a[224:227], v[164:167], v[16:19]\n\t"
      "v_mfma_f32_16x16x32_bf16 v[12:15], a[64:67], v[68:71], 0\n\t"
      "v_mfma_f32_16x16x32_bf16 v[20:23], a[228:231], v[164:167], v[20:23]\n\t"
      "v_and_b32 v195, s74, v225\n\t"
      "v_lshlrev_b32 v194, 16, v225\n\t"
      "ds_read_b128 v[116:119], v255 offset:13312\n\t"
      "s_waitcnt lgkmcnt(11)\n\t"
      "v_mfma_f32_16x16x32_bf16 v[0:3], a[4:7], v[72:75], v[0:3]\n\t"
      "v_mfma_f32_16x16x32_bf16 v[16:19], a[224:227], v[168:171], v[16:19]\n\t"
      "v_mfma_f32_16x16x32_bf16 v[4:7], a[68:71], v[72:75], v[4:7]\n\t"
      "v_mfma_f32_16x16x32_bf16 v[20:23], a[228:231], v[168:171], v[20:23]\n\t"
      "v_and_b32 v197, s74, v226\n\t"
      "v_lshlrev_b32 v196, 16, v226\n\t"
      "ds_read_b128 v[120:123], v255 offset:14336\n\t"
      "s_waitcnt lgkmcnt(11)\n\t"
      "v_mfma_f32_16x16x32_bf16 v[8:11], a[4:7], v[76:79], v[8:11]\n\t"
      "v_mfma_f32_16x16x32_bf16 v[16:19], a[224:227], v[172:175], v[16:19]\n\t"
      "v_mfma_f32_16x16x32_bf16 v[12:15], a[68:71], v[76:79], v[12:15]\n\t"
      "v_mfma_f32_16x16x32_bf16 v[20:23], a[228:231], v[172:175], v[20:23]\n\t"
      "v_and_b32 v199, s74, v227\n\t"
      "v_lshlrev_b32 v198, 16, v227\n\t"
      "ds_read_b128 v[124:127], v255 offset:15360\n\t"
      "s_waitcnt lgkmcnt(11)\n\t"
      "v_mfma_f32_16x16x32_bf16 v[0:3], a[8:11], v[80:83], v[0:3]\n\t"
      "v_mfma_f32_16x16x32_bf16 v[16:19], a[224:227], v[176:179], v[16:19]\n\t"
      "v_mfma_f32_16x16x32_bf16 v[4:7], a[72:75], v[80:83], v[4:7]\n\t"
      "v_mfma_f32_16x16x32_bf16 v[20:23], a[228:231], v[176:179], v[20:23]\n\t"
      "ds_read_b128 v[64:67], v255 offset:16384\n\t"
      "s_waitcnt lgkmcnt(11)\n\t"
      "v_mfma_f32_16x16x32_bf16 v[8:11], a[8:11], v[84:87], v[8:11]\n\t"
      "v_mfma_f32_16x16x32_bf16 v[16:19], a[224:227], v[180:183], v[16:19]\n\t"
      "v_mfma_f32_16x16x32_bf16 v[12:15], a[72:75], v[84:87], v[12:15]\n\t"
      "v_mfma_f32_16x16x32_bf16 v[20:23], a[228:231], v[180:183], v[20:23]\n\t"
      "ds_read_b128 v[68:71], v255 offset:17408\n\t"
      "s_waitcnt lgkmcnt(11)\n\t"
      "v_mfma_f32_16x16x32_bf16 v[0:3], a[12:15], v[88:91], v[0:3]\n\t"
      "v_mfma_f32_16x16x32_bf16 v[16:19], a[224:227], v[184:187], v[16:19]\n\t"
      "v_mfma_f32_16x16x32_bf16 v[4:7], a[76:79], v[88:91], v[4:7]\n\t"
      "v_mfma_f32_16x16x32_bf16 v[20:23], a[228:231], v[184:187], v[20:23]\n\t"
      "ds_read_b128 v[72:75], v255 offset:18432\n\t"
      "s_waitcnt lgkmcnt(11)\n\t"
      "v_mfma_f32_16x16x32_bf16 v[8:11], a[12:15], v[92:95], v[8:11]\n\t"
      "v_mfma_f32_16x16x32_bf16 v[16:19], a[224:227], v[188:191], v[16:19]\n\t"
      "v_mfma_f32_16x16x32_bf16 v[12:15], a[76:79], v[92:95], v[12:15]\n\t"
      "v_mfma_f32_16x16x32_bf16 v[20:23], a[228:231], v[188:191], v[20:23]\n\t"
      "ds_read_b128 v[76:79], v255 offset:19456\n\t"
      "s_waitcnt lgkmcnt(11)\n\t"
      "v_mfma_f32_16x16x32_bf16 v[0:3], a[16:19], v[96:99], v[0:3]\n\t"
      "v_cvt_pk_bf16_f32 v32, v16, v17\n\t"
      "v_cvt_pk_bf16_f32 v34, v18, v19\n\t"
      "v_mfma_f32_16x16x32_bf16 v[4:7], a[80:83], v[96:99], v[4:7]\n\t"
      "ds_read_b128 v[80:83], v255 offset:20480\n\t"
      "s_waitcnt lgkmcnt(11)\n\t"
      "v_mfma_f32_16x16x32_bf16 v[8:11], a[16:19], v[100:103], v[8:11]\n\t"
      "v_cvt_pk_bf16_f32 v36, v20, v21\n\t"
      "v_cvt_pk_bf16_f32 v38, v22, v23\n\t"
      "v_mfma_f32_16x16x32_bf16 v[12:15], a[80:83], v[100:103], v[12:15]\n\t"
      "v_and_b32 v33, s74, v32\n\t"
      "v_lshlrev_b32 v32, 16, v32\n\t"
      "ds_read_b128 v[84:87], v255 offset:21504\n\t"
      "s_waitcnt lgkmcnt(11)\n\t"
      "v_mfma_f32_16x16x32_bf16 v[0:3], a[20:23], v[104:107], v[0:3]\n\t"
      "v_and_b32 v35, s74, v34\n\t"
      "v_lshlrev_b32 v34, 16, v34\n\t"
      "v_mfma_f32_16x16x32_bf16 v[4:7], a[84:87], v[104:107], v[4:7]\n\t"
      "v_and_b32 v37, s74, v36\n\t"
      "v_lshlrev_b32 v36, 16, v36\n\t"
      "ds_read_b128 v[88:91], v255 offset:22528\n\t"
      "s_waitcnt lgkmcnt(11)\n\t"
      "v_mfma_f32_16x16x32_bf16 v[8:11], a[20:23], v[108:111], v[8:11]\n\t"
      "v_and_b32 v39, s74, v38\n\t"
      "v_lshlrev_b32 v38, 16, v38\n\t"
      "v_mfma_f32_16x16x32_bf16 v[12:15], a[84:87], v[108:111], v[12:15]\n\t"
      "v_add_f32 v192, v192, v32\n\t"
      "v_add_f32 v193, v193, v33\n\t"
      "ds_read_b128 v[92:95], v255 offset:23552\n\t"
      "s_waitcnt lgkmcnt(11)\n\t"
      "v_mfma_f32_16x16x32_bf16 v[0:3], a[24:27], v[112:115], v[0:3]\n\t"
      "v_add_f32 v194, v194, v34\n\t"
      "v_add_f32 v195, v195, v35\n\t"
      "v_mfma_f32_16x16x32_bf16 v[4:7], a[88:91], v[112:115], v[4:7]\n\t"
      "v_add_f32 v196, v196, v36\n\t"
      "v_add_f32 v197, v197, v37\n\t"
      "ds_read_b128 v[96:99], v255 offset:24576\n\t"
      "s_waitcnt lgkmcnt(11)\n\t"
      "v_mfma_f32_16x16x32_bf16 v[8:11], a[24:27], v[116:119], v[8:11]\n\t"
      "v_add_f32 v198, v198, v38\n\t"
      "v_add_f32 v199, v199, v39\n\t"
      "v_mfma_f32_16x16x32_bf16 v[12:15], a[88:91], v[116:119], v[12:15]\n\t"
      "v_cvt_pk_bf16_f32 v200, v192, v193\n\t"
      "v_cvt_pk_bf16_f32 v201, v194, v195\n\t"
      "ds_read_b128 v[100:103], v255 offset:25600\n\t"
      "s_waitcnt lgkmcnt(11)\n\t"
      "v_mfma_f32_16x16x32_bf16 v[0:3], a[28:31], v[120:123], v[0:3]\n\t"
      "v_cvt_pk_bf16_f32 v202, v196, v197\n\t"
      "v_cvt_pk_bf16_f32 v203, v198, v199\n\t"
      "v_mfma_f32_16x16x32_bf16 v[4:7], a[92:95], v[120:123], v[4:7]\n\t"
      "v_and_b32 v193, s74, v200\n\t"
      "v_lshlrev_b32 v192, 16, v200\n\t"
      "ds_read_b128 v[104:107], v255 offset:26624\n\t"
      "s_waitcnt lgkmcnt(11)\n\t"
      "v_mfma_f32_16x16x32_bf16 v[8:11], a[28:31], v[124:127], v[8:11]\n\t"
      "v_and_b32 v195, s74, v201\n\t"
      "v_lshlrev_b32 v194, 16, v201\n\t"
      "v_mfma_f32_16x16x32_bf16 v[12:15], a[92:95], v[124:127], v[12:15]\n\t"
      "v_and_b32 v197, s74, v202\n\t"
      "v_lshlrev_b32 v196, 16, v202\n\t"
      "ds_read_b128 v[108:111], v255 offset:27648\n\t"
      "s_waitcnt lgkmcnt(11)\n\t"
      "v_mfma_f32_16x16x32_bf16 v[0:3], a[32:35], v[64:67], v[0:3]\n\t"
      "v_and_b32 v199, s74, v203\n\t"
      "v_lshlrev_b32 v198, 16, v203\n\t"
      "v_mfma_f32_16x16x32_bf16 v[4:7], a[96:99], v[64:67], v[4:7]\n\t"
      "v_add_f32 v32, v192, v196\n\t"
      "v_add_f32 v33, v193, v197\n\t"
      "ds_read_b128 v[112:115], v255 offset:28672\n\t"
      "s_waitcnt lgkmcnt(11)\n\t"
      "v_mfma_f32_16x16x32_bf16 v[8:11], a[32:35], v[68:71], v[8:11]\n\t"
      "v_add_f32 v34, v194, v198\n\t"
      "v_add_f32 v35, v195, v199\n\t"
      "v_mfma_f32_16x16x32_bf16 v[12:15], a[96:99], v[68:71], v[12:15]\n\t"
      "v_add_f32 v32, v32, v34\n\t"
      "v_add_f32 v33, v33, v35\n\t"
      "ds_read_b128 v[116:119], v255 offset:29696\n\t"
      "s_waitcnt lgkmcnt(11)\n\t"
      "v_mfma_f32_16x16x32_bf16 v[0:3], a[36:39], v[72:75], v[0:3]\n\t"
      "v_add_f32 v32, v32, v33\n\t"
      "v_mfma_f32_16x16x32_bf16 v[4:7], a[100:103], v[72:75], v[4:7]\n\t"
      "ds_read_b128 v[120:123], v255 offset:30720\n\t"
      "s_waitcnt lgkmcnt(11)\n\t"
      "v_mfma_f32_16x16x32_bf16 v[8:11], a[36:39], v[76:79], v[8:11]\n\t"
      "v_add_f32_dpp v32, v32, v32 quad_perm:[1,0,3,2] row_mask:0xf bank_mask:0xf\n\t"
      "v_mfma_f32_16x16x32_bf16 v[12:15], a[100:103], v[76:79], v[12:15]\n\t"
      "ds_read_b128 v[124:127], v255 offset:31744\n\t"
      "s_waitcnt lgkmcnt(11)\n\t"
      "v_mfma_f32_16x16x32_bf16 v[0:3], a[40:43], v[80:83], v[0:3]\n\t"
      "v_add_f32_dpp v32, v32, v32 quad_perm:[2,3,0,1] row_mask:0xf bank_mask:0xf\n\t"
      "v_mfma_f32_16x16x32_bf16 v[4:7], a[104:107], v[80:83], v[4:7]\n\t"
      "s_waitcnt lgkmcnt(10)\n\t"
      "v_mfma_f32_16x16x32_bf16 v[8:11], a[40:43], v[84:87], v[8:11]\n\t"
      "v_add_f32_dpp v32, v32, v32 row_ror:4 row_mask:0xf bank_mask:0xf\n\t"
      "v_mfma_f32_16x16x32_bf16 v[12:15], a[104:107], v[84:87], v[12:15]\n\t"
      "s_waitcnt lgkmcnt(9)\n\t"
      "v_mfma_f32_16x16x32_bf16 v[0:3], a[44:47], v[88:91], v[0:3]\n\t"
      "v_add_f32_dpp v32, v32, v32 row_ror:8 row_mask:0xf bank_mask:0xf\n\t"
      "v_mov_b32 v33, v32\n\t"
      "v_mfma_f32_16x16x32_bf16 v[4:7], a[108:111], v[88:91], v[4:7]\n\t"
      "s_waitcnt lgkmcnt(8)\n\t"
      "v_mfma_f32_16x16x32_bf16 v[8:11], a[44:47], v[92:95], v[8:11]\n\t"
      "v_permlane16_swap_b32 v32, v33\n\t"
      "v_add_f32 v32, v32, v33\n\t"
      "v_mfma_f32_16x16x32_bf16 v[12:15], a[108:111], v[92:95], v[12:15]\n\t"
      "v_mov_b32 v33, v32\n\t"
      "s_waitcnt lgkmcnt(7)\n\t"
      "v_mfma_f32_16x16x32_bf16 v[0:3], a[48:51], v[96:99], v[0:3]\n\t"
      "v_permlane32_swap_b32 v32, v33\n\t"
      "v_add_f32 v32, v32, v33\n\t"
      "v_mfma_f32_16x16x32_bf16 v[4:7], a[112:115], v[96:99], v[4:7]\n\t"
      "v_mul_f32 v204, 0x3b000000, v32\n\t"
      "v_sub_f32 v40, v192, v204\n\t"
      "s_waitcnt lgkmcnt(6)\n\t"
      "v_mfma_f32_16x16x32_bf16 v[8:11], a[48:51], v[100:103], v[8:11]\n\t"
      "v_sub_f32 v41, v193, v204\n\t"
      "v_sub_f32 v42, v194, v204\n\t"
      "v_mfma_f32_16x16x32_bf16 v[12:15], a[112:115], v[100:103], v[12:15]\n\t"
      "v_sub_f32 v43, v195, v204\n\t"
      "v_sub_f32 v44, v196, v204\n\t"
      "s_waitcnt lgkmcnt(5)\n\t"
      "v_mfma_f32_16x16x32_bf16 v[0:3], a[52:55], v[104:107], v[0:3]\n\t"
      "v_sub_f32 v45, v197, v204\n\t"
      "v_sub_f32 v46, v198, v204\n\t"
      "v_mfma_f32_16x16x32_bf16 v[4:7], a[116:119], v[104:107], v[4:7]\n\t"
      "v_sub_f32 v47, v199, v204\n\t"
      "v_mul_f32 v32, v40, v40\n\t"
      "s_waitcnt lgkmcnt(4)\n\t"
      "v_mfma_f32_16x16x32_bf16 v[8:11], a[52:55], v[108:111], v[8:11]\n\t"
      "v_mul_f32 v33, v41, v41\n\t"
      "v_fma_f32 v32, v42, v42, v32\n\t"
      "v_mfma_f32_16x16x32_bf16 v[12:15], a[116:119], v[108:111], v[12:15]\n\t"
      "v_fma_f32 v33, v43, v43, v33\n\t"
      "v_fma_f32 v32, v44, v44, v32\n\t"
      "s_waitcnt lgkmcnt(3)\n\t"
      "v_mfma_f32_16x16x32_bf16 v[0:3], a[56:59], v[112:115], v[0:3]\n\t"
      "v_fma_f32 v33, v45, v45, v33\n\t"
      "v_fma_f32 v32, v46, v46, v32\n\t"
      "v_mfma_f32_16x16x32_bf16 v[4:7], a[120:123], v[112:115], v[4:7]\n\t"
      "v_fma_f32 v33, v47, v47, v33\n\t"
      "v_add_f32 v32, v32, v33\n\t"
      "s_waitcnt lgkmcnt(2)\n\t"
      "v_mfma_f32_16x16x32_bf16 v[8:11], a[56:59], v[116:119], v[8:11]\n\t"
      "v_add_f32_dpp v32, v32, v32 quad_perm:[1,0,3,2] row_mask:0xf bank_mask:0xf\n\t"
      "v_mfma_f32_16x16x32_bf16 v[12:15], a[120:123], v[116:119], v[12:15]\n\t"
      "s_waitcnt lgkmcnt(1)\n\t"
      "v_mfma_f32_16x16x32_bf16 v[0:3], a[60:63], v[120:123], v[0:3]\n\t"
      "v_add_f32_dpp v32, v32, v32 quad_perm:[2,3,0,1] row_mask:0xf bank_mask:0xf\n\t"
      "v_mfma_f32_16x16x32_bf16 v[4:7], a[124:127], v[120:123], v[4:7]\n\t"
      "s_waitcnt lgkmcnt(0)\n\t"
      "v_mfma_f32_16x16x32_bf16 v[8:11], a[60:63], v[124:127], v[8:11]\n\t"
      "v_add_f32_dpp v32, v32, v32 row_ror:4 row_mask:0xf bank_mask:0xf\n\t"
      "v_mfma_f32_16x16x32_bf16 v[12:15], a[124:127], v[124:127], v[12:15]\n\t"
      "s_nop 0\n\t"
      "v_add_f32_dpp v32, v32, v32 row_ror:8 row_mask:0xf bank_mask:0xf\n\t"
      "v_mov_b32 v33, v32\n\t"
      "s_nop 1\n\t"
      "v_permlane16_swap_b32 v32, v33\n\t"
      "v_add_f32 v32, v32, v33\n\t"
      "v_mov_b32 v33, v32\n\t"
      "s_nop 1\n\t"
      "v_permlane32_swap_b32 v32, v33\n\t"
      "v_add_f32 v32, v32, v33\n\t"
      "v_mul_f32 v205, 0x3b000000, v32\n\t"
      "v_xor_b32 v254, 0x8000, v254\n\t"
      "v_xor_b32 v255, 0x8000, v255\n\t"
      "s_add_u32 s69, s53, 0x4000\n\t"
      "s_add_u32 s48, s48, 4\n\t"
      "s_waitcnt vmcnt(40)\n\t"
      "v_cvt_pk_bf16_f32 v0, v0, v1\n\t"
      "v_cvt_pk_bf16_f32 v1, v2, v3\n\t"
      "v_cvt_pk_bf16_f32 v2, v4, v5\n\t"
      "v_cvt_pk_bf16_f32 v3, v6, v7\n\t"
      "v_min_u32 v0, -2, v0\n\t"
      "v_min_u32 v1, -2, v1\n\t"
      "v_min_u32 v2, -2, v2\n\t"
      "v_min_u32 v3, -2, v3\n\t"
      "buffer_store_dwordx4 v[0:3], v238, s[36:39], s53 offen sc1\n\t"
      "v_cvt_pk_bf16_f32 v8, v8, v9\n\t"
      "v_cvt_pk_bf16_f32 v9, v10, v11\n\t"
      "v_cvt_pk_bf16_f32 v10, v12, v13\n\t"
      "v_cvt_pk_bf16_f32 v11, v14, v15\n\t"
      "v_min_u32 v8, -2, v8\n\t"
      "v_min_u32 v9, -2, v9\n\t"
      "v_min_u32 v10, -2, v10\n\t"
      "v_min_u32 v11, -2, v11\n\t"
      "buffer_store_dwordx4 v[8:11], v238, s[36:39], s69 offen sc1\n\t"
      "s_add_u32 s53, s53, 1048576\n\t"
      "s_nop 1\n\t"
      "s_branch WTOP%=\n\t"
      ".p2align 5\n\t"
      "WOL3%=:\n\t"
      "buffer_load_dwordx4 v[128:131], v252, s[36:39], s51 offen sc1\n\t"
      "s_waitcnt lgkmcnt(11)\n\t"
      "v_mfma_f32_16x16x32_bf16 v[0:3], a[0:3], v[64:67], 0\n\t"
      "v_mfma_f32_16x16x32_bf16 v[16:19], a[224:227], v[160:163], 0\n\t"
      "v_mfma_f32_16x16x32_bf16 v[4:7], a[64:67], v[64:67], 0\n\t"
      "v_mfma_f32_16x16x32_bf16 v[20:23], a[228:231], v[160:163], 0\n\t"
      "v_and_b32 v193, s74, v224\n\t"
      "v_lshlrev_b32 v192, 16, v224\n\t"
      "ds_read_b128 v[112:115], v255 offset:12288\n\t"
      "s_waitcnt lgkmcnt(11)\n\t"
      "v_mfma_f32_16x16x32_bf16 v[8:11], a[0:3], v[68:71], 0\n\t"
      "v_mfma_f32_16x16x32_bf16 v[16:19], a[224:227], v[164:167], v[16:19]\n\t"
      "v_mfma_f32_16x16x32_bf16 v[12:15], a[64:67], v[68:71], 0\n\t"
      "v_mfma_f32_16x16x32_bf16 v[20:23], a[228:231], v[164:167], v[20:23]\n\t"
      "v_and_b32 v195, s74, v225\n\t"
      "v_lshlrev_b32 v194, 16, v225\n\t"
      "ds_read_b128 v[116:119], v255 offset:13312\n\t"
      "s_waitcnt lgkmcnt(11)\n\t"
      "v_mfma_f32_16x16x32_bf16 v[0:3], a[4:7], v[72:75], v[0:3]\n\t"
      "v_mfma_f32_16x16x32_bf16 v[16:19], a[224:227], v[168:171], v[16:19]\n\t"
      "v_mfma_f32_16x16x32_bf16 v[4:7], a[68:71], v[72:75], v[4:7]\n\t"
      "v_mfma_f32_16x16x32_bf16 v[20:23], a[228:231], v[168:171], v[20:23]\n\t"
      "v_and_b32 v197, s74, v226\n\t"
      "v_lshlrev_b32 v196, 16, v226\n\t"
      "ds_read_b128 v[120:123], v255 offset:14336\n\t"
      "s_waitcnt lgkmcnt(11)\n\t"
      "v_mfma_f32_16x16x32_bf16 v[8:11], a[4:7], v[76:79], v[8:11]\n\t"
      "v_mfma_f32_16x16x32_bf16 v[16:19], a[224:227], v[172:175], v[16:19]\n\t"
      "v_mfma_f32_16x16x32_bf16 v[12:15], a[68:71], v[76:79], v[12:15]\n\t"
      "buffer_load_dwordx4 v[132:135], v252, s[36:39], s51 offen offset:512 sc1\n\t"
      "ds_read_b128 v[124:127], v255 offset:15360\n\t"
      "s_waitcnt lgkmcnt(11)\n\t"
      "v_mfma_f32_16x16x32_bf16 v[0:3], a[8:11], v[80:83], v[0:3]\n\t"
      "v_mfma_f32_16x16x32_bf16 v[20:23], a[228:231], v[172:175], v[20:23]\n\t"
      "v_and_b32 v199, s74, v227\n\t"
      "v_lshlrev_b32 v198, 16, v227\n\t"
      "v_mfma_f32_16x16x32_bf16 v[4:7], a[72:75], v[80:83], v[4:7]\n\t"
      "v_mfma_f32_16x16x32_bf16 v[16:19], a[224:227], v[176:179], v[16:19]\n\t"
      "ds_read_b128 v[64:67], v255 offset:16384\n\t"
      "s_waitcnt lgkmcnt(11)\n\t"
      "v_mfma_f32_16x16x32_bf16 v[8:11], a[8:11], v[84:87], v[8:11]\n\t"
      "v_mfma_f32_16x16x32_bf16 v[20:23], a[228:231], v[176:179], v[20:23]\n\t"
      "v_mfma_f32_16x16x32_bf16 v[12:15], a[72:75], v[84:87], v[12:15]\n\t"
      "v_mfma_f32_16x16x32_bf16 v[16:19], a[224:227], v[180:183], v[16:19]\n\t"
      "ds_read_b128 v[68:71], v255 offset:17408\n\t"
      "s_waitcnt lgkmcnt(11)\n\t"
      "v_mfma_f32_16x16x32_bf16 v[0:3], a[12:15], v[88:91], v[0:3]\n\t"
      "v_mfma_f32_16x16x32_bf16 v[20:23], a[228:231], v[180:183], v[20:23]\n\t"
      "v_mfma_f32_16x16x32_bf16 v[4:7], a[76:79], v[88:91], v[4:7]\n\t"
      "v_mfma_f32_16x16x32_bf16 v[16:19], a[224:227], v[184:187], v[16:19]\n\t"
      "ds_read_b128 v[72:75], v255 offset:18432\n\t"
      "s_waitcnt lgkmcnt(11)\n\t"
      "v_mfma_f32_16x16x32_bf16 v[8:11], a[12:15], v[92:95], v[8:11]\n\t"
      "v_mfma_f32_16x16x32_bf16 v[20:23], a[228:231], v[184:187], v[20:23]\n\t"
      "v_mfma_f32_16x16x32_bf16 v[12:15], a[76:79], v[92:95], v[12:15]\n\t"
      "buffer_load_dwordx4 v[136:139], v252, s[36:39], s51 offen offset:1024 sc1\n\t"
      "ds_read_b128 v[76:79], v255 offset:19456\n\t"
      "s_waitcnt lgkmcnt(11)\n\t"
      "v_mfma_f32_16x16x32_bf16 v[0:3], a[16:19], v[96:99], v[0:3]\n\t"
      "v_mfma_f32_16x16x32_bf16 v[16:19], a[224:227], v[188:191], v[16:19]\n\t"
      "v_mfma_f32_16x16x32_bf16 v[4:7], a[80:83], v[96:99], v[4:7]\n\t"
      "v_mfma_f32_16x16x32_bf16 v[20:23], a[228:231], v[188:191], v[20:23]\n\t"
      "ds_read_b128 v[80:83], v255 offset:20480\n\t"
      "s_waitcnt lgkmcnt(11)\n\t"
      "v_mfma_f32_16x16x32_bf16 v[8:11], a[16:19], v[100:103], v[8:11]\n\t"
      "v_cvt_pk_bf16_f32 v32, v16, v17\n\t"
      "v_cvt_pk_bf16_f32 v34, v18, v19\n\t"
      "v_mfma_f32_16x16x32_bf16 v[12:15], a[80:83], v[100:103], v[12:15]\n\t"
      "ds_read_b128 v[84:87], v255 offset:21504\n\t"
      "s_waitcnt lgkmcnt(11)\n\t"
      "v_mfma_f32_16x16x32_bf16 v[0:3], a[20:23], v[104:107], v[0:3]\n\t"
      "v_cvt_pk_bf16_f32 v36, v20, v21\n\t"
      "v_cvt_pk_bf16_f32 v38, v22, v23\n\t"
      "v_mfma_f32_16x16x32_bf16 v[4:7], a[84:87], v[104:107], v[4:7]\n\t"
      "v_and_b32 v33, s74, v32\n\t"
      "v_lshlrev_b32 v32, 16, v32\n\t"
      "ds_read_b128 v[88:91], v255 offset:22528\n\t"
      "s_waitcnt lgkmcnt(11)\n\t"
      "v_mfma_f32_16x16x32_bf16 v[8:11], a[20:23], v[108:111], v[8:11]\n\t"
      "v_and_b32 v35, s74, v34\n\t"
      "v_lshlrev_b32 v34, 16, v34\n\t"
      "v_mfma_f32_16x16x32_bf16 v[12:15], a[84:87], v[108:111], v[12:15]\n\t"
      "buffer_load_dwordx4 v[140:143], v252, s[36:39], s51 offen offset:1536 sc1\n\t"
      "ds_read_b128 v[92:95], v255 offset:23552\n\t"
      "s_waitcnt lgkmcnt(11)\n\t"
      "v_mfma_f32_16x16x32_bf16 v[0:3], a[24:27], v[112:115], v[0:3]\n\t"
      "v_and_b32 v37, s74, v36\n\t"
      "v_lshlrev_b32 v36, 16, v36\n\t"
      "v_mfma_f32_16x16x32_bf16 v[4:7], a[88:91], v[112:115], v[4:7]\n\t"
      "v_and_b32 v39, s74, v38\n\t"
      "v_lshlrev_b32 v38, 16, v38\n\t"
      "ds_read_b128 v[96:99], v255 offset:24576\n\t"
      "s_waitcnt lgkmcnt(11)\n\t"
      "v_mfma_f32_16x16x32_bf16 v[8:11], a[24:27], v[116:119], v[8:11]\n\t"
      "v_add_f32 v192, v192, v32\n\t"
      "v_add_f32 v193, v193, v33\n\t"
      "v_mfma_f32_16x16x32_bf16 v[12:15], a[88:91], v[116:119], v[12:15]\n\t"
      "v_add_f32 v194, v194, v34\n\t"
      "v_add_f32 v195, v195, v35\n\t"
      "ds_read_b128 v[100:103], v255 offset:25600\n\t"
      "s_waitcnt lgkmcnt(11)\n\t"
      "v_mfma_f32_16x16x32_bf16 v[0:3], a[28:31], v[120:123], v[0:3]\n\t"
      "v_add_f32 v196, v196, v36\n\t"
      "v_add_f32 v197, v197, v37\n\t"
      "v_mfma_f32_16x16x32_bf16 v[4:7], a[92:95], v[120:123], v[4:7]\n\t"
      "v_add_f32 v198, v198, v38\n\t"
      "v_add_f32 v199, v199, v39\n\t"
      "ds_read_b128 v[104:107], v255 offset:26624\n\t"
      "s_waitcnt lgkmcnt(11)\n\t"
      "v_mfma_f32_16x16x32_bf16 v[8:11], a[28:31], v[124:127], v[8:11]\n\t"
      "buffer_load_dwordx4 v[144:147], v253, s[36:39], s52 offen sc1\n\t"
      "v_mfma_f32_16x16x32_bf16 v[12:15], a[92:95], v[124:127], v[12:15]\n\t"
      "v_cvt_pk_bf16_f32 v200, v192, v193\n\t"
      "v_cvt_pk_bf16_f32 v201, v194, v195\n\t"
      "ds_read_b128 v[108:111], v255 offset:27648\n\t"
      "s_waitcnt lgkmcnt(11)\n\t"
      "v_mfma_f32_16x16x32_bf16 v[0:3], a[32:35], v[64:67], v[0:3]\n\t"
      "v_cvt_pk_bf16_f32 v202, v196, v197\n\t"
      "v_cvt_pk_bf16_f32 v203, v198, v199\n\t"
      "v_mfma_f32_16x16x32_bf16 v[4:7], a[96:99], v[64:67], v[4:7]\n\t"
      "v_and_b32 v193, s74, v200\n\t"
      "v_lshlrev_b32 v192, 16, v200\n\t"
      "ds_read_b128 v[112:115], v255 offset:28672\n\t"
      "s_waitcnt lgkmcnt(11)\n\t"
      "v_mfma_f32_16x16x32_bf16 v[8:11], a[32:35], v[68:71], v[8:11]\n\t"
      "v_and_b32 v195, s74, v201\n\t"
      "v_lshlrev_b32 v194, 16, v201\n\t"
      "v_mfma_f32_16x16x32_bf16 v[12:15], a[96:99], v[68:71], v[12:15]\n\t"
      "v_and_b32 v197, s74, v202\n\t"
      "v_lshlrev_b32 v196, 16, v202\n\t"
      "ds_read_b128 v[116:119], v255 offset:29696\n\t"
      "s_waitcnt lgkmcnt(11)\n\t"
      "v_mfma_f32_16x16x32_bf16 v[0:3], a[36:39], v[72:75], v[0:3]\n\t"
      "v_and_b32 v199, s74, v203\n\t"
      "v_lshlrev_b32 v198, 16, v203\n\t"
      "v_mfma_f32_16x16x32_bf16 v[4:7], a[100:103], v[72:75], v[4:7]\n\t"
      "v_add_f32 v32, v192, v196\n\t"
      "v_add_f32 v33, v193, v197\n\t"
      "ds_read_b128 v[120:123], v255 offset:30720\n\t"
      "s_waitcnt lgkmcnt(11)\n\t"
      "v_mfma_f32_16x16x32_bf16 v[8:11], a[36:39], v[76:79], v[8:11]\n\t"
      "buffer_load_dwordx4 v[148:151], v253, s[36:39], s52 offen offset:512 sc1\n\t"
      "v_mfma_f32_16x16x32_bf16 v[12:15], a[100:103], v[76:79], v[12:15]\n\t"
      "v_add_f32 v34, v194, v198\n\t"
      "v_add_f32 v35, v195, v199\n\t"
      "ds_read_b128 v[124:127], v255 offset:31744\n\t"
      "s_waitcnt lgkmcnt(11)\n\t"
      "v_mfma_f32_16x16x32_bf16 v[0:3], a[40:43], v[80:83], v[0:3]\n\t"
      "v_add_f32 v32, v32, v34\n\t"
      "v_add_f32 v33, v33, v35\n\t"
      "v_mfma_f32_16x16x32_bf16 v[4:7], a[104:107], v[80:83], v[4:7]\n\t"
      "v_add_f32 v32, v32, v33\n\t"
      "s_waitcnt lgkmcnt(10)\n\t"
      "v_mfma_f32_16x16x32_bf16 v[8:11], a[40:43], v[84:87], v[8:11]\n\t"
      "v_add_f32_dpp v32, v32, v32 quad_perm:[1,0,3,2] row_mask:0xf bank_mask:0xf\n\t"
      "v_mfma_f32_16x16x32_bf16 v[12:15], a[104:107], v[84:87], v[12:15]\n\t"
      "s_waitcnt lgkmcnt(9)\n\t"
      "v_mfma_f32_16x16x32_bf16 v[0:3], a[44:47], v[88:91], v[0:3]\n\t"
      "v_add_f32_dpp v32, v32, v32 quad_perm:[2,3,0,1] row_mask:0xf bank_mask:0xf\n\t"
      "v_mfma_f32_16x16x32_bf16 v[4:7], a[108:111], v[88:91], v[4:7]\n\t"
      "s_waitcnt lgkmcnt(8)\n\t"
      "v_mfma_f32_16x16x32_bf16 v[8:11], a[44:47], v[92:95], v[8:11]\n\t"
      "buffer_load_dwordx4 v[152:155], v253, s[36:39], s52 offen offset:1024 sc1\n\t"
      "v_mfma_f32_16x16x32_bf16 v[12:15], a[108:111], v[92:95], v[12:15]\n\t"
      "v_add_f32_dpp v32, v32, v32 row_ror:4 row_mask:0xf bank_mask:0xf\n\t"
      "s_waitcnt lgkmcnt(7)\n\t"
      "v_mfma_f32_16x16x32_bf16 v[0:3], a[48:51], v[96:99], v[0:3]\n\t"
      "v_add_f32_dpp v32, v32, v32 row_ror:8 row_mask:0xf bank_mask:0xf\n\t"
      "v_mov_b32 v33, v32\n\t"
      "v_mfma_f32_16x16x32_bf16 v[4:7], a[112:115], v[96:99], v[4:7]\n\t"
      "s_waitcnt lgkmcnt(6)\n\t"
      "v_mfma_f32_16x16x32_bf16 v[8:11], a[48:51], v[100:103], v[8:11]\n\t"
      "v_permlane16_swap_b32 v32, v33\n\t"
      "v_add_f32 v32, v32, v33\n\t"
      "v_mfma_f32_16x16x32_bf16 v[12:15], a[112:115], v[100:103], v[12:15]\n\t"
      "v_mov_b32 v33, v32\n\t"
      "s_waitcnt lgkmcnt(5)\n\t"
      "v_mfma_f32_16x16x32_bf16 v[0:3], a[52:55], v[104:107], v[0:3]\n\t"
      "v_permlane32_swap_b32 v32, v33\n\t"
      "v_add_f32 v32, v32, v33\n\t"
      "v_mfma_f32_16x16x32_bf16 v[4:7], a[116:119], v[104:107], v[4:7]\n\t"
      "v_mul_f32 v204, 0x3b000000, v32\n\t"
      "v_sub_f32 v40, v192, v204\n\t"
      "s_waitcnt lgkmcnt(4)\n\t"
      "v_mfma_f32_16x16x32_bf16 v[8:11], a[52:55], v[108:111], v[8:11]\n\t"
      "v_sub_f32 v41, v193, v204\n\t"
      "v_sub_f32 v42, v194, v204\n\t"
      "v_mfma_f32_16x16x32_bf16 v[12:15], a[116:119], v[108:111], v[12:15]\n\t"
      "buffer_load_dwordx4 v[156:159], v253, s[36:39], s52 offen offset:1536 sc1\n\t"
      "s_waitcnt lgkmcnt(3)\n\t"
      "v_mfma_f32_16x16x32_bf16 v[0:3], a[56:59], v[112:115], v[0:3]\n\t"
      "v_sub_f32 v43, v195, v204\n\t"
      "v_sub_f32 v44, v196, v204\n\t"
      "v_mfma_f32_16x16x32_bf16 v[4:7], a[120:123], v[112:115], v[4:7]\n\t"
      "v_sub_f32 v45, v197, v204\n\t"
      "v_sub_f32 v46, v198, v204\n\t"
      "s_waitcnt lgkmcnt(2)\n\t"
      "v_mfma_f32_16x16x32_bf16 v[8:11], a[56:59], v[116:119], v[8:11]\n\t"
      "v_sub_f32 v47, v199, v204\n\t"
      "v_mul_f32 v32, v40, v40\n\t"
      "v_mfma_f32_16x16x32_bf16 v[12:15], a[120:123], v[116:119], v[12:15]\n\t"
      "v_mul_f32 v33, v41, v41\n\t"
      "v_fma_f32 v32, v42, v42, v32\n\t"
      "s_waitcnt lgkmcnt(1)\n\t"
      "v_mfma_f32_16x16x32_bf16 v[0:3], a[60:63], v[120:123], v[0:3]\n\t"
      "v_fma_f32 v33, v43, v43, v33\n\t"
      "v_fma_f32 v32, v44, v44, v32\n\t"
      "v_mfma_f32_16x16x32_bf16 v[4:7], a[124:127], v[120:123], v[4:7]\n\t"
      "v_fma_f32 v33, v45, v45, v33\n\t"
      "v_fma_f32 v32, v46, v46, v32\n\t"
      "s_waitcnt lgkmcnt(0)\n\t"
      "v_mfma_f32_16x16x32_bf16 v[8:11], a[60:63], v[124:127], v[8:11]\n\t"
      "v_fma_f32 v33, v47, v47, v33\n\t"
      "v_add_f32 v32, v32, v33\n\t"
      "v_mfma_f32_16x16x32_bf16 v[12:15], a[124:127], v[124:127], v[12:15]\n\t"
      "s_nop 0\n\t"
      "v_add_f32_dpp v32, v32, v32 quad_perm:[1,0,3,2] row_mask:0xf bank_mask:0xf\n\t"
      "s_nop 1\n\t"
      "v_add_f32_dpp v32, v32, v32 quad_perm:[2,3,0,1] row_mask:0xf bank_mask:0xf\n\t"
      "s_nop 1\n\t"
      "v_add_f32_dpp v32, v32, v32 row_ror:4 row_mask:0xf bank_mask:0xf\n\t"
      "s_nop 1\n\t"
      "v_add_f32_dpp v32, v32, v32 row_ror:8 row_mask:0xf bank_mask:0xf\n\t"
      "v_mov_b32 v33, v32\n\t"
      "s_nop 1\n\t"
      "v_permlane16_swap_b32 v32, v33\n\t"
      "v_add_f32 v32, v32, v33\n\t"
      "v_mov_b32 v33, v32\n\t"
      "s_nop 1\n\t"
      "v_permlane32_swap_b32 v32, v33\n\t"
      "v_add_f32 v32, v32, v33\n\t"
      "v_mul_f32 v205, 0x3b000000, v32\n\t"
      "v_xor_b32 v254, 0x8000, v254\n\t"
      "v_xor_b32 v255, 0x8000, v255\n\t"
      "s_add_u32 s69, s53, 0x4000\n\t"
      "s_add_u32 s48, s48, 4\n\t"
      "s_waitcnt vmcnt(40)\n\t"
      "v_cvt_pk_bf16_f32 v0, v0, v1\n\t"
      "v_cvt_pk_bf16_f32 v1, v2, v3\n\t"
      "v_cvt_pk_bf16_f32 v2, v4, v5\n\t"
      "v_cvt_pk_bf16_f32 v3, v6, v7\n\t"
      "v_min_u32 v0, -2, v0\n\t"
      "v_min_u32 v1, -2, v1\n\t"
      "v_min_u32 v2, -2, v2\n\t"
      "v_min_u32 v3, -2, v3\n\t"
      "buffer_store_dwordx4 v[0:3], v238, s[36:39], s53 offen sc1\n\t"
      "v_cvt_pk_bf16_f32 v8, v8, v9\n\t"
      "v_cvt_pk_bf16_f32 v9, v10, v11\n\t"
      "v_cvt_pk_bf16_f32 v10, v12, v13\n\t"
      "v_cvt_pk_bf16_f32 v11, v14, v15\n\t"
      "v_min_u32 v8, -2, v8\n\t"
      "v_min_u32 v9, -2, v9\n\t"
      "v_min_u32 v10, -2, v10\n\t"
      "v_min_u32 v11, -2, v11\n\t"
      "buffer_store_dwordx4 v[8:11], v238, s[36:39], s69 offen sc1\n\t"
      "s_add_u32 s53, s53, 1048576\n\t"
      "s_nop 1\n\t"
      "s_branch WTOP%=\n\t"
      ".p2align 5\n\t"
      "WOM2%=:\n\t"
      "s_mov_b32 s65, 0\n\t"
      "s_add_u32 s24, s96, 8\n\t"
      "s_cmp_lg_u32 s96, 0\n\t"
      "s_cbranch_scc1 WOL5%=\n\t"
      "WOL4%=:\n\t"
      "s_waitcnt lgkmcnt(11)\n\t"
      "v_mfma_f32_16x16x32_bf16 v[0:3], a[0:3], v[64:67], 0\n\t"
      "v_cvt_pk_bf16_f32 v204, v204, v205\n\t"
      "v_and_b32 v205, s74, v204\n\t"
      "v_mfma_f32_16x16x32_bf16 v[4:7], a[64:67], v[64:67], 0\n\t"
      "v_lshlrev_b32 v204, 16, v204\n\t"
      "v_add_f32 v205, 0x3727c5ac, v205\n\t"
      "ds_read_b128 v[112:115], v255 offset:12288\n\t"
      "s_waitcnt lgkmcnt(11)\n\t"
      "v_mfma_f32_16x16x32_bf16 v[8:11], a[0:3], v[68:71], 0\n\t"
      "v_sub_f32 v40, v192, v204\n\t"
      "v_sub_f32 v41, v193, v204\n\t"
      "v_mfma_f32_16x16x32_bf16 v[12:15], a[64:67], v[68:71], 0\n\t"
      "v_sub_f32 v42, v194, v204\n\t"
      "v_sub_f32 v43, v195, v204\n\t"
      "ds_read_b128 v[116:119], v255 offset:13312\n\t"
      "s_waitcnt lgkmcnt(11)\n\t"
      "v_mfma_f32_16x16x32_bf16 v[0:3], a[4:7], v[72:75], v[0:3]\n\t"
      "v_sub_f32 v44, v196, v204\n\t"
      "v_sub_f32 v45, v197, v204\n\t"
      "v_mfma_f32_16x16x32_bf16 v[4:7], a[68:71], v[72:75], v[4:7]\n\t"
      "v_sub_f32 v46, v198, v204\n\t"
      "v_sub_f32 v47, v199, v204\n\t"
      "ds_read_b128 v[120:123], v255 offset:14336\n\t"
      "s_waitcnt lgkmcnt(11)\n\t"
      "v_mfma_f32_16x16x32_bf16 v[8:11], a[4:7], v[76:79], v[8:11]\n\t"
      "v_cvt_pk_bf16_f32 v205, v205, v205\n\t"
      "v_and_b32 v205, s74, v205\n\t"
      "v_mfma_f32_16x16x32_bf16 v[12:15], a[68:71], v[76:79], v[12:15]\n\t"
      "v_sqrt_f32 v205, v205\n\t"
      "ds_read_b128 v[124:127], v255 offset:15360\n\t"
      "s_waitcnt lgkmcnt(11)\n\t"
      "v_mfma_f32_16x16x32_bf16 v[0:3], a[8:11], v[80:83], v[0:3]\n\t"
      "v_cvt_pk_bf16_f32 v40, v40, v41\n\t"
      "v_cvt_pk_bf16_f32 v42, v42, v43\n\t"
      "v_mfma_f32_16x16x32_bf16 v[4:7], a[72:75], v[80:83], v[4:7]\n\t"
      "v_cvt_pk_bf16_f32 v44, v44, v45\n\t"
      "v_cvt_pk_bf16_f32 v46, v46, v47\n\t"
      "ds_read_b128 v[64:67], v255 offset:16384\n\t"
      "s_waitcnt lgkmcnt(11)\n\t"
      "v_mfma_f32_16x16x32_bf16 v[8:11], a[8:11], v[84:87], v[8:11]\n\t"
      "v_cvt_pk_bf16_f32 v205, v205, v205\n\t"
      "v_and_b32 v205, s74, v205\n\t"
      "v_mfma_f32_16x16x32_bf16 v[12:15], a[72:75], v[84:87], v[12:15]\n\t"
      "v_rcp_f32 v204, v205\n\t"
      "ds_read_b128 v[68:71], v255 offset:17408\n\t"
      "s_waitcnt lgkmcnt(11)\n\t"
      "v_mfma_f32_16x16x32_bf16 v[0:3], a[12:15], v[88:91], v[0:3]\n\t"
      "v_and_b32 v41, s74, v40\n\t"
      "v_lshlrev_b32 v40, 16, v40\n\t"
      "v_mfma_f32_16x16x32_bf16 v[4:7], a[76:79], v[88:91], v[4:7]\n\t"
      "v_and_b32 v43, s74, v42\n\t"
      "v_lshlrev_b32 v42, 16, v42\n\t"
      "ds_read_b128 v[72:75], v255 offset:18432\n\t"
      "s_waitcnt lgkmcnt(11)\n\t"
      "v_mfma_f32_16x16x32_bf16 v[8:11], a[12:15], v[92:95], v[8:11]\n\t"
      "v_and_b32 v45, s74, v44\n\t"
      "v_lshlrev_b32 v44, 16, v44\n\t"
      "v_mfma_f32_16x16x32_bf16 v[12:15], a[76:79], v[92:95], v[12:15]\n\t"
      "v_and_b32 v47, s74, v46\n\t"
      "v_lshlrev_b32 v46, 16, v46\n\t"
      "ds_read_b128 v[76:79], v255 offset:19456\n\t"
      "s_waitcnt lgkmcnt(11)\n\t"
      "v_mfma_f32_16x16x32_bf16 v[0:3], a[16:19], v[96:99], v[0:3]\n\t"
      "v_mul_f32 v40, v40, v204\n\t"
      "v_mul_f32 v41, v41, v204\n\t"
      "v_mfma_f32_16x16x32_bf16 v[4:7], a[80:83], v[96:99], v[4:7]\n\t"
      "v_mul_f32 v42, v42, v204\n\t"
      "v_mul_f32 v43, v43, v204\n\t"
      "ds_read_b128 v[80:83], v255 offset:20480\n\t"
      "s_waitcnt lgkmcnt(11)\n\t"
      "v_mfma_f32_16x16x32_bf16 v[8:11], a[16:19], v[100:103], v[8:11]\n\t"
      "v_mul_f32 v44, v44, v204\n\t"
      "v_mul_f32 v45, v45, v204\n\t"
      "v_mfma_f32_16x16x32_bf16 v[12:15], a[80:83], v[100:103], v[12:15]\n\t"
      "v_mul_f32 v46, v46, v204\n\t"
      "v_mul_f32 v47, v47, v204\n\t"
      "ds_read_b128 v[84:87], v255 offset:21504\n\t"
      "s_waitcnt lgkmcnt(11)\n\t"
      "v_mfma_f32_16x16x32_bf16 v[0:3], a[20:23], v[104:107], v[0:3]\n\t"
      "v_cvt_pk_bf16_f32 v52, v40, v41\n\t"
      "v_cvt_pk_bf16_f32 v53, v42, v43\n\t"
      "v_mfma_f32_16x16x32_bf16 v[4:7], a[84:87], v[104:107], v[4:7]\n\t"
      "v_cvt_pk_bf16_f32 v54, v44, v45\n\t"
      "v_cvt_pk_bf16_f32 v55, v46, v47\n\t"
      "ds_read_b128 v[88:91], v255 offset:22528\n\t"
      "s_waitcnt lgkmcnt(11)\n\t"
      "v_mfma_f32_16x16x32_bf16 v[8:11], a[20:23], v[108:111], v[8:11]\n\t"
      "v_and_b32 v33, s74, v52\n\t"
      "v_lshlrev_b32 v32, 16, v52\n\t"
      "v_mfma_f32_16x16x32_bf16 v[12:15], a[84:87], v[108:111], v[12:15]\n\t"
      "v_and_b32 v35, s74, v53\n\t"
      "v_lshlrev_b32 v34, 16, v53\n\t"
      "ds_read_b128 v[92:95], v255 offset:23552\n\t"
      "s_waitcnt lgkmcnt(11)\n\t"
      "v_mfma_f32_16x16x32_bf16 v[0:3], a[24:27], v[112:115], v[0:3]\n\t"
      "v_and_b32 v37, s74, v54\n\t"
      "v_lshlrev_b32 v36, 16, v54\n\t"
      "v_mfma_f32_16x16x32_bf16 v[4:7], a[88:91], v[112:115], v[4:7]\n\t"
      "v_and_b32 v39, s74, v55\n\t"
      "v_lshlrev_b32 v38, 16, v55\n\t"
      "ds_read_b128 v[96:99], v255 offset:24576\n\t"
      "s_waitcnt lgkmcnt(11)\n\t"
      "v_mfma_f32_16x16x32_bf16 v[8:11], a[24:27], v[116:119], v[8:11]\n\t"
      "v_mul_f32 v40, v32, v240\n\t"
      "v_mul_f32 v41, v33, v241\n\t"
      "v_mfma_f32_16x16x32_bf16 v[12:15], a[88:91], v[116:119], v[12:15]\n\t"
      "v_fma_f32 v40, v34, v242, v40\n\t"
      "v_fma_f32 v41, v35, v243, v41\n\t"
      "ds_read_b128 v[100:103], v255 offset:25600\n\t"
      "s_waitcnt lgkmcnt(11)\n\t"
      "v_mfma_f32_16x16x32_bf16 v[0:3], a[28:31], v[120:123], v[0:3]\n\t"
      "v_fma_f32 v40, v36, v244, v40\n\t"
      "v_fma_f32 v41, v37, v245, v41\n\t"
      "v_mfma_f32_16x16x32_bf16 v[4:7], a[92:95], v[120:123], v[4:7]\n\t"
      "v_fma_f32 v40, v38, v246, v40\n\t"
      "v_fma_f32 v41, v39, v247, v41\n\t"
      "ds_read_b128 v[104:107], v255 offset:26624\n\t"
      "s_waitcnt lgkmcnt(11)\n\t"
      "v_mfma_f32_16x16x32_bf16 v[8:11], a[28:31], v[124:127], v[8:11]\n\t"
      "v_add_f32 v40, v40, v41\n\t"
      "s_lshl_b32 s73, s49, 5\n\t"
      "s_add_u32 s73, s73, s60\n\t"
      "s_lshl_b32 s69, s73, 2\n\t"
      "s_add_u32 s69, s69, s57\n\t"
      "s_lshl_b32 s70, s73, 7\n\t"
      "s_add_u32 s70, s70, s57\n\t"
      "s_sub_u32 s70, s70, 212992\n\t"
      "s_cmp_ge_u32 s49, 56\n\t"
      "s_cselect_b32 s70, s70, s69\n\t"
      "v_add_f32_dpp v40, v40, v40 quad_perm:[1,0,3,2] row_mask:0xf bank_mask:0xf\n\t"
      "v_mfma_f32_16x16x32_bf16 v[12:15], a[92:95], v[124:127], v[12:15]\n\t"
      "ds_read_b128 v[108:111], v255 offset:27648\n\t"
      "s_waitcnt lgkmcnt(11)\n\t"
      "v_mfma_f32_16x16x32_bf16 v[0:3], a[32:35], v[64:67], v[0:3]\n\t"
      "v_add_f32_dpp v40, v40, v40 quad_perm:[2,3,0,1] row_mask:0xf bank_mask:0xf\n\t"
      "v_mfma_f32_16x16x32_bf16 v[4:7], a[96:99], v[64:67], v[4:7]\n\t"
      "ds_read_b128 v[112:115], v255 offset:28672\n\t"
      "s_waitcnt lgkmcnt(11)\n\t"
      "v_mfma_f32_16x16x32_bf16 v[8:11], a[32:35], v[68:71], v[8:11]\n\t"
      "v_add_f32_dpp v40, v40, v40 row_ror:4 row_mask:0xf bank_mask:0xf\n\t"
      "v_mfma_f32_16x16x32_bf16 v[12:15], a[96:99], v[68:71], v[12:15]\n\t"
      "ds_read_b128 v[116:119], v255 offset:29696\n\t"
      "s_waitcnt lgkmcnt(11)\n\t"
      "v_mfma_f32_16x16x32_bf16 v[0:3], a[36:39], v[72:75], v[0:3]\n\t"
      "v_add_f32_dpp v40, v40, v40 row_ror:8 row_mask:0xf bank_mask:0xf\n\t"
      "v_mov_b32 v41, v40\n\t"
      "v_mfma_f32_16x16x32_bf16 v[4:7], a[100:103], v[72:75], v[4:7]\n\t"
      "ds_read_b128 v[120:123], v255 offset:30720\n\t"
      "s_waitcnt lgkmcnt(11)\n\t"
      "v_mfma_f32_16x16x32_bf16 v[8:11], a[36:39], v[76:79], v[8:11]\n\t"
      "v_permlane16_swap_b32 v40, v41\n\t"
      "v_add_f32 v40, v40, v41\n\t"
      "v_mfma_f32_16x16x32_bf16 v[12:15], a[100:103], v[76:79], v[12:15]\n\t"
      "v_mov_b32 v41, v40\n\t"
      "ds_read_b128 v[124:127], v255 offset:31744\n\t"
      "s_waitcnt lgkmcnt(11)\n\t"
      "v_mfma_f32_16x16x32_bf16 v[0:3], a[40:43], v[80:83], v[0:3]\n\t"
      "v_permlane32_swap_b32 v40, v41\n\t"
      "v_add_f32 v40, v40, v41\n\t"
      "v_mfma_f32_16x16x32_bf16 v[4:7], a[104:107], v[80:83], v[4:7]\n\t"
      "v_cvt_pk_bf16_f32 v40, v40, v40\n\t"
      "v_and_b32 v40, s74, v40\n\t"
      "s_waitcnt lgkmcnt(10)\n\t"
      "v_mfma_f32_16x16x32_bf16 v[8:11], a[40:43], v[84:87], v[8:11]\n\t"
      "s_mov_b64 exec, 1\n\t"
      "buffer_store_dword v40, off, s[36:39], s70 sc1\n\t"
      "s_mov_b64 exec, -1\n\t"
      "v_add_f32 v192, v192, v32\n\t"
      "v_add_f32 v193, v193, v33\n\t"
      "v_mfma_f32_16x16x32_bf16 v[12:15], a[104:107], v[84:87], v[12:15]\n\t"
      "v_add_f32 v194, v194, v34\n\t"
      "v_add_f32 v195, v195, v35\n\t"
      "s_waitcnt lgkmcnt(9)\n\t"
      "v_mfma_f32_16x16x32_bf16 v[0:3], a[44:47], v[88:91], v[0:3]\n\t"
      "v_add_f32 v196, v196, v36\n\t"
      "v_add_f32 v197, v197, v37\n\t"
      "v_mfma_f32_16x16x32_bf16 v[4:7], a[108:111], v[88:91], v[4:7]\n\t"
      "v_add_f32 v198, v198, v38\n\t"
      "v_add_f32 v199, v199, v39\n\t"
      "s_waitcnt lgkmcnt(8)\n\t"
      "v_mfma_f32_16x16x32_bf16 v[8:11], a[44:47], v[92:95], v[8:11]\n\t"
      "v_readfirstlane_b32 s68, v40\n\t"
      "v_cvt_pk_bf16_f32 v56, v192, v193\n\t"
      "v_mfma_f32_16x16x32_bf16 v[12:15], a[108:111], v[92:95], v[12:15]\n\t"
      "v_cvt_pk_bf16_f32 v57, v194, v195\n\t"
      "v_cvt_pk_bf16_f32 v58, v196, v197\n\t"
      "s_waitcnt lgkmcnt(7)\n\t"
      "v_mfma_f32_16x16x32_bf16 v[0:3], a[48:51], v[96:99], v[0:3]\n\t"
      "v_cvt_pk_bf16_f32 v59, v198, v199\n\t"
      "s_add_u32 s67, s69, 8192\n\t"
      "s_lshl_b32 s73, s73, 10\n\t"
      "s_add_u32 s69, s73, s58\n\t"
      "s_add_u32 s70, s73, s59\n\t"
      "buffer_store_dwordx2 v[56:57], v249, s[44:47], s73 offen sc1\n\t"
      "v_mfma_f32_16x16x32_bf16 v[4:7], a[112:115], v[96:99], v[4:7]\n\t"
      "buffer_store_dwordx2 v[52:53], v249, s[36:39], s69 offen sc1\n\t"
      "buffer_store_dwordx2 v[200:201], v249, s[36:39], s70 offen sc1\n\t"
      "s_waitcnt lgkmcnt(6)\n\t"
      "v_mfma_f32_16x16x32_bf16 v[8:11], a[48:51], v[100:103], v[8:11]\n\t"
      "buffer_store_dwordx2 v[58:59], v249, s[44:47], s73 offen offset:512 sc1\n\t"
      "buffer_store_dwordx2 v[54:55], v249, s[36:39], s69 offen offset:512 sc1\n\t"
      "v_mfma_f32_16x16x32_bf16 v[12:15], a[112:115], v[100:103], v[12:15]\n\t"
      "buffer_store_dwordx2 v[202:203], v249, s[36:39], s70 offen offset:512 sc1\n\t"
      "s_mov_b32 s49, s89\n\t"
      "s_add_u32 s89, s89, 16\n\t"
      "s_lshr_b32 s69, s89, 4\n\t"
      "s_cmp_eq_u32 s69, 3\n\t"
      "s_cselect_b32 s89, s93, s89\n\t"
      "s_cmp_ge_u32 s49, 48\n\t"
      "s_cselect_b32 s60, s94, s60\n\t"
      "s_cselect_b32 s54, s95, s54\n\t"
      "s_waitcnt lgkmcnt(5)\n\t"
      "v_mfma_f32_16x16x32_bf16 v[0:3], a[52:55], v[104:107], v[0:3]\n\t"
      "v_mfma_f32_16x16x32_bf16 v[4:7], a[116:119], v[104:107], v[4:7]\n\t"
      "s_waitcnt lgkmcnt(4)\n\t"
      "v_mfma_f32_16x16x32_bf16 v[8:11], a[52:55], v[108:111], v[8:11]\n\t"
      "v_mfma_f32_16x16x32_bf16 v[12:15], a[116:119], v[108:111], v[12:15]\n\t"
      "s_waitcnt lgkmcnt(3)\n\t"
      "v_mfma_f32_16x16x32_bf16 v[0:3], a[56:59], v[112:115], v[0:3]\n\t"
      "v_mfma_f32_16x16x32_bf16 v[4:7], a[120:123], v[112:115], v[4:7]\n\t"
      "s_waitcnt lgkmcnt(2)\n\t"
      "v_mfma_f32_16x16x32_bf16 v[8:11], a[56:59], v[116:119], v[8:11]\n\t"
      "v_mfma_f32_16x16x32_bf16 v[12:15], a[120:123], v[116:119], v[12:15]\n\t"
      "s_waitcnt lgkmcnt(1)\n\t"
      "v_mfma_f32_16x16x32_bf16 v[0:3], a[60:63], v[120:123], v[0:3]\n\t"
      "v_mfma_f32_16x16x32_bf16 v[4:7], a[124:127], v[120:123], v[4:7]\n\t"
      "s_waitcnt lgkmcnt(0)\n\t"
      "v_mfma_f32_16x16x32_bf16 v[8:11], a[60:63], v[124:127], v[8:11]\n\t"
      "v_mfma_f32_16x16x32_bf16 v[12:15], a[124:127], v[124:127], v[12:15]\n\t"
      "v_xor_b32 v254, 0x8000, v254\n\t"
      "v_xor_b32 v255, 0x8000, v255\n\t"
      "s_add_u32 s69, s53, 0x4000\n\t"
      "s_add_u32 s48, s48, 4\n\t"
      "s_waitcnt vmcnt(40)\n\t"
      "v_cvt_pk_bf16_f32 v0, v0, v1\n\t"
      "v_cvt_pk_bf16_f32 v1, v2, v3\n\t"
      "v_cvt_pk_bf16_f32 v2, v4, v5\n\t"
      "v_cvt_pk_bf16_f32 v3, v6, v7\n\t"
      "v_min_u32 v0, -2, v0\n\t"
      "v_min_u32 v1, -2, v1\n\t"
      "v_min_u32 v2, -2, v2\n\t"
      "v_min_u32 v3, -2, v3\n\t"
      "buffer_store_dwordx4 v[0:3], v238, s[36:39], s53 offen sc1\n\t"
      "v_cvt_pk_bf16_f32 v8, v8, v9\n\t"
      "v_cvt_pk_bf16_f32 v9, v10, v11\n\t"
      "v_cvt_pk_bf16_f32 v10, v12, v13\n\t"
      "v_cvt_pk_bf16_f32 v11, v14, v15\n\t"
      "v_min_u32 v8, -2, v8\n\t"
      "v_min_u32 v9, -2, v9\n\t"
      "v_min_u32 v10, -2, v10\n\t"
      "v_min_u32 v11, -2, v11\n\t"
      "buffer_store_dwordx4 v[8:11], v238, s[36:39], s69 offen sc1\n\t"
      "s_add_u32 s53, s53, 1048576\n\t"
      "s_nop 1\n\t"
      "s_branch WTOP%=\n\t"
      ".p2align 5\n\t"
      "WOL5%=:\n\t"
      "buffer_load_dwordx4 v[128:131], v252, s[36:39], s51 offen sc1\n\t"
      "s_waitcnt lgkmcnt(11)\n\t"
      "v_mfma_f32_16x16x32_bf16 v[0:3], a[0:3], v[64:67], 0\n\t"
      "v_cvt_pk_bf16_f32 v204, v204, v205\n\t"
      "v_and_b32 v205, s74, v204\n\t"
      "v_mfma_f32_16x16x32_bf16 v[4:7], a[64:67], v[64:67], 0\n\t"
      "v_lshlrev_b32 v204, 16, v204\n\t"
      "v_add_f32 v205, 0x3727c5ac, v205\n\t"
      "ds_read_b128 v[112:115], v255 offset:12288\n\t"
      "s_waitcnt lgkmcnt(11)\n\t"
      "v_mfma_f32_16x16x32_bf16 v[8:11], a[0:3], v[68:71], 0\n\t"
      "v_sub_f32 v40, v192, v204\n\t"
      "v_sub_f32 v41, v193, v204\n\t"
      "v_mfma_f32_16x16x32_bf16 v[12:15], a[64:67], v[68:71], 0\n\t"
      "v_sub_f32 v42, v194, v204\n\t"
      "v_sub_f32 v43, v195, v204\n\t"
      "ds_read_b128 v[116:119], v255 offset:13312\n\t"
      "s_waitcnt lgkmcnt(11)\n\t"
      "v_mfma_f32_16x16x32_bf16 v[0:3], a[4:7], v[72:75], v[0:3]\n\t"
      "v_sub_f32 v44, v196, v204\n\t"
      "v_sub_f32 v45, v197, v204\n\t"
      "v_mfma_f32_16x16x32_bf16 v[4:7], a[68:71], v[72:75], v[4:7]\n\t"
      "v_sub_f32 v46, v198, v204\n\t"
      "v_sub_f32 v47, v199, v204\n\t"
      "ds_read_b128 v[120:123], v255 offset:14336\n\t"
      "s_waitcnt lgkmcnt(11)\n\t"
      "v_mfma_f32_16x16x32_bf16 v[8:11], a[4:7], v[76:79], v[8:11]\n\t"
      "v_cvt_pk_bf16_f32 v205, v205, v205\n\t"
      "v_and_b32 v205, s74, v205\n\t"
      "v_mfma_f32_16x16x32_bf16 v[12:15], a[68:71], v[76:79], v[12:15]\n\t"
      "buffer_load_dwordx4 v[132:135], v252, s[36:39], s51 offen offset:512 sc1\n\t"
      "ds_read_b128 v[124:127], v255 offset:15360\n\t"
      "s_waitcnt lgkmcnt(11)\n\t"
      "v_mfma_f32_16x16x32_bf16 v[0:3], a[8:11], v[80:83], v[0:3]\n\t"
      "v_sqrt_f32 v205, v205\n\t"
      "v_mfma_f32_16x16x32_bf16 v[4:7], a[72:75], v[80:83], v[4:7]\n\t"
      "v_cvt_pk_bf16_f32 v40, v40, v41\n\t"
      "v_cvt_pk_bf16_f32 v42, v42, v43\n\t"
      "ds_read_b128 v[64:67], v255 offset:16384\n\t"
      "s_waitcnt lgkmcnt(11)\n\t"
      "v_mfma_f32_16x16x32_bf16 v[8:11], a[8:11], v[84:87], v[8:11]\n\t"
      "v_cvt_pk_bf16_f32 v44, v44, v45\n\t"
      "v_cvt_pk_bf16_f32 v46, v46, v47\n\t"
      "v_mfma_f32_16x16x32_bf16 v[12:15], a[72:75], v[84:87], v[12:15]\n\t"
      "v_cvt_pk_bf16_f32 v205, v205, v205\n\t"
      "v_and_b32 v205, s74, v205\n\t"
      "ds_read_b128 v[68:71], v255 offset:17408\n\t"
      "s_waitcnt lgkmcnt(11)\n\t"
      "v_mfma_f32_16x16x32_bf16 v[0:3], a[12:15], v[88:91], v[0:3]\n\t"
      "v_rcp_f32 v204, v205\n\t"
      "v_mfma_f32_16x16x32_bf16 v[4:7], a[76:79], v[88:91], v[4:7]\n\t"
      "v_and_b32 v41, s74, v40\n\t"
      "v_lshlrev_b32 v40, 16, v40\n\t"
      "ds_read_b128 v[72:75], v255 offset:18432\n\t"
      "s_waitcnt lgkmcnt(11)\n\t"
      "v_mfma_f32_16x16x32_bf16 v[8:11], a[12:15], v[92:95], v[8:11]\n\t"
      "v_and_b32 v43, s74, v42\n\t"
      "v_lshlrev_b32 v42, 16, v42\n\t"
      "v_mfma_f32_16x16x32_bf16 v[12:15], a[76:79], v[92:95], v[12:15]\n\t"
      "buffer_load_dwordx4 v[136:139], v252, s[36:39], s51 offen offset:1024 sc1\n\t"
      "ds_read_b128 v[76:79], v255 offset:19456\n\t"
      "s_waitcnt lgkmcnt(11)\n\t"
      "v_mfma_f32_16x16x32_bf16 v[0:3], a[16:19], v[96:99], v[0:3]\n\t"
      "v_and_b32 v45, s74, v44\n\t"
      "v_lshlrev_b32 v44, 16, v44\n\t"
      "v_mfma_f32_16x16x32_bf16 v[4:7], a[80:83], v[96:99], v[4:7]\n\t"
      "v_and_b32 v47, s74, v46\n\t"
      "v_lshlrev_b32 v46, 16, v46\n\t"
      "ds_read_b128 v[80:83], v255 offset:20480\n\t"
      "s_waitcnt lgkmcnt(11)\n\t"
      "v_mfma_f32_16x16x32_bf16 v[8:11], a[16:19], v[100:103], v[8:11]\n\t"
      "v_mul_f32 v40, v40, v204\n\t"
      "v_mul_f32 v41, v41, v204\n\t"
      "v_mfma_f32_16x16x32_bf16 v[12:15], a[80:83], v[100:103], v[12:15]\n\t"
      "v_mul_f32 v42, v42, v204\n\t"
      "v_mul_f32 v43, v43, v204\n\t"
      "ds_read_b128 v[84:87], v255 offset:21504\n\t"
      "s_waitcnt lgkmcnt(11)\n\t"
      "v_mfma_f32_16x16x32_bf16 v[0:3], a[20:23], v[104:107], v[0:3]\n\t"
      "v_mul_f32 v44, v44, v204\n\t"
      "v_mul_f32 v45, v45, v204\n\t"
      "v_mfma_f32_16x16x32_bf16 v[4:7], a[84:87], v[104:107], v[4:7]\n\t"
      "v_mul_f32 v46, v46, v204\n\t"
      "v_mul_f32 v47, v47, v204\n\t"
      "ds_read_b128 v[88:91], v255 offset:22528\n\t"
      "s_waitcnt lgkmcnt(11)\n\t"
      "v_mfma_f32_16x16x32_bf16 v[8:11], a[20:23], v[108:111], v[8:11]\n\t"
      "v_cvt_pk_bf16_f32 v52, v40, v41\n\t"
      "v_cvt_pk_bf16_f32 v53, v42, v43\n\t"
      "v_mfma_f32_16x16x32_bf16 v[12:15], a[84:87], v[108:111], v[12:15]\n\t"
      "buffer_load_dwordx4 v[140:143], v252, s[36:39], s51 offen offset:1536 sc1\n\t"
      "ds_read_b128 v[92:95], v255 offset:23552\n\t"
      "s_waitcnt lgkmcnt(11)\n\t"
      "v_mfma_f32_16x16x32_bf16 v[0:3], a[24:27], v[112:115], v[0:3]\n\t"
      "v_cvt_pk_bf16_f32 v54, v44, v45\n\t"
      "v_cvt_pk_bf16_f32 v55, v46, v47\n\t"
      "v_mfma_f32_16x16x32_bf16 v[4:7], a[88:91], v[112:115], v[4:7]\n\t"
      "v_and_b32 v33, s74, v52\n\t"
      "v_lshlrev_b32 v32, 16, v52\n\t"
      "ds_read_b128 v[96:99], v255 offset:24576\n\t"
      "s_waitcnt lgkmcnt(11)\n\t"
      "v_mfma_f32_16x16x32_bf16 v[8:11], a[24:27], v[116:119], v[8:11]\n\t"
      "v_and_b32 v35, s74, v53\n\t"
      "v_lshlrev_b32 v34, 16, v53\n\t"
      "v_mfma_f32_16x16x32_bf16 v[12:15], a[88:91], v[116:119], v[12:15]\n\t"
      "v_and_b32 v37, s74, v54\n\t"
      "v_lshlrev_b32 v36, 16, v54\n\t"
      "ds_read_b128 v[100:103], v255 offset:25600\n\t"
      "s_waitcnt lgkmcnt(11)\n\t"
      "v_mfma_f32_16x16x32_bf16 v[0:3], a[28:31], v[120:123], v[0:3]\n\t"
      "v_and_b32 v39, s74, v55\n\t"
      "v_lshlrev_b32 v38, 16, v55\n\t"
      "v_mfma_f32_16x16x32_bf16 v[4:7], a[92:95], v[120:123], v[4:7]\n\t"
      "v_mul_f32 v40, v32, v240\n\t"
      "v_mul_f32 v41, v33, v241\n\t"
      "ds_read_b128 v[104:107], v255 offset:26624\n\t"
      "s_waitcnt lgkmcnt(11)\n\t"
      "v_mfma_f32_16x16x32_bf16 v[8:11], a[28:31], v[124:127], v[8:11]\n\t"
      "buffer_load_dwordx4 v[144:147], v253, s[36:39], s52 offen sc1\n\t"
      "v_mfma_f32_16x16x32_bf16 v[12:15], a[92:95], v[124:127], v[12:15]\n\t"
      "v_fma_f32 v40, v34, v242, v40\n\t"
      "v_fma_f32 v41, v35, v243, v41\n\t"
      "ds_read_b128 v[108:111], v255 offset:27648\n\t"
      "s_waitcnt lgkmcnt(11)\n\t"
      "v_mfma_f32_16x16x32_bf16 v[0:3], a[32:35], v[64:67], v[0:3]\n\t"
      "v_fma_f32 v40, v36, v244, v40\n\t"
      "v_fma_f32 v41, v37, v245, v41\n\t"
      "v_mfma_f32_16x16x32_bf16 v[4:7], a[96:99], v[64:67], v[4:7]\n\t"
      "v_fma_f32 v40, v38, v246, v40\n\t"
      "v_fma_f32 v41, v39, v247, v41\n\t"
      "ds_read_b128 v[112:115], v255 offset:28672\n\t"
      "s_waitcnt lgkmcnt(11)\n\t"
      "v_mfma_f32_16x16x32_bf16 v[8:11], a[32:35], v[68:71], v[8:11]\n\t"
      "v_add_f32 v40, v40, v41\n\t"
      "s_lshl_b32 s73, s49, 5\n\t"
      "s_add_u32 s73, s73, s60\n\t"
      "s_lshl_b32 s69, s73, 2\n\t"
      "s_add_u32 s69, s69, s57\n\t"
      "s_lshl_b32 s70, s73, 7\n\t"
      "s_add_u32 s70, s70, s57\n\t"
      "s_sub_u32 s70, s70, 212992\n\t"
      "s_cmp_ge_u32 s49, 56\n\t"
      "s_cselect_b32 s70, s70, s69\n\t"
      "v_add_f32_dpp v40, v40, v40 quad_perm:[1,0,3,2] row_mask:0xf bank_mask:0xf\n\t"
      "v_mfma_f32_16x16x32_bf16 v[12:15], a[96:99], v[68:71], v[12:15]\n\t"
      "ds_read_b128 v[116:119], v255 offset:29696\n\t"
      "s_waitcnt lgkmcnt(11)\n\t"
      "v_mfma_f32_16x16x32_bf16 v[0:3], a[36:39], v[72:75], v[0:3]\n\t"
      "v_add_f32_dpp v40, v40, v40 quad_perm:[2,3,0,1] row_mask:0xf bank_mask:0xf\n\t"
      "v_mfma_f32_16x16x32_bf16 v[4:7], a[100:103], v[72:75], v[4:7]\n\t"
      "ds_read_b128 v[120:123], v255 offset:30720\n\t"
      "s_waitcnt lgkmcnt(11)\n\t"
      "v_mfma_f32_16x16x32_bf16 v[8:11], a[36:39], v[76:79], v[8:11]\n\t"
      "buffer_load_dwordx4 v[148:151], v253, s[36:39], s52 offen offset:512 sc1\n\t"
      "v_mfma_f32_16x16x32_bf16 v[12:15], a[100:103], v[76:79], v[12:15]\n\t"
      "v_add_f32_dpp v40, v40, v40 row_ror:4 row_mask:0xf bank_mask:0xf\n\t"
      "ds_read_b128 v[124:127], v255 offset:31744\n\t"
      "s_waitcnt lgkmcnt(11)\n\t"
      "v_mfma_f32_16x16x32_bf16 v[0:3], a[40:43], v[80:83], v[0:3]\n\t"
      "v_add_f32_dpp v40, v40, v40 row_ror:8 row_mask:0xf bank_mask:0xf\n\t"
      "v_mov_b32 v41, v40\n\t"
      "v_mfma_f32_16x16x32_bf16 v[4:7], a[104:107], v[80:83], v[4:7]\n\t"
      "s_waitcnt lgkmcnt(10)\n\t"
      "v_mfma_f32_16x16x32_bf16 v[8:11], a[40:43], v[84:87], v[8:11]\n\t"
      "v_permlane16_swap_b32 v40, v41\n\t"
      "v_add_f32 v40, v40, v41\n\t"
      "v_mfma_f32_16x16x32_bf16 v[12:15], a[104:107], v[84:87], v[12:15]\n\t"
      "v_mov_b32 v41, v40\n\t"
      "s_waitcnt lgkmcnt(9)\n\t"
      "v_mfma_f32_16x16x32_bf16 v[0:3], a[44:47], v[88:91], v[0:3]\n\t"
      "v_permlane32_swap_b32 v40, v41\n\t"
      "v_add_f32 v40, v40, v41\n\t"
      "v_mfma_f32_16x16x32_bf16 v[4:7], a[108:111], v[88:91], v[4:7]\n\t"
      "v_cvt_pk_bf16_f32 v40, v40, v40\n\t"
      "v_and_b32 v40, s74, v40\n\t"
      "s_waitcnt lgkmcnt(8)\n\t"
      "v_mfma_f32_16x16x32_bf16 v[8:11], a[44:47], v[92:95], v[8:11]\n\t"
      "buffer_load_dwordx4 v[152:155], v253, s[36:39], s52 offen offset:1024 sc1\n\t"
      "v_mfma_f32_16x16x32_bf16 v[12:15], a[108:111], v[92:95], v[12:15]\n\t"
      "s_mov_b64 exec, 1\n\t"
      "buffer_store_dword v40, off, s[36:39], s70 sc1\n\t"
      "s_mov_b64 exec, -1\n\t"
      "v_add_f32 v192, v192, v32\n\t"
      "v_add_f32 v193, v193, v33\n\t"
      "s_waitcnt lgkmcnt(7)\n\t"
      "v_mfma_f32_16x16x32_bf16 v[0:3], a[48:51], v[96:99], v[0:3]\n\t"
      "v_add_f32 v194, v194, v34\n\t"
      "v_add_f32 v195, v195, v35\n\t"
      "v_mfma_f32_16x16x32_bf16 v[4:7], a[112:115], v[96:99], v[4:7]\n\t"
      "v_add_f32 v196, v196, v36\n\t"
      "v_add_f32 v197, v197, v37\n\t"
      "s_waitcnt lgkmcnt(6)\n\t"
      "v_mfma_f32_16x16x32_bf16 v[8:11], a[48:51], v[100:103], v[8:11]\n\t"
      "v_add_f32 v198, v198, v38\n\t"
      "v_add_f32 v199, v199, v39\n\t"
      "v_mfma_f32_16x16x32_bf16 v[12:15], a[112:115], v[100:103], v[12:15]\n\t"
      "v_readfirstlane_b32 s68, v40\n\t"
      "v_cvt_pk_bf16_f32 v56, v192, v193\n\t"
      "s_waitcnt lgkmcnt(5)\n\t"
      "v_mfma_f32_16x16x32_bf16 v[0:3], a[52:55], v[104:107], v[0:3]\n\t"
      "v_cvt_pk_bf16_f32 v57, v194, v195\n\t"
      "v_cvt_pk_bf16_f32 v58, v196, v197\n\t"
      "v_mfma_f32_16x16x32_bf16 v[4:7], a[116:119], v[104:107], v[4:7]\n\t"
      "v_cvt_pk_bf16_f32 v59, v198, v199\n\t"
      "s_add_u32 s67, s69, 8192\n\t"
      "s_lshl_b32 s73, s73, 10\n\t"
      "s_add_u32 s69, s73, s58\n\t"
      "s_add_u32 s70, s73, s59\n\t"
      "buffer_store_dwordx2 v[56:57], v249, s[44:47], s73 offen sc1\n\t"
      "s_waitcnt lgkmcnt(4)\n\t"
      "v_mfma_f32_16x16x32_bf16 v[8:11], a[52:55], v[108:111], v[8:11]\n\t"
      "buffer_store_dwordx2 v[52:53], v249, s[36:39], s69 offen sc1\n\t"
      "buffer_store_dwordx2 v[200:201], v249, s[36:39], s70 offen sc1\n\t"
      "v_mfma_f32_16x16x32_bf16 v[12:15], a[116:119], v[108:111], v[12:15]\n\t"
      "buffer_load_dwordx4 v[156:159], v253, s[36:39], s52 offen offset:1536 sc1\n\t"
      "s_waitcnt lgkmcnt(3)\n\t"
      "v_mfma_f32_16x16x32_bf16 v[0:3], a[56:59], v[112:115], v[0:3]\n\t"
      "buffer_store_dwordx2 v[58:59], v249, s[44:47], s73 offen offset:512 sc1\n\t"
      "buffer_store_dwordx2 v[54:55], v249, s[36:39], s69 offen offset:512 sc1\n\t"
      "v_mfma_f32_16x16x32_bf16 v[4:7], a[120:123], v[112:115], v[4:7]\n\t"
      "buffer_store_dwordx2 v[202:203], v249, s[36:39], s70 offen offset:512 sc1\n\t"
      "s_mov_b32 s49, s89\n\t"
      "s_add_u32 s89, s89, 16\n\t"
      "s_lshr_b32 s69, s89, 4\n\t"
      "s_cmp_eq_u32 s69, 3\n\t"
      "s_cselect_b32 s89, s93, s89\n\t"
      "s_cmp_ge_u32 s49, 48\n\t"
      "s_cselect_b32 s60, s94, s60\n\t"
      "s_cselect_b32 s54, s95, s54\n\t"
      "s_waitcnt lgkmcnt(2)\n\t"
      "v_mfma_f32_16x16x32_bf16 v[8:11], a[56:59], v[116:119], v[8:11]\n\t"
      "v_mfma_f32_16x16x32_bf16 v[12:15], a[120:123], v[116:119], v[12:15]\n\t"
      "s_waitcnt lgkmcnt(1)\n\t"
      "v_mfma_f32_16x16x32_bf16 v[0:3], a[60:63], v[120:123], v[0:3]\n\t"
      "v_mfma_f32_16x16x32_bf16 v[4:7], a[124:127], v[120:123], v[4:7]\n\t"
      "s_waitcnt lgkmcnt(0)\n\t"
      "v_mfma_f32_16x16x32_bf16 v[8:11], a[60:63], v[124:127], v[8:11]\n\t"
      "v_mfma_f32_16x16x32_bf16 v[12:15], a[124:127], v[124:127], v[12:15]\n\t"
      "v_xor_b32 v254, 0x8000, v254\n\t"
      "v_xor_b32 v255, 0x8000, v255\n\t"
      "s_add_u32 s69, s53, 0x4000\n\t"
      "s_add_u32 s48, s48, 4\n\t"
      "s_waitcnt vmcnt(40)\n\t"
      "v_cvt_pk_bf16_f32 v0, v0, v1\n\t"
      "v_cvt_pk_bf16_f32 v1, v2, v3\n\t"
      "v_cvt_pk_bf16_f32 v2, v4, v5\n\t"
      "v_cvt_pk_bf16_f32 v3, v6, v7\n\t"
      "v_min_u32 v0, -2, v0\n\t"
      "v_min_u32 v1, -2, v1\n\t"
      "v_min_u32 v2, -2, v2\n\t"
      "v_min_u32 v3, -2, v3\n\t"
      "buffer_store_dwordx4 v[0:3], v238, s[36:39], s53 offen sc1\n\t"
      "v_cvt_pk_bf16_f32 v8, v8, v9\n\t"
      "v_cvt_pk_bf16_f32 v9, v10, v11\n\t"
      "v_cvt_pk_bf16_f32 v10, v12, v13\n\t"
      "v_cvt_pk_bf16_f32 v11, v14, v15\n\t"
      "v_min_u32 v8, -2, v8\n\t"
      "v_min_u32 v9, -2, v9\n\t"
      "v_min_u32 v10, -2, v10\n\t"
      "v_min_u32 v11, -2, v11\n\t"
      "buffer_store_dwordx4 v[8:11], v238, s[36:39], s69 offen sc1\n\t"
      "s_add_u32 s53, s53, 1048576\n\t"
      "s_nop 1\n\t"
      "s_branch WTOP%=\n\t"
      ".p2align 5\n\t"
      "WKF%=:\n\t"
      "s_cmp_eq_u32 s48, 63\n\t"
      "s_cselect_b32 s69, 0, -9\n\t"
      "s_add_u32 s88, s88, s69\n\t"
      "s_add_u32 s88, s88, 39\n\t"
      "WKT%=:\n\t"
      "s_memrealtime s[90:91]\n\t"
      "s_waitcnt lgkmcnt(0)\n\t"
      "s_sub_u32 s69, s88, s90\n\t"
      "s_and_b32 s69, s69, 0xffff\n\t"
      "s_cmp_lt_u32 s69, 400\n\t"
      "s_cbranch_scc1 WKT%=\n\t"
      "WKL%=:\n\t"
      "buffer_load_dwordx4 v[128:131], v252, s[36:39], s51 offen sc1\n\t"
      "buffer_load_dwordx4 v[132:135], v252, s[36:39], s51 offen offset:512 sc1\n\t"
      "buffer_load_dwordx4 v[136:139], v252, s[36:39], s51 offen offset:1024 sc1\n\t"
      "buffer_load_dwordx4 v[140:143], v252, s[36:39], s51 offen offset:1536 sc1\n\t"
      "buffer_load_dwordx4 v[144:147], v253, s[36:39], s52 offen sc1\n\t"
      "buffer_load_dwordx4 v[148:151], v253, s[36:39], s52 offen offset:512 sc1\n\t"
      "buffer_load_dwordx4 v[152:155], v253, s[36:39], s52 offen offset:1024 sc1\n\t"
      "buffer_load_dwordx4 v[156:159], v253, s[36:39], s52 offen offset:1536 sc1\n\t"
      "v_lshlrev_b32 v6, 4, v248\n\t"
      "v_mov_b64 v[4:5], 0\n\t"
      "s_cmp_ge_u32 s49, s48\n\t"
      "s_cbranch_scc1 WKL1%=\n\t"
      "buffer_load_dword v4, v6, s[36:39], s55 offen sc1\n\t"
      "buffer_load_dword v5, v6, s[36:39], s55 offen offset:128 sc1\n\t"
      "WKL1%=:\n\t"
      "s_waitcnt vmcnt(0)\n\t"
      "v_min_u32 v4, v4, v5\n\t"
      "v_cmp_ge_u32 vcc, s49, v4\n\t"
      "s_nop 0\n\t"
      "s_cmp_eq_u64 vcc, 0\n\t"
      "s_cselect_b32 s66, 1, 0\n\t"
      "s_cmp_lt_u32 s48, 40\n\t"
      "s_cbranch_scc1 WKV%=\n\t"
      "WKC%=:\n\t"
      "v_max3_u32 v0, v128, v129, v130\n\t"
      "v_max3_u32 v0, v0, v131, v132\n\t"
      "v_max3_u32 v0, v0, v133, v134\n\t"
      "v_max3_u32 v0, v0, v135, v136\n\t"
      "v_max3_u32 v0, v0, v137, v138\n\t"
      "v_max3_u32 v0, v0, v139, v140\n\t"
      "v_max3_u32 v0, v0, v141, v142\n\t"
      "v_max3_u32 v0, v0, v143, v144\n\t"
      "v_max3_u32 v0, v0, v145, v146\n\t"
      "v_max3_u32 v0, v0, v147, v148\n\t"
      "v_max3_u32 v0, v0, v149, v150\n\t"
      "v_max3_u32 v0, v0, v151, v152\n\t"
      "v_max3_u32 v0, v0, v153, v154\n\t"
      "v_max3_u32 v0, v0, v155, v156\n\t"
      "v_max3_u32 v0, v0, v157, v158\n\t"
      "v_max_u32 v0, v0, v159\n\t"
      "v_cmp_eq_u32 vcc, -1, v0\n\t"
      "s_nop 0\n\t"
      "s_cbranch_vccz WKV%=\n\t"
      "s_sleep 2\n\t"
      "s_sub_u32 s72, s72, 1\n\t"
      "s_cmp_gt_i32 s72, 0\n\t"
      "s_cbranch_scc1 WKL%=\n\t"
      "WKV%=:\n\t"
      "s_cmp_lt_u32 s48, 60\n\t"
      "s_cbranch_scc1 WKV1%=\n\t"
      "s_cmp_eq_u32 s66, 0\n\t"
      "s_cbranch_scc1 WB0%=\n\t"
      "s_lshl_b32 s70, s49, 5\n\t"
      "s_add_u32 s70, s70, s60\n\t"
      "s_lshl_b32 s70, s70, 10\n\t"
      "buffer_load_dwordx2 v[224:225], v249, s[40:43], s70 offen \n\t"
      "buffer_load_dwordx2 v[226:227], v249, s[40:43], s70 offen offset:512 \n\t"
      "s_lshl_b32 s69, s49, 18\n\t"
      "s_add_u32 s69, s69, s54\n\t"
      "buffer_load_dwordx4 v[160:163], v248, s[36:39], s69 offen sc1\n\t"
      "s_add_u32 s69, s69, 0x8000\n\t"
      "buffer_load_dwordx4 v[164:167], v248, s[36:39], s69 offen sc1\n\t"
      "s_add_u32 s69, s69, 0x8000\n\t"
      "buffer_load_dwordx4 v[168:171], v248, s[36:39], s69 offen sc1\n\t"
      "s_add_u32 s69, s69, 0x8000\n\t"
      "buffer_load_dwordx4 v[172:175], v248, s[36:39], s69 offen sc1\n\t"
      "s_add_u32 s69, s69, 0x8000\n\t"
      "buffer_load_dwordx4 v[176:179], v248, s[36:39], s69 offen sc1\n\t"
      "s_add_u32 s69, s69, 0x8000\n\t"
      "buffer_load_dwordx4 v[180:183], v248, s[36:39], s69 offen sc1\n\t"
      "s_add_u32 s69, s69, 0x8000\n\t"
      "buffer_load_dwordx4 v[184:187], v248, s[36:39], s69 offen sc1\n\t"
      "s_add_u32 s69, s69, 0x8000\n\t"
      "buffer_load_dwordx4 v[188:191], v248, s[36:39], s69 offen sc1\n\t"
      "s_mov_b32 s65, 1\n\t"
      "WB0%=:\n\t"
      "v_mfma_f32_16x16x32_bf16 v[0:3], v[128:131], a[160:163], 0\n\t"
      "v_mfma_f32_16x16x32_bf16 v[4:7], v[128:131], a[164:167], 0\n\t"
      "v_mfma_f32_16x16x32_bf16 v[0:3], v[132:135], a[168:171], v[0:3]\n\t"
      "v_mfma_f32_16x16x32_bf16 v[4:7], v[132:135], a[172:175], v[4:7]\n\t"
      "v_mfma_f32_16x16x32_bf16 v[0:3], v[136:139], a[176:179], v[0:3]\n\t"
      "v_mfma_f32_16x16x32_bf16 v[4:7], v[136:139], a[180:183], v[4:7]\n\t"
      "v_mfma_f32_16x16x32_bf16 v[0:3], v[140:143], a[184:187], v[0:3]\n\t"
      "v_mfma_f32_16x16x32_bf16 v[4:7], v[140:143], a[188:191], v[4:7]\n\t"
      "s_add_u32 s51, s51, 262144\n\t"
      "s_add_u32 s52, s52, 262144\n\t"
      "s_nop 4\n\t"
      "v_max3_f32 v8, v0, v1, v2\n\t"
      "v_max3_f32 v8, v8, v3, v234\n\t"
      "v_max3_f32 v9, v4, v5, v6\n\t"
      "v_max3_f32 v9, v9, v7, v235\n\t"
      "s_nop 1\n\t"
      "v_permlane16_swap_b32 v8, v9\n\t"
      "v_max_f32 v8, v8, v9\n\t"
      "v_mov_b32 v9, v8\n\t"
      "s_nop 1\n\t"
      "v_permlane16_swap_b32 v8, v9\n\t"
      "v_pk_mul_f32 v[8:9], v[8:9], s[86:87]\n\t"
      "v_pk_fma_f32 v[10:11], v[234:235], s[86:87], v[8:9] neg_lo:[0,0,1] neg_hi:[0,0,1]\n\t"
      "v_pk_fma_f32 v[0:1], v[0:1], s[86:87], v[8:9] op_sel:[0,0,0] op_sel_hi:[1,1,0] neg_lo:[0,0,1] neg_hi:[0,0,1]\n\t"
      "v_pk_fma_f32 v[2:3], v[2:3], s[86:87], v[8:9] op_sel:[0,0,0] op_sel_hi:[1,1,0] neg_lo:[0,0,1] neg_hi:[0,0,1]\n\t"
      "v_pk_fma_f32 v[4:5], v[4:5], s[86:87], v[8:9] op_sel:[0,0,1] op_sel_hi:[1,1,1] neg_lo:[0,0,1] neg_hi:[0,0,1]\n\t"
      "v_pk_fma_f32 v[6:7], v[6:7], s[86:87], v[8:9] op_sel:[0,0,1] op_sel_hi:[1,1,1] neg_lo:[0,0,1] neg_hi:[0,0,1]\n\t"
      "v_exp_f32 v10, v10\n\t"
      "v_exp_f32 v11, v11\n\t"
      "v_exp_f32 v0, v0\n\t"
      "v_exp_f32 v1, v1\n\t"
      "v_exp_f32 v2, v2\n\t"
      "v_exp_f32 v3, v3\n\t"
      "v_exp_f32 v4, v4\n\t"
      "v_exp_f32 v5, v5\n\t"
      "v_exp_f32 v6, v6\n\t"
      "v_exp_f32 v7, v7\n\t"
      "v_pk_mul_f32 v[12:13], v[236:237], v[10:11]\n\t"
      "v_pk_add_f32 v[14:15], v[0:1], v[2:3]\n\t"
      "v_pk_add_f32 v[16:17], v[4:5], v[6:7]\n\t"
      "v_add_f32 v14, v14, v15\n\t"
      "v_add_f32 v15, v16, v17\n\t"
      "v_fma_f32 v12, v14, 0.5, v12\n\t"
      "v_fma_f32 v13, v15, 0.5, v13\n\t"
      "s_nop 1\n\t"
      "v_permlane16_swap_b32 v12, v13\n\t"
      "v_add_f32 v12, v12, v13\n\t"
      "v_mov_b32 v13, v12\n\t"
      "s_nop 1\n\t"
      "v_permlane32_swap_b32 v12, v13\n\t"
      "v_add_f32 v12, v12, v13\n\t"
      "v_mov_b32 v13, v12\n\t"
      "s_nop 1\n\t"
      "v_permlane16_swap_b32 v12, v13\n\t"
      "v_rcp_f32 v12, v12\n\t"
      "v_rcp_f32 v13, v13\n\t"
      "s_nop 0\n\t"
      "v_pk_mul_f32 v[10:11], v[10:11], v[12:13]\n\t"
      "v_pk_mul_f32 v[12:13], v[12:13], 0.5 op_sel_hi:[1,0]\n\t"
      "v_pk_mul_f32 v[0:1], v[0:1], v[12:13] op_sel:[0,0] op_sel_hi:[1,0]\n\t"
      "v_pk_mul_f32 v[2:3], v[2:3], v[12:13] op_sel:[0,0] op_sel_hi:[1,0]\n\t"
      "v_pk_mul_f32 v[4:5], v[4:5], v[12:13] op_sel:[0,1] op_sel_hi:[1,1]\n\t"
      "v_pk_mul_f32 v[6:7], v[6:7], v[12:13] op_sel:[0,1] op_sel_hi:[1,1]\n\t"
      "v_cvt_pk_bf16_f32 v16, v0, v1\n\t"
      "v_cvt_pk_bf16_f32 v17, v2, v3\n\t"
      "v_cvt_pk_bf16_f32 v18, v4, v5\n\t"
      "v_cvt_pk_bf16_f32 v19, v6, v7\n\t"
      "v_pk_mul_f32 v[64:65], v[64:65], v[10:11] op_sel:[0,0] op_sel_hi:[1,0]\n\t"
      "v_pk_mul_f32 v[66:67], v[66:67], v[10:11] op_sel:[0,0] op_sel_hi:[1,0]\n\t"
      "v_pk_mul_f32 v[68:69], v[68:69], v[10:11] op_sel:[0,0] op_sel_hi:[1,0]\n\t"
      "v_pk_mul_f32 v[70:71], v[70:71], v[10:11] op_sel:[0,0] op_sel_hi:[1,0]\n\t"
      "v_mfma_f32_16x16x16_bf16 v[64:67], v[144:145], v[16:17], v[64:67]\n\t"
      "v_pk_mul_f32 v[72:73], v[72:73], v[10:11] op_sel:[0,0] op_sel_hi:[1,0]\n\t"
      "v_pk_mul_f32 v[74:75], v[74:75], v[10:11] op_sel:[0,0] op_sel_hi:[1,0]\n\t"
      "v_mfma_f32_16x16x16_bf16 v[68:71], v[146:147], v[16:17], v[68:71]\n\t"
      "v_pk_mul_f32 v[76:77], v[76:77], v[10:11] op_sel:[0,0] op_sel_hi:[1,0]\n\t"
      "v_pk_mul_f32 v[78:79], v[78:79], v[10:11] op_sel:[0,0] op_sel_hi:[1,0]\n\t"
      "v_mfma_f32_16x16x16_bf16 v[72:75], v[148:149], v[16:17], v[72:75]\n\t"
      "v_pk_mul_f32 v[80:81], v[80:81], v[10:11] op_sel:[0,0] op_sel_hi:[1,0]\n\t"
      "v_pk_mul_f32 v[82:83], v[82:83], v[10:11] op_sel:[0,0] op_sel_hi:[1,0]\n\t"
      "v_mfma_f32_16x16x16_bf16 v[76:79], v[150:151], v[16:17], v[76:79]\n\t"
      "v_pk_mul_f32 v[84:85], v[84:85], v[10:11] op_sel:[0,0] op_sel_hi:[1,0]\n\t"
      "v_pk_mul_f32 v[86:87], v[86:87], v[10:11] op_sel:[0,0] op_sel_hi:[1,0]\n\t"
      "v_mfma_f32_16x16x16_bf16 v[80:83], v[152:153], v[16:17], v[80:83]\n\t"
      "v_pk_mul_f32 v[88:89], v[88:89], v[10:11] op_sel:[0,0] op_sel_hi:[1,0]\n\t"
      "v_pk_mul_f32 v[90:91], v[90:91], v[10:11] op_sel:[0,0] op_sel_hi:[1,0]\n\t"
      "v_mfma_f32_16x16x16_bf16 v[84:87], v[154:155], v[16:17], v[84:87]\n\t"
      "v_pk_mul_f32 v[92:93], v[92:93], v[10:11] op_sel:[0,0] op_sel_hi:[1,0]\n\t"
      "v_pk_mul_f32 v[94:95], v[94:95], v[10:11] op_sel:[0,0] op_sel_hi:[1,0]\n\t"
      "v_cvt_pk_bf16_f32 v24, v64, v65\n\t"
      "v_cvt_pk_bf16_f32 v25, v66, v67\n\t"
      "v_cvt_pk_bf16_f32 v26, v68, v69\n\t"
      "v_cvt_pk_bf16_f32 v27, v70, v71\n\t"
      "ds_write_b128 v254, v[24:27]\n\t"
      "v_mfma_f32_16x16x16_bf16 v[88:91], v[156:157], v[16:17], v[88:91]\n\t"
      "v_pk_mul_f32 v[96:97], v[96:97], v[10:11] op_sel:[0,1] op_sel_hi:[1,1]\n\t"
      "v_pk_mul_f32 v[98:99], v[98:99], v[10:11] op_sel:[0,1] op_sel_hi:[1,1]\n\t"
      "v_mfma_f32_16x16x16_bf16 v[92:95], v[158:159], v[16:17], v[92:95]\n\t"
      "v_pk_mul_f32 v[100:101], v[100:101], v[10:11] op_sel:[0,1] op_sel_hi:[1,1]\n\t"
      "v_pk_mul_f32 v[102:103], v[102:103], v[10:11] op_sel:[0,1] op_sel_hi:[1,1]\n\t"
      "v_cvt_pk_bf16_f32 v28, v72, v73\n\t"
      "v_cvt_pk_bf16_f32 v29, v74, v75\n\t"
      "v_cvt_pk_bf16_f32 v30, v76, v77\n\t"
      "v_cvt_pk_bf16_f32 v31, v78, v79\n\t"
      "ds_write_b128 v254, v[28:31] offset:2048\n\t"
      "v_mfma_f32_16x16x16_bf16 v[96:99], v[144:145], v[18:19], v[96:99]\n\t"
      "v_pk_mul_f32 v[104:105], v[104:105], v[10:11] op_sel:[0,1] op_sel_hi:[1,1]\n\t"
      "v_pk_mul_f32 v[106:107], v[106:107], v[10:11] op_sel:[0,1] op_sel_hi:[1,1]\n\t"
      "v_mfma_f32_16x16x16_bf16 v[100:103], v[146:147], v[18:19], v[100:103]\n\t"
      "v_pk_mul_f32 v[108:109], v[108:109], v[10:11] op_sel:[0,1] op_sel_hi:[1,1]\n\t"
      "v_pk_mul_f32 v[110:111], v[110:111], v[10:11] op_sel:[0,1] op_sel_hi:[1,1]\n\t"
      "v_cvt_pk_bf16_f32 v24, v80, v81\n\t"
      "v_cvt_pk_bf16_f32 v25, v82, v83\n\t"
      "v_cvt_pk_bf16_f32 v26, v84, v85\n\t"
      "v_cvt_pk_bf16_f32 v27, v86, v87\n\t"
      "ds_write_b128 v254, v[24:27] offset:4096\n\t"
      "v_mfma_f32_16x16x16_bf16 v[104:107], v[148:149], v[18:19], v[104:107]\n\t"
      "v_pk_mul_f32 v[112:113], v[112:113], v[10:11] op_sel:[0,1] op_sel_hi:[1,1]\n\t"
      "v_pk_mul_f32 v[114:115], v[114:115], v[10:11] op_sel:[0,1] op_sel_hi:[1,1]\n\t"
      "v_mfma_f32_16x16x16_bf16 v[108:111], v[150:151], v[18:19], v[108:111]\n\t"
      "v_pk_mul_f32 v[116:117], v[116:117], v[10:11] op_sel:[0,1] op_sel_hi:[1,1]\n\t"
      "v_pk_mul_f32 v[118:119], v[118:119], v[10:11] op_sel:[0,1] op_sel_hi:[1,1]\n\t"
      "v_cvt_pk_bf16_f32 v28, v88, v89\n\t"
      "v_cvt_pk_bf16_f32 v29, v90, v91\n\t"
      "v_cvt_pk_bf16_f32 v30, v92, v93\n\t"
      "v_cvt_pk_bf16_f32 v31, v94, v95\n\t"
      "ds_write_b128 v254, v[28:31] offset:6144\n\t"
      "v_mfma_f32_16x16x16_bf16 v[112:115], v[152:153], v[18:19], v[112:115]\n\t"
      "v_pk_mul_f32 v[120:121], v[120:121], v[10:11] op_sel:[0,1] op_sel_hi:[1,1]\n\t"
      "v_pk_mul_f32 v[122:123], v[122:123], v[10:11] op_sel:[0,1] op_sel_hi:[1,1]\n\t"
      "v_mfma_f32_16x16x16_bf16 v[116:119], v[154:155], v[18:19], v[116:119]\n\t"
      "v_pk_mul_f32 v[124:125], v[124:125], v[10:11] op_sel:[0,1] op_sel_hi:[1,1]\n\t"
      "v_pk_mul_f32 v[126:127], v[126:127], v[10:11] op_sel:[0,1] op_sel_hi:[1,1]\n\t"
      "v_cvt_pk_bf16_f32 v24, v96, v97\n\t"
      "v_cvt_pk_bf16_f32 v25, v98, v99\n\t"
      "v_cvt_pk_bf16_f32 v26, v100, v101\n\t"
      "v_cvt_pk_bf16_f32 v27, v102, v103\n\t"
      "ds_write_b128 v254, v[24:27] offset:1024\n\t"
      "v_mfma_f32_16x16x16_bf16 v[120:123], v[156:157], v[18:19], v[120:123]\n\t"
      "v_mfma_f32_16x16x16_bf16 v[124:127], v[158:159], v[18:19], v[124:127]\n\t"
      "v_cvt_pk_bf16_f32 v28, v104, v105\n\t"
      "v_cvt_pk_bf16_f32 v29, v106, v107\n\t"
      "v_cvt_pk_bf16_f32 v30, v108, v109\n\t"
      "v_cvt_pk_bf16_f32 v31, v110, v111\n\t"
      "ds_write_b128 v254, v[28:31] offset:3072\n\t"
      "v_cvt_pk_bf16_f32 v24, v112, v113\n\t"
      "v_cvt_pk_bf16_f32 v25, v114, v115\n\t"
      "v_cvt_pk_bf16_f32 v26, v116, v117\n\t"
      "v_cvt_pk_bf16_f32 v27, v118, v119\n\t"
      "ds_write_b128 v254, v[24:27] offset:5120\n\t"
      "v_cvt_pk_bf16_f32 v28, v120, v121\n\t"
      "v_cvt_pk_bf16_f32 v29, v122, v123\n\t"
      "v_cvt_pk_bf16_f32 v30, v124, v125\n\t"
      "v_cvt_pk_bf16_f32 v31, v126, v127\n\t"
      "ds_write_b128 v254, v[28:31] offset:7168\n\t"
      "s_waitcnt vmcnt(40) lgkmcnt(0)\n\t"
      "s_barrier\n\t"
      "WO%=:\n\t"
      "s_waitcnt vmcnt(16)\n\t"
      "ds_read_b128 v[64:67], v255\n\t"
      "ds_read_b128 v[68:71], v255 offset:1024\n\t"
      "ds_read_b128 v[72:75], v255 offset:2048\n\t"
      "ds_read_b128 v[76:79], v255 offset:3072\n\t"
      "ds_read_b128 v[80:83], v255 offset:4096\n\t"
      "ds_read_b128 v[84:87], v255 offset:5120\n\t"
      "ds_read_b128 v[88:91], v255 offset:6144\n\t"
      "ds_read_b128 v[92:95], v255 offset:7168\n\t"
      "ds_read_b128 v[96:99], v255 offset:8192\n\t"
      "ds_read_b128 v[100:103], v255 offset:9216\n\t"
      "ds_read_b128 v[104:107], v255 offset:10240\n\t"
      "ds_read_b128 v[108:111], v255 offset:11264\n\t"
      "s_cmp_ge_u32 s48, 32\n\t"
      "s_cbranch_scc1 WO3%=\n\t"
      "v_mov_b64 v[20:21], -1\n\t"
      "v_mov_b64 v[22:23], -1\n\t"
      "buffer_store_dwordx4 v[20:23], v238, s[36:39], s98 offen sc1\n\t"
      "s_bitcmp0_b32 s48, 2\n\t"
      "s_cselect_b32 s69, 1, 63\n\t"
      "s_lshl_b32 s69, s69, 14\n\t"
      "s_add_u32 s98, s98, s69\n\t"
      "WO3%=:\n\t"
      "v_lshlrev_b32 v16, 4, v248\n\t"
      "s_cmp_ge_u32 s48, 60\n\t"
      "s_cbranch_scc1 WO1%=\n\t"
      "buffer_load_dwordx4 v[228:231], off, s[36:39], s56 sc1\n\t"
      "s_mov_b32 s97, s96\n\t"
      "v_mov_b64 v[232:233], 0\n\t"
      "s_cmp_lg_u32 s65, 0\n\t"
      "s_cselect_b32 s70, s89, s49\n\t"
      "s_cmp_ge_u32 s70, s48\n\t"
      "s_cbranch_scc1 WO1%=\n\t"
      "buffer_load_dword v232, v16, s[36:39], s55 offen sc1\n\t"
      "buffer_load_dword v233, v16, s[36:39], s55 offen offset:128 sc1\n\t"
      "WO1%=:\n\t"
      "s_cmp_lt_u32 s48, 56\n\t"
      "s_cselect_b32 s69, 3145728, 0\n\t"
      "s_add_u32 s50, s50, s69\n\t"
      "s_mov_b32 s24, 0\n\t"
      "s_cmp_eq_u32 s65, 2\n\t"
      "s_cbranch_scc1 WOM2%=\n\t"
      "s_cmp_lt_u32 s48, 60\n\t"
      "s_cselect_b32 s69, s65, 0\n\t"
      "s_cmp_lg_u32 s69, 0\n\t"
      "s_cbranch_scc1 WOM1%=\n\t"
      "s_cmp_lg_u32 s96, 0\n\t"
      "s_cbranch_scc1 WOL1%=\n\t"
      "WOL0%=:\n\t"
      "s_waitcnt lgkmcnt(11)\n\t"
      "v_mfma_f32_16x16x32_bf16 v[0:3], a[0:3], v[64:67], 0\n\t"
      "v_mfma_f32_16x16x32_bf16 v[4:7], a[64:67], v[64:67], 0\n\t"
      "ds_read_b128 v[112:115], v255 offset:12288\n\t"
      "s_waitcnt lgkmcnt(11)\n\t"
      "v_mfma_f32_16x16x32_bf16 v[8:11], a[0:3], v[68:71], 0\n\t"
      "v_mfma_f32_16x16x32_bf16 v[12:15], a[64:67], v[68:71], 0\n\t"
      "ds_read_b128 v[116:119], v255 offset:13312\n\t"
      "s_waitcnt lgkmcnt(11)\n\t"
      "v_mfma_f32_16x16x32_bf16 v[0:3], a[4:7], v[72:75], v[0:3]\n\t"
      "v_mfma_f32_16x16x32_bf16 v[4:7], a[68:71], v[72:75], v[4:7]\n\t"
      "ds_read_b128 v[120:123], v255 offset:14336\n\t"
      "s_waitcnt lgkmcnt(11)\n\t"
      "v_mfma_f32_16x16x32_bf16 v[8:11], a[4:7], v[76:79], v[8:11]\n\t"
      "v_mfma_f32_16x16x32_bf16 v[12:15], a[68:71], v[76:79], v[12:15]\n\t"
      "ds_read_b128 v[124:127], v255 offset:15360\n\t"
      "s_waitcnt lgkmcnt(11)\n\t"
      "v_mfma_f32_16x16x32_bf16 v[0:3], a[8:11], v[80:83], v[0:3]\n\t"
      "v_mfma_f32_16x16x32_bf16 v[4:7], a[72:75], v[80:83], v[4:7]\n\t"
      "ds_read_b128 v[64:67], v255 offset:16384\n\t"
      "s_waitcnt lgkmcnt(11)\n\t"
      "v_mfma_f32_16x16x32_bf16 v[8:11], a[8:11], v[84:87], v[8:11]\n\t"
      "v_mfma_f32_16x16x32_bf16 v[12:15], a[72:75], v[84:87], v[12:15]\n\t"
      "ds_read_b128 v[68:71], v255 offset:17408\n\t"
      "s_waitcnt lgkmcnt(11)\n\t"
      "v_mfma_f32_16x16x32_bf16 v[0:3], a[12:15], v[88:91], v[0:3]\n\t"
      "v_mfma_f32_16x16x32_bf16 v[4:7], a[76:79], v[88:91], v[4:7]\n\t"
      "ds_read_b128 v[72:75], v255 offset:18432\n\t"
      "s_waitcnt lgkmcnt(11)\n\t"
      "v_mfma_f32_16x16x32_bf16 v[8:11], a[12:15], v[92:95], v[8:11]\n\t"
      "v_mfma_f32_16x16x32_bf16 v[12:15], a[76:79], v[92:95], v[12:15]\n\t"
      "ds_read_b128 v[76:79], v255 offset:19456\n\t"
      "s_waitcnt lgkmcnt(11)\n\t"
      "v_mfma_f32_16x16x32_bf16 v[0:3], a[16:19], v[96:99], v[0:3]\n\t"
      "v_mfma_f32_16x16x32_bf16 v[4:7], a[80:83], v[96:99], v[4:7]\n\t"
      "ds_read_b128 v[80:83], v255 offset:20480\n\t"
      "s_waitcnt lgkmcnt(11)\n\t"
      "v_mfma_f32_16x16x32_bf16 v[8:11], a[16:19], v[100:103], v[8:11]\n\t"
      "v_mfma_f32_16x16x32_bf16 v[12:15], a[80:83], v[100:103], v[12:15]\n\t"
      "ds_read_b128 v[84:87], v255 offset:21504\n\t"
      "s_waitcnt lgkmcnt(11)\n\t"
      "v_mfma_f32_16x16x32_bf16 v[0:3], a[20:23], v[104:107], v[0:3]\n\t"
      "v_mfma_f32_16x16x32_bf16 v[4:7], a[84:87], v[104:107], v[4:7]\n\t"
      "ds_read_b128 v[88:91], v255 offset:22528\n\t"
      "s_waitcnt lgkmcnt(11)\n\t"
      "v_mfma_f32_16x16x32_bf16 v[8:11], a[20:23], v[108:111], v[8:11]\n\t"
      "v_mfma_f32_16x16x32_bf16 v[12:15], a[84:87], v[108:111], v[12:15]\n\t"
      "ds_read_b128 v[92:95], v255 offset:23552\n\t"
      "s_waitcnt lgkmcnt(11)\n\t"
      "v_mfma_f32_16x16x32_bf16 v[0:3], a[24:27], v[112:115], v[0:3]\n\t"
      "v_mfma_f32_16x16x32_bf16 v[4:7], a[88:91], v[112:115], v[4:7]\n\t"
      "ds_read_b128 v[96:99], v255 offset:24576\n\t"
      "s_waitcnt lgkmcnt(11)\n\t"
      "v_mfma_f32_16x16x32_bf16 v[8:11], a[24:27], v[116:119], v[8:11]\n\t"
      "v_mfma_f32_16x16x32_bf16 v[12:15], a[88:91], v[116:119], v[12:15]\n\t"
      "ds_read_b128 v[100:103], v255 offset:25600\n\t"
      "s_waitcnt lgkmcnt(11)\n\t"
      "v_mfma_f32_16x16x32_bf16 v[0:3], a[28:31], v[120:123], v[0:3]\n\t"
      "v_mfma_f32_16x16x32_bf16 v[4:7], a[92:95], v[120:123], v[4:7]\n\t"
      "ds_read_b128 v[104:107], v255 offset:26624\n\t"
      "s_waitcnt lgkmcnt(11)\n\t"
      "v_mfma_f32_16x16x32_bf16 v[8:11], a[28:31], v[124:127], v[8:11]\n\t"
      "v_mfma_f32_16x16x32_bf16 v[12:15], a[92:95], v[124:127], v[12:15]\n\t"
      "ds_read_b128 v[108:111], v255 offset:27648\n\t"
      "s_waitcnt lgkmcnt(11)\n\t"
      "v_mfma_f32_16x16x32_bf16 v[0:3], a[32:35], v[64:67], v[0:3]\n\t"
      "v_mfma_f32_16x16x32_bf16 v[4:7], a[96:99], v[64:67], v[4:7]\n\t"
      "ds_read_b128 v[112:115], v255 offset:28672\n\t"
      "s_waitcnt lgkmcnt(11)\n\t"
      "v_mfma_f32_16x16x32_bf16 v[8:11], a[32:35], v[68:71], v[8:11]\n\t"
      "v_mfma_f32_16x16x32_bf16 v[12:15], a[96:99], v[68:71], v[12:15]\n\t"
      "ds_read_b128 v[116:119], v255 offset:29696\n\t"
      "s_waitcnt lgkmcnt(11)\n\t"
      "v_mfma_f32_16x16x32_bf16 v[0:3], a[36:39], v[72:75], v[0:3]\n\t"
      "v_mfma_f32_16x16x32_bf16 v[4:7], a[100:103], v[72:75], v[4:7]\n\t"
      "ds_read_b128 v[120:123], v255 offset:30720\n\t"
      "s_waitcnt lgkmcnt(11)\n\t"
      "v_mfma_f32_16x16x32_bf16 v[8:11], a[36:39], v[76:79], v[8:11]\n\t"
      "v_mfma_f32_16x16x32_bf16 v[12:15], a[100:103], v[76:79], v[12:15]\n\t"
      "ds_read_b128 v[124:127], v255 offset:31744\n\t"
      "s_waitcnt lgkmcnt(11)\n\t"
      "v_mfma_f32_16x16x32_bf16 v[0:3], a[40:43], v[80:83], v[0:3]\n\t"
      "v_mfma_f32_16x16x32_bf16 v[4:7], a[104:107], v[80:83], v[4:7]\n\t"
      "s_waitcnt lgkmcnt(10)\n\t"
      "v_mfma_f32_16x16x32_bf16 v[8:11], a[40:43], v[84:87], v[8:11]\n\t"
      "v_mfma_f32_16x16x32_bf16 v[12:15], a[104:107], v[84:87], v[12:15]\n\t"
      "s_waitcnt lgkmcnt(9)\n\t"
      "v_mfma_f32_16x16x32_bf16 v[0:3], a[44:47], v[88:91], v[0:3]\n\t"
      "v_mfma_f32_16x16x32_bf16 v[4:7], a[108:111], v[88:91], v[4:7]\n\t"
      "s_waitcnt lgkmcnt(8)\n\t"
      "v_mfma_f32_16x16x32_bf16 v[8:11], a[44:47], v[92:95], v[8:11]\n\t"
      "v_mfma_f32_16x16x32_bf16 v[12:15], a[108:111], v[92:95], v[12:15]\n\t"
      "s_waitcnt lgkmcnt(7)\n\t"
      "v_mfma_f32_16x16x32_bf16 v[0:3], a[48:51], v[96:99], v[0:3]\n\t"
      "v_mfma_f32_16x16x32_bf16 v[4:7], a[112:115], v[96:99], v[4:7]\n\t"
      "s_waitcnt lgkmcnt(6)\n\t"
      "v_mfma_f32_16x16x32_bf16 v[8:11], a[48:51], v[100:103], v[8:11]\n\t"
      "v_mfma_f32_16x16x32_bf16 v[12:15], a[112:115], v[100:103], v[12:15]\n\t"
      "s_waitcnt lgkmcnt(5)\n\t"
      "v_mfma_f32_16x16x32_bf16 v[0:3], a[52:55], v[104:107], v[0:3]\n\t"
      "v_mfma_f32_16x16x32_bf16 v[4:7], a[116:119], v[104:107], v[4:7]\n\t"
      "s_waitcnt lgkmcnt(4)\n\t"
      "v_mfma_f32_16x16x32_bf16 v[8:11], a[52:55], v[108:111], v[8:11]\n\t"
      "v_mfma_f32_16x16x32_bf16 v[12:15], a[116:119], v[108:111], v[12:15]\n\t"
      "s_waitcnt lgkmcnt(3)\n\t"
      "v_mfma_f32_16x16x32_bf16 v[0:3], a[56:59], v[112:115], v[0:3]\n\t"
      "v_mfma_f32_16x16x32_bf16 v[4:7], a[120:123], v[112:115], v[4:7]\n\t"
      "s_waitcnt lgkmcnt(2)\n\t"
      "v_mfma_f32_16x16x32_bf16 v[8:11], a[56:59], v[116:119], v[8:11]\n\t"
      "v_mfma_f32_16x16x32_bf16 v[12:15], a[120:123], v[116:119], v[12:15]\n\t"
      "s_waitcnt lgkmcnt(1)\n\t"
      "v_mfma_f32_16x16x32_bf16 v[0:3], a[60:63], v[120:123], v[0:3]\n\t"
      "v_mfma_f32_16x16x32_bf16 v[4:7], a[124:127], v[120:123], v[4:7]\n\t"
      "s_waitcnt lgkmcnt(0)\n\t"
      "v_mfma_f32_16x16x32_bf16 v[8:11], a[60:63], v[124:127], v[8:11]\n\t"
      "v_mfma_f32_16x16x32_bf16 v[12:15], a[124:127], v[124:127], v[12:15]\n\t"
      "v_xor_b32 v254, 0x8000, v254\n\t"
      "v_xor_b32 v255, 0x8000, v255\n\t"
      "s_add_u32 s69, s53, 0x4000\n\t"
      "s_add_u32 s48, s48, 4\n\t"
      "s_waitcnt vmcnt(40)\n\t"
      "v_cvt_pk_bf16_f32 v0, v0, v1\n\t"
      "v_cvt_pk_bf16_f32 v1, v2, v3\n\t"
      "v_cvt_pk_bf16_f32 v2, v4, v5\n\t"
      "v_cvt_pk_bf16_f32 v3, v6, v7\n\t"
      "v_min_u32 v0, -2, v0\n\t"
      "v_min_u32 v1, -2, v1\n\t"
      "v_min_u32 v2, -2, v2\n\t"
      "v_min_u32 v3, -2, v3\n\t"
      "buffer_store_dwordx4 v[0:3], v238, s[36:39], s53 offen sc1\n\t"
      "v_cvt_pk_bf16_f32 v8, v8, v9\n\t"
      "v_cvt_pk_bf16_f32 v9, v10, v11\n\t"
      "v_cvt_pk_bf16_f32 v10, v12, v13\n\t"
      "v_cvt_pk_bf16_f32 v11, v14, v15\n\t"
      "v_min_u32 v8, -2, v8\n\t"
      "v_min_u32 v9, -2, v9\n\t"
      "v_min_u32 v10, -2, v10\n\t"
      "v_min_u32 v11, -2, v11\n\t"
      "buffer_store_dwordx4 v[8:11], v238, s[36:39], s69 offen sc1\n\t"
      "s_add_u32 s53, s53, 1048576\n\t"
      "s_nop 1\n\t"
      "s_branch WTOP%=\n\t"
      ".p2align 5\n\t"
      "WEND%=:\n\t"
      : "+{v[0:15]}"(c0), "+{v[16:31]}"(c1), "+{v[240:255]}"(c2), "+{v238}"(c3), "+{v[32:39]}"(c4), "+s"(w), "+s"(wv), "+s"(A),
        "+{a[0:31]}"(wo.a), "+{a[32:63]}"(wo.b), "+{a[64:95]}"(wo.c), "+{a[96:127]}"(wo.d)
      :
      : "memory", "vcc", "scc", "v40", "v41", "v42", "v43", "v44", "v45", "v46", "v47", "v48", "v49", "v50", "v51", "v52", "v53", "v54", "v55", "v56", "v57", "v58", "v59", "v60", "v61", "v62", "v63", "v64", "v65", "v66", "v67", "v68", "v69", "v70", "v71", "v72", "v73", "v74", "v75", "v76", "v77", "v78", "v79", "v80", "v81", "v82", "v83", "v84", "v85", "v86", "v87", "v88", "v89", "v90", "v91", "v92", "v93", "v94", "v95", "v96", "v97", "v98", "v99", "v100", "v101", "v102", "v103", "v104", "v105", "v106", "v107", "v108", "v109", "v110", "v111", "v112", "v113", "v114", "v115", "v116", "v117", "v118", "v119", "v120", "v121", "v122", "v123", "v124", "v125", "v126", "v127", "v128", "v129", "v130", "v131", "v132", "v133", "v134", "v135", "v136", "v137", "v138", "v139", "v140", "v141", "v142", "v143", "v144", "v145", "v146", "v147", "v148", "v149", "v150", "v151", "v152", "v153", "v154", "v155", "v156", "v157", "v158", "v159", "v160", "v161", "v162", "v163", "v164", "v165", "v166", "v167", "v168", "v169", "v170", "v171", "v172", "v173", "v174", "v175", "v176", "v177", "v178", "v179", "v180", "v181", "v182", "v183", "v184", "v185", "v186", "v187", "v188", "v189", "v190", "v191", "v192", "v193", "v194", "v195", "v196", "v197", "v198", "v199", "v200", "v201", "v202", "v203", "v204", "v205", "v206", "v207", "v208", "v209", "v210", "v211", "v212", "v213", "v214", "v215", "v216", "v217", "v218", "v219", "v220", "v221", "v222", "v223", "v224", "v225", "v226", "v227", "v228", "v229", "v230", "v231", "v232", "v233", "v234", "v235", "v236", "v237", "v239", "a128", "a129", "a130", "a131", "a132", "a133", "a134", "a135", "a136", "a137", "a138", "a139", "a140", "a141", "a142", "a143", "a144", "a145", "a146", "a147", "a148", "a149", "a150", "a151", "a152", "a153", "a154", "a155", "a156", "a157", "a158", "a159", "a160", "a161", "a162", "a163", "a164", "a165", "a166", "a167", "a168", "a169", "a170", "a171", "a172", "a173", "a174", "a175", "a176", "a177", "a178", "a179", "a180", "a181", "a182", "a183", "a184", "a185", "a186", "a187", "a188", "a189", "a190", "a191", "a192", "a193", "a194", "a195", "a196", "a197", "a198", "a199", "a200", "a201", "a202", "a203", "a204", "a205", "a206", "a207", "a208", "a209", "a210", "a211", "a212", "a213", "a214", "a215", "a216", "a217", "a218", "a219", "a220", "a221", "a222", "a223", "a224", "a225", "a226", "a227", "a228", "a229", "a230", "a231", "a232", "a233", "a234", "a235", "a236", "a237", "a238", "a239", "a240", "a241", "a242", "a243", "a244", "a245", "a246", "a247", "a248", "a249", "a250", "a251", "a252", "a253", "a254", "a255", "s24", "s36", "s37", "s38", "s39", "s40", "s41", "s42", "s43", "s44", "s45", "s46", "s47", "s48", "s49", "s50", "s51", "s52", "s53", "s54", "s55", "s56", "s57", "s58", "s59", "s60", "s61", "s62", "s63", "s64", "s65", "s66", "s67", "s68", "s69", "s70", "s71", "s72", "s73", "s74", "s75", "s76", "s77", "s78", "s79", "s80", "s81", "s82", "s83", "s84", "s85", "s86", "s87", "s88", "s89", "s90", "s91", "s92", "s93", "s94", "s95", "s96", "s97", "s98", "s99");
}

__global__ void __launch_bounds__(256, 1) mega_kernel(MegaParams P) {
  __shared__ __attribute__((aligned(16))) char lds[LDS_BYTES];
  int b, wv;
  KArgs A = (KArgs)__builtin_amdgcn_kernarg_segment_ptr();
  u32x16 ka; u32x8 kb;
  asm volatile("s_load_dwordx16 s[36:51], %4, 0\n\t"
               "s_load_dwordx8 s[52:59], %4, 64\n\t"
               "s_getpc_b64 s[60:61]\n"
               "KPC:\n\t"
               "s_lshr_b32 %0, %5, 3\n\t"
               "s_and_b32 %0, %0, 7\n\t"
               "s_lshl_b32 %0, %0, 14\n\t"
               "v_lshl_add_u32 v254, %6, 6, %0\n\t"
               "v_min_u32 v254, KENDW-KPC, v254\n\t"
               "global_load_dword v255, v254, s[60:61]\n\t"
               "v_readfirstlane_b32 %1, %6\n\t"
               "s_lshr_b32 %0, %5, 1\n\t"
               "s_andn2_b32 %0, %0, 3\n\t"
               "s_and_b32 s60, %5, 3\n\t"
               "s_or_b32 %0, %0, s60\n\t"
               "s_lshr_b32 %1, %1, 6\n\t"
               "s_bitcmp1_b32 %5, 2\n\t"
               "s_cbranch_scc1 GSTART\n\t"
               "s_waitcnt lgkmcnt(0)"
               : "=&{s34}"(b), "=&{s33}"(wv), "=&{s[36:51]}"(ka), "=&{s[52:59]}"(kb), "+{s[0:1]}"(A)
               : "s"(blockIdx.x), "v"(threadIdx.x) : "v252", "v253", "v254", "v255", "s60", "s61", "s62", "s63", "scc");
  static_assert(XS == 0, "");
  if (b >= NREC) {
    int w = b, wv2 = wv;
    WoRegs wo;
    const unsigned tag = worker_role(w, wv2, A, wo);
    worker_tasks(lds, w, wv2, A, tag, wo);
    asm volatile(".p2align 6");
    if (w == 0 && wv2 == 0) {
      const int lane = fresh_lane();
#pragma unroll 1
      for (unsigned spins = 0; spins < (1u << 16); ++spins) {
        const u32x2 sel = top2_wave((const float*)(A->ws + O_RBUF) + S_, lane, spins == 0, spins < 8);
        if (spins && sel[1]) {
          if (lane < 8) __hip_atomic_store((gu32*)(A->ws + O_CNT + lane * 64 + 8), (unsigned)__builtin_amdgcn_s_memrealtime(), RLX_AGENT);
          if (lane < 8) __hip_atomic_store((gu64*)(A->ws + O_CNT + lane * 64), (u64)sel[0] | ((u64)sel[1] << 32), RLX_AGENT);
          break;
        }
        __builtin_amdgcn_s_sleep(SSLEEP);
        if (spins == 0 && SWAIT) {
          unsigned f = 0;
#pragma unroll 1
          for (unsigned n = 0; n < 4096; ++n) {
            f = __builtin_amdgcn_readfirstlane(__hip_atomic_load((gu32*)(A->ws + O_KVSTEP), RLX_AGENT));
            if ((f >> 16) >= 61) break;
            __builtin_amdgcn_s_sleep(4);
          }
          f += SWAIT - ((f >> 16) - 61) * 156;
#pragma unroll 1
          while (((f - (unsigned)__builtin_amdgcn_s_memrealtime()) & 0xffffu) < 400u) __builtin_amdgcn_s_sleep(1);
        }
      }
    }
    __syncthreads();
    if ((w & 2) == 0) mlp2_role(A, lds, (w & 1) * 32 + (w >> 2), wv2 * 64 + fresh_lane());
    asm volatile("KENDW:");
    __builtin_amdgcn_endpgm();
    __builtin_unreachable();
  } else {
    KArgs kp = A;
    rec_role(lds, kp, b, wv, ka, kb);
    asm volatile(".p2align 6");
    const int tid = wv * 64 + fresh_lane();
    __syncthreads();
    if ((b & 31) < M1_WGS / 4) mlp1_role(kp, lds, (b & 31) * 4 + (b >> 5), tid);
    else if (SW2 && b == M1_WGS / 4 + 1) {
      if (wv == 0) {
        const unsigned t0 = (unsigned)__builtin_amdgcn_s_memrealtime();
#pragma unroll 1
        for (unsigned spins = 0; spins < (1u << 16); ++spins) {
          const u32x2 sel = top2_wave((const float*)(kp->ws + O_RBUF) + S_, tid, spins == 0, false);
          if (spins && sel[1]) {
            if (tid < 32) __hip_atomic_store((gu64*)(kp->ws + O_SEL2 + tid * 128), (u64)sel[0] | ((u64)sel[1] << 32), RLX_AGENT);
            break;
          }
#pragma unroll 1
          while ((unsigned)__builtin_amdgcn_s_memrealtime() - t0 < SW2) __builtin_amdgcn_s_sleep(1);
        }
      }
    }
    else if (b == M1_WGS / 4) {
      if (tid == 64) {
        gu32* e = (gu32*)(kp->ws + O_EPOCH);
        __hip_atomic_store(e, mktag(__hip_atomic_load(e, RLX_AGENT)), RLX_AGENT);
      }
    }
    __builtin_amdgcn_endpgm();
    __builtin_unreachable();
  }
}

extern "C" void kernel_launch(void* const* d_in, const int* in_sizes, int n_in, void* d_out, int out_size, void* d_ws, size_t ws_size,
                              hipStream_t stream) {
  MegaParams P;
  P.x = (const u16*)d_in[0]; P.mem0 = (const u16*)d_in[1];
  P.Wq = (const u16*)d_in[2]; P.Wk = (const u16*)d_in[3]; P.Wv = (const u16*)d_in[4]; P.Wo = (const u16*)d_in[5];
  P.w_r = (const u16*)d_in[8]; P.wg = (const u16*)d_in[9]; P.wu = (const u16*)d_in[10]; P.wd = (const u16*)d_in[11];
  P.out = (u16*)d_out; P.ws = (char*)d_ws;
  mega_kernel<<<256, 256, 0, stream>>>(P);
}
```
